```python
import torch

def _softmax(x, axis=-1):
    return torch.softmax(x, dim=axis)
def _gelu(x):
    return torch.nn.functional.gelu(x, approximate="tanh")
def _silu(x):
    return torch.nn.functional.silu(x)
def _sigmoid(x):
    return torch.sigmoid(x)
def _layernorm(x, axis=-1, eps=1e-5):
    m = x.mean(dim=axis, keepdim=True)
    v = x.var(dim=axis, keepdim=True, unbiased=False)
    return (x - m) / torch.sqrt(v + eps)
def _scatter_add(tgt, idx, src, axis=0):
    return tgt.clone().index_add_(axis, idx, src)
def _segment_sum(src, seg, n):
    out = torch.zeros((n,) + src.shape[1:], dtype=src.dtype)
    return out.index_add_(0, seg, src)
def _pad(x, pw):
    flat = tuple(v for pair in reversed(pw) for v in pair)
    return torch.nn.functional.pad(x, flat)

kernel_name = 'gated_mix__linear_kernel_hedgehog__moe_arch_mod__prompt'

def setup_inputs():

    _w = (lambda a: (lambda t: t.to(torch.bfloat16) if t.is_floating_point() else t)(torch.as_tensor(a)))
    def _A_setup():
        B, S, D, H = 1, 2048, 1024, 32
        Dh = D // H
        return {
            "x": _w((lambda *s: torch.randn(*s, dtype=torch.bfloat16) * 0.05)(B, S, D)),
            "wqkv": _w((lambda *s: torch.randn(*s, dtype=torch.bfloat16) * 1.6)(D, 3 * D)),
            "w_phi": _w((lambda *s: torch.randn(*s, dtype=torch.bfloat16) * 1.6)(H, Dh, Dh) * (Dh ** -0.5)),
            "wo": _w((lambda *s: torch.randn(*s, dtype=torch.bfloat16) * 1.6)(D, D)),
        }
    _A_in = _A_setup()

    def _B_setup():
        B, S, D, F, C = 1, 2048, 1024, 3584, 1
        return {
            "x": _w((lambda *s: torch.randn(*s, dtype=torch.bfloat16) * 0.05)(B, S, D)),
            "w_r": _w((lambda *s: torch.randn(*s, dtype=torch.bfloat16) * 1.6)(D)),
            "w_gate": _w((lambda *s: torch.randn(*s, dtype=torch.bfloat16) * 0.8)(D, F)),
            "w_up": _w((lambda *s: torch.randn(*s, dtype=torch.bfloat16) * 0.8)(D, F)),
            "w_down": _w((lambda *s: torch.randn(*s, dtype=torch.bfloat16) * 0.8)(F, D)),
        }
    _B_in = _B_setup()

    return {
        "A_x": _A_in["x"],
        "A_wqkv": _A_in["wqkv"],
        "A_w_phi": _A_in["w_phi"],
        "A_wo": _A_in["wo"],
        "B_x": _B_in["x"],
        "B_w_r": _B_in["w_r"],
        "B_w_gate": _B_in["w_gate"],
        "B_w_up": _B_in["w_up"],
        "B_w_down": _B_in["w_down"],
        "gate_w": _w((lambda *s: torch.randn(*s, dtype=torch.bfloat16) * 0.05)(1024) * 16.0),
    }

def reference(A_x, A_wqkv, A_w_phi, A_wo, B_x, B_w_r, B_w_gate, B_w_up, B_w_down, gate_w):
    def _A_ref(x, wqkv, w_phi, wo):
        B, S, D = x.shape
        H = 32; Dh = D // H
        qkv = (lambda x, *d: x.permute(*d))((x @ wqkv).reshape(B, S, 3, H, Dh), 2, 0, 3, 1, 4)
        q, k, v = qkv[0], qkv[1], qkv[2]
        pq = torch.einsum("bhsd,hdf->bhsf", q, w_phi)
        pk = torch.einsum("bhsd,hdf->bhsf", k, w_phi)
        phi_q = torch.concatenate([_softmax(pq, axis=-1), _softmax(-pq, axis=-1)], axis=-1)
        phi_k = torch.concatenate([_softmax(pk, axis=-1), _softmax(-pk, axis=-1)], axis=-1)
        kv = torch.einsum("bhsf,bhsd->bhfd", phi_k, v)
        z  = torch.einsum("bhsf->bhf", phi_k)
        num = torch.einsum("bhqf,bhfd->bhqd", phi_q, kv)
        den = torch.einsum("bhqf,bhf->bhq", phi_q, z)[..., None] + 1e-6
        o = num / den
        return (lambda x, *d: x.permute(*d))(o, 0, 2, 1, 3).reshape(B, S, D) @ wo

    def _B_ref(x, w_r, w_gate, w_up, w_down):
        B, S, D = x.shape
        C = 1
        r = x @ w_r
        idx = torch.argsort(-r, axis=-1)[:, :C]
        idx_e = (lambda x, n, axis: torch.repeat_interleave(x, n, dim=axis))(idx.reshape(B, C, 1), D, axis=2)
        x_sel = (lambda x, i, axis: torch.take_along_dim(x, i, dim=axis))(x, idx_e, axis=1)
        g = _silu(x_sel @ w_gate)
        u = x_sel @ w_up
        y = (g * u) @ w_down
        r_sel = _sigmoid((lambda x, i, axis: torch.take_along_dim(x, i, dim=axis))(r, idx, axis=1))
        y = y * r_sel.reshape(B, C, 1)
        b_off = (torch.arange(B) * S).reshape(B, 1)
        flat_idx = (idx + b_off).reshape(B * C)
        zero = (x * 0.0).reshape(B * S, D)
        upd = _scatter_add(zero, flat_idx, y.reshape(B * C, D), axis=0)
        return x + upd.reshape(B, S, D)

    x = A_x
    g = _sigmoid((x * gate_w).sum(-1, keepdims=True))
    a = _A_ref(x, A_wqkv, A_w_phi, A_wo)
    b = _B_ref(x, B_w_r, B_w_gate, B_w_up, B_w_down)
    return g * a + (1 - g) * b
```

```cpp
#define KNOBS KNOB(PRED_C,105);KNOB(D3,150);KNOB(Y_AT,270);
#include <hip/hip_runtime.h>
#include <stdint.h>
#include <type_traits>

typedef __attribute__((ext_vector_type(8))) short bf16x8;
typedef __attribute__((ext_vector_type(4))) short s16x4;
typedef __attribute__((ext_vector_type(4))) float f32x4;
typedef __attribute__((ext_vector_type(2))) float f32x2;
typedef __attribute__((ext_vector_type(32))) float f32x32;
typedef __attribute__((ext_vector_type(2))) unsigned u32x2;
typedef __attribute__((ext_vector_type(4))) unsigned u32x4;
typedef __attribute__((ext_vector_type(8))) unsigned u32x8;
typedef __attribute__((ext_vector_type(16))) unsigned u32x16;
typedef __attribute__((ext_vector_type(32))) unsigned u32x32;
typedef __attribute__((ext_vector_type(2))) __bf16 bf16x2_t;
typedef unsigned short u16;
typedef __amdgpu_buffer_rsrc_t rsrc_t;

#define DI __device__ __forceinline__
#define AS3 __attribute__((address_space(3)))
#define RLX_WG __ATOMIC_RELAXED, __HIP_MEMORY_SCOPE_WORKGROUP

constexpr int AUX_SC1 = 16;

DI float bf2f(u16 v) { return __uint_as_float(((unsigned)v) << 16); }
DI u16 f2bf(float f) { __bf16 b = (__bf16)f; return __builtin_bit_cast(u16, b); }
DI float rbf(float f) { return bf2f(f2bf(f)); }
DI unsigned pack2(float lo, float hi) {
    f32x2 v = {lo, hi};
    bf16x2_t b = __builtin_convertvector(v, bf16x2_t);
    return __builtin_bit_cast(unsigned, b);
}
DI float lo16(unsigned u) { return __uint_as_float(u << 16); }
DI float hi16(unsigned u) { return __uint_as_float(u & 0xffff0000u); }
DI float dot2(unsigned a, unsigned b, float c) {
    return __builtin_amdgcn_fdot2_f32_bf16(__builtin_bit_cast(bf16x2_t, a), __builtin_bit_cast(bf16x2_t, b), c, false);
}
DI s16x4 tr_read(unsigned a) { return __builtin_amdgcn_ds_read_tr16_b64_v4i16((AS3 s16x4*)(size_t)a); }
DI bf16x8 cat8(s16x4 a, s16x4 b) { return __builtin_shufflevector(a, b, 0, 1, 2, 3, 4, 5, 6, 7); }
DI bf16x8 lds_rd128(unsigned a) { return *(AS3 const bf16x8*)(size_t)a; }
DI void lds_wr128(unsigned a, bf16x8 v) { *(AS3 bf16x8*)(size_t)a = v; }
DI f32x4 mfma16(bf16x8 a, bf16x8 b, f32x4 c) { return __builtin_amdgcn_mfma_f32_16x16x32_bf16(a, b, c, 0, 0, 0); }
DI bf16x8 pack44(f32x4 a, f32x4 b) {
    u32x4 p = {pack2(a[0], a[1]), pack2(a[2], a[3]), pack2(b[0], b[1]), pack2(b[2], b[3])};
    return __builtin_bit_cast(bf16x8, p);
}
#define FU(x) __float_as_uint(x)
#define UF(x) __uint_as_float(x)
DI float x32_sum(float v) { auto r = __builtin_amdgcn_permlane32_swap(FU(v), FU(v), false, false); return UF(r[0]) + UF(r[1]); }
DI float x16_sum(float v) { auto r = __builtin_amdgcn_permlane16_swap(FU(v), FU(v), false, false); return UF(r[0]) + UF(r[1]); }
DI float xg_sum(float v) { return x16_sum(x32_sum(v)); }
template <int CTRL> DI float dpp_add(float v) { return v + UF(__builtin_amdgcn_update_dpp(0, (int)FU(v), CTRL, 0xf, 0xf, false)); }
DI float row16_sum(float v) { return dpp_add<0x121>(dpp_add<0x122>(dpp_add<0x124>(dpp_add<0x128>(v)))); }
template <int CTRL> DI unsigned dpp_umax(unsigned v) { return max(v, (unsigned)__builtin_amdgcn_update_dpp(0, (int)v, CTRL, 0xf, 0xf, false)); }
DI unsigned wave_umax(unsigned v) {
    auto r = __builtin_amdgcn_permlane32_swap(v, v, false, false);
    v = max(r[0], r[1]);
    auto q = __builtin_amdgcn_permlane16_swap(v, v, false, false);
    v = max(q[0], q[1]);
    return dpp_umax<0x121>(dpp_umax<0x122>(dpp_umax<0x124>(dpp_umax<0x128>(v))));
}
DI unsigned add4(u32x4 v, unsigned s) { return (v[0] + v[1] + v[2]) + (v[3] + s); }
template <int CTRL> DI unsigned dpp_uadd(unsigned v) { return v + (unsigned)__builtin_amdgcn_update_dpp(0, (int)v, CTRL, 0xf, 0xf, false); }
DI unsigned row16_usum(unsigned v) { return dpp_uadd<0x121>(dpp_uadd<0x122>(dpp_uadd<0x124>(dpp_uadd<0x128>(v)))); }
DI rsrc_t mk_rsrc(const void* p, unsigned bytes) { return __builtin_amdgcn_make_buffer_rsrc((void*)p, (short)0, (int)bytes, 0x00020000); }
DI u32x4 ld128c(rsrc_t rs, unsigned vo, unsigned so) { return __builtin_amdgcn_raw_buffer_load_b128(rs, vo, so, AUX_SC1); }
DI u32x2 ld64c(rsrc_t rs, unsigned vo, unsigned so) { return __builtin_amdgcn_raw_buffer_load_b64(rs, vo, so, AUX_SC1); }
DI unsigned ld32c(rsrc_t rs, unsigned vo, unsigned so) { return __builtin_amdgcn_raw_buffer_load_b32(rs, vo, so, AUX_SC1); }
DI void st128c(u32x4 v, rsrc_t rs, unsigned vo, unsigned so) {
    __builtin_amdgcn_raw_buffer_store_b128(v, rs, vo, so, AUX_SC1);
    asm volatile("s_nop 1" :: "v"(v) : "memory");
}
DI void st64c(u32x2 v, rsrc_t rs, unsigned vo, unsigned so) { __builtin_amdgcn_raw_buffer_store_b64(v, rs, vo, so, AUX_SC1); }
DI void st32c(unsigned v, rsrc_t rs, unsigned vo, unsigned so) { __builtin_amdgcn_raw_buffer_store_b32(v, rs, vo, so, AUX_SC1); }

DI void dma16(rsrc_t rs, unsigned voff, unsigned soff, unsigned lds_addr) {
    asm volatile("s_mov_b32 m0, %0\n\ts_nop 0\n\tbuffer_load_dwordx4 %1, %2, %3 offen lds"
                 :: "s"(lds_addr), "v"(voff), "s"(rs), "s"(soff) : "memory");
}
template <int PC> DI void dma4(rsrc_t rs, const unsigned* v, unsigned soff, unsigned lds_addr) {
#define D4(p) asm volatile("s_mov_b32 m0, %0\n\ts_nop 0\n\tbuffer_load_dwordx4 %1, %5, %6 offen lds\n\t" p "buffer_load_dwordx4 %2, %5, %6 offen offset:1024 lds\n\t" p \
                 "buffer_load_dwordx4 %3, %5, %6 offen offset:2048 lds\n\t" p "buffer_load_dwordx4 %4, %5, %6 offen offset:3072 lds" \
                 :: "s"(lds_addr), "v"(v[0]), "v"(v[1]), "v"(v[2]), "v"(v[3]), "s"(rs), "s"(soff), "i"(PC ? PC - 1 : 0) : "memory")
    if (PC) D4("s_nop %7\n\t"); else D4("");
}
template <int N> DI void wait_vm() { asm volatile("s_waitcnt vmcnt(%0)" :: "i"(N) : "memory"); }
DI void wait_lgkm0() { asm volatile("" ::: "memory"); __builtin_amdgcn_s_waitcnt(0xC07F); asm volatile("" ::: "memory"); }
DI void bar() { asm volatile("" ::: "memory"); __builtin_amdgcn_s_barrier(); asm volatile("" ::: "memory"); }
DI void fence_c() { asm volatile("" ::: "memory"); }
DI int fresh_lane() {
    int v;
    asm volatile("v_mbcnt_lo_u32_b32 %0, -1, 0\n\tv_mbcnt_hi_u32_b32 %0, -1, %0" : "=v"(v));
    return v;
}
#define LANE_VARS const int l = fresh_lane(), c = l & 15, g = l >> 4, i4 = (l & 15) >> 2, i3 = l & 3; (void)c; (void)g; (void)i4; (void)i3
#define PSW ((unsigned)(4 * g + i4) * 128u + (unsigned)((i4 ^ (g & 1)) << 5))
#define WPO ((unsigned)(4 * g + i4) * 64u + (unsigned)((g & 1) << 5) + (unsigned)(8 * i3))
#define WSW ((unsigned)(8 * g + i4) * 128u + (unsigned)((i4 ^ (g & 1)) << 5) + (unsigned)(8 * i3))

#define WO_VOFF ((unsigned)(l >> 3) * 2048u + (unsigned)(((((((l & 7) >> 1) ^ ((l >> 3) & 3) ^ (l >> 5)) << 1) | (l & 1))) << 4))

DI void lds_wait(unsigned addr, unsigned target) {
#pragma unroll 1
    for (int it = 0; it < (1 << 22); ++it) {
        unsigned v = __hip_atomic_load((AS3 unsigned*)(size_t)addr, RLX_WG);
        if (__builtin_amdgcn_readfirstlane(v) >= target) break;
        __builtin_amdgcn_s_sleep(1);
    }
    fence_c();
}
DI unsigned now32() { unsigned long long t; asm volatile("s_memrealtime %0\n\ts_waitcnt lgkmcnt(0)" : "=s"(t) :: "memory"); return (unsigned)t; }
DI void wait_until(unsigned t) {
#pragma unroll 1
    for (int it = 0; it < 256; ++it) {
        const int rem = (int)(t - now32());
        if (rem <= 4) break;
        if (rem > 12) __builtin_amdgcn_s_sleep(2);
    }
    fence_c();
}
DI unsigned wave_usum(unsigned m) {
    auto p = __builtin_amdgcn_permlane32_swap(m, m, false, false);
    m = p[0] + p[1];
    auto q = __builtin_amdgcn_permlane16_swap(m, m, false, false);
    return row16_usum(q[0] + q[1]);
}
DI void post(unsigned addr, unsigned v) {
    fence_c();
    *(AS3 volatile unsigned*)(size_t)addr = v;
    fence_c();
}
DI void wait4(unsigned addr, unsigned target) {
#pragma unroll 1
    for (int it = 0; it < (1 << 22); ++it) {
        const u32x4 v = *(AS3 volatile const u32x4*)(size_t)addr;
        if (__builtin_amdgcn_readfirstlane(min(min(v[0], v[1]), min(v[2], v[3]))) >= target) break;
        __builtin_amdgcn_s_sleep(1);
    }
    fence_c();
}
constexpr int POLL_LIMIT = 1 << 16, TRY_LIMIT = 1 << 12;

#define KNOB(n, v) static constexpr int n = v
struct Dflt {
    KNOB(KT0, 0x76534210); KNOB(KT1, 0x76453201); KNOB(KT2, 0x75643120); KNOB(KT3, 0x67542310);
    KNOB(BGT, 0x76227622); KNOB(BUT, 0x77327732); KNOB(BRT, 0x77777777); KNOB(BST, 0x33333333); KNOB(PBT, (int)0xaaaaaaaa);
    KNOB(SLG, 0); KNOB(SLS, 20); KNOB(X0, 130); KNOB(X1, 2); KNOB(X2, 2); KNOB(TOPV, 1);
    KNOB(PRED_K, 16); KNOB(PRED_C, 90);
    KNOB(D1, 40); KNOB(D2, 120); KNOB(D3, 140); KNOB(FG_AT, -130); KNOB(Y_AT, 260); KNOB(QA, 30); KNOB(QB, 40);
    KNOB(RETRY_S, 8); KNOB(PT_S, 12); KNOB(KF_S, 10); KNOB(WO_D, 24); KNOB(FD, 12);
    KNOB(PT_B, 10); KNOB(XE, 20);
    KNOB(RE1, 1); KNOB(L2_S, 12);
    KNOB(FORCE_REDO, 0);
    KNOB(ND, 6); KNOB(XAH, 0); KNOB(QG, 2); KNOB(HP, 2); KNOB(Q5E, 1); KNOB(PADS, 1); KNOB(WOSK, 0); KNOB(WOE, 0); KNOB(TNT, 0); KNOB(TWB, 13); KNOB(TMW, 0); KNOB(DWP, 1); KNOB(WOG, 0); KNOB(PK13, 3); KNOB(DS1, 2); KNOB(DS2, 9); KNOB(DS3, 16); KNOB(PACE, 0);
};
struct K : Dflt { KNOBS };

constexpr unsigned WS_EPOCH = 0;
constexpr unsigned WS_RG = 4096;
constexpr unsigned WS_PT = 6144;
constexpr unsigned WS_G = 8192;
constexpr unsigned WS_YF = 24576;
constexpr unsigned WS_FG = 65536;
constexpr unsigned WS_Y = 1u << 19;
constexpr unsigned WS_BYTES = 1u << 20;
constexpr unsigned WB_KVP = 0;
constexpr unsigned WB_SM = 1u << 20;
constexpr unsigned WB_PRB = 3u << 19;
constexpr unsigned WB_OG = 7u << 18;
constexpr unsigned WB_OF = 8u << 20;
constexpr unsigned WB_BYTES = 13u << 20;
DI char* bulk_base(char* ws) { return (char*)(((size_t)ws + (3u << 20)) & ~(size_t)((2u << 20) - 1)); }

constexpr unsigned SLOT = 40960;
constexpr unsigned L_PHI = 98304;
constexpr unsigned L_VF = 131072;
constexpr unsigned L_MISC = 147456;
constexpr unsigned L_WP = L_MISC, L_KVF = L_MISC + 4096, L_SM = L_MISC + 12288;
constexpr unsigned C_ST = L_SM + 0  , C_R = L_SM + 8  , C_P = L_SM + 16  ;
constexpr unsigned C_WO = L_SM + 56, C_YRDY = L_SM + 68, C_IDX = L_SM + 76, C_TM = L_SM + 80, C_XE = L_SM + 84, C_Y1 = L_SM + 88;
constexpr unsigned P_E14 = 1, P_E15 = 2, P_PHA = 3, P_PHB = 4, P_KVD = 5, P_PUB = 6, P_Q1 = 7  , P_QRD = 11, P_O = 12;
constexpr unsigned C_Q = L_SM + 92, C_HOLD = L_SM + 96;
constexpr unsigned XS0[3] = {16384, 98304, L_VF}, XS1[3] = {57344, 122880, L_VF + 8192};
constexpr unsigned QX[3] = {40960, 81920, 0}, QW[6] = {L_KVF, 65536, 73728, 24576, 32768, L_PHI};
constexpr unsigned R_L1I = 1, R_L1 = 2, R_L2I = 3, R_KVF = 4;
constexpr unsigned L_XE = L_VF;
constexpr unsigned L_KEY = L_SM + 256, L_TAG = L_SM + 288, L_IDX = L_SM + 296, L_TM = L_SM + 304  , L_PLC = L_SM + 320  , L_PRB = L_SM + 336  , L_ZF = L_SM + 512, L_Y = L_SM + 1536;

constexpr unsigned long long L2E2 = 0x3fb8aa3b3fb8aa3bull;

#define PHASE_AB_ASM \
    "s_branch .Lpad_ab0\n\t" \
    ".Lab_s1:\n\t" \
    "s_mov_b32 s89, 0x400000\n\t" \
    ".Lab_s1a:\n\t" \
    "s_waitcnt lgkmcnt(0)\n\t" \
    "ds_read_b32 v237, v249\n\t" \
    "ds_read_b32 v238, v249 offset:4\n\t" \
    "s_waitcnt lgkmcnt(0)\n\t" \
    "v_min_u32 v237, v237, v238\n\t" \
    "s_nop 1\n\t" \
    "v_readfirstlane_b32 s85, v237\n\t" \
    "s_sub_u32 s89, s89, 1\n\t" \
    "s_cbranch_scc1 .Lab_s1b\n\t" \
    "s_cmp_lt_u32 s85, 1\n\t" \
    "s_cbranch_scc1 .Lab_s1a\n\t" \
    ".Lab_s1b:\n\t" \
    "s_nop 7\n\t" \
    "s_waitcnt lgkmcnt(0)\n\t" \
    "s_branch .Lab_s1r\n\t" \
    ".Lab_s2:\n\t" \
    "s_mov_b32 s89, 0x400000\n\t" \
    ".Lab_s2a:\n\t" \
    "s_waitcnt lgkmcnt(0)\n\t" \
    "ds_read_b32 v237, v249 offset:16\n\t" \
    "ds_read_b32 v238, v249 offset:20\n\t" \
    "ds_read_b32 v255, v249 offset:24\n\t" \
    "s_waitcnt lgkmcnt(0)\n\t" \
    "v_min3_u32 v237, v237, v238, v255\n\t" \
    "ds_read_b32 v238, v249 offset:28\n\t" \
    "s_waitcnt lgkmcnt(0)\n\t" \
    "v_min_u32 v237, v237, v238\n\t" \
    "s_nop 1\n\t" \
    "v_readfirstlane_b32 s85, v237\n\t" \
    "s_sub_u32 s89, s89, 1\n\t" \
    "s_cbranch_scc1 .Lab_s2b\n\t" \
    "s_cmp_lt_u32 s85, 2\n\t" \
    "s_cbranch_scc1 .Lab_s2a\n\t" \
    ".Lab_s2b:\n\t" \
    "s_nop 7\n\t" \
    "s_waitcnt lgkmcnt(0)\n\t" \
    "s_branch .Lab_s2r\n\t" \
    ".Lab_s3:\n\t" \
    "s_mov_b32 s89, 0x400000\n\t" \
    ".Lab_s3a:\n\t" \
    "s_waitcnt lgkmcnt(0)\n\t" \
    "ds_read_b32 v237, v250\n\t" \
    "s_waitcnt lgkmcnt(0)\n\t" \
    "s_nop 1\n\t" \
    "v_readfirstlane_b32 s85, v237\n\t" \
    "s_sub_u32 s89, s89, 1\n\t" \
    "s_cbranch_scc1 .Lab_s3b\n\t" \
    "s_cmp_lt_u32 s85, 3\n\t" \
    "s_cbranch_scc1 .Lab_s3a\n\t" \
    ".Lab_s3b:\n\t" \
    "s_nop 7\n\t" \
    "ds_read_b64_tr_b16 v[104:105], v245\n\t" \
    "ds_read_b64_tr_b16 v[106:107], v245 offset:2048\n\t" \
    "ds_read_b64_tr_b16 v[108:109], v255\n\t" \
    "ds_read_b64_tr_b16 v[110:111], v255 offset:2048\n\t" \
    "ds_read_b128 v[80:83], v242\n\t" \
    "ds_read_b128 v[84:87], v242 offset:1024\n\t" \
    "ds_read_b128 v[88:91], v242 offset:4096\n\t" \
    "ds_read_b128 v[92:95], v242 offset:5120\n\t" \
    "s_waitcnt lgkmcnt(0)\n\t" \
    "s_branch .Lab_s3r\n\t" \
    ".Lab_s4:\n\t" \
    "s_mov_b32 s89, 0x400000\n\t" \
    ".Lab_s4a:\n\t" \
    "s_waitcnt lgkmcnt(0)\n\t" \
    "ds_read_b32 v237, v250\n\t" \
    "s_waitcnt lgkmcnt(0)\n\t" \
    "s_nop 1\n\t" \
    "v_readfirstlane_b32 s85, v237\n\t" \
    "s_sub_u32 s89, s89, 1\n\t" \
    "s_cbranch_scc1 .Lab_s4b\n\t" \
    "s_cmp_lt_u32 s85, 4\n\t" \
    "s_cbranch_scc1 .Lab_s4a\n\t" \
    ".Lab_s4b:\n\t" \
    "s_nop 7\n\t" \
    "ds_read_b64_tr_b16 v[8:9], v245 offset:4096\n\t" \
    "ds_read_b64_tr_b16 v[10:11], v245 offset:6144\n\t" \
    "ds_read_b64_tr_b16 v[12:13], v255 offset:4096\n\t" \
    "ds_read_b64_tr_b16 v[14:15], v255 offset:6144\n\t" \
    "v_mov_b32 v237, 5\n\t" \
    "s_waitcnt lgkmcnt(0)\n\t" \
    "s_branch .Lab_s4r\n\t" \
    ".Lab_s5:\n\t" \
    "s_mov_b32 s89, 0x400000\n\t" \
    ".Lab_s5a:\n\t" \
    "s_waitcnt lgkmcnt(0)\n\t" \
    "ds_read_b32 v237, v249 offset:92\n\t" \
    "s_waitcnt lgkmcnt(0)\n\t" \
    "s_nop 1\n\t" \
    "v_readfirstlane_b32 s85, v237\n\t" \
    "s_sub_u32 s89, s89, 1\n\t" \
    "s_cbranch_scc1 .Lab_s5b\n\t" \
    "s_cmp_lt_u32 s85, 1\n\t" \
    "s_cbranch_scc1 .Lab_s5a\n\t" \
    ".Lab_s5b:\n\t" \
    "s_nop 7\n\t" \
    "ds_read_b64_tr_b16 v[80:81], v253\n\t" \
    "ds_read_b64_tr_b16 v[82:83], v253 offset:512\n\t" \
    "ds_read_b64_tr_b16 v[84:85], v251\n\t" \
    "ds_read_b64_tr_b16 v[86:87], v251 offset:512\n\t" \
    "ds_read_b64_tr_b16 v[88:89], v253 offset:4096\n\t" \
    "ds_read_b64_tr_b16 v[90:91], v253 offset:4608\n\t" \
    "ds_read_b64_tr_b16 v[92:93], v251 offset:4096\n\t" \
    "ds_read_b64_tr_b16 v[94:95], v251 offset:4608\n\t" \
    "s_waitcnt lgkmcnt(0)\n\t" \
    "s_branch .Lab_s5r\n\t" \
    ".Lab_s6:\n\t" \
    "s_mov_b32 s89, 0x400000\n\t" \
    ".Lab_s6a:\n\t" \
    "s_waitcnt lgkmcnt(0)\n\t" \
    "ds_read_b32 v237, v249 offset:92\n\t" \
    "s_waitcnt lgkmcnt(0)\n\t" \
    "s_nop 1\n\t" \
    "v_readfirstlane_b32 s85, v237\n\t" \
    "s_sub_u32 s89, s89, 1\n\t" \
    "s_cbranch_scc1 .Lab_s6b\n\t" \
    "s_cmp_lt_u32 s85, 2\n\t" \
    "s_cbranch_scc1 .Lab_s6a\n\t" \
    ".Lab_s6b:\n\t" \
    "s_nop 7\n\t" \
    "v_mov_b32 v237, 10\n\t" \
    "s_waitcnt lgkmcnt(0)\n\t" \
    "s_branch .Lab_s6r\n\t" \
    ".Lab_s7:\n\t" \
    "s_mov_b32 s89, 0x400000\n\t" \
    ".Lab_s7a:\n\t" \
    "s_sleep 1\n\t" \
    "ds_read_b32 v237, v249 offset:8\n\t" \
    "s_waitcnt lgkmcnt(0)\n\t" \
    "v_readfirstlane_b32 s85, v237\n\t" \
    "s_sub_u32 s89, s89, 1\n\t" \
    "s_cbranch_scc1 .Lab_s7r\n\t" \
    "s_cmp_lt_u32 s85, 3\n\t" \
    "s_cbranch_scc1 .Lab_s7a\n\t" \
    "s_sleep 5\n\t" \
    "s_branch .Lab_s7r\n\t" \
    ".Lab_sa:\n\t" \
    "ds_read_b32 v36, v249 offset:8\n\t" \
    "s_waitcnt lgkmcnt(0)\n\t" \
    "v_readfirstlane_b32 s80, v36\n\t" \
    "s_sub_u32 s89, s89, 1\n\t" \
    "s_cbranch_scc1 .Lab_sar\n\t" \
    "s_cmp_lt_u32 s80, s82\n\t" \
    "s_cbranch_scc1 .Lab_sa\n\t" \
    "s_cmp_eq_u32 s80, s82\n\t" \
    "s_cbranch_scc1 .Lab_sar\n\t" \
    "s_waitcnt vmcnt(0)\n\t" \
    "s_branch .Lab_k\n\t" \
    ".Lab_s8:\n\t" \
    "s_mov_b32 s89, 0x10000\n\t" \
    "v_lshlrev_b32 v35, 3, v34\n\t" \
    ".Lab_s8a:\n\t" \
    "s_sleep 8\n\t" \
    "buffer_load_dwordx2 v[232:233], v35, %[rsw], %[sg] offen sc1\n\t" \
    "buffer_load_dwordx2 v[234:235], v35, %[rsw], %[sg] offen offset:128 sc1\n\t" \
    "s_sub_u32 s89, s89, 1\n\t" \
    "s_waitcnt vmcnt(0)\n\t" \
    "s_cbranch_scc1 .Lab_s8r\n\t" \
    "v_cmp_ne_u32 vcc, v236, v233\n\t" \
    "v_cmp_ne_u32_e64 s[92:93], v236, v235\n\t" \
    "s_nop 0\n\t" \
    "s_or_b64 vcc, vcc, s[92:93]\n\t" \
    "s_cbranch_scc1 .Lab_s8a\n\t" \
    "s_branch .Lab_s8r\n\t" \
    ".Lab_s9:\n\t" \
    "s_mov_b32 s89, 0x400000\n\t" \
    ".Lab_s9a:\n\t" \
    "s_waitcnt lgkmcnt(0)\n\t" \
    "ds_read_b32 v237, v249 offset:56\n\t" \
    "s_waitcnt lgkmcnt(0)\n\t" \
    "s_nop 1\n\t" \
    "v_readfirstlane_b32 s85, v237\n\t" \
    "s_sub_u32 s89, s89, 1\n\t" \
    "s_cbranch_scc1 .Lab_s9b\n\t" \
    "s_cmp_lt_u32 s85, 2\n\t" \
    "s_cbranch_scc1 .Lab_s9a\n\t" \
    ".Lab_s9b:\n\t" \
    "s_nop 7\n\t" \
    "s_waitcnt lgkmcnt(0)\n\t" \
    "s_branch .Lab_s9r\n\t" \
    ".p2align 12\n\t" \
    ".Lpad_ab0:\n\t" \
    "s_cbranch_execz .Lpad_top\n\t" \
    "v_mov_b32 v32, 0\n\t" \
    "v_mov_b32 v33, 0\n\t" \
    "v_mov_b32 v34, 0\n\t" \
    "v_mov_b32 v35, 0\n\t" \
    "v_mov_b32 v36, 0\n\t" \
    "v_mov_b32 v37, 0\n\t" \
    "v_mov_b32 v38, 0\n\t" \
    "v_mov_b32 v39, 0\n\t" \
    "v_mov_b32 v40, 0\n\t" \
    "v_mov_b32 v41, 0\n\t" \
    "v_mov_b32 v42, 0\n\t" \
    "v_mov_b32 v43, 0\n\t" \
    "v_mov_b32 v44, 0\n\t" \
    "v_mov_b32 v45, 0\n\t" \
    "v_mov_b32 v46, 0\n\t" \
    "v_mov_b32 v47, 0\n\t" \
    "v_mov_b32 v48, 0\n\t" \
    "v_mov_b32 v49, 0\n\t" \
    "v_mov_b32 v50, 0\n\t" \
    "v_mov_b32 v51, 0\n\t" \
    "v_mov_b32 v52, 0\n\t" \
    "v_mov_b32 v53, 0\n\t" \
    "v_mov_b32 v54, 0\n\t" \
    "v_mov_b32 v55, 0\n\t" \
    "v_mov_b32 v56, 0\n\t" \
    "v_mov_b32 v57, 0\n\t" \
    "v_mov_b32 v58, 0\n\t" \
    "v_mov_b32 v59, 0\n\t" \
    "v_mov_b32 v60, 0\n\t" \
    "v_mov_b32 v61, 0\n\t" \
    "v_mov_b32 v62, 0\n\t" \
    "v_mov_b32 v63, 0\n\t" \
    "v_mov_b32 v80, 0\n\t" \
    "v_mov_b32 v81, 0\n\t" \
    "v_mov_b32 v82, 0\n\t" \
    "v_mov_b32 v83, 0\n\t" \
    "v_mov_b32 v84, 0\n\t" \
    "v_mov_b32 v85, 0\n\t" \
    "v_mov_b32 v86, 0\n\t" \
    "v_mov_b32 v87, 0\n\t" \
    "s_waitcnt lgkmcnt(0)\n\t" \
    "s_memtime s[82:83]\n\t" \
    "s_load_dword s80, %[prb], 0\n\t" \
    "v_mov_b32 v88, 0\n\t" \
    "v_mov_b32 v89, 0\n\t" \
    "v_mov_b32 v90, 0\n\t" \
    "v_mov_b32 v91, 0\n\t" \
    "v_mov_b32 v92, 0\n\t" \
    "v_mov_b32 v93, 0\n\t" \
    "v_mov_b32 v94, 0\n\t" \
    "v_mov_b32 v95, 0\n\t" \
    "v_mov_b32 v96, 0\n\t" \
    "v_mov_b32 v97, 0\n\t" \
    "v_mov_b32 v98, 0\n\t" \
    "v_mov_b32 v99, 0\n\t" \
    "v_mov_b32 v100, 0\n\t" \
    "v_mov_b32 v101, 0\n\t" \
    "v_mov_b32 v102, 0\n\t" \
    "v_mov_b32 v103, 0\n\t" \
    "v_mov_b32 v104, 0\n\t" \
    "v_mov_b32 v105, 0\n\t" \
    "v_mov_b32 v106, 0\n\t" \
    "v_mov_b32 v107, 0\n\t" \
    "v_mov_b32 v108, 0\n\t" \
    "v_mov_b32 v109, 0\n\t" \
    "v_mov_b32 v110, 0\n\t" \
    "v_mov_b32 v111, 0\n\t" \
    "v_mov_b32 v192, 0\n\t" \
    "v_mov_b32 v193, 0\n\t" \
    "v_mov_b32 v194, 0\n\t" \
    "v_mov_b32 v195, 0\n\t" \
    "v_mov_b32 v196, 0\n\t" \
    "v_mov_b32 v197, 0\n\t" \
    "v_mov_b32 v198, 0\n\t" \
    "v_mov_b32 v199, 0\n\t" \
    "v_mov_b32 v200, 0\n\t" \
    "v_mov_b32 v201, 0\n\t" \
    "v_mov_b32 v202, 0\n\t" \
    "v_mov_b32 v203, 0\n\t" \
    "v_mov_b32 v204, 0\n\t" \
    "v_mov_b32 v205, 0\n\t" \
    "v_mov_b32 v206, 0\n\t" \
    "v_mov_b32 v207, 0\n\t" \
    "v_mov_b32 v208, 0\n\t" \
    "v_mov_b32 v209, 0\n\t" \
    "v_mov_b32 v210, 0\n\t" \
    "v_mov_b32 v211, 0\n\t" \
    "v_mov_b32 v212, 0\n\t" \
    "v_mov_b32 v213, 0\n\t" \
    "v_mov_b32 v214, 0\n\t" \
    "v_mov_b32 v215, 0\n\t" \
    "v_mov_b32 v216, 0\n\t" \
    "v_mov_b32 v217, 0\n\t" \
    "v_mov_b32 v218, 0\n\t" \
    "v_mov_b32 v219, 0\n\t" \
    "v_mov_b32 v220, 0\n\t" \
    "v_mov_b32 v221, 0\n\t" \
    "v_mov_b32 v222, 0\n\t" \
    "v_mov_b32 v223, 0\n\t" \
    "s_waitcnt lgkmcnt(0)\n\t" \
    "s_memtime s[84:85]\n\t" \
    "s_waitcnt lgkmcnt(0)\n\t" \
    "s_sub_u32 s82, s84, s82\n\t" \
    "v_mov_b32 v237, s82\n\t" \
    "ds_write_b32 v248, v237 offset:320\n\t" \
    "v_mov_b32 v232, 0x3f803f80\n\t" \
    "v_mov_b32 v233, 0x3f803f80\n\t" \
    "v_mov_b32 v234, 0x3f803f80\n\t" \
    "v_mov_b32 v235, 0x3f803f80\n\t" \
    "v_mov_b32 v251, v240\n\t" \
    "v_xor_b32 v252, 64, v240\n\t" \
    "v_mov_b32 v253, v241\n\t" \
    "v_xor_b32 v254, 32, v241\n\t" \
    "s_mov_b32 s80, 0\n\t" \
    "s_mov_b32 s81, 10\n\t" \
    "s_mov_b32 s90, 0x3fb8aa3b\n\t" \
    "s_mov_b32 s91, 0x3fb8aa3b\n\t" \
    "v_cmp_eq_u32_e64 s[92:93], 2, v250\n\t" \
    "v_cmp_gt_u32_e64 s[94:95], 2, v250\n\t" \
    "v_cmp_eq_u32 vcc, 0, v250\n\t" \
    "v_xor_b32 v250, 4, v248\n\t" \
    "s_mov_b32 s84, 0x140a0014\n\t" \
    "s_lshl_b32 s83, %[xs0], 4\n\t" \
    "s_lshl_b32 s85, %[xs1], 12\n\t" \
    "s_or_b32 s83, s83, s85\n\t" \
    "s_or_b32 s83, s83, 0x14\n\t" \
    "s_waitcnt lgkmcnt(0)\n\t" \
    "s_barrier\n\t" \
    "ds_read_b128 v[128:131], v251\n\t" \
    "ds_read_b128 v[132:135], v251 offset:2048\n\t" \
    "ds_read_b128 v[136:139], v251 offset:4096\n\t" \
    "ds_read_b128 v[140:143], v251 offset:6144\n\t" \
    "ds_read_b64_tr_b16 v[0:1], v253 offset:8192\n\t" \
    "ds_read_b64_tr_b16 v[2:3], v253 offset:8704\n\t" \
    "ds_read_b64_tr_b16 v[4:5], v254 offset:8192\n\t" \
    "ds_read_b64_tr_b16 v[6:7], v254 offset:8704\n\t" \
    "ds_read_b64_tr_b16 v[8:9], v253 offset:16384\n\t" \
    "ds_read_b64_tr_b16 v[10:11], v253 offset:16896\n\t" \
    "ds_read_b64_tr_b16 v[12:13], v254 offset:16384\n\t" \
    "ds_read_b64_tr_b16 v[14:15], v254 offset:16896\n\t" \
    "ds_read_b64_tr_b16 v[16:17], v253\n\t" \
    "ds_read_b64_tr_b16 v[18:19], v253 offset:512\n\t" \
    "ds_read_b64_tr_b16 v[20:21], v254\n\t" \
    "ds_read_b64_tr_b16 v[22:23], v254 offset:512\n\t" \
    ".Lab_l1:\n\t" \
    "s_waitcnt lgkmcnt(0)\n\t" \
    "v_mfma_f32_16x16x32_bf16 v[32:35], v[0:3], v[128:131], v[32:35]\n\t" \
    "ds_read_b128 v[144:147], v252\n\t" \
    "v_mfma_f32_16x16x32_bf16 v[40:43], v[0:3], v[132:135], v[40:43]\n\t" \
    "ds_read_b128 v[148:151], v252 offset:2048\n\t" \
    "v_mfma_f32_16x16x32_bf16 v[48:51], v[0:3], v[136:139], v[48:51]\n\t" \
    "ds_read_b128 v[152:155], v252 offset:4096\n\t" \
    "v_mfma_f32_16x16x32_bf16 v[56:59], v[0:3], v[140:143], v[56:59]\n\t" \
    "ds_read_b128 v[156:159], v252 offset:6144\n\t" \
    "v_mfma_f32_16x16x32_bf16 v[36:39], v[4:7], v[128:131], v[36:39]\n\t" \
    "ds_read_b64_tr_b16 v[112:113], v253 offset:12288\n\t" \
    "v_mfma_f32_16x16x32_bf16 v[44:47], v[4:7], v[132:135], v[44:47]\n\t" \
    "ds_read_b64_tr_b16 v[114:115], v253 offset:12800\n\t" \
    "v_mfma_f32_16x16x32_bf16 v[52:55], v[4:7], v[136:139], v[52:55]\n\t" \
    "ds_read_b64_tr_b16 v[116:117], v254 offset:12288\n\t" \
    "v_mfma_f32_16x16x32_bf16 v[60:63], v[4:7], v[140:143], v[60:63]\n\t" \
    "ds_read_b64_tr_b16 v[118:119], v254 offset:12800\n\t" \
    "v_mfma_f32_16x16x32_bf16 v[80:83], v[128:131], v[8:11], v[80:83]\n\t" \
    "ds_read_b64_tr_b16 v[120:121], v253 offset:20480\n\t" \
    "v_mfma_f32_16x16x32_bf16 v[88:91], v[132:135], v[8:11], v[88:91]\n\t" \
    "ds_read_b64_tr_b16 v[122:123], v253 offset:20992\n\t" \
    "v_mfma_f32_16x16x32_bf16 v[96:99], v[136:139], v[8:11], v[96:99]\n\t" \
    "ds_read_b64_tr_b16 v[124:125], v254 offset:20480\n\t" \
    "v_mfma_f32_16x16x32_bf16 v[104:107], v[140:143], v[8:11], v[104:107]\n\t" \
    "ds_read_b64_tr_b16 v[126:127], v254 offset:20992\n\t" \
    "v_mfma_f32_16x16x32_bf16 v[84:87], v[128:131], v[12:15], v[84:87]\n\t" \
    "ds_read_b64_tr_b16 v[24:25], v253 offset:4096\n\t" \
    "v_mfma_f32_16x16x32_bf16 v[92:95], v[132:135], v[12:15], v[92:95]\n\t" \
    "ds_read_b64_tr_b16 v[26:27], v253 offset:4608\n\t" \
    "v_mfma_f32_16x16x32_bf16 v[100:103], v[136:139], v[12:15], v[100:103]\n\t" \
    "ds_read_b64_tr_b16 v[28:29], v254 offset:4096\n\t" \
    "v_mfma_f32_16x16x32_bf16 v[108:111], v[140:143], v[12:15], v[108:111]\n\t" \
    "ds_read_b64_tr_b16 v[30:31], v254 offset:4608\n\t" \
    "v_mfma_f32_16x16x32_bf16 v[192:195], v[16:19], v[128:131], v[192:195]\n\t" \
    "s_add_u32 s85, s80, 40960\n\t" \
    "s_cmp_lg_u32 s80, 81920\n\t" \
    "v_mfma_f32_16x16x32_bf16 v[200:203], v[16:19], v[132:135], v[200:203]\n\t" \
    "s_cselect_b32 s80, s85, 0\n\t" \
    "v_mfma_f32_16x16x32_bf16 v[208:211], v[16:19], v[136:139], v[208:211]\n\t" \
    "v_add_u32 v251, s80, v240\n\t" \
    "v_mfma_f32_16x16x32_bf16 v[216:219], v[16:19], v[140:143], v[216:219]\n\t" \
    "v_add_u32 v253, s80, v241\n\t" \
    "v_mfma_f32_16x16x32_bf16 v[196:199], v[20:23], v[128:131], v[196:199]\n\t" \
    "v_xor_b32 v254, 32, v253\n\t" \
    "v_mfma_f32_16x16x32_bf16 v[204:207], v[20:23], v[132:135], v[204:207]\n\t" \
    "v_mfma_f32_16x16x32_bf16 v[212:215], v[20:23], v[136:139], v[212:215]\n\t" \
    "v_mfma_f32_16x16x32_bf16 v[220:223], v[20:23], v[140:143], v[220:223]\n\t" \
    "s_waitcnt lgkmcnt(0)\n\t" \
    "s_barrier\n\t" \
    "v_mfma_f32_16x16x32_bf16 v[32:35], v[112:115], v[144:147], v[32:35]\n\t" \
    "ds_read_b128 v[128:131], v251\n\t" \
    "v_mfma_f32_16x16x32_bf16 v[40:43], v[112:115], v[148:151], v[40:43]\n\t" \
    "ds_read_b128 v[132:135], v251 offset:2048\n\t" \
    "v_mfma_f32_16x16x32_bf16 v[48:51], v[112:115], v[152:155], v[48:51]\n\t" \
    "ds_read_b128 v[136:139], v251 offset:4096\n\t" \
    "v_mfma_f32_16x16x32_bf16 v[56:59], v[112:115], v[156:159], v[56:59]\n\t" \
    "ds_read_b128 v[140:143], v251 offset:6144\n\t" \
    "v_mfma_f32_16x16x32_bf16 v[36:39], v[116:119], v[144:147], v[36:39]\n\t" \
    "ds_read_b64_tr_b16 v[0:1], v253 offset:8192\n\t" \
    "v_mfma_f32_16x16x32_bf16 v[44:47], v[116:119], v[148:151], v[44:47]\n\t" \
    "ds_read_b64_tr_b16 v[2:3], v253 offset:8704\n\t" \
    "v_mfma_f32_16x16x32_bf16 v[52:55], v[116:119], v[152:155], v[52:55]\n\t" \
    "ds_read_b64_tr_b16 v[4:5], v254 offset:8192\n\t" \
    "v_mfma_f32_16x16x32_bf16 v[60:63], v[116:119], v[156:159], v[60:63]\n\t" \
    "ds_read_b64_tr_b16 v[6:7], v254 offset:8704\n\t" \
    "v_mfma_f32_16x16x32_bf16 v[80:83], v[144:147], v[120:123], v[80:83]\n\t" \
    "ds_read_b64_tr_b16 v[8:9], v253 offset:16384\n\t" \
    "v_mfma_f32_16x16x32_bf16 v[88:91], v[148:151], v[120:123], v[88:91]\n\t" \
    "ds_read_b64_tr_b16 v[10:11], v253 offset:16896\n\t" \
    "v_mfma_f32_16x16x32_bf16 v[96:99], v[152:155], v[120:123], v[96:99]\n\t" \
    "ds_read_b64_tr_b16 v[12:13], v254 offset:16384\n\t" \
    "v_mfma_f32_16x16x32_bf16 v[104:107], v[156:159], v[120:123], v[104:107]\n\t" \
    "ds_read_b64_tr_b16 v[14:15], v254 offset:16896\n\t" \
    "v_mfma_f32_16x16x32_bf16 v[84:87], v[144:147], v[124:127], v[84:87]\n\t" \
    "ds_read_b64_tr_b16 v[16:17], v253\n\t" \
    "v_mfma_f32_16x16x32_bf16 v[92:95], v[148:151], v[124:127], v[92:95]\n\t" \
    "ds_read_b64_tr_b16 v[18:19], v253 offset:512\n\t" \
    "v_mfma_f32_16x16x32_bf16 v[100:103], v[152:155], v[124:127], v[100:103]\n\t" \
    "ds_read_b64_tr_b16 v[20:21], v254\n\t" \
    "v_mfma_f32_16x16x32_bf16 v[108:111], v[156:159], v[124:127], v[108:111]\n\t" \
    "ds_read_b64_tr_b16 v[22:23], v254 offset:512\n\t" \
    "v_mfma_f32_16x16x32_bf16 v[192:195], v[24:27], v[144:147], v[192:195]\n\t" \
    "v_xor_b32 v252, 64, v251\n\t" \
    "v_mfma_f32_16x16x32_bf16 v[200:203], v[24:27], v[148:151], v[200:203]\n\t" \
    "v_mfma_f32_16x16x32_bf16 v[208:211], v[24:27], v[152:155], v[208:211]\n\t" \
    "s_sub_u32 s81, s81, 1\n\t" \
    "v_mfma_f32_16x16x32_bf16 v[216:219], v[24:27], v[156:159], v[216:219]\n\t" \
    "s_cmp_lg_u32 s81, 0\n\t" \
    "v_mfma_f32_16x16x32_bf16 v[196:199], v[28:31], v[144:147], v[196:199]\n\t" \
    "v_mfma_f32_16x16x32_bf16 v[204:207], v[28:31], v[148:151], v[204:207]\n\t" \
    "v_mfma_f32_16x16x32_bf16 v[212:215], v[28:31], v[152:155], v[212:215]\n\t" \
    "v_mfma_f32_16x16x32_bf16 v[220:223], v[28:31], v[156:159], v[220:223]\n\t" \
    "s_cbranch_scc1 .Lab_l1\n\t" \
    ".Lab_l2:\n\t" \
    "s_waitcnt lgkmcnt(0)\n\t" \
    "v_mfma_f32_16x16x32_bf16 v[32:35], v[0:3], v[128:131], v[32:35]\n\t" \
    "ds_read_b128 v[144:147], v252\n\t" \
    "ds_read_b64_tr_b16 v[112:113], v253 offset:12288\n\t" \
    "v_mfma_f32_16x16x32_bf16 v[40:43], v[0:3], v[132:135], v[40:43]\n\t" \
    "ds_read_b128 v[148:151], v252 offset:2048\n\t" \
    "ds_read_b64_tr_b16 v[114:115], v253 offset:12800\n\t" \
    "v_mfma_f32_16x16x32_bf16 v[48:51], v[0:3], v[136:139], v[48:51]\n\t" \
    "ds_read_b128 v[152:155], v252 offset:4096\n\t" \
    "s_and_b32 s80, s84, 255\n\t" \
    "v_mfma_f32_16x16x32_bf16 v[56:59], v[0:3], v[140:143], v[56:59]\n\t" \
    "ds_read_b128 v[156:159], v252 offset:6144\n\t" \
    "s_and_b32 s82, s83, 255\n\t" \
    "v_mfma_f32_16x16x32_bf16 v[36:39], v[4:7], v[128:131], v[36:39]\n\t" \
    "ds_read_b64_tr_b16 v[116:117], v254 offset:12288\n\t" \
    "s_lshl_b32 s80, s80, 12\n\t" \
    "v_mfma_f32_16x16x32_bf16 v[44:47], v[4:7], v[132:135], v[44:47]\n\t" \
    "ds_read_b64_tr_b16 v[118:119], v254 offset:12800\n\t" \
    "s_lshl_b32 s82, s82, 12\n\t" \
    "v_mfma_f32_16x16x32_bf16 v[52:55], v[4:7], v[136:139], v[52:55]\n\t" \
    "ds_read_b64_tr_b16 v[120:121], v253 offset:20480\n\t" \
    "v_mfma_f32_16x16x32_bf16 v[60:63], v[4:7], v[140:143], v[60:63]\n\t" \
    "ds_read_b64_tr_b16 v[122:123], v253 offset:20992\n\t" \
    "v_mfma_f32_16x16x32_bf16 v[80:83], v[128:131], v[8:11], v[80:83]\n\t" \
    "ds_read_b64_tr_b16 v[124:125], v254 offset:20480\n\t" \
    "v_mfma_f32_16x16x32_bf16 v[88:91], v[132:135], v[8:11], v[88:91]\n\t" \
    "ds_read_b64_tr_b16 v[126:127], v254 offset:20992\n\t" \
    "v_mfma_f32_16x16x32_bf16 v[96:99], v[136:139], v[8:11], v[96:99]\n\t" \
    "v_add_u32 v251, s82, v240\n\t" \
    "v_mfma_f32_16x16x32_bf16 v[104:107], v[140:143], v[8:11], v[104:107]\n\t" \
    "v_add_u32 v253, s80, v241\n\t" \
    "v_mfma_f32_16x16x32_bf16 v[84:87], v[128:131], v[12:15], v[84:87]\n\t" \
    "v_xor_b32 v254, 32, v253\n\t" \
    "v_mfma_f32_16x16x32_bf16 v[92:95], v[132:135], v[12:15], v[92:95]\n\t" \
    "v_mfma_f32_16x16x32_bf16 v[100:103], v[136:139], v[12:15], v[100:103]\n\t" \
    "v_mfma_f32_16x16x32_bf16 v[108:111], v[140:143], v[12:15], v[108:111]\n\t" \
    "s_waitcnt lgkmcnt(0)\n\t" \
    "s_barrier\n\t" \
    "v_mfma_f32_16x16x32_bf16 v[32:35], v[112:115], v[144:147], v[32:35]\n\t" \
    "ds_read_b128 v[128:131], v251\n\t" \
    "ds_read_b64_tr_b16 v[0:1], v253 offset:8192\n\t" \
    "v_mfma_f32_16x16x32_bf16 v[40:43], v[112:115], v[148:151], v[40:43]\n\t" \
    "ds_read_b128 v[132:135], v251 offset:2048\n\t" \
    "ds_read_b64_tr_b16 v[2:3], v253 offset:8704\n\t" \
    "v_mfma_f32_16x16x32_bf16 v[48:51], v[112:115], v[152:155], v[48:51]\n\t" \
    "ds_read_b128 v[136:139], v251 offset:4096\n\t" \
    "v_mfma_f32_16x16x32_bf16 v[56:59], v[112:115], v[156:159], v[56:59]\n\t" \
    "ds_read_b128 v[140:143], v251 offset:6144\n\t" \
    "v_mfma_f32_16x16x32_bf16 v[36:39], v[116:119], v[144:147], v[36:39]\n\t" \
    "ds_read_b64_tr_b16 v[4:5], v254 offset:8192\n\t" \
    "v_mfma_f32_16x16x32_bf16 v[44:47], v[116:119], v[148:151], v[44:47]\n\t" \
    "ds_read_b64_tr_b16 v[6:7], v254 offset:8704\n\t" \
    "v_mfma_f32_16x16x32_bf16 v[52:55], v[116:119], v[152:155], v[52:55]\n\t" \
    "ds_read_b64_tr_b16 v[8:9], v253 offset:16384\n\t" \
    "v_mfma_f32_16x16x32_bf16 v[60:63], v[116:119], v[156:159], v[60:63]\n\t" \
    "ds_read_b64_tr_b16 v[10:11], v253 offset:16896\n\t" \
    "v_mfma_f32_16x16x32_bf16 v[80:83], v[144:147], v[120:123], v[80:83]\n\t" \
    "ds_read_b64_tr_b16 v[12:13], v254 offset:16384\n\t" \
    "v_mfma_f32_16x16x32_bf16 v[88:91], v[148:151], v[120:123], v[88:91]\n\t" \
    "ds_read_b64_tr_b16 v[14:15], v254 offset:16896\n\t" \
    "v_mfma_f32_16x16x32_bf16 v[96:99], v[152:155], v[120:123], v[96:99]\n\t" \
    "v_xor_b32 v252, 64, v251\n\t" \
    "v_mfma_f32_16x16x32_bf16 v[104:107], v[156:159], v[120:123], v[104:107]\n\t" \
    "s_lshr_b32 s83, s83, 8\n\t" \
    "v_mfma_f32_16x16x32_bf16 v[84:87], v[144:147], v[124:127], v[84:87]\n\t" \
    "s_lshr_b32 s84, s84, 8\n\t" \
    "v_mfma_f32_16x16x32_bf16 v[92:95], v[148:151], v[124:127], v[92:95]\n\t" \
    "v_mfma_f32_16x16x32_bf16 v[100:103], v[152:155], v[124:127], v[100:103]\n\t" \
    "v_mfma_f32_16x16x32_bf16 v[108:111], v[156:159], v[124:127], v[108:111]\n\t" \
    "s_cbranch_scc1 .Lab_l2\n\t" \
    "s_waitcnt lgkmcnt(0)\n\t" \
    "v_mfma_f32_16x16x32_bf16 v[32:35], v[0:3], v[128:131], v[32:35]\n\t" \
    "ds_read_b128 v[144:147], v252\n\t" \
    "ds_read_b64_tr_b16 v[112:113], v253 offset:12288\n\t" \
    "v_mfma_f32_16x16x32_bf16 v[40:43], v[0:3], v[132:135], v[40:43]\n\t" \
    "ds_read_b128 v[148:151], v252 offset:2048\n\t" \
    "ds_read_b64_tr_b16 v[114:115], v253 offset:12800\n\t" \
    "v_mfma_f32_16x16x32_bf16 v[48:51], v[0:3], v[136:139], v[48:51]\n\t" \
    "ds_read_b128 v[152:155], v252 offset:4096\n\t" \
    "ds_read_b64_tr_b16 v[116:117], v254 offset:12288\n\t" \
    "v_mfma_f32_16x16x32_bf16 v[56:59], v[0:3], v[140:143], v[56:59]\n\t" \
    "ds_read_b128 v[156:159], v252 offset:6144\n\t" \
    "ds_read_b64_tr_b16 v[118:119], v254 offset:12800\n\t" \
    "v_mfma_f32_16x16x32_bf16 v[36:39], v[4:7], v[128:131], v[36:39]\n\t" \
    "ds_read_b64_tr_b16 v[120:121], v253 offset:20480\n\t" \
    "ds_read_b64_tr_b16 v[122:123], v253 offset:20992\n\t" \
    "v_mfma_f32_16x16x32_bf16 v[44:47], v[4:7], v[132:135], v[44:47]\n\t" \
    "ds_read_b64_tr_b16 v[124:125], v254 offset:20480\n\t" \
    "ds_read_b64_tr_b16 v[126:127], v254 offset:20992\n\t" \
    "v_mfma_f32_16x16x32_bf16 v[52:55], v[4:7], v[136:139], v[52:55]\n\t" \
    "ds_read_b64_tr_b16 v[224:225], v245\n\t" \
    "ds_read_b64_tr_b16 v[226:227], v245 offset:1024\n\t" \
    "v_mfma_f32_16x16x32_bf16 v[60:63], v[4:7], v[140:143], v[60:63]\n\t" \
    "v_xor_b32 v245, 32, v245\n\t" \
    "ds_read_b32 v236, v249 offset:288\n\t" \
    "v_mfma_f32_16x16x32_bf16 v[80:83], v[128:131], v[8:11], v[80:83]\n\t" \
    "ds_read_b64_tr_b16 v[228:229], v245\n\t" \
    "ds_read_b64_tr_b16 v[230:231], v245 offset:1024\n\t" \
    "v_mfma_f32_16x16x32_bf16 v[88:91], v[132:135], v[8:11], v[88:91]\n\t" \
    "ds_read_b128 v[68:71], v249 offset:320\n\t" \
    "ds_read_b64 v[64:65], v249 offset:0\n\t" \
    "v_mfma_f32_16x16x32_bf16 v[96:99], v[136:139], v[8:11], v[96:99]\n\t" \
    "v_add_u32 v251, 131072, v240\n\t" \
    "v_mfma_f32_16x16x32_bf16 v[104:107], v[140:143], v[8:11], v[104:107]\n\t" \
    "v_xor_b32 v254, 32, v241\n\t" \
    "v_mfma_f32_16x16x32_bf16 v[84:87], v[128:131], v[12:15], v[84:87]\n\t" \
    "v_mov_b32 v253, v241\n\t" \
    "v_mfma_f32_16x16x32_bf16 v[92:95], v[132:135], v[12:15], v[92:95]\n\t" \
    "v_xor_b32 v252, 64, v251\n\t" \
    "v_mfma_f32_16x16x32_bf16 v[100:103], v[136:139], v[12:15], v[100:103]\n\t" \
    "v_mfma_f32_16x16x32_bf16 v[108:111], v[140:143], v[12:15], v[108:111]\n\t" \
    "s_waitcnt lgkmcnt(0)\n\t" \
    "v_mfma_f32_16x16x32_bf16 v[32:35], v[112:115], v[144:147], v[32:35]\n\t" \
    "v_min_u32 v64, v64, v65\n\t" \
    "v_mfma_f32_16x16x32_bf16 v[40:43], v[112:115], v[148:151], v[40:43]\n\t" \
    "v_readfirstlane_b32 s85, v64\n\t" \
    "s_cmp_lt_u32 s85, 1\n\t" \
    "s_cbranch_scc1 .Lab_s1\n\t" \
    ".Lab_s1r:\n\t" \
    "v_mfma_f32_16x16x32_bf16 v[48:51], v[112:115], v[152:155], v[48:51]\n\t" \
    "ds_read_b128 v[160:163], v251\n\t" \
    "ds_read_b64_tr_b16 v[0:1], v253 offset:8192\n\t" \
    "v_mfma_f32_16x16x32_bf16 v[56:59], v[112:115], v[156:159], v[56:59]\n\t" \
    "ds_read_b128 v[164:167], v251 offset:2048\n\t" \
    "ds_read_b64_tr_b16 v[2:3], v253 offset:8704\n\t" \
    "v_mfma_f32_16x16x32_bf16 v[36:39], v[116:119], v[144:147], v[36:39]\n\t" \
    "ds_read_b128 v[168:171], v251 offset:4096\n\t" \
    "ds_read_b64_tr_b16 v[4:5], v254 offset:8192\n\t" \
    "v_mfma_f32_16x16x32_bf16 v[44:47], v[116:119], v[148:151], v[44:47]\n\t" \
    "ds_read_b128 v[172:175], v251 offset:6144\n\t" \
    "ds_read_b64_tr_b16 v[6:7], v254 offset:8704\n\t" \
    "v_mfma_f32_16x16x32_bf16 v[52:55], v[116:119], v[152:155], v[52:55]\n\t" \
    "ds_read_b64_tr_b16 v[8:9], v253 offset:16384\n\t" \
    "ds_read_b64_tr_b16 v[10:11], v253 offset:16896\n\t" \
    "v_mfma_f32_16x16x32_bf16 v[60:63], v[116:119], v[156:159], v[60:63]\n\t" \
    "ds_read_b64_tr_b16 v[12:13], v254 offset:16384\n\t" \
    "ds_read_b64_tr_b16 v[14:15], v254 offset:16896\n\t" \
    "v_mfma_f32_16x16x32_bf16 v[80:83], v[144:147], v[120:123], v[80:83]\n\t" \
    "v_readfirstlane_b32 s87, v68\n\t" \
    "v_mfma_f32_16x16x32_bf16 v[88:91], v[148:151], v[120:123], v[88:91]\n\t" \
    "v_readfirstlane_b32 s88, v69\n\t" \
    "v_mfma_f32_16x16x32_bf16 v[96:99], v[152:155], v[120:123], v[96:99]\n\t" \
    "v_readfirstlane_b32 %[sof], v70\n\t" \
    "v_mfma_f32_16x16x32_bf16 v[104:107], v[156:159], v[120:123], v[104:107]\n\t" \
    "s_xor_b32 s87, s87, %[kpx]\n\t" \
    "s_add_u32 s88, s88, %[kma]\n\t" \
    "v_mfma_f32_16x16x32_bf16 v[84:87], v[144:147], v[124:127], v[84:87]\n\t" \
    "s_add_u32 s87, s87, %[kpa]\n\t" \
    "v_mfma_f32_16x16x32_bf16 v[92:95], v[148:151], v[124:127], v[92:95]\n\t" \
    "s_xor_b32 s86, s87, 0x8000\n\t" \
    "v_mfma_f32_16x16x32_bf16 v[100:103], v[152:155], v[124:127], v[100:103]\n\t" \
    "v_mfma_f32_16x16x32_bf16 v[108:111], v[156:159], v[124:127], v[108:111]\n\t" \
    "s_waitcnt lgkmcnt(0)\n\t" \
    "v_mfma_f32_16x16x32_bf16 v[32:35], v[0:3], v[160:163], v[32:35]\n\t" \
    "ds_read_b128 v[176:179], v252\n\t" \
    "ds_read_b64_tr_b16 v[112:113], v253 offset:12288\n\t" \
    "v_mfma_f32_16x16x32_bf16 v[40:43], v[0:3], v[164:167], v[40:43]\n\t" \
    "ds_read_b128 v[180:183], v252 offset:2048\n\t" \
    "ds_read_b64_tr_b16 v[114:115], v253 offset:12800\n\t" \
    "v_mfma_f32_16x16x32_bf16 v[48:51], v[0:3], v[168:171], v[48:51]\n\t" \
    "ds_read_b128 v[184:187], v252 offset:4096\n\t" \
    "v_mov_b32 v237, 2\n\t" \
    "v_mfma_f32_16x16x32_bf16 v[56:59], v[0:3], v[172:175], v[56:59]\n\t" \
    "ds_read_b128 v[188:191], v252 offset:6144\n\t" \
    "v_mfma_f32_16x16x32_bf16 v[36:39], v[4:7], v[160:163], v[36:39]\n\t" \
    "ds_read_b64_tr_b16 v[116:117], v254 offset:12288\n\t" \
    "v_mfma_f32_16x16x32_bf16 v[44:47], v[4:7], v[164:167], v[44:47]\n\t" \
    "ds_read_b64_tr_b16 v[118:119], v254 offset:12800\n\t" \
    "v_mfma_f32_16x16x32_bf16 v[52:55], v[4:7], v[168:171], v[52:55]\n\t" \
    "ds_read_b64_tr_b16 v[120:121], v253 offset:20480\n\t" \
    "v_mfma_f32_16x16x32_bf16 v[60:63], v[4:7], v[172:175], v[60:63]\n\t" \
    "ds_read_b64_tr_b16 v[122:123], v253 offset:20992\n\t" \
    "v_mfma_f32_16x16x32_bf16 v[80:83], v[160:163], v[8:11], v[80:83]\n\t" \
    "ds_read_b64_tr_b16 v[124:125], v254 offset:20480\n\t" \
    "v_mfma_f32_16x16x32_bf16 v[88:91], v[164:167], v[8:11], v[88:91]\n\t" \
    "ds_read_b64_tr_b16 v[126:127], v254 offset:20992\n\t" \
    "v_mfma_f32_16x16x32_bf16 v[96:99], v[168:171], v[8:11], v[96:99]\n\t" \
    "ds_write_b32 v248, v237\n\t" \
    "v_mfma_f32_16x16x32_bf16 v[104:107], v[172:175], v[8:11], v[104:107]\n\t" \
    "v_xor_b32 v251, 32, v243\n\t" \
    "v_mfma_f32_16x16x32_bf16 v[84:87], v[160:163], v[12:15], v[84:87]\n\t" \
    "v_xor_b32 v252, 64, v243\n\t" \
    "v_mfma_f32_16x16x32_bf16 v[92:95], v[164:167], v[12:15], v[92:95]\n\t" \
    "v_xor_b32 v253, 0x60, v243\n\t" \
    "v_mfma_f32_16x16x32_bf16 v[100:103], v[168:171], v[12:15], v[100:103]\n\t" \
    "s_setprio 3\n\t" \
    "v_mfma_f32_16x16x32_bf16 v[108:111], v[172:175], v[12:15], v[108:111]\n\t" \
    "s_waitcnt lgkmcnt(0)\n\t" \
    "v_mfma_f32_16x16x32_bf16 v[32:35], v[112:115], v[176:179], v[32:35]\n\t" \
    "v_mfma_f32_16x16x32_bf16 v[36:39], v[116:119], v[176:179], v[36:39]\n\t" \
    "v_mfma_f32_16x16x32_bf16 v[40:43], v[112:115], v[180:183], v[40:43]\n\t" \
    "v_mfma_f32_16x16x32_bf16 v[44:47], v[116:119], v[180:183], v[44:47]\n\t" \
    "ds_read_b128 v[28:31], v249 offset:16\n\t" \
    "v_mfma_f32_16x16x32_bf16 v[48:51], v[112:115], v[184:187], v[48:51]\n\t" \
    "v_mfma_f32_16x16x32_bf16 v[52:55], v[116:119], v[184:187], v[52:55]\n\t" \
    "v_mfma_f32_16x16x32_bf16 v[56:59], v[112:115], v[188:191], v[56:59]\n\t" \
    "v_mfma_f32_16x16x32_bf16 v[60:63], v[116:119], v[188:191], v[60:63]\n\t" \
    "v_cvt_pk_bf16_f32 v64, v32, v33\n\t" \
    "v_cvt_pk_bf16_f32 v65, v34, v35\n\t" \
    "v_mfma_f32_16x16x32_bf16 v[80:83], v[176:179], v[120:123], v[80:83]\n\t" \
    "v_cvt_pk_bf16_f32 v66, v36, v37\n\t" \
    "v_cvt_pk_bf16_f32 v67, v38, v39\n\t" \
    "v_mfma_f32_16x16x32_bf16 v[88:91], v[180:183], v[120:123], v[88:91]\n\t" \
    "v_cvt_pk_bf16_f32 v68, v40, v41\n\t" \
    "v_cvt_pk_bf16_f32 v69, v42, v43\n\t" \
    "v_mfma_f32_16x16x32_bf16 v[84:87], v[176:179], v[124:127], v[84:87]\n\t" \
    "v_cvt_pk_bf16_f32 v70, v44, v45\n\t" \
    "v_cvt_pk_bf16_f32 v71, v46, v47\n\t" \
    "v_mfma_f32_16x16x32_bf16 v[92:95], v[180:183], v[124:127], v[92:95]\n\t" \
    "v_cvt_pk_bf16_f32 v72, v48, v49\n\t" \
    "v_cvt_pk_bf16_f32 v73, v50, v51\n\t" \
    "v_mfma_f32_16x16x32_bf16 v[96:99], v[184:187], v[120:123], v[96:99]\n\t" \
    "v_cvt_pk_bf16_f32 v74, v52, v53\n\t" \
    "v_cvt_pk_bf16_f32 v75, v54, v55\n\t" \
    "v_mfma_f32_16x16x32_bf16 v[104:107], v[188:191], v[120:123], v[104:107]\n\t" \
    "v_cvt_pk_bf16_f32 v76, v56, v57\n\t" \
    "v_cvt_pk_bf16_f32 v77, v58, v59\n\t" \
    "v_mfma_f32_16x16x32_bf16 v[100:103], v[184:187], v[124:127], v[100:103]\n\t" \
    "v_cvt_pk_bf16_f32 v78, v60, v61\n\t" \
    "v_cvt_pk_bf16_f32 v79, v62, v63\n\t" \
    "v_mfma_f32_16x16x32_bf16 v[108:111], v[188:191], v[124:127], v[108:111]\n\t" \
    "s_waitcnt lgkmcnt(0)\n\t" \
    "v_min_u32 v238, v28, v29\n\t" \
    "v_min3_u32 v238, v238, v30, v31\n\t" \
    "v_mfma_f32_16x16x32_bf16 v[0:3], v[224:227], v[64:67], 0\n\t" \
    "v_cvt_pk_bf16_f32 v112, v80, v81\n\t" \
    "v_cvt_pk_bf16_f32 v113, v82, v83\n\t" \
    "v_mfma_f32_16x16x32_bf16 v[4:7], v[228:231], v[64:67], 0\n\t" \
    "v_cvt_pk_bf16_f32 v114, v88, v89\n\t" \
    "v_cvt_pk_bf16_f32 v115, v90, v91\n\t" \
    "v_mfma_f32_16x16x32_bf16 v[8:11], v[224:227], v[68:71], 0\n\t" \
    "v_cvt_pk_bf16_f32 v116, v84, v85\n\t" \
    "v_cvt_pk_bf16_f32 v117, v86, v87\n\t" \
    "v_mfma_f32_16x16x32_bf16 v[12:15], v[228:231], v[68:71], 0\n\t" \
    "v_cvt_pk_bf16_f32 v118, v92, v93\n\t" \
    "v_cvt_pk_bf16_f32 v119, v94, v95\n\t" \
    "v_mfma_f32_16x16x32_bf16 v[16:19], v[224:227], v[72:75], 0\n\t" \
    "v_cvt_pk_bf16_f32 v120, v96, v97\n\t" \
    "v_cvt_pk_bf16_f32 v121, v98, v99\n\t" \
    "v_mfma_f32_16x16x32_bf16 v[20:23], v[228:231], v[72:75], 0\n\t" \
    "v_cvt_pk_bf16_f32 v122, v104, v105\n\t" \
    "v_cvt_pk_bf16_f32 v123, v106, v107\n\t" \
    "v_mfma_f32_16x16x32_bf16 v[24:27], v[224:227], v[76:79], 0\n\t" \
    "v_cvt_pk_bf16_f32 v124, v100, v101\n\t" \
    "v_cvt_pk_bf16_f32 v125, v102, v103\n\t" \
    "v_mfma_f32_16x16x32_bf16 v[28:31], v[228:231], v[76:79], 0\n\t" \
    "v_cvt_pk_bf16_f32 v126, v108, v109\n\t" \
    "v_cvt_pk_bf16_f32 v127, v110, v111\n\t" \
    "v_readfirstlane_b32 s85, v238\n\t" \
    "s_cmp_lt_u32 s85, 2\n\t" \
    "s_cbranch_scc1 .Lab_s2\n\t" \
    ".Lab_s2r:\n\t" \
    "ds_write_b128 v242, v[112:115]\n\t" \
    "v_max3_f32 v64, v0, v1, v2\n\t" \
    "v_max3_f32 v66, -v0, -v1, -v2\n\t" \
    "v_max3_f32 v68, v8, v9, v10\n\t" \
    "v_max3_f32 v64, v64, v3, v4\n\t" \
    "v_max3_f32 v66, v66, -v3, -v4\n\t" \
    "v_max3_f32 v68, v68, v11, v12\n\t" \
    "v_max3_f32 v64, v64, v5, v6\n\t" \
    "v_max3_f32 v66, v66, -v5, -v6\n\t" \
    "ds_write_b128 v242, v[116:119] offset:1024\n\t" \
    "v_max3_f32 v68, v68, v13, v14\n\t" \
    "v_max_f32_e64 v64, v64, v7\n\t" \
    "v_max_f32_e64 v66, v66, -v7\n\t" \
    "v_max_f32_e64 v68, v68, v15\n\t" \
    "v_max3_f32 v70, -v8, -v9, -v10\n\t" \
    "v_permlane32_swap_b32 v64, v66\n\t" \
    "v_max3_f32 v72, v16, v17, v18\n\t" \
    "v_max3_f32 v70, v70, -v11, -v12\n\t" \
    "ds_write_b128 v242, v[120:123] offset:4096\n\t" \
    "v_max_f32 v64, v64, v66\n\t" \
    "v_max3_f32 v72, v72, v19, v20\n\t" \
    "v_max3_f32 v70, v70, -v13, -v14\n\t" \
    "v_max3_f32 v74, -v16, -v17, -v18\n\t" \
    "v_max3_f32 v72, v72, v21, v22\n\t" \
    "v_max_f32_e64 v70, v70, -v15\n\t" \
    "v_max3_f32 v74, v74, -v19, -v20\n\t" \
    "v_max_f32_e64 v72, v72, v23\n\t" \
    "ds_write_b128 v242, v[124:127] offset:5120\n\t" \
    "v_permlane32_swap_b32 v68, v70\n\t" \
    "v_max3_f32 v74, v74, -v21, -v22\n\t" \
    "v_max3_f32 v76, v24, v25, v26\n\t" \
    "v_max_f32 v68, v68, v70\n\t" \
    "v_max_f32_e64 v74, v74, -v23\n\t" \
    "v_max3_f32 v76, v76, v27, v28\n\t" \
    "v_permlane16_swap_b32 v64, v68\n\t" \
    "v_permlane32_swap_b32 v72, v74\n\t" \
    "v_max3_f32 v76, v76, v29, v30\n\t" \
    "v_max_f32 v66, v64, v68\n\t" \
    "v_max_f32 v72, v72, v74\n\t" \
    "v_max_f32_e64 v76, v76, v31\n\t" \
    "v_xor_b32 v254, 32, v244\n\t" \
    "v_mul_f32 v64, 0xbfb8aa3b, v66\n\t" \
    "v_mul_f32 v68, 0xbfb8aa3b, v66\n\t" \
    "v_xor_b32 v245, 0x2000, v244\n\t" \
    "v_max3_f32 v78, -v24, -v25, -v26\n\t" \
    "v_max3_f32 v78, v78, -v27, -v28\n\t" \
    "v_xor_b32 v255, 0x2020, v244\n\t" \
    "v_permlane16_swap_b32 v64, v68\n\t" \
    "v_mov_b32 v66, v64\n\t" \
    "v_xor_b32 v242, 0x2000, v242\n\t" \
    "v_max3_f32 v78, v78, -v29, -v30\n\t" \
    "v_mov_b32 v70, v68\n\t" \
    "v_permlane32_swap_b32 v64, v66\n\t" \
    "v_max_f32_e64 v78, v78, -v31\n\t" \
    "v_permlane32_swap_b32 v68, v70\n\t" \
    "v_pk_fma_f32 v[32:33], v[0:1], s[90:91], v[64:65] op_sel_hi:[1,1,0]\n\t" \
    "v_permlane32_swap_b32 v76, v78\n\t" \
    "v_pk_fma_f32 v[0:1], v[0:1], s[90:91], v[66:67] op_sel_hi:[1,1,0] neg_lo:[1,0,0] neg_hi:[1,0,0]\n\t" \
    "v_pk_fma_f32 v[34:35], v[2:3], s[90:91], v[64:65] op_sel_hi:[1,1,0]\n\t" \
    "v_max_f32 v76, v76, v78\n\t" \
    "v_pk_fma_f32 v[2:3], v[2:3], s[90:91], v[66:67] op_sel_hi:[1,1,0] neg_lo:[1,0,0] neg_hi:[1,0,0]\n\t" \
    "v_pk_fma_f32 v[36:37], v[4:5], s[90:91], v[64:65] op_sel_hi:[1,1,0]\n\t" \
    "v_permlane16_swap_b32 v72, v76\n\t" \
    "v_pk_fma_f32 v[4:5], v[4:5], s[90:91], v[66:67] op_sel_hi:[1,1,0] neg_lo:[1,0,0] neg_hi:[1,0,0]\n\t" \
    "v_pk_fma_f32 v[38:39], v[6:7], s[90:91], v[64:65] op_sel_hi:[1,1,0]\n\t" \
    "v_max_f32 v74, v72, v76\n\t" \
    "v_pk_fma_f32 v[6:7], v[6:7], s[90:91], v[66:67] op_sel_hi:[1,1,0] neg_lo:[1,0,0] neg_hi:[1,0,0]\n\t" \
    "v_exp_f32 v32, v32\n\t" \
    "v_mul_f32 v72, 0xbfb8aa3b, v74\n\t" \
    "v_mul_f32 v76, 0xbfb8aa3b, v74\n\t" \
    "v_exp_f32 v0, v0\n\t" \
    "v_exp_f32 v33, v33\n\t" \
    "v_permlane16_swap_b32 v72, v76\n\t" \
    "v_exp_f32 v1, v1\n\t" \
    "v_exp_f32 v34, v34\n\t" \
    "v_mov_b32 v74, v72\n\t" \
    "v_mov_b32 v78, v76\n\t" \
    "v_exp_f32 v2, v2\n\t" \
    "v_permlane32_swap_b32 v72, v74\n\t" \
    "v_permlane32_swap_b32 v76, v78\n\t" \
    "v_exp_f32 v35, v35\n\t" \
    "v_exp_f32 v3, v3\n\t" \
    "v_exp_f32 v36, v36\n\t" \
    "v_pk_add_f32 v[64:65], v[32:33], v[34:35]\n\t" \
    "v_pk_add_f32 v[66:67], v[0:1], v[2:3]\n\t" \
    "v_exp_f32 v4, v4\n\t" \
    "v_exp_f32 v37, v37\n\t" \
    "v_exp_f32 v5, v5\n\t" \
    "v_exp_f32 v38, v38\n\t" \
    "v_pk_add_f32 v[64:65], v[64:65], v[36:37]\n\t" \
    "v_pk_add_f32 v[66:67], v[66:67], v[4:5]\n\t" \
    "v_exp_f32 v6, v6\n\t" \
    "v_exp_f32 v39, v39\n\t" \
    "v_exp_f32 v7, v7\n\t" \
    "v_pk_fma_f32 v[40:41], v[8:9], s[90:91], v[68:69] op_sel_hi:[1,1,0]\n\t" \
    "v_pk_add_f32 v[64:65], v[64:65], v[38:39]\n\t" \
    "v_pk_add_f32 v[66:67], v[66:67], v[6:7]\n\t" \
    "v_pk_fma_f32 v[8:9], v[8:9], s[90:91], v[70:71] op_sel_hi:[1,1,0] neg_lo:[1,0,0] neg_hi:[1,0,0]\n\t" \
    "v_add_f32 v64, v64, v65\n\t" \
    "v_add_f32 v66, v66, v67\n\t" \
    "v_pk_fma_f32 v[42:43], v[10:11], s[90:91], v[68:69] op_sel_hi:[1,1,0]\n\t" \
    "v_pk_fma_f32 v[10:11], v[10:11], s[90:91], v[70:71] op_sel_hi:[1,1,0] neg_lo:[1,0,0] neg_hi:[1,0,0]\n\t" \
    "v_permlane32_swap_b32 v64, v66\n\t" \
    "v_pk_fma_f32 v[44:45], v[12:13], s[90:91], v[68:69] op_sel_hi:[1,1,0]\n\t" \
    "v_pk_fma_f32 v[12:13], v[12:13], s[90:91], v[70:71] op_sel_hi:[1,1,0] neg_lo:[1,0,0] neg_hi:[1,0,0]\n\t" \
    "v_add_f32 v64, v64, v66\n\t" \
    "v_pk_fma_f32 v[46:47], v[14:15], s[90:91], v[68:69] op_sel_hi:[1,1,0]\n\t" \
    "v_pk_fma_f32 v[14:15], v[14:15], s[90:91], v[70:71] op_sel_hi:[1,1,0] neg_lo:[1,0,0] neg_hi:[1,0,0]\n\t" \
    "v_exp_f32 v40, v40\n\t" \
    "v_exp_f32 v8, v8\n\t" \
    "v_exp_f32 v41, v41\n\t" \
    "v_exp_f32 v9, v9\n\t" \
    "v_exp_f32 v42, v42\n\t" \
    "v_exp_f32 v10, v10\n\t" \
    "v_exp_f32 v43, v43\n\t" \
    "v_exp_f32 v11, v11\n\t" \
    "v_exp_f32 v44, v44\n\t" \
    "v_pk_add_f32 v[68:69], v[40:41], v[42:43]\n\t" \
    "v_pk_add_f32 v[70:71], v[8:9], v[10:11]\n\t" \
    "v_exp_f32 v12, v12\n\t" \
    "v_exp_f32 v45, v45\n\t" \
    "v_exp_f32 v13, v13\n\t" \
    "v_exp_f32 v46, v46\n\t" \
    "v_pk_add_f32 v[68:69], v[68:69], v[44:45]\n\t" \
    "v_pk_add_f32 v[70:71], v[70:71], v[12:13]\n\t" \
    "v_exp_f32 v14, v14\n\t" \
    "v_exp_f32 v47, v47\n\t" \
    "v_exp_f32 v15, v15\n\t" \
    "v_pk_fma_f32 v[48:49], v[16:17], s[90:91], v[72:73] op_sel_hi:[1,1,0]\n\t" \
    "v_pk_add_f32 v[68:69], v[68:69], v[46:47]\n\t" \
    "v_pk_add_f32 v[70:71], v[70:71], v[14:15]\n\t" \
    "v_pk_fma_f32 v[16:17], v[16:17], s[90:91], v[74:75] op_sel_hi:[1,1,0] neg_lo:[1,0,0] neg_hi:[1,0,0]\n\t" \
    "v_add_f32 v68, v68, v69\n\t" \
    "v_add_f32 v70, v70, v71\n\t" \
    "v_pk_fma_f32 v[50:51], v[18:19], s[90:91], v[72:73] op_sel_hi:[1,1,0]\n\t" \
    "v_pk_fma_f32 v[18:19], v[18:19], s[90:91], v[74:75] op_sel_hi:[1,1,0] neg_lo:[1,0,0] neg_hi:[1,0,0]\n\t" \
    "v_permlane32_swap_b32 v68, v70\n\t" \
    "v_pk_fma_f32 v[52:53], v[20:21], s[90:91], v[72:73] op_sel_hi:[1,1,0]\n\t" \
    ".Lpad_ab1:\n\t" \
    "s_cbranch_execz .Lpad_ret\n\t" \
    "v_pk_fma_f32 v[20:21], v[20:21], s[90:91], v[74:75] op_sel_hi:[1,1,0] neg_lo:[1,0,0] neg_hi:[1,0,0]\n\t" \
    "v_add_f32 v68, v68, v70\n\t" \
    "v_pk_fma_f32 v[54:55], v[22:23], s[90:91], v[72:73] op_sel_hi:[1,1,0]\n\t" \
    "v_pk_fma_f32 v[22:23], v[22:23], s[90:91], v[74:75] op_sel_hi:[1,1,0] neg_lo:[1,0,0] neg_hi:[1,0,0]\n\t" \
    "v_permlane16_swap_b32 v64, v68\n\t" \
    "v_exp_f32 v48, v48\n\t" \
    "v_exp_f32 v16, v16\n\t" \
    "v_add_f32 v66, v64, v68\n\t" \
    "v_exp_f32 v49, v49\n\t" \
    "v_exp_f32 v17, v17\n\t" \
    "v_rcp_f32 v64, v66\n\t" \
    "v_exp_f32 v50, v50\n\t" \
    "v_exp_f32 v18, v18\n\t" \
    "v_mov_b32 v68, v64\n\t" \
    "v_exp_f32 v51, v51\n\t" \
    "v_exp_f32 v19, v19\n\t" \
    "v_permlane16_swap_b32 v64, v68\n\t" \
    "v_pk_add_f32 v[72:73], v[48:49], v[50:51]\n\t" \
    "v_pk_add_f32 v[74:75], v[16:17], v[18:19]\n\t" \
    "v_mov_b32 v66, v64\n\t" \
    "v_mov_b32 v70, v68\n\t" \
    "v_exp_f32 v52, v52\n\t" \
    "v_permlane32_swap_b32 v64, v66\n\t" \
    "v_permlane32_swap_b32 v68, v70\n\t" \
    "v_exp_f32 v20, v20\n\t" \
    "v_pk_mul_f32 v[0:1], v[0:1], v[66:67] op_sel_hi:[1,0]\n\t" \
    "v_pk_mul_f32 v[32:33], v[32:33], v[64:65] op_sel_hi:[1,0]\n\t" \
    "v_pk_mul_f32 v[2:3], v[2:3], v[66:67] op_sel_hi:[1,0]\n\t" \
    "v_pk_mul_f32 v[34:35], v[34:35], v[64:65] op_sel_hi:[1,0]\n\t" \
    "v_pk_mul_f32 v[4:5], v[4:5], v[66:67] op_sel_hi:[1,0]\n\t" \
    "v_pk_mul_f32 v[36:37], v[36:37], v[64:65] op_sel_hi:[1,0]\n\t" \
    "v_pk_mul_f32 v[6:7], v[6:7], v[66:67] op_sel_hi:[1,0]\n\t" \
    "v_pk_mul_f32 v[38:39], v[38:39], v[64:65] op_sel_hi:[1,0]\n\t" \
    "v_exp_f32 v53, v53\n\t" \
    "v_cvt_pk_bf16_f32 v7, v6, v7\n\t" \
    "v_cvt_pk_bf16_f32 v6, v4, v5\n\t" \
    "v_cvt_pk_bf16_f32 v5, v2, v3\n\t" \
    "v_cvt_pk_bf16_f32 v4, v0, v1\n\t" \
    "ds_write_b64 v253, v[6:7] offset:0\n\t" \
    "v_cvt_pk_bf16_f32 v0, v32, v33\n\t" \
    "v_cvt_pk_bf16_f32 v1, v34, v35\n\t" \
    "v_cvt_pk_bf16_f32 v2, v36, v37\n\t" \
    "v_cvt_pk_bf16_f32 v3, v38, v39\n\t" \
    "v_pk_add_f32 v[72:73], v[72:73], v[52:53]\n\t" \
    "ds_write_b64 v243, v[0:1] offset:0\n\t" \
    "v_exp_f32 v21, v21\n\t" \
    "v_exp_f32 v54, v54\n\t" \
    "v_exp_f32 v22, v22\n\t" \
    "v_pk_add_f32 v[74:75], v[74:75], v[20:21]\n\t" \
    "v_exp_f32 v55, v55\n\t" \
    "ds_write_b64 v251, v[2:3] offset:0\n\t" \
    "v_exp_f32 v23, v23\n\t" \
    "v_pk_add_f32 v[72:73], v[72:73], v[54:55]\n\t" \
    "v_pk_fma_f32 v[56:57], v[24:25], s[90:91], v[76:77] op_sel_hi:[1,1,0]\n\t" \
    "v_pk_add_f32 v[74:75], v[74:75], v[22:23]\n\t" \
    "v_add_f32 v72, v72, v73\n\t" \
    "ds_write_b64 v252, v[4:5] offset:0\n\t" \
    "v_add_f32 v74, v74, v75\n\t" \
    "v_pk_fma_f32 v[24:25], v[24:25], s[90:91], v[78:79] op_sel_hi:[1,1,0] neg_lo:[1,0,0] neg_hi:[1,0,0]\n\t" \
    "v_pk_fma_f32 v[58:59], v[26:27], s[90:91], v[76:77] op_sel_hi:[1,1,0]\n\t" \
    "v_permlane32_swap_b32 v72, v74\n\t" \
    "v_pk_fma_f32 v[26:27], v[26:27], s[90:91], v[78:79] op_sel_hi:[1,1,0] neg_lo:[1,0,0] neg_hi:[1,0,0]\n\t" \
    "v_pk_fma_f32 v[60:61], v[28:29], s[90:91], v[76:77] op_sel_hi:[1,1,0]\n\t" \
    "v_add_f32 v72, v72, v74\n\t" \
    "v_pk_fma_f32 v[28:29], v[28:29], s[90:91], v[78:79] op_sel_hi:[1,1,0] neg_lo:[1,0,0] neg_hi:[1,0,0]\n\t" \
    "v_pk_fma_f32 v[62:63], v[30:31], s[90:91], v[76:77] op_sel_hi:[1,1,0]\n\t" \
    "v_pk_fma_f32 v[30:31], v[30:31], s[90:91], v[78:79] op_sel_hi:[1,1,0] neg_lo:[1,0,0] neg_hi:[1,0,0]\n\t" \
    "v_exp_f32 v56, v56\n\t" \
    "v_exp_f32 v24, v24\n\t" \
    "v_exp_f32 v57, v57\n\t" \
    "v_exp_f32 v25, v25\n\t" \
    "v_exp_f32 v58, v58\n\t" \
    "v_exp_f32 v26, v26\n\t" \
    "v_exp_f32 v59, v59\n\t" \
    "v_exp_f32 v27, v27\n\t" \
    "v_exp_f32 v60, v60\n\t" \
    "v_pk_add_f32 v[76:77], v[56:57], v[58:59]\n\t" \
    "v_pk_add_f32 v[78:79], v[24:25], v[26:27]\n\t" \
    "v_exp_f32 v28, v28\n\t" \
    "v_exp_f32 v61, v61\n\t" \
    "v_exp_f32 v29, v29\n\t" \
    "v_exp_f32 v62, v62\n\t" \
    "v_pk_add_f32 v[76:77], v[76:77], v[60:61]\n\t" \
    "v_pk_add_f32 v[78:79], v[78:79], v[28:29]\n\t" \
    "v_exp_f32 v30, v30\n\t" \
    "v_exp_f32 v63, v63\n\t" \
    "v_exp_f32 v31, v31\n\t" \
    "v_pk_mul_f32 v[8:9], v[8:9], v[70:71] op_sel_hi:[1,0]\n\t" \
    "v_pk_add_f32 v[76:77], v[76:77], v[62:63]\n\t" \
    "v_pk_add_f32 v[78:79], v[78:79], v[30:31]\n\t" \
    "v_pk_mul_f32 v[40:41], v[40:41], v[68:69] op_sel_hi:[1,0]\n\t" \
    "v_add_f32 v76, v76, v77\n\t" \
    "v_add_f32 v78, v78, v79\n\t" \
    "v_pk_mul_f32 v[10:11], v[10:11], v[70:71] op_sel_hi:[1,0]\n\t" \
    "v_pk_mul_f32 v[42:43], v[42:43], v[68:69] op_sel_hi:[1,0]\n\t" \
    "v_permlane32_swap_b32 v76, v78\n\t" \
    "v_pk_mul_f32 v[12:13], v[12:13], v[70:71] op_sel_hi:[1,0]\n\t" \
    "v_pk_mul_f32 v[44:45], v[44:45], v[68:69] op_sel_hi:[1,0]\n\t" \
    "v_add_f32 v76, v76, v78\n\t" \
    "v_pk_mul_f32 v[14:15], v[14:15], v[70:71] op_sel_hi:[1,0]\n\t" \
    "v_pk_mul_f32 v[46:47], v[46:47], v[68:69] op_sel_hi:[1,0]\n\t" \
    "v_permlane16_swap_b32 v72, v76\n\t" \
    "v_cvt_pk_bf16_f32 v15, v14, v15\n\t" \
    "v_cvt_pk_bf16_f32 v14, v12, v13\n\t" \
    "v_add_f32 v74, v72, v76\n\t" \
    "v_cvt_pk_bf16_f32 v13, v10, v11\n\t" \
    "ds_write_b64 v253, v[14:15] offset:2048\n\t" \
    "v_rcp_f32 v72, v74\n\t" \
    "v_cvt_pk_bf16_f32 v12, v8, v9\n\t" \
    "v_cvt_pk_bf16_f32 v8, v40, v41\n\t" \
    "v_mov_b32 v76, v72\n\t" \
    "v_cvt_pk_bf16_f32 v9, v42, v43\n\t" \
    "ds_write_b64 v252, v[12:13] offset:2048\n\t" \
    "v_permlane16_swap_b32 v72, v76\n\t" \
    "v_cvt_pk_bf16_f32 v10, v44, v45\n\t" \
    "v_cvt_pk_bf16_f32 v11, v46, v47\n\t" \
    "v_mov_b32 v74, v72\n\t" \
    "v_mov_b32 v78, v76\n\t" \
    "ds_write_b64 v243, v[8:9] offset:2048\n\t" \
    "v_permlane32_swap_b32 v72, v74\n\t" \
    "v_permlane32_swap_b32 v76, v78\n\t" \
    "ds_write_b64 v251, v[10:11] offset:2048\n\t" \
    "v_mov_b32 v237, 3\n\t" \
    "v_pk_mul_f32 v[16:17], v[16:17], v[74:75] op_sel_hi:[1,0]\n\t" \
    "ds_write_b32 v248, v237\n\t" \
    "v_pk_mul_f32 v[48:49], v[48:49], v[72:73] op_sel_hi:[1,0]\n\t" \
    "ds_read_b64_tr_b16 v[96:97], v244\n\t" \
    "v_pk_mul_f32 v[18:19], v[18:19], v[74:75] op_sel_hi:[1,0]\n\t" \
    "ds_read_b64_tr_b16 v[98:99], v244 offset:2048\n\t" \
    "v_pk_mul_f32 v[50:51], v[50:51], v[72:73] op_sel_hi:[1,0]\n\t" \
    "ds_read_b64_tr_b16 v[100:101], v254\n\t" \
    "v_pk_mul_f32 v[20:21], v[20:21], v[74:75] op_sel_hi:[1,0]\n\t" \
    "ds_read_b64_tr_b16 v[102:103], v254 offset:2048\n\t" \
    "v_pk_mul_f32 v[52:53], v[52:53], v[72:73] op_sel_hi:[1,0]\n\t" \
    "v_pk_mul_f32 v[22:23], v[22:23], v[74:75] op_sel_hi:[1,0]\n\t" \
    "v_pk_mul_f32 v[54:55], v[54:55], v[72:73] op_sel_hi:[1,0]\n\t" \
    "v_pk_mul_f32 v[24:25], v[24:25], v[78:79] op_sel_hi:[1,0]\n\t" \
    "v_cvt_pk_bf16_f32 v23, v22, v23\n\t" \
    "v_cvt_pk_bf16_f32 v22, v20, v21\n\t" \
    "v_cvt_pk_bf16_f32 v21, v18, v19\n\t" \
    "v_cvt_pk_bf16_f32 v20, v16, v17\n\t" \
    "ds_write_b64 v253, v[22:23] offset:4096\n\t" \
    "v_cvt_pk_bf16_f32 v16, v48, v49\n\t" \
    "v_cvt_pk_bf16_f32 v17, v50, v51\n\t" \
    "v_cvt_pk_bf16_f32 v18, v52, v53\n\t" \
    "v_cvt_pk_bf16_f32 v19, v54, v55\n\t" \
    "v_pk_mul_f32 v[56:57], v[56:57], v[76:77] op_sel_hi:[1,0]\n\t" \
    "ds_write_b64 v243, v[16:17] offset:4096\n\t" \
    "v_pk_mul_f32 v[26:27], v[26:27], v[78:79] op_sel_hi:[1,0]\n\t" \
    "v_pk_mul_f32 v[58:59], v[58:59], v[76:77] op_sel_hi:[1,0]\n\t" \
    "v_pk_mul_f32 v[28:29], v[28:29], v[78:79] op_sel_hi:[1,0]\n\t" \
    "v_pk_mul_f32 v[60:61], v[60:61], v[76:77] op_sel_hi:[1,0]\n\t" \
    "v_pk_mul_f32 v[30:31], v[30:31], v[78:79] op_sel_hi:[1,0]\n\t" \
    "ds_write_b64 v251, v[18:19] offset:4096\n\t" \
    "v_pk_mul_f32 v[62:63], v[62:63], v[76:77] op_sel_hi:[1,0]\n\t" \
    "v_cvt_pk_bf16_f32 v31, v30, v31\n\t" \
    "v_cvt_pk_bf16_f32 v30, v28, v29\n\t" \
    "v_cvt_pk_bf16_f32 v29, v26, v27\n\t" \
    "v_cvt_pk_bf16_f32 v28, v24, v25\n\t" \
    "ds_write_b64 v252, v[20:21] offset:4096\n\t" \
    "ds_read_b32 v238, v250\n\t" \
    "v_cvt_pk_bf16_f32 v24, v56, v57\n\t" \
    "ds_read_b64_tr_b16 v[104:105], v245\n\t" \
    "v_cvt_pk_bf16_f32 v25, v58, v59\n\t" \
    "ds_read_b64_tr_b16 v[106:107], v245 offset:2048\n\t" \
    "v_cvt_pk_bf16_f32 v26, v60, v61\n\t" \
    "ds_read_b64_tr_b16 v[108:109], v255\n\t" \
    "v_cvt_pk_bf16_f32 v27, v62, v63\n\t" \
    "ds_read_b64_tr_b16 v[110:111], v255 offset:2048\n\t" \
    "ds_write_b64 v243, v[24:25] offset:6144\n\t" \
    "ds_read_b128 v[80:83], v242\n\t" \
    "ds_write_b64 v251, v[26:27] offset:6144\n\t" \
    "ds_read_b128 v[84:87], v242 offset:1024\n\t" \
    "ds_write_b64 v252, v[28:29] offset:6144\n\t" \
    "ds_read_b128 v[88:91], v242 offset:4096\n\t" \
    "ds_write_b64 v253, v[30:31] offset:6144\n\t" \
    "ds_read_b128 v[92:95], v242 offset:5120\n\t" \
    "v_mov_b32 v237, 4\n\t" \
    "ds_write_b32 v248, v237\n\t" \
    "s_waitcnt lgkmcnt(14)\n\t" \
    "v_mfma_f32_16x16x32_bf16 v[64:67], v[96:99], v[232:235], 0\n\t" \
    "ds_read_b64_tr_b16 v[0:1], v244 offset:4096\n\t" \
    "ds_read_b64_tr_b16 v[2:3], v244 offset:6144\n\t" \
    "v_mfma_f32_16x16x32_bf16 v[68:71], v[100:103], v[232:235], 0\n\t" \
    "ds_read_b64_tr_b16 v[4:5], v254 offset:4096\n\t" \
    "ds_read_b64_tr_b16 v[6:7], v254 offset:6144\n\t" \
    "v_mfma_f32_16x16x32_bf16 v[32:35], v[96:99], v[112:115], 0\n\t" \
    "v_mfma_f32_16x16x32_bf16 v[40:43], v[100:103], v[112:115], 0\n\t" \
    "s_waitcnt lgkmcnt(14)\n\t" \
    "v_readfirstlane_b32 s85, v238\n\t" \
    "s_cmp_lt_u32 s85, 3\n\t" \
    "s_cbranch_scc1 .Lab_s3\n\t" \
    ".Lab_s3r:\n\t" \
    "v_mfma_f32_16x16x32_bf16 v[36:39], v[96:99], v[116:119], 0\n\t" \
    "v_mfma_f32_16x16x32_bf16 v[44:47], v[100:103], v[116:119], 0\n\t" \
    "ds_read_b32 v238, v250\n\t" \
    "ds_read_b64_tr_b16 v[8:9], v245 offset:4096\n\t" \
    "v_mfma_f32_16x16x32_bf16 v[64:67], v[104:107], v[232:235], v[64:67]\n\t" \
    "ds_read_b64_tr_b16 v[10:11], v245 offset:6144\n\t" \
    "ds_read_b64_tr_b16 v[12:13], v255 offset:4096\n\t" \
    "s_waitcnt lgkmcnt(14)\n\t" \
    "v_mfma_f32_16x16x32_bf16 v[68:71], v[108:111], v[232:235], v[68:71]\n\t" \
    "ds_read_b64_tr_b16 v[14:15], v255 offset:6144\n\t" \
    "v_mfma_f32_16x16x32_bf16 v[32:35], v[104:107], v[80:83], v[32:35]\n\t" \
    "v_mfma_f32_16x16x32_bf16 v[40:43], v[108:111], v[80:83], v[40:43]\n\t" \
    "s_waitcnt lgkmcnt(14)\n\t" \
    "v_mfma_f32_16x16x32_bf16 v[36:39], v[104:107], v[84:87], v[36:39]\n\t" \
    "v_add_u32 v253, 135168, v241\n\t" \
    "v_mfma_f32_16x16x32_bf16 v[44:47], v[108:111], v[84:87], v[44:47]\n\t" \
    "v_xor_b32 v251, 32, v253\n\t" \
    "s_waitcnt lgkmcnt(7)\n\t" \
    "v_mfma_f32_16x16x32_bf16 v[64:67], v[0:3], v[232:235], v[64:67]\n\t" \
    "v_mov_b32 v237, 5\n\t" \
    "s_waitcnt lgkmcnt(5)\n\t" \
    "v_mfma_f32_16x16x32_bf16 v[68:71], v[4:7], v[232:235], v[68:71]\n\t" \
    "v_mfma_f32_16x16x32_bf16 v[32:35], v[0:3], v[120:123], v[32:35]\n\t" \
    "v_mfma_f32_16x16x32_bf16 v[40:43], v[4:7], v[120:123], v[40:43]\n\t" \
    "v_mfma_f32_16x16x32_bf16 v[36:39], v[0:3], v[124:127], v[36:39]\n\t" \
    "s_waitcnt lgkmcnt(4)\n\t" \
    "v_readfirstlane_b32 s85, v238\n\t" \
    "s_cmp_lt_u32 s85, 4\n\t" \
    "s_cbranch_scc1 .Lab_s4\n\t" \
    ".Lab_s4r:\n\t" \
    "v_mfma_f32_16x16x32_bf16 v[44:47], v[4:7], v[124:127], v[44:47]\n\t" \
    "ds_write_b32 v248, v237\n\t" \
    "s_waitcnt lgkmcnt(3)\n\t" \
    "v_mfma_f32_16x16x32_bf16 v[64:67], v[8:11], v[232:235], v[64:67]\n\t" \
    "ds_read_b32 v238, v249 offset:92\n\t" \
    "s_waitcnt lgkmcnt(2)\n\t" \
    "v_mfma_f32_16x16x32_bf16 v[68:71], v[12:15], v[232:235], v[68:71]\n\t" \
    "v_mfma_f32_16x16x32_bf16 v[32:35], v[8:11], v[88:91], v[32:35]\n\t" \
    "v_mfma_f32_16x16x32_bf16 v[40:43], v[12:15], v[88:91], v[40:43]\n\t" \
    "v_mfma_f32_16x16x32_bf16 v[36:39], v[8:11], v[92:95], v[36:39]\n\t" \
    "v_mfma_f32_16x16x32_bf16 v[44:47], v[12:15], v[92:95], v[44:47]\n\t" \
    "ds_read_b64_tr_b16 v[80:81], v253\n\t" \
    "ds_read_b64_tr_b16 v[82:83], v253 offset:512\n\t" \
    "ds_read_b64_tr_b16 v[84:85], v251\n\t" \
    "s_nop 0\n\t" \
    "v_cndmask_b32 v24, v68, v64, vcc\n\t" \
    "v_cndmask_b32 v25, v69, v65, vcc\n\t" \
    "v_cndmask_b32 v26, v70, v66, vcc\n\t" \
    "v_cndmask_b32 v27, v71, v67, vcc\n\t" \
    "v_add3_u32 v30, v24, v25, v26\n\t" \
    "v_add_u32 v30, v30, v27\n\t" \
    "v_cvt_pk_bf16_f32 v16, v32, v33\n\t" \
    "v_cvt_pk_bf16_f32 v17, v34, v35\n\t" \
    "v_cvt_pk_bf16_f32 v18, v40, v41\n\t" \
    "v_cvt_pk_bf16_f32 v19, v42, v43\n\t" \
    "v_cvt_pk_bf16_f32 v20, v36, v37\n\t" \
    "v_cvt_pk_bf16_f32 v21, v38, v39\n\t" \
    "v_cvt_pk_bf16_f32 v22, v44, v45\n\t" \
    "v_cvt_pk_bf16_f32 v23, v46, v47\n\t" \
    "v_add3_u32 v28, v16, v17, v18\n\t" \
    "v_add3_u32 v29, v20, v21, v22\n\t" \
    "v_add_u32 v28, v28, v19\n\t" \
    "v_add_u32 v29, v29, v23\n\t" \
    "v_cndmask_b32_e64 v30, 0, v30, s[94:95]\n\t" \
    "buffer_store_dwordx4 v[16:19], v246, %[rsb], s87 offen sc1\n\t" \
    "v_permlane16_swap_b32 v28, v29\n\t" \
    "v_add3_u32 v28, v28, v29, v30\n\t" \
    "buffer_store_dwordx4 v[20:23], v246, %[rsb], s86 offen sc1\n\t" \
    "v_cndmask_b32_e64 v25, v25, v236, s[92:93]\n\t" \
    "v_add_u32_dpp v28, v28, v28 row_ror:8 row_mask:0xf bank_mask:0xf bound_ctrl:1\n\t" \
    "v_mov_b32 v237, 6\n\t" \
    "ds_read_b64_tr_b16 v[86:87], v251 offset:512\n\t" \
    "v_add_u32_dpp v28, v28, v28 row_ror:4 row_mask:0xf bank_mask:0xf bound_ctrl:1\n\t" \
    "ds_read_b64_tr_b16 v[88:89], v253 offset:4096\n\t" \
    "ds_read_b64_tr_b16 v[90:91], v253 offset:4608\n\t" \
    "v_add_u32_dpp v28, v28, v28 row_ror:2 row_mask:0xf bank_mask:0xf bound_ctrl:1\n\t" \
    "ds_read_b64_tr_b16 v[92:93], v251 offset:4096\n\t" \
    "ds_read_b64_tr_b16 v[94:95], v251 offset:4608\n\t" \
    "v_add_u32_dpp v28, v28, v28 row_ror:1 row_mask:0xf bank_mask:0xf bound_ctrl:1\n\t" \
    "v_cndmask_b32_e64 v24, v24, v28, s[92:93]\n\t" \
    "buffer_store_dwordx4 v[24:27], v247, %[rsb], s88 offen sc1\n\t" \
    "ds_write_b32 v248, v237\n\t" \
    "s_setprio 1\n\t" \
    "s_waitcnt lgkmcnt(9)\n\t" \
    "v_readfirstlane_b32 s85, v238\n\t" \
    "s_cmp_lt_u32 s85, 1\n\t" \
    "s_cbranch_scc1 .Lab_s5\n\t" \
    ".Lab_s5r:\n\t" \
    "s_waitcnt lgkmcnt(7)\n\t" \
    "v_mfma_f32_16x16x32_bf16 v[192:195], v[80:83], v[128:131], v[192:195]\n\t" \
    "v_add_u32 v244, 49152, v241\n\t" \
    "v_xor_b32 v245, 32, v244\n\t" \
    "v_mfma_f32_16x16x32_bf16 v[200:203], v[80:83], v[132:135], v[200:203]\n\t" \
    "ds_read_b64_tr_b16 v[96:97], v244\n\t" \
    "ds_read_b64_tr_b16 v[98:99], v244 offset:512\n\t" \
    "v_mfma_f32_16x16x32_bf16 v[208:211], v[80:83], v[136:139], v[208:211]\n\t" \
    "ds_read_b64_tr_b16 v[100:101], v245\n\t" \
    "ds_read_b64_tr_b16 v[102:103], v245 offset:512\n\t" \
    "v_mfma_f32_16x16x32_bf16 v[216:219], v[80:83], v[140:143], v[216:219]\n\t" \
    "ds_read_b64_tr_b16 v[104:105], v244 offset:4096\n\t" \
    "ds_read_b64_tr_b16 v[106:107], v244 offset:4608\n\t" \
    "s_waitcnt lgkmcnt(11)\n\t" \
    "v_mfma_f32_16x16x32_bf16 v[196:199], v[84:87], v[128:131], v[196:199]\n\t" \
    "ds_read_b64_tr_b16 v[108:109], v245 offset:4096\n\t" \
    "ds_read_b64_tr_b16 v[110:111], v245 offset:4608\n\t" \
    "v_mfma_f32_16x16x32_bf16 v[204:207], v[84:87], v[132:135], v[204:207]\n\t" \
    "v_xor_b32 v246, 64, v240\n\t" \
    "v_subrev_u32 v242, 16384, v241\n\t" \
    "v_mfma_f32_16x16x32_bf16 v[212:215], v[84:87], v[136:139], v[212:215]\n\t" \
    "v_xor_b32 v243, 32, v242\n\t" \
    "v_add_u32 v247, 65536, v240\n\t" \
    "v_mfma_f32_16x16x32_bf16 v[220:223], v[84:87], v[140:143], v[220:223]\n\t" \
    "v_mov_b32 v237, 7\n\t" \
    "ds_write_b32 v248, v237\n\t" \
    "s_waitcnt lgkmcnt(12)\n\t" \
    "v_mfma_f32_16x16x32_bf16 v[192:195], v[88:91], v[144:147], v[192:195]\n\t" \
    "ds_read_b64_tr_b16 v[80:81], v244 offset:8192\n\t" \
    "v_mfma_f32_16x16x32_bf16 v[200:203], v[88:91], v[148:151], v[200:203]\n\t" \
    "ds_read_b128 v[128:131], v240 offset:40960\n\t" \
    "v_mfma_f32_16x16x32_bf16 v[208:211], v[88:91], v[152:155], v[208:211]\n\t" \
    "ds_read_b64_tr_b16 v[82:83], v244 offset:8704\n\t" \
    "v_add_u32 v237, 1, v237\n\t" \
    "v_mfma_f32_16x16x32_bf16 v[216:219], v[88:91], v[156:159], v[216:219]\n\t" \
    "ds_read_b128 v[132:135], v240 offset:43008\n\t" \
    "s_waitcnt lgkmcnt(14)\n\t" \
    "v_mfma_f32_16x16x32_bf16 v[196:199], v[92:95], v[144:147], v[196:199]\n\t" \
    "ds_read_b64_tr_b16 v[84:85], v245 offset:8192\n\t" \
    "v_xor_b32 v255, 64, v247\n\t" \
    "v_mfma_f32_16x16x32_bf16 v[204:207], v[92:95], v[148:151], v[204:207]\n\t" \
    "ds_read_b128 v[136:139], v240 offset:45056\n\t" \
    "v_mfma_f32_16x16x32_bf16 v[212:215], v[92:95], v[152:155], v[212:215]\n\t" \
    "ds_read_b64_tr_b16 v[86:87], v245 offset:8704\n\t" \
    "v_mfma_f32_16x16x32_bf16 v[220:223], v[92:95], v[156:159], v[220:223]\n\t" \
    "ds_read_b128 v[140:143], v240 offset:47104\n\t" \
    "s_waitcnt lgkmcnt(14)\n\t" \
    "v_mfma_f32_16x16x32_bf16 v[192:195], v[96:99], v[160:163], v[192:195]\n\t" \
    "ds_read_b64_tr_b16 v[88:89], v244 offset:12288\n\t" \
    "v_mfma_f32_16x16x32_bf16 v[200:203], v[96:99], v[164:167], v[200:203]\n\t" \
    "ds_read_b128 v[144:147], v246 offset:40960\n\t" \
    "v_mfma_f32_16x16x32_bf16 v[208:211], v[96:99], v[168:171], v[208:211]\n\t" \
    "ds_read_b64_tr_b16 v[90:91], v244 offset:12800\n\t" \
    "v_mfma_f32_16x16x32_bf16 v[216:219], v[96:99], v[172:175], v[216:219]\n\t" \
    "ds_read_b128 v[148:151], v246 offset:43008\n\t" \
    "s_waitcnt lgkmcnt(14)\n\t" \
    "v_mfma_f32_16x16x32_bf16 v[196:199], v[100:103], v[160:163], v[196:199]\n\t" \
    "ds_read_b64_tr_b16 v[92:93], v245 offset:12288\n\t" \
    "v_mfma_f32_16x16x32_bf16 v[204:207], v[100:103], v[164:167], v[204:207]\n\t" \
    "ds_read_b128 v[152:155], v246 offset:45056\n\t" \
    "v_mfma_f32_16x16x32_bf16 v[212:215], v[100:103], v[168:171], v[212:215]\n\t" \
    "ds_read_b64_tr_b16 v[94:95], v245 offset:12800\n\t" \
    "v_mfma_f32_16x16x32_bf16 v[220:223], v[100:103], v[172:175], v[220:223]\n\t" \
    "ds_read_b128 v[156:159], v246 offset:47104\n\t" \
    "ds_write_b32 v248, v237\n\t" \
    "v_mfma_f32_16x16x32_bf16 v[192:195], v[104:107], v[176:179], v[192:195]\n\t" \
    "ds_read_b64_tr_b16 v[96:97], v242 offset:24576\n\t" \
    "v_mfma_f32_16x16x32_bf16 v[200:203], v[104:107], v[180:183], v[200:203]\n\t" \
    "ds_read_b128 v[160:163], v247 offset:16384\n\t" \
    "v_mfma_f32_16x16x32_bf16 v[208:211], v[104:107], v[184:187], v[208:211]\n\t" \
    "ds_read_b64_tr_b16 v[98:99], v242 offset:25088\n\t" \
    "v_add_u32 v237, 1, v237\n\t" \
    "v_mfma_f32_16x16x32_bf16 v[216:219], v[104:107], v[188:191], v[216:219]\n\t" \
    "ds_read_b128 v[164:167], v247 offset:18432\n\t" \
    "s_waitcnt lgkmcnt(14)\n\t" \
    "v_mfma_f32_16x16x32_bf16 v[196:199], v[108:111], v[176:179], v[196:199]\n\t" \
    "ds_read_b64_tr_b16 v[100:101], v243 offset:24576\n\t" \
    "v_mfma_f32_16x16x32_bf16 v[204:207], v[108:111], v[180:183], v[204:207]\n\t" \
    "ds_read_b128 v[168:171], v247 offset:20480\n\t" \
    "v_mfma_f32_16x16x32_bf16 v[212:215], v[108:111], v[184:187], v[212:215]\n\t" \
    "ds_read_b64_tr_b16 v[102:103], v243 offset:25088\n\t" \
    "v_mfma_f32_16x16x32_bf16 v[220:223], v[108:111], v[188:191], v[220:223]\n\t" \
    "ds_read_b128 v[172:175], v247 offset:22528\n\t" \
    "v_mfma_f32_16x16x32_bf16 v[192:195], v[80:83], v[128:131], v[192:195]\n\t" \
    "ds_read_b64_tr_b16 v[104:105], v242 offset:28672\n\t" \
    "v_mfma_f32_16x16x32_bf16 v[200:203], v[80:83], v[132:135], v[200:203]\n\t" \
    "ds_read_b128 v[176:179], v255 offset:16384\n\t" \
    "v_mfma_f32_16x16x32_bf16 v[208:211], v[80:83], v[136:139], v[208:211]\n\t" \
    "ds_read_b64_tr_b16 v[106:107], v242 offset:29184\n\t" \
    "s_waitcnt lgkmcnt(14)\n\t" \
    "v_mfma_f32_16x16x32_bf16 v[216:219], v[80:83], v[140:143], v[216:219]\n\t" \
    "ds_read_b128 v[180:183], v255 offset:18432\n\t" \
    "v_mfma_f32_16x16x32_bf16 v[196:199], v[84:87], v[128:131], v[196:199]\n\t" \
    "ds_read_b64_tr_b16 v[108:109], v243 offset:28672\n\t" \
    "v_mfma_f32_16x16x32_bf16 v[204:207], v[84:87], v[132:135], v[204:207]\n\t" \
    "ds_read_b128 v[184:187], v255 offset:20480\n\t" \
    "v_mfma_f32_16x16x32_bf16 v[212:215], v[84:87], v[136:139], v[212:215]\n\t" \
    "ds_read_b64_tr_b16 v[110:111], v243 offset:29184\n\t" \
    "v_mfma_f32_16x16x32_bf16 v[220:223], v[84:87], v[140:143], v[220:223]\n\t" \
    "ds_read_b128 v[188:191], v255 offset:22528\n\t" \
    "ds_write_b32 v248, v237\n\t" \
    "v_mfma_f32_16x16x32_bf16 v[192:195], v[88:91], v[144:147], v[192:195]\n\t" \
    "ds_read_b64_tr_b16 v[80:81], v242 offset:32768\n\t" \
    "v_mfma_f32_16x16x32_bf16 v[200:203], v[88:91], v[148:151], v[200:203]\n\t" \
    "ds_read_b128 v[128:131], v240\n\t" \
    "v_mfma_f32_16x16x32_bf16 v[208:211], v[88:91], v[152:155], v[208:211]\n\t" \
    "ds_read_b64_tr_b16 v[82:83], v242 offset:33280\n\t" \
    "v_add_u32 v237, 1, v237\n\t" \
    "s_waitcnt lgkmcnt(14)\n\t" \
    "v_mfma_f32_16x16x32_bf16 v[216:219], v[88:91], v[156:159], v[216:219]\n\t" \
    "ds_read_b128 v[132:135], v240 offset:2048\n\t" \
    "v_mfma_f32_16x16x32_bf16 v[196:199], v[92:95], v[144:147], v[196:199]\n\t" \
    "ds_read_b64_tr_b16 v[84:85], v243 offset:32768\n\t" \
    "v_mfma_f32_16x16x32_bf16 v[204:207], v[92:95], v[148:151], v[204:207]\n\t" \
    "ds_read_b128 v[136:139], v240 offset:4096\n\t" \
    "v_mfma_f32_16x16x32_bf16 v[212:215], v[92:95], v[152:155], v[212:215]\n\t" \
    "ds_read_b64_tr_b16 v[86:87], v243 offset:33280\n\t" \
    "ds_read_b32 v238, v249 offset:92\n\t" \
    "v_mfma_f32_16x16x32_bf16 v[220:223], v[92:95], v[156:159], v[220:223]\n\t" \
    "ds_read_b128 v[140:143], v240 offset:6144\n\t" \
    "v_mfma_f32_16x16x32_bf16 v[192:195], v[96:99], v[160:163], v[192:195]\n\t" \
    "ds_read_b64_tr_b16 v[88:89], v242 offset:36864\n\t" \
    "v_add_u32 v247, %[xs0], v240\n\t" \
    "v_mfma_f32_16x16x32_bf16 v[200:203], v[96:99], v[164:167], v[200:203]\n\t" \
    "ds_read_b128 v[144:147], v246\n\t" \
    "v_mfma_f32_16x16x32_bf16 v[208:211], v[96:99], v[168:171], v[208:211]\n\t" \
    "ds_read_b64_tr_b16 v[90:91], v242 offset:37376\n\t" \
    "v_xor_b32 v255, 64, v247\n\t" \
    "s_waitcnt lgkmcnt(14)\n\t" \
    "v_mfma_f32_16x16x32_bf16 v[216:219], v[96:99], v[172:175], v[216:219]\n\t" \
    "ds_read_b128 v[148:151], v246 offset:2048\n\t" \
    "v_mfma_f32_16x16x32_bf16 v[196:199], v[100:103], v[160:163], v[196:199]\n\t" \
    "ds_read_b64_tr_b16 v[92:93], v243 offset:36864\n\t" \
    "v_mfma_f32_16x16x32_bf16 v[204:207], v[100:103], v[164:167], v[204:207]\n\t" \
    "ds_read_b128 v[152:155], v246 offset:4096\n\t" \
    "v_mfma_f32_16x16x32_bf16 v[212:215], v[100:103], v[168:171], v[212:215]\n\t" \
    "ds_read_b64_tr_b16 v[94:95], v243 offset:37376\n\t" \
    "v_mfma_f32_16x16x32_bf16 v[220:223], v[100:103], v[172:175], v[220:223]\n\t" \
    "ds_read_b128 v[156:159], v246 offset:6144\n\t" \
    "ds_write_b32 v248, v237\n\t" \
    "v_mfma_f32_16x16x32_bf16 v[192:195], v[104:107], v[176:179], v[192:195]\n\t" \
    "s_waitcnt lgkmcnt(10)\n\t" \
    "v_readfirstlane_b32 s85, v238\n\t" \
    "s_cmp_lt_u32 s85, 2\n\t" \
    "s_cbranch_scc1 .Lab_s6\n\t" \
    ".Lab_s6r:\n\t" \
    "ds_read_b64_tr_b16 v[96:97], v244 offset:32768\n\t" \
    "v_mfma_f32_16x16x32_bf16 v[200:203], v[104:107], v[180:183], v[200:203]\n\t" \
    "ds_read_b128 v[160:163], v247\n\t" \
    "v_mfma_f32_16x16x32_bf16 v[208:211], v[104:107], v[184:187], v[208:211]\n\t" \
    "ds_read_b64_tr_b16 v[98:99], v244 offset:33280\n\t" \
    "v_add_u32 v237, 1, v237\n\t" \
    "v_mfma_f32_16x16x32_bf16 v[216:219], v[104:107], v[188:191], v[216:219]\n\t" \
    "ds_read_b128 v[164:167], v247 offset:2048\n\t" \
    "v_mfma_f32_16x16x32_bf16 v[196:199], v[108:111], v[176:179], v[196:199]\n\t" \
    "ds_read_b64_tr_b16 v[100:101], v245 offset:32768\n\t" \
    "v_mfma_f32_16x16x32_bf16 v[204:207], v[108:111], v[180:183], v[204:207]\n\t" \
    "ds_read_b128 v[168:171], v247 offset:4096\n\t" \
    "v_mfma_f32_16x16x32_bf16 v[212:215], v[108:111], v[184:187], v[212:215]\n\t" \
    "ds_read_b64_tr_b16 v[102:103], v245 offset:33280\n\t" \
    "v_mfma_f32_16x16x32_bf16 v[220:223], v[108:111], v[188:191], v[220:223]\n\t" \
    "ds_read_b128 v[172:175], v247 offset:6144\n\t" \
    "v_mfma_f32_16x16x32_bf16 v[192:195], v[80:83], v[128:131], v[192:195]\n\t" \
    "ds_read_b64_tr_b16 v[104:105], v244 offset:36864\n\t" \
    "v_mfma_f32_16x16x32_bf16 v[200:203], v[80:83], v[132:135], v[200:203]\n\t" \
    "ds_read_b128 v[176:179], v255\n\t" \
    "v_mfma_f32_16x16x32_bf16 v[208:211], v[80:83], v[136:139], v[208:211]\n\t" \
    "ds_read_b64_tr_b16 v[106:107], v244 offset:37376\n\t" \
    "s_waitcnt lgkmcnt(14)\n\t" \
    "v_mfma_f32_16x16x32_bf16 v[216:219], v[80:83], v[140:143], v[216:219]\n\t" \
    "ds_read_b128 v[180:183], v255 offset:2048\n\t" \
    "v_mfma_f32_16x16x32_bf16 v[196:199], v[84:87], v[128:131], v[196:199]\n\t" \
    "ds_read_b64_tr_b16 v[108:109], v245 offset:36864\n\t" \
    "v_mfma_f32_16x16x32_bf16 v[204:207], v[84:87], v[132:135], v[204:207]\n\t" \
    "ds_read_b128 v[184:187], v255 offset:4096\n\t" \
    "v_mfma_f32_16x16x32_bf16 v[212:215], v[84:87], v[136:139], v[212:215]\n\t" \
    "ds_read_b64_tr_b16 v[110:111], v245 offset:37376\n\t" \
    "v_mfma_f32_16x16x32_bf16 v[220:223], v[84:87], v[140:143], v[220:223]\n\t" \
    "ds_read_b128 v[188:191], v255 offset:6144\n\t" \
    "ds_write_b32 v248, v237\n\t" \
    "v_mfma_f32_16x16x32_bf16 v[192:195], v[88:91], v[144:147], v[192:195]\n\t" \
    "v_mbcnt_lo_u32_b32 v250, -1, 0\n\t" \
    "v_mbcnt_hi_u32_b32 v250, -1, v250\n\t" \
    "v_mfma_f32_16x16x32_bf16 v[200:203], v[88:91], v[148:151], v[200:203]\n\t" \
    "v_and_b32 v238, 15, v250\n\t" \
    "v_lshlrev_b32 v238, 3, v238\n\t" \
    "v_mfma_f32_16x16x32_bf16 v[208:211], v[88:91], v[152:155], v[208:211]\n\t" \
    "v_add_u32 v237, 1, v237\n\t" \
    "s_waitcnt lgkmcnt(14)\n\t" \
    "v_mfma_f32_16x16x32_bf16 v[216:219], v[88:91], v[156:159], v[216:219]\n\t" \
    "buffer_load_dwordx2 v[232:233], v238, %[rsw], %[sg] offen sc1\n\t" \
    "v_mfma_f32_16x16x32_bf16 v[196:199], v[92:95], v[144:147], v[196:199]\n\t" \
    "v_lshlrev_b32 v251, 4, v250\n\t" \
    "s_add_u32 s85, %[sof], %[ofc]\n\t" \
    "v_mfma_f32_16x16x32_bf16 v[204:207], v[92:95], v[148:151], v[204:207]\n\t" \
    "v_add_u32 v240, s85, v251\n\t" \
    "s_add_u32 s87, %[sof], %[ofp]\n\t" \
    "v_mfma_f32_16x16x32_bf16 v[212:215], v[92:95], v[152:155], v[212:215]\n\t" \
    "buffer_load_dwordx2 v[234:235], v238, %[rsw], %[sg] offen offset:128 sc1\n\t" \
    "v_mfma_f32_16x16x32_bf16 v[220:223], v[92:95], v[156:159], v[220:223]\n\t" \
    "v_add_u32 v241, 0x1000, v240\n\t" \
    "s_add_u32 s88, %[sof], %[ogp]\n\t" \
    "v_mfma_f32_16x16x32_bf16 v[192:195], v[96:99], v[160:163], v[192:195]\n\t" \
    "s_mov_b32 s86, 0x10000\n\t" \
    "v_add_u32 v242, 0x2000, v240\n\t" \
    "v_add_u32 v243, 0x3000, v240\n\t" \
    "s_waitcnt lgkmcnt(10)\n\t" \
    "v_mfma_f32_16x16x32_bf16 v[196:199], v[100:103], v[160:163], v[196:199]\n\t" \
    "v_bfe_u32 v244, v251, 1, 8\n\t" \
    "v_add_u32 v253, %[kac], v251\n\t" \
    "s_waitcnt lgkmcnt(6)\n\t" \
    "v_mfma_f32_16x16x32_bf16 v[192:195], v[104:107], v[176:179], v[192:195]\n\t" \
    "v_lshrrev_b32 v112, 2, v250\n\t" \
    "v_bfe_u32 v113, v250, 4, 1\n\t" \
    "s_waitcnt lgkmcnt(2)\n\t" \
    "v_mfma_f32_16x16x32_bf16 v[196:199], v[108:111], v[176:179], v[196:199]\n\t" \
    "v_lshlrev_b32 v245, 7, v112\n\t" \
    "v_bitop3_b32 v112, v112, v113, 3 bitop3:0x28\n\t" \
    "v_mfma_f32_16x16x32_bf16 v[200:203], v[96:99], v[164:167], v[200:203]\n\t" \
    "v_lshl_or_b32 v245, v112, 5, v245\n\t" \
    "v_and_b32 v112, 24, v238\n\t" \
    "v_mfma_f32_16x16x32_bf16 v[204:207], v[100:103], v[164:167], v[204:207]\n\t" \
    "v_or3_b32 v245, v245, v112, s86\n\t" \
    "v_lshlrev_b32 v247, 8, v238\n\t" \
    "v_mfma_f32_16x16x32_bf16 v[200:203], v[104:107], v[180:183], v[200:203]\n\t" \
    "v_lshl_or_b32 v247, v113, 5, v247\n\t" \
    "v_lshrrev_b32 v112, 5, v250\n\t" \
    "v_mfma_f32_16x16x32_bf16 v[204:207], v[108:111], v[180:183], v[204:207]\n\t" \
    "v_lshl_or_b32 v247, v112, 4, v247\n\t" \
    "v_and_b32 v252, 48, v247\n\t" \
    "v_mfma_f32_16x16x32_bf16 v[208:211], v[96:99], v[168:171], v[208:211]\n\t" \
    "v_lshl_add_u32 v252, v252, 2, %[zac]\n\t" \
    "v_lshlrev_b32 v246, 4, v238\n\t" \
    "v_mfma_f32_16x16x32_bf16 v[212:215], v[100:103], v[168:171], v[212:215]\n\t" \
    "v_cvt_pk_bf16_f32 v64, v192, v193\n\t" \
    "v_cvt_pk_bf16_f32 v65, v194, v195\n\t" \
    "v_mfma_f32_16x16x32_bf16 v[208:211], v[104:107], v[184:187], v[208:211]\n\t" \
    "v_cvt_pk_bf16_f32 v66, v196, v197\n\t" \
    "v_cvt_pk_bf16_f32 v67, v198, v199\n\t" \
    "v_mfma_f32_16x16x32_bf16 v[212:215], v[108:111], v[184:187], v[212:215]\n\t" \
    "v_lshl_or_b32 v246, v113, 3, v246\n\t" \
    "v_lshrrev_b32 v113, 4, v238\n\t" \
    "v_mfma_f32_16x16x32_bf16 v[216:219], v[96:99], v[172:175], v[216:219]\n\t" \
    "v_cvt_pk_bf16_f32 v68, v200, v201\n\t" \
    "v_cvt_pk_bf16_f32 v69, v202, v203\n\t" \
    "v_mfma_f32_16x16x32_bf16 v[220:223], v[100:103], v[172:175], v[220:223]\n\t" \
    "v_cvt_pk_bf16_f32 v70, v204, v205\n\t" \
    "v_cvt_pk_bf16_f32 v71, v206, v207\n\t" \
    "s_waitcnt lgkmcnt(1)\n\t" \
    "v_mfma_f32_16x16x32_bf16 v[216:219], v[104:107], v[188:191], v[216:219]\n\t" \
    "v_xor_b32 v112, v112, v113\n\t" \
    "v_lshl_or_b32 v246, v112, 4, v246\n\t" \
    "v_mfma_f32_16x16x32_bf16 v[220:223], v[108:111], v[188:191], v[220:223]\n\t" \
    "v_add_u32 v246, %[xec], v246\n\t" \
    "v_mfma_f32_16x16x32_bf16 v[0:3], v[224:227], v[64:67], 0\n\t" \
    "v_cvt_pk_bf16_f32 v72, v208, v209\n\t" \
    "v_cvt_pk_bf16_f32 v73, v210, v211\n\t" \
    "v_mfma_f32_16x16x32_bf16 v[4:7], v[228:231], v[64:67], 0\n\t" \
    "v_cvt_pk_bf16_f32 v74, v212, v213\n\t" \
    "v_cvt_pk_bf16_f32 v75, v214, v215\n\t" \
    "v_mfma_f32_16x16x32_bf16 v[8:11], v[224:227], v[68:71], 0\n\t" \
    "v_mov_b32 v120, 0x358637bd\n\t" \
    "v_mov_b32 v121, 0x358637bd\n\t" \
    "v_mfma_f32_16x16x32_bf16 v[12:15], v[228:231], v[68:71], 0\n\t" \
    "v_mov_b32 v122, 0x358637bd\n\t" \
    "v_mov_b32 v123, 0x358637bd\n\t" \
    "v_mfma_f32_16x16x32_bf16 v[16:19], v[224:227], v[72:75], 0\n\t" \
    "v_cvt_pk_bf16_f32 v76, v216, v217\n\t" \
    "v_cvt_pk_bf16_f32 v77, v218, v219\n\t" \
    "v_mfma_f32_16x16x32_bf16 v[20:23], v[228:231], v[72:75], 0\n\t" \
    "v_cvt_pk_bf16_f32 v78, v220, v221\n\t" \
    "v_cvt_pk_bf16_f32 v79, v222, v223\n\t" \
    "s_nop 1\n\t" \
    "v_mfma_f32_16x16x32_bf16 v[24:27], v[224:227], v[76:79], 0\n\t" \
    "v_mfma_f32_16x16x32_bf16 v[28:31], v[228:231], v[76:79], 0\n\t" \
    "v_max3_f32 v64, v0, v1, v2\n\t" \
    "v_max3_f32 v66, -v0, -v1, -v2\n\t" \
    "v_max3_f32 v68, v8, v9, v10\n\t" \
    "v_max3_f32 v64, v64, v3, v4\n\t" \
    "v_max3_f32 v66, v66, -v3, -v4\n\t" \
    "v_max3_f32 v68, v68, v11, v12\n\t" \
    "v_max3_f32 v64, v64, v5, v6\n\t" \
    "v_max3_f32 v66, v66, -v5, -v6\n\t" \
    "v_max3_f32 v68, v68, v13, v14\n\t" \
    "v_max_f32_e64 v64, v64, v7\n\t" \
    "v_max_f32_e64 v66, v66, -v7\n\t" \
    "v_max_f32_e64 v68, v68, v15\n\t" \
    "v_max3_f32 v70, -v8, -v9, -v10\n\t" \
    "v_permlane32_swap_b32 v64, v66\n\t" \
    "v_max3_f32 v72, v16, v17, v18\n\t" \
    "v_max3_f32 v70, v70, -v11, -v12\n\t" \
    "v_max_f32 v64, v64, v66\n\t" \
    "v_max3_f32 v72, v72, v19, v20\n\t" \
    "v_max3_f32 v70, v70, -v13, -v14\n\t" \
    "v_max3_f32 v74, -v16, -v17, -v18\n\t" \
    "v_max3_f32 v72, v72, v21, v22\n\t" \
    "v_max_f32_e64 v70, v70, -v15\n\t" \
    "v_max3_f32 v74, v74, -v19, -v20\n\t" \
    "v_max_f32_e64 v72, v72, v23\n\t" \
    "v_permlane32_swap_b32 v68, v70\n\t" \
    "v_max3_f32 v74, v74, -v21, -v22\n\t" \
    "v_max3_f32 v76, v24, v25, v26\n\t" \
    "v_max_f32 v68, v68, v70\n\t" \
    "v_max_f32_e64 v74, v74, -v23\n\t" \
    "v_max3_f32 v76, v76, v27, v28\n\t" \
    "v_permlane16_swap_b32 v64, v68\n\t" \
    "v_permlane32_swap_b32 v72, v74\n\t" \
    "v_max3_f32 v76, v76, v29, v30\n\t" \
    "v_max_f32 v66, v64, v68\n\t" \
    "v_max_f32 v72, v72, v74\n\t" \
    "v_max_f32_e64 v76, v76, v31\n\t" \
    ".Lpad_ab2:\n\t" \
    "s_cbranch_execz .Lpad_ret\n\t" \
    "v_mul_f32 v64, 0xbfb8aa3b, v66\n\t" \
    "v_mul_f32 v68, 0xbfb8aa3b, v66\n\t" \
    "v_max3_f32 v78, -v24, -v25, -v26\n\t" \
    "v_max3_f32 v78, v78, -v27, -v28\n\t" \
    "v_permlane16_swap_b32 v64, v68\n\t" \
    "v_mov_b32 v66, v64\n\t" \
    "v_max3_f32 v78, v78, -v29, -v30\n\t" \
    "v_mov_b32 v70, v68\n\t" \
    "v_permlane32_swap_b32 v64, v66\n\t" \
    "v_max_f32_e64 v78, v78, -v31\n\t" \
    "v_permlane32_swap_b32 v68, v70\n\t" \
    "v_pk_fma_f32 v[32:33], v[0:1], s[90:91], v[64:65] op_sel_hi:[1,1,0]\n\t" \
    "v_permlane32_swap_b32 v76, v78\n\t" \
    "v_pk_fma_f32 v[0:1], v[0:1], s[90:91], v[66:67] op_sel_hi:[1,1,0] neg_lo:[1,0,0] neg_hi:[1,0,0]\n\t" \
    "v_pk_fma_f32 v[34:35], v[2:3], s[90:91], v[64:65] op_sel_hi:[1,1,0]\n\t" \
    "v_max_f32 v76, v76, v78\n\t" \
    "v_pk_fma_f32 v[2:3], v[2:3], s[90:91], v[66:67] op_sel_hi:[1,1,0] neg_lo:[1,0,0] neg_hi:[1,0,0]\n\t" \
    "v_pk_fma_f32 v[36:37], v[4:5], s[90:91], v[64:65] op_sel_hi:[1,1,0]\n\t" \
    "v_permlane16_swap_b32 v72, v76\n\t" \
    "v_pk_fma_f32 v[4:5], v[4:5], s[90:91], v[66:67] op_sel_hi:[1,1,0] neg_lo:[1,0,0] neg_hi:[1,0,0]\n\t" \
    "v_pk_fma_f32 v[38:39], v[6:7], s[90:91], v[64:65] op_sel_hi:[1,1,0]\n\t" \
    "v_max_f32 v74, v72, v76\n\t" \
    "v_pk_fma_f32 v[6:7], v[6:7], s[90:91], v[66:67] op_sel_hi:[1,1,0] neg_lo:[1,0,0] neg_hi:[1,0,0]\n\t" \
    "v_exp_f32 v32, v32\n\t" \
    "v_mul_f32 v72, 0xbfb8aa3b, v74\n\t" \
    "v_mul_f32 v76, 0xbfb8aa3b, v74\n\t" \
    "v_exp_f32 v0, v0\n\t" \
    "v_exp_f32 v33, v33\n\t" \
    "v_permlane16_swap_b32 v72, v76\n\t" \
    "v_exp_f32 v1, v1\n\t" \
    "v_exp_f32 v34, v34\n\t" \
    "v_mov_b32 v74, v72\n\t" \
    "v_mov_b32 v78, v76\n\t" \
    "v_exp_f32 v2, v2\n\t" \
    "v_permlane32_swap_b32 v72, v74\n\t" \
    "v_permlane32_swap_b32 v76, v78\n\t" \
    "v_exp_f32 v35, v35\n\t" \
    "v_exp_f32 v3, v3\n\t" \
    "v_exp_f32 v36, v36\n\t" \
    "v_pk_add_f32 v[64:65], v[32:33], v[34:35]\n\t" \
    "v_pk_add_f32 v[66:67], v[0:1], v[2:3]\n\t" \
    "v_exp_f32 v4, v4\n\t" \
    "v_exp_f32 v37, v37\n\t" \
    "v_exp_f32 v5, v5\n\t" \
    "v_exp_f32 v38, v38\n\t" \
    "v_pk_add_f32 v[64:65], v[64:65], v[36:37]\n\t" \
    "v_pk_add_f32 v[66:67], v[66:67], v[4:5]\n\t" \
    "v_exp_f32 v6, v6\n\t" \
    "v_exp_f32 v39, v39\n\t" \
    "v_exp_f32 v7, v7\n\t" \
    "v_pk_fma_f32 v[40:41], v[8:9], s[90:91], v[68:69] op_sel_hi:[1,1,0]\n\t" \
    "v_pk_add_f32 v[64:65], v[64:65], v[38:39]\n\t" \
    "v_pk_add_f32 v[66:67], v[66:67], v[6:7]\n\t" \
    "v_pk_fma_f32 v[8:9], v[8:9], s[90:91], v[70:71] op_sel_hi:[1,1,0] neg_lo:[1,0,0] neg_hi:[1,0,0]\n\t" \
    "v_add_f32 v64, v64, v65\n\t" \
    "v_add_f32 v66, v66, v67\n\t" \
    "v_pk_fma_f32 v[42:43], v[10:11], s[90:91], v[68:69] op_sel_hi:[1,1,0]\n\t" \
    "v_pk_fma_f32 v[10:11], v[10:11], s[90:91], v[70:71] op_sel_hi:[1,1,0] neg_lo:[1,0,0] neg_hi:[1,0,0]\n\t" \
    "v_permlane32_swap_b32 v64, v66\n\t" \
    "v_pk_fma_f32 v[44:45], v[12:13], s[90:91], v[68:69] op_sel_hi:[1,1,0]\n\t" \
    "v_pk_fma_f32 v[12:13], v[12:13], s[90:91], v[70:71] op_sel_hi:[1,1,0] neg_lo:[1,0,0] neg_hi:[1,0,0]\n\t" \
    "v_add_f32 v64, v64, v66\n\t" \
    "v_pk_fma_f32 v[46:47], v[14:15], s[90:91], v[68:69] op_sel_hi:[1,1,0]\n\t" \
    "v_pk_fma_f32 v[14:15], v[14:15], s[90:91], v[70:71] op_sel_hi:[1,1,0] neg_lo:[1,0,0] neg_hi:[1,0,0]\n\t" \
    "v_exp_f32 v40, v40\n\t" \
    "v_exp_f32 v8, v8\n\t" \
    "v_exp_f32 v41, v41\n\t" \
    "v_exp_f32 v9, v9\n\t" \
    "v_exp_f32 v42, v42\n\t" \
    "v_exp_f32 v10, v10\n\t" \
    "v_exp_f32 v43, v43\n\t" \
    "v_exp_f32 v11, v11\n\t" \
    "v_exp_f32 v44, v44\n\t" \
    "v_pk_add_f32 v[68:69], v[40:41], v[42:43]\n\t" \
    "v_pk_add_f32 v[70:71], v[8:9], v[10:11]\n\t" \
    "v_exp_f32 v12, v12\n\t" \
    "v_exp_f32 v45, v45\n\t" \
    "v_exp_f32 v13, v13\n\t" \
    "v_exp_f32 v46, v46\n\t" \
    "v_pk_add_f32 v[68:69], v[68:69], v[44:45]\n\t" \
    "v_pk_add_f32 v[70:71], v[70:71], v[12:13]\n\t" \
    "v_exp_f32 v14, v14\n\t" \
    "v_exp_f32 v47, v47\n\t" \
    "v_exp_f32 v15, v15\n\t" \
    "v_pk_fma_f32 v[48:49], v[16:17], s[90:91], v[72:73] op_sel_hi:[1,1,0]\n\t" \
    "v_pk_add_f32 v[68:69], v[68:69], v[46:47]\n\t" \
    "v_pk_add_f32 v[70:71], v[70:71], v[14:15]\n\t" \
    "v_pk_fma_f32 v[16:17], v[16:17], s[90:91], v[74:75] op_sel_hi:[1,1,0] neg_lo:[1,0,0] neg_hi:[1,0,0]\n\t" \
    "v_add_f32 v68, v68, v69\n\t" \
    "v_add_f32 v70, v70, v71\n\t" \
    "v_pk_fma_f32 v[50:51], v[18:19], s[90:91], v[72:73] op_sel_hi:[1,1,0]\n\t" \
    "v_pk_fma_f32 v[18:19], v[18:19], s[90:91], v[74:75] op_sel_hi:[1,1,0] neg_lo:[1,0,0] neg_hi:[1,0,0]\n\t" \
    "v_permlane32_swap_b32 v68, v70\n\t" \
    "v_pk_fma_f32 v[52:53], v[20:21], s[90:91], v[72:73] op_sel_hi:[1,1,0]\n\t" \
    "v_pk_fma_f32 v[20:21], v[20:21], s[90:91], v[74:75] op_sel_hi:[1,1,0] neg_lo:[1,0,0] neg_hi:[1,0,0]\n\t" \
    "v_add_f32 v68, v68, v70\n\t" \
    "v_pk_fma_f32 v[54:55], v[22:23], s[90:91], v[72:73] op_sel_hi:[1,1,0]\n\t" \
    "v_pk_fma_f32 v[22:23], v[22:23], s[90:91], v[74:75] op_sel_hi:[1,1,0] neg_lo:[1,0,0] neg_hi:[1,0,0]\n\t" \
    "v_permlane16_swap_b32 v64, v68\n\t" \
    "v_exp_f32 v48, v48\n\t" \
    "v_exp_f32 v16, v16\n\t" \
    "v_add_f32 v66, v64, v68\n\t" \
    "v_exp_f32 v49, v49\n\t" \
    "v_exp_f32 v17, v17\n\t" \
    "v_rcp_f32 v64, v66\n\t" \
    "v_exp_f32 v50, v50\n\t" \
    "v_exp_f32 v18, v18\n\t" \
    "v_mov_b32 v68, v64\n\t" \
    "v_exp_f32 v51, v51\n\t" \
    "v_exp_f32 v19, v19\n\t" \
    "v_permlane16_swap_b32 v64, v68\n\t" \
    "v_pk_add_f32 v[72:73], v[48:49], v[50:51]\n\t" \
    "v_pk_add_f32 v[74:75], v[16:17], v[18:19]\n\t" \
    "v_mov_b32 v66, v64\n\t" \
    "v_mov_b32 v70, v68\n\t" \
    "v_exp_f32 v52, v52\n\t" \
    "v_permlane32_swap_b32 v64, v66\n\t" \
    "v_permlane32_swap_b32 v68, v70\n\t" \
    "v_exp_f32 v20, v20\n\t" \
    "v_exp_f32 v53, v53\n\t" \
    "v_exp_f32 v21, v21\n\t" \
    "v_exp_f32 v54, v54\n\t" \
    "v_pk_add_f32 v[72:73], v[72:73], v[52:53]\n\t" \
    "v_pk_add_f32 v[74:75], v[74:75], v[20:21]\n\t" \
    "v_exp_f32 v22, v22\n\t" \
    "v_exp_f32 v55, v55\n\t" \
    "v_exp_f32 v23, v23\n\t" \
    "v_pk_fma_f32 v[56:57], v[24:25], s[90:91], v[76:77] op_sel_hi:[1,1,0]\n\t" \
    "v_pk_add_f32 v[72:73], v[72:73], v[54:55]\n\t" \
    "v_pk_add_f32 v[74:75], v[74:75], v[22:23]\n\t" \
    "v_pk_fma_f32 v[24:25], v[24:25], s[90:91], v[78:79] op_sel_hi:[1,1,0] neg_lo:[1,0,0] neg_hi:[1,0,0]\n\t" \
    "v_add_f32 v72, v72, v73\n\t" \
    "v_add_f32 v74, v74, v75\n\t" \
    "v_pk_fma_f32 v[58:59], v[26:27], s[90:91], v[76:77] op_sel_hi:[1,1,0]\n\t" \
    "v_pk_fma_f32 v[26:27], v[26:27], s[90:91], v[78:79] op_sel_hi:[1,1,0] neg_lo:[1,0,0] neg_hi:[1,0,0]\n\t" \
    "v_permlane32_swap_b32 v72, v74\n\t" \
    "v_pk_fma_f32 v[60:61], v[28:29], s[90:91], v[76:77] op_sel_hi:[1,1,0]\n\t" \
    "v_pk_fma_f32 v[28:29], v[28:29], s[90:91], v[78:79] op_sel_hi:[1,1,0] neg_lo:[1,0,0] neg_hi:[1,0,0]\n\t" \
    "v_add_f32 v72, v72, v74\n\t" \
    "v_pk_fma_f32 v[62:63], v[30:31], s[90:91], v[76:77] op_sel_hi:[1,1,0]\n\t" \
    "v_pk_fma_f32 v[30:31], v[30:31], s[90:91], v[78:79] op_sel_hi:[1,1,0] neg_lo:[1,0,0] neg_hi:[1,0,0]\n\t" \
    "v_exp_f32 v56, v56\n\t" \
    "v_exp_f32 v24, v24\n\t" \
    "v_exp_f32 v57, v57\n\t" \
    "v_exp_f32 v25, v25\n\t" \
    "v_exp_f32 v58, v58\n\t" \
    "v_exp_f32 v26, v26\n\t" \
    "v_exp_f32 v59, v59\n\t" \
    "v_exp_f32 v27, v27\n\t" \
    "v_exp_f32 v60, v60\n\t" \
    "v_pk_add_f32 v[76:77], v[56:57], v[58:59]\n\t" \
    "v_pk_add_f32 v[78:79], v[24:25], v[26:27]\n\t" \
    "v_exp_f32 v28, v28\n\t" \
    "v_exp_f32 v61, v61\n\t" \
    "v_exp_f32 v29, v29\n\t" \
    "v_exp_f32 v62, v62\n\t" \
    "v_pk_add_f32 v[76:77], v[76:77], v[60:61]\n\t" \
    "v_pk_add_f32 v[78:79], v[78:79], v[28:29]\n\t" \
    "v_exp_f32 v30, v30\n\t" \
    "v_exp_f32 v63, v63\n\t" \
    "v_exp_f32 v31, v31\n\t" \
    "v_pk_mul_f32 v[0:1], v[0:1], v[66:67] op_sel_hi:[1,0]\n\t" \
    "v_pk_add_f32 v[76:77], v[76:77], v[62:63]\n\t" \
    "v_pk_add_f32 v[78:79], v[78:79], v[30:31]\n\t" \
    "v_pk_mul_f32 v[32:33], v[32:33], v[64:65] op_sel_hi:[1,0]\n\t" \
    "v_add_f32 v76, v76, v77\n\t" \
    "v_add_f32 v78, v78, v79\n\t" \
    "v_pk_mul_f32 v[2:3], v[2:3], v[66:67] op_sel_hi:[1,0]\n\t" \
    "v_pk_mul_f32 v[34:35], v[34:35], v[64:65] op_sel_hi:[1,0]\n\t" \
    "v_permlane32_swap_b32 v76, v78\n\t" \
    "v_pk_mul_f32 v[4:5], v[4:5], v[66:67] op_sel_hi:[1,0]\n\t" \
    "v_pk_mul_f32 v[36:37], v[36:37], v[64:65] op_sel_hi:[1,0]\n\t" \
    "v_add_f32 v76, v76, v78\n\t" \
    "v_pk_mul_f32 v[6:7], v[6:7], v[66:67] op_sel_hi:[1,0]\n\t" \
    "v_pk_mul_f32 v[38:39], v[38:39], v[64:65] op_sel_hi:[1,0]\n\t" \
    "v_permlane16_swap_b32 v72, v76\n\t" \
    "v_cvt_pk_bf16_f32 v7, v6, v7\n\t" \
    "v_cvt_pk_bf16_f32 v6, v4, v5\n\t" \
    "v_add_f32 v74, v72, v76\n\t" \
    "v_cvt_pk_bf16_f32 v5, v2, v3\n\t" \
    "v_cvt_pk_bf16_f32 v4, v0, v1\n\t" \
    "v_rcp_f32 v72, v74\n\t" \
    "v_cvt_pk_bf16_f32 v0, v32, v33\n\t" \
    "v_cvt_pk_bf16_f32 v1, v34, v35\n\t" \
    "v_mov_b32 v76, v72\n\t" \
    "v_cvt_pk_bf16_f32 v2, v36, v37\n\t" \
    "v_cvt_pk_bf16_f32 v3, v38, v39\n\t" \
    "v_permlane16_swap_b32 v72, v76\n\t" \
    "v_pk_mul_f32 v[8:9], v[8:9], v[70:71] op_sel_hi:[1,0]\n\t" \
    "v_pk_mul_f32 v[40:41], v[40:41], v[68:69] op_sel_hi:[1,0]\n\t" \
    "v_mov_b32 v74, v72\n\t" \
    "v_mov_b32 v78, v76\n\t" \
    "v_pk_mul_f32 v[10:11], v[10:11], v[70:71] op_sel_hi:[1,0]\n\t" \
    "v_permlane32_swap_b32 v72, v74\n\t" \
    "v_permlane32_swap_b32 v76, v78\n\t" \
    "v_pk_mul_f32 v[42:43], v[42:43], v[68:69] op_sel_hi:[1,0]\n\t" \
    "v_pk_mul_f32 v[12:13], v[12:13], v[70:71] op_sel_hi:[1,0]\n\t" \
    "v_pk_mul_f32 v[44:45], v[44:45], v[68:69] op_sel_hi:[1,0]\n\t" \
    "v_pk_mul_f32 v[14:15], v[14:15], v[70:71] op_sel_hi:[1,0]\n\t" \
    "v_pk_mul_f32 v[46:47], v[46:47], v[68:69] op_sel_hi:[1,0]\n\t" \
    "v_pk_mul_f32 v[16:17], v[16:17], v[74:75] op_sel_hi:[1,0]\n\t" \
    "v_cvt_pk_bf16_f32 v15, v14, v15\n\t" \
    "v_cvt_pk_bf16_f32 v14, v12, v13\n\t" \
    "v_cvt_pk_bf16_f32 v13, v10, v11\n\t" \
    "v_cvt_pk_bf16_f32 v12, v8, v9\n\t" \
    "v_cvt_pk_bf16_f32 v8, v40, v41\n\t" \
    "v_cvt_pk_bf16_f32 v9, v42, v43\n\t" \
    "v_cvt_pk_bf16_f32 v10, v44, v45\n\t" \
    "v_cvt_pk_bf16_f32 v11, v46, v47\n\t" \
    "v_pk_mul_f32 v[48:49], v[48:49], v[72:73] op_sel_hi:[1,0]\n\t" \
    "v_pk_mul_f32 v[18:19], v[18:19], v[74:75] op_sel_hi:[1,0]\n\t" \
    "v_pk_mul_f32 v[50:51], v[50:51], v[72:73] op_sel_hi:[1,0]\n\t" \
    "v_pk_mul_f32 v[20:21], v[20:21], v[74:75] op_sel_hi:[1,0]\n\t" \
    "v_pk_mul_f32 v[52:53], v[52:53], v[72:73] op_sel_hi:[1,0]\n\t" \
    "v_pk_mul_f32 v[22:23], v[22:23], v[74:75] op_sel_hi:[1,0]\n\t" \
    "v_pk_mul_f32 v[54:55], v[54:55], v[72:73] op_sel_hi:[1,0]\n\t" \
    "ds_read_b32 v237, v249 offset:8\n\t" \
    "v_pk_mul_f32 v[24:25], v[24:25], v[78:79] op_sel_hi:[1,0]\n\t" \
    "v_cvt_pk_bf16_f32 v23, v22, v23\n\t" \
    "v_cvt_pk_bf16_f32 v22, v20, v21\n\t" \
    "v_cvt_pk_bf16_f32 v21, v18, v19\n\t" \
    "v_cvt_pk_bf16_f32 v20, v16, v17\n\t" \
    "v_cvt_pk_bf16_f32 v16, v48, v49\n\t" \
    "v_cvt_pk_bf16_f32 v17, v50, v51\n\t" \
    "v_cvt_pk_bf16_f32 v18, v52, v53\n\t" \
    "v_cvt_pk_bf16_f32 v19, v54, v55\n\t" \
    "v_pk_mul_f32 v[56:57], v[56:57], v[76:77] op_sel_hi:[1,0]\n\t" \
    "v_pk_mul_f32 v[26:27], v[26:27], v[78:79] op_sel_hi:[1,0]\n\t" \
    "v_pk_mul_f32 v[58:59], v[58:59], v[76:77] op_sel_hi:[1,0]\n\t" \
    "v_pk_mul_f32 v[28:29], v[28:29], v[78:79] op_sel_hi:[1,0]\n\t" \
    "v_pk_mul_f32 v[60:61], v[60:61], v[76:77] op_sel_hi:[1,0]\n\t" \
    "v_pk_mul_f32 v[30:31], v[30:31], v[78:79] op_sel_hi:[1,0]\n\t" \
    "v_pk_mul_f32 v[62:63], v[62:63], v[76:77] op_sel_hi:[1,0]\n\t" \
    "v_cvt_pk_bf16_f32 v31, v30, v31\n\t" \
    "v_cvt_pk_bf16_f32 v30, v28, v29\n\t" \
    "v_cvt_pk_bf16_f32 v29, v26, v27\n\t" \
    "v_cvt_pk_bf16_f32 v28, v24, v25\n\t" \
    "v_cvt_pk_bf16_f32 v24, v56, v57\n\t" \
    "v_cvt_pk_bf16_f32 v25, v58, v59\n\t" \
    "v_cvt_pk_bf16_f32 v26, v60, v61\n\t" \
    "v_cvt_pk_bf16_f32 v27, v62, v63\n\t" \
    "s_setprio 2\n\t" \
    "s_waitcnt lgkmcnt(0)\n\t" \
    "v_readfirstlane_b32 s85, v237\n\t" \
    "s_add_u32 s86, s87, 0x10000\n\t" \
    "s_cmp_lt_u32 s85, 3\n\t" \
    "s_cbranch_scc1 .Lab_s7\n\t" \
    ".Lab_s7r:\n\t" \
    "s_mov_b32 s89, 0x400000\n\t" \
    "s_mov_b32 s82, 4\n\t" \
    ".Lab_k:\n\t" \
    "ds_read_b32 v237, v249 offset:8\n\t" \
    "ds_read_b128 v[32:35], v252\n\t" \
    "ds_read_b128 v[36:39], v252 offset:256\n\t" \
    "ds_read_b128 v[40:43], v253\n\t" \
    "ds_read_b128 v[44:47], v253 offset:1024\n\t" \
    "ds_read_b128 v[48:51], v253 offset:2048\n\t" \
    "ds_read_b128 v[52:55], v253 offset:3072\n\t" \
    "s_sub_u32 s89, s89, 1\n\t" \
    "s_cbranch_scc1 .Lab_kr\n\t" \
    "s_waitcnt lgkmcnt(6)\n\t" \
    "v_readfirstlane_b32 s85, v237\n\t" \
    "s_cmp_lt_u32 s85, s82\n\t" \
    "s_cbranch_scc1 .Lab_k\n\t" \
    ".Lab_kr:\n\t" \
    "s_waitcnt lgkmcnt(5)\n\t" \
    "v_mfma_f32_16x16x32_bf16 v[56:59], v[32:35], v[0:3], v[120:123]\n\t" \
    "ds_read_b32 v126, v249 offset:296\n\t" \
    "v_mfma_f32_16x16x32_bf16 v[60:63], v[32:35], v[8:11], v[120:123]\n\t" \
    "ds_read_b32 v127, v249 offset:308\n\t" \
    "v_mfma_f32_16x16x32_bf16 v[64:67], v[32:35], v[16:19], v[120:123]\n\t" \
    "v_cmp_eq_u32 vcc, 0, v244\n\t" \
    "v_mfma_f32_16x16x32_bf16 v[68:71], v[32:35], v[24:27], v[120:123]\n\t" \
    "v_lshrrev_b32 v237, 1, v251\n\t" \
    "v_bfrev_b32 v238, 1\n\t" \
    "s_waitcnt lgkmcnt(6)\n\t" \
    "v_mfma_f32_16x16x32_bf16 v[56:59], v[36:39], v[4:7], v[56:59]\n\t" \
    "v_mfma_f32_16x16x32_bf16 v[60:63], v[36:39], v[12:15], v[60:63]\n\t" \
    "v_cndmask_b32 v254, v238, v237, vcc\n\t" \
    "v_mfma_f32_16x16x32_bf16 v[64:67], v[36:39], v[20:23], v[64:67]\n\t" \
    "s_or_b32 s82, s85, 1\n\t" \
    "v_mfma_f32_16x16x32_bf16 v[68:71], v[36:39], v[28:31], v[68:71]\n\t" \
    "s_waitcnt lgkmcnt(5)\n\t" \
    "v_mfma_f32_16x16x32_bf16 v[72:75], v[40:43], v[0:3], 0\n\t" \
    "s_waitcnt lgkmcnt(4)\n\t" \
    "v_mfma_f32_16x16x32_bf16 v[76:79], v[44:47], v[0:3], 0\n\t" \
    "s_waitcnt lgkmcnt(3)\n\t" \
    "v_mfma_f32_16x16x32_bf16 v[72:75], v[48:51], v[4:7], v[72:75]\n\t" \
    "s_waitcnt lgkmcnt(2)\n\t" \
    "v_mfma_f32_16x16x32_bf16 v[76:79], v[52:55], v[4:7], v[76:79]\n\t" \
    "v_mfma_f32_16x16x32_bf16 v[80:83], v[40:43], v[8:11], 0\n\t" \
    "v_rcp_f32 v56, v56\n\t" \
    "v_mfma_f32_16x16x32_bf16 v[84:87], v[44:47], v[8:11], 0\n\t" \
    "v_rcp_f32 v60, v60\n\t" \
    "v_mfma_f32_16x16x32_bf16 v[80:83], v[48:51], v[12:15], v[80:83]\n\t" \
    "v_rcp_f32 v64, v64\n\t" \
    "v_mfma_f32_16x16x32_bf16 v[84:87], v[52:55], v[12:15], v[84:87]\n\t" \
    "v_rcp_f32 v68, v68\n\t" \
    "v_mfma_f32_16x16x32_bf16 v[88:91], v[40:43], v[16:19], 0\n\t" \
    "v_mfma_f32_16x16x32_bf16 v[92:95], v[44:47], v[16:19], 0\n\t" \
    "v_mul_f32 v72, v72, v56\n\t" \
    "v_mul_f32 v73, v73, v56\n\t" \
    "v_mfma_f32_16x16x32_bf16 v[88:91], v[48:51], v[20:23], v[88:91]\n\t" \
    "v_mul_f32 v74, v74, v56\n\t" \
    "v_mul_f32 v75, v75, v56\n\t" \
    "v_mfma_f32_16x16x32_bf16 v[92:95], v[52:55], v[20:23], v[92:95]\n\t" \
    "v_mul_f32 v76, v76, v56\n\t" \
    "v_mul_f32 v77, v77, v56\n\t" \
    "v_mfma_f32_16x16x32_bf16 v[96:99], v[40:43], v[24:27], 0\n\t" \
    "v_mul_f32 v78, v78, v56\n\t" \
    "v_mul_f32 v79, v79, v56\n\t" \
    "v_mfma_f32_16x16x32_bf16 v[100:103], v[44:47], v[24:27], 0\n\t" \
    "v_cvt_pk_bf16_f32 v104, v72, v73\n\t" \
    "v_cvt_pk_bf16_f32 v105, v74, v75\n\t" \
    "v_mfma_f32_16x16x32_bf16 v[96:99], v[48:51], v[28:31], v[96:99]\n\t" \
    "v_cvt_pk_bf16_f32 v106, v76, v77\n\t" \
    "v_cvt_pk_bf16_f32 v107, v78, v79\n\t" \
    "v_mfma_f32_16x16x32_bf16 v[100:103], v[52:55], v[28:31], v[100:103]\n\t" \
    "buffer_store_dwordx4 v[104:107], v251, %[rsb], s87 offen sc1\n\t" \
    "v_pk_mul_f32 v[80:81], v[80:81], v[60:61] op_sel_hi:[1,0]\n\t" \
    "v_pk_mul_f32 v[82:83], v[82:83], v[60:61] op_sel_hi:[1,0]\n\t" \
    "v_pk_mul_f32 v[84:85], v[84:85], v[60:61] op_sel_hi:[1,0]\n\t" \
    "v_pk_mul_f32 v[86:87], v[86:87], v[60:61] op_sel_hi:[1,0]\n\t" \
    "v_cvt_pk_bf16_f32 v108, v80, v81\n\t" \
    "v_cvt_pk_bf16_f32 v109, v82, v83\n\t" \
    "v_cvt_pk_bf16_f32 v110, v84, v85\n\t" \
    "v_cvt_pk_bf16_f32 v111, v86, v87\n\t" \
    "buffer_store_dwordx4 v[108:111], v251, %[rsb], s87 offen offset:1024 sc1\n\t" \
    "v_pk_mul_f32 v[88:89], v[88:89], v[64:65] op_sel_hi:[1,0]\n\t" \
    "v_pk_mul_f32 v[90:91], v[90:91], v[64:65] op_sel_hi:[1,0]\n\t" \
    "v_pk_mul_f32 v[92:93], v[92:93], v[64:65] op_sel_hi:[1,0]\n\t" \
    "v_pk_mul_f32 v[94:95], v[94:95], v[64:65] op_sel_hi:[1,0]\n\t" \
    "v_cvt_pk_bf16_f32 v112, v88, v89\n\t" \
    "v_cvt_pk_bf16_f32 v113, v90, v91\n\t" \
    "v_cvt_pk_bf16_f32 v114, v92, v93\n\t" \
    "v_cvt_pk_bf16_f32 v115, v94, v95\n\t" \
    "buffer_store_dwordx4 v[112:115], v251, %[rsb], s86 offen sc1\n\t" \
    "v_pk_mul_f32 v[96:97], v[96:97], v[68:69] op_sel_hi:[1,0]\n\t" \
    "v_pk_mul_f32 v[98:99], v[98:99], v[68:69] op_sel_hi:[1,0]\n\t" \
    "v_pk_mul_f32 v[100:101], v[100:101], v[68:69] op_sel_hi:[1,0]\n\t" \
    "v_pk_mul_f32 v[102:103], v[102:103], v[68:69] op_sel_hi:[1,0]\n\t" \
    "v_cvt_pk_bf16_f32 v116, v96, v97\n\t" \
    "v_cvt_pk_bf16_f32 v117, v98, v99\n\t" \
    "v_cvt_pk_bf16_f32 v118, v100, v101\n\t" \
    "v_cvt_pk_bf16_f32 v119, v102, v103\n\t" \
    "v_add3_u32 v32, v104, v105, 0\n\t" \
    "v_add3_u32 v32, v106, v107, v32\n\t" \
    "v_add3_u32 v33, v112, v113, 0\n\t" \
    "v_add3_u32 v33, v114, v115, v33\n\t" \
    "v_add3_u32 v32, v108, v109, v32\n\t" \
    "v_add3_u32 v32, v110, v111, v32\n\t" \
    "v_add3_u32 v33, v116, v117, v33\n\t" \
    "v_add3_u32 v33, v118, v119, v33\n\t" \
    "buffer_store_dwordx4 v[116:119], v251, %[rsb], s86 offen offset:1024 sc1\n\t" \
    "ds_read_b32 v36, v249 offset:8\n\t" \
    "v_permlane32_swap_b32 v32, v33\n\t" \
    "v_add_u32 v32, v32, v33\n\t" \
    "v_mov_b32 v33, v32\n\t" \
    "s_waitcnt lgkmcnt(2)\n\t" \
    "v_readfirstlane_b32 %[rid], v126\n\t" \
    "s_waitcnt lgkmcnt(1)\n\t" \
    "v_readfirstlane_b32 %[t3], v127\n\t" \
    "v_permlane16_swap_b32 v32, v33\n\t" \
    "v_add_u32 v32, v32, v33\n\t" \
    "v_mov_b32 v33, v236\n\t" \
    "ds_read_b32 v238, v249 offset:56\n\t" \
    "v_add_u32_dpp v32, v32, v32 row_ror:8 row_mask:0xf bank_mask:0xf bound_ctrl:1\n\t" \
    "v_mov_b32 v237, 12\n\t" \
    "v_and_b32 v34, 15, v250\n\t" \
    "v_add_u32_dpp v32, v32, v32 row_ror:4 row_mask:0xf bank_mask:0xf bound_ctrl:1\n\t" \
    "s_waitcnt lgkmcnt(1)\n\t" \
    "v_readfirstlane_b32 s80, v36\n\t" \
    "s_and_b32 s85, %[rid], 15\n\t" \
    "v_add_u32_dpp v32, v32, v32 row_ror:2 row_mask:0xf bank_mask:0xf bound_ctrl:1\n\t" \
    "v_cmp_eq_u32_e64 %[rm], s85, v34\n\t" \
    "s_cmp_lg_u32 s80, s82\n\t" \
    "v_add_u32_dpp v32, v32, v32 row_ror:1 row_mask:0xf bank_mask:0xf bound_ctrl:1\n\t" \
    "s_cbranch_scc1 .Lab_sa\n\t" \
    ".Lab_sar:\n\t" \
    "buffer_store_dwordx2 v[32:33], v254, %[rsb], s88 offen sc1\n\t" \
    "ds_write_b32 v248, v237\n\t" \
    "v_and_b32 v252, 48, v250\n\t" \
    "s_lshr_b32 s85, %[rid], 5\n\t" \
    "v_add_u32 v252, 161536, v252\n\t" \
    "s_cmp_eq_u32 s85, %[own]\n\t" \
    "s_cselect_b64 %[rm], %[rm], 0\n\t" \
    "s_waitcnt vmcnt(5)\n\t" \
    "v_cmp_ne_u32 vcc, v236, v233\n\t" \
    "v_cmp_ne_u32_e64 s[92:93], v236, v235\n\t" \
    "s_nop 0\n\t" \
    "s_or_b64 vcc, vcc, s[92:93]\n\t" \
    "s_cbranch_scc1 .Lab_s8\n\t" \
    ".Lab_s8r:\n\t" \
    "v_sub_f32 v254, 1.0, v232\n\t" \
    "v_sub_f32 v255, 1.0, v234\n\t" \
    "v_cvt_pk_bf16_f32 v255, v254, v255\n\t" \
    "v_lshlrev_b32 v254, 16, v255\n\t" \
    "v_and_b32 v255, 0xffff0000, v255\n\t" \
    "s_waitcnt lgkmcnt(1)\n\t" \
    "v_readfirstlane_b32 s85, v238\n\t" \
    "s_cmp_lt_u32 s85, 2\n\t" \
    "s_cbranch_scc1 .Lab_s9\n\t" \
    ".Lab_s9r:\n\t" \
    "s_waitcnt lgkmcnt(0)\n\t"
#define PHASE_C_ASM \
    "s_branch .Lpad_c0\n\t" \
    ".Lc_x:\n\t" \
    "s_waitcnt vmcnt(0) lgkmcnt(0)\n\t" \
    "s_branch .Lc_end\n\t" \
    ".Lc_w0:\n\t" \
    "s_mov_b32 s86, 0x400000\n\t" \
    ".Lc_a0:\n\t" \
    "ds_read_b32 v160, v249 offset:56\n\t" \
    "s_waitcnt lgkmcnt(0)\n\t" \
    "v_readfirstlane_b32 s83, v160\n\t" \
    "s_sub_u32 s86, s86, 1\n\t" \
    "s_cbranch_scc1 .Lc_r0\n\t" \
    "s_cmp_lt_u32 s83, s82\n\t" \
    "s_cbranch_scc1 .Lc_a0\n\t" \
    "s_branch .Lc_r0\n\t" \
    ".Lc_w1:\n\t" \
    "s_mov_b32 s86, 0x400000\n\t" \
    ".Lc_a1:\n\t" \
    "ds_read_b32 v160, v249 offset:56\n\t" \
    "s_waitcnt lgkmcnt(0)\n\t" \
    "v_readfirstlane_b32 s83, v160\n\t" \
    "s_sub_u32 s86, s86, 1\n\t" \
    "s_cbranch_scc1 .Lc_r1\n\t" \
    "s_cmp_lt_u32 s83, s82\n\t" \
    "s_cbranch_scc1 .Lc_a1\n\t" \
    "s_branch .Lc_r1\n\t" \
    ".Lc_y:\n\t" \
    "s_mov_b32 s86, 0x400000\n\t" \
    ".Lc_ya:\n\t" \
    "s_waitcnt lgkmcnt(0)\n\t" \
    "v_readfirstlane_b32 s83, v231\n\t" \
    "s_sub_u32 s86, s86, 1\n\t" \
    "s_cbranch_scc1 .Lc_yb\n\t" \
    "s_cmp_lg_u32 s83, 0\n\t" \
    "s_cbranch_scc1 .Lc_yb\n\t" \
    "ds_read_b32 v231, v249 offset:68\n\t" \
    "ds_read_b128 v[96:99], v252\n\t" \
    "ds_read_b128 v[100:103], v252 offset:64\n\t" \
    "ds_read_b128 v[104:107], v252 offset:128\n\t" \
    "ds_read_b128 v[108:111], v252 offset:192\n\t" \
    "s_branch .Lc_ya\n\t" \
    ".Lc_yb:\n\t" \
    "s_mov_b64 exec, %[rm]\n\t" \
    "s_bitcmp1_b32 %[rid], 4\n\t" \
    "s_cbranch_scc1 .Lc_yc\n\t" \
    "v_pk_mul_f32 v[194:195], v[96:97], v[254:255] op_sel_hi:[1,0]\n\t" \
    "v_pk_mul_f32 v[196:197], v[98:99], v[254:255] op_sel_hi:[1,0]\n\t" \
    "v_pk_mul_f32 v[198:199], v[100:101], v[254:255] op_sel_hi:[1,0]\n\t" \
    "v_pk_mul_f32 v[200:201], v[102:103], v[254:255] op_sel_hi:[1,0]\n\t" \
    "v_pk_mul_f32 v[202:203], v[104:105], v[254:255] op_sel_hi:[1,0]\n\t" \
    "v_pk_mul_f32 v[204:205], v[106:107], v[254:255] op_sel_hi:[1,0]\n\t" \
    "v_pk_mul_f32 v[206:207], v[108:109], v[254:255] op_sel_hi:[1,0]\n\t" \
    "v_pk_mul_f32 v[208:209], v[110:111], v[254:255] op_sel_hi:[1,0]\n\t" \
    "s_mov_b64 exec, -1\n\t" \
    "s_branch .Lc_yr\n\t" \
    ".Lc_yc:\n\t" \
    "v_pk_mul_f32 v[210:211], v[96:97], v[254:255] op_sel:[0,1] op_sel_hi:[1,1]\n\t" \
    "v_pk_mul_f32 v[212:213], v[98:99], v[254:255] op_sel:[0,1] op_sel_hi:[1,1]\n\t" \
    "v_pk_mul_f32 v[214:215], v[100:101], v[254:255] op_sel:[0,1] op_sel_hi:[1,1]\n\t" \
    "v_pk_mul_f32 v[216:217], v[102:103], v[254:255] op_sel:[0,1] op_sel_hi:[1,1]\n\t" \
    "v_pk_mul_f32 v[218:219], v[104:105], v[254:255] op_sel:[0,1] op_sel_hi:[1,1]\n\t" \
    "v_pk_mul_f32 v[220:221], v[106:107], v[254:255] op_sel:[0,1] op_sel_hi:[1,1]\n\t" \
    "v_pk_mul_f32 v[222:223], v[108:109], v[254:255] op_sel:[0,1] op_sel_hi:[1,1]\n\t" \
    "v_pk_mul_f32 v[224:225], v[110:111], v[254:255] op_sel:[0,1] op_sel_hi:[1,1]\n\t" \
    "s_mov_b64 exec, -1\n\t" \
    "s_branch .Lc_yr\n\t" \
    ".p2align 12\n\t" \
    ".Lpad_c0:\n\t" \
    "s_cbranch_execz .Lpad_ret\n\t" \
    "s_setprio 2\n\t" \
    "v_xor_b32 v226, %[flip], v245\n\t" \
    "v_xor_b32 v227, 32, v226\n\t" \
    "v_xor_b32 v228, 64, v226\n\t" \
    "v_xor_b32 v229, 96, v226\n\t" \
    "v_and_b32 v231, 0x7f, v244\n\t" \
    "s_lshl_b32 s80, %[grp], 14\n\t" \
    "s_lshl_b32 s83, %[grp], 3\n\t" \
    "s_lshl_b32 s84, 0xffff, s83\n\t" \
    "s_lshl_b32 s85, %[grp], 6\n\t" \
    "s_add_u32 s85, s85, %[sog]\n\t" \
    "s_mov_b32 s81, 256\n\t" \
    "v_xor_b32 v1, 32, v246\n\t" \
    "v_xor_b32 v2, 64, v246\n\t" \
    "v_xor_b32 v3, 96, v246\n\t" \
    ".Lc_t:\n\t" \
    "s_memrealtime s[86:87]\n\t" \
    "s_sub_u32 s81, s81, 1\n\t" \
    "s_cbranch_scc1 .Lc_go\n\t" \
    "s_waitcnt lgkmcnt(0)\n\t" \
    "s_sub_u32 s86, %[t3], s86\n\t" \
    "s_cmp_lt_i32 s86, 5\n\t" \
    "s_cbranch_scc1 .Lc_go\n\t" \
    "s_cmp_lt_i32 s86, 13\n\t" \
    "s_cbranch_scc1 .Lc_t\n\t" \
    "s_sleep 2\n\t" \
    "s_branch .Lc_t\n\t" \
    ".Lc_go:\n\t" \
    "buffer_load_dwordx2 v[160:161], v231, %[rsb], s85 offen sc1\n\t" \
    "buffer_load_dwordx4 v[32:35], v240, %[rsb], s80 offen offset:2048 sc1\n\t" \
    "buffer_load_dwordx4 v[36:39], v240, %[rsb], s80 offen offset:3072 sc1\n\t" \
    "buffer_load_dwordx4 v[40:43], v241, %[rsb], s80 offen sc1\n\t" \
    "buffer_load_dwordx4 v[44:47], v241, %[rsb], s80 offen offset:1024 sc1\n\t" \
    "buffer_load_dwordx4 v[48:51], v241, %[rsb], s80 offen offset:2048 sc1\n\t" \
    "buffer_load_dwordx4 v[52:55], v241, %[rsb], s80 offen offset:3072 sc1\n\t" \
    "buffer_load_dwordx4 v[56:59], v242, %[rsb], s80 offen sc1\n\t" \
    "buffer_load_dwordx4 v[60:63], v242, %[rsb], s80 offen offset:1024 sc1\n\t" \
    "buffer_load_dwordx4 v[64:67], v242, %[rsb], s80 offen offset:2048 sc1\n\t" \
    "buffer_load_dwordx4 v[68:71], v242, %[rsb], s80 offen offset:3072 sc1\n\t" \
    "buffer_load_dwordx4 v[72:75], v243, %[rsb], s80 offen sc1\n\t" \
    "buffer_load_dwordx4 v[76:79], v243, %[rsb], s80 offen offset:1024 sc1\n\t" \
    "buffer_load_dwordx4 v[80:83], v243, %[rsb], s80 offen offset:2048 sc1\n\t" \
    "buffer_load_dwordx4 v[84:87], v243, %[rsb], s80 offen offset:3072 sc1\n\t" \
    "s_add_u32 s80, s80, 0x4000\n\t" \
    "s_and_b32 s80, s80, 0xc000\n\t" \
    "ds_read_b64 v[196:197], v246\n\t" \
    "ds_read_b64 v[200:201], v1\n\t" \
    "ds_read_b64 v[204:205], v2\n\t" \
    "ds_read_b64 v[208:209], v3\n\t" \
    "ds_read_b64 v[212:213], v246 offset:2048\n\t" \
    "ds_read_b64 v[216:217], v1 offset:2048\n\t" \
    "ds_read_b64 v[220:221], v2 offset:2048\n\t" \
    "ds_read_b64 v[224:225], v3 offset:2048\n\t" \
    "ds_read_b64_tr_b16 v[96:97], v226\n\t" \
    "ds_read_b64_tr_b16 v[98:99], v226 offset:2048\n\t" \
    "ds_read_b64_tr_b16 v[100:101], v227\n\t" \
    "ds_read_b64_tr_b16 v[102:103], v227 offset:2048\n\t" \
    "ds_read_b64_tr_b16 v[104:105], v228\n\t" \
    "ds_read_b64_tr_b16 v[106:107], v228 offset:2048\n\t" \
    "ds_read_b64_tr_b16 v[108:109], v229\n\t" \
    "ds_read_b64_tr_b16 v[110:111], v229 offset:2048\n\t" \
    "ds_read_b64_tr_b16 v[112:113], v226 offset:4096\n\t" \
    "ds_read_b64_tr_b16 v[114:115], v226 offset:6144\n\t" \
    "ds_read_b64_tr_b16 v[116:117], v227 offset:4096\n\t" \
    "ds_read_b64_tr_b16 v[118:119], v227 offset:6144\n\t" \
    "ds_read_b64_tr_b16 v[120:121], v228 offset:4096\n\t" \
    "ds_read_b64_tr_b16 v[122:123], v228 offset:6144\n\t" \
    "ds_read_b64_tr_b16 v[124:125], v229 offset:4096\n\t" \
    "ds_read_b64_tr_b16 v[126:127], v229 offset:6144\n\t" \
    "v_mov_b32 v0, 0\n\t" \
    "v_mov_b32 v1, 0\n\t" \
    "v_mov_b32 v2, 0\n\t" \
    "v_mov_b32 v3, 0\n\t" \
    "v_mov_b32 v4, 0\n\t" \
    "v_mov_b32 v5, 0\n\t" \
    "v_mov_b32 v6, 0\n\t" \
    "v_mov_b32 v7, 0\n\t" \
    "v_mov_b32 v8, 0\n\t" \
    "v_mov_b32 v9, 0\n\t" \
    "v_mov_b32 v10, 0\n\t" \
    "v_mov_b32 v11, 0\n\t" \
    "v_mov_b32 v12, 0\n\t" \
    "v_mov_b32 v13, 0\n\t" \
    "v_mov_b32 v14, 0\n\t" \
    "v_mov_b32 v15, 0\n\t" \
    "v_mov_b32 v16, 0\n\t" \
    "v_mov_b32 v17, 0\n\t" \
    "v_mov_b32 v18, 0\n\t" \
    "v_mov_b32 v19, 0\n\t" \
    "v_mov_b32 v20, 0\n\t" \
    "v_mov_b32 v21, 0\n\t" \
    "v_mov_b32 v22, 0\n\t" \
    "v_mov_b32 v23, 0\n\t" \
    "v_mov_b32 v24, 0\n\t" \
    "v_mov_b32 v25, 0\n\t" \
    "v_mov_b32 v26, 0\n\t" \
    "v_mov_b32 v27, 0\n\t" \
    "v_mov_b32 v28, 0\n\t" \
    "v_mov_b32 v29, 0\n\t" \
    "v_mov_b32 v30, 0\n\t" \
    "v_mov_b32 v31, 0\n\t" \
    "s_waitcnt lgkmcnt(0)\n\t" \
    "v_lshlrev_b32 v194, 16, v196\n\t" \
    "v_and_b32 v195, 0xffff0000, v196\n\t" \
    "v_lshlrev_b32 v196, 16, v197\n\t" \
    "v_and_b32 v197, 0xffff0000, v197\n\t" \
    "v_lshlrev_b32 v198, 16, v200\n\t" \
    "v_and_b32 v199, 0xffff0000, v200\n\t" \
    "v_lshlrev_b32 v200, 16, v201\n\t" \
    "v_and_b32 v201, 0xffff0000, v201\n\t" \
    "v_lshlrev_b32 v202, 16, v204\n\t" \
    "v_and_b32 v203, 0xffff0000, v204\n\t" \
    "v_lshlrev_b32 v204, 16, v205\n\t" \
    "v_and_b32 v205, 0xffff0000, v205\n\t" \
    "v_lshlrev_b32 v206, 16, v208\n\t" \
    "v_and_b32 v207, 0xffff0000, v208\n\t" \
    "v_lshlrev_b32 v208, 16, v209\n\t" \
    "v_and_b32 v209, 0xffff0000, v209\n\t" \
    "v_lshlrev_b32 v210, 16, v212\n\t" \
    "v_and_b32 v211, 0xffff0000, v212\n\t" \
    "v_lshlrev_b32 v212, 16, v213\n\t" \
    "v_and_b32 v213, 0xffff0000, v213\n\t" \
    "v_lshlrev_b32 v214, 16, v216\n\t" \
    "v_and_b32 v215, 0xffff0000, v216\n\t" \
    "v_lshlrev_b32 v216, 16, v217\n\t" \
    "v_and_b32 v217, 0xffff0000, v217\n\t" \
    "v_lshlrev_b32 v218, 16, v220\n\t" \
    "v_and_b32 v219, 0xffff0000, v220\n\t" \
    "v_lshlrev_b32 v220, 16, v221\n\t" \
    "v_and_b32 v221, 0xffff0000, v221\n\t" \
    "v_lshlrev_b32 v222, 16, v224\n\t" \
    "v_and_b32 v223, 0xffff0000, v224\n\t" \
    "v_lshlrev_b32 v224, 16, v225\n\t" \
    "v_and_b32 v225, 0xffff0000, v225\n\t" \
    "v_pk_mul_f32 v[194:195], v[194:195], v[254:255] op_sel_hi:[1,0]\n\t" \
    "v_pk_mul_f32 v[196:197], v[196:197], v[254:255] op_sel_hi:[1,0]\n\t" \
    "v_pk_mul_f32 v[198:199], v[198:199], v[254:255] op_sel_hi:[1,0]\n\t" \
    "v_pk_mul_f32 v[200:201], v[200:201], v[254:255] op_sel_hi:[1,0]\n\t" \
    "v_pk_mul_f32 v[202:203], v[202:203], v[254:255] op_sel_hi:[1,0]\n\t" \
    "v_pk_mul_f32 v[204:205], v[204:205], v[254:255] op_sel_hi:[1,0]\n\t" \
    "v_pk_mul_f32 v[206:207], v[206:207], v[254:255] op_sel_hi:[1,0]\n\t" \
    "v_pk_mul_f32 v[208:209], v[208:209], v[254:255] op_sel_hi:[1,0]\n\t" \
    "v_pk_mul_f32 v[210:211], v[210:211], v[254:255] op_sel:[0,1] op_sel_hi:[1,1]\n\t" \
    "v_pk_mul_f32 v[212:213], v[212:213], v[254:255] op_sel:[0,1] op_sel_hi:[1,1]\n\t" \
    "v_pk_mul_f32 v[214:215], v[214:215], v[254:255] op_sel:[0,1] op_sel_hi:[1,1]\n\t" \
    "v_pk_mul_f32 v[216:217], v[216:217], v[254:255] op_sel:[0,1] op_sel_hi:[1,1]\n\t" \
    "v_pk_mul_f32 v[218:219], v[218:219], v[254:255] op_sel:[0,1] op_sel_hi:[1,1]\n\t" \
    "v_pk_mul_f32 v[220:221], v[220:221], v[254:255] op_sel:[0,1] op_sel_hi:[1,1]\n\t" \
    "v_pk_mul_f32 v[222:223], v[222:223], v[254:255] op_sel:[0,1] op_sel_hi:[1,1]\n\t" \
    "v_pk_mul_f32 v[224:225], v[224:225], v[254:255] op_sel:[0,1] op_sel_hi:[1,1]\n\t" \
    "v_mov_b32 v230, 0\n\t" \
    "s_mov_b32 s81, 2\n\t" \
    "s_mov_b32 s82, 1\n\t" \
    "s_mov_b32 %[st], 1\n\t" \
    "s_waitcnt vmcnt(14)\n\t" \
    "v_cmp_ne_u32 vcc, v236, v161\n\t" \
    "s_nop 1\n\t" \
    "s_and_b32 vcc_lo, vcc_lo, s84\n\t" \
    "s_cbranch_scc1 .Lc_x\n\t" \
    ".Lc_l:\n\t" \
    "s_waitcnt vmcnt(12) lgkmcnt(8)\n\t" \
    "v_mfma_f32_16x16x32_bf16 v[0:3], v[96:99], v[32:35], v[0:3]\n\t" \
    "ds_read_b64_tr_b16 v[128:129], v226 offset:8192\n\t" \
    "buffer_load_dwordx4 v[88:91], v240, %[rsb], s80 offen sc1\n\t" \
    "v_mfma_f32_16x16x32_bf16 v[16:19], v[96:99], v[36:39], v[16:19]\n\t" \
    "ds_read_b64_tr_b16 v[130:131], v226 offset:10240\n\t" \
    "v_add3_u32 v230, v32, v33, v230\n\t" \
    "v_mfma_f32_16x16x32_bf16 v[4:7], v[100:103], v[32:35], v[4:7]\n\t" \
    "ds_read_b64_tr_b16 v[132:133], v227 offset:8192\n\t" \
    "buffer_load_dwordx4 v[92:95], v240, %[rsb], s80 offen offset:1024 sc1\n\t" \
    "v_mfma_f32_16x16x32_bf16 v[20:23], v[100:103], v[36:39], v[20:23]\n\t" \
    "ds_read_b64_tr_b16 v[134:135], v227 offset:10240\n\t" \
    "v_add3_u32 v230, v34, v35, v230\n\t" \
    "v_mfma_f32_16x16x32_bf16 v[8:11], v[104:107], v[32:35], v[8:11]\n\t" \
    "ds_read_b64_tr_b16 v[136:137], v228 offset:8192\n\t" \
    "v_add3_u32 v230, v36, v37, v230\n\t" \
    "v_mfma_f32_16x16x32_bf16 v[24:27], v[104:107], v[36:39], v[24:27]\n\t" \
    "ds_read_b64_tr_b16 v[138:139], v228 offset:10240\n\t" \
    "v_add3_u32 v230, v38, v39, v230\n\t" \
    "v_mfma_f32_16x16x32_bf16 v[12:15], v[108:111], v[32:35], v[12:15]\n\t" \
    "ds_read_b64_tr_b16 v[140:141], v229 offset:8192\n\t" \
    "v_mfma_f32_16x16x32_bf16 v[28:31], v[108:111], v[36:39], v[28:31]\n\t" \
    "ds_read_b64_tr_b16 v[142:143], v229 offset:10240\n\t" \
    "s_waitcnt vmcnt(12) lgkmcnt(8)\n\t" \
    "v_mfma_f32_16x16x32_bf16 v[0:3], v[112:115], v[40:43], v[0:3]\n\t" \
    "ds_read_b64_tr_b16 v[144:145], v226 offset:12288\n\t" \
    "buffer_load_dwordx4 v[32:35], v240, %[rsb], s80 offen offset:2048 sc1\n\t" \
    "v_mfma_f32_16x16x32_bf16 v[16:19], v[112:115], v[44:47], v[16:19]\n\t" \
    "ds_read_b64_tr_b16 v[146:147], v226 offset:14336\n\t" \
    "v_add3_u32 v230, v40, v41, v230\n\t" \
    "v_mfma_f32_16x16x32_bf16 v[4:7], v[116:119], v[40:43], v[4:7]\n\t" \
    "ds_read_b64_tr_b16 v[148:149], v227 offset:12288\n\t" \
    "buffer_load_dwordx4 v[36:39], v240, %[rsb], s80 offen offset:3072 sc1\n\t" \
    "v_mfma_f32_16x16x32_bf16 v[20:23], v[116:119], v[44:47], v[20:23]\n\t" \
    "ds_read_b64_tr_b16 v[150:151], v227 offset:14336\n\t" \
    "v_add3_u32 v230, v42, v43, v230\n\t" \
    "v_mfma_f32_16x16x32_bf16 v[8:11], v[120:123], v[40:43], v[8:11]\n\t" \
    "ds_read_b64_tr_b16 v[152:153], v228 offset:12288\n\t" \
    "v_add3_u32 v230, v44, v45, v230\n\t" \
    "v_mfma_f32_16x16x32_bf16 v[24:27], v[120:123], v[44:47], v[24:27]\n\t" \
    "ds_read_b64_tr_b16 v[154:155], v228 offset:14336\n\t" \
    "v_add3_u32 v230, v46, v47, v230\n\t" \
    "v_mfma_f32_16x16x32_bf16 v[12:15], v[124:127], v[40:43], v[12:15]\n\t" \
    "ds_read_b64_tr_b16 v[156:157], v229 offset:12288\n\t" \
    "v_mfma_f32_16x16x32_bf16 v[28:31], v[124:127], v[44:47], v[28:31]\n\t" \
    "ds_read_b64_tr_b16 v[158:159], v229 offset:14336\n\t" \
    "s_waitcnt vmcnt(12) lgkmcnt(8)\n\t" \
    "v_mfma_f32_16x16x32_bf16 v[0:3], v[128:131], v[48:51], v[0:3]\n\t" \
    "ds_read_b64_tr_b16 v[96:97], v226 offset:16384\n\t" \
    "buffer_load_dwordx4 v[40:43], v241, %[rsb], s80 offen sc1\n\t" \
    "v_mfma_f32_16x16x32_bf16 v[16:19], v[128:131], v[52:55], v[16:19]\n\t" \
    "ds_read_b64_tr_b16 v[98:99], v226 offset:18432\n\t" \
    "v_add3_u32 v230, v48, v49, v230\n\t" \
    "v_mfma_f32_16x16x32_bf16 v[4:7], v[132:135], v[48:51], v[4:7]\n\t" \
    "ds_read_b64_tr_b16 v[100:101], v227 offset:16384\n\t" \
    "buffer_load_dwordx4 v[44:47], v241, %[rsb], s80 offen offset:1024 sc1\n\t" \
    "v_mfma_f32_16x16x32_bf16 v[20:23], v[132:135], v[52:55], v[20:23]\n\t" \
    "ds_read_b64_tr_b16 v[102:103], v227 offset:18432\n\t" \
    "v_add3_u32 v230, v50, v51, v230\n\t" \
    "v_mfma_f32_16x16x32_bf16 v[8:11], v[136:139], v[48:51], v[8:11]\n\t" \
    "ds_read_b64_tr_b16 v[104:105], v228 offset:16384\n\t" \
    "v_add3_u32 v230, v52, v53, v230\n\t" \
    "v_mfma_f32_16x16x32_bf16 v[24:27], v[136:139], v[52:55], v[24:27]\n\t" \
    "ds_read_b64_tr_b16 v[106:107], v228 offset:18432\n\t" \
    "v_add3_u32 v230, v54, v55, v230\n\t" \
    "v_mfma_f32_16x16x32_bf16 v[12:15], v[140:143], v[48:51], v[12:15]\n\t" \
    "ds_read_b64_tr_b16 v[108:109], v229 offset:16384\n\t" \
    "v_mfma_f32_16x16x32_bf16 v[28:31], v[140:143], v[52:55], v[28:31]\n\t" \
    "ds_read_b64_tr_b16 v[110:111], v229 offset:18432\n\t" \
    "s_waitcnt vmcnt(12) lgkmcnt(8)\n\t" \
    "v_mfma_f32_16x16x32_bf16 v[0:3], v[144:147], v[56:59], v[0:3]\n\t" \
    "ds_read_b64_tr_b16 v[112:113], v226 offset:20480\n\t" \
    "buffer_load_dwordx4 v[48:51], v241, %[rsb], s80 offen offset:2048 sc1\n\t" \
    "v_mfma_f32_16x16x32_bf16 v[16:19], v[144:147], v[60:63], v[16:19]\n\t" \
    "ds_read_b64_tr_b16 v[114:115], v226 offset:22528\n\t" \
    "v_add3_u32 v230, v56, v57, v230\n\t" \
    "v_mfma_f32_16x16x32_bf16 v[4:7], v[148:151], v[56:59], v[4:7]\n\t" \
    "ds_read_b64_tr_b16 v[116:117], v227 offset:20480\n\t" \
    "buffer_load_dwordx4 v[52:55], v241, %[rsb], s80 offen offset:3072 sc1\n\t" \
    "v_mfma_f32_16x16x32_bf16 v[20:23], v[148:151], v[60:63], v[20:23]\n\t" \
    "ds_read_b64_tr_b16 v[118:119], v227 offset:22528\n\t" \
    "v_add3_u32 v230, v58, v59, v230\n\t" \
    "v_mfma_f32_16x16x32_bf16 v[8:11], v[152:155], v[56:59], v[8:11]\n\t" \
    "ds_read_b64_tr_b16 v[120:121], v228 offset:20480\n\t" \
    "v_add3_u32 v230, v60, v61, v230\n\t" \
    "v_mfma_f32_16x16x32_bf16 v[24:27], v[152:155], v[60:63], v[24:27]\n\t" \
    "ds_read_b64_tr_b16 v[122:123], v228 offset:22528\n\t" \
    "v_add3_u32 v230, v62, v63, v230\n\t" \
    "v_mfma_f32_16x16x32_bf16 v[12:15], v[156:159], v[56:59], v[12:15]\n\t" \
    "ds_read_b64_tr_b16 v[124:125], v229 offset:20480\n\t" \
    "v_mfma_f32_16x16x32_bf16 v[28:31], v[156:159], v[60:63], v[28:31]\n\t" \
    "ds_read_b64_tr_b16 v[126:127], v229 offset:22528\n\t" \
    "s_waitcnt vmcnt(12) lgkmcnt(8)\n\t" \
    "ds_read_b32 v160, v249 offset:56\n\t" \
    "v_mfma_f32_16x16x32_bf16 v[0:3], v[96:99], v[64:67], v[0:3]\n\t" \
    "ds_read_b64_tr_b16 v[128:129], v226 offset:24576\n\t" \
    "buffer_load_dwordx4 v[56:59], v242, %[rsb], s80 offen sc1\n\t" \
    "v_mfma_f32_16x16x32_bf16 v[16:19], v[96:99], v[68:71], v[16:19]\n\t" \
    "ds_read_b64_tr_b16 v[130:131], v226 offset:26624\n\t" \
    "v_add3_u32 v230, v64, v65, v230\n\t" \
    "v_mfma_f32_16x16x32_bf16 v[4:7], v[100:103], v[64:67], v[4:7]\n\t" \
    "ds_read_b64_tr_b16 v[132:133], v227 offset:24576\n\t" \
    "buffer_load_dwordx4 v[60:63], v242, %[rsb], s80 offen offset:1024 sc1\n\t" \
    "v_mfma_f32_16x16x32_bf16 v[20:23], v[100:103], v[68:71], v[20:23]\n\t" \
    "ds_read_b64_tr_b16 v[134:135], v227 offset:26624\n\t" \
    "v_add3_u32 v230, v66, v67, v230\n\t" \
    "v_mfma_f32_16x16x32_bf16 v[8:11], v[104:107], v[64:67], v[8:11]\n\t" \
    "ds_read_b64_tr_b16 v[136:137], v228 offset:24576\n\t" \
    "v_add3_u32 v230, v68, v69, v230\n\t" \
    "v_mfma_f32_16x16x32_bf16 v[24:27], v[104:107], v[68:71], v[24:27]\n\t" \
    "ds_read_b64_tr_b16 v[138:139], v228 offset:26624\n\t" \
    "v_add3_u32 v230, v70, v71, v230\n\t" \
    "v_mfma_f32_16x16x32_bf16 v[12:15], v[108:111], v[64:67], v[12:15]\n\t" \
    "ds_read_b64_tr_b16 v[140:141], v229 offset:24576\n\t" \
    "v_mfma_f32_16x16x32_bf16 v[28:31], v[108:111], v[68:71], v[28:31]\n\t" \
    "ds_read_b64_tr_b16 v[142:143], v229 offset:26624\n\t" \
    "s_waitcnt vmcnt(12) lgkmcnt(8)\n\t" \
    "v_mfma_f32_16x16x32_bf16 v[0:3], v[112:115], v[72:75], v[0:3]\n\t" \
    "ds_read_b64_tr_b16 v[144:145], v226 offset:28672\n\t" \
    "buffer_load_dwordx4 v[64:67], v242, %[rsb], s80 offen offset:2048 sc1\n\t" \
    "v_mfma_f32_16x16x32_bf16 v[16:19], v[112:115], v[76:79], v[16:19]\n\t" \
    "ds_read_b64_tr_b16 v[146:147], v226 offset:30720\n\t" \
    "v_add3_u32 v230, v72, v73, v230\n\t" \
    "v_mfma_f32_16x16x32_bf16 v[4:7], v[116:119], v[72:75], v[4:7]\n\t" \
    "ds_read_b64_tr_b16 v[148:149], v227 offset:28672\n\t" \
    "buffer_load_dwordx4 v[68:71], v242, %[rsb], s80 offen offset:3072 sc1\n\t" \
    "v_add_u32 v226, 0x8000, v226\n\t" \
    "v_and_b32 v226, 0x1ffff, v226\n\t" \
    "v_mfma_f32_16x16x32_bf16 v[20:23], v[116:119], v[76:79], v[20:23]\n\t" \
    "ds_read_b64_tr_b16 v[150:151], v227 offset:30720\n\t" \
    "v_add3_u32 v230, v74, v75, v230\n\t" \
    "v_mfma_f32_16x16x32_bf16 v[8:11], v[120:123], v[72:75], v[8:11]\n\t" \
    "ds_read_b64_tr_b16 v[152:153], v228 offset:28672\n\t" \
    "v_add3_u32 v230, v76, v77, v230\n\t" \
    "v_add_u32 v227, 0x8000, v227\n\t" \
    "v_and_b32 v227, 0x1ffff, v227\n\t" \
    "v_mfma_f32_16x16x32_bf16 v[24:27], v[120:123], v[76:79], v[24:27]\n\t" \
    "ds_read_b64_tr_b16 v[154:155], v228 offset:30720\n\t" \
    "v_add3_u32 v230, v78, v79, v230\n\t" \
    "v_mfma_f32_16x16x32_bf16 v[12:15], v[124:127], v[72:75], v[12:15]\n\t" \
    "v_readfirstlane_b32 s83, v160\n\t" \
    "s_add_u32 s82, s82, 1\n\t" \
    "ds_read_b64_tr_b16 v[156:157], v229 offset:28672\n\t" \
    "v_add_u32 v228, 0x8000, v228\n\t" \
    "v_and_b32 v228, 0x1ffff, v228\n\t" \
    "v_mfma_f32_16x16x32_bf16 v[28:31], v[124:127], v[76:79], v[28:31]\n\t" \
    "s_cmp_lt_u32 s83, s82\n\t" \
    "s_cbranch_scc1 .Lc_w0\n\t" \
    ".Lc_r0:\n\t" \
    "ds_read_b64_tr_b16 v[158:159], v229 offset:30720\n\t" \
    "v_add_u32 v229, 0x8000, v229\n\t" \
    "v_and_b32 v229, 0x1ffff, v229\n\t" \
    "s_waitcnt vmcnt(12) lgkmcnt(8)\n\t" \
    "v_mfma_f32_16x16x32_bf16 v[0:3], v[128:131], v[80:83], v[0:3]\n\t" \
    "ds_read_b64_tr_b16 v[96:97], v226\n\t" \
    "buffer_load_dwordx4 v[72:75], v243, %[rsb], s80 offen sc1\n\t" \
    "v_mfma_f32_16x16x32_bf16 v[16:19], v[128:131], v[84:87], v[16:19]\n\t" \
    "ds_read_b64_tr_b16 v[98:99], v226 offset:2048\n\t" \
    "v_add3_u32 v230, v80, v81, v230\n\t" \
    "v_mfma_f32_16x16x32_bf16 v[4:7], v[132:135], v[80:83], v[4:7]\n\t" \
    "ds_read_b64_tr_b16 v[100:101], v227\n\t" \
    "buffer_load_dwordx4 v[76:79], v243, %[rsb], s80 offen offset:1024 sc1\n\t" \
    "v_mfma_f32_16x16x32_bf16 v[20:23], v[132:135], v[84:87], v[20:23]\n\t" \
    "ds_read_b64_tr_b16 v[102:103], v227 offset:2048\n\t" \
    "v_add3_u32 v230, v82, v83, v230\n\t" \
    "v_mfma_f32_16x16x32_bf16 v[8:11], v[136:139], v[80:83], v[8:11]\n\t" \
    "ds_read_b64_tr_b16 v[104:105], v228\n\t" \
    "v_add3_u32 v230, v84, v85, v230\n\t" \
    "v_mfma_f32_16x16x32_bf16 v[24:27], v[136:139], v[84:87], v[24:27]\n\t" \
    "ds_read_b64_tr_b16 v[106:107], v228 offset:2048\n\t" \
    "v_add3_u32 v230, v86, v87, v230\n\t" \
    "v_mfma_f32_16x16x32_bf16 v[12:15], v[140:143], v[80:83], v[12:15]\n\t" \
    "ds_read_b64_tr_b16 v[108:109], v229\n\t" \
    "v_mfma_f32_16x16x32_bf16 v[28:31], v[140:143], v[84:87], v[28:31]\n\t" \
    "ds_read_b64_tr_b16 v[110:111], v229 offset:2048\n\t" \
    "s_waitcnt vmcnt(12) lgkmcnt(8)\n\t" \
    "v_mfma_f32_16x16x32_bf16 v[0:3], v[144:147], v[88:91], v[0:3]\n\t" \
    "ds_read_b64_tr_b16 v[112:113], v226 offset:4096\n\t" \
    "buffer_load_dwordx4 v[80:83], v243, %[rsb], s80 offen offset:2048 sc1\n\t" \
    "v_mfma_f32_16x16x32_bf16 v[16:19], v[144:147], v[92:95], v[16:19]\n\t" \
    "ds_read_b64_tr_b16 v[114:115], v226 offset:6144\n\t" \
    "v_add3_u32 v230, v88, v89, v230\n\t" \
    "v_mfma_f32_16x16x32_bf16 v[4:7], v[148:151], v[88:91], v[4:7]\n\t" \
    "ds_read_b64_tr_b16 v[116:117], v227 offset:4096\n\t" \
    "buffer_load_dwordx4 v[84:87], v243, %[rsb], s80 offen offset:3072 sc1\n\t" \
    "v_mfma_f32_16x16x32_bf16 v[20:23], v[148:151], v[92:95], v[20:23]\n\t" \
    "ds_read_b64_tr_b16 v[118:119], v227 offset:6144\n\t" \
    "v_add3_u32 v230, v90, v91, v230\n\t" \
    "v_mfma_f32_16x16x32_bf16 v[8:11], v[152:155], v[88:91], v[8:11]\n\t" \
    "ds_read_b64_tr_b16 v[120:121], v228 offset:4096\n\t" \
    "v_add3_u32 v230, v92, v93, v230\n\t" \
    "v_mfma_f32_16x16x32_bf16 v[24:27], v[152:155], v[92:95], v[24:27]\n\t" \
    "ds_read_b64_tr_b16 v[122:123], v228 offset:6144\n\t" \
    "v_add3_u32 v230, v94, v95, v230\n\t" \
    "v_mfma_f32_16x16x32_bf16 v[12:15], v[156:159], v[88:91], v[12:15]\n\t" \
    "ds_read_b64_tr_b16 v[124:125], v229 offset:4096\n\t" \
    "s_add_u32 s80, s80, 0x4000\n\t" \
    "s_and_b32 s80, s80, 0xc000\n\t" \
    "v_mfma_f32_16x16x32_bf16 v[28:31], v[156:159], v[92:95], v[28:31]\n\t" \
    "ds_read_b64_tr_b16 v[126:127], v229 offset:6144\n\t" \
    "s_sub_u32 s81, s81, 1\n\t" \
    "s_cmp_lg_u32 s81, 0\n\t" \
    "s_cbranch_scc1 .Lc_l\n\t" \
    "s_waitcnt vmcnt(12) lgkmcnt(8)\n\t" \
    "v_mfma_f32_16x16x32_bf16 v[162:165], v[96:99], v[32:35], 0\n\t" \
    "ds_read_b64_tr_b16 v[128:129], v226 offset:8192\n\t" \
    "buffer_load_dwordx4 v[88:91], v240, %[rsb], s80 offen sc1\n\t" \
    "v_mfma_f32_16x16x32_bf16 v[178:181], v[96:99], v[36:39], 0\n\t" \
    "ds_read_b64_tr_b16 v[130:131], v226 offset:10240\n\t" \
    "v_add3_u32 v230, v32, v33, v230\n\t" \
    "v_mfma_f32_16x16x32_bf16 v[166:169], v[100:103], v[32:35], 0\n\t" \
    "ds_read_b64_tr_b16 v[132:133], v227 offset:8192\n\t" \
    "buffer_load_dwordx4 v[92:95], v240, %[rsb], s80 offen offset:1024 sc1\n\t" \
    "v_mfma_f32_16x16x32_bf16 v[182:185], v[100:103], v[36:39], 0\n\t" \
    "ds_read_b64_tr_b16 v[134:135], v227 offset:10240\n\t" \
    "v_add3_u32 v230, v34, v35, v230\n\t" \
    "v_mfma_f32_16x16x32_bf16 v[170:173], v[104:107], v[32:35], 0\n\t" \
    "ds_read_b64_tr_b16 v[136:137], v228 offset:8192\n\t" \
    "v_add3_u32 v230, v36, v37, v230\n\t" \
    "v_mfma_f32_16x16x32_bf16 v[186:189], v[104:107], v[36:39], 0\n\t" \
    "ds_read_b64_tr_b16 v[138:139], v228 offset:10240\n\t" \
    "v_add3_u32 v230, v38, v39, v230\n\t" \
    "v_mfma_f32_16x16x32_bf16 v[174:177], v[108:111], v[32:35], 0\n\t" \
    "ds_read_b64_tr_b16 v[140:141], v229 offset:8192\n\t" \
    "v_mfma_f32_16x16x32_bf16 v[190:193], v[108:111], v[36:39], 0\n\t" \
    "ds_read_b64_tr_b16 v[142:143], v229 offset:10240\n\t" \
    "s_waitcnt vmcnt(12) lgkmcnt(8)\n\t" \
    "v_mfma_f32_16x16x32_bf16 v[162:165], v[112:115], v[40:43], v[162:165]\n\t" \
    "ds_read_b64_tr_b16 v[144:145], v226 offset:12288\n\t" \
    "buffer_load_dwordx4 v[32:35], v240, %[rsb], s80 offen offset:2048 sc1\n\t" \
    "v_mfma_f32_16x16x32_bf16 v[178:181], v[112:115], v[44:47], v[178:181]\n\t" \
    "ds_read_b64_tr_b16 v[146:147], v226 offset:14336\n\t" \
    "v_add3_u32 v230, v40, v41, v230\n\t" \
    "v_mfma_f32_16x16x32_bf16 v[166:169], v[116:119], v[40:43], v[166:169]\n\t" \
    "ds_read_b64_tr_b16 v[148:149], v227 offset:12288\n\t" \
    "buffer_load_dwordx4 v[36:39], v240, %[rsb], s80 offen offset:3072 sc1\n\t" \
    "v_mfma_f32_16x16x32_bf16 v[182:185], v[116:119], v[44:47], v[182:185]\n\t" \
    "ds_read_b64_tr_b16 v[150:151], v227 offset:14336\n\t" \
    "v_add3_u32 v230, v42, v43, v230\n\t" \
    "v_mfma_f32_16x16x32_bf16 v[170:173], v[120:123], v[40:43], v[170:173]\n\t" \
    "ds_read_b64_tr_b16 v[152:153], v228 offset:12288\n\t" \
    "v_add3_u32 v230, v44, v45, v230\n\t" \
    "v_mfma_f32_16x16x32_bf16 v[186:189], v[120:123], v[44:47], v[186:189]\n\t" \
    "ds_read_b64_tr_b16 v[154:155], v228 offset:14336\n\t" \
    "v_add3_u32 v230, v46, v47, v230\n\t" \
    "v_mfma_f32_16x16x32_bf16 v[174:177], v[124:127], v[40:43], v[174:177]\n\t" \
    "ds_read_b64_tr_b16 v[156:157], v229 offset:12288\n\t" \
    "v_mfma_f32_16x16x32_bf16 v[190:193], v[124:127], v[44:47], v[190:193]\n\t" \
    "ds_read_b64_tr_b16 v[158:159], v229 offset:14336\n\t" \
    "s_waitcnt vmcnt(12) lgkmcnt(8)\n\t" \
    "v_mfma_f32_16x16x32_bf16 v[162:165], v[128:131], v[48:51], v[162:165]\n\t" \
    "ds_read_b64_tr_b16 v[96:97], v226 offset:16384\n\t" \
    "buffer_load_dwordx4 v[40:43], v241, %[rsb], s80 offen sc1\n\t" \
    "v_mfma_f32_16x16x32_bf16 v[178:181], v[128:131], v[52:55], v[178:181]\n\t" \
    "ds_read_b64_tr_b16 v[98:99], v226 offset:18432\n\t" \
    "v_add3_u32 v230, v48, v49, v230\n\t" \
    "v_mfma_f32_16x16x32_bf16 v[166:169], v[132:135], v[48:51], v[166:169]\n\t" \
    "ds_read_b64_tr_b16 v[100:101], v227 offset:16384\n\t" \
    "buffer_load_dwordx4 v[44:47], v241, %[rsb], s80 offen offset:1024 sc1\n\t" \
    "v_mfma_f32_16x16x32_bf16 v[182:185], v[132:135], v[52:55], v[182:185]\n\t" \
    "ds_read_b64_tr_b16 v[102:103], v227 offset:18432\n\t" \
    "v_add3_u32 v230, v50, v51, v230\n\t" \
    "v_mfma_f32_16x16x32_bf16 v[170:173], v[136:139], v[48:51], v[170:173]\n\t" \
    "ds_read_b64_tr_b16 v[104:105], v228 offset:16384\n\t" \
    "v_add3_u32 v230, v52, v53, v230\n\t" \
    "v_mfma_f32_16x16x32_bf16 v[186:189], v[136:139], v[52:55], v[186:189]\n\t" \
    "ds_read_b64_tr_b16 v[106:107], v228 offset:18432\n\t" \
    "v_add3_u32 v230, v54, v55, v230\n\t" \
    "v_mfma_f32_16x16x32_bf16 v[174:177], v[140:143], v[48:51], v[174:177]\n\t" \
    "ds_read_b64_tr_b16 v[108:109], v229 offset:16384\n\t" \
    "v_mfma_f32_16x16x32_bf16 v[190:193], v[140:143], v[52:55], v[190:193]\n\t" \
    "ds_read_b64_tr_b16 v[110:111], v229 offset:18432\n\t" \
    "s_waitcnt vmcnt(12) lgkmcnt(8)\n\t" \
    "v_mfma_f32_16x16x32_bf16 v[162:165], v[144:147], v[56:59], v[162:165]\n\t" \
    "ds_read_b64_tr_b16 v[112:113], v226 offset:20480\n\t" \
    "buffer_load_dwordx4 v[48:51], v241, %[rsb], s80 offen offset:2048 sc1\n\t" \
    "v_mfma_f32_16x16x32_bf16 v[178:181], v[144:147], v[60:63], v[178:181]\n\t" \
    "ds_read_b64_tr_b16 v[114:115], v226 offset:22528\n\t" \
    "v_add3_u32 v230, v56, v57, v230\n\t" \
    "v_mfma_f32_16x16x32_bf16 v[166:169], v[148:151], v[56:59], v[166:169]\n\t" \
    "ds_read_b64_tr_b16 v[116:117], v227 offset:20480\n\t" \
    "buffer_load_dwordx4 v[52:55], v241, %[rsb], s80 offen offset:3072 sc1\n\t" \
    "v_mfma_f32_16x16x32_bf16 v[182:185], v[148:151], v[60:63], v[182:185]\n\t" \
    "ds_read_b64_tr_b16 v[118:119], v227 offset:22528\n\t" \
    "v_add3_u32 v230, v58, v59, v230\n\t" \
    "v_mfma_f32_16x16x32_bf16 v[170:173], v[152:155], v[56:59], v[170:173]\n\t" \
    "ds_read_b64_tr_b16 v[120:121], v228 offset:20480\n\t" \
    "v_add3_u32 v230, v60, v61, v230\n\t" \
    "v_mfma_f32_16x16x32_bf16 v[186:189], v[152:155], v[60:63], v[186:189]\n\t" \
    "ds_read_b64_tr_b16 v[122:123], v228 offset:22528\n\t" \
    "v_add3_u32 v230, v62, v63, v230\n\t" \
    "v_mfma_f32_16x16x32_bf16 v[174:177], v[156:159], v[56:59], v[174:177]\n\t" \
    "ds_read_b64_tr_b16 v[124:125], v229 offset:20480\n\t" \
    "v_mfma_f32_16x16x32_bf16 v[190:193], v[156:159], v[60:63], v[190:193]\n\t" \
    "ds_read_b64_tr_b16 v[126:127], v229 offset:22528\n\t" \
    "s_waitcnt vmcnt(12) lgkmcnt(8)\n\t" \
    "ds_read_b32 v160, v249 offset:56\n\t" \
    "v_mfma_f32_16x16x32_bf16 v[162:165], v[96:99], v[64:67], v[162:165]\n\t" \
    "ds_read_b64_tr_b16 v[128:129], v226 offset:24576\n\t" \
    "buffer_load_dwordx4 v[56:59], v242, %[rsb], s80 offen sc1\n\t" \
    "v_mfma_f32_16x16x32_bf16 v[178:181], v[96:99], v[68:71], v[178:181]\n\t" \
    "ds_read_b64_tr_b16 v[130:131], v226 offset:26624\n\t" \
    "v_add3_u32 v230, v64, v65, v230\n\t" \
    "v_mfma_f32_16x16x32_bf16 v[166:169], v[100:103], v[64:67], v[166:169]\n\t" \
    "ds_read_b64_tr_b16 v[132:133], v227 offset:24576\n\t" \
    "buffer_load_dwordx4 v[60:63], v242, %[rsb], s80 offen offset:1024 sc1\n\t" \
    "v_mfma_f32_16x16x32_bf16 v[182:185], v[100:103], v[68:71], v[182:185]\n\t" \
    "ds_read_b64_tr_b16 v[134:135], v227 offset:26624\n\t" \
    "v_add3_u32 v230, v66, v67, v230\n\t" \
    "v_mfma_f32_16x16x32_bf16 v[170:173], v[104:107], v[64:67], v[170:173]\n\t" \
    "ds_read_b64_tr_b16 v[136:137], v228 offset:24576\n\t" \
    "v_add3_u32 v230, v68, v69, v230\n\t" \
    "v_mfma_f32_16x16x32_bf16 v[186:189], v[104:107], v[68:71], v[186:189]\n\t" \
    "ds_read_b64_tr_b16 v[138:139], v228 offset:26624\n\t" \
    "v_add3_u32 v230, v70, v71, v230\n\t" \
    "v_mfma_f32_16x16x32_bf16 v[174:177], v[108:111], v[64:67], v[174:177]\n\t" \
    "ds_read_b64_tr_b16 v[140:141], v229 offset:24576\n\t" \
    "v_mfma_f32_16x16x32_bf16 v[190:193], v[108:111], v[68:71], v[190:193]\n\t" \
    "ds_read_b64_tr_b16 v[142:143], v229 offset:26624\n\t" \
    "s_waitcnt vmcnt(12) lgkmcnt(8)\n\t" \
    "v_mfma_f32_16x16x32_bf16 v[162:165], v[112:115], v[72:75], v[162:165]\n\t" \
    "ds_read_b64_tr_b16 v[144:145], v226 offset:28672\n\t" \
    "buffer_load_dwordx4 v[64:67], v242, %[rsb], s80 offen offset:2048 sc1\n\t" \
    "v_mfma_f32_16x16x32_bf16 v[178:181], v[112:115], v[76:79], v[178:181]\n\t" \
    "ds_read_b64_tr_b16 v[146:147], v226 offset:30720\n\t" \
    "v_add3_u32 v230, v72, v73, v230\n\t" \
    "v_mfma_f32_16x16x32_bf16 v[166:169], v[116:119], v[72:75], v[166:169]\n\t" \
    "ds_read_b64_tr_b16 v[148:149], v227 offset:28672\n\t" \
    "buffer_load_dwordx4 v[68:71], v242, %[rsb], s80 offen offset:3072 sc1\n\t" \
    "v_add_u32 v226, 0x8000, v226\n\t" \
    "v_and_b32 v226, 0x1ffff, v226\n\t" \
    "v_mfma_f32_16x16x32_bf16 v[182:185], v[116:119], v[76:79], v[182:185]\n\t" \
    "ds_read_b64_tr_b16 v[150:151], v227 offset:30720\n\t" \
    "v_add3_u32 v230, v74, v75, v230\n\t" \
    "v_mfma_f32_16x16x32_bf16 v[170:173], v[120:123], v[72:75], v[170:173]\n\t" \
    "ds_read_b64_tr_b16 v[152:153], v228 offset:28672\n\t" \
    "v_add3_u32 v230, v76, v77, v230\n\t" \
    "v_add_u32 v227, 0x8000, v227\n\t" \
    "v_and_b32 v227, 0x1ffff, v227\n\t" \
    "v_mfma_f32_16x16x32_bf16 v[186:189], v[120:123], v[76:79], v[186:189]\n\t" \
    "ds_read_b64_tr_b16 v[154:155], v228 offset:30720\n\t" \
    "v_add3_u32 v230, v78, v79, v230\n\t" \
    "v_mfma_f32_16x16x32_bf16 v[174:177], v[124:127], v[72:75], v[174:177]\n\t" \
    "v_readfirstlane_b32 s83, v160\n\t" \
    "s_add_u32 s82, s82, 1\n\t" \
    "ds_read_b64_tr_b16 v[156:157], v229 offset:28672\n\t" \
    "v_add_u32 v228, 0x8000, v228\n\t" \
    "v_and_b32 v228, 0x1ffff, v228\n\t" \
    "v_mfma_f32_16x16x32_bf16 v[190:193], v[124:127], v[76:79], v[190:193]\n\t" \
    "s_cmp_lt_u32 s83, s82\n\t" \
    "s_cbranch_scc1 .Lc_w1\n\t" \
    ".Lc_r1:\n\t" \
    "ds_read_b64_tr_b16 v[158:159], v229 offset:30720\n\t" \
    "v_add_u32 v229, 0x8000, v229\n\t" \
    "v_and_b32 v229, 0x1ffff, v229\n\t" \
    "s_waitcnt vmcnt(12) lgkmcnt(8)\n\t" \
    "v_mfma_f32_16x16x32_bf16 v[162:165], v[128:131], v[80:83], v[162:165]\n\t" \
    "ds_read_b64_tr_b16 v[96:97], v226\n\t" \
    "buffer_load_dwordx4 v[72:75], v243, %[rsb], s80 offen sc1\n\t" \
    "v_mfma_f32_16x16x32_bf16 v[178:181], v[128:131], v[84:87], v[178:181]\n\t" \
    "ds_read_b64_tr_b16 v[98:99], v226 offset:2048\n\t" \
    "v_add3_u32 v230, v80, v81, v230\n\t" \
    "v_mfma_f32_16x16x32_bf16 v[166:169], v[132:135], v[80:83], v[166:169]\n\t" \
    "ds_read_b64_tr_b16 v[100:101], v227\n\t" \
    "buffer_load_dwordx4 v[76:79], v243, %[rsb], s80 offen offset:1024 sc1\n\t" \
    "v_mfma_f32_16x16x32_bf16 v[182:185], v[132:135], v[84:87], v[182:185]\n\t" \
    "ds_read_b64_tr_b16 v[102:103], v227 offset:2048\n\t" \
    "v_add3_u32 v230, v82, v83, v230\n\t" \
    "v_mfma_f32_16x16x32_bf16 v[170:173], v[136:139], v[80:83], v[170:173]\n\t" \
    "ds_read_b64_tr_b16 v[104:105], v228\n\t" \
    "v_add3_u32 v230, v84, v85, v230\n\t" \
    "v_mfma_f32_16x16x32_bf16 v[186:189], v[136:139], v[84:87], v[186:189]\n\t" \
    "ds_read_b64_tr_b16 v[106:107], v228 offset:2048\n\t" \
    "v_add3_u32 v230, v86, v87, v230\n\t" \
    "v_mfma_f32_16x16x32_bf16 v[174:177], v[140:143], v[80:83], v[174:177]\n\t" \
    "ds_read_b64_tr_b16 v[108:109], v229\n\t" \
    "v_mfma_f32_16x16x32_bf16 v[190:193], v[140:143], v[84:87], v[190:193]\n\t" \
    "ds_read_b64_tr_b16 v[110:111], v229 offset:2048\n\t" \
    "s_waitcnt vmcnt(12) lgkmcnt(8)\n\t" \
    "v_mfma_f32_16x16x32_bf16 v[162:165], v[144:147], v[88:91], v[162:165]\n\t" \
    "ds_read_b64_tr_b16 v[112:113], v226 offset:4096\n\t" \
    "buffer_load_dwordx4 v[80:83], v243, %[rsb], s80 offen offset:2048 sc1\n\t" \
    "v_mfma_f32_16x16x32_bf16 v[178:181], v[144:147], v[92:95], v[178:181]\n\t" \
    "ds_read_b64_tr_b16 v[114:115], v226 offset:6144\n\t" \
    "v_add3_u32 v230, v88, v89, v230\n\t" \
    "v_mfma_f32_16x16x32_bf16 v[166:169], v[148:151], v[88:91], v[166:169]\n\t" \
    "ds_read_b64_tr_b16 v[116:117], v227 offset:4096\n\t" \
    "buffer_load_dwordx4 v[84:87], v243, %[rsb], s80 offen offset:3072 sc1\n\t" \
    "v_mfma_f32_16x16x32_bf16 v[182:185], v[148:151], v[92:95], v[182:185]\n\t" \
    "ds_read_b64_tr_b16 v[118:119], v227 offset:6144\n\t" \
    "v_add3_u32 v230, v90, v91, v230\n\t" \
    "v_mfma_f32_16x16x32_bf16 v[170:173], v[152:155], v[88:91], v[170:173]\n\t" \
    "ds_read_b64_tr_b16 v[120:121], v228 offset:4096\n\t" \
    "v_add3_u32 v230, v92, v93, v230\n\t" \
    "v_mfma_f32_16x16x32_bf16 v[186:189], v[152:155], v[92:95], v[186:189]\n\t" \
    "ds_read_b64_tr_b16 v[122:123], v228 offset:6144\n\t" \
    "v_add3_u32 v230, v94, v95, v230\n\t" \
    "v_mfma_f32_16x16x32_bf16 v[174:177], v[156:159], v[88:91], v[174:177]\n\t" \
    "ds_read_b64_tr_b16 v[124:125], v229 offset:4096\n\t" \
    "s_add_u32 s80, s80, 0x4000\n\t" \
    "s_and_b32 s80, s80, 0xc000\n\t" \
    "v_mfma_f32_16x16x32_bf16 v[190:193], v[156:159], v[92:95], v[190:193]\n\t" \
    "ds_read_b64_tr_b16 v[126:127], v229 offset:6144\n\t" \
    "s_waitcnt vmcnt(12) lgkmcnt(8)\n\t" \
    "v_mfma_f32_16x16x32_bf16 v[162:165], v[96:99], v[32:35], v[162:165]\n\t" \
    "ds_read_b64_tr_b16 v[128:129], v226 offset:8192\n\t" \
    "buffer_load_dwordx4 v[88:91], v240, %[rsb], s80 offen sc1\n\t" \
    "v_mfma_f32_16x16x32_bf16 v[178:181], v[96:99], v[36:39], v[178:181]\n\t" \
    "ds_read_b64_tr_b16 v[130:131], v226 offset:10240\n\t" \
    "v_add3_u32 v230, v32, v33, v230\n\t" \
    ".Lpad_c1:\n\t" \
    "s_cbranch_execz .Lpad_ret\n\t" \
    "v_mfma_f32_16x16x32_bf16 v[166:169], v[100:103], v[32:35], v[166:169]\n\t" \
    "ds_read_b64_tr_b16 v[132:133], v227 offset:8192\n\t" \
    "buffer_load_dwordx4 v[92:95], v240, %[rsb], s80 offen offset:1024 sc1\n\t" \
    "v_mfma_f32_16x16x32_bf16 v[182:185], v[100:103], v[36:39], v[182:185]\n\t" \
    "ds_read_b64_tr_b16 v[134:135], v227 offset:10240\n\t" \
    "v_add3_u32 v230, v34, v35, v230\n\t" \
    "v_mfma_f32_16x16x32_bf16 v[170:173], v[104:107], v[32:35], v[170:173]\n\t" \
    "ds_read_b64_tr_b16 v[136:137], v228 offset:8192\n\t" \
    "v_add3_u32 v230, v36, v37, v230\n\t" \
    "v_mfma_f32_16x16x32_bf16 v[186:189], v[104:107], v[36:39], v[186:189]\n\t" \
    "ds_read_b64_tr_b16 v[138:139], v228 offset:10240\n\t" \
    "v_add3_u32 v230, v38, v39, v230\n\t" \
    "v_mfma_f32_16x16x32_bf16 v[174:177], v[108:111], v[32:35], v[174:177]\n\t" \
    "ds_read_b64_tr_b16 v[140:141], v229 offset:8192\n\t" \
    "buffer_load_dwordx2 v[160:161], v244, %[rsb], %[sog] offen sc1\n\t" \
    "v_mfma_f32_16x16x32_bf16 v[190:193], v[108:111], v[36:39], v[190:193]\n\t" \
    "ds_read_b64_tr_b16 v[142:143], v229 offset:10240\n\t" \
    "s_waitcnt vmcnt(13) lgkmcnt(8)\n\t" \
    "v_mfma_f32_16x16x32_bf16 v[162:165], v[112:115], v[40:43], v[162:165]\n\t" \
    "ds_read_b64_tr_b16 v[144:145], v226 offset:12288\n\t" \
    "v_mfma_f32_16x16x32_bf16 v[178:181], v[112:115], v[44:47], v[178:181]\n\t" \
    "ds_read_b64_tr_b16 v[146:147], v226 offset:14336\n\t" \
    "v_add3_u32 v230, v40, v41, v230\n\t" \
    "v_mfma_f32_16x16x32_bf16 v[166:169], v[116:119], v[40:43], v[166:169]\n\t" \
    "ds_read_b64_tr_b16 v[148:149], v227 offset:12288\n\t" \
    "v_mfma_f32_16x16x32_bf16 v[182:185], v[116:119], v[44:47], v[182:185]\n\t" \
    "ds_read_b64_tr_b16 v[150:151], v227 offset:14336\n\t" \
    "v_add3_u32 v230, v42, v43, v230\n\t" \
    "v_mfma_f32_16x16x32_bf16 v[170:173], v[120:123], v[40:43], v[170:173]\n\t" \
    "ds_read_b64_tr_b16 v[152:153], v228 offset:12288\n\t" \
    "v_add3_u32 v230, v44, v45, v230\n\t" \
    "v_mfma_f32_16x16x32_bf16 v[186:189], v[120:123], v[44:47], v[186:189]\n\t" \
    "ds_read_b64_tr_b16 v[154:155], v228 offset:14336\n\t" \
    "v_add3_u32 v230, v46, v47, v230\n\t" \
    "v_mfma_f32_16x16x32_bf16 v[174:177], v[124:127], v[40:43], v[174:177]\n\t" \
    "ds_read_b64_tr_b16 v[156:157], v229 offset:12288\n\t" \
    "v_mfma_f32_16x16x32_bf16 v[190:193], v[124:127], v[44:47], v[190:193]\n\t" \
    "ds_read_b64_tr_b16 v[158:159], v229 offset:14336\n\t" \
    "s_waitcnt vmcnt(11) lgkmcnt(8)\n\t" \
    "v_mfma_f32_16x16x32_bf16 v[162:165], v[128:131], v[48:51], v[162:165]\n\t" \
    "ds_read_b64_tr_b16 v[96:97], v226 offset:16384\n\t" \
    "v_mfma_f32_16x16x32_bf16 v[178:181], v[128:131], v[52:55], v[178:181]\n\t" \
    "ds_read_b64_tr_b16 v[98:99], v226 offset:18432\n\t" \
    "v_add3_u32 v230, v48, v49, v230\n\t" \
    "v_mfma_f32_16x16x32_bf16 v[166:169], v[132:135], v[48:51], v[166:169]\n\t" \
    "ds_read_b64_tr_b16 v[100:101], v227 offset:16384\n\t" \
    "v_mfma_f32_16x16x32_bf16 v[182:185], v[132:135], v[52:55], v[182:185]\n\t" \
    "ds_read_b64_tr_b16 v[102:103], v227 offset:18432\n\t" \
    "v_add3_u32 v230, v50, v51, v230\n\t" \
    "v_mfma_f32_16x16x32_bf16 v[170:173], v[136:139], v[48:51], v[170:173]\n\t" \
    "ds_read_b64_tr_b16 v[104:105], v228 offset:16384\n\t" \
    "v_add3_u32 v230, v52, v53, v230\n\t" \
    "v_mfma_f32_16x16x32_bf16 v[186:189], v[136:139], v[52:55], v[186:189]\n\t" \
    "ds_read_b64_tr_b16 v[106:107], v228 offset:18432\n\t" \
    "v_add3_u32 v230, v54, v55, v230\n\t" \
    "v_mfma_f32_16x16x32_bf16 v[174:177], v[140:143], v[48:51], v[174:177]\n\t" \
    "ds_read_b64_tr_b16 v[108:109], v229 offset:16384\n\t" \
    "v_mfma_f32_16x16x32_bf16 v[190:193], v[140:143], v[52:55], v[190:193]\n\t" \
    "ds_read_b64_tr_b16 v[110:111], v229 offset:18432\n\t" \
    "s_waitcnt vmcnt(9) lgkmcnt(8)\n\t" \
    "v_mfma_f32_16x16x32_bf16 v[162:165], v[144:147], v[56:59], v[162:165]\n\t" \
    "ds_read_b64_tr_b16 v[112:113], v226 offset:20480\n\t" \
    "v_mfma_f32_16x16x32_bf16 v[178:181], v[144:147], v[60:63], v[178:181]\n\t" \
    "ds_read_b64_tr_b16 v[114:115], v226 offset:22528\n\t" \
    "v_add3_u32 v230, v56, v57, v230\n\t" \
    "v_mfma_f32_16x16x32_bf16 v[166:169], v[148:151], v[56:59], v[166:169]\n\t" \
    "ds_read_b64_tr_b16 v[116:117], v227 offset:20480\n\t" \
    "v_mfma_f32_16x16x32_bf16 v[182:185], v[148:151], v[60:63], v[182:185]\n\t" \
    "ds_read_b64_tr_b16 v[118:119], v227 offset:22528\n\t" \
    "v_add3_u32 v230, v58, v59, v230\n\t" \
    "v_mfma_f32_16x16x32_bf16 v[170:173], v[152:155], v[56:59], v[170:173]\n\t" \
    "ds_read_b64_tr_b16 v[120:121], v228 offset:20480\n\t" \
    "v_add3_u32 v230, v60, v61, v230\n\t" \
    "v_mfma_f32_16x16x32_bf16 v[186:189], v[152:155], v[60:63], v[186:189]\n\t" \
    "ds_read_b64_tr_b16 v[122:123], v228 offset:22528\n\t" \
    "v_add3_u32 v230, v62, v63, v230\n\t" \
    "v_mfma_f32_16x16x32_bf16 v[174:177], v[156:159], v[56:59], v[174:177]\n\t" \
    "ds_read_b64_tr_b16 v[124:125], v229 offset:20480\n\t" \
    "v_mfma_f32_16x16x32_bf16 v[190:193], v[156:159], v[60:63], v[190:193]\n\t" \
    "ds_read_b64_tr_b16 v[126:127], v229 offset:22528\n\t" \
    "s_waitcnt vmcnt(7) lgkmcnt(8)\n\t" \
    "v_mfma_f32_16x16x32_bf16 v[162:165], v[96:99], v[64:67], v[162:165]\n\t" \
    "ds_read_b64_tr_b16 v[128:129], v226 offset:24576\n\t" \
    "v_mfma_f32_16x16x32_bf16 v[178:181], v[96:99], v[68:71], v[178:181]\n\t" \
    "ds_read_b64_tr_b16 v[130:131], v226 offset:26624\n\t" \
    "v_add3_u32 v230, v64, v65, v230\n\t" \
    "v_mfma_f32_16x16x32_bf16 v[166:169], v[100:103], v[64:67], v[166:169]\n\t" \
    "ds_read_b64_tr_b16 v[132:133], v227 offset:24576\n\t" \
    "v_mfma_f32_16x16x32_bf16 v[182:185], v[100:103], v[68:71], v[182:185]\n\t" \
    "ds_read_b64_tr_b16 v[134:135], v227 offset:26624\n\t" \
    "v_add3_u32 v230, v66, v67, v230\n\t" \
    "v_mfma_f32_16x16x32_bf16 v[170:173], v[104:107], v[64:67], v[170:173]\n\t" \
    "ds_read_b64_tr_b16 v[136:137], v228 offset:24576\n\t" \
    "v_add3_u32 v230, v68, v69, v230\n\t" \
    "v_mfma_f32_16x16x32_bf16 v[186:189], v[104:107], v[68:71], v[186:189]\n\t" \
    "ds_read_b64_tr_b16 v[138:139], v228 offset:26624\n\t" \
    "v_add3_u32 v230, v70, v71, v230\n\t" \
    "v_mfma_f32_16x16x32_bf16 v[174:177], v[108:111], v[64:67], v[174:177]\n\t" \
    "ds_read_b64_tr_b16 v[140:141], v229 offset:24576\n\t" \
    "v_mfma_f32_16x16x32_bf16 v[190:193], v[108:111], v[68:71], v[190:193]\n\t" \
    "ds_read_b64_tr_b16 v[142:143], v229 offset:26624\n\t" \
    "s_waitcnt vmcnt(5) lgkmcnt(8)\n\t" \
    "v_mfma_f32_16x16x32_bf16 v[162:165], v[112:115], v[72:75], v[162:165]\n\t" \
    "ds_read_b64_tr_b16 v[144:145], v226 offset:28672\n\t" \
    "v_mfma_f32_16x16x32_bf16 v[178:181], v[112:115], v[76:79], v[178:181]\n\t" \
    "ds_read_b64_tr_b16 v[146:147], v226 offset:30720\n\t" \
    "v_add3_u32 v230, v72, v73, v230\n\t" \
    "v_mfma_f32_16x16x32_bf16 v[166:169], v[116:119], v[72:75], v[166:169]\n\t" \
    "ds_read_b64_tr_b16 v[148:149], v227 offset:28672\n\t" \
    "v_mfma_f32_16x16x32_bf16 v[182:185], v[116:119], v[76:79], v[182:185]\n\t" \
    "ds_read_b64_tr_b16 v[150:151], v227 offset:30720\n\t" \
    "v_add3_u32 v230, v74, v75, v230\n\t" \
    "v_mfma_f32_16x16x32_bf16 v[170:173], v[120:123], v[72:75], v[170:173]\n\t" \
    "ds_read_b64_tr_b16 v[152:153], v228 offset:28672\n\t" \
    "v_add3_u32 v230, v76, v77, v230\n\t" \
    "v_mfma_f32_16x16x32_bf16 v[186:189], v[120:123], v[76:79], v[186:189]\n\t" \
    "ds_read_b64_tr_b16 v[154:155], v228 offset:30720\n\t" \
    "v_add3_u32 v230, v78, v79, v230\n\t" \
    "v_mfma_f32_16x16x32_bf16 v[174:177], v[124:127], v[72:75], v[174:177]\n\t" \
    "ds_read_b64_tr_b16 v[156:157], v229 offset:28672\n\t" \
    "v_mfma_f32_16x16x32_bf16 v[190:193], v[124:127], v[76:79], v[190:193]\n\t" \
    "ds_read_b64_tr_b16 v[158:159], v229 offset:30720\n\t" \
    "s_waitcnt vmcnt(3) lgkmcnt(8)\n\t" \
    "v_mfma_f32_16x16x32_bf16 v[162:165], v[128:131], v[80:83], v[162:165]\n\t" \
    "ds_read_b32 v231, v249 offset:68\n\t" \
    "v_mfma_f32_16x16x32_bf16 v[178:181], v[128:131], v[84:87], v[178:181]\n\t" \
    "v_add3_u32 v230, v80, v81, v230\n\t" \
    "v_mfma_f32_16x16x32_bf16 v[166:169], v[132:135], v[80:83], v[166:169]\n\t" \
    "ds_read_b128 v[96:99], v252\n\t" \
    "v_mfma_f32_16x16x32_bf16 v[182:185], v[132:135], v[84:87], v[182:185]\n\t" \
    "ds_read_b128 v[100:103], v252 offset:64\n\t" \
    "v_add3_u32 v230, v82, v83, v230\n\t" \
    "v_mfma_f32_16x16x32_bf16 v[170:173], v[136:139], v[80:83], v[170:173]\n\t" \
    "ds_read_b128 v[104:107], v252 offset:128\n\t" \
    "v_add3_u32 v230, v84, v85, v230\n\t" \
    "v_mfma_f32_16x16x32_bf16 v[186:189], v[136:139], v[84:87], v[186:189]\n\t" \
    "ds_read_b128 v[108:111], v252 offset:192\n\t" \
    "v_add3_u32 v230, v86, v87, v230\n\t" \
    "v_mfma_f32_16x16x32_bf16 v[174:177], v[140:143], v[80:83], v[174:177]\n\t" \
    "v_mfma_f32_16x16x32_bf16 v[190:193], v[140:143], v[84:87], v[190:193]\n\t" \
    "s_waitcnt vmcnt(1) lgkmcnt(5)\n\t" \
    "v_mfma_f32_16x16x32_bf16 v[162:165], v[144:147], v[88:91], v[162:165]\n\t" \
    "v_mfma_f32_16x16x32_bf16 v[166:169], v[148:151], v[88:91], v[166:169]\n\t" \
    "v_add3_u32 v230, v88, v89, v230\n\t" \
    "v_mfma_f32_16x16x32_bf16 v[170:173], v[152:155], v[88:91], v[170:173]\n\t" \
    "v_mfma_f32_16x16x32_bf16 v[174:177], v[156:159], v[88:91], v[174:177]\n\t" \
    "v_add3_u32 v230, v90, v91, v230\n\t" \
    "v_mfma_f32_16x16x32_bf16 v[178:181], v[144:147], v[92:95], v[178:181]\n\t" \
    "v_add3_u32 v230, v92, v93, v230\n\t" \
    "v_mfma_f32_16x16x32_bf16 v[182:185], v[148:151], v[92:95], v[182:185]\n\t" \
    "v_add3_u32 v230, v94, v95, v230\n\t" \
    "v_mfma_f32_16x16x32_bf16 v[186:189], v[152:155], v[92:95], v[186:189]\n\t" \
    "s_cmp_lg_u64 %[rm], 0\n\t" \
    "v_mfma_f32_16x16x32_bf16 v[190:193], v[156:159], v[92:95], v[190:193]\n\t" \
    "s_cbranch_scc1 .Lc_y\n\t" \
    ".Lc_yr:\n\t" \
    "s_add_u32 s85, %[sout], 0x8000\n\t" \
    "v_pk_add_f32 v[0:1], v[0:1], v[162:163]\n\t" \
    "v_pk_add_f32 v[2:3], v[2:3], v[164:165]\n\t" \
    "v_pk_add_f32 v[4:5], v[4:5], v[166:167]\n\t" \
    "v_pk_add_f32 v[6:7], v[6:7], v[168:169]\n\t" \
    "v_pk_fma_f32 v[0:1], v[232:233], v[0:1], v[194:195] op_sel_hi:[0,1,1]\n\t" \
    "v_pk_fma_f32 v[2:3], v[232:233], v[2:3], v[196:197] op_sel_hi:[0,1,1]\n\t" \
    "v_pk_fma_f32 v[4:5], v[232:233], v[4:5], v[198:199] op_sel_hi:[0,1,1]\n\t" \
    "v_pk_fma_f32 v[6:7], v[232:233], v[6:7], v[200:201] op_sel_hi:[0,1,1]\n\t" \
    "v_cvt_pk_bf16_f32 v32, v0, v1\n\t" \
    "v_cvt_pk_bf16_f32 v34, v4, v5\n\t" \
    "v_cvt_pk_bf16_f32 v33, v2, v3\n\t" \
    "v_cvt_pk_bf16_f32 v35, v6, v7\n\t" \
    "s_nop 0\n\t" \
    "v_permlane16_swap_b32 v32, v34\n\t" \
    "v_permlane16_swap_b32 v33, v35\n\t" \
    "buffer_store_dwordx4 v[32:35], v247, %[rso], %[sout] offen sc1\n\t" \
    "v_pk_add_f32 v[8:9], v[8:9], v[170:171]\n\t" \
    "v_pk_add_f32 v[10:11], v[10:11], v[172:173]\n\t" \
    "v_pk_add_f32 v[12:13], v[12:13], v[174:175]\n\t" \
    "v_pk_add_f32 v[14:15], v[14:15], v[176:177]\n\t" \
    "v_pk_fma_f32 v[8:9], v[232:233], v[8:9], v[202:203] op_sel_hi:[0,1,1]\n\t" \
    "v_pk_fma_f32 v[10:11], v[232:233], v[10:11], v[204:205] op_sel_hi:[0,1,1]\n\t" \
    "v_pk_fma_f32 v[12:13], v[232:233], v[12:13], v[206:207] op_sel_hi:[0,1,1]\n\t" \
    "v_pk_fma_f32 v[14:15], v[232:233], v[14:15], v[208:209] op_sel_hi:[0,1,1]\n\t" \
    "v_cvt_pk_bf16_f32 v36, v8, v9\n\t" \
    "v_cvt_pk_bf16_f32 v38, v12, v13\n\t" \
    "v_cvt_pk_bf16_f32 v37, v10, v11\n\t" \
    "v_cvt_pk_bf16_f32 v39, v14, v15\n\t" \
    "v_cmp_gt_u32 vcc, 32, v250\n\t" \
    "v_permlane16_swap_b32 v36, v38\n\t" \
    "v_permlane16_swap_b32 v37, v39\n\t" \
    "buffer_store_dwordx4 v[36:39], v247, %[rso], %[sout] offen offset:64 sc1\n\t" \
    "v_pk_add_f32 v[16:17], v[16:17], v[178:179]\n\t" \
    "v_pk_add_f32 v[18:19], v[18:19], v[180:181]\n\t" \
    "v_pk_add_f32 v[20:21], v[20:21], v[182:183]\n\t" \
    "v_pk_add_f32 v[22:23], v[22:23], v[184:185]\n\t" \
    "v_pk_fma_f32 v[16:17], v[234:235], v[16:17], v[210:211] op_sel_hi:[0,1,1]\n\t" \
    "v_pk_fma_f32 v[18:19], v[234:235], v[18:19], v[212:213] op_sel_hi:[0,1,1]\n\t" \
    "v_pk_fma_f32 v[20:21], v[234:235], v[20:21], v[214:215] op_sel_hi:[0,1,1]\n\t" \
    "v_pk_fma_f32 v[22:23], v[234:235], v[22:23], v[216:217] op_sel_hi:[0,1,1]\n\t" \
    "v_cvt_pk_bf16_f32 v40, v16, v17\n\t" \
    "v_cvt_pk_bf16_f32 v42, v20, v21\n\t" \
    "v_cvt_pk_bf16_f32 v41, v18, v19\n\t" \
    "v_cvt_pk_bf16_f32 v43, v22, v23\n\t" \
    "s_waitcnt vmcnt(2)\n\t" \
    "v_permlane16_swap_b32 v40, v42\n\t" \
    "v_permlane16_swap_b32 v41, v43\n\t" \
    "buffer_store_dwordx4 v[40:43], v247, %[rso], s85 offen sc1\n\t" \
    "v_pk_add_f32 v[24:25], v[24:25], v[186:187]\n\t" \
    "v_pk_add_f32 v[26:27], v[26:27], v[188:189]\n\t" \
    "v_pk_add_f32 v[28:29], v[28:29], v[190:191]\n\t" \
    "v_pk_add_f32 v[30:31], v[30:31], v[192:193]\n\t" \
    "v_pk_fma_f32 v[24:25], v[234:235], v[24:25], v[218:219] op_sel_hi:[0,1,1]\n\t" \
    "v_pk_fma_f32 v[26:27], v[234:235], v[26:27], v[220:221] op_sel_hi:[0,1,1]\n\t" \
    "v_pk_fma_f32 v[28:29], v[234:235], v[28:29], v[222:223] op_sel_hi:[0,1,1]\n\t" \
    "v_pk_fma_f32 v[30:31], v[234:235], v[30:31], v[224:225] op_sel_hi:[0,1,1]\n\t" \
    "v_cvt_pk_bf16_f32 v44, v24, v25\n\t" \
    "v_cvt_pk_bf16_f32 v46, v28, v29\n\t" \
    "v_cvt_pk_bf16_f32 v45, v26, v27\n\t" \
    "v_cvt_pk_bf16_f32 v47, v30, v31\n\t" \
    "s_waitcnt lgkmcnt(4)\n\t" \
    "v_cndmask_b32 v231, 0, v160, vcc\n\t" \
    "v_permlane16_swap_b32 v44, v46\n\t" \
    "v_permlane16_swap_b32 v45, v47\n\t" \
    "buffer_store_dwordx4 v[44:47], v247, %[rso], s85 offen offset:64 sc1\n\t" \
    "v_sub_u32 v231, v230, v231\n\t" \
    "v_cmp_ne_u32 vcc, v236, v161\n\t" \
    "v_mov_b32 v0, v231\n\t" \
    "s_nop 1\n\t" \
    "v_permlane32_swap_b32 v231, v0\n\t" \
    "v_add_u32 v231, v231, v0\n\t" \
    "v_mov_b32 v0, v231\n\t" \
    "s_nop 1\n\t" \
    "v_permlane16_swap_b32 v231, v0\n\t" \
    "v_add_u32 v231, v231, v0\n\t" \
    "s_nop 1\n\t" \
    "v_add_u32_dpp v231, v231, v231 row_ror:8 row_mask:0xf bank_mask:0xf bound_ctrl:1\n\t" \
    "s_nop 1\n\t" \
    "v_add_u32_dpp v231, v231, v231 row_ror:4 row_mask:0xf bank_mask:0xf bound_ctrl:1\n\t" \
    "s_nop 1\n\t" \
    "v_add_u32_dpp v231, v231, v231 row_ror:2 row_mask:0xf bank_mask:0xf bound_ctrl:1\n\t" \
    "s_nop 1\n\t" \
    "v_add_u32_dpp v231, v231, v231 row_ror:1 row_mask:0xf bank_mask:0xf bound_ctrl:1\n\t" \
    "s_nop 0\n\t" \
    "v_readfirstlane_b32 s83, v231\n\t" \
    "s_or_b32 s83, s83, vcc_lo\n\t" \
    "s_cselect_b32 %[st], 2, 0\n\t" \
    "s_waitcnt lgkmcnt(0)\n\t" \
    ".Lc_end:\n\t"
#define RED1_ASM \
    "s_mov_b32 s80, 256\n\t" \
    "s_add_u32 s81, %[s0], 0x1000\n\t" \
    "v_mov_b32 v41, 1\n\t" \
    ".Lr_t:\n\t" \
    "s_memrealtime s[82:83]\n\t" \
    "s_sub_u32 s80, s80, 1\n\t" \
    "s_cbranch_scc1 .Lr_go\n\t" \
    "s_waitcnt lgkmcnt(0)\n\t" \
    "s_sub_u32 s82, %[t1], s82\n\t" \
    "s_cmp_lt_i32 s82, 5\n\t" \
    "s_cbranch_scc1 .Lr_go\n\t" \
    "s_cmp_lt_i32 s82, 13\n\t" \
    "s_cbranch_scc1 .Lr_t\n\t" \
    "s_sleep 2\n\t" \
    "s_branch .Lr_t\n\t" \
    ".Lr_go:\n\t" \
    "buffer_load_dwordx4 v[0:3], %[v16], %[rsb], %[sm] offen sc1\n\t" \
    "buffer_load_dwordx4 v[4:7], %[v16], %[rsb], %[s0] offen sc1\n\t" \
    "buffer_load_dwordx4 v[8:11], %[v16], %[rsb], %[s0] offen offset:1024 sc1\n\t" \
    "buffer_load_dwordx4 v[12:15], %[v16], %[rsb], %[s0] offen offset:2048 sc1\n\t" \
    "buffer_load_dwordx4 v[16:19], %[v16], %[rsb], %[s0] offen offset:3072 sc1\n\t" \
    "buffer_load_dwordx4 v[20:23], %[v16], %[rsb], s81 offen sc1\n\t" \
    "buffer_load_dwordx4 v[24:27], %[v16], %[rsb], s81 offen offset:1024 sc1\n\t" \
    "buffer_load_dwordx4 v[28:31], %[v16], %[rsb], s81 offen offset:2048 sc1\n\t" \
    "buffer_load_dwordx4 v[32:35], %[v16], %[rsb], s81 offen offset:3072 sc1\n\t" \
    "ds_write_b32 %[ra], v41\n\t" \
    "s_waitcnt vmcnt(8)\n\t" \
    "v_add3_u32 v40, v0, v1, v2\n\t" \
    "v_xor_b32 v41, %[tag], v1\n\t" \
    "v_and_b32 v42, %[mk2], v0\n\t" \
    "v_add_u32 v40, v40, v3\n\t" \
    "v_add_f32_dpp v0, v0, v0 row_ror:8 row_mask:0xf bank_mask:0xf bound_ctrl:1\n\t" \
    "v_add_f32_dpp v1, v1, v1 row_ror:8 row_mask:0xf bank_mask:0xf bound_ctrl:1\n\t" \
    "v_add_f32_dpp v2, v2, v2 row_ror:8 row_mask:0xf bank_mask:0xf bound_ctrl:1\n\t" \
    "v_add_f32_dpp v3, v3, v3 row_ror:8 row_mask:0xf bank_mask:0xf bound_ctrl:1\n\t" \
    "v_and_b32 v40, %[mk01], v40\n\t" \
    "v_sub_u32 v40, v40, v42\n\t" \
    "s_waitcnt vmcnt(7)\n\t" \
    "v_mfma_f32_16x16x32_bf16 v[36:39], %[sel], v[4:7], 0\n\t" \
    "v_add3_u32 v40, v4, v5, v40\n\t" \
    "v_add3_u32 v40, v6, v7, v40\n\t" \
    "v_add_f32_dpp v0, v0, v0 row_ror:4 row_mask:0xf bank_mask:0xf bound_ctrl:1\n\t" \
    "v_add_f32_dpp v1, v1, v1 row_ror:4 row_mask:0xf bank_mask:0xf bound_ctrl:1\n\t" \
    "s_waitcnt vmcnt(6)\n\t" \
    "v_mfma_f32_16x16x32_bf16 v[36:39], %[sel], v[8:11], v[36:39]\n\t" \
    "v_add3_u32 v40, v8, v9, v40\n\t" \
    "v_add3_u32 v40, v10, v11, v40\n\t" \
    "v_add_f32_dpp v2, v2, v2 row_ror:4 row_mask:0xf bank_mask:0xf bound_ctrl:1\n\t" \
    "v_add_f32_dpp v3, v3, v3 row_ror:4 row_mask:0xf bank_mask:0xf bound_ctrl:1\n\t" \
    "s_waitcnt vmcnt(5)\n\t" \
    "v_mfma_f32_16x16x32_bf16 v[36:39], %[sel], v[12:15], v[36:39]\n\t" \
    "v_add3_u32 v40, v12, v13, v40\n\t" \
    "v_add3_u32 v40, v14, v15, v40\n\t" \
    "v_and_b32 v41, %[mk2], v41\n\t" \
    "v_cmp_ne_u32 vcc, 0, v41\n\t" \
    "s_waitcnt vmcnt(4)\n\t" \
    "v_mfma_f32_16x16x32_bf16 v[36:39], %[sel], v[16:19], v[36:39]\n\t" \
    "v_add3_u32 v40, v16, v17, v40\n\t" \
    "v_add3_u32 v40, v18, v19, v40\n\t" \
    "v_permlane32_swap_b32 v0, v2\n\t" \
    "v_permlane32_swap_b32 v1, v3\n\t" \
    "s_waitcnt vmcnt(3)\n\t" \
    "v_mfma_f32_16x16x32_bf16 v[36:39], %[sel], v[20:23], v[36:39]\n\t" \
    "v_add3_u32 v40, v20, v21, v40\n\t" \
    "v_add3_u32 v40, v22, v23, v40\n\t" \
    "v_add_f32 v0, v0, v2\n\t" \
    "v_add_f32 v1, v1, v3\n\t" \
    "s_waitcnt vmcnt(2)\n\t" \
    "v_mfma_f32_16x16x32_bf16 v[36:39], %[sel], v[24:27], v[36:39]\n\t" \
    "v_add3_u32 v40, v24, v25, v40\n\t" \
    "v_add3_u32 v40, v26, v27, v40\n\t" \
    "v_mov_b32 v2, v0\n\t" \
    "v_mov_b32 v3, v1\n\t" \
    "s_waitcnt vmcnt(1)\n\t" \
    "v_mfma_f32_16x16x32_bf16 v[36:39], %[sel], v[28:31], v[36:39]\n\t" \
    "v_add3_u32 v40, v28, v29, v40\n\t" \
    "v_add3_u32 v40, v30, v31, v40\n\t" \
    "s_or_b32 s80, vcc_lo, vcc_hi\n\t" \
    "s_waitcnt vmcnt(0)\n\t" \
    "v_mfma_f32_16x16x32_bf16 v[36:39], %[sel], v[32:35], v[36:39]\n\t" \
    "v_add3_u32 v40, v32, v33, v40\n\t" \
    "v_add3_u32 v40, v34, v35, v40\n\t" \
    "v_permlane16_swap_b32 v0, v2\n\t" \
    "v_permlane16_swap_b32 v1, v3\n\t" \
    "v_add_f32 v0, v0, v2\n\t" \
    "v_add_f32 v1, v1, v3\n\t" \
    "v_cvt_pk_bf16_f32 v44, v0, v1\n\t" \
    "v_mov_b32 v45, v44\n\t" \
    "v_cvt_pk_bf16_f32 v48, v36, v37\n\t" \
    "v_cvt_pk_bf16_f32 v49, v38, v39\n\t" \
    "v_and_b32 v46, %[mkl], v44\n\t" \
    "v_add3_u32 v46, v46, v48, v49\n\t" \
    "v_permlane32_swap_b32 v44, v45\n\t" \
    "buffer_store_dwordx2 v[48:49], %[vkf], %[rsb], %[sf] offen sc1\n\t" \
    "v_permlane32_swap_b32 v40, v46\n\t" \
    "v_add_u32 v40, v40, v46\n\t" \
    "v_mov_b32 v46, v40\n\t" \
    "v_cndmask_b32_e64 v45, v45, %[tag], %[hi]\n\t" \
    "s_nop 0\n\t" \
    "v_permlane16_swap_b32 v40, v46\n\t" \
    "v_add_u32 v40, v40, v46\n\t" \
    "s_nop 1\n\t" \
    "v_add_u32_dpp v40, v40, v40 row_ror:8 row_mask:0xf bank_mask:0xf bound_ctrl:1\n\t" \
    "s_nop 1\n\t" \
    "v_add_u32_dpp v40, v40, v40 row_ror:4 row_mask:0xf bank_mask:0xf bound_ctrl:1\n\t" \
    "s_nop 1\n\t" \
    "v_add_u32_dpp v40, v40, v40 row_ror:2 row_mask:0xf bank_mask:0xf bound_ctrl:1\n\t" \
    "s_nop 1\n\t" \
    "v_add_u32_dpp v40, v40, v40 row_ror:1 row_mask:0xf bank_mask:0xf bound_ctrl:1\n\t" \
    "s_nop 0\n\t" \
    "v_readfirstlane_b32 s81, v40\n\t" \
    "v_cndmask_b32_e64 v44, v44, v40, %[hi]\n\t" \
    "s_or_b32 %[st], s80, s81\n\t" \
    "s_cselect_b32 s82, 0x70000000, %[sg]\n\t" \
    "buffer_store_dwordx2 v[44:45], %[vfm], %[rsb], s82 offen sc1\n\t"
#define RED2_ASM \
    "s_mov_b32 s80, 256\n\t" \
    "s_add_u32 s81, %[s0], 0x1000\n\t" \
    "s_add_u32 s84, %[s0], 0x4000\n\t" \
    "s_mov_b32 m0, 160256\n\t" \
    ".Lr_u:\n\t" \
    "s_memrealtime s[82:83]\n\t" \
    "s_sub_u32 s80, s80, 1\n\t" \
    "s_cbranch_scc1 .Lr_g2\n\t" \
    "s_waitcnt lgkmcnt(0)\n\t" \
    "s_sub_u32 s82, %[t2], s82\n\t" \
    "s_cmp_lt_i32 s82, 5\n\t" \
    "s_cbranch_scc1 .Lr_g2\n\t" \
    "s_cmp_lt_i32 s82, 13\n\t" \
    "s_cbranch_scc1 .Lr_u\n\t" \
    "s_sleep 2\n\t" \
    "s_branch .Lr_u\n\t" \
    ".Lr_g2:\n\t" \
    "buffer_load_dwordx4 %[v16], %[rsb], s84 offen sc1 lds\n\t" \
    "s_mov_b32 m0, 151552\n\t" \
    "s_nop 0\n\t" \
    "buffer_load_dwordx4 %[v16], %[rsb], %[s0] offen sc1 lds\n\t" \
    "buffer_load_dwordx4 %[v16], %[rsb], %[s0] offen offset:1024 sc1 lds\n\t" \
    "buffer_load_dwordx4 %[v16], %[rsb], %[s0] offen offset:2048 sc1 lds\n\t" \
    "buffer_load_dwordx4 %[v16], %[rsb], %[s0] offen offset:3072 sc1 lds\n\t" \
    "s_mov_b32 m0, 155648\n\t" \
    "s_nop 0\n\t" \
    "buffer_load_dwordx4 %[v16], %[rsb], s81 offen sc1 lds\n\t" \
    "buffer_load_dwordx4 %[v16], %[rsb], s81 offen offset:1024 sc1 lds\n\t" \
    "buffer_load_dwordx4 %[v16], %[rsb], s81 offen offset:2048 sc1 lds\n\t" \
    "buffer_load_dwordx4 %[v16], %[rsb], s81 offen offset:3072 sc1 lds\n\t" \
    "ds_write_b32 %[ra], %[pv]\n\t" \
    "v_or_b32 %[pv], 1, %[pv]\n\t" \
    "v_add_u32 %[pv], 1, %[pv]\n\t" \
    "s_waitcnt vmcnt(0)\n\t" \
    "ds_write_b32 %[ra], %[pv]\n\t" \
    "ds_read_b128 v[0:3], %[kwa] offset:8704\n\t" \
    "ds_read_b128 v[4:7], %[kwa]\n\t" \
    "ds_read_b128 v[8:11], %[kwa] offset:1024\n\t" \
    "ds_read_b128 v[12:15], %[kwa] offset:2048\n\t" \
    "ds_read_b128 v[16:19], %[kwa] offset:3072\n\t" \
    "ds_read_b128 v[20:23], %[kwa] offset:4096\n\t" \
    "ds_read_b128 v[24:27], %[kwa] offset:5120\n\t" \
    "ds_read_b128 v[28:31], %[kwa] offset:6144\n\t" \
    "ds_read_b128 v[32:35], %[kwa] offset:7168\n\t" \
    "s_waitcnt lgkmcnt(8)\n\t" \
    "v_add3_u32 v40, v0, v1, v2\n\t" \
    "v_xor_b32 v41, %[tag], v1\n\t" \
    "v_and_b32 v42, %[mk1], v0\n\t" \
    "v_add_u32 v40, v40, v3\n\t" \
    "v_and_b32 v41, %[mk1], v41\n\t" \
    "v_and_b32 v40, %[mk0], v40\n\t" \
    "v_cmp_ne_u32 vcc, 0, v41\n\t" \
    "v_sub_u32 v40, v40, v42\n\t" \
    "v_mov_b32 v42, %[frc]\n\t" \
    "s_waitcnt lgkmcnt(4)\n\t" \
    "v_add3_u32 v40, v4, v5, v40\n\t" \
    "v_add3_u32 v42, v6, v7, v42\n\t" \
    "v_add3_u32 v40, v8, v9, v40\n\t" \
    "v_add3_u32 v42, v10, v11, v42\n\t" \
    "v_add3_u32 v40, v12, v13, v40\n\t" \
    "v_add3_u32 v42, v14, v15, v42\n\t" \
    "v_add3_u32 v40, v16, v17, v40\n\t" \
    "v_add3_u32 v42, v18, v19, v42\n\t" \
    "s_waitcnt lgkmcnt(0)\n\t" \
    "v_add3_u32 v40, v20, v21, v40\n\t" \
    "v_add3_u32 v42, v22, v23, v42\n\t" \
    "v_add3_u32 v40, v24, v25, v40\n\t" \
    "v_add3_u32 v42, v26, v27, v42\n\t" \
    "v_add3_u32 v40, v28, v29, v40\n\t" \
    "v_add3_u32 v42, v30, v31, v42\n\t" \
    "v_add3_u32 v40, v32, v33, v40\n\t" \
    "v_add3_u32 v42, v34, v35, v42\n\t" \
    "s_or_b32 s80, vcc_lo, vcc_hi\n\t" \
    "s_nop 0\n\t" \
    "v_permlane32_swap_b32 v40, v42\n\t" \
    "v_add_u32 v40, v40, v42\n\t" \
    "s_nop 1\n\t" \
    "v_add_u32_dpp v40, v40, v40 row_ror:8 row_mask:0xf bank_mask:0xf bound_ctrl:1\n\t" \
    "s_nop 1\n\t" \
    "v_add_u32_dpp v40, v40, v40 row_ror:4 row_mask:0xf bank_mask:0xf bound_ctrl:1\n\t" \
    "s_nop 1\n\t" \
    "v_add_u32_dpp v40, v40, v40 row_ror:2 row_mask:0xf bank_mask:0xf bound_ctrl:1\n\t" \
    "s_nop 1\n\t" \
    "v_add_u32_dpp v40, v40, v40 row_ror:1 row_mask:0xf bank_mask:0xf bound_ctrl:1\n\t" \
    "s_nop 0\n\t" \
    "v_readlane_b32 s81, v40, 0\n\t" \
    "v_readlane_b32 s82, v40, 16\n\t" \
    "v_readlane_b32 s83, v40, 32\n\t" \
    "v_readlane_b32 s84, v40, 48\n\t" \
    "s_add_u32 s81, s81, s82\n\t" \
    "s_add_u32 s83, s83, s84\n\t" \
    "s_add_u32 s81, s81, s83\n\t" \
    "s_or_b32 %[st], s80, s81\n\t" \
    "s_cselect_b32 s81, 0, 1\n\t" \
    "v_add_u32 v41, s81, %[pv]\n\t" \
    "ds_write_b32 %[ra], v41\n\t"
#define PHASE_AB_CLOB "v0", "v1", "v2", "v3", "v4", "v5", "v6", "v7", "v8", "v9", "v10", "v11", "v12", "v13", "v14", "v15", "v16", "v17", "v18", "v19", "v20", "v21", "v22", "v23", "v24", "v25", "v26", "v27", "v28", "v29", "v30", "v31", "v32", "v33", "v34", "v35", "v36", "v37", "v38", "v39", "v40", "v41", "v42", "v43", "v44", "v45", "v46", "v47", "v48", "v49", "v50", "v51", "v52", "v53", "v54", "v55", "v56", "v57", "v58", "v59", "v60", "v61", "v62", "v63", "v64", "v65", "v66", "v67", "v68", "v69", "v70", "v71", "v72", "v73", "v74", "v75", "v76", "v77", "v78", "v79", "v80", "v81", "v82", "v83", "v84", "v85", "v86", "v87", "v88", "v89", "v90", "v91", "v92", "v93", "v94", "v95", "v96", "v97", "v98", "v99", "v100", "v101", "v102", "v103", "v104", "v105", "v106", "v107", "v108", "v109", "v110", "v111", "v112", "v113", "v114", "v115", "v116", "v117", "v118", "v119", "v120", "v121", "v122", "v123", "v124", "v125", "v126", "v127", "v128", "v129", "v130", "v131", "v132", "v133", "v134", "v135", "v136", "v137", "v138", "v139", "v140", "v141", "v142", "v143", "v144", "v145", "v146", "v147", "v148", "v149", "v150", "v151", "v152", "v153", "v154", "v155", "v156", "v157", "v158", "v159", "v160", "v161", "v162", "v163", "v164", "v165", "v166", "v167", "v168", "v169", "v170", "v171", "v172", "v173", "v174", "v175", "v176", "v177", "v178", "v179", "v180", "v181", "v182", "v183", "v184", "v185", "v186", "v187", "v188", "v189", "v190", "v191", "v192", "v193", "v194", "v195", "v196", "v197", "v198", "v199", "v200", "v201", "v202", "v203", "v204", "v205", "v206", "v207", "v208", "v209", "v210", "v211", "v212", "v213", "v214", "v215", "v216", "v217", "v218", "v219", "v220", "v221", "v222", "v223", "v224", "v225", "v226", "v227", "v228", "v229", "v230", "v231", "v237", "v238", "s81", "s82", "s83", "s84", "s85", "s86", "s87", "s88", "s89", "s90", "s91", "s92", "s93", "s94", "s95"
#define PHASE_C_CLOB "v0", "v1", "v2", "v3", "v4", "v5", "v6", "v7", "v8", "v9", "v10", "v11", "v12", "v13", "v14", "v15", "v16", "v17", "v18", "v19", "v20", "v21", "v22", "v23", "v24", "v25", "v26", "v27", "v28", "v29", "v30", "v31", "v32", "v33", "v34", "v35", "v36", "v37", "v38", "v39", "v40", "v41", "v42", "v43", "v44", "v45", "v46", "v47", "v48", "v49", "v50", "v51", "v52", "v53", "v54", "v55", "v56", "v57", "v58", "v59", "v60", "v61", "v62", "v63", "v64", "v65", "v66", "v67", "v68", "v69", "v70", "v71", "v72", "v73", "v74", "v75", "v76", "v77", "v78", "v79", "v80", "v81", "v82", "v83", "v84", "v85", "v86", "v87", "v88", "v89", "v90", "v91", "v92", "v93", "v94", "v95", "v96", "v97", "v98", "v99", "v100", "v101", "v102", "v103", "v104", "v105", "v106", "v107", "v108", "v109", "v110", "v111", "v112", "v113", "v114", "v115", "v116", "v117", "v118", "v119", "v120", "v121", "v122", "v123", "v124", "v125", "v126", "v127", "v128", "v129", "v130", "v131", "v132", "v133", "v134", "v135", "v136", "v137", "v138", "v139", "v140", "v141", "v142", "v143", "v144", "v145", "v146", "v147", "v148", "v149", "v150", "v151", "v152", "v153", "v154", "v155", "v156", "v157", "v158", "v159", "v160", "v161", "v162", "v163", "v164", "v165", "v166", "v167", "v168", "v169", "v170", "v171", "v172", "v173", "v174", "v175", "v176", "v177", "v178", "v179", "v180", "v181", "v182", "v183", "v184", "v185", "v186", "v187", "v188", "v189", "v190", "v191", "v192", "v193", "v194", "v195", "v196", "v197", "v198", "v199", "v200", "v201", "v202", "v203", "v204", "v205", "v206", "v207", "v208", "v209", "v210", "v211", "v212", "v213", "v214", "v215", "v216", "v217", "v218", "v219", "v220", "v221", "v222", "v223", "v224", "v225", "v226", "v227", "v228", "v229", "v231", "s80", "s81", "s82", "s83", "s84", "s85", "s86", "s87"
#define RED1_CLOB "v0", "v1", "v2", "v3", "v4", "v5", "v6", "v7", "v8", "v9", "v10", "v11", "v12", "v13", "v14", "v15", "v16", "v17", "v18", "v19", "v20", "v21", "v22", "v23", "v24", "v25", "v26", "v27", "v28", "v29", "v30", "v31", "v32", "v33", "v34", "v35", "v36", "v37", "v38", "v39", "v40", "v41", "v42", "v43", "v44", "v45", "v46", "v47", "v48", "v49", "s80", "s81", "s82", "s83", "s84"

#define KNAME "fused_fwd"
#define CODE_FETCH(dst, kb) do { unsigned long long pc_; unsigned len_, off_; \
    asm volatile("s_getpc_b64 %0\n1:\n\ts_mov_b32 %1, 1b-" KNAME "\n\ts_mov_b32 %2, .Lff_end0-" KNAME "\n\ts_max_u32 %2, %2, .Lff_end1-" KNAME \
                 "\n\ts_max_u32 %2, %2, .Lff_end2-" KNAME : "=s"(pc_), "=s"(off_), "=s"(len_) :: "scc"); \
    const rsrc_t rsC_ = mk_rsrc((const void*)(pc_ - off_), len_); \
    asm volatile("s_nop 4\n\tbuffer_load_dwordx4 %0, %1, %2, %3 offen" : "=v"(dst) : "v"(fresh_lane() * 16), "s"(rsC_), "s"((unsigned)(kb) * 1024u) : "memory"); } while (0)

extern "C" __global__ __launch_bounds__(512, 2) void fused_fwd(const u16* __restrict__ x, const u16* __restrict__ wqkv,
                                                    const u16* __restrict__ wphi, const u16* __restrict__ wo,
                                                    const u16* __restrict__ wr, const u16* __restrict__ wgp,
                                                    const u16* __restrict__ wup, const u16* __restrict__ wdp,
                                                    const u16* __restrict__ gw, char* ws, u16* out) {
    __shared__ __attribute__((aligned(16))) char smem[163840];
    const int w = __builtin_amdgcn_readfirstlane(threadIdx.x >> 6);
    if (K::TOPV) {
        asm volatile("s_mov_b64 exec, 0\n\ts_cmp_eq_u32 %0, 6\n\ts_cbranch_scc1 .Lpad_x\n\t"
                     "s_cmp_eq_u32 %0, 0\n\ts_cbranch_scc1 .Lpad_d\n\ts_cmp_eq_u32 %0, 1\n\ts_cbranch_scc1 .Lpad_ab0\n"
                     ".Lpad_top:\n\ts_mov_b64 exec, -1" :: "s"(w) : "scc", "memory");
    } else {
        asm volatile(".Lpad_top:");
    }
    asm volatile("" :: "s"(x), "s"(wqkv));
    const int bid = blockIdx.x, xcd = bid & 7, slot = bid >> 3;
    const int mtA = (xcd & 1) * 8 + (slot & 7), hpA = (xcd >> 1) * 4 + (slot >> 3);
    const int mtB = (xcd & 1) * 8 + ((xcd >> 1) & 1) * 4 + (slot & 3), hpB = (xcd >> 2) * 8 + (slot >> 2);
    const int rotA = 8 * (xcd & 1);
    const bool has_ffn = slot < 24 || (xcd & 2);
    const int fb = slot < 24 ? bid : 192 + (slot - 24) * 4 + (xcd >> 2) * 2 + (xcd & 1);
    const unsigned lds0 = (unsigned)(size_t)(AS3 char*)smem;
    const rsrc_t rsWS = mk_rsrc(ws, WS_BYTES);
    const rsrc_t rsX = mk_rsrc(x, 2048u * 2048u);

    auto touch3 = [&](unsigned& dst, int half, bool on = true) {
        const rsrc_t rsT = mk_rsrc(wqkv, (on && slot < 24 && !(xcd & 2)) ? 1024u * 6144u : 0u);
        const unsigned so = (unsigned)(((slot & 7) + 8 * half + rotA) & 15) * (64u * 6144u) + (unsigned)(slot >> 3) * 2048u + (unsigned)((xcd >> 1) * 4 + 3) * 128u;
        asm volatile("s_nop 4\n\tbuffer_load_dword %0, %1, %2, %3 offen" : "=v"(dst) : "v"(fresh_lane() * 6144), "s"(rsT), "s"(so) : "memory");
    };
    unsigned gate_t = 0;
    int gate_s = 0;
    auto hush = [&]() {
        if (gate_s == 0) {
            gate_t = __builtin_amdgcn_readfirstlane(*(AS3 volatile const unsigned*)(size_t)(lds0 + L_TM + 8)) - K::QA;
            gate_s = 1;
        }
        if (gate_s == 3 || (int)(now32() - gate_t) < 0) return;
        if (gate_s == 1) {
            wait4(lds0 + C_P, P_PUB);
            lds_wait(lds0 + C_TM, 1);
            gate_t = __builtin_amdgcn_readfirstlane(*(AS3 volatile const unsigned*)(size_t)(lds0 + L_TM)) - K::QB;
            gate_s = 2;
            if ((int)(now32() - gate_t) < 0) return;
        }
        lds_wait(lds0 + C_R, R_L1I);
        gate_s = 3;
    };

    unsigned ep, pf0;
    u32x4 xv[4][2], rv[2], gv[2], pf;
    auto ldi = [&]() {
            const int l = fresh_lane(), a = w & 1, row0 = mtB * 128 + hpB * 8 + 4 * a;
            const rsrc_t rsR = mk_rsrc(wr, 2048u), rsGW = mk_rsrc(gw, 2048u);
            const unsigned v16 = (unsigned)l * 16u;
            asm volatile("s_nop 4\n\tbuffer_load_dword %0, off, %1, 0 sc1" : "=v"(ep) : "s"(rsWS) : "memory");
#pragma unroll
            for (int r = 0; r < 4; ++r) {
                asm volatile("buffer_load_dwordx4 %0, %1, %2, %3 offen" : "=v"(xv[r][0]) : "v"(v16), "s"(rsX), "s"((unsigned)(row0 + r) * 2048u) : "memory");
                asm volatile("buffer_load_dwordx4 %0, %1, %2, %3 offen offset:1024" : "=v"(xv[r][1]) : "v"(v16), "s"(rsX), "s"((unsigned)(row0 + r) * 2048u) : "memory");
            }
            asm volatile("buffer_load_dwordx4 %0, %1, %2, 0 offen" : "=v"(rv[0]) : "v"(v16), "s"(rsR) : "memory");
            asm volatile("buffer_load_dwordx4 %0, %1, %2, 0 offen offset:1024" : "=v"(rv[1]) : "v"(v16), "s"(rsR) : "memory");
            asm volatile("buffer_load_dwordx4 %0, %1, %2, 0 offen" : "=v"(gv[0]) : "v"(v16), "s"(rsGW) : "memory");
            asm volatile("buffer_load_dwordx4 %0, %1, %2, 0 offen offset:1024" : "=v"(gv[1]) : "v"(v16), "s"(rsGW) : "memory");
            {
                const rsrc_t rsP = mk_rsrc(wphi, 32u * 2048u);
                const unsigned vp = (unsigned)(l ^ (((l >> 4) & 1) << 1)) * 16u;
#pragma unroll
                for (int i = 0; i < 2; ++i) dma16(rsP, vp, (unsigned)hpA * 4096u + a * 2048u + i * 1024u, lds0 + L_WP + a * 2048 + i * 1024);
            }
            CODE_FETCH(pf, slot * 2 + a);
            touch3(pf0, 0, a == 0);
    };
    if (__builtin_expect((w >> 1) == 2, 1)) {
        const int d = w - 4;
        LANE_VARS;
        const int srow = l >> 3, pos = l & 7;
        const rsrc_t rsW = mk_rsrc(wqkv, 1024u * 6144u);
        const unsigned vx0 = srow * 2048 + ((pos ^ ((srow >> 1) & 7)) << 4);
        const unsigned vw0 = srow * 6144 + (((((pos >> 1) ^ (srow & 3)) << 1) | (pos & 1)) << 4);
        unsigned vxs[4], vws[4];
#pragma unroll
        for (int i = 0; i < 4; ++i) {
            vxs[i] = (vx0 ^ ((i & 1) * 64u)) + i * (16384u - 1024u);
            vws[i] = (vw0 ^ ((i & 1) * 32u)) + i * (8u * 6144u - 1024u);
        }
        const unsigned sx0 = (unsigned)(mtA * 128) * 2048u, sw0 = (unsigned)hpA * 128u;
        const int lq = ((slot >> 3) - slot) & 3;
        const unsigned ktab = lq == 0 ? K::KT0 : lq == 1 ? K::KT1 : lq == 2 ? K::KT2 : K::KT3;
        auto chunk = [&](int kt) { return (((ktab >> (4 * (kt >> 1))) & 7u) * 2u + (unsigned)((kt & 1) + rotA)) & 15u; };
        auto dx = [&](unsigned kk, int i, unsigned la) { dma4<K::PACE>(rsX, vxs, sx0 + kk * 128u + i * 65536u, lds0 + la + i * 4096); };
        auto dw = [&](unsigned kk, int p, int i, unsigned la) { dma4<K::PACE>(rsW, vws, sw0 + p * 2048u + (kk * 64u + i * 32u) * 6144u, lds0 + la + i * 4096); };
        asm volatile("s_nop 4" ::: "memory");
        unsigned lb = 0;
#pragma unroll 1
        for (int kt = 0; kt < 13; ++kt) {
            if (kt >= 2) {
                if (kt <= 10) wait_vm<20>(); else wait_vm<16>();
                bar();
            }
            const unsigned kk = chunk(kt);
            dx(kk, 2 * d, lb);
            dw(kk, 1 + d, 0, lb + 24576 + d * 8192);
            dx(kk, 2 * d + 1, lb);
            dw(kk, 1 + d, 1, lb + 24576 + d * 8192);
            if (kt < 10) dw(kk, 0, d, lb + 16384);
            lb = lb == 2 * SLOT ? 0u : lb + SLOT;
        }
        auto dxs = [&](int i) {
            const unsigned kk = chunk(13 + i), lx = d ? XS1[i] - 8192u : XS0[i];
            dx(kk, 2 * d, lx);
            dx(kk, 2 * d + 1, lx);
        };
        auto dws = [&](int i) {
            const unsigned kk = chunk(13 + i), lw = ((1 + i) % 3) * SLOT + 24576 + d * 8192;
            dw(kk, 1 + d, 0, lw);
            dw(kk, 1 + d, 1, lw);
        };
        auto dq = [&](int i) {
            const unsigned kk = chunk(i < 2 ? 14 + i : 8 + i);
            dw(kk, 0, 0, QW[i]);
            dw(kk, 0, 1, QW[i]);
        };
        if (K::XAH) dxs(0);
        wait_vm<K::XAH ? 24 : 16>();
        bar();
        dws(0);
        dxs(K::XAH ? 1 : 0);
        wait_vm<K::XAH ? 24 : 16>();
        bar();
        dws(1);
        dxs(K::XAH ? 2 : 1);
        wait_vm<K::XAH ? 24 : 16>();
        bar();
        dws(2);
        if (!K::XAH) dxs(2);
        if (d == 0) {
            dq(0);
            wait_vm<24>();
        } else {
            wait_vm<16>();
        }
        bar();
        if (d == 0) wait_vm<8>(); else wait_vm<0>();
        post(lds0 + C_ST + d * 4, 1);
        __builtin_amdgcn_s_setprio(3);
        if (d == 0) {
            const rsrc_t rsO = mk_rsrc(wo, 1024u * 2048u);
            unsigned vos[4];
#pragma unroll
            for (int i = 0; i < 4; ++i) vos[i] = WO_VOFF + i * (16384u - 1024u);
            unsigned sb[2] = {0u, 0u};
            auto wo_run = [&](int b0, int b1) {
#pragma unroll
                for (int b = b0; b < b1; ++b) {
                    unsigned so;
                    asm volatile("s_mov_b32 m0, %7\n\ts_add_u32 %0, %6, %8\n\tbuffer_load_dwordx4 %1, %5, %0 offen lds\n\tbuffer_load_dwordx4 %2, %5, %0 offen offset:1024 lds\n\t"
                                 "buffer_load_dwordx4 %3, %5, %0 offen offset:2048 lds\n\tbuffer_load_dwordx4 %4, %5, %0 offen offset:3072 lds\n\ts_waitcnt vmcnt(%9)"
                                 : "=&s"(so) : "v"(vos[0]), "v"(vos[1]), "v"(vos[2]), "v"(vos[3]), "s"(rsO), "s"(sb[b >> 4]), "i"(b * 4096), "i"((b & 15) * 65536), "i"(K::WO_D)
                                 : "memory", "scc");
                }
            };
            dq(1);
            dq(2);
            wait4(lds0 + C_P, P_E15);
            dq(3);
            dq(4);
            wait_vm<0>();
            post(lds0 + C_Q, 1);
            wait4(lds0 + C_P, P_KVD);
            dq(5);
            wait_vm<0>();
            post(lds0 + C_Q, 2);
            wait4(lds0 + C_P, P_PUB);
            {
                const unsigned s0 = (unsigned)(mtB * 128) * 2048u + (unsigned)hpB * 128u;
#pragma unroll
                for (int i = 0; i < 4; ++i) dma4<0>(rsX, vxs, s0 + i * 65536u, lds0 + L_XE + i * 4096);
            }
            lds_wait(lds0 + C_TM, 1);
            sb[1] = (__builtin_amdgcn_readfirstlane(*(AS3 volatile const unsigned*)(size_t)(lds0 + L_TM + 4)) & 1u) * (16u * 65536u) + (unsigned)hpB * 128u;
            sb[0] = sb[1] ^ (16u * 65536u);
            asm volatile("s_nop 4" : "+s"(sb[0]), "+s"(sb[1]));
            wo_run(26, 32);
            post(lds0 + C_XE, 1);
            wait4(lds0 + C_P, P_Q1 + 2);
            wo_run(16, 24);
            wait4(lds0 + C_P, P_QRD);
            wo_run(24, 26);
            wo_run(0, 6);
            post(lds0 + C_WO, 2);
            wo_run(6, 14);
            post(lds0 + C_WO, 3);
            wo_run(14, 16);
            wait_vm<0>();
            post(lds0 + C_WO, 4);
        } else {
            const unsigned v16 = (unsigned)l * 16u;
            const unsigned tag = *(AS3 const unsigned*)(size_t)(lds0 + L_TAG);
            const rsrc_t rsB = mk_rsrc(bulk_base(ws), WB_BYTES);
            lds_wait(lds0 + C_IDX, 1);
            const u32x2 pl = *(AS3 const u32x2*)(size_t)(lds0 + L_PLC);
            const unsigned okp = __builtin_amdgcn_readfirstlane(pl[0]), osm = __builtin_amdgcn_readfirstlane(pl[1]);
            unsigned t1 = 0;
            const unsigned base = now32() - 4096u;
            u32x4 p0, p1;
            CODE_FETCH(p0, slot * 2);
            CODE_FETCH(p1, slot * 2 + 1);
            const int ch = l & 3;
            unsigned mk01 = ch < 2 ? ~0u : 0u, mk0 = ch == 0 ? ~0u : 0u, mk1 = ch == 1 ? ~0u : 0u, mk2 = ch == 2 ? ~0u : 0u, mkl = (l & 31) < 2 ? ~0u : 0u;
            unsigned vkf = (unsigned)((g >> 1) * 16 + c) * 16u + (unsigned)(g & 1) * 8u, vfm = l < 2 ? (unsigned)l * 8u : l == 32 ? 16u : 0x80000000u;
            unsigned kwa = lds0 + L_KVF + l * 16, ra = lds0 + C_R;
            asm volatile("" : "+v"(mk01), "+v"(mk0), "+v"(mk1), "+v"(mk2), "+v"(mkl), "+v"(vkf), "+v"(vfm), "+v"(kwa), "+v"(ra));
            bf16x8 sel;
#pragma unroll
            for (int j = 0; j < 8; ++j) sel[j] = ((c & 7) == j && (c >> 3) == (g & 1)) ? (short)0x3f80 : (short)0;
            asm volatile("" : "+v"(sel));
            {
                unsigned ta, tb;
                auto latest = [&](unsigned off, bool& all) {
                    const u32x4 pa = ld128c(rsWS, v16, off), pb = ld128c(rsWS, v16 + 1024u, off);
                    const unsigned ma = max(pa[1] == tag ? min(pa[0] - base, 8192u) : 0u, pa[3] == tag ? min(pa[2] - base, 8192u) : 0u);
                    const unsigned mb = max(pb[1] == tag ? min(pb[0] - base, 8192u) : 0u, pb[3] == tag ? min(pb[2] - base, 8192u) : 0u);
                    all = __all(pa[1] == tag && pa[3] == tag && pb[1] == tag && pb[3] == tag);
                    ta = __builtin_amdgcn_readfirstlane(wave_umax(ma));
                    tb = __builtin_amdgcn_readfirstlane(wave_umax(mb));
                    return base + max(ta, tb);
                };
                bool all;
                wait4(lds0 + C_P, has_ffn ? K::TMW : P_PUB);
#pragma unroll 1
                for (int it = 0; it < 4; ++it) {
                    t1 = latest(WS_PT, all) + K::D1;
                    if (all) break;
                    wait4(lds0 + C_P, P_PUB);
                    __builtin_amdgcn_s_sleep(K::PT_S);
                }
                asm volatile("s_waitcnt vmcnt(0)" : "+v"(p0), "+v"(p1) :: "memory");
                post(lds0 + L_TM, t1);
                const unsigned t3 = t1 + (K::D2 + K::D3);
                post(lds0 + L_TM + 4, (t3 & ~1u) | (tb < ta ? 1u : 0u));
                post(lds0 + C_TM, 1);
                asm volatile("s_branch .Lpad_r\n\t.p2align 12\n.Lpad_r:\n\ts_cbranch_execz .Lpad_ret" ::: "memory");
                wait4(lds0 + C_P, P_PUB);
            }
            bool late = false;
#pragma unroll 1
            for (int it = 0; it < TRY_LIMIT; ++it) {
                unsigned bad;
                asm volatile(RED1_ASM : [st] "=&s"(bad)
                             : [rsb] "s"(rsB), [t1] "s"(t1), [s0] "s"(okp ^ ((unsigned)mtA * 16384u)), [sm] "s"(osm + (unsigned)((mtA >> 3) * 16384 + (mtA & 7) * 1024)),
                               [sf] "s"(osm + 32768u + (unsigned)mtA * 512u), [sg] "s"(osm + 49152u + (unsigned)mtA * 64u), [hi] "s"(0xffffffff00000000ull),
                               [v16] "v"(v16), [ra] "v"(ra), [tag] "v"(tag), [mk01] "v"(mk01), [mk2] "v"(mk2), [mkl] "v"(mkl), [sel] "v"(sel), [vkf] "v"(vkf), [vfm] "v"(vfm)
                             : "memory", "vcc", "scc", RED1_CLOB);
                if (__builtin_expect(!bad, 1)) break;
                late = true;
                if (it < K::RE1) continue;
                int i2 = 0;
#pragma unroll 1
                for (; i2 < POLL_LIMIT; ++i2) {
                    __builtin_amdgcn_s_sleep(K::RETRY_S);
                    fence_c();
                    if (__all(ld32c(rsB, (unsigned)c * 64u + 36u, osm + (unsigned)((mtA >> 3) * 16384 + (mtA & 7) * 1024)) == tag)) break;
                }
                if (i2 == POLL_LIMIT) break;
            }
            post(lds0 + C_R, R_L1);
            wait4(lds0 + C_P, P_Q1);
            if (late) __builtin_amdgcn_s_sleep(K::L2_S);
            unsigned pv = R_L2I;
#pragma unroll 1
            for (int it = 0; it < TRY_LIMIT; ++it) {
                unsigned bad;
                asm volatile(RED2_ASM : [st] "=&s"(bad), [pv] "+v"(pv)
                             : [rsb] "s"(rsB), [t2] "s"(t1 + K::D2), [s0] "s"(osm + 32768u), [frc] "s"(K::FORCE_REDO && it == 0 ? 1 : 0),
                               [v16] "v"(v16), [ra] "v"(ra), [kwa] "v"(kwa), [tag] "v"(tag), [mk0] "v"(mk0), [mk1] "v"(mk1)
                             : "memory", "vcc", "scc", RED1_CLOB);
                if (__builtin_expect(!bad, 1)) break;
                if (it < K::RE1) continue;
                int i2 = 0;
#pragma unroll 1
                for (; i2 < POLL_LIMIT; ++i2) {
                    __builtin_amdgcn_s_sleep(K::RETRY_S);
                    fence_c();
                    if (__all(ld32c(rsB, (unsigned)c * 64u + 20u, osm + 49152u) == tag)) break;
                }
                if (i2 == POLL_LIMIT) break;
            }
            post(ra, pv + 1u);
            asm volatile(".Lff_end1:");
        }
        return;
    }

    asm volatile(".Lpad_d:\n\ts_cbranch_execz .Lpad_top" ::: "memory");
    __builtin_amdgcn_sched_barrier(0);
    if (w >= 6) {
        const int a = w - 6;
        const int cb = fb >> 2, kq = fb & 3;
        u32x4 wg_[16], wu_[16], wd_[16];
        unsigned tag;
        int idx;
        u32x4 xs[2];
        {
        LANE_VARS;
        const int srow = l >> 3, pos = l & 7;
        const unsigned nb = has_ffn ? 1024u * 7168u : 0u;
        const rsrc_t rsG = mk_rsrc(wgp, nb), rsU = mk_rsrc(wup, nb), rsD = mk_rsrc(wdp, nb);
        const unsigned vf = (unsigned)(16 * srow) * 7168u + (unsigned)pos * 16u;
        const int k8 = fb & 7, cg = (fb >> 4) * 4 + ((fb >> 3) & 1) * 2 + a, cu = cg ^ 2;
        const unsigned sg = (unsigned)(k8 * 128) * 7168u + (unsigned)cg * 128u, su = sg ^ 256u;
        auto ldg = [&]() {
#pragma unroll
            for (int i = 0; i < 16; ++i) {
                asm volatile("buffer_load_dwordx4 %0, %1, %2, %3 offen nt" : "=v"(wg_[i]) : "v"(vf), "s"(rsG), "s"(sg + i * 7168u) : "memory");
            }
        };
        auto ldu = [&]() {
#pragma unroll
            for (int i = 0; i < 16; ++i) {
                asm volatile("buffer_load_dwordx4 %0, %1, %2, %3 offen nt" : "=v"(wu_[i]) : "v"(vf), "s"(rsU), "s"(su + i * 7168u) : "memory");
            }
        };
        auto ldd = [&](int i0, int i1) {
            const unsigned vd = (unsigned)g * (16u * 2048u) + (unsigned)c * 16u;
            const unsigned sd = (unsigned)(cb * 64) * 2048u + (unsigned)(kq * 512 + a * 256);
#pragma unroll
            for (int i = i0; i < i1; ++i) {
                if ((i & 3) == 0) { wait_vm<K::FD>(); hush(); }
                asm volatile("buffer_load_dwordx4 %0, %1, %2, %3 offen nt" : "=v"(wd_[i]) : "v"(vd), "s"(rsD), "s"(sd + i * 2048u) : "memory");
            }
        };
        const int row0 = mtB * 128 + hpB * 8 + 4 * a;
        if (a == 0) {
            *(AS3 unsigned*)(size_t)(lds0 + L_SM + l * 4) = 0u;
            wait_lgkm0();
        }
        int b = 0;
        unsigned tl = 0, pmin = 255;
        auto tick = [&]() {
            bar();
            if (a == 1) {
                const unsigned t = now32();
                if (b > 2 && b <= 16 - K::ND) pmin = min(pmin, t - tl);
                tl = t;
            }
            ++b;
        };
        auto upto = [&](int e) {
#pragma unroll 1
            while (b <= e) tick();
        };
        auto pads = [&]() {
#pragma unroll 1
            for (int i = a; i < 6; i += 2) {
                unsigned long long ex;
                asm volatile("s_mov_b64 %0, exec\n\ts_mov_b64 exec, 0\n\t"
                             "s_cmp_eq_u32 %1, 0\n\ts_cbranch_scc1 .Lpad_ab1\n\ts_cmp_eq_u32 %1, 1\n\ts_cbranch_scc1 .Lpad_ab2\n\t"
                             "s_cmp_eq_u32 %1, 2\n\ts_cbranch_scc1 .Lpad_c0\n\ts_cmp_eq_u32 %1, 3\n\ts_cbranch_scc1 .Lpad_c1\n\t"
                             "s_cmp_eq_u32 %1, 4\n\ts_cbranch_scc1 .Lpad_r\n"
                             ".Lpad_ret:\n\ts_mov_b64 exec, %0" : "=&s"(ex) : "s"(i) : "scc", "memory");
                if (i < 4 && b <= 9) tick();
            }
        };
        auto nap = [&](int n) {
#pragma unroll 1
            for (int i = 0; i < n; ++i) __builtin_amdgcn_s_sleep(1);
        };
        const int lq4 = ((((slot >> 3) - slot) & 3) + (slot & 4)) * 4;
        const int bg = ((unsigned)K::BGT >> lq4) & 15, bu = ((unsigned)K::BUT >> lq4) & 15;
        unsigned pf3 = 0;
        ldi();
        u32x2 gr;
        unsigned vgr;
        auto router = [&](auto n_) {
            tick();
            asm volatile("s_waitcnt vmcnt(%15)"
                         : "+v"(ep), "+v"(xv[0][0]), "+v"(xv[0][1]), "+v"(xv[1][0]), "+v"(xv[1][1]), "+v"(xv[2][0]), "+v"(xv[2][1]), "+v"(xv[3][0]),
                           "+v"(xv[3][1]), "+v"(rv[0]), "+v"(rv[1]), "+v"(gv[0]), "+v"(gv[1]), "+v"(pf), "+v"(pf0)
                         : "i"(decltype(n_)::value) : "memory");
            tag = __builtin_amdgcn_readfirstlane(ep) * 2654435761u + 0x9e3779b9u;
            float ra[4], ga[4];
#pragma unroll
            for (int r = 0; r < 4; ++r) {
                float racc = 0.f, gacc = 0.f;
#pragma unroll
                for (int i = 0; i < 2; ++i)
#pragma unroll
                    for (int j = 0; j < 4; ++j) {
                        racc = dot2(xv[r][i][j], rv[i][j], racc);
                        gacc = dot2(xv[r][i][j], gv[i][j], gacc);
                    }
                ra[r] = racc;
                ga[r] = gacc;
                asm volatile("" : "+v"(ra[r]), "+v"(ga[r]));
            }
            tick();
            float r2[2], g2[2];
#pragma unroll
            for (int i = 0; i < 2; ++i) {
                auto p = __builtin_amdgcn_permlane32_swap(FU(ra[2 * i]), FU(ra[2 * i + 1]), false, false);
                r2[i] = UF(p[0]) + UF(p[1]);
                auto q = __builtin_amdgcn_permlane32_swap(FU(ga[2 * i]), FU(ga[2 * i + 1]), false, false);
                g2[i] = UF(q[0]) + UF(q[1]);
            }
            auto p = __builtin_amdgcn_permlane16_swap(FU(r2[0]), FU(r2[1]), false, false);
            auto q = __builtin_amdgcn_permlane16_swap(FU(g2[0]), FU(g2[1]), false, false);
            float rs = row16_sum(UF(p[0]) + UF(p[1])), gs = row16_sum(UF(q[0]) + UF(q[1]));
            asm volatile("" : "+v"(rs), "+v"(gs));
            const int row = row0 + (g >> 1) + 2 * (g & 1);
            const unsigned rbits = f2bf(rs);
            const unsigned okey = (rbits & 0x8000u) ? (~rbits & 0xffffu) : (rbits | 0x8000u);
            if (c == 0) *(AS3 unsigned*)(size_t)(lds0 + L_KEY + (4 * a + g) * 4) = (okey << 16) | (unsigned)(0xffff - row);
            if (l == 0 && a == 0) *(AS3 unsigned*)(size_t)(lds0 + L_TAG) = tag;
            gr = u32x2{FU(rbf(1.f / (1.f + __expf(-rbf(gs))))), tag};
            vgr = c == 0 ? WS_G + (unsigned)row * 8u : 0x80000000u;
            asm volatile("" : "+v"(gr), "+v"(vgr));
            wait_lgkm0();
            tick();
            if (a == 0) {
                unsigned k8 = *(AS3 const unsigned*)(size_t)(lds0 + L_KEY + (l & 7) * 4);
                k8 = max(k8, (unsigned)__shfl_xor((int)k8, 1));
                k8 = max(k8, (unsigned)__shfl_xor((int)k8, 2));
                k8 = max(k8, (unsigned)__shfl_xor((int)k8, 4));
                const u32x4 pv = *(AS3 const u32x4*)(size_t)(lds0 + L_PRB);
                st64c(u32x2{k8 ^ (((pv[1] < pv[0] ? 1u : 0u) + (pv[3] < pv[2] ? 1u : 0u)) << 11), tag}, rsWS, l ? 0x80000000u : WS_RG + (unsigned)bid * 8u, 0);
            }
        };
        u32x4 ga, gb;
        const unsigned vr = WS_RG + (unsigned)l * 16u;
        auto ldrg = [&]() {
            asm volatile("buffer_load_dwordx4 %0, %1, %2, 0 offen sc1" : "=v"(ga) : "v"(vr), "s"(rsWS) : "memory");
            asm volatile("buffer_load_dwordx4 %0, %1, %2, 0 offen offset:1024 sc1" : "=v"(gb) : "v"(vr), "s"(rsWS) : "memory");
        };
        router(std::integral_constant<int, 0>{});
        __builtin_amdgcn_s_setprio(2);
        upto(bg);
        nap(K::SLG);
        if (a == 0) {
            touch3(pf3, 1);
        } else {
            const rsrc_t rsT = mk_rsrc(wqkv, 1024u * 6144u);
            asm volatile("s_nop 4\n\tbuffer_load_dword %0, %1, %2, %3 offen" : "=v"(pf3) : "v"((unsigned)(l & 31) * 6144u), "s"(rsT),
                         "s"((unsigned)(((12 + rotA) & 15) * 64 + (slot & 7) * 32) * 6144u + (unsigned)hpA * 128u) : "memory");
        }
        ldg();
        upto(bu);
        if (bu != bg) nap(K::SLG);
        ldu();
        upto(((unsigned)K::BRT >> lq4) & 15);
        ldrg();
        pads();
        upto(9);
        asm volatile("s_waitcnt vmcnt(0)"
                     : "+v"(wg_[0]), "+v"(wg_[1]), "+v"(wg_[2]), "+v"(wg_[3]), "+v"(wg_[4]), "+v"(wg_[5]), "+v"(wg_[6]), "+v"(wg_[7]),
                       "+v"(wg_[8]), "+v"(wg_[9]), "+v"(wg_[10]), "+v"(wg_[11]), "+v"(wg_[12]), "+v"(wg_[13]), "+v"(wg_[14]), "+v"(wg_[15]),
                       "+v"(ga), "+v"(gb), "+v"(pf3)
                     :: "memory");
        asm volatile(""
                     : "+v"(wu_[0]), "+v"(wu_[1]), "+v"(wu_[2]), "+v"(wu_[3]), "+v"(wu_[4]), "+v"(wu_[5]), "+v"(wu_[6]), "+v"(wu_[7]),
                       "+v"(wu_[8]), "+v"(wu_[9]), "+v"(wu_[10]), "+v"(wu_[11]), "+v"(wu_[12]), "+v"(wu_[13]), "+v"(wu_[14]), "+v"(wu_[15])
                     :: "memory");
#pragma unroll 1
        for (int it = 0; it < POLL_LIMIT; ++it) {
            if (__all((ga[1] == tag) && (ga[3] == tag) && (gb[1] == tag) && (gb[3] == tag))) break;
            __builtin_amdgcn_s_sleep(4 * K::RETRY_S);
            fence_c();
            ga = ld128c(rsWS, vr, 0);
            gb = ld128c(rsWS, vr + 1024u, 0);
        }
        const unsigned key = __builtin_amdgcn_readfirstlane(wave_umax(max(max(ga[0] | 0x3800u, ga[2] | 0x3800u), max(gb[0] | 0x3800u, gb[2] | 0x3800u))));
        idx = (0xffff - (int)(key & 0xffffu)) & 2047;
        unsigned tw[2];
        auto twf = [&]() {
            const rsrc_t rsO = mk_rsrc(wo, 1024u * 2048u);
            const unsigned vt = (unsigned)srow * 2048u + (unsigned)pos * 128u, st = (unsigned)(slot * 32 + a * 16) * 2048u + (unsigned)(xcd >> 2) * 1024u;
            nap(K::X1);
            if (K::TNT) {
            asm volatile("s_nop 4\n\tbuffer_load_dword %0, %1, %2, %3 offen nt" : "=v"(tw[0]) : "v"(vt), "s"(rsO), "s"(st) : "memory");
            asm volatile("buffer_load_dword %0, %1, %2, %3 offen nt" : "=v"(tw[1]) : "v"(vt), "s"(rsO), "s"(st + 16384u) : "memory");
            } else {
            asm volatile("s_nop 4\n\tbuffer_load_dword %0, %1, %2, %3 offen" : "=v"(tw[0]) : "v"(vt), "s"(rsO), "s"(st) : "memory");
            asm volatile("buffer_load_dword %0, %1, %2, %3 offen" : "=v"(tw[1]) : "v"(vt), "s"(rsO), "s"(st + 16384u) : "memory");
            }
        };
        tick();
        if (K::TWB == 10) twf();
        {
            const unsigned vs = (unsigned)srow * 32u, ss = (unsigned)idx * 2048u + (unsigned)(k8 * 256);
            asm volatile("s_nop 4\n\tbuffer_load_dwordx4 %0, %1, %2, %3 offen" : "=v"(xs[0]) : "v"(vs), "s"(rsX), "s"(ss) : "memory");
            asm volatile("buffer_load_dwordx4 %0, %1, %2, %3 offen offset:16" : "=v"(xs[1]) : "v"(vs), "s"(rsX), "s"(ss) : "memory");
        }
        if (a == 1) {
            const u32x2 pt = {tl + (pmin * (K::PRED_K + 12) >> 2) + K::PRED_C + K::XE, tag};
            st64c(pt, rsWS, l ? 0x80000000u : WS_PT + (unsigned)(hpA * 16 + mtA) * 8u, 0);
        } else {
            const unsigned kb = key >> 16;
            const unsigned rb_ = (kb & 0x8000u) ? (kb & 0x7fffu) : (~kb & 0xffffu);
            const float rsel = rbf(1.f / (1.f + __expf(-UF(rb_ << 16))));
            const u32x2 iv = {(unsigned)idx, FU(rsel)};
            *(AS3 u32x2*)(size_t)(lds0 + L_IDX) = iv;
            const unsigned nv = (~ga[0] & 0x3800u) + (~ga[2] & 0x3800u) + (~gb[0] & 0x3800u) + (~gb[2] & 0x3800u);
            const unsigned votes = wave_usum((l & 3) == (xcd >> 1) ? nv >> 11 : 0u);
            const unsigned o0 = (unsigned)(xcd >> 1) * (2u << 20) + (K::X2 < 2 ? K::X2 * 8192u : votes > 64u ? 8192u : 0u);
            const u32x4 pl = {o0 + WB_KVP + (((unsigned)(hpA & 3) * 8u) ^ (tag & 31u)) * 32768u, o0 + WB_SM + (((unsigned)hpA & 3u) ^ ((tag >> 5) & 7u)) * 65536u,
                              ((tag >> 8) & 63u) * 2048u, 0u};
            *(AS3 u32x4*)(size_t)(lds0 + L_PLC) = pl;
            post(lds0 + C_IDX, 1);
            wait_lgkm0();
        }
        unsigned mk[4];
#pragma unroll
        for (int k = 0; k < 4; ++k) mk[k] = (pos >> 1) == k ? ((pos & 1) ? 0xffff0000u : 0x0000ffffu) : 0u;
        f32x4 sg4 = {0.f, 0.f, 0.f, 0.f}, su4 = sg4;
        auto dot = [&](int i0, int i1) {
#pragma unroll
            for (int i = i0; i < i1; ++i) {
                const unsigned xw = xs[i >> 3][(i >> 1) & 3];
                const unsigned xd = __builtin_amdgcn_perm(xw, xw, (i & 1) ? 0x03020302u : 0x01000100u);
                const u32x4 am = {xd & mk[0], xd & mk[1], xd & mk[2], xd & mk[3]};
                sg4 = mfma16(__builtin_bit_cast(bf16x8, am), __builtin_bit_cast(bf16x8, wg_[i]), sg4);
                su4 = mfma16(__builtin_bit_cast(bf16x8, am), __builtin_bit_cast(bf16x8, wu_[i]), su4);
            }
            asm volatile("" : "+v"(sg4), "+v"(su4));
        };
        tick();
        asm volatile("s_waitcnt vmcnt(0)" : "+v"(xs[0]), "+v"(xs[1]) :: "memory");
        if (K::TWB == 11) twf();
        dot(0, K::DS1);
        tick();
        dot(K::DS1, K::DS2);
        if (K::TWB == 12) twf();
        tick();
        if (a == 1) {
            post(lds0 + L_TM + 8, tl + (pmin * (K::PRED_K + K::PK13) >> 2) + K::PRED_C);
            wait_lgkm0();
        }
        dot(K::DS2, K::DS3);
        if (K::TWB == 13) twf();
        __builtin_amdgcn_s_setprio(3);
        tick();
        ldd(0, 8);
        dot(K::DS3, 16);
        {
            unsigned fo[4];
#pragma unroll
            for (int r = 0; r < 4; ++r) {
                auto p = __builtin_amdgcn_permlane32_swap(FU(sg4[r]), FU(su4[r]), false, false);
                fo[r] = FU(UF(p[0]) + UF(__builtin_amdgcn_update_dpp(0, (int)p[1], 0x128, 0xf, 0xf, false)));
            }
            const unsigned vf0 = ((l & 8) || !has_ffn) ? 0x80000000u : WS_FG + (unsigned)(((l >> 5) ? cu : cg) * 8 + k8) * 1024u + (unsigned)((l >> 5) * 512 + (pos * 8 + (g & 1) * 4) * 8);
            const u32x4 s0 = {fo[0], tag, fo[1], tag}, s1 = {fo[2], tag, fo[3], tag};
            st128c(s0, rsWS, vf0, 0);
            st128c(s1, rsWS, vf0 + 16u, 0);
            st64c(gr, rsWS, vgr, 0);
        }
        if (bid == 0 && a == 0 && l == 0) st32c(tag, rsWS, WS_EPOCH, 0);
        ldd(8, 16);
        lds_wait(lds0 + C_TM, 1);
        const unsigned tm0 = *(AS3 const unsigned*)(size_t)(lds0 + L_TM);
        const unsigned vq = (unsigned)((l & 8) * 64 + (g * 8 + (l & 7)) * 16);
        u32x4 fq[8];
        if (has_ffn) {
            wait_until(__builtin_amdgcn_readfirstlane(tm0) + K::FG_AT);
#pragma unroll 1
            for (int it = 0; it < POLL_LIMIT; ++it) {
                unsigned bad = 0;
#pragma unroll
                for (int i = 0; i < 8; ++i) fq[i] = ld128c(rsWS, vq + (i & 3) * 1024u, WS_FG + (unsigned)cb * 8192u + (i >> 2) * 4096u);
#pragma unroll
                for (int i = 0; i < 8; ++i) bad |= (fq[i][1] ^ tag) | (fq[i][3] ^ tag);
                if (__all(bad == 0u)) break;
                __builtin_amdgcn_s_sleep(2 * K::RETRY_S);
                fence_c();
            }
        }
        asm volatile("s_waitcnt vmcnt(0)"
                     : "+v"(wd_[0]), "+v"(wd_[1]), "+v"(wd_[2]), "+v"(wd_[3]), "+v"(wd_[4]), "+v"(wd_[5]), "+v"(wd_[6]), "+v"(wd_[7]),
                       "+v"(wd_[8]), "+v"(wd_[9]), "+v"(wd_[10]), "+v"(wd_[11]), "+v"(wd_[12]), "+v"(wd_[13]), "+v"(wd_[14]), "+v"(wd_[15]),
                       "+v"(tw[0]), "+v"(tw[1])
                     :: "memory");
        if (has_ffn) {
            if (K::DWP) lds_wait(lds0 + C_P + (2 + a) * 4, P_PUB);
            __builtin_amdgcn_s_setprio(K::HP);
            unsigned dp[8][8];
#pragma unroll
            for (int m = 0; m < 8; ++m)
#pragma unroll
                for (int j = 0; j < 4; ++j) {
                    dp[m][2 * j] = __builtin_amdgcn_perm(wd_[2 * m + 1][j], wd_[2 * m][j], 0x05040100u);
                    dp[m][2 * j + 1] = __builtin_amdgcn_perm(wd_[2 * m + 1][j], wd_[2 * m][j], 0x07060302u);
                }
            float hv[2];
#pragma unroll
            for (int j = 0; j < 2; ++j) {
                const float gs = rbf(((UF(fq[0][2 * j]) + UF(fq[1][2 * j])) + (UF(fq[2][2 * j]) + UF(fq[3][2 * j]))) +
                                     ((UF(fq[4][2 * j]) + UF(fq[5][2 * j])) + (UF(fq[6][2 * j]) + UF(fq[7][2 * j]))));
                const float us = UF(__builtin_amdgcn_update_dpp(0, (int)FU(gs), 0x128, 0xf, 0xf, false));
                hv[j] = rbf(gs / (1.f + __expf(-gs))) * us;
            }
            const unsigned hp = pack2(hv[0], hv[1]);
            float ya[8];
#pragma unroll
            for (int j = 0; j < 8; ++j) ya[j] = 0.f;
            auto dn = [&](auto mc) {
                constexpr int m = decltype(mc)::value;
                const unsigned hb = (unsigned)__builtin_amdgcn_update_dpp(0, (int)hp, 0x150 + m, 0xf, 0xf, false);
#pragma unroll
                for (int j = 0; j < 8; ++j) ya[j] = dot2(dp[m][j], hb, ya[j]);
            };
            dn(std::integral_constant<int, 0>{}); dn(std::integral_constant<int, 1>{}); dn(std::integral_constant<int, 2>{}); dn(std::integral_constant<int, 3>{});
            dn(std::integral_constant<int, 4>{}); dn(std::integral_constant<int, 5>{}); dn(std::integral_constant<int, 6>{}); dn(std::integral_constant<int, 7>{});
            float y4[4], y2[2];
#pragma unroll
            for (int j = 0; j < 4; ++j) {
                auto p = __builtin_amdgcn_permlane32_swap(FU(ya[j]), FU(ya[4 + j]), false, false);
                y4[j] = UF(p[0]) + UF(p[1]);
            }
#pragma unroll
            for (int j = 0; j < 2; ++j) {
                auto p = __builtin_amdgcn_permlane16_swap(FU(y4[j]), FU(y4[2 + j]), false, false);
                y2[j] = UF(p[0]) + UF(p[1]);
            }
            const u32x4 st = {FU(y2[0]), tag, FU(y2[1]), tag};
            st128c(st, rsWS, (unsigned)(c * 64 + g * 16), WS_Y + (unsigned)cb * 8192u + (unsigned)(kq * 2048 + a * 1024));
        }
        {
            const unsigned col8 = (unsigned)(xcd * 128 + slot * 4 + a * 2) * 8u;
            const unsigned vy = l < 56 ? (unsigned)l * 8192u : 0x80000000u;
            u32x4 t;
            __builtin_amdgcn_s_setprio(3);
            wait_until(__builtin_amdgcn_readfirstlane(tm0) + K::Y_AT);
#pragma unroll 1
            for (int it = 0; it < POLL_LIMIT; ++it) {
                t = ld128c(rsWS, vy, WS_Y + col8);
                if (__all(l >= 56 || (t[1] == tag && t[3] == tag))) break;
                __builtin_amdgcn_s_sleep(4 * K::RETRY_S);
                fence_c();
            }
            auto p = __builtin_amdgcn_permlane32_swap(t[0], t[2], false, false);
            const float s = row16_sum(x16_sum(UF(p[0]) + UF(p[1])));
            st64c(u32x2{FU(s), tag}, rsWS, (l & 31) ? 0x80000000u : (unsigned)(l >> 2), WS_YF + col8);
        }
        if ((idx >> 7) == mtB && a == 0) {
            u32x4 t;
            wait_until(__builtin_amdgcn_readfirstlane(tm0) + K::Y_AT + K::X0);
#pragma unroll 1
            for (int it = 0; it < POLL_LIMIT; ++it) {
                t = ld128c(rsWS, (unsigned)(l & 31) * 16u, WS_YF + (unsigned)hpB * 512u);
                if (__all(t[1] == tag && t[3] == tag)) break;
                __builtin_amdgcn_s_sleep(2 * K::RETRY_S);
                fence_c();
            }
            lds_wait(lds0 + C_XE, 1);
            const int rr = idx & 127;
            const unsigned xx = *(AS3 const unsigned*)(size_t)(lds0 + L_XE + rr * 128 + ((((l & 31) >> 2) ^ ((rr >> 1) & 7)) << 4) + (l & 3) * 4);
            const float rsel = *(AS3 const float*)(size_t)(lds0 + L_IDX + 4);
            *(AS3 f32x2*)(size_t)(lds0 + L_Y + 256 + (l & 31) * 8) = f32x2{rbf(lo16(xx) + rbf(rbf(UF(t[0])) * rsel)), rbf(hi16(xx) + rbf(rbf(UF(t[2])) * rsel))};
            post(lds0 + C_YRDY, 1);
        }
        }
        asm volatile(".Lff_end0:");
        return;
    }

    const int v = w & 1, hd = w >> 1;
    const rsrc_t rsB = mk_rsrc(bulk_base(ws), WB_BYTES);
    unsigned tag, sof, t3, rid;
    unsigned long long rm;
    u32x16 ad;
    u32x4 g4;
    {
        const unsigned pi = (unsigned)((slot * 2 + (xcd & 1)) * 2 + hd);
        const char* pa = bulk_base(ws) + ((unsigned)(xcd >> 1) * (2u << 20) + WB_PRB + (pi >> 5) * 16384u + (pi & 31u) * 256u + v * 8192u);
        unsigned j0;
        asm volatile("s_load_dword %0, %1, 128" : "={s80}"(j0) : "s"(pa) : "memory");
        LANE_VARS;
        ad[0] = lds0 + (unsigned)(64 * v + c) * 128u + (unsigned)((g ^ ((c >> 1) & 7)) << 4);
        ad[1] = lds0 + 16384u + (WSW ^ (unsigned)(hd << 6));
        ad[2] = lds0 + L_VF + (unsigned)(hd * 2048 + v * 8192 + l * 16);
        ad[3] = lds0 + L_PHI + hd * 16384 + (unsigned)(64 * v + c) * 128u + (unsigned)((((c & 3) ^ ((c >> 2) & 1)) << 5) + ((g ^ (c >> 2)) << 3));
        ad[4] = lds0 + L_PHI + hd * 16384 + v * 8192 + ((PSW + (unsigned)((i3 ^ g) << 3)) ^ (unsigned)(v << 6));
        ad[5] = lds0 + L_WP + hd * 2048 + WPO;
        ad[6] = (unsigned)(l >> 5) * 16384u + (unsigned)(l & 31) * 16u;
        ad[7] = c < 3 ? (unsigned)(((g & 1) * 2 + (g >> 1)) * 1024 + c * 16) : 0x80000000u;
        ad[8] = lds0 + C_P + w * 4;
        ad[9] = lds0 + L_SM;
        ad[10] = c;
#pragma unroll
        for (int i = 11; i < 16; ++i) ad[i] = 0u;
        asm volatile(PHASE_AB_ASM
                     : "+{v[240:255]}"(ad), "={v[232:235]}"(g4), "={v236}"(tag), [sof] "=&s"(sof), [t3] "=&s"(t3), [rid] "=&s"(rid), [rm] "=&s"(rm), "+{s80}"(j0)
                     : [rsb] "s"(rsB), [rsw] "s"(rsWS), [xs0] "s"(v ? XS1[0] - 8192u : XS0[0]), [xs1] "s"(v ? XS1[1] - 8192u : XS0[1]), [kpx] "s"(w * 65536),
                       [kpa] "s"(mtA * 512), [kma] "s"(hd * 16384 + v * 4096 + mtA * 64), [sg] "s"(WS_G + (unsigned)(mtB * 128 + 32 * w) * 8u),
                       [ofc] "s"(WB_OF + (unsigned)(mtB * 4 + w) * 65536u), [ofp] "s"(WB_OF + (unsigned)((mtA * 4 + 2 * v) * 32 + ((2 * hpA + hd + 1) & 31)) * 2048u),
                       [ogp] "s"(WB_OG + (unsigned)((mtA * 4 + 2 * v) * 32 + 2 * hpA + hd) * 8u), [kac] "s"(L_KVF + hd * 4096), [zac] "s"(L_ZF + hd * 512),
                       [xec] "s"(L_XE + w * 4096), [own] "s"(mtB * 4 + w), [prb] "s"(pa)
                     : "memory", "vcc", "scc", PHASE_AB_CLOB);
    }
    {
        const rsrc_t rsOut = mk_rsrc(out, 2048u * 2048u);
        const unsigned sog = WB_OG + sof + (unsigned)(mtB * 4 + w) * 256u;
        unsigned grp = (t3 & 1u) * 2u, flip = 0;
#pragma unroll 1
        for (int att = 0; att < TRY_LIMIT; ++att) {
            unsigned cs, st;
            asm volatile(PHASE_C_ASM
                         : "={v230}"(cs), [st] "=&s"(st)
                         : "{v[240:255]}"(ad), "{v[232:235]}"(g4), "{v236}"(tag), [rsb] "s"(rsB), [rso] "s"(rsOut), [sog] "s"(sog), [grp] "s"(grp), [flip] "s"(flip),
                           [t3] "s"(t3), [rid] "s"(rid), [rm] "s"(rm), [sout] "s"((unsigned)(mtB * 128 + 32 * w) * 2048u + (unsigned)hpB * 128u)
                         : "memory", "vcc", "scc", PHASE_C_CLOB);
            if (K::FORCE_REDO && att < 2) st = 2 - att, cs += 1u;
            if (__builtin_expect(st == 0u, 1)) break;
            u32x2 og = ld64c(rsB, ad[4], sog);
            const unsigned miss = (unsigned)__builtin_amdgcn_ballot_w64(og[1] != tag);
            if (st == 1u && ((miss && !(miss & ~(0xffffu << (8u * grp)))) || K::FORCE_REDO)) {
                grp ^= 2u;
                flip ^= 65536u;
                lds_wait(lds0 + C_WO, 4);
                continue;
            }
            int it = 0;
#pragma unroll 1
            for (; it < POLL_LIMIT && !__all(og[1] == tag); ++it) {
                __builtin_amdgcn_s_sleep(K::RETRY_S);
                fence_c();
                og = ld64c(rsB, ad[4], sog);
            }
            if (it == POLL_LIMIT) break;
            if (st == 2u) {
                if (__all(wave_usum(cs - (ad[10] < 32u ? og[0] : 0u)) == 0u)) break;
            }
        }
    }
    asm volatile(".Lff_end2:\n.Lpad_x:\n\ts_cbranch_execz .Lpad_top");
}

extern "C" void kernel_launch(void* const* d_in, const int* in_sizes, int n_in, void* d_out, int out_size,
                              void* d_ws, size_t ws_size, hipStream_t stream) {
    const u16* x = (const u16*)d_in[0];
    const u16* wqkv = (const u16*)d_in[1];
    const u16* wphi = (const u16*)d_in[2];
    const u16* wo = (const u16*)d_in[3];
    const u16* wr = (const u16*)d_in[5];
    const u16* wg = (const u16*)d_in[6];
    const u16* wu = (const u16*)d_in[7];
    const u16* wd = (const u16*)d_in[8];
    const u16* gw = (const u16*)d_in[9];
    fused_fwd<<<256, 512, 0, stream>>>(x, wqkv, wphi, wo, wr, wg, wu, wd, gw, (char*)d_ws, (u16*)d_out);
}
```

```cpp
#define DBG_BID 0
#define DBG_K 0
#define KNOBS KNOB(PRED_C,105);KNOB(D3,150);KNOB(Y_AT,270);
#include <hip/hip_runtime.h>
#include <stdint.h>
#include <type_traits>

typedef __attribute__((ext_vector_type(8))) short bf16x8;
typedef __attribute__((ext_vector_type(4))) short s16x4;
typedef __attribute__((ext_vector_type(4))) float f32x4;
typedef __attribute__((ext_vector_type(2))) float f32x2;
typedef __attribute__((ext_vector_type(32))) float f32x32;
typedef __attribute__((ext_vector_type(2))) unsigned u32x2;
typedef __attribute__((ext_vector_type(4))) unsigned u32x4;
typedef __attribute__((ext_vector_type(8))) unsigned u32x8;
typedef __attribute__((ext_vector_type(16))) unsigned u32x16;
typedef __attribute__((ext_vector_type(32))) unsigned u32x32;
typedef __attribute__((ext_vector_type(2))) __bf16 bf16x2_t;
typedef unsigned short u16;
typedef __amdgpu_buffer_rsrc_t rsrc_t;

#define DI __device__ __forceinline__
#define AS3 __attribute__((address_space(3)))
#define RLX_WG __ATOMIC_RELAXED, __HIP_MEMORY_SCOPE_WORKGROUP

constexpr int AUX_SC1 = 16;

DI float bf2f(u16 v) { return __uint_as_float(((unsigned)v) << 16); }
DI u16 f2bf(float f) { __bf16 b = (__bf16)f; return __builtin_bit_cast(u16, b); }
DI float rbf(float f) { return bf2f(f2bf(f)); }
DI unsigned pack2(float lo, float hi) {
    f32x2 v = {lo, hi};
    bf16x2_t b = __builtin_convertvector(v, bf16x2_t);
    return __builtin_bit_cast(unsigned, b);
}
DI float lo16(unsigned u) { return __uint_as_float(u << 16); }
DI float hi16(unsigned u) { return __uint_as_float(u & 0xffff0000u); }
DI float dot2(unsigned a, unsigned b, float c) {
    return __builtin_amdgcn_fdot2_f32_bf16(__builtin_bit_cast(bf16x2_t, a), __builtin_bit_cast(bf16x2_t, b), c, false);
}
DI s16x4 tr_read(unsigned a) { return __builtin_amdgcn_ds_read_tr16_b64_v4i16((AS3 s16x4*)(size_t)a); }
DI bf16x8 cat8(s16x4 a, s16x4 b) { return __builtin_shufflevector(a, b, 0, 1, 2, 3, 4, 5, 6, 7); }
DI bf16x8 lds_rd128(unsigned a) { return *(AS3 const bf16x8*)(size_t)a; }
DI void lds_wr128(unsigned a, bf16x8 v) { *(AS3 bf16x8*)(size_t)a = v; }
DI f32x4 mfma16(bf16x8 a, bf16x8 b, f32x4 c) { return __builtin_amdgcn_mfma_f32_16x16x32_bf16(a, b, c, 0, 0, 0); }
DI bf16x8 pack44(f32x4 a, f32x4 b) {
    u32x4 p = {pack2(a[0], a[1]), pack2(a[2], a[3]), pack2(b[0], b[1]), pack2(b[2], b[3])};
    return __builtin_bit_cast(bf16x8, p);
}
#define FU(x) __float_as_uint(x)
#define UF(x) __uint_as_float(x)
DI float x32_sum(float v) { auto r = __builtin_amdgcn_permlane32_swap(FU(v), FU(v), false, false); return UF(r[0]) + UF(r[1]); }
DI float x16_sum(float v) { auto r = __builtin_amdgcn_permlane16_swap(FU(v), FU(v), false, false); return UF(r[0]) + UF(r[1]); }
DI float xg_sum(float v) { return x16_sum(x32_sum(v)); }
template <int CTRL> DI float dpp_add(float v) { return v + UF(__builtin_amdgcn_update_dpp(0, (int)FU(v), CTRL, 0xf, 0xf, false)); }
DI float row16_sum(float v) { return dpp_add<0x121>(dpp_add<0x122>(dpp_add<0x124>(dpp_add<0x128>(v)))); }
template <int CTRL> DI unsigned dpp_umax(unsigned v) { return max(v, (unsigned)__builtin_amdgcn_update_dpp(0, (int)v, CTRL, 0xf, 0xf, false)); }
DI unsigned wave_umax(unsigned v) {
    auto r = __builtin_amdgcn_permlane32_swap(v, v, false, false);
    v = max(r[0], r[1]);
    auto q = __builtin_amdgcn_permlane16_swap(v, v, false, false);
    v = max(q[0], q[1]);
    return dpp_umax<0x121>(dpp_umax<0x122>(dpp_umax<0x124>(dpp_umax<0x128>(v))));
}
DI unsigned add4(u32x4 v, unsigned s) { return (v[0] + v[1] + v[2]) + (v[3] + s); }
template <int CTRL> DI unsigned dpp_uadd(unsigned v) { return v + (unsigned)__builtin_amdgcn_update_dpp(0, (int)v, CTRL, 0xf, 0xf, false); }
DI unsigned row16_usum(unsigned v) { return dpp_uadd<0x121>(dpp_uadd<0x122>(dpp_uadd<0x124>(dpp_uadd<0x128>(v)))); }
DI rsrc_t mk_rsrc(const void* p, unsigned bytes) { return __builtin_amdgcn_make_buffer_rsrc((void*)p, (short)0, (int)bytes, 0x00020000); }
DI u32x4 ld128c(rsrc_t rs, unsigned vo, unsigned so) { return __builtin_amdgcn_raw_buffer_load_b128(rs, vo, so, AUX_SC1); }
DI u32x2 ld64c(rsrc_t rs, unsigned vo, unsigned so) { return __builtin_amdgcn_raw_buffer_load_b64(rs, vo, so, AUX_SC1); }
DI unsigned ld32c(rsrc_t rs, unsigned vo, unsigned so) { return __builtin_amdgcn_raw_buffer_load_b32(rs, vo, so, AUX_SC1); }
DI void st128c(u32x4 v, rsrc_t rs, unsigned vo, unsigned so) {
    __builtin_amdgcn_raw_buffer_store_b128(v, rs, vo, so, AUX_SC1);
    asm volatile("s_nop 1" :: "v"(v) : "memory");
}
DI void st64c(u32x2 v, rsrc_t rs, unsigned vo, unsigned so) { __builtin_amdgcn_raw_buffer_store_b64(v, rs, vo, so, AUX_SC1); }
DI void st32c(unsigned v, rsrc_t rs, unsigned vo, unsigned so) { __builtin_amdgcn_raw_buffer_store_b32(v, rs, vo, so, AUX_SC1); }

DI void dma16(rsrc_t rs, unsigned voff, unsigned soff, unsigned lds_addr) {
    asm volatile("s_mov_b32 m0, %0\n\ts_nop 0\n\tbuffer_load_dwordx4 %1, %2, %3 offen lds"
                 :: "s"(lds_addr), "v"(voff), "s"(rs), "s"(soff) : "memory");
}
template <int PC> DI void dma4(rsrc_t rs, const unsigned* v, unsigned soff, unsigned lds_addr) {
#define D4(p) asm volatile("s_mov_b32 m0, %0\n\ts_nop 0\n\tbuffer_load_dwordx4 %1, %5, %6 offen lds\n\t" p "buffer_load_dwordx4 %2, %5, %6 offen offset:1024 lds\n\t" p \
                 "buffer_load_dwordx4 %3, %5, %6 offen offset:2048 lds\n\t" p "buffer_load_dwordx4 %4, %5, %6 offen offset:3072 lds" \
                 :: "s"(lds_addr), "v"(v[0]), "v"(v[1]), "v"(v[2]), "v"(v[3]), "s"(rs), "s"(soff), "i"(PC ? PC - 1 : 0) : "memory")
    if (PC) D4("s_nop %7\n\t"); else D4("");
}
template <int N> DI void wait_vm() { asm volatile("s_waitcnt vmcnt(%0)" :: "i"(N) : "memory"); }
DI void wait_lgkm0() { asm volatile("" ::: "memory"); __builtin_amdgcn_s_waitcnt(0xC07F); asm volatile("" ::: "memory"); }
DI void bar() { asm volatile("" ::: "memory"); __builtin_amdgcn_s_barrier(); asm volatile("" ::: "memory"); }
DI void fence_c() { asm volatile("" ::: "memory"); }
DI int fresh_lane() {
    int v;
    asm volatile("v_mbcnt_lo_u32_b32 %0, -1, 0\n\tv_mbcnt_hi_u32_b32 %0, -1, %0" : "=v"(v));
    return v;
}
#define LANE_VARS const int l = fresh_lane(), c = l & 15, g = l >> 4, i4 = (l & 15) >> 2, i3 = l & 3; (void)c; (void)g; (void)i4; (void)i3
#define PSW ((unsigned)(4 * g + i4) * 128u + (unsigned)((i4 ^ (g & 1)) << 5))
#define WPO ((unsigned)(4 * g + i4) * 64u + (unsigned)((g & 1) << 5) + (unsigned)(8 * i3))
#define WSW ((unsigned)(8 * g + i4) * 128u + (unsigned)((i4 ^ (g & 1)) << 5) + (unsigned)(8 * i3))

#define WO_VOFF ((unsigned)(l >> 3) * 2048u + (unsigned)(((((((l & 7) >> 1) ^ ((l >> 3) & 3) ^ (l >> 5)) << 1) | (l & 1))) << 4))

DI void lds_wait(unsigned addr, unsigned target) {
#pragma unroll 1
    for (int it = 0; it < (1 << 22); ++it) {
        unsigned v = __hip_atomic_load((AS3 unsigned*)(size_t)addr, RLX_WG);
        if (__builtin_amdgcn_readfirstlane(v) >= target) break;
        __builtin_amdgcn_s_sleep(1);
    }
    fence_c();
}
DI unsigned now32() { unsigned long long t; asm volatile("s_memrealtime %0\n\ts_waitcnt lgkmcnt(0)" : "=s"(t) :: "memory"); return (unsigned)t; }
DI void wait_until(unsigned t) {
#pragma unroll 1
    for (int it = 0; it < 256; ++it) {
        const int rem = (int)(t - now32());
        if (rem <= 4) break;
        if (rem > 12) __builtin_amdgcn_s_sleep(2);
    }
    fence_c();
}
DI unsigned wave_usum(unsigned m) {
    auto p = __builtin_amdgcn_permlane32_swap(m, m, false, false);
    m = p[0] + p[1];
    auto q = __builtin_amdgcn_permlane16_swap(m, m, false, false);
    return row16_usum(q[0] + q[1]);
}
DI void post(unsigned addr, unsigned v) {
    fence_c();
    *(AS3 volatile unsigned*)(size_t)addr = v;
    fence_c();
}
DI void wait4(unsigned addr, unsigned target) {
#pragma unroll 1
    for (int it = 0; it < (1 << 22); ++it) {
        const u32x4 v = *(AS3 volatile const u32x4*)(size_t)addr;
        if (__builtin_amdgcn_readfirstlane(min(min(v[0], v[1]), min(v[2], v[3]))) >= target) break;
        __builtin_amdgcn_s_sleep(1);
    }
    fence_c();
}
constexpr int POLL_LIMIT = 1 << 16, TRY_LIMIT = 1 << 12;

#define KNOB(n, v) static constexpr int n = v
struct Dflt {
    KNOB(KT0, 0x76534210); KNOB(KT1, 0x76453201); KNOB(KT2, 0x75643120); KNOB(KT3, 0x67542310);
    KNOB(BGT, 0x76227622); KNOB(BUT, 0x77327732); KNOB(BRT, 0x77777777); KNOB(BST, 0x33333333); KNOB(PBT, (int)0xaaaaaaaa);
    KNOB(SLG, 0); KNOB(SLS, 20); KNOB(X0, 130); KNOB(X1, 2); KNOB(X2, 2); KNOB(TOPV, 1);
    KNOB(PRED_K, 16); KNOB(PRED_C, 90);
    KNOB(D1, 40); KNOB(D2, 120); KNOB(D3, 140); KNOB(FG_AT, -130); KNOB(Y_AT, 260); KNOB(QA, 30); KNOB(QB, 40);
    KNOB(RETRY_S, 8); KNOB(PT_S, 12); KNOB(KF_S, 10); KNOB(WO_D, 24); KNOB(FD, 12);
    KNOB(PT_B, 10); KNOB(XE, 20);
    KNOB(RE1, 1); KNOB(L2_S, 12);
    KNOB(FORCE_REDO, 0);
    KNOB(ND, 6); KNOB(XAH, 0); KNOB(QG, 2); KNOB(HP, 2); KNOB(Q5E, 1); KNOB(PADS, 1); KNOB(WOSK, 0); KNOB(WOE, 0); KNOB(TNT, 0); KNOB(TWB, 13); KNOB(TMW, 0); KNOB(DWP, 1); KNOB(WOG, 0); KNOB(PK13, 3); KNOB(DS1, 2); KNOB(DS2, 9); KNOB(DS3, 16); KNOB(PACE, 0);
};
struct K : Dflt { KNOBS };

constexpr unsigned WS_EPOCH = 0;
constexpr unsigned WS_RG = 4096;
constexpr unsigned WS_PT = 6144;
constexpr unsigned WS_G = 8192;
constexpr unsigned WS_YF = 24576;
constexpr unsigned WS_FG = 65536;
constexpr unsigned WS_Y = 1u << 19;
constexpr unsigned WS_BYTES = 1u << 20;
constexpr unsigned WB_KVP = 0;
constexpr unsigned WB_SM = 1u << 20;
constexpr unsigned WB_PRB = 3u << 19;
constexpr unsigned WB_OG = 7u << 18;
constexpr unsigned WB_OF = 8u << 20;
constexpr unsigned WB_BYTES = 13u << 20;
DI char* bulk_base(char* ws) { return (char*)(((size_t)ws + (3u << 20)) & ~(size_t)((2u << 20) - 1)); }

constexpr unsigned SLOT = 40960;
constexpr unsigned L_PHI = 98304;
constexpr unsigned L_VF = 131072;
constexpr unsigned L_MISC = 147456;
constexpr unsigned L_WP = L_MISC, L_KVF = L_MISC + 4096, L_SM = L_MISC + 12288;
constexpr unsigned C_ST = L_SM + 0  , C_R = L_SM + 8  , C_P = L_SM + 16  ;
constexpr unsigned C_WO = L_SM + 56, C_YRDY = L_SM + 68, C_IDX = L_SM + 76, C_TM = L_SM + 80, C_XE = L_SM + 84, C_Y1 = L_SM + 88;
constexpr unsigned P_E14 = 1, P_E15 = 2, P_PHA = 3, P_PHB = 4, P_KVD = 5, P_PUB = 6, P_Q1 = 7  , P_QRD = 11, P_O = 12;
constexpr unsigned C_Q = L_SM + 92, C_HOLD = L_SM + 96;
constexpr unsigned XS0[3] = {16384, 98304, L_VF}, XS1[3] = {57344, 122880, L_VF + 8192};
constexpr unsigned QX[3] = {40960, 81920, 0}, QW[6] = {L_KVF, 65536, 73728, 24576, 32768, L_PHI};
constexpr unsigned R_L1I = 1, R_L1 = 2, R_L2I = 3, R_KVF = 4;
constexpr unsigned L_XE = L_VF;
constexpr unsigned L_KEY = L_SM + 256, L_TAG = L_SM + 288, L_IDX = L_SM + 296, L_TM = L_SM + 304  , L_PLC = L_SM + 320  , L_PRB = L_SM + 336  , L_ZF = L_SM + 512, L_Y = L_SM + 1536;

constexpr unsigned long long L2E2 = 0x3fb8aa3b3fb8aa3bull;

#define PHASE_AB_ASM \
    "s_branch .Lpad_ab0\n\t" \
    ".Lab_s1:\n\t" \
    "s_mov_b32 s89, 0x400000\n\t" \
    ".Lab_s1a:\n\t" \
    "s_waitcnt lgkmcnt(0)\n\t" \
    "ds_read_b32 v237, v249\n\t" \
    "ds_read_b32 v238, v249 offset:4\n\t" \
    "s_waitcnt lgkmcnt(0)\n\t" \
    "v_min_u32 v237, v237, v238\n\t" \
    "s_nop 1\n\t" \
    "v_readfirstlane_b32 s85, v237\n\t" \
    "s_sub_u32 s89, s89, 1\n\t" \
    "s_cbranch_scc1 .Lab_s1b\n\t" \
    "s_cmp_lt_u32 s85, 1\n\t" \
    "s_cbranch_scc1 .Lab_s1a\n\t" \
    ".Lab_s1b:\n\t" \
    "s_nop 7\n\t" \
    "s_waitcnt lgkmcnt(0)\n\t" \
    "s_branch .Lab_s1r\n\t" \
    ".Lab_s2:\n\t" \
    "s_mov_b32 s89, 0x400000\n\t" \
    ".Lab_s2a:\n\t" \
    "s_waitcnt lgkmcnt(0)\n\t" \
    "ds_read_b32 v237, v249 offset:16\n\t" \
    "ds_read_b32 v238, v249 offset:20\n\t" \
    "ds_read_b32 v255, v249 offset:24\n\t" \
    "s_waitcnt lgkmcnt(0)\n\t" \
    "v_min3_u32 v237, v237, v238, v255\n\t" \
    "ds_read_b32 v238, v249 offset:28\n\t" \
    "s_waitcnt lgkmcnt(0)\n\t" \
    "v_min_u32 v237, v237, v238\n\t" \
    "s_nop 1\n\t" \
    "v_readfirstlane_b32 s85, v237\n\t" \
    "s_sub_u32 s89, s89, 1\n\t" \
    "s_cbranch_scc1 .Lab_s2b\n\t" \
    "s_cmp_lt_u32 s85, 2\n\t" \
    "s_cbranch_scc1 .Lab_s2a\n\t" \
    ".Lab_s2b:\n\t" \
    "s_nop 7\n\t" \
    "s_waitcnt lgkmcnt(0)\n\t" \
    "s_branch .Lab_s2r\n\t" \
    ".Lab_s3:\n\t" \
    "s_mov_b32 s89, 0x400000\n\t" \
    ".Lab_s3a:\n\t" \
    "s_waitcnt lgkmcnt(0)\n\t" \
    "ds_read_b32 v237, v250\n\t" \
    "s_waitcnt lgkmcnt(0)\n\t" \
    "s_nop 1\n\t" \
    "v_readfirstlane_b32 s85, v237\n\t" \
    "s_sub_u32 s89, s89, 1\n\t" \
    "s_cbranch_scc1 .Lab_s3b\n\t" \
    "s_cmp_lt_u32 s85, 3\n\t" \
    "s_cbranch_scc1 .Lab_s3a\n\t" \
    ".Lab_s3b:\n\t" \
    "s_nop 7\n\t" \
    "ds_read_b64_tr_b16 v[104:105], v245\n\t" \
    "ds_read_b64_tr_b16 v[106:107], v245 offset:2048\n\t" \
    "ds_read_b64_tr_b16 v[108:109], v255\n\t" \
    "ds_read_b64_tr_b16 v[110:111], v255 offset:2048\n\t" \
    "ds_read_b128 v[80:83], v242\n\t" \
    "ds_read_b128 v[84:87], v242 offset:1024\n\t" \
    "ds_read_b128 v[88:91], v242 offset:4096\n\t" \
    "ds_read_b128 v[92:95], v242 offset:5120\n\t" \
    "s_waitcnt lgkmcnt(0)\n\t" \
    "s_branch .Lab_s3r\n\t" \
    ".Lab_s4:\n\t" \
    "s_mov_b32 s89, 0x400000\n\t" \
    ".Lab_s4a:\n\t" \
    "s_waitcnt lgkmcnt(0)\n\t" \
    "ds_read_b32 v237, v250\n\t" \
    "s_waitcnt lgkmcnt(0)\n\t" \
    "s_nop 1\n\t" \
    "v_readfirstlane_b32 s85, v237\n\t" \
    "s_sub_u32 s89, s89, 1\n\t" \
    "s_cbranch_scc1 .Lab_s4b\n\t" \
    "s_cmp_lt_u32 s85, 4\n\t" \
    "s_cbranch_scc1 .Lab_s4a\n\t" \
    ".Lab_s4b:\n\t" \
    "s_nop 7\n\t" \
    "ds_read_b64_tr_b16 v[8:9], v245 offset:4096\n\t" \
    "ds_read_b64_tr_b16 v[10:11], v245 offset:6144\n\t" \
    "ds_read_b64_tr_b16 v[12:13], v255 offset:4096\n\t" \
    "ds_read_b64_tr_b16 v[14:15], v255 offset:6144\n\t" \
    "v_mov_b32 v237, 5\n\t" \
    "s_waitcnt lgkmcnt(0)\n\t" \
    "s_branch .Lab_s4r\n\t" \
    ".Lab_s5:\n\t" \
    "s_mov_b32 s89, 0x400000\n\t" \
    ".Lab_s5a:\n\t" \
    "s_waitcnt lgkmcnt(0)\n\t" \
    "ds_read_b32 v237, v249 offset:92\n\t" \
    "s_waitcnt lgkmcnt(0)\n\t" \
    "s_nop 1\n\t" \
    "v_readfirstlane_b32 s85, v237\n\t" \
    "s_sub_u32 s89, s89, 1\n\t" \
    "s_cbranch_scc1 .Lab_s5b\n\t" \
    "s_cmp_lt_u32 s85, 1\n\t" \
    "s_cbranch_scc1 .Lab_s5a\n\t" \
    ".Lab_s5b:\n\t" \
    "s_nop 7\n\t" \
    "ds_read_b64_tr_b16 v[80:81], v253\n\t" \
    "ds_read_b64_tr_b16 v[82:83], v253 offset:512\n\t" \
    "ds_read_b64_tr_b16 v[84:85], v251\n\t" \
    "ds_read_b64_tr_b16 v[86:87], v251 offset:512\n\t" \
    "ds_read_b64_tr_b16 v[88:89], v253 offset:4096\n\t" \
    "ds_read_b64_tr_b16 v[90:91], v253 offset:4608\n\t" \
    "ds_read_b64_tr_b16 v[92:93], v251 offset:4096\n\t" \
    "ds_read_b64_tr_b16 v[94:95], v251 offset:4608\n\t" \
    "s_waitcnt lgkmcnt(0)\n\t" \
    "s_branch .Lab_s5r\n\t" \
    ".Lab_s6:\n\t" \
    "s_mov_b32 s89, 0x400000\n\t" \
    ".Lab_s6a:\n\t" \
    "s_waitcnt lgkmcnt(0)\n\t" \
    "ds_read_b32 v237, v249 offset:92\n\t" \
    "s_waitcnt lgkmcnt(0)\n\t" \
    "s_nop 1\n\t" \
    "v_readfirstlane_b32 s85, v237\n\t" \
    "s_sub_u32 s89, s89, 1\n\t" \
    "s_cbranch_scc1 .Lab_s6b\n\t" \
    "s_cmp_lt_u32 s85, 2\n\t" \
    "s_cbranch_scc1 .Lab_s6a\n\t" \
    ".Lab_s6b:\n\t" \
    "s_nop 7\n\t" \
    "v_mov_b32 v237, 10\n\t" \
    "s_waitcnt lgkmcnt(0)\n\t" \
    "s_branch .Lab_s6r\n\t" \
    ".Lab_s7:\n\t" \
    "s_mov_b32 s89, 0x400000\n\t" \
    ".Lab_s7a:\n\t" \
    "s_sleep 1\n\t" \
    "ds_read_b32 v237, v249 offset:8\n\t" \
    "s_waitcnt lgkmcnt(0)\n\t" \
    "v_readfirstlane_b32 s85, v237\n\t" \
    "s_sub_u32 s89, s89, 1\n\t" \
    "s_cbranch_scc1 .Lab_s7r\n\t" \
    "s_cmp_lt_u32 s85, 3\n\t" \
    "s_cbranch_scc1 .Lab_s7a\n\t" \
    "s_sleep 5\n\t" \
    "s_branch .Lab_s7r\n\t" \
    ".Lab_sa:\n\t" \
    "ds_read_b32 v36, v249 offset:8\n\t" \
    "s_waitcnt lgkmcnt(0)\n\t" \
    "v_readfirstlane_b32 s80, v36\n\t" \
    "s_sub_u32 s89, s89, 1\n\t" \
    "s_cbranch_scc1 .Lab_sar\n\t" \
    "s_cmp_lt_u32 s80, s82\n\t" \
    "s_cbranch_scc1 .Lab_sa\n\t" \
    "s_cmp_eq_u32 s80, s82\n\t" \
    "s_cbranch_scc1 .Lab_sar\n\t" \
    "s_waitcnt vmcnt(0)\n\t" \
    "s_branch .Lab_k\n\t" \
    ".Lab_s8:\n\t" \
    "s_mov_b32 s89, 0x10000\n\t" \
    "v_lshlrev_b32 v35, 3, v34\n\t" \
    ".Lab_s8a:\n\t" \
    "s_sleep 8\n\t" \
    "buffer_load_dwordx2 v[232:233], v35, %[rsw], %[sg] offen sc1\n\t" \
    "buffer_load_dwordx2 v[234:235], v35, %[rsw], %[sg] offen offset:128 sc1\n\t" \
    "s_sub_u32 s89, s89, 1\n\t" \
    "s_waitcnt vmcnt(0)\n\t" \
    "s_cbranch_scc1 .Lab_s8r\n\t" \
    "v_cmp_ne_u32 vcc, v236, v233\n\t" \
    "v_cmp_ne_u32_e64 s[92:93], v236, v235\n\t" \
    "s_nop 0\n\t" \
    "s_or_b64 vcc, vcc, s[92:93]\n\t" \
    "s_cbranch_scc1 .Lab_s8a\n\t" \
    "s_branch .Lab_s8r\n\t" \
    ".Lab_s9:\n\t" \
    "s_mov_b32 s89, 0x400000\n\t" \
    ".Lab_s9a:\n\t" \
    "s_waitcnt lgkmcnt(0)\n\t" \
    "ds_read_b32 v237, v249 offset:56\n\t" \
    "s_waitcnt lgkmcnt(0)\n\t" \
    "s_nop 1\n\t" \
    "v_readfirstlane_b32 s85, v237\n\t" \
    "s_sub_u32 s89, s89, 1\n\t" \
    "s_cbranch_scc1 .Lab_s9b\n\t" \
    "s_cmp_lt_u32 s85, 2\n\t" \
    "s_cbranch_scc1 .Lab_s9a\n\t" \
    ".Lab_s9b:\n\t" \
    "s_nop 7\n\t" \
    "s_waitcnt lgkmcnt(0)\n\t" \
    "s_branch .Lab_s9r\n\t" \
    ".p2align 12\n\t" \
    ".Lpad_ab0:\n\t" \
    "s_cbranch_execz .Lpad_top\n\t" \
    "v_mov_b32 v32, 0\n\t" \
    "v_mov_b32 v33, 0\n\t" \
    "v_mov_b32 v34, 0\n\t" \
    "v_mov_b32 v35, 0\n\t" \
    "v_mov_b32 v36, 0\n\t" \
    "v_mov_b32 v37, 0\n\t" \
    "v_mov_b32 v38, 0\n\t" \
    "v_mov_b32 v39, 0\n\t" \
    "v_mov_b32 v40, 0\n\t" \
    "v_mov_b32 v41, 0\n\t" \
    "v_mov_b32 v42, 0\n\t" \
    "v_mov_b32 v43, 0\n\t" \
    "v_mov_b32 v44, 0\n\t" \
    "v_mov_b32 v45, 0\n\t" \
    "v_mov_b32 v46, 0\n\t" \
    "v_mov_b32 v47, 0\n\t" \
    "v_mov_b32 v48, 0\n\t" \
    "v_mov_b32 v49, 0\n\t" \
    "v_mov_b32 v50, 0\n\t" \
    "v_mov_b32 v51, 0\n\t" \
    "v_mov_b32 v52, 0\n\t" \
    "v_mov_b32 v53, 0\n\t" \
    "v_mov_b32 v54, 0\n\t" \
    "v_mov_b32 v55, 0\n\t" \
    "v_mov_b32 v56, 0\n\t" \
    "v_mov_b32 v57, 0\n\t" \
    "v_mov_b32 v58, 0\n\t" \
    "v_mov_b32 v59, 0\n\t" \
    "v_mov_b32 v60, 0\n\t" \
    "v_mov_b32 v61, 0\n\t" \
    "v_mov_b32 v62, 0\n\t" \
    "v_mov_b32 v63, 0\n\t" \
    "v_mov_b32 v80, 0\n\t" \
    "v_mov_b32 v81, 0\n\t" \
    "v_mov_b32 v82, 0\n\t" \
    "v_mov_b32 v83, 0\n\t" \
    "v_mov_b32 v84, 0\n\t" \
    "v_mov_b32 v85, 0\n\t" \
    "v_mov_b32 v86, 0\n\t" \
    "v_mov_b32 v87, 0\n\t" \
    "s_waitcnt lgkmcnt(0)\n\t" \
    "s_memtime s[82:83]\n\t" \
    "s_load_dword s80, %[prb], 0\n\t" \
    "v_mov_b32 v88, 0\n\t" \
    "v_mov_b32 v89, 0\n\t" \
    "v_mov_b32 v90, 0\n\t" \
    "v_mov_b32 v91, 0\n\t" \
    "v_mov_b32 v92, 0\n\t" \
    "v_mov_b32 v93, 0\n\t" \
    "v_mov_b32 v94, 0\n\t" \
    "v_mov_b32 v95, 0\n\t" \
    "v_mov_b32 v96, 0\n\t" \
    "v_mov_b32 v97, 0\n\t" \
    "v_mov_b32 v98, 0\n\t" \
    "v_mov_b32 v99, 0\n\t" \
    "v_mov_b32 v100, 0\n\t" \
    "v_mov_b32 v101, 0\n\t" \
    "v_mov_b32 v102, 0\n\t" \
    "v_mov_b32 v103, 0\n\t" \
    "v_mov_b32 v104, 0\n\t" \
    "v_mov_b32 v105, 0\n\t" \
    "v_mov_b32 v106, 0\n\t" \
    "v_mov_b32 v107, 0\n\t" \
    "v_mov_b32 v108, 0\n\t" \
    "v_mov_b32 v109, 0\n\t" \
    "v_mov_b32 v110, 0\n\t" \
    "v_mov_b32 v111, 0\n\t" \
    "v_mov_b32 v192, 0\n\t" \
    "v_mov_b32 v193, 0\n\t" \
    "v_mov_b32 v194, 0\n\t" \
    "v_mov_b32 v195, 0\n\t" \
    "v_mov_b32 v196, 0\n\t" \
    "v_mov_b32 v197, 0\n\t" \
    "v_mov_b32 v198, 0\n\t" \
    "v_mov_b32 v199, 0\n\t" \
    "v_mov_b32 v200, 0\n\t" \
    "v_mov_b32 v201, 0\n\t" \
    "v_mov_b32 v202, 0\n\t" \
    "v_mov_b32 v203, 0\n\t" \
    "v_mov_b32 v204, 0\n\t" \
    "v_mov_b32 v205, 0\n\t" \
    "v_mov_b32 v206, 0\n\t" \
    "v_mov_b32 v207, 0\n\t" \
    "v_mov_b32 v208, 0\n\t" \
    "v_mov_b32 v209, 0\n\t" \
    "v_mov_b32 v210, 0\n\t" \
    "v_mov_b32 v211, 0\n\t" \
    "v_mov_b32 v212, 0\n\t" \
    "v_mov_b32 v213, 0\n\t" \
    "v_mov_b32 v214, 0\n\t" \
    "v_mov_b32 v215, 0\n\t" \
    "v_mov_b32 v216, 0\n\t" \
    "v_mov_b32 v217, 0\n\t" \
    "v_mov_b32 v218, 0\n\t" \
    "v_mov_b32 v219, 0\n\t" \
    "v_mov_b32 v220, 0\n\t" \
    "v_mov_b32 v221, 0\n\t" \
    "v_mov_b32 v222, 0\n\t" \
    "v_mov_b32 v223, 0\n\t" \
    "s_waitcnt lgkmcnt(0)\n\t" \
    "s_memtime s[84:85]\n\t" \
    "s_waitcnt lgkmcnt(0)\n\t" \
    "s_sub_u32 s82, s84, s82\n\t" \
    "v_mov_b32 v237, s82\n\t" \
    "ds_write_b32 v248, v237 offset:320\n\t" \
    "v_mov_b32 v232, 0x3f803f80\n\t" \
    "v_mov_b32 v233, 0x3f803f80\n\t" \
    "v_mov_b32 v234, 0x3f803f80\n\t" \
    "v_mov_b32 v235, 0x3f803f80\n\t" \
    "v_mov_b32 v251, v240\n\t" \
    "v_xor_b32 v252, 64, v240\n\t" \
    "v_mov_b32 v253, v241\n\t" \
    "v_xor_b32 v254, 32, v241\n\t" \
    "s_mov_b32 s80, 0\n\t" \
    "s_mov_b32 s81, 10\n\t" \
    "s_mov_b32 s90, 0x3fb8aa3b\n\t" \
    "s_mov_b32 s91, 0x3fb8aa3b\n\t" \
    "v_cmp_eq_u32_e64 s[92:93], 2, v250\n\t" \
    "v_cmp_gt_u32_e64 s[94:95], 2, v250\n\t" \
    "v_cmp_eq_u32 vcc, 0, v250\n\t" \
    "v_xor_b32 v250, 4, v248\n\t" \
    "s_mov_b32 s84, 0x140a0014\n\t" \
    "s_lshl_b32 s83, %[xs0], 4\n\t" \
    "s_lshl_b32 s85, %[xs1], 12\n\t" \
    "s_or_b32 s83, s83, s85\n\t" \
    "s_or_b32 s83, s83, 0x14\n\t" \
    "s_waitcnt lgkmcnt(0)\n\t" \
    "s_barrier\n\t" \
    "ds_read_b128 v[128:131], v251\n\t" \
    "ds_read_b128 v[132:135], v251 offset:2048\n\t" \
    "ds_read_b128 v[136:139], v251 offset:4096\n\t" \
    "ds_read_b128 v[140:143], v251 offset:6144\n\t" \
    "ds_read_b64_tr_b16 v[0:1], v253 offset:8192\n\t" \
    "ds_read_b64_tr_b16 v[2:3], v253 offset:8704\n\t" \
    "ds_read_b64_tr_b16 v[4:5], v254 offset:8192\n\t" \
    "ds_read_b64_tr_b16 v[6:7], v254 offset:8704\n\t" \
    "ds_read_b64_tr_b16 v[8:9], v253 offset:16384\n\t" \
    "ds_read_b64_tr_b16 v[10:11], v253 offset:16896\n\t" \
    "ds_read_b64_tr_b16 v[12:13], v254 offset:16384\n\t" \
    "ds_read_b64_tr_b16 v[14:15], v254 offset:16896\n\t" \
    "ds_read_b64_tr_b16 v[16:17], v253\n\t" \
    "ds_read_b64_tr_b16 v[18:19], v253 offset:512\n\t" \
    "ds_read_b64_tr_b16 v[20:21], v254\n\t" \
    "ds_read_b64_tr_b16 v[22:23], v254 offset:512\n\t" \
    ".Lab_l1:\n\t" \
    "s_waitcnt lgkmcnt(0)\n\t" \
    "v_mfma_f32_16x16x32_bf16 v[32:35], v[0:3], v[128:131], v[32:35]\n\t" \
    "ds_read_b128 v[144:147], v252\n\t" \
    "v_mfma_f32_16x16x32_bf16 v[40:43], v[0:3], v[132:135], v[40:43]\n\t" \
    "ds_read_b128 v[148:151], v252 offset:2048\n\t" \
    "v_mfma_f32_16x16x32_bf16 v[48:51], v[0:3], v[136:139], v[48:51]\n\t" \
    "ds_read_b128 v[152:155], v252 offset:4096\n\t" \
    "v_mfma_f32_16x16x32_bf16 v[56:59], v[0:3], v[140:143], v[56:59]\n\t" \
    "ds_read_b128 v[156:159], v252 offset:6144\n\t" \
    "v_mfma_f32_16x16x32_bf16 v[36:39], v[4:7], v[128:131], v[36:39]\n\t" \
    "ds_read_b64_tr_b16 v[112:113], v253 offset:12288\n\t" \
    "v_mfma_f32_16x16x32_bf16 v[44:47], v[4:7], v[132:135], v[44:47]\n\t" \
    "ds_read_b64_tr_b16 v[114:115], v253 offset:12800\n\t" \
    "v_mfma_f32_16x16x32_bf16 v[52:55], v[4:7], v[136:139], v[52:55]\n\t" \
    "ds_read_b64_tr_b16 v[116:117], v254 offset:12288\n\t" \
    "v_mfma_f32_16x16x32_bf16 v[60:63], v[4:7], v[140:143], v[60:63]\n\t" \
    "ds_read_b64_tr_b16 v[118:119], v254 offset:12800\n\t" \
    "v_mfma_f32_16x16x32_bf16 v[80:83], v[128:131], v[8:11], v[80:83]\n\t" \
    "ds_read_b64_tr_b16 v[120:121], v253 offset:20480\n\t" \
    "v_mfma_f32_16x16x32_bf16 v[88:91], v[132:135], v[8:11], v[88:91]\n\t" \
    "ds_read_b64_tr_b16 v[122:123], v253 offset:20992\n\t" \
    "v_mfma_f32_16x16x32_bf16 v[96:99], v[136:139], v[8:11], v[96:99]\n\t" \
    "ds_read_b64_tr_b16 v[124:125], v254 offset:20480\n\t" \
    "v_mfma_f32_16x16x32_bf16 v[104:107], v[140:143], v[8:11], v[104:107]\n\t" \
    "ds_read_b64_tr_b16 v[126:127], v254 offset:20992\n\t" \
    "v_mfma_f32_16x16x32_bf16 v[84:87], v[128:131], v[12:15], v[84:87]\n\t" \
    "ds_read_b64_tr_b16 v[24:25], v253 offset:4096\n\t" \
    "v_mfma_f32_16x16x32_bf16 v[92:95], v[132:135], v[12:15], v[92:95]\n\t" \
    "ds_read_b64_tr_b16 v[26:27], v253 offset:4608\n\t" \
    "v_mfma_f32_16x16x32_bf16 v[100:103], v[136:139], v[12:15], v[100:103]\n\t" \
    "ds_read_b64_tr_b16 v[28:29], v254 offset:4096\n\t" \
    "v_mfma_f32_16x16x32_bf16 v[108:111], v[140:143], v[12:15], v[108:111]\n\t" \
    "ds_read_b64_tr_b16 v[30:31], v254 offset:4608\n\t" \
    "v_mfma_f32_16x16x32_bf16 v[192:195], v[16:19], v[128:131], v[192:195]\n\t" \
    "s_add_u32 s85, s80, 40960\n\t" \
    "s_cmp_lg_u32 s80, 81920\n\t" \
    "v_mfma_f32_16x16x32_bf16 v[200:203], v[16:19], v[132:135], v[200:203]\n\t" \
    "s_cselect_b32 s80, s85, 0\n\t" \
    "v_mfma_f32_16x16x32_bf16 v[208:211], v[16:19], v[136:139], v[208:211]\n\t" \
    "v_add_u32 v251, s80, v240\n\t" \
    "v_mfma_f32_16x16x32_bf16 v[216:219], v[16:19], v[140:143], v[216:219]\n\t" \
    "v_add_u32 v253, s80, v241\n\t" \
    "v_mfma_f32_16x16x32_bf16 v[196:199], v[20:23], v[128:131], v[196:199]\n\t" \
    "v_xor_b32 v254, 32, v253\n\t" \
    "v_mfma_f32_16x16x32_bf16 v[204:207], v[20:23], v[132:135], v[204:207]\n\t" \
    "v_mfma_f32_16x16x32_bf16 v[212:215], v[20:23], v[136:139], v[212:215]\n\t" \
    "v_mfma_f32_16x16x32_bf16 v[220:223], v[20:23], v[140:143], v[220:223]\n\t" \
    "s_waitcnt lgkmcnt(0)\n\t" \
    "s_barrier\n\t" \
    "v_mfma_f32_16x16x32_bf16 v[32:35], v[112:115], v[144:147], v[32:35]\n\t" \
    "ds_read_b128 v[128:131], v251\n\t" \
    "v_mfma_f32_16x16x32_bf16 v[40:43], v[112:115], v[148:151], v[40:43]\n\t" \
    "ds_read_b128 v[132:135], v251 offset:2048\n\t" \
    "v_mfma_f32_16x16x32_bf16 v[48:51], v[112:115], v[152:155], v[48:51]\n\t" \
    "ds_read_b128 v[136:139], v251 offset:4096\n\t" \
    "v_mfma_f32_16x16x32_bf16 v[56:59], v[112:115], v[156:159], v[56:59]\n\t" \
    "ds_read_b128 v[140:143], v251 offset:6144\n\t" \
    "v_mfma_f32_16x16x32_bf16 v[36:39], v[116:119], v[144:147], v[36:39]\n\t" \
    "ds_read_b64_tr_b16 v[0:1], v253 offset:8192\n\t" \
    "v_mfma_f32_16x16x32_bf16 v[44:47], v[116:119], v[148:151], v[44:47]\n\t" \
    "ds_read_b64_tr_b16 v[2:3], v253 offset:8704\n\t" \
    "v_mfma_f32_16x16x32_bf16 v[52:55], v[116:119], v[152:155], v[52:55]\n\t" \
    "ds_read_b64_tr_b16 v[4:5], v254 offset:8192\n\t" \
    "v_mfma_f32_16x16x32_bf16 v[60:63], v[116:119], v[156:159], v[60:63]\n\t" \
    "ds_read_b64_tr_b16 v[6:7], v254 offset:8704\n\t" \
    "v_mfma_f32_16x16x32_bf16 v[80:83], v[144:147], v[120:123], v[80:83]\n\t" \
    "ds_read_b64_tr_b16 v[8:9], v253 offset:16384\n\t" \
    "v_mfma_f32_16x16x32_bf16 v[88:91], v[148:151], v[120:123], v[88:91]\n\t" \
    "ds_read_b64_tr_b16 v[10:11], v253 offset:16896\n\t" \
    "v_mfma_f32_16x16x32_bf16 v[96:99], v[152:155], v[120:123], v[96:99]\n\t" \
    "ds_read_b64_tr_b16 v[12:13], v254 offset:16384\n\t" \
    "v_mfma_f32_16x16x32_bf16 v[104:107], v[156:159], v[120:123], v[104:107]\n\t" \
    "ds_read_b64_tr_b16 v[14:15], v254 offset:16896\n\t" \
    "v_mfma_f32_16x16x32_bf16 v[84:87], v[144:147], v[124:127], v[84:87]\n\t" \
    "ds_read_b64_tr_b16 v[16:17], v253\n\t" \
    "v_mfma_f32_16x16x32_bf16 v[92:95], v[148:151], v[124:127], v[92:95]\n\t" \
    "ds_read_b64_tr_b16 v[18:19], v253 offset:512\n\t" \
    "v_mfma_f32_16x16x32_bf16 v[100:103], v[152:155], v[124:127], v[100:103]\n\t" \
    "ds_read_b64_tr_b16 v[20:21], v254\n\t" \
    "v_mfma_f32_16x16x32_bf16 v[108:111], v[156:159], v[124:127], v[108:111]\n\t" \
    "ds_read_b64_tr_b16 v[22:23], v254 offset:512\n\t" \
    "v_mfma_f32_16x16x32_bf16 v[192:195], v[24:27], v[144:147], v[192:195]\n\t" \
    "v_xor_b32 v252, 64, v251\n\t" \
    "v_mfma_f32_16x16x32_bf16 v[200:203], v[24:27], v[148:151], v[200:203]\n\t" \
    "v_mfma_f32_16x16x32_bf16 v[208:211], v[24:27], v[152:155], v[208:211]\n\t" \
    "s_sub_u32 s81, s81, 1\n\t" \
    "v_mfma_f32_16x16x32_bf16 v[216:219], v[24:27], v[156:159], v[216:219]\n\t" \
    "s_cmp_lg_u32 s81, 0\n\t" \
    "v_mfma_f32_16x16x32_bf16 v[196:199], v[28:31], v[144:147], v[196:199]\n\t" \
    "v_mfma_f32_16x16x32_bf16 v[204:207], v[28:31], v[148:151], v[204:207]\n\t" \
    "v_mfma_f32_16x16x32_bf16 v[212:215], v[28:31], v[152:155], v[212:215]\n\t" \
    "v_mfma_f32_16x16x32_bf16 v[220:223], v[28:31], v[156:159], v[220:223]\n\t" \
    "s_cbranch_scc1 .Lab_l1\n\t" \
    ".Lab_l2:\n\t" \
    "s_waitcnt lgkmcnt(0)\n\t" \
    "v_mfma_f32_16x16x32_bf16 v[32:35], v[0:3], v[128:131], v[32:35]\n\t" \
    "ds_read_b128 v[144:147], v252\n\t" \
    "ds_read_b64_tr_b16 v[112:113], v253 offset:12288\n\t" \
    "v_mfma_f32_16x16x32_bf16 v[40:43], v[0:3], v[132:135], v[40:43]\n\t" \
    "ds_read_b128 v[148:151], v252 offset:2048\n\t" \
    "ds_read_b64_tr_b16 v[114:115], v253 offset:12800\n\t" \
    "v_mfma_f32_16x16x32_bf16 v[48:51], v[0:3], v[136:139], v[48:51]\n\t" \
    "ds_read_b128 v[152:155], v252 offset:4096\n\t" \
    "s_and_b32 s80, s84, 255\n\t" \
    "v_mfma_f32_16x16x32_bf16 v[56:59], v[0:3], v[140:143], v[56:59]\n\t" \
    "ds_read_b128 v[156:159], v252 offset:6144\n\t" \
    "s_and_b32 s82, s83, 255\n\t" \
    "v_mfma_f32_16x16x32_bf16 v[36:39], v[4:7], v[128:131], v[36:39]\n\t" \
    "ds_read_b64_tr_b16 v[116:117], v254 offset:12288\n\t" \
    "s_lshl_b32 s80, s80, 12\n\t" \
    "v_mfma_f32_16x16x32_bf16 v[44:47], v[4:7], v[132:135], v[44:47]\n\t" \
    "ds_read_b64_tr_b16 v[118:119], v254 offset:12800\n\t" \
    "s_lshl_b32 s82, s82, 12\n\t" \
    "v_mfma_f32_16x16x32_bf16 v[52:55], v[4:7], v[136:139], v[52:55]\n\t" \
    "ds_read_b64_tr_b16 v[120:121], v253 offset:20480\n\t" \
    "v_mfma_f32_16x16x32_bf16 v[60:63], v[4:7], v[140:143], v[60:63]\n\t" \
    "ds_read_b64_tr_b16 v[122:123], v253 offset:20992\n\t" \
    "v_mfma_f32_16x16x32_bf16 v[80:83], v[128:131], v[8:11], v[80:83]\n\t" \
    "ds_read_b64_tr_b16 v[124:125], v254 offset:20480\n\t" \
    "v_mfma_f32_16x16x32_bf16 v[88:91], v[132:135], v[8:11], v[88:91]\n\t" \
    "ds_read_b64_tr_b16 v[126:127], v254 offset:20992\n\t" \
    "v_mfma_f32_16x16x32_bf16 v[96:99], v[136:139], v[8:11], v[96:99]\n\t" \
    "v_add_u32 v251, s82, v240\n\t" \
    "v_mfma_f32_16x16x32_bf16 v[104:107], v[140:143], v[8:11], v[104:107]\n\t" \
    "v_add_u32 v253, s80, v241\n\t" \
    "v_mfma_f32_16x16x32_bf16 v[84:87], v[128:131], v[12:15], v[84:87]\n\t" \
    "v_xor_b32 v254, 32, v253\n\t" \
    "v_mfma_f32_16x16x32_bf16 v[92:95], v[132:135], v[12:15], v[92:95]\n\t" \
    "v_mfma_f32_16x16x32_bf16 v[100:103], v[136:139], v[12:15], v[100:103]\n\t" \
    "v_mfma_f32_16x16x32_bf16 v[108:111], v[140:143], v[12:15], v[108:111]\n\t" \
    "s_waitcnt lgkmcnt(0)\n\t" \
    "s_barrier\n\t" \
    "v_mfma_f32_16x16x32_bf16 v[32:35], v[112:115], v[144:147], v[32:35]\n\t" \
    "ds_read_b128 v[128:131], v251\n\t" \
    "ds_read_b64_tr_b16 v[0:1], v253 offset:8192\n\t" \
    "v_mfma_f32_16x16x32_bf16 v[40:43], v[112:115], v[148:151], v[40:43]\n\t" \
    "ds_read_b128 v[132:135], v251 offset:2048\n\t" \
    "ds_read_b64_tr_b16 v[2:3], v253 offset:8704\n\t" \
    "v_mfma_f32_16x16x32_bf16 v[48:51], v[112:115], v[152:155], v[48:51]\n\t" \
    "ds_read_b128 v[136:139], v251 offset:4096\n\t" \
    "v_mfma_f32_16x16x32_bf16 v[56:59], v[112:115], v[156:159], v[56:59]\n\t" \
    "ds_read_b128 v[140:143], v251 offset:6144\n\t" \
    "v_mfma_f32_16x16x32_bf16 v[36:39], v[116:119], v[144:147], v[36:39]\n\t" \
    "ds_read_b64_tr_b16 v[4:5], v254 offset:8192\n\t" \
    "v_mfma_f32_16x16x32_bf16 v[44:47], v[116:119], v[148:151], v[44:47]\n\t" \
    "ds_read_b64_tr_b16 v[6:7], v254 offset:8704\n\t" \
    "v_mfma_f32_16x16x32_bf16 v[52:55], v[116:119], v[152:155], v[52:55]\n\t" \
    "ds_read_b64_tr_b16 v[8:9], v253 offset:16384\n\t" \
    "v_mfma_f32_16x16x32_bf16 v[60:63], v[116:119], v[156:159], v[60:63]\n\t" \
    "ds_read_b64_tr_b16 v[10:11], v253 offset:16896\n\t" \
    "v_mfma_f32_16x16x32_bf16 v[80:83], v[144:147], v[120:123], v[80:83]\n\t" \
    "ds_read_b64_tr_b16 v[12:13], v254 offset:16384\n\t" \
    "v_mfma_f32_16x16x32_bf16 v[88:91], v[148:151], v[120:123], v[88:91]\n\t" \
    "ds_read_b64_tr_b16 v[14:15], v254 offset:16896\n\t" \
    "v_mfma_f32_16x16x32_bf16 v[96:99], v[152:155], v[120:123], v[96:99]\n\t" \
    "v_xor_b32 v252, 64, v251\n\t" \
    "v_mfma_f32_16x16x32_bf16 v[104:107], v[156:159], v[120:123], v[104:107]\n\t" \
    "s_lshr_b32 s83, s83, 8\n\t" \
    "v_mfma_f32_16x16x32_bf16 v[84:87], v[144:147], v[124:127], v[84:87]\n\t" \
    "s_lshr_b32 s84, s84, 8\n\t" \
    "v_mfma_f32_16x16x32_bf16 v[92:95], v[148:151], v[124:127], v[92:95]\n\t" \
    "v_mfma_f32_16x16x32_bf16 v[100:103], v[152:155], v[124:127], v[100:103]\n\t" \
    "v_mfma_f32_16x16x32_bf16 v[108:111], v[156:159], v[124:127], v[108:111]\n\t" \
    "s_cbranch_scc1 .Lab_l2\n\t" \
    "s_waitcnt lgkmcnt(0)\n\t" \
    "v_mfma_f32_16x16x32_bf16 v[32:35], v[0:3], v[128:131], v[32:35]\n\t" \
    "ds_read_b128 v[144:147], v252\n\t" \
    "ds_read_b64_tr_b16 v[112:113], v253 offset:12288\n\t" \
    "v_mfma_f32_16x16x32_bf16 v[40:43], v[0:3], v[132:135], v[40:43]\n\t" \
    "ds_read_b128 v[148:151], v252 offset:2048\n\t" \
    "ds_read_b64_tr_b16 v[114:115], v253 offset:12800\n\t" \
    "v_mfma_f32_16x16x32_bf16 v[48:51], v[0:3], v[136:139], v[48:51]\n\t" \
    "ds_read_b128 v[152:155], v252 offset:4096\n\t" \
    "ds_read_b64_tr_b16 v[116:117], v254 offset:12288\n\t" \
    "v_mfma_f32_16x16x32_bf16 v[56:59], v[0:3], v[140:143], v[56:59]\n\t" \
    "ds_read_b128 v[156:159], v252 offset:6144\n\t" \
    "ds_read_b64_tr_b16 v[118:119], v254 offset:12800\n\t" \
    "v_mfma_f32_16x16x32_bf16 v[36:39], v[4:7], v[128:131], v[36:39]\n\t" \
    "ds_read_b64_tr_b16 v[120:121], v253 offset:20480\n\t" \
    "ds_read_b64_tr_b16 v[122:123], v253 offset:20992\n\t" \
    "v_mfma_f32_16x16x32_bf16 v[44:47], v[4:7], v[132:135], v[44:47]\n\t" \
    "ds_read_b64_tr_b16 v[124:125], v254 offset:20480\n\t" \
    "ds_read_b64_tr_b16 v[126:127], v254 offset:20992\n\t" \
    "v_mfma_f32_16x16x32_bf16 v[52:55], v[4:7], v[136:139], v[52:55]\n\t" \
    "ds_read_b64_tr_b16 v[224:225], v245\n\t" \
    "ds_read_b64_tr_b16 v[226:227], v245 offset:1024\n\t" \
    "v_mfma_f32_16x16x32_bf16 v[60:63], v[4:7], v[140:143], v[60:63]\n\t" \
    "v_xor_b32 v245, 32, v245\n\t" \
    "ds_read_b32 v236, v249 offset:288\n\t" \
    "v_mfma_f32_16x16x32_bf16 v[80:83], v[128:131], v[8:11], v[80:83]\n\t" \
    "ds_read_b64_tr_b16 v[228:229], v245\n\t" \
    "ds_read_b64_tr_b16 v[230:231], v245 offset:1024\n\t" \
    "v_mfma_f32_16x16x32_bf16 v[88:91], v[132:135], v[8:11], v[88:91]\n\t" \
    "ds_read_b128 v[68:71], v249 offset:320\n\t" \
    "ds_read_b64 v[64:65], v249 offset:0\n\t" \
    "v_mfma_f32_16x16x32_bf16 v[96:99], v[136:139], v[8:11], v[96:99]\n\t" \
    "v_add_u32 v251, 131072, v240\n\t" \
    "v_mfma_f32_16x16x32_bf16 v[104:107], v[140:143], v[8:11], v[104:107]\n\t" \
    "v_xor_b32 v254, 32, v241\n\t" \
    "v_mfma_f32_16x16x32_bf16 v[84:87], v[128:131], v[12:15], v[84:87]\n\t" \
    "v_mov_b32 v253, v241\n\t" \
    "v_mfma_f32_16x16x32_bf16 v[92:95], v[132:135], v[12:15], v[92:95]\n\t" \
    "v_xor_b32 v252, 64, v251\n\t" \
    "v_mfma_f32_16x16x32_bf16 v[100:103], v[136:139], v[12:15], v[100:103]\n\t" \
    "v_mfma_f32_16x16x32_bf16 v[108:111], v[140:143], v[12:15], v[108:111]\n\t" \
    "s_waitcnt lgkmcnt(0)\n\t" \
    "v_mfma_f32_16x16x32_bf16 v[32:35], v[112:115], v[144:147], v[32:35]\n\t" \
    "v_min_u32 v64, v64, v65\n\t" \
    "v_mfma_f32_16x16x32_bf16 v[40:43], v[112:115], v[148:151], v[40:43]\n\t" \
    "v_readfirstlane_b32 s85, v64\n\t" \
    "s_cmp_lt_u32 s85, 1\n\t" \
    "s_cbranch_scc1 .Lab_s1\n\t" \
    ".Lab_s1r:\n\t" \
    "v_mfma_f32_16x16x32_bf16 v[48:51], v[112:115], v[152:155], v[48:51]\n\t" \
    "ds_read_b128 v[160:163], v251\n\t" \
    "ds_read_b64_tr_b16 v[0:1], v253 offset:8192\n\t" \
    "v_mfma_f32_16x16x32_bf16 v[56:59], v[112:115], v[156:159], v[56:59]\n\t" \
    "ds_read_b128 v[164:167], v251 offset:2048\n\t" \
    "ds_read_b64_tr_b16 v[2:3], v253 offset:8704\n\t" \
    "v_mfma_f32_16x16x32_bf16 v[36:39], v[116:119], v[144:147], v[36:39]\n\t" \
    "ds_read_b128 v[168:171], v251 offset:4096\n\t" \
    "ds_read_b64_tr_b16 v[4:5], v254 offset:8192\n\t" \
    "v_mfma_f32_16x16x32_bf16 v[44:47], v[116:119], v[148:151], v[44:47]\n\t" \
    "ds_read_b128 v[172:175], v251 offset:6144\n\t" \
    "ds_read_b64_tr_b16 v[6:7], v254 offset:8704\n\t" \
    "v_mfma_f32_16x16x32_bf16 v[52:55], v[116:119], v[152:155], v[52:55]\n\t" \
    "ds_read_b64_tr_b16 v[8:9], v253 offset:16384\n\t" \
    "ds_read_b64_tr_b16 v[10:11], v253 offset:16896\n\t" \
    "v_mfma_f32_16x16x32_bf16 v[60:63], v[116:119], v[156:159], v[60:63]\n\t" \
    "ds_read_b64_tr_b16 v[12:13], v254 offset:16384\n\t" \
    "ds_read_b64_tr_b16 v[14:15], v254 offset:16896\n\t" \
    "v_mfma_f32_16x16x32_bf16 v[80:83], v[144:147], v[120:123], v[80:83]\n\t" \
    "v_readfirstlane_b32 s87, v68\n\t" \
    "v_mfma_f32_16x16x32_bf16 v[88:91], v[148:151], v[120:123], v[88:91]\n\t" \
    "v_readfirstlane_b32 s88, v69\n\t" \
    "v_mfma_f32_16x16x32_bf16 v[96:99], v[152:155], v[120:123], v[96:99]\n\t" \
    "v_readfirstlane_b32 %[sof], v70\n\t" \
    "v_mfma_f32_16x16x32_bf16 v[104:107], v[156:159], v[120:123], v[104:107]\n\t" \
    "s_xor_b32 s87, s87, %[kpx]\n\t" \
    "s_add_u32 s88, s88, %[kma]\n\t" \
    "v_mfma_f32_16x16x32_bf16 v[84:87], v[144:147], v[124:127], v[84:87]\n\t" \
    "s_add_u32 s87, s87, %[kpa]\n\t" \
    "v_mfma_f32_16x16x32_bf16 v[92:95], v[148:151], v[124:127], v[92:95]\n\t" \
    "s_xor_b32 s86, s87, 0x8000\n\t" \
    "v_mfma_f32_16x16x32_bf16 v[100:103], v[152:155], v[124:127], v[100:103]\n\t" \
    "v_mfma_f32_16x16x32_bf16 v[108:111], v[156:159], v[124:127], v[108:111]\n\t" \
    "s_waitcnt lgkmcnt(0)\n\t" \
    "v_mfma_f32_16x16x32_bf16 v[32:35], v[0:3], v[160:163], v[32:35]\n\t" \
    "ds_read_b128 v[176:179], v252\n\t" \
    "ds_read_b64_tr_b16 v[112:113], v253 offset:12288\n\t" \
    "v_mfma_f32_16x16x32_bf16 v[40:43], v[0:3], v[164:167], v[40:43]\n\t" \
    "ds_read_b128 v[180:183], v252 offset:2048\n\t" \
    "ds_read_b64_tr_b16 v[114:115], v253 offset:12800\n\t" \
    "v_mfma_f32_16x16x32_bf16 v[48:51], v[0:3], v[168:171], v[48:51]\n\t" \
    "ds_read_b128 v[184:187], v252 offset:4096\n\t" \
    "v_mov_b32 v237, 2\n\t" \
    "v_mfma_f32_16x16x32_bf16 v[56:59], v[0:3], v[172:175], v[56:59]\n\t" \
    "ds_read_b128 v[188:191], v252 offset:6144\n\t" \
    "v_mfma_f32_16x16x32_bf16 v[36:39], v[4:7], v[160:163], v[36:39]\n\t" \
    "ds_read_b64_tr_b16 v[116:117], v254 offset:12288\n\t" \
    "v_mfma_f32_16x16x32_bf16 v[44:47], v[4:7], v[164:167], v[44:47]\n\t" \
    "ds_read_b64_tr_b16 v[118:119], v254 offset:12800\n\t" \
    "v_mfma_f32_16x16x32_bf16 v[52:55], v[4:7], v[168:171], v[52:55]\n\t" \
    "ds_read_b64_tr_b16 v[120:121], v253 offset:20480\n\t" \
    "v_mfma_f32_16x16x32_bf16 v[60:63], v[4:7], v[172:175], v[60:63]\n\t" \
    "ds_read_b64_tr_b16 v[122:123], v253 offset:20992\n\t" \
    "v_mfma_f32_16x16x32_bf16 v[80:83], v[160:163], v[8:11], v[80:83]\n\t" \
    "ds_read_b64_tr_b16 v[124:125], v254 offset:20480\n\t" \
    "v_mfma_f32_16x16x32_bf16 v[88:91], v[164:167], v[8:11], v[88:91]\n\t" \
    "ds_read_b64_tr_b16 v[126:127], v254 offset:20992\n\t" \
    "v_mfma_f32_16x16x32_bf16 v[96:99], v[168:171], v[8:11], v[96:99]\n\t" \
    "ds_write_b32 v248, v237\n\t" \
    "v_mfma_f32_16x16x32_bf16 v[104:107], v[172:175], v[8:11], v[104:107]\n\t" \
    "v_xor_b32 v251, 32, v243\n\t" \
    "v_mfma_f32_16x16x32_bf16 v[84:87], v[160:163], v[12:15], v[84:87]\n\t" \
    "v_xor_b32 v252, 64, v243\n\t" \
    "v_mfma_f32_16x16x32_bf16 v[92:95], v[164:167], v[12:15], v[92:95]\n\t" \
    "v_xor_b32 v253, 0x60, v243\n\t" \
    "v_mfma_f32_16x16x32_bf16 v[100:103], v[168:171], v[12:15], v[100:103]\n\t" \
    "s_setprio 3\n\t" \
    "v_mfma_f32_16x16x32_bf16 v[108:111], v[172:175], v[12:15], v[108:111]\n\t" \
    "s_waitcnt lgkmcnt(0)\n\t" \
    "v_mfma_f32_16x16x32_bf16 v[32:35], v[112:115], v[176:179], v[32:35]\n\t" \
    "v_mfma_f32_16x16x32_bf16 v[36:39], v[116:119], v[176:179], v[36:39]\n\t" \
    "v_mfma_f32_16x16x32_bf16 v[40:43], v[112:115], v[180:183], v[40:43]\n\t" \
    "v_mfma_f32_16x16x32_bf16 v[44:47], v[116:119], v[180:183], v[44:47]\n\t" \
    "ds_read_b128 v[28:31], v249 offset:16\n\t" \
    "v_mfma_f32_16x16x32_bf16 v[48:51], v[112:115], v[184:187], v[48:51]\n\t" \
    "v_mfma_f32_16x16x32_bf16 v[52:55], v[116:119], v[184:187], v[52:55]\n\t" \
    "v_mfma_f32_16x16x32_bf16 v[56:59], v[112:115], v[188:191], v[56:59]\n\t" \
    "v_mfma_f32_16x16x32_bf16 v[60:63], v[116:119], v[188:191], v[60:63]\n\t" \
    "v_cvt_pk_bf16_f32 v64, v32, v33\n\t" \
    "v_cvt_pk_bf16_f32 v65, v34, v35\n\t" \
    "v_mfma_f32_16x16x32_bf16 v[80:83], v[176:179], v[120:123], v[80:83]\n\t" \
    "v_cvt_pk_bf16_f32 v66, v36, v37\n\t" \
    "v_cvt_pk_bf16_f32 v67, v38, v39\n\t" \
    "v_mfma_f32_16x16x32_bf16 v[88:91], v[180:183], v[120:123], v[88:91]\n\t" \
    "v_cvt_pk_bf16_f32 v68, v40, v41\n\t" \
    "v_cvt_pk_bf16_f32 v69, v42, v43\n\t" \
    "v_mfma_f32_16x16x32_bf16 v[84:87], v[176:179], v[124:127], v[84:87]\n\t" \
    "v_cvt_pk_bf16_f32 v70, v44, v45\n\t" \
    "v_cvt_pk_bf16_f32 v71, v46, v47\n\t" \
    "v_mfma_f32_16x16x32_bf16 v[92:95], v[180:183], v[124:127], v[92:95]\n\t" \
    "v_cvt_pk_bf16_f32 v72, v48, v49\n\t" \
    "v_cvt_pk_bf16_f32 v73, v50, v51\n\t" \
    "v_mfma_f32_16x16x32_bf16 v[96:99], v[184:187], v[120:123], v[96:99]\n\t" \
    "v_cvt_pk_bf16_f32 v74, v52, v53\n\t" \
    "v_cvt_pk_bf16_f32 v75, v54, v55\n\t" \
    "v_mfma_f32_16x16x32_bf16 v[104:107], v[188:191], v[120:123], v[104:107]\n\t" \
    "v_cvt_pk_bf16_f32 v76, v56, v57\n\t" \
    "v_cvt_pk_bf16_f32 v77, v58, v59\n\t" \
    "v_mfma_f32_16x16x32_bf16 v[100:103], v[184:187], v[124:127], v[100:103]\n\t" \
    "v_cvt_pk_bf16_f32 v78, v60, v61\n\t" \
    "v_cvt_pk_bf16_f32 v79, v62, v63\n\t" \
    "v_mfma_f32_16x16x32_bf16 v[108:111], v[188:191], v[124:127], v[108:111]\n\t" \
    "s_waitcnt lgkmcnt(0)\n\t" \
    "v_min_u32 v238, v28, v29\n\t" \
    "v_min3_u32 v238, v238, v30, v31\n\t" \
    "v_mfma_f32_16x16x32_bf16 v[0:3], v[224:227], v[64:67], 0\n\t" \
    "v_cvt_pk_bf16_f32 v112, v80, v81\n\t" \
    "v_cvt_pk_bf16_f32 v113, v82, v83\n\t" \
    "v_mfma_f32_16x16x32_bf16 v[4:7], v[228:231], v[64:67], 0\n\t" \
    "v_cvt_pk_bf16_f32 v114, v88, v89\n\t" \
    "v_cvt_pk_bf16_f32 v115, v90, v91\n\t" \
    "v_mfma_f32_16x16x32_bf16 v[8:11], v[224:227], v[68:71], 0\n\t" \
    "v_cvt_pk_bf16_f32 v116, v84, v85\n\t" \
    "v_cvt_pk_bf16_f32 v117, v86, v87\n\t" \
    "v_mfma_f32_16x16x32_bf16 v[12:15], v[228:231], v[68:71], 0\n\t" \
    "v_cvt_pk_bf16_f32 v118, v92, v93\n\t" \
    "v_cvt_pk_bf16_f32 v119, v94, v95\n\t" \
    "v_mfma_f32_16x16x32_bf16 v[16:19], v[224:227], v[72:75], 0\n\t" \
    "v_cvt_pk_bf16_f32 v120, v96, v97\n\t" \
    "v_cvt_pk_bf16_f32 v121, v98, v99\n\t" \
    "v_mfma_f32_16x16x32_bf16 v[20:23], v[228:231], v[72:75], 0\n\t" \
    "v_cvt_pk_bf16_f32 v122, v104, v105\n\t" \
    "v_cvt_pk_bf16_f32 v123, v106, v107\n\t" \
    "v_mfma_f32_16x16x32_bf16 v[24:27], v[224:227], v[76:79], 0\n\t" \
    "v_cvt_pk_bf16_f32 v124, v100, v101\n\t" \
    "v_cvt_pk_bf16_f32 v125, v102, v103\n\t" \
    "v_mfma_f32_16x16x32_bf16 v[28:31], v[228:231], v[76:79], 0\n\t" \
    "v_cvt_pk_bf16_f32 v126, v108, v109\n\t" \
    "v_cvt_pk_bf16_f32 v127, v110, v111\n\t" \
    "v_readfirstlane_b32 s85, v238\n\t" \
    "s_cmp_lt_u32 s85, 2\n\t" \
    "s_cbranch_scc1 .Lab_s2\n\t" \
    ".Lab_s2r:\n\t" \
    "ds_write_b128 v242, v[112:115]\n\t" \
    "v_max3_f32 v64, v0, v1, v2\n\t" \
    "v_max3_f32 v66, -v0, -v1, -v2\n\t" \
    "v_max3_f32 v68, v8, v9, v10\n\t" \
    "v_max3_f32 v64, v64, v3, v4\n\t" \
    "v_max3_f32 v66, v66, -v3, -v4\n\t" \
    "v_max3_f32 v68, v68, v11, v12\n\t" \
    "v_max3_f32 v64, v64, v5, v6\n\t" \
    "v_max3_f32 v66, v66, -v5, -v6\n\t" \
    "ds_write_b128 v242, v[116:119] offset:1024\n\t" \
    "v_max3_f32 v68, v68, v13, v14\n\t" \
    "v_max_f32_e64 v64, v64, v7\n\t" \
    "v_max_f32_e64 v66, v66, -v7\n\t" \
    "v_max_f32_e64 v68, v68, v15\n\t" \
    "v_max3_f32 v70, -v8, -v9, -v10\n\t" \
    "v_permlane32_swap_b32 v64, v66\n\t" \
    "v_max3_f32 v72, v16, v17, v18\n\t" \
    "v_max3_f32 v70, v70, -v11, -v12\n\t" \
    "ds_write_b128 v242, v[120:123] offset:4096\n\t" \
    "v_max_f32 v64, v64, v66\n\t" \
    "v_max3_f32 v72, v72, v19, v20\n\t" \
    "v_max3_f32 v70, v70, -v13, -v14\n\t" \
    "v_max3_f32 v74, -v16, -v17, -v18\n\t" \
    "v_max3_f32 v72, v72, v21, v22\n\t" \
    "v_max_f32_e64 v70, v70, -v15\n\t" \
    "v_max3_f32 v74, v74, -v19, -v20\n\t" \
    "v_max_f32_e64 v72, v72, v23\n\t" \
    "ds_write_b128 v242, v[124:127] offset:5120\n\t" \
    "v_permlane32_swap_b32 v68, v70\n\t" \
    "v_max3_f32 v74, v74, -v21, -v22\n\t" \
    "v_max3_f32 v76, v24, v25, v26\n\t" \
    "v_max_f32 v68, v68, v70\n\t" \
    "v_max_f32_e64 v74, v74, -v23\n\t" \
    "v_max3_f32 v76, v76, v27, v28\n\t" \
    "v_permlane16_swap_b32 v64, v68\n\t" \
    "v_permlane32_swap_b32 v72, v74\n\t" \
    "v_max3_f32 v76, v76, v29, v30\n\t" \
    "v_max_f32 v66, v64, v68\n\t" \
    "v_max_f32 v72, v72, v74\n\t" \
    "v_max_f32_e64 v76, v76, v31\n\t" \
    "v_xor_b32 v254, 32, v244\n\t" \
    "v_mul_f32 v64, 0xbfb8aa3b, v66\n\t" \
    "v_mul_f32 v68, 0xbfb8aa3b, v66\n\t" \
    "v_xor_b32 v245, 0x2000, v244\n\t" \
    "v_max3_f32 v78, -v24, -v25, -v26\n\t" \
    "v_max3_f32 v78, v78, -v27, -v28\n\t" \
    "v_xor_b32 v255, 0x2020, v244\n\t" \
    "v_permlane16_swap_b32 v64, v68\n\t" \
    "v_mov_b32 v66, v64\n\t" \
    "v_xor_b32 v242, 0x2000, v242\n\t" \
    "v_max3_f32 v78, v78, -v29, -v30\n\t" \
    "v_mov_b32 v70, v68\n\t" \
    "v_permlane32_swap_b32 v64, v66\n\t" \
    "v_max_f32_e64 v78, v78, -v31\n\t" \
    "v_permlane32_swap_b32 v68, v70\n\t" \
    "v_pk_fma_f32 v[32:33], v[0:1], s[90:91], v[64:65] op_sel_hi:[1,1,0]\n\t" \
    "v_permlane32_swap_b32 v76, v78\n\t" \
    "v_pk_fma_f32 v[0:1], v[0:1], s[90:91], v[66:67] op_sel_hi:[1,1,0] neg_lo:[1,0,0] neg_hi:[1,0,0]\n\t" \
    "v_pk_fma_f32 v[34:35], v[2:3], s[90:91], v[64:65] op_sel_hi:[1,1,0]\n\t" \
    "v_max_f32 v76, v76, v78\n\t" \
    "v_pk_fma_f32 v[2:3], v[2:3], s[90:91], v[66:67] op_sel_hi:[1,1,0] neg_lo:[1,0,0] neg_hi:[1,0,0]\n\t" \
    "v_pk_fma_f32 v[36:37], v[4:5], s[90:91], v[64:65] op_sel_hi:[1,1,0]\n\t" \
    "v_permlane16_swap_b32 v72, v76\n\t" \
    "v_pk_fma_f32 v[4:5], v[4:5], s[90:91], v[66:67] op_sel_hi:[1,1,0] neg_lo:[1,0,0] neg_hi:[1,0,0]\n\t" \
    "v_pk_fma_f32 v[38:39], v[6:7], s[90:91], v[64:65] op_sel_hi:[1,1,0]\n\t" \
    "v_max_f32 v74, v72, v76\n\t" \
    "v_pk_fma_f32 v[6:7], v[6:7], s[90:91], v[66:67] op_sel_hi:[1,1,0] neg_lo:[1,0,0] neg_hi:[1,0,0]\n\t" \
    "v_exp_f32 v32, v32\n\t" \
    "v_mul_f32 v72, 0xbfb8aa3b, v74\n\t" \
    "v_mul_f32 v76, 0xbfb8aa3b, v74\n\t" \
    "v_exp_f32 v0, v0\n\t" \
    "v_exp_f32 v33, v33\n\t" \
    "v_permlane16_swap_b32 v72, v76\n\t" \
    "v_exp_f32 v1, v1\n\t" \
    "v_exp_f32 v34, v34\n\t" \
    "v_mov_b32 v74, v72\n\t" \
    "v_mov_b32 v78, v76\n\t" \
    "v_exp_f32 v2, v2\n\t" \
    "v_permlane32_swap_b32 v72, v74\n\t" \
    "v_permlane32_swap_b32 v76, v78\n\t" \
    "v_exp_f32 v35, v35\n\t" \
    "v_exp_f32 v3, v3\n\t" \
    "v_exp_f32 v36, v36\n\t" \
    "v_pk_add_f32 v[64:65], v[32:33], v[34:35]\n\t" \
    "v_pk_add_f32 v[66:67], v[0:1], v[2:3]\n\t" \
    "v_exp_f32 v4, v4\n\t" \
    "v_exp_f32 v37, v37\n\t" \
    "v_exp_f32 v5, v5\n\t" \
    "v_exp_f32 v38, v38\n\t" \
    "v_pk_add_f32 v[64:65], v[64:65], v[36:37]\n\t" \
    "v_pk_add_f32 v[66:67], v[66:67], v[4:5]\n\t" \
    "v_exp_f32 v6, v6\n\t" \
    "v_exp_f32 v39, v39\n\t" \
    "v_exp_f32 v7, v7\n\t" \
    "v_pk_fma_f32 v[40:41], v[8:9], s[90:91], v[68:69] op_sel_hi:[1,1,0]\n\t" \
    "v_pk_add_f32 v[64:65], v[64:65], v[38:39]\n\t" \
    "v_pk_add_f32 v[66:67], v[66:67], v[6:7]\n\t" \
    "v_pk_fma_f32 v[8:9], v[8:9], s[90:91], v[70:71] op_sel_hi:[1,1,0] neg_lo:[1,0,0] neg_hi:[1,0,0]\n\t" \
    "v_add_f32 v64, v64, v65\n\t" \
    "v_add_f32 v66, v66, v67\n\t" \
    "v_pk_fma_f32 v[42:43], v[10:11], s[90:91], v[68:69] op_sel_hi:[1,1,0]\n\t" \
    "v_pk_fma_f32 v[10:11], v[10:11], s[90:91], v[70:71] op_sel_hi:[1,1,0] neg_lo:[1,0,0] neg_hi:[1,0,0]\n\t" \
    "v_permlane32_swap_b32 v64, v66\n\t" \
    "v_pk_fma_f32 v[44:45], v[12:13], s[90:91], v[68:69] op_sel_hi:[1,1,0]\n\t" \
    "v_pk_fma_f32 v[12:13], v[12:13], s[90:91], v[70:71] op_sel_hi:[1,1,0] neg_lo:[1,0,0] neg_hi:[1,0,0]\n\t" \
    "v_add_f32 v64, v64, v66\n\t" \
    "v_pk_fma_f32 v[46:47], v[14:15], s[90:91], v[68:69] op_sel_hi:[1,1,0]\n\t" \
    "v_pk_fma_f32 v[14:15], v[14:15], s[90:91], v[70:71] op_sel_hi:[1,1,0] neg_lo:[1,0,0] neg_hi:[1,0,0]\n\t" \
    "v_exp_f32 v40, v40\n\t" \
    "v_exp_f32 v8, v8\n\t" \
    "v_exp_f32 v41, v41\n\t" \
    "v_exp_f32 v9, v9\n\t" \
    "v_exp_f32 v42, v42\n\t" \
    "v_exp_f32 v10, v10\n\t" \
    "v_exp_f32 v43, v43\n\t" \
    "v_exp_f32 v11, v11\n\t" \
    "v_exp_f32 v44, v44\n\t" \
    "v_pk_add_f32 v[68:69], v[40:41], v[42:43]\n\t" \
    "v_pk_add_f32 v[70:71], v[8:9], v[10:11]\n\t" \
    "v_exp_f32 v12, v12\n\t" \
    "v_exp_f32 v45, v45\n\t" \
    "v_exp_f32 v13, v13\n\t" \
    "v_exp_f32 v46, v46\n\t" \
    "v_pk_add_f32 v[68:69], v[68:69], v[44:45]\n\t" \
    "v_pk_add_f32 v[70:71], v[70:71], v[12:13]\n\t" \
    "v_exp_f32 v14, v14\n\t" \
    "v_exp_f32 v47, v47\n\t" \
    "v_exp_f32 v15, v15\n\t" \
    "v_pk_fma_f32 v[48:49], v[16:17], s[90:91], v[72:73] op_sel_hi:[1,1,0]\n\t" \
    "v_pk_add_f32 v[68:69], v[68:69], v[46:47]\n\t" \
    "v_pk_add_f32 v[70:71], v[70:71], v[14:15]\n\t" \
    "v_pk_fma_f32 v[16:17], v[16:17], s[90:91], v[74:75] op_sel_hi:[1,1,0] neg_lo:[1,0,0] neg_hi:[1,0,0]\n\t" \
    "v_add_f32 v68, v68, v69\n\t" \
    "v_add_f32 v70, v70, v71\n\t" \
    "v_pk_fma_f32 v[50:51], v[18:19], s[90:91], v[72:73] op_sel_hi:[1,1,0]\n\t" \
    "v_pk_fma_f32 v[18:19], v[18:19], s[90:91], v[74:75] op_sel_hi:[1,1,0] neg_lo:[1,0,0] neg_hi:[1,0,0]\n\t" \
    "v_permlane32_swap_b32 v68, v70\n\t" \
    "v_pk_fma_f32 v[52:53], v[20:21], s[90:91], v[72:73] op_sel_hi:[1,1,0]\n\t" \
    ".Lpad_ab1:\n\t" \
    "s_cbranch_execz .Lpad_ret\n\t" \
    "v_pk_fma_f32 v[20:21], v[20:21], s[90:91], v[74:75] op_sel_hi:[1,1,0] neg_lo:[1,0,0] neg_hi:[1,0,0]\n\t" \
    "v_add_f32 v68, v68, v70\n\t" \
    "v_pk_fma_f32 v[54:55], v[22:23], s[90:91], v[72:73] op_sel_hi:[1,1,0]\n\t" \
    "v_pk_fma_f32 v[22:23], v[22:23], s[90:91], v[74:75] op_sel_hi:[1,1,0] neg_lo:[1,0,0] neg_hi:[1,0,0]\n\t" \
    "v_permlane16_swap_b32 v64, v68\n\t" \
    "v_exp_f32 v48, v48\n\t" \
    "v_exp_f32 v16, v16\n\t" \
    "v_add_f32 v66, v64, v68\n\t" \
    "v_exp_f32 v49, v49\n\t" \
    "v_exp_f32 v17, v17\n\t" \
    "v_rcp_f32 v64, v66\n\t" \
    "v_exp_f32 v50, v50\n\t" \
    "v_exp_f32 v18, v18\n\t" \
    "v_mov_b32 v68, v64\n\t" \
    "v_exp_f32 v51, v51\n\t" \
    "v_exp_f32 v19, v19\n\t" \
    "v_permlane16_swap_b32 v64, v68\n\t" \
    "v_pk_add_f32 v[72:73], v[48:49], v[50:51]\n\t" \
    "v_pk_add_f32 v[74:75], v[16:17], v[18:19]\n\t" \
    "v_mov_b32 v66, v64\n\t" \
    "v_mov_b32 v70, v68\n\t" \
    "v_exp_f32 v52, v52\n\t" \
    "v_permlane32_swap_b32 v64, v66\n\t" \
    "v_permlane32_swap_b32 v68, v70\n\t" \
    "v_exp_f32 v20, v20\n\t" \
    "v_pk_mul_f32 v[0:1], v[0:1], v[66:67] op_sel_hi:[1,0]\n\t" \
    "v_pk_mul_f32 v[32:33], v[32:33], v[64:65] op_sel_hi:[1,0]\n\t" \
    "v_pk_mul_f32 v[2:3], v[2:3], v[66:67] op_sel_hi:[1,0]\n\t" \
    "v_pk_mul_f32 v[34:35], v[34:35], v[64:65] op_sel_hi:[1,0]\n\t" \
    "v_pk_mul_f32 v[4:5], v[4:5], v[66:67] op_sel_hi:[1,0]\n\t" \
    "v_pk_mul_f32 v[36:37], v[36:37], v[64:65] op_sel_hi:[1,0]\n\t" \
    "v_pk_mul_f32 v[6:7], v[6:7], v[66:67] op_sel_hi:[1,0]\n\t" \
    "v_pk_mul_f32 v[38:39], v[38:39], v[64:65] op_sel_hi:[1,0]\n\t" \
    "v_exp_f32 v53, v53\n\t" \
    "v_cvt_pk_bf16_f32 v7, v6, v7\n\t" \
    "v_cvt_pk_bf16_f32 v6, v4, v5\n\t" \
    "v_cvt_pk_bf16_f32 v5, v2, v3\n\t" \
    "v_cvt_pk_bf16_f32 v4, v0, v1\n\t" \
    "ds_write_b64 v253, v[6:7] offset:0\n\t" \
    "v_cvt_pk_bf16_f32 v0, v32, v33\n\t" \
    "v_cvt_pk_bf16_f32 v1, v34, v35\n\t" \
    "v_cvt_pk_bf16_f32 v2, v36, v37\n\t" \
    "v_cvt_pk_bf16_f32 v3, v38, v39\n\t" \
    "v_pk_add_f32 v[72:73], v[72:73], v[52:53]\n\t" \
    "ds_write_b64 v243, v[0:1] offset:0\n\t" \
    "v_exp_f32 v21, v21\n\t" \
    "v_exp_f32 v54, v54\n\t" \
    "v_exp_f32 v22, v22\n\t" \
    "v_pk_add_f32 v[74:75], v[74:75], v[20:21]\n\t" \
    "v_exp_f32 v55, v55\n\t" \
    "ds_write_b64 v251, v[2:3] offset:0\n\t" \
    "v_exp_f32 v23, v23\n\t" \
    "v_pk_add_f32 v[72:73], v[72:73], v[54:55]\n\t" \
    "v_pk_fma_f32 v[56:57], v[24:25], s[90:91], v[76:77] op_sel_hi:[1,1,0]\n\t" \
    "v_pk_add_f32 v[74:75], v[74:75], v[22:23]\n\t" \
    "v_add_f32 v72, v72, v73\n\t" \
    "ds_write_b64 v252, v[4:5] offset:0\n\t" \
    "v_add_f32 v74, v74, v75\n\t" \
    "v_pk_fma_f32 v[24:25], v[24:25], s[90:91], v[78:79] op_sel_hi:[1,1,0] neg_lo:[1,0,0] neg_hi:[1,0,0]\n\t" \
    "v_pk_fma_f32 v[58:59], v[26:27], s[90:91], v[76:77] op_sel_hi:[1,1,0]\n\t" \
    "v_permlane32_swap_b32 v72, v74\n\t" \
    "v_pk_fma_f32 v[26:27], v[26:27], s[90:91], v[78:79] op_sel_hi:[1,1,0] neg_lo:[1,0,0] neg_hi:[1,0,0]\n\t" \
    "v_pk_fma_f32 v[60:61], v[28:29], s[90:91], v[76:77] op_sel_hi:[1,1,0]\n\t" \
    "v_add_f32 v72, v72, v74\n\t" \
    "v_pk_fma_f32 v[28:29], v[28:29], s[90:91], v[78:79] op_sel_hi:[1,1,0] neg_lo:[1,0,0] neg_hi:[1,0,0]\n\t" \
    "v_pk_fma_f32 v[62:63], v[30:31], s[90:91], v[76:77] op_sel_hi:[1,1,0]\n\t" \
    "v_pk_fma_f32 v[30:31], v[30:31], s[90:91], v[78:79] op_sel_hi:[1,1,0] neg_lo:[1,0,0] neg_hi:[1,0,0]\n\t" \
    "v_exp_f32 v56, v56\n\t" \
    "v_exp_f32 v24, v24\n\t" \
    "v_exp_f32 v57, v57\n\t" \
    "v_exp_f32 v25, v25\n\t" \
    "v_exp_f32 v58, v58\n\t" \
    "v_exp_f32 v26, v26\n\t" \
    "v_exp_f32 v59, v59\n\t" \
    "v_exp_f32 v27, v27\n\t" \
    "v_exp_f32 v60, v60\n\t" \
    "v_pk_add_f32 v[76:77], v[56:57], v[58:59]\n\t" \
    "v_pk_add_f32 v[78:79], v[24:25], v[26:27]\n\t" \
    "v_exp_f32 v28, v28\n\t" \
    "v_exp_f32 v61, v61\n\t" \
    "v_exp_f32 v29, v29\n\t" \
    "v_exp_f32 v62, v62\n\t" \
    "v_pk_add_f32 v[76:77], v[76:77], v[60:61]\n\t" \
    "v_pk_add_f32 v[78:79], v[78:79], v[28:29]\n\t" \
    "v_exp_f32 v30, v30\n\t" \
    "v_exp_f32 v63, v63\n\t" \
    "v_exp_f32 v31, v31\n\t" \
    "v_pk_mul_f32 v[8:9], v[8:9], v[70:71] op_sel_hi:[1,0]\n\t" \
    "v_pk_add_f32 v[76:77], v[76:77], v[62:63]\n\t" \
    "v_pk_add_f32 v[78:79], v[78:79], v[30:31]\n\t" \
    "v_pk_mul_f32 v[40:41], v[40:41], v[68:69] op_sel_hi:[1,0]\n\t" \
    "v_add_f32 v76, v76, v77\n\t" \
    "v_add_f32 v78, v78, v79\n\t" \
    "v_pk_mul_f32 v[10:11], v[10:11], v[70:71] op_sel_hi:[1,0]\n\t" \
    "v_pk_mul_f32 v[42:43], v[42:43], v[68:69] op_sel_hi:[1,0]\n\t" \
    "v_permlane32_swap_b32 v76, v78\n\t" \
    "v_pk_mul_f32 v[12:13], v[12:13], v[70:71] op_sel_hi:[1,0]\n\t" \
    "v_pk_mul_f32 v[44:45], v[44:45], v[68:69] op_sel_hi:[1,0]\n\t" \
    "v_add_f32 v76, v76, v78\n\t" \
    "v_pk_mul_f32 v[14:15], v[14:15], v[70:71] op_sel_hi:[1,0]\n\t" \
    "v_pk_mul_f32 v[46:47], v[46:47], v[68:69] op_sel_hi:[1,0]\n\t" \
    "v_permlane16_swap_b32 v72, v76\n\t" \
    "v_cvt_pk_bf16_f32 v15, v14, v15\n\t" \
    "v_cvt_pk_bf16_f32 v14, v12, v13\n\t" \
    "v_add_f32 v74, v72, v76\n\t" \
    "v_cvt_pk_bf16_f32 v13, v10, v11\n\t" \
    "ds_write_b64 v253, v[14:15] offset:2048\n\t" \
    "v_rcp_f32 v72, v74\n\t" \
    "v_cvt_pk_bf16_f32 v12, v8, v9\n\t" \
    "v_cvt_pk_bf16_f32 v8, v40, v41\n\t" \
    "v_mov_b32 v76, v72\n\t" \
    "v_cvt_pk_bf16_f32 v9, v42, v43\n\t" \
    "ds_write_b64 v252, v[12:13] offset:2048\n\t" \
    "v_permlane16_swap_b32 v72, v76\n\t" \
    "v_cvt_pk_bf16_f32 v10, v44, v45\n\t" \
    "v_cvt_pk_bf16_f32 v11, v46, v47\n\t" \
    "v_mov_b32 v74, v72\n\t" \
    "v_mov_b32 v78, v76\n\t" \
    "ds_write_b64 v243, v[8:9] offset:2048\n\t" \
    "v_permlane32_swap_b32 v72, v74\n\t" \
    "v_permlane32_swap_b32 v76, v78\n\t" \
    "ds_write_b64 v251, v[10:11] offset:2048\n\t" \
    "v_mov_b32 v237, 3\n\t" \
    "v_pk_mul_f32 v[16:17], v[16:17], v[74:75] op_sel_hi:[1,0]\n\t" \
    "ds_write_b32 v248, v237\n\t" \
    "v_pk_mul_f32 v[48:49], v[48:49], v[72:73] op_sel_hi:[1,0]\n\t" \
    "ds_read_b64_tr_b16 v[96:97], v244\n\t" \
    "v_pk_mul_f32 v[18:19], v[18:19], v[74:75] op_sel_hi:[1,0]\n\t" \
    "ds_read_b64_tr_b16 v[98:99], v244 offset:2048\n\t" \
    "v_pk_mul_f32 v[50:51], v[50:51], v[72:73] op_sel_hi:[1,0]\n\t" \
    "ds_read_b64_tr_b16 v[100:101], v254\n\t" \
    "v_pk_mul_f32 v[20:21], v[20:21], v[74:75] op_sel_hi:[1,0]\n\t" \
    "ds_read_b64_tr_b16 v[102:103], v254 offset:2048\n\t" \
    "v_pk_mul_f32 v[52:53], v[52:53], v[72:73] op_sel_hi:[1,0]\n\t" \
    "v_pk_mul_f32 v[22:23], v[22:23], v[74:75] op_sel_hi:[1,0]\n\t" \
    "v_pk_mul_f32 v[54:55], v[54:55], v[72:73] op_sel_hi:[1,0]\n\t" \
    "v_pk_mul_f32 v[24:25], v[24:25], v[78:79] op_sel_hi:[1,0]\n\t" \
    "v_cvt_pk_bf16_f32 v23, v22, v23\n\t" \
    "v_cvt_pk_bf16_f32 v22, v20, v21\n\t" \
    "v_cvt_pk_bf16_f32 v21, v18, v19\n\t" \
    "v_cvt_pk_bf16_f32 v20, v16, v17\n\t" \
    "ds_write_b64 v253, v[22:23] offset:4096\n\t" \
    "v_cvt_pk_bf16_f32 v16, v48, v49\n\t" \
    "v_cvt_pk_bf16_f32 v17, v50, v51\n\t" \
    "v_cvt_pk_bf16_f32 v18, v52, v53\n\t" \
    "v_cvt_pk_bf16_f32 v19, v54, v55\n\t" \
    "v_pk_mul_f32 v[56:57], v[56:57], v[76:77] op_sel_hi:[1,0]\n\t" \
    "ds_write_b64 v243, v[16:17] offset:4096\n\t" \
    "v_pk_mul_f32 v[26:27], v[26:27], v[78:79] op_sel_hi:[1,0]\n\t" \
    "v_pk_mul_f32 v[58:59], v[58:59], v[76:77] op_sel_hi:[1,0]\n\t" \
    "v_pk_mul_f32 v[28:29], v[28:29], v[78:79] op_sel_hi:[1,0]\n\t" \
    "v_pk_mul_f32 v[60:61], v[60:61], v[76:77] op_sel_hi:[1,0]\n\t" \
    "v_pk_mul_f32 v[30:31], v[30:31], v[78:79] op_sel_hi:[1,0]\n\t" \
    "ds_write_b64 v251, v[18:19] offset:4096\n\t" \
    "v_pk_mul_f32 v[62:63], v[62:63], v[76:77] op_sel_hi:[1,0]\n\t" \
    "v_cvt_pk_bf16_f32 v31, v30, v31\n\t" \
    "v_cvt_pk_bf16_f32 v30, v28, v29\n\t" \
    "v_cvt_pk_bf16_f32 v29, v26, v27\n\t" \
    "v_cvt_pk_bf16_f32 v28, v24, v25\n\t" \
    "ds_write_b64 v252, v[20:21] offset:4096\n\t" \
    "ds_read_b32 v238, v250\n\t" \
    "v_cvt_pk_bf16_f32 v24, v56, v57\n\t" \
    "ds_read_b64_tr_b16 v[104:105], v245\n\t" \
    "v_cvt_pk_bf16_f32 v25, v58, v59\n\t" \
    "ds_read_b64_tr_b16 v[106:107], v245 offset:2048\n\t" \
    "v_cvt_pk_bf16_f32 v26, v60, v61\n\t" \
    "ds_read_b64_tr_b16 v[108:109], v255\n\t" \
    "v_cvt_pk_bf16_f32 v27, v62, v63\n\t" \
    "ds_read_b64_tr_b16 v[110:111], v255 offset:2048\n\t" \
    "ds_write_b64 v243, v[24:25] offset:6144\n\t" \
    "ds_read_b128 v[80:83], v242\n\t" \
    "ds_write_b64 v251, v[26:27] offset:6144\n\t" \
    "ds_read_b128 v[84:87], v242 offset:1024\n\t" \
    "ds_write_b64 v252, v[28:29] offset:6144\n\t" \
    "ds_read_b128 v[88:91], v242 offset:4096\n\t" \
    "ds_write_b64 v253, v[30:31] offset:6144\n\t" \
    "ds_read_b128 v[92:95], v242 offset:5120\n\t" \
    "v_mov_b32 v237, 4\n\t" \
    "ds_write_b32 v248, v237\n\t" \
    "s_waitcnt lgkmcnt(14)\n\t" \
    "v_mfma_f32_16x16x32_bf16 v[64:67], v[96:99], v[232:235], 0\n\t" \
    "ds_read_b64_tr_b16 v[0:1], v244 offset:4096\n\t" \
    "ds_read_b64_tr_b16 v[2:3], v244 offset:6144\n\t" \
    "v_mfma_f32_16x16x32_bf16 v[68:71], v[100:103], v[232:235], 0\n\t" \
    "ds_read_b64_tr_b16 v[4:5], v254 offset:4096\n\t" \
    "ds_read_b64_tr_b16 v[6:7], v254 offset:6144\n\t" \
    "v_mfma_f32_16x16x32_bf16 v[32:35], v[96:99], v[112:115], 0\n\t" \
    "v_mfma_f32_16x16x32_bf16 v[40:43], v[100:103], v[112:115], 0\n\t" \
    "s_waitcnt lgkmcnt(14)\n\t" \
    "v_readfirstlane_b32 s85, v238\n\t" \
    "s_cmp_lt_u32 s85, 3\n\t" \
    "s_cbranch_scc1 .Lab_s3\n\t" \
    ".Lab_s3r:\n\t" \
    "v_mfma_f32_16x16x32_bf16 v[36:39], v[96:99], v[116:119], 0\n\t" \
    "v_mfma_f32_16x16x32_bf16 v[44:47], v[100:103], v[116:119], 0\n\t" \
    "ds_read_b32 v238, v250\n\t" \
    "ds_read_b64_tr_b16 v[8:9], v245 offset:4096\n\t" \
    "v_mfma_f32_16x16x32_bf16 v[64:67], v[104:107], v[232:235], v[64:67]\n\t" \
    "ds_read_b64_tr_b16 v[10:11], v245 offset:6144\n\t" \
    "ds_read_b64_tr_b16 v[12:13], v255 offset:4096\n\t" \
    "s_waitcnt lgkmcnt(14)\n\t" \
    "v_mfma_f32_16x16x32_bf16 v[68:71], v[108:111], v[232:235], v[68:71]\n\t" \
    "ds_read_b64_tr_b16 v[14:15], v255 offset:6144\n\t" \
    "v_mfma_f32_16x16x32_bf16 v[32:35], v[104:107], v[80:83], v[32:35]\n\t" \
    "v_mfma_f32_16x16x32_bf16 v[40:43], v[108:111], v[80:83], v[40:43]\n\t" \
    "s_waitcnt lgkmcnt(14)\n\t" \
    "v_mfma_f32_16x16x32_bf16 v[36:39], v[104:107], v[84:87], v[36:39]\n\t" \
    "v_add_u32 v253, 135168, v241\n\t" \
    "v_mfma_f32_16x16x32_bf16 v[44:47], v[108:111], v[84:87], v[44:47]\n\t" \
    "v_xor_b32 v251, 32, v253\n\t" \
    "s_waitcnt lgkmcnt(7)\n\t" \
    "v_mfma_f32_16x16x32_bf16 v[64:67], v[0:3], v[232:235], v[64:67]\n\t" \
    "v_mov_b32 v237, 5\n\t" \
    "s_waitcnt lgkmcnt(5)\n\t" \
    "v_mfma_f32_16x16x32_bf16 v[68:71], v[4:7], v[232:235], v[68:71]\n\t" \
    "v_mfma_f32_16x16x32_bf16 v[32:35], v[0:3], v[120:123], v[32:35]\n\t" \
    "v_mfma_f32_16x16x32_bf16 v[40:43], v[4:7], v[120:123], v[40:43]\n\t" \
    "v_mfma_f32_16x16x32_bf16 v[36:39], v[0:3], v[124:127], v[36:39]\n\t" \
    "s_waitcnt lgkmcnt(4)\n\t" \
    "v_readfirstlane_b32 s85, v238\n\t" \
    "s_cmp_lt_u32 s85, 4\n\t" \
    "s_cbranch_scc1 .Lab_s4\n\t" \
    ".Lab_s4r:\n\t" \
    "v_mfma_f32_16x16x32_bf16 v[44:47], v[4:7], v[124:127], v[44:47]\n\t" \
    "ds_write_b32 v248, v237\n\t" \
    "s_waitcnt lgkmcnt(3)\n\t" \
    "v_mfma_f32_16x16x32_bf16 v[64:67], v[8:11], v[232:235], v[64:67]\n\t" \
    "ds_read_b32 v238, v249 offset:92\n\t" \
    "s_waitcnt lgkmcnt(2)\n\t" \
    "v_mfma_f32_16x16x32_bf16 v[68:71], v[12:15], v[232:235], v[68:71]\n\t" \
    "v_mfma_f32_16x16x32_bf16 v[32:35], v[8:11], v[88:91], v[32:35]\n\t" \
    "v_mfma_f32_16x16x32_bf16 v[40:43], v[12:15], v[88:91], v[40:43]\n\t" \
    "v_mfma_f32_16x16x32_bf16 v[36:39], v[8:11], v[92:95], v[36:39]\n\t" \
    "v_mfma_f32_16x16x32_bf16 v[44:47], v[12:15], v[92:95], v[44:47]\n\t" \
    "ds_read_b64_tr_b16 v[80:81], v253\n\t" \
    "ds_read_b64_tr_b16 v[82:83], v253 offset:512\n\t" \
    "ds_read_b64_tr_b16 v[84:85], v251\n\t" \
    "s_nop 0\n\t" \
    "v_cndmask_b32 v24, v68, v64, vcc\n\t" \
    "v_cndmask_b32 v25, v69, v65, vcc\n\t" \
    "v_cndmask_b32 v26, v70, v66, vcc\n\t" \
    "v_cndmask_b32 v27, v71, v67, vcc\n\t" \
    "v_add3_u32 v30, v24, v25, v26\n\t" \
    "v_add_u32 v30, v30, v27\n\t" \
    "v_cvt_pk_bf16_f32 v16, v32, v33\n\t" \
    "v_cvt_pk_bf16_f32 v17, v34, v35\n\t" \
    "v_cvt_pk_bf16_f32 v18, v40, v41\n\t" \
    "v_cvt_pk_bf16_f32 v19, v42, v43\n\t" \
    "v_cvt_pk_bf16_f32 v20, v36, v37\n\t" \
    "v_cvt_pk_bf16_f32 v21, v38, v39\n\t" \
    "v_cvt_pk_bf16_f32 v22, v44, v45\n\t" \
    "v_cvt_pk_bf16_f32 v23, v46, v47\n\t" \
    "v_add3_u32 v28, v16, v17, v18\n\t" \
    "v_add3_u32 v29, v20, v21, v22\n\t" \
    "v_add_u32 v28, v28, v19\n\t" \
    "v_add_u32 v29, v29, v23\n\t" \
    "v_cndmask_b32_e64 v30, 0, v30, s[94:95]\n\t" \
    "buffer_store_dwordx4 v[16:19], v246, %[rsb], s87 offen sc1\n\t" \
    "v_permlane16_swap_b32 v28, v29\n\t" \
    "v_add3_u32 v28, v28, v29, v30\n\t" \
    "buffer_store_dwordx4 v[20:23], v246, %[rsb], s86 offen sc1\n\t" \
    "v_cndmask_b32_e64 v25, v25, v236, s[92:93]\n\t" \
    "v_add_u32_dpp v28, v28, v28 row_ror:8 row_mask:0xf bank_mask:0xf bound_ctrl:1\n\t" \
    "v_mov_b32 v237, 6\n\t" \
    "ds_read_b64_tr_b16 v[86:87], v251 offset:512\n\t" \
    "v_add_u32_dpp v28, v28, v28 row_ror:4 row_mask:0xf bank_mask:0xf bound_ctrl:1\n\t" \
    "ds_read_b64_tr_b16 v[88:89], v253 offset:4096\n\t" \
    "ds_read_b64_tr_b16 v[90:91], v253 offset:4608\n\t" \
    "v_add_u32_dpp v28, v28, v28 row_ror:2 row_mask:0xf bank_mask:0xf bound_ctrl:1\n\t" \
    "ds_read_b64_tr_b16 v[92:93], v251 offset:4096\n\t" \
    "ds_read_b64_tr_b16 v[94:95], v251 offset:4608\n\t" \
    "v_add_u32_dpp v28, v28, v28 row_ror:1 row_mask:0xf bank_mask:0xf bound_ctrl:1\n\t" \
    "v_cndmask_b32_e64 v24, v24, v28, s[92:93]\n\t" \
    "buffer_store_dwordx4 v[24:27], v247, %[rsb], s88 offen sc1\n\t" \
    "ds_write_b32 v248, v237\n\t" \
    "s_setprio 1\n\t" \
    "s_waitcnt lgkmcnt(9)\n\t" \
    "v_readfirstlane_b32 s85, v238\n\t" \
    "s_cmp_lt_u32 s85, 1\n\t" \
    "s_cbranch_scc1 .Lab_s5\n\t" \
    ".Lab_s5r:\n\t" \
    "s_waitcnt lgkmcnt(7)\n\t" \
    "v_mfma_f32_16x16x32_bf16 v[192:195], v[80:83], v[128:131], v[192:195]\n\t" \
    "v_add_u32 v244, 49152, v241\n\t" \
    "v_xor_b32 v245, 32, v244\n\t" \
    "v_mfma_f32_16x16x32_bf16 v[200:203], v[80:83], v[132:135], v[200:203]\n\t" \
    "ds_read_b64_tr_b16 v[96:97], v244\n\t" \
    "ds_read_b64_tr_b16 v[98:99], v244 offset:512\n\t" \
    "v_mfma_f32_16x16x32_bf16 v[208:211], v[80:83], v[136:139], v[208:211]\n\t" \
    "ds_read_b64_tr_b16 v[100:101], v245\n\t" \
    "ds_read_b64_tr_b16 v[102:103], v245 offset:512\n\t" \
    "v_mfma_f32_16x16x32_bf16 v[216:219], v[80:83], v[140:143], v[216:219]\n\t" \
    "ds_read_b64_tr_b16 v[104:105], v244 offset:4096\n\t" \
    "ds_read_b64_tr_b16 v[106:107], v244 offset:4608\n\t" \
    "s_waitcnt lgkmcnt(11)\n\t" \
    "v_mfma_f32_16x16x32_bf16 v[196:199], v[84:87], v[128:131], v[196:199]\n\t" \
    "ds_read_b64_tr_b16 v[108:109], v245 offset:4096\n\t" \
    "ds_read_b64_tr_b16 v[110:111], v245 offset:4608\n\t" \
    "v_mfma_f32_16x16x32_bf16 v[204:207], v[84:87], v[132:135], v[204:207]\n\t" \
    "v_xor_b32 v246, 64, v240\n\t" \
    "v_subrev_u32 v242, 16384, v241\n\t" \
    "v_mfma_f32_16x16x32_bf16 v[212:215], v[84:87], v[136:139], v[212:215]\n\t" \
    "v_xor_b32 v243, 32, v242\n\t" \
    "v_add_u32 v247, 65536, v240\n\t" \
    "v_mfma_f32_16x16x32_bf16 v[220:223], v[84:87], v[140:143], v[220:223]\n\t" \
    "v_mov_b32 v237, 7\n\t" \
    "ds_write_b32 v248, v237\n\t" \
    "s_waitcnt lgkmcnt(12)\n\t" \
    "v_mfma_f32_16x16x32_bf16 v[192:195], v[88:91], v[144:147], v[192:195]\n\t" \
    "ds_read_b64_tr_b16 v[80:81], v244 offset:8192\n\t" \
    "v_mfma_f32_16x16x32_bf16 v[200:203], v[88:91], v[148:151], v[200:203]\n\t" \
    "ds_read_b128 v[128:131], v240 offset:40960\n\t" \
    "v_mfma_f32_16x16x32_bf16 v[208:211], v[88:91], v[152:155], v[208:211]\n\t" \
    "ds_read_b64_tr_b16 v[82:83], v244 offset:8704\n\t" \
    "v_add_u32 v237, 1, v237\n\t" \
    "v_mfma_f32_16x16x32_bf16 v[216:219], v[88:91], v[156:159], v[216:219]\n\t" \
    "ds_read_b128 v[132:135], v240 offset:43008\n\t" \
    "s_waitcnt lgkmcnt(14)\n\t" \
    "v_mfma_f32_16x16x32_bf16 v[196:199], v[92:95], v[144:147], v[196:199]\n\t" \
    "ds_read_b64_tr_b16 v[84:85], v245 offset:8192\n\t" \
    "v_xor_b32 v255, 64, v247\n\t" \
    "v_mfma_f32_16x16x32_bf16 v[204:207], v[92:95], v[148:151], v[204:207]\n\t" \
    "ds_read_b128 v[136:139], v240 offset:45056\n\t" \
    "v_mfma_f32_16x16x32_bf16 v[212:215], v[92:95], v[152:155], v[212:215]\n\t" \
    "ds_read_b64_tr_b16 v[86:87], v245 offset:8704\n\t" \
    "v_mfma_f32_16x16x32_bf16 v[220:223], v[92:95], v[156:159], v[220:223]\n\t" \
    "ds_read_b128 v[140:143], v240 offset:47104\n\t" \
    "s_waitcnt lgkmcnt(14)\n\t" \
    "v_mfma_f32_16x16x32_bf16 v[192:195], v[96:99], v[160:163], v[192:195]\n\t" \
    "ds_read_b64_tr_b16 v[88:89], v244 offset:12288\n\t" \
    "v_mfma_f32_16x16x32_bf16 v[200:203], v[96:99], v[164:167], v[200:203]\n\t" \
    "ds_read_b128 v[144:147], v246 offset:40960\n\t" \
    "v_mfma_f32_16x16x32_bf16 v[208:211], v[96:99], v[168:171], v[208:211]\n\t" \
    "ds_read_b64_tr_b16 v[90:91], v244 offset:12800\n\t" \
    "v_mfma_f32_16x16x32_bf16 v[216:219], v[96:99], v[172:175], v[216:219]\n\t" \
    "ds_read_b128 v[148:151], v246 offset:43008\n\t" \
    "s_waitcnt lgkmcnt(14)\n\t" \
    "v_mfma_f32_16x16x32_bf16 v[196:199], v[100:103], v[160:163], v[196:199]\n\t" \
    "ds_read_b64_tr_b16 v[92:93], v245 offset:12288\n\t" \
    "v_mfma_f32_16x16x32_bf16 v[204:207], v[100:103], v[164:167], v[204:207]\n\t" \
    "ds_read_b128 v[152:155], v246 offset:45056\n\t" \
    "v_mfma_f32_16x16x32_bf16 v[212:215], v[100:103], v[168:171], v[212:215]\n\t" \
    "ds_read_b64_tr_b16 v[94:95], v245 offset:12800\n\t" \
    "v_mfma_f32_16x16x32_bf16 v[220:223], v[100:103], v[172:175], v[220:223]\n\t" \
    "ds_read_b128 v[156:159], v246 offset:47104\n\t" \
    "ds_write_b32 v248, v237\n\t" \
    "v_mfma_f32_16x16x32_bf16 v[192:195], v[104:107], v[176:179], v[192:195]\n\t" \
    "ds_read_b64_tr_b16 v[96:97], v242 offset:24576\n\t" \
    "v_mfma_f32_16x16x32_bf16 v[200:203], v[104:107], v[180:183], v[200:203]\n\t" \
    "ds_read_b128 v[160:163], v247 offset:16384\n\t" \
    "v_mfma_f32_16x16x32_bf16 v[208:211], v[104:107], v[184:187], v[208:211]\n\t" \
    "ds_read_b64_tr_b16 v[98:99], v242 offset:25088\n\t" \
    "v_add_u32 v237, 1, v237\n\t" \
    "v_mfma_f32_16x16x32_bf16 v[216:219], v[104:107], v[188:191], v[216:219]\n\t" \
    "ds_read_b128 v[164:167], v247 offset:18432\n\t" \
    "s_waitcnt lgkmcnt(14)\n\t" \
    "v_mfma_f32_16x16x32_bf16 v[196:199], v[108:111], v[176:179], v[196:199]\n\t" \
    "ds_read_b64_tr_b16 v[100:101], v243 offset:24576\n\t" \
    "v_mfma_f32_16x16x32_bf16 v[204:207], v[108:111], v[180:183], v[204:207]\n\t" \
    "ds_read_b128 v[168:171], v247 offset:20480\n\t" \
    "v_mfma_f32_16x16x32_bf16 v[212:215], v[108:111], v[184:187], v[212:215]\n\t" \
    "ds_read_b64_tr_b16 v[102:103], v243 offset:25088\n\t" \
    "v_mfma_f32_16x16x32_bf16 v[220:223], v[108:111], v[188:191], v[220:223]\n\t" \
    "ds_read_b128 v[172:175], v247 offset:22528\n\t" \
    "v_mfma_f32_16x16x32_bf16 v[192:195], v[80:83], v[128:131], v[192:195]\n\t" \
    "ds_read_b64_tr_b16 v[104:105], v242 offset:28672\n\t" \
    "v_mfma_f32_16x16x32_bf16 v[200:203], v[80:83], v[132:135], v[200:203]\n\t" \
    "ds_read_b128 v[176:179], v255 offset:16384\n\t" \
    "v_mfma_f32_16x16x32_bf16 v[208:211], v[80:83], v[136:139], v[208:211]\n\t" \
    "ds_read_b64_tr_b16 v[106:107], v242 offset:29184\n\t" \
    "s_waitcnt lgkmcnt(14)\n\t" \
    "v_mfma_f32_16x16x32_bf16 v[216:219], v[80:83], v[140:143], v[216:219]\n\t" \
    "ds_read_b128 v[180:183], v255 offset:18432\n\t" \
    "v_mfma_f32_16x16x32_bf16 v[196:199], v[84:87], v[128:131], v[196:199]\n\t" \
    "ds_read_b64_tr_b16 v[108:109], v243 offset:28672\n\t" \
    "v_mfma_f32_16x16x32_bf16 v[204:207], v[84:87], v[132:135], v[204:207]\n\t" \
    "ds_read_b128 v[184:187], v255 offset:20480\n\t" \
    "v_mfma_f32_16x16x32_bf16 v[212:215], v[84:87], v[136:139], v[212:215]\n\t" \
    "ds_read_b64_tr_b16 v[110:111], v243 offset:29184\n\t" \
    "v_mfma_f32_16x16x32_bf16 v[220:223], v[84:87], v[140:143], v[220:223]\n\t" \
    "ds_read_b128 v[188:191], v255 offset:22528\n\t" \
    "ds_write_b32 v248, v237\n\t" \
    "v_mfma_f32_16x16x32_bf16 v[192:195], v[88:91], v[144:147], v[192:195]\n\t" \
    "ds_read_b64_tr_b16 v[80:81], v242 offset:32768\n\t" \
    "v_mfma_f32_16x16x32_bf16 v[200:203], v[88:91], v[148:151], v[200:203]\n\t" \
    "ds_read_b128 v[128:131], v240\n\t" \
    "v_mfma_f32_16x16x32_bf16 v[208:211], v[88:91], v[152:155], v[208:211]\n\t" \
    "ds_read_b64_tr_b16 v[82:83], v242 offset:33280\n\t" \
    "v_add_u32 v237, 1, v237\n\t" \
    "s_waitcnt lgkmcnt(14)\n\t" \
    "v_mfma_f32_16x16x32_bf16 v[216:219], v[88:91], v[156:159], v[216:219]\n\t" \
    "ds_read_b128 v[132:135], v240 offset:2048\n\t" \
    "v_mfma_f32_16x16x32_bf16 v[196:199], v[92:95], v[144:147], v[196:199]\n\t" \
    "ds_read_b64_tr_b16 v[84:85], v243 offset:32768\n\t" \
    "v_mfma_f32_16x16x32_bf16 v[204:207], v[92:95], v[148:151], v[204:207]\n\t" \
    "ds_read_b128 v[136:139], v240 offset:4096\n\t" \
    "v_mfma_f32_16x16x32_bf16 v[212:215], v[92:95], v[152:155], v[212:215]\n\t" \
    "ds_read_b64_tr_b16 v[86:87], v243 offset:33280\n\t" \
    "ds_read_b32 v238, v249 offset:92\n\t" \
    "v_mfma_f32_16x16x32_bf16 v[220:223], v[92:95], v[156:159], v[220:223]\n\t" \
    "ds_read_b128 v[140:143], v240 offset:6144\n\t" \
    "v_mfma_f32_16x16x32_bf16 v[192:195], v[96:99], v[160:163], v[192:195]\n\t" \
    "ds_read_b64_tr_b16 v[88:89], v242 offset:36864\n\t" \
    "v_add_u32 v247, %[xs0], v240\n\t" \
    "v_mfma_f32_16x16x32_bf16 v[200:203], v[96:99], v[164:167], v[200:203]\n\t" \
    "ds_read_b128 v[144:147], v246\n\t" \
    "v_mfma_f32_16x16x32_bf16 v[208:211], v[96:99], v[168:171], v[208:211]\n\t" \
    "ds_read_b64_tr_b16 v[90:91], v242 offset:37376\n\t" \
    "v_xor_b32 v255, 64, v247\n\t" \
    "s_waitcnt lgkmcnt(14)\n\t" \
    "v_mfma_f32_16x16x32_bf16 v[216:219], v[96:99], v[172:175], v[216:219]\n\t" \
    "ds_read_b128 v[148:151], v246 offset:2048\n\t" \
    "v_mfma_f32_16x16x32_bf16 v[196:199], v[100:103], v[160:163], v[196:199]\n\t" \
    "ds_read_b64_tr_b16 v[92:93], v243 offset:36864\n\t" \
    "v_mfma_f32_16x16x32_bf16 v[204:207], v[100:103], v[164:167], v[204:207]\n\t" \
    "ds_read_b128 v[152:155], v246 offset:4096\n\t" \
    "v_mfma_f32_16x16x32_bf16 v[212:215], v[100:103], v[168:171], v[212:215]\n\t" \
    "ds_read_b64_tr_b16 v[94:95], v243 offset:37376\n\t" \
    "v_mfma_f32_16x16x32_bf16 v[220:223], v[100:103], v[172:175], v[220:223]\n\t" \
    "ds_read_b128 v[156:159], v246 offset:6144\n\t" \
    "ds_write_b32 v248, v237\n\t" \
    "v_mfma_f32_16x16x32_bf16 v[192:195], v[104:107], v[176:179], v[192:195]\n\t" \
    "s_waitcnt lgkmcnt(10)\n\t" \
    "v_readfirstlane_b32 s85, v238\n\t" \
    "s_cmp_lt_u32 s85, 2\n\t" \
    "s_cbranch_scc1 .Lab_s6\n\t" \
    ".Lab_s6r:\n\t" \
    "ds_read_b64_tr_b16 v[96:97], v244 offset:32768\n\t" \
    "v_mfma_f32_16x16x32_bf16 v[200:203], v[104:107], v[180:183], v[200:203]\n\t" \
    "ds_read_b128 v[160:163], v247\n\t" \
    "v_mfma_f32_16x16x32_bf16 v[208:211], v[104:107], v[184:187], v[208:211]\n\t" \
    "ds_read_b64_tr_b16 v[98:99], v244 offset:33280\n\t" \
    "v_add_u32 v237, 1, v237\n\t" \
    "v_mfma_f32_16x16x32_bf16 v[216:219], v[104:107], v[188:191], v[216:219]\n\t" \
    "ds_read_b128 v[164:167], v247 offset:2048\n\t" \
    "v_mfma_f32_16x16x32_bf16 v[196:199], v[108:111], v[176:179], v[196:199]\n\t" \
    "ds_read_b64_tr_b16 v[100:101], v245 offset:32768\n\t" \
    "v_mfma_f32_16x16x32_bf16 v[204:207], v[108:111], v[180:183], v[204:207]\n\t" \
    "ds_read_b128 v[168:171], v247 offset:4096\n\t" \
    "v_mfma_f32_16x16x32_bf16 v[212:215], v[108:111], v[184:187], v[212:215]\n\t" \
    "ds_read_b64_tr_b16 v[102:103], v245 offset:33280\n\t" \
    "v_mfma_f32_16x16x32_bf16 v[220:223], v[108:111], v[188:191], v[220:223]\n\t" \
    "ds_read_b128 v[172:175], v247 offset:6144\n\t" \
    "v_mfma_f32_16x16x32_bf16 v[192:195], v[80:83], v[128:131], v[192:195]\n\t" \
    "ds_read_b64_tr_b16 v[104:105], v244 offset:36864\n\t" \
    "v_mfma_f32_16x16x32_bf16 v[200:203], v[80:83], v[132:135], v[200:203]\n\t" \
    "ds_read_b128 v[176:179], v255\n\t" \
    "v_mfma_f32_16x16x32_bf16 v[208:211], v[80:83], v[136:139], v[208:211]\n\t" \
    "ds_read_b64_tr_b16 v[106:107], v244 offset:37376\n\t" \
    "s_waitcnt lgkmcnt(14)\n\t" \
    "v_mfma_f32_16x16x32_bf16 v[216:219], v[80:83], v[140:143], v[216:219]\n\t" \
    "ds_read_b128 v[180:183], v255 offset:2048\n\t" \
    "v_mfma_f32_16x16x32_bf16 v[196:199], v[84:87], v[128:131], v[196:199]\n\t" \
    "ds_read_b64_tr_b16 v[108:109], v245 offset:36864\n\t" \
    "v_mfma_f32_16x16x32_bf16 v[204:207], v[84:87], v[132:135], v[204:207]\n\t" \
    "ds_read_b128 v[184:187], v255 offset:4096\n\t" \
    "v_mfma_f32_16x16x32_bf16 v[212:215], v[84:87], v[136:139], v[212:215]\n\t" \
    "ds_read_b64_tr_b16 v[110:111], v245 offset:37376\n\t" \
    "v_mfma_f32_16x16x32_bf16 v[220:223], v[84:87], v[140:143], v[220:223]\n\t" \
    "ds_read_b128 v[188:191], v255 offset:6144\n\t" \
    "ds_write_b32 v248, v237\n\t" \
    "v_mfma_f32_16x16x32_bf16 v[192:195], v[88:91], v[144:147], v[192:195]\n\t" \
    "v_mbcnt_lo_u32_b32 v250, -1, 0\n\t" \
    "v_mbcnt_hi_u32_b32 v250, -1, v250\n\t" \
    "v_mfma_f32_16x16x32_bf16 v[200:203], v[88:91], v[148:151], v[200:203]\n\t" \
    "v_and_b32 v238, 15, v250\n\t" \
    "v_lshlrev_b32 v238, 3, v238\n\t" \
    "v_mfma_f32_16x16x32_bf16 v[208:211], v[88:91], v[152:155], v[208:211]\n\t" \
    "v_add_u32 v237, 1, v237\n\t" \
    "s_waitcnt lgkmcnt(14)\n\t" \
    "v_mfma_f32_16x16x32_bf16 v[216:219], v[88:91], v[156:159], v[216:219]\n\t" \
    "buffer_load_dwordx2 v[232:233], v238, %[rsw], %[sg] offen sc1\n\t" \
    "v_mfma_f32_16x16x32_bf16 v[196:199], v[92:95], v[144:147], v[196:199]\n\t" \
    "v_lshlrev_b32 v251, 4, v250\n\t" \
    "s_add_u32 s85, %[sof], %[ofc]\n\t" \
    "v_mfma_f32_16x16x32_bf16 v[204:207], v[92:95], v[148:151], v[204:207]\n\t" \
    "v_add_u32 v240, s85, v251\n\t" \
    "s_add_u32 s87, %[sof], %[ofp]\n\t" \
    "v_mfma_f32_16x16x32_bf16 v[212:215], v[92:95], v[152:155], v[212:215]\n\t" \
    "buffer_load_dwordx2 v[234:235], v238, %[rsw], %[sg] offen offset:128 sc1\n\t" \
    "v_mfma_f32_16x16x32_bf16 v[220:223], v[92:95], v[156:159], v[220:223]\n\t" \
    "v_add_u32 v241, 0x1000, v240\n\t" \
    "s_add_u32 s88, %[sof], %[ogp]\n\t" \
    "v_mfma_f32_16x16x32_bf16 v[192:195], v[96:99], v[160:163], v[192:195]\n\t" \
    "s_mov_b32 s86, 0x10000\n\t" \
    "v_add_u32 v242, 0x2000, v240\n\t" \
    "v_add_u32 v243, 0x3000, v240\n\t" \
    "s_waitcnt lgkmcnt(10)\n\t" \
    "v_mfma_f32_16x16x32_bf16 v[196:199], v[100:103], v[160:163], v[196:199]\n\t" \
    "v_bfe_u32 v244, v251, 1, 8\n\t" \
    "v_add_u32 v253, %[kac], v251\n\t" \
    "s_waitcnt lgkmcnt(6)\n\t" \
    "v_mfma_f32_16x16x32_bf16 v[192:195], v[104:107], v[176:179], v[192:195]\n\t" \
    "v_lshrrev_b32 v112, 2, v250\n\t" \
    "v_bfe_u32 v113, v250, 4, 1\n\t" \
    "s_waitcnt lgkmcnt(2)\n\t" \
    "v_mfma_f32_16x16x32_bf16 v[196:199], v[108:111], v[176:179], v[196:199]\n\t" \
    "v_lshlrev_b32 v245, 7, v112\n\t" \
    "v_bitop3_b32 v112, v112, v113, 3 bitop3:0x28\n\t" \
    "v_mfma_f32_16x16x32_bf16 v[200:203], v[96:99], v[164:167], v[200:203]\n\t" \
    "v_lshl_or_b32 v245, v112, 5, v245\n\t" \
    "v_and_b32 v112, 24, v238\n\t" \
    "v_mfma_f32_16x16x32_bf16 v[204:207], v[100:103], v[164:167], v[204:207]\n\t" \
    "v_or3_b32 v245, v245, v112, s86\n\t" \
    "v_lshlrev_b32 v247, 8, v238\n\t" \
    "v_mfma_f32_16x16x32_bf16 v[200:203], v[104:107], v[180:183], v[200:203]\n\t" \
    "v_lshl_or_b32 v247, v113, 5, v247\n\t" \
    "v_lshrrev_b32 v112, 5, v250\n\t" \
    "v_mfma_f32_16x16x32_bf16 v[204:207], v[108:111], v[180:183], v[204:207]\n\t" \
    "v_lshl_or_b32 v247, v112, 4, v247\n\t" \
    "v_and_b32 v252, 48, v247\n\t" \
    "v_mfma_f32_16x16x32_bf16 v[208:211], v[96:99], v[168:171], v[208:211]\n\t" \
    "v_lshl_add_u32 v252, v252, 2, %[zac]\n\t" \
    "v_lshlrev_b32 v246, 4, v238\n\t" \
    "v_mfma_f32_16x16x32_bf16 v[212:215], v[100:103], v[168:171], v[212:215]\n\t" \
    "v_cvt_pk_bf16_f32 v64, v192, v193\n\t" \
    "v_cvt_pk_bf16_f32 v65, v194, v195\n\t" \
    "v_mfma_f32_16x16x32_bf16 v[208:211], v[104:107], v[184:187], v[208:211]\n\t" \
    "v_cvt_pk_bf16_f32 v66, v196, v197\n\t" \
    "v_cvt_pk_bf16_f32 v67, v198, v199\n\t" \
    "v_mfma_f32_16x16x32_bf16 v[212:215], v[108:111], v[184:187], v[212:215]\n\t" \
    "v_lshl_or_b32 v246, v113, 3, v246\n\t" \
    "v_lshrrev_b32 v113, 4, v238\n\t" \
    "v_mfma_f32_16x16x32_bf16 v[216:219], v[96:99], v[172:175], v[216:219]\n\t" \
    "v_cvt_pk_bf16_f32 v68, v200, v201\n\t" \
    "v_cvt_pk_bf16_f32 v69, v202, v203\n\t" \
    "v_mfma_f32_16x16x32_bf16 v[220:223], v[100:103], v[172:175], v[220:223]\n\t" \
    "v_cvt_pk_bf16_f32 v70, v204, v205\n\t" \
    "v_cvt_pk_bf16_f32 v71, v206, v207\n\t" \
    "s_waitcnt lgkmcnt(1)\n\t" \
    "v_mfma_f32_16x16x32_bf16 v[216:219], v[104:107], v[188:191], v[216:219]\n\t" \
    "v_xor_b32 v112, v112, v113\n\t" \
    "v_lshl_or_b32 v246, v112, 4, v246\n\t" \
    "v_mfma_f32_16x16x32_bf16 v[220:223], v[108:111], v[188:191], v[220:223]\n\t" \
    "v_add_u32 v246, %[xec], v246\n\t" \
    "v_mfma_f32_16x16x32_bf16 v[0:3], v[224:227], v[64:67], 0\n\t" \
    "v_cvt_pk_bf16_f32 v72, v208, v209\n\t" \
    "v_cvt_pk_bf16_f32 v73, v210, v211\n\t" \
    "v_mfma_f32_16x16x32_bf16 v[4:7], v[228:231], v[64:67], 0\n\t" \
    "v_cvt_pk_bf16_f32 v74, v212, v213\n\t" \
    "v_cvt_pk_bf16_f32 v75, v214, v215\n\t" \
    "v_mfma_f32_16x16x32_bf16 v[8:11], v[224:227], v[68:71], 0\n\t" \
    "v_mov_b32 v120, 0x358637bd\n\t" \
    "v_mov_b32 v121, 0x358637bd\n\t" \
    "v_mfma_f32_16x16x32_bf16 v[12:15], v[228:231], v[68:71], 0\n\t" \
    "v_mov_b32 v122, 0x358637bd\n\t" \
    "v_mov_b32 v123, 0x358637bd\n\t" \
    "v_mfma_f32_16x16x32_bf16 v[16:19], v[224:227], v[72:75], 0\n\t" \
    "v_cvt_pk_bf16_f32 v76, v216, v217\n\t" \
    "v_cvt_pk_bf16_f32 v77, v218, v219\n\t" \
    "v_mfma_f32_16x16x32_bf16 v[20:23], v[228:231], v[72:75], 0\n\t" \
    "v_cvt_pk_bf16_f32 v78, v220, v221\n\t" \
    "v_cvt_pk_bf16_f32 v79, v222, v223\n\t" \
    "s_nop 1\n\t" \
    "v_mfma_f32_16x16x32_bf16 v[24:27], v[224:227], v[76:79], 0\n\t" \
    "v_mfma_f32_16x16x32_bf16 v[28:31], v[228:231], v[76:79], 0\n\t" \
    "v_max3_f32 v64, v0, v1, v2\n\t" \
    "v_max3_f32 v66, -v0, -v1, -v2\n\t" \
    "v_max3_f32 v68, v8, v9, v10\n\t" \
    "v_max3_f32 v64, v64, v3, v4\n\t" \
    "v_max3_f32 v66, v66, -v3, -v4\n\t" \
    "v_max3_f32 v68, v68, v11, v12\n\t" \
    "v_max3_f32 v64, v64, v5, v6\n\t" \
    "v_max3_f32 v66, v66, -v5, -v6\n\t" \
    "v_max3_f32 v68, v68, v13, v14\n\t" \
    "v_max_f32_e64 v64, v64, v7\n\t" \
    "v_max_f32_e64 v66, v66, -v7\n\t" \
    "v_max_f32_e64 v68, v68, v15\n\t" \
    "v_max3_f32 v70, -v8, -v9, -v10\n\t" \
    "v_permlane32_swap_b32 v64, v66\n\t" \
    "v_max3_f32 v72, v16, v17, v18\n\t" \
    "v_max3_f32 v70, v70, -v11, -v12\n\t" \
    "v_max_f32 v64, v64, v66\n\t" \
    "v_max3_f32 v72, v72, v19, v20\n\t" \
    "v_max3_f32 v70, v70, -v13, -v14\n\t" \
    "v_max3_f32 v74, -v16, -v17, -v18\n\t" \
    "v_max3_f32 v72, v72, v21, v22\n\t" \
    "v_max_f32_e64 v70, v70, -v15\n\t" \
    "v_max3_f32 v74, v74, -v19, -v20\n\t" \
    "v_max_f32_e64 v72, v72, v23\n\t" \
    "v_permlane32_swap_b32 v68, v70\n\t" \
    "v_max3_f32 v74, v74, -v21, -v22\n\t" \
    "v_max3_f32 v76, v24, v25, v26\n\t" \
    "v_max_f32 v68, v68, v70\n\t" \
    "v_max_f32_e64 v74, v74, -v23\n\t" \
    "v_max3_f32 v76, v76, v27, v28\n\t" \
    "v_permlane16_swap_b32 v64, v68\n\t" \
    "v_permlane32_swap_b32 v72, v74\n\t" \
    "v_max3_f32 v76, v76, v29, v30\n\t" \
    "v_max_f32 v66, v64, v68\n\t" \
    "v_max_f32 v72, v72, v74\n\t" \
    "v_max_f32_e64 v76, v76, v31\n\t" \
    ".Lpad_ab2:\n\t" \
    "s_cbranch_execz .Lpad_ret\n\t" \
    "v_mul_f32 v64, 0xbfb8aa3b, v66\n\t" \
    "v_mul_f32 v68, 0xbfb8aa3b, v66\n\t" \
    "v_max3_f32 v78, -v24, -v25, -v26\n\t" \
    "v_max3_f32 v78, v78, -v27, -v28\n\t" \
    "v_permlane16_swap_b32 v64, v68\n\t" \
    "v_mov_b32 v66, v64\n\t" \
    "v_max3_f32 v78, v78, -v29, -v30\n\t" \
    "v_mov_b32 v70, v68\n\t" \
    "v_permlane32_swap_b32 v64, v66\n\t" \
    "v_max_f32_e64 v78, v78, -v31\n\t" \
    "v_permlane32_swap_b32 v68, v70\n\t" \
    "v_pk_fma_f32 v[32:33], v[0:1], s[90:91], v[64:65] op_sel_hi:[1,1,0]\n\t" \
    "v_permlane32_swap_b32 v76, v78\n\t" \
    "v_pk_fma_f32 v[0:1], v[0:1], s[90:91], v[66:67] op_sel_hi:[1,1,0] neg_lo:[1,0,0] neg_hi:[1,0,0]\n\t" \
    "v_pk_fma_f32 v[34:35], v[2:3], s[90:91], v[64:65] op_sel_hi:[1,1,0]\n\t" \
    "v_max_f32 v76, v76, v78\n\t" \
    "v_pk_fma_f32 v[2:3], v[2:3], s[90:91], v[66:67] op_sel_hi:[1,1,0] neg_lo:[1,0,0] neg_hi:[1,0,0]\n\t" \
    "v_pk_fma_f32 v[36:37], v[4:5], s[90:91], v[64:65] op_sel_hi:[1,1,0]\n\t" \
    "v_permlane16_swap_b32 v72, v76\n\t" \
    "v_pk_fma_f32 v[4:5], v[4:5], s[90:91], v[66:67] op_sel_hi:[1,1,0] neg_lo:[1,0,0] neg_hi:[1,0,0]\n\t" \
    "v_pk_fma_f32 v[38:39], v[6:7], s[90:91], v[64:65] op_sel_hi:[1,1,0]\n\t" \
    "v_max_f32 v74, v72, v76\n\t" \
    "v_pk_fma_f32 v[6:7], v[6:7], s[90:91], v[66:67] op_sel_hi:[1,1,0] neg_lo:[1,0,0] neg_hi:[1,0,0]\n\t" \
    "v_exp_f32 v32, v32\n\t" \
    "v_mul_f32 v72, 0xbfb8aa3b, v74\n\t" \
    "v_mul_f32 v76, 0xbfb8aa3b, v74\n\t" \
    "v_exp_f32 v0, v0\n\t" \
    "v_exp_f32 v33, v33\n\t" \
    "v_permlane16_swap_b32 v72, v76\n\t" \
    "v_exp_f32 v1, v1\n\t" \
    "v_exp_f32 v34, v34\n\t" \
    "v_mov_b32 v74, v72\n\t" \
    "v_mov_b32 v78, v76\n\t" \
    "v_exp_f32 v2, v2\n\t" \
    "v_permlane32_swap_b32 v72, v74\n\t" \
    "v_permlane32_swap_b32 v76, v78\n\t" \
    "v_exp_f32 v35, v35\n\t" \
    "v_exp_f32 v3, v3\n\t" \
    "v_exp_f32 v36, v36\n\t" \
    "v_pk_add_f32 v[64:65], v[32:33], v[34:35]\n\t" \
    "v_pk_add_f32 v[66:67], v[0:1], v[2:3]\n\t" \
    "v_exp_f32 v4, v4\n\t" \
    "v_exp_f32 v37, v37\n\t" \
    "v_exp_f32 v5, v5\n\t" \
    "v_exp_f32 v38, v38\n\t" \
    "v_pk_add_f32 v[64:65], v[64:65], v[36:37]\n\t" \
    "v_pk_add_f32 v[66:67], v[66:67], v[4:5]\n\t" \
    "v_exp_f32 v6, v6\n\t" \
    "v_exp_f32 v39, v39\n\t" \
    "v_exp_f32 v7, v7\n\t" \
    "v_pk_fma_f32 v[40:41], v[8:9], s[90:91], v[68:69] op_sel_hi:[1,1,0]\n\t" \
    "v_pk_add_f32 v[64:65], v[64:65], v[38:39]\n\t" \
    "v_pk_add_f32 v[66:67], v[66:67], v[6:7]\n\t" \
    "v_pk_fma_f32 v[8:9], v[8:9], s[90:91], v[70:71] op_sel_hi:[1,1,0] neg_lo:[1,0,0] neg_hi:[1,0,0]\n\t" \
    "v_add_f32 v64, v64, v65\n\t" \
    "v_add_f32 v66, v66, v67\n\t" \
    "v_pk_fma_f32 v[42:43], v[10:11], s[90:91], v[68:69] op_sel_hi:[1,1,0]\n\t" \
    "v_pk_fma_f32 v[10:11], v[10:11], s[90:91], v[70:71] op_sel_hi:[1,1,0] neg_lo:[1,0,0] neg_hi:[1,0,0]\n\t" \
    "v_permlane32_swap_b32 v64, v66\n\t" \
    "v_pk_fma_f32 v[44:45], v[12:13], s[90:91], v[68:69] op_sel_hi:[1,1,0]\n\t" \
    "v_pk_fma_f32 v[12:13], v[12:13], s[90:91], v[70:71] op_sel_hi:[1,1,0] neg_lo:[1,0,0] neg_hi:[1,0,0]\n\t" \
    "v_add_f32 v64, v64, v66\n\t" \
    "v_pk_fma_f32 v[46:47], v[14:15], s[90:91], v[68:69] op_sel_hi:[1,1,0]\n\t" \
    "v_pk_fma_f32 v[14:15], v[14:15], s[90:91], v[70:71] op_sel_hi:[1,1,0] neg_lo:[1,0,0] neg_hi:[1,0,0]\n\t" \
    "v_exp_f32 v40, v40\n\t" \
    "v_exp_f32 v8, v8\n\t" \
    "v_exp_f32 v41, v41\n\t" \
    "v_exp_f32 v9, v9\n\t" \
    "v_exp_f32 v42, v42\n\t" \
    "v_exp_f32 v10, v10\n\t" \
    "v_exp_f32 v43, v43\n\t" \
    "v_exp_f32 v11, v11\n\t" \
    "v_exp_f32 v44, v44\n\t" \
    "v_pk_add_f32 v[68:69], v[40:41], v[42:43]\n\t" \
    "v_pk_add_f32 v[70:71], v[8:9], v[10:11]\n\t" \
    "v_exp_f32 v12, v12\n\t" \
    "v_exp_f32 v45, v45\n\t" \
    "v_exp_f32 v13, v13\n\t" \
    "v_exp_f32 v46, v46\n\t" \
    "v_pk_add_f32 v[68:69], v[68:69], v[44:45]\n\t" \
    "v_pk_add_f32 v[70:71], v[70:71], v[12:13]\n\t" \
    "v_exp_f32 v14, v14\n\t" \
    "v_exp_f32 v47, v47\n\t" \
    "v_exp_f32 v15, v15\n\t" \
    "v_pk_fma_f32 v[48:49], v[16:17], s[90:91], v[72:73] op_sel_hi:[1,1,0]\n\t" \
    "v_pk_add_f32 v[68:69], v[68:69], v[46:47]\n\t" \
    "v_pk_add_f32 v[70:71], v[70:71], v[14:15]\n\t" \
    "v_pk_fma_f32 v[16:17], v[16:17], s[90:91], v[74:75] op_sel_hi:[1,1,0] neg_lo:[1,0,0] neg_hi:[1,0,0]\n\t" \
    "v_add_f32 v68, v68, v69\n\t" \
    "v_add_f32 v70, v70, v71\n\t" \
    "v_pk_fma_f32 v[50:51], v[18:19], s[90:91], v[72:73] op_sel_hi:[1,1,0]\n\t" \
    "v_pk_fma_f32 v[18:19], v[18:19], s[90:91], v[74:75] op_sel_hi:[1,1,0] neg_lo:[1,0,0] neg_hi:[1,0,0]\n\t" \
    "v_permlane32_swap_b32 v68, v70\n\t" \
    "v_pk_fma_f32 v[52:53], v[20:21], s[90:91], v[72:73] op_sel_hi:[1,1,0]\n\t" \
    "v_pk_fma_f32 v[20:21], v[20:21], s[90:91], v[74:75] op_sel_hi:[1,1,0] neg_lo:[1,0,0] neg_hi:[1,0,0]\n\t" \
    "v_add_f32 v68, v68, v70\n\t" \
    "v_pk_fma_f32 v[54:55], v[22:23], s[90:91], v[72:73] op_sel_hi:[1,1,0]\n\t" \
    "v_pk_fma_f32 v[22:23], v[22:23], s[90:91], v[74:75] op_sel_hi:[1,1,0] neg_lo:[1,0,0] neg_hi:[1,0,0]\n\t" \
    "v_permlane16_swap_b32 v64, v68\n\t" \
    "v_exp_f32 v48, v48\n\t" \
    "v_exp_f32 v16, v16\n\t" \
    "v_add_f32 v66, v64, v68\n\t" \
    "v_exp_f32 v49, v49\n\t" \
    "v_exp_f32 v17, v17\n\t" \
    "v_rcp_f32 v64, v66\n\t" \
    "v_exp_f32 v50, v50\n\t" \
    "v_exp_f32 v18, v18\n\t" \
    "v_mov_b32 v68, v64\n\t" \
    "v_exp_f32 v51, v51\n\t" \
    "v_exp_f32 v19, v19\n\t" \
    "v_permlane16_swap_b32 v64, v68\n\t" \
    "v_pk_add_f32 v[72:73], v[48:49], v[50:51]\n\t" \
    "v_pk_add_f32 v[74:75], v[16:17], v[18:19]\n\t" \
    "v_mov_b32 v66, v64\n\t" \
    "v_mov_b32 v70, v68\n\t" \
    "v_exp_f32 v52, v52\n\t" \
    "v_permlane32_swap_b32 v64, v66\n\t" \
    "v_permlane32_swap_b32 v68, v70\n\t" \
    "v_exp_f32 v20, v20\n\t" \
    "v_exp_f32 v53, v53\n\t" \
    "v_exp_f32 v21, v21\n\t" \
    "v_exp_f32 v54, v54\n\t" \
    "v_pk_add_f32 v[72:73], v[72:73], v[52:53]\n\t" \
    "v_pk_add_f32 v[74:75], v[74:75], v[20:21]\n\t" \
    "v_exp_f32 v22, v22\n\t" \
    "v_exp_f32 v55, v55\n\t" \
    "v_exp_f32 v23, v23\n\t" \
    "v_pk_fma_f32 v[56:57], v[24:25], s[90:91], v[76:77] op_sel_hi:[1,1,0]\n\t" \
    "v_pk_add_f32 v[72:73], v[72:73], v[54:55]\n\t" \
    "v_pk_add_f32 v[74:75], v[74:75], v[22:23]\n\t" \
    "v_pk_fma_f32 v[24:25], v[24:25], s[90:91], v[78:79] op_sel_hi:[1,1,0] neg_lo:[1,0,0] neg_hi:[1,0,0]\n\t" \
    "v_add_f32 v72, v72, v73\n\t" \
    "v_add_f32 v74, v74, v75\n\t" \
    "v_pk_fma_f32 v[58:59], v[26:27], s[90:91], v[76:77] op_sel_hi:[1,1,0]\n\t" \
    "v_pk_fma_f32 v[26:27], v[26:27], s[90:91], v[78:79] op_sel_hi:[1,1,0] neg_lo:[1,0,0] neg_hi:[1,0,0]\n\t" \
    "v_permlane32_swap_b32 v72, v74\n\t" \
    "v_pk_fma_f32 v[60:61], v[28:29], s[90:91], v[76:77] op_sel_hi:[1,1,0]\n\t" \
    "v_pk_fma_f32 v[28:29], v[28:29], s[90:91], v[78:79] op_sel_hi:[1,1,0] neg_lo:[1,0,0] neg_hi:[1,0,0]\n\t" \
    "v_add_f32 v72, v72, v74\n\t" \
    "v_pk_fma_f32 v[62:63], v[30:31], s[90:91], v[76:77] op_sel_hi:[1,1,0]\n\t" \
    "v_pk_fma_f32 v[30:31], v[30:31], s[90:91], v[78:79] op_sel_hi:[1,1,0] neg_lo:[1,0,0] neg_hi:[1,0,0]\n\t" \
    "v_exp_f32 v56, v56\n\t" \
    "v_exp_f32 v24, v24\n\t" \
    "v_exp_f32 v57, v57\n\t" \
    "v_exp_f32 v25, v25\n\t" \
    "v_exp_f32 v58, v58\n\t" \
    "v_exp_f32 v26, v26\n\t" \
    "v_exp_f32 v59, v59\n\t" \
    "v_exp_f32 v27, v27\n\t" \
    "v_exp_f32 v60, v60\n\t" \
    "v_pk_add_f32 v[76:77], v[56:57], v[58:59]\n\t" \
    "v_pk_add_f32 v[78:79], v[24:25], v[26:27]\n\t" \
    "v_exp_f32 v28, v28\n\t" \
    "v_exp_f32 v61, v61\n\t" \
    "v_exp_f32 v29, v29\n\t" \
    "v_exp_f32 v62, v62\n\t" \
    "v_pk_add_f32 v[76:77], v[76:77], v[60:61]\n\t" \
    "v_pk_add_f32 v[78:79], v[78:79], v[28:29]\n\t" \
    "v_exp_f32 v30, v30\n\t" \
    "v_exp_f32 v63, v63\n\t" \
    "v_exp_f32 v31, v31\n\t" \
    "v_pk_mul_f32 v[0:1], v[0:1], v[66:67] op_sel_hi:[1,0]\n\t" \
    "v_pk_add_f32 v[76:77], v[76:77], v[62:63]\n\t" \
    "v_pk_add_f32 v[78:79], v[78:79], v[30:31]\n\t" \
    "v_pk_mul_f32 v[32:33], v[32:33], v[64:65] op_sel_hi:[1,0]\n\t" \
    "v_add_f32 v76, v76, v77\n\t" \
    "v_add_f32 v78, v78, v79\n\t" \
    "v_pk_mul_f32 v[2:3], v[2:3], v[66:67] op_sel_hi:[1,0]\n\t" \
    "v_pk_mul_f32 v[34:35], v[34:35], v[64:65] op_sel_hi:[1,0]\n\t" \
    "v_permlane32_swap_b32 v76, v78\n\t" \
    "v_pk_mul_f32 v[4:5], v[4:5], v[66:67] op_sel_hi:[1,0]\n\t" \
    "v_pk_mul_f32 v[36:37], v[36:37], v[64:65] op_sel_hi:[1,0]\n\t" \
    "v_add_f32 v76, v76, v78\n\t" \
    "v_pk_mul_f32 v[6:7], v[6:7], v[66:67] op_sel_hi:[1,0]\n\t" \
    "v_pk_mul_f32 v[38:39], v[38:39], v[64:65] op_sel_hi:[1,0]\n\t" \
    "v_permlane16_swap_b32 v72, v76\n\t" \
    "v_cvt_pk_bf16_f32 v7, v6, v7\n\t" \
    "v_cvt_pk_bf16_f32 v6, v4, v5\n\t" \
    "v_add_f32 v74, v72, v76\n\t" \
    "v_cvt_pk_bf16_f32 v5, v2, v3\n\t" \
    "v_cvt_pk_bf16_f32 v4, v0, v1\n\t" \
    "v_rcp_f32 v72, v74\n\t" \
    "v_cvt_pk_bf16_f32 v0, v32, v33\n\t" \
    "v_cvt_pk_bf16_f32 v1, v34, v35\n\t" \
    "v_mov_b32 v76, v72\n\t" \
    "v_cvt_pk_bf16_f32 v2, v36, v37\n\t" \
    "v_cvt_pk_bf16_f32 v3, v38, v39\n\t" \
    "v_permlane16_swap_b32 v72, v76\n\t" \
    "v_pk_mul_f32 v[8:9], v[8:9], v[70:71] op_sel_hi:[1,0]\n\t" \
    "v_pk_mul_f32 v[40:41], v[40:41], v[68:69] op_sel_hi:[1,0]\n\t" \
    "v_mov_b32 v74, v72\n\t" \
    "v_mov_b32 v78, v76\n\t" \
    "v_pk_mul_f32 v[10:11], v[10:11], v[70:71] op_sel_hi:[1,0]\n\t" \
    "v_permlane32_swap_b32 v72, v74\n\t" \
    "v_permlane32_swap_b32 v76, v78\n\t" \
    "v_pk_mul_f32 v[42:43], v[42:43], v[68:69] op_sel_hi:[1,0]\n\t" \
    "v_pk_mul_f32 v[12:13], v[12:13], v[70:71] op_sel_hi:[1,0]\n\t" \
    "v_pk_mul_f32 v[44:45], v[44:45], v[68:69] op_sel_hi:[1,0]\n\t" \
    "v_pk_mul_f32 v[14:15], v[14:15], v[70:71] op_sel_hi:[1,0]\n\t" \
    "v_pk_mul_f32 v[46:47], v[46:47], v[68:69] op_sel_hi:[1,0]\n\t" \
    "v_pk_mul_f32 v[16:17], v[16:17], v[74:75] op_sel_hi:[1,0]\n\t" \
    "v_cvt_pk_bf16_f32 v15, v14, v15\n\t" \
    "v_cvt_pk_bf16_f32 v14, v12, v13\n\t" \
    "v_cvt_pk_bf16_f32 v13, v10, v11\n\t" \
    "v_cvt_pk_bf16_f32 v12, v8, v9\n\t" \
    "v_cvt_pk_bf16_f32 v8, v40, v41\n\t" \
    "v_cvt_pk_bf16_f32 v9, v42, v43\n\t" \
    "v_cvt_pk_bf16_f32 v10, v44, v45\n\t" \
    "v_cvt_pk_bf16_f32 v11, v46, v47\n\t" \
    "v_pk_mul_f32 v[48:49], v[48:49], v[72:73] op_sel_hi:[1,0]\n\t" \
    "v_pk_mul_f32 v[18:19], v[18:19], v[74:75] op_sel_hi:[1,0]\n\t" \
    "v_pk_mul_f32 v[50:51], v[50:51], v[72:73] op_sel_hi:[1,0]\n\t" \
    "v_pk_mul_f32 v[20:21], v[20:21], v[74:75] op_sel_hi:[1,0]\n\t" \
    "v_pk_mul_f32 v[52:53], v[52:53], v[72:73] op_sel_hi:[1,0]\n\t" \
    "v_pk_mul_f32 v[22:23], v[22:23], v[74:75] op_sel_hi:[1,0]\n\t" \
    "v_pk_mul_f32 v[54:55], v[54:55], v[72:73] op_sel_hi:[1,0]\n\t" \
    "ds_read_b32 v237, v249 offset:8\n\t" \
    "v_pk_mul_f32 v[24:25], v[24:25], v[78:79] op_sel_hi:[1,0]\n\t" \
    "v_cvt_pk_bf16_f32 v23, v22, v23\n\t" \
    "v_cvt_pk_bf16_f32 v22, v20, v21\n\t" \
    "v_cvt_pk_bf16_f32 v21, v18, v19\n\t" \
    "v_cvt_pk_bf16_f32 v20, v16, v17\n\t" \
    "v_cvt_pk_bf16_f32 v16, v48, v49\n\t" \
    "v_cvt_pk_bf16_f32 v17, v50, v51\n\t" \
    "v_cvt_pk_bf16_f32 v18, v52, v53\n\t" \
    "v_cvt_pk_bf16_f32 v19, v54, v55\n\t" \
    "v_pk_mul_f32 v[56:57], v[56:57], v[76:77] op_sel_hi:[1,0]\n\t" \
    "v_pk_mul_f32 v[26:27], v[26:27], v[78:79] op_sel_hi:[1,0]\n\t" \
    "v_pk_mul_f32 v[58:59], v[58:59], v[76:77] op_sel_hi:[1,0]\n\t" \
    "v_pk_mul_f32 v[28:29], v[28:29], v[78:79] op_sel_hi:[1,0]\n\t" \
    "v_pk_mul_f32 v[60:61], v[60:61], v[76:77] op_sel_hi:[1,0]\n\t" \
    "v_pk_mul_f32 v[30:31], v[30:31], v[78:79] op_sel_hi:[1,0]\n\t" \
    "v_pk_mul_f32 v[62:63], v[62:63], v[76:77] op_sel_hi:[1,0]\n\t" \
    "v_cvt_pk_bf16_f32 v31, v30, v31\n\t" \
    "v_cvt_pk_bf16_f32 v30, v28, v29\n\t" \
    "v_cvt_pk_bf16_f32 v29, v26, v27\n\t" \
    "v_cvt_pk_bf16_f32 v28, v24, v25\n\t" \
    "v_cvt_pk_bf16_f32 v24, v56, v57\n\t" \
    "v_cvt_pk_bf16_f32 v25, v58, v59\n\t" \
    "v_cvt_pk_bf16_f32 v26, v60, v61\n\t" \
    "v_cvt_pk_bf16_f32 v27, v62, v63\n\t" \
    "s_setprio 2\n\t" \
    "s_waitcnt lgkmcnt(0)\n\t" \
    "v_readfirstlane_b32 s85, v237\n\t" \
    "s_add_u32 s86, s87, 0x10000\n\t" \
    "s_cmp_lt_u32 s85, 3\n\t" \
    "s_cbranch_scc1 .Lab_s7\n\t" \
    ".Lab_s7r:\n\t" \
    "s_mov_b32 s89, 0x400000\n\t" \
    "s_mov_b32 s82, 4\n\t" \
    ".Lab_k:\n\t" \
    "ds_read_b32 v237, v249 offset:8\n\t" \
    "ds_read_b128 v[32:35], v252\n\t" \
    "ds_read_b128 v[36:39], v252 offset:256\n\t" \
    "ds_read_b128 v[40:43], v253\n\t" \
    "ds_read_b128 v[44:47], v253 offset:1024\n\t" \
    "ds_read_b128 v[48:51], v253 offset:2048\n\t" \
    "ds_read_b128 v[52:55], v253 offset:3072\n\t" \
    "s_sub_u32 s89, s89, 1\n\t" \
    "s_cbranch_scc1 .Lab_kr\n\t" \
    "s_waitcnt lgkmcnt(6)\n\t" \
    "v_readfirstlane_b32 s85, v237\n\t" \
    "s_cmp_lt_u32 s85, s82\n\t" \
    "s_cbranch_scc1 .Lab_k\n\t" \
    ".Lab_kr:\n\t" \
    "s_waitcnt lgkmcnt(5)\n\t" \
    "v_mfma_f32_16x16x32_bf16 v[56:59], v[32:35], v[0:3], v[120:123]\n\t" \
    "ds_read_b32 v126, v249 offset:296\n\t" \
    "v_mfma_f32_16x16x32_bf16 v[60:63], v[32:35], v[8:11], v[120:123]\n\t" \
    "ds_read_b32 v127, v249 offset:308\n\t" \
    "v_mfma_f32_16x16x32_bf16 v[64:67], v[32:35], v[16:19], v[120:123]\n\t" \
    "v_cmp_eq_u32 vcc, 0, v244\n\t" \
    "v_mfma_f32_16x16x32_bf16 v[68:71], v[32:35], v[24:27], v[120:123]\n\t" \
    "v_lshrrev_b32 v237, 1, v251\n\t" \
    "v_bfrev_b32 v238, 1\n\t" \
    "s_waitcnt lgkmcnt(6)\n\t" \
    "v_mfma_f32_16x16x32_bf16 v[56:59], v[36:39], v[4:7], v[56:59]\n\t" \
    "v_mfma_f32_16x16x32_bf16 v[60:63], v[36:39], v[12:15], v[60:63]\n\t" \
    "v_cndmask_b32 v254, v238, v237, vcc\n\t" \
    "v_mfma_f32_16x16x32_bf16 v[64:67], v[36:39], v[20:23], v[64:67]\n\t" \
    "s_or_b32 s82, s85, 1\n\t" \
    "v_mfma_f32_16x16x32_bf16 v[68:71], v[36:39], v[28:31], v[68:71]\n\t" \
    "s_waitcnt lgkmcnt(5)\n\t" \
    "v_mfma_f32_16x16x32_bf16 v[72:75], v[40:43], v[0:3], 0\n\t" \
    "s_waitcnt lgkmcnt(4)\n\t" \
    "v_mfma_f32_16x16x32_bf16 v[76:79], v[44:47], v[0:3], 0\n\t" \
    "s_waitcnt lgkmcnt(3)\n\t" \
    "v_mfma_f32_16x16x32_bf16 v[72:75], v[48:51], v[4:7], v[72:75]\n\t" \
    "s_waitcnt lgkmcnt(2)\n\t" \
    "v_mfma_f32_16x16x32_bf16 v[76:79], v[52:55], v[4:7], v[76:79]\n\t" \
    "v_mfma_f32_16x16x32_bf16 v[80:83], v[40:43], v[8:11], 0\n\t" \
    "v_rcp_f32 v56, v56\n\t" \
    "v_mfma_f32_16x16x32_bf16 v[84:87], v[44:47], v[8:11], 0\n\t" \
    "v_rcp_f32 v60, v60\n\t" \
    "v_mfma_f32_16x16x32_bf16 v[80:83], v[48:51], v[12:15], v[80:83]\n\t" \
    "v_rcp_f32 v64, v64\n\t" \
    "v_mfma_f32_16x16x32_bf16 v[84:87], v[52:55], v[12:15], v[84:87]\n\t" \
    "v_rcp_f32 v68, v68\n\t" \
    "v_mfma_f32_16x16x32_bf16 v[88:91], v[40:43], v[16:19], 0\n\t" \
    "v_mfma_f32_16x16x32_bf16 v[92:95], v[44:47], v[16:19], 0\n\t" \
    "v_mul_f32 v72, v72, v56\n\t" \
    "v_mul_f32 v73, v73, v56\n\t" \
    "v_mfma_f32_16x16x32_bf16 v[88:91], v[48:51], v[20:23], v[88:91]\n\t" \
    "v_mul_f32 v74, v74, v56\n\t" \
    "v_mul_f32 v75, v75, v56\n\t" \
    "v_mfma_f32_16x16x32_bf16 v[92:95], v[52:55], v[20:23], v[92:95]\n\t" \
    "v_mul_f32 v76, v76, v56\n\t" \
    "v_mul_f32 v77, v77, v56\n\t" \
    "v_mfma_f32_16x16x32_bf16 v[96:99], v[40:43], v[24:27], 0\n\t" \
    "v_mul_f32 v78, v78, v56\n\t" \
    "v_mul_f32 v79, v79, v56\n\t" \
    "v_mfma_f32_16x16x32_bf16 v[100:103], v[44:47], v[24:27], 0\n\t" \
    "v_cvt_pk_bf16_f32 v104, v72, v73\n\t" \
    "v_cvt_pk_bf16_f32 v105, v74, v75\n\t" \
    "v_mfma_f32_16x16x32_bf16 v[96:99], v[48:51], v[28:31], v[96:99]\n\t" \
    "v_cvt_pk_bf16_f32 v106, v76, v77\n\t" \
    "v_cvt_pk_bf16_f32 v107, v78, v79\n\t" \
    "v_mfma_f32_16x16x32_bf16 v[100:103], v[52:55], v[28:31], v[100:103]\n\t" \
    "buffer_store_dwordx4 v[104:107], v251, %[rsb], s87 offen sc1\n\t" \
    "v_pk_mul_f32 v[80:81], v[80:81], v[60:61] op_sel_hi:[1,0]\n\t" \
    "v_pk_mul_f32 v[82:83], v[82:83], v[60:61] op_sel_hi:[1,0]\n\t" \
    "v_pk_mul_f32 v[84:85], v[84:85], v[60:61] op_sel_hi:[1,0]\n\t" \
    "v_pk_mul_f32 v[86:87], v[86:87], v[60:61] op_sel_hi:[1,0]\n\t" \
    "v_cvt_pk_bf16_f32 v108, v80, v81\n\t" \
    "v_cvt_pk_bf16_f32 v109, v82, v83\n\t" \
    "v_cvt_pk_bf16_f32 v110, v84, v85\n\t" \
    "v_cvt_pk_bf16_f32 v111, v86, v87\n\t" \
    "buffer_store_dwordx4 v[108:111], v251, %[rsb], s87 offen offset:1024 sc1\n\t" \
    "v_pk_mul_f32 v[88:89], v[88:89], v[64:65] op_sel_hi:[1,0]\n\t" \
    "v_pk_mul_f32 v[90:91], v[90:91], v[64:65] op_sel_hi:[1,0]\n\t" \
    "v_pk_mul_f32 v[92:93], v[92:93], v[64:65] op_sel_hi:[1,0]\n\t" \
    "v_pk_mul_f32 v[94:95], v[94:95], v[64:65] op_sel_hi:[1,0]\n\t" \
    "v_cvt_pk_bf16_f32 v112, v88, v89\n\t" \
    "v_cvt_pk_bf16_f32 v113, v90, v91\n\t" \
    "v_cvt_pk_bf16_f32 v114, v92, v93\n\t" \
    "v_cvt_pk_bf16_f32 v115, v94, v95\n\t" \
    "buffer_store_dwordx4 v[112:115], v251, %[rsb], s86 offen sc1\n\t" \
    "v_pk_mul_f32 v[96:97], v[96:97], v[68:69] op_sel_hi:[1,0]\n\t" \
    "v_pk_mul_f32 v[98:99], v[98:99], v[68:69] op_sel_hi:[1,0]\n\t" \
    "v_pk_mul_f32 v[100:101], v[100:101], v[68:69] op_sel_hi:[1,0]\n\t" \
    "v_pk_mul_f32 v[102:103], v[102:103], v[68:69] op_sel_hi:[1,0]\n\t" \
    "v_cvt_pk_bf16_f32 v116, v96, v97\n\t" \
    "v_cvt_pk_bf16_f32 v117, v98, v99\n\t" \
    "v_cvt_pk_bf16_f32 v118, v100, v101\n\t" \
    "v_cvt_pk_bf16_f32 v119, v102, v103\n\t" \
    "v_add3_u32 v32, v104, v105, 0\n\t" \
    "v_add3_u32 v32, v106, v107, v32\n\t" \
    "v_add3_u32 v33, v112, v113, 0\n\t" \
    "v_add3_u32 v33, v114, v115, v33\n\t" \
    "v_add3_u32 v32, v108, v109, v32\n\t" \
    "v_add3_u32 v32, v110, v111, v32\n\t" \
    "v_add3_u32 v33, v116, v117, v33\n\t" \
    "v_add3_u32 v33, v118, v119, v33\n\t" \
    "buffer_store_dwordx4 v[116:119], v251, %[rsb], s86 offen offset:1024 sc1\n\t" \
    "ds_read_b32 v36, v249 offset:8\n\t" \
    "v_permlane32_swap_b32 v32, v33\n\t" \
    "v_add_u32 v32, v32, v33\n\t" \
    "v_mov_b32 v33, v32\n\t" \
    "s_waitcnt lgkmcnt(2)\n\t" \
    "v_readfirstlane_b32 %[rid], v126\n\t" \
    "s_waitcnt lgkmcnt(1)\n\t" \
    "v_readfirstlane_b32 %[t3], v127\n\t" \
    "v_permlane16_swap_b32 v32, v33\n\t" \
    "v_add_u32 v32, v32, v33\n\t" \
    "v_mov_b32 v33, v236\n\t" \
    "ds_read_b32 v238, v249 offset:56\n\t" \
    "v_add_u32_dpp v32, v32, v32 row_ror:8 row_mask:0xf bank_mask:0xf bound_ctrl:1\n\t" \
    "v_mov_b32 v237, 12\n\t" \
    "v_and_b32 v34, 15, v250\n\t" \
    "v_add_u32_dpp v32, v32, v32 row_ror:4 row_mask:0xf bank_mask:0xf bound_ctrl:1\n\t" \
    "s_waitcnt lgkmcnt(1)\n\t" \
    "v_readfirstlane_b32 s80, v36\n\t" \
    "s_and_b32 s85, %[rid], 15\n\t" \
    "v_add_u32_dpp v32, v32, v32 row_ror:2 row_mask:0xf bank_mask:0xf bound_ctrl:1\n\t" \
    "v_cmp_eq_u32_e64 %[rm], s85, v34\n\t" \
    "s_cmp_lg_u32 s80, s82\n\t" \
    "v_add_u32_dpp v32, v32, v32 row_ror:1 row_mask:0xf bank_mask:0xf bound_ctrl:1\n\t" \
    "s_cbranch_scc1 .Lab_sa\n\t" \
    ".Lab_sar:\n\t" \
    "buffer_store_dwordx2 v[32:33], v254, %[rsb], s88 offen sc1\n\t" \
    "ds_write_b32 v248, v237\n\t" \
    "v_and_b32 v252, 48, v250\n\t" \
    "s_lshr_b32 s85, %[rid], 5\n\t" \
    "v_add_u32 v252, 161536, v252\n\t" \
    "s_cmp_eq_u32 s85, %[own]\n\t" \
    "s_cselect_b64 %[rm], %[rm], 0\n\t" \
    "s_waitcnt vmcnt(5)\n\t" \
    "v_cmp_ne_u32 vcc, v236, v233\n\t" \
    "v_cmp_ne_u32_e64 s[92:93], v236, v235\n\t" \
    "s_nop 0\n\t" \
    "s_or_b64 vcc, vcc, s[92:93]\n\t" \
    "s_cbranch_scc1 .Lab_s8\n\t" \
    ".Lab_s8r:\n\t" \
    "v_sub_f32 v254, 1.0, v232\n\t" \
    "v_sub_f32 v255, 1.0, v234\n\t" \
    "v_cvt_pk_bf16_f32 v255, v254, v255\n\t" \
    "v_lshlrev_b32 v254, 16, v255\n\t" \
    "v_and_b32 v255, 0xffff0000, v255\n\t" \
    "s_waitcnt lgkmcnt(1)\n\t" \
    "v_readfirstlane_b32 s85, v238\n\t" \
    "s_cmp_lt_u32 s85, 2\n\t" \
    "s_cbranch_scc1 .Lab_s9\n\t" \
    ".Lab_s9r:\n\t" \
    "s_waitcnt lgkmcnt(0)\n\t"
#define PHASE_C_ASM \
    "s_branch .Lpad_c0\n\t" \
    ".Lc_x:\n\t" \
    "s_waitcnt vmcnt(0) lgkmcnt(0)\n\t" \
    "s_branch .Lc_end\n\t" \
    ".Lc_w0:\n\t" \
    "s_mov_b32 s86, 0x400000\n\t" \
    ".Lc_a0:\n\t" \
    "ds_read_b32 v160, v249 offset:56\n\t" \
    "s_waitcnt lgkmcnt(0)\n\t" \
    "v_readfirstlane_b32 s83, v160\n\t" \
    "s_sub_u32 s86, s86, 1\n\t" \
    "s_cbranch_scc1 .Lc_r0\n\t" \
    "s_cmp_lt_u32 s83, s82\n\t" \
    "s_cbranch_scc1 .Lc_a0\n\t" \
    "s_branch .Lc_r0\n\t" \
    ".Lc_w1:\n\t" \
    "s_mov_b32 s86, 0x400000\n\t" \
    ".Lc_a1:\n\t" \
    "ds_read_b32 v160, v249 offset:56\n\t" \
    "s_waitcnt lgkmcnt(0)\n\t" \
    "v_readfirstlane_b32 s83, v160\n\t" \
    "s_sub_u32 s86, s86, 1\n\t" \
    "s_cbranch_scc1 .Lc_r1\n\t" \
    "s_cmp_lt_u32 s83, s82\n\t" \
    "s_cbranch_scc1 .Lc_a1\n\t" \
    "s_branch .Lc_r1\n\t" \
    ".Lc_y:\n\t" \
    "s_mov_b32 s86, 0x400000\n\t" \
    ".Lc_ya:\n\t" \
    "s_waitcnt lgkmcnt(0)\n\t" \
    "v_readfirstlane_b32 s83, v231\n\t" \
    "s_sub_u32 s86, s86, 1\n\t" \
    "s_cbranch_scc1 .Lc_yb\n\t" \
    "s_cmp_lg_u32 s83, 0\n\t" \
    "s_cbranch_scc1 .Lc_yb\n\t" \
    "ds_read_b32 v231, v249 offset:68\n\t" \
    "ds_read_b128 v[96:99], v252\n\t" \
    "ds_read_b128 v[100:103], v252 offset:64\n\t" \
    "ds_read_b128 v[104:107], v252 offset:128\n\t" \
    "ds_read_b128 v[108:111], v252 offset:192\n\t" \
    "s_branch .Lc_ya\n\t" \
    ".Lc_yb:\n\t" \
    "s_mov_b64 exec, %[rm]\n\t" \
    "s_bitcmp1_b32 %[rid], 4\n\t" \
    "s_cbranch_scc1 .Lc_yc\n\t" \
    "v_pk_mul_f32 v[194:195], v[96:97], v[254:255] op_sel_hi:[1,0]\n\t" \
    "v_pk_mul_f32 v[196:197], v[98:99], v[254:255] op_sel_hi:[1,0]\n\t" \
    "v_pk_mul_f32 v[198:199], v[100:101], v[254:255] op_sel_hi:[1,0]\n\t" \
    "v_pk_mul_f32 v[200:201], v[102:103], v[254:255] op_sel_hi:[1,0]\n\t" \
    "v_pk_mul_f32 v[202:203], v[104:105], v[254:255] op_sel_hi:[1,0]\n\t" \
    "v_pk_mul_f32 v[204:205], v[106:107], v[254:255] op_sel_hi:[1,0]\n\t" \
    "v_pk_mul_f32 v[206:207], v[108:109], v[254:255] op_sel_hi:[1,0]\n\t" \
    "v_pk_mul_f32 v[208:209], v[110:111], v[254:255] op_sel_hi:[1,0]\n\t" \
    "s_mov_b64 exec, -1\n\t" \
    "s_branch .Lc_yr\n\t" \
    ".Lc_yc:\n\t" \
    "v_pk_mul_f32 v[210:211], v[96:97], v[254:255] op_sel:[0,1] op_sel_hi:[1,1]\n\t" \
    "v_pk_mul_f32 v[212:213], v[98:99], v[254:255] op_sel:[0,1] op_sel_hi:[1,1]\n\t" \
    "v_pk_mul_f32 v[214:215], v[100:101], v[254:255] op_sel:[0,1] op_sel_hi:[1,1]\n\t" \
    "v_pk_mul_f32 v[216:217], v[102:103], v[254:255] op_sel:[0,1] op_sel_hi:[1,1]\n\t" \
    "v_pk_mul_f32 v[218:219], v[104:105], v[254:255] op_sel:[0,1] op_sel_hi:[1,1]\n\t" \
    "v_pk_mul_f32 v[220:221], v[106:107], v[254:255] op_sel:[0,1] op_sel_hi:[1,1]\n\t" \
    "v_pk_mul_f32 v[222:223], v[108:109], v[254:255] op_sel:[0,1] op_sel_hi:[1,1]\n\t" \
    "v_pk_mul_f32 v[224:225], v[110:111], v[254:255] op_sel:[0,1] op_sel_hi:[1,1]\n\t" \
    "s_mov_b64 exec, -1\n\t" \
    "s_branch .Lc_yr\n\t" \
    ".p2align 12\n\t" \
    ".Lpad_c0:\n\t" \
    "s_cbranch_execz .Lpad_ret\n\t" \
    "s_setprio 2\n\t" \
    "v_xor_b32 v226, %[flip], v245\n\t" \
    "v_xor_b32 v227, 32, v226\n\t" \
    "v_xor_b32 v228, 64, v226\n\t" \
    "v_xor_b32 v229, 96, v226\n\t" \
    "v_and_b32 v231, 0x7f, v244\n\t" \
    "s_lshl_b32 s80, %[grp], 14\n\t" \
    "s_lshl_b32 s83, %[grp], 3\n\t" \
    "s_lshl_b32 s84, 0xffff, s83\n\t" \
    "s_lshl_b32 s85, %[grp], 6\n\t" \
    "s_add_u32 s85, s85, %[sog]\n\t" \
    "s_mov_b32 s81, 256\n\t" \
    "v_xor_b32 v1, 32, v246\n\t" \
    "v_xor_b32 v2, 64, v246\n\t" \
    "v_xor_b32 v3, 96, v246\n\t" \
    ".Lc_t:\n\t" \
    "s_memrealtime s[86:87]\n\t" \
    "s_sub_u32 s81, s81, 1\n\t" \
    "s_cbranch_scc1 .Lc_go\n\t" \
    "s_waitcnt lgkmcnt(0)\n\t" \
    "s_sub_u32 s86, %[t3], s86\n\t" \
    "s_cmp_lt_i32 s86, 5\n\t" \
    "s_cbranch_scc1 .Lc_go\n\t" \
    "s_cmp_lt_i32 s86, 13\n\t" \
    "s_cbranch_scc1 .Lc_t\n\t" \
    "s_sleep 2\n\t" \
    "s_branch .Lc_t\n\t" \
    ".Lc_go:\n\t" \
    "buffer_load_dwordx2 v[160:161], v231, %[rsb], s85 offen sc1\n\t" \
    "buffer_load_dwordx4 v[32:35], v240, %[rsb], s80 offen offset:2048 sc1\n\t" \
    "buffer_load_dwordx4 v[36:39], v240, %[rsb], s80 offen offset:3072 sc1\n\t" \
    "buffer_load_dwordx4 v[40:43], v241, %[rsb], s80 offen sc1\n\t" \
    "buffer_load_dwordx4 v[44:47], v241, %[rsb], s80 offen offset:1024 sc1\n\t" \
    "buffer_load_dwordx4 v[48:51], v241, %[rsb], s80 offen offset:2048 sc1\n\t" \
    "buffer_load_dwordx4 v[52:55], v241, %[rsb], s80 offen offset:3072 sc1\n\t" \
    "buffer_load_dwordx4 v[56:59], v242, %[rsb], s80 offen sc1\n\t" \
    "buffer_load_dwordx4 v[60:63], v242, %[rsb], s80 offen offset:1024 sc1\n\t" \
    "buffer_load_dwordx4 v[64:67], v242, %[rsb], s80 offen offset:2048 sc1\n\t" \
    "buffer_load_dwordx4 v[68:71], v242, %[rsb], s80 offen offset:3072 sc1\n\t" \
    "buffer_load_dwordx4 v[72:75], v243, %[rsb], s80 offen sc1\n\t" \
    "buffer_load_dwordx4 v[76:79], v243, %[rsb], s80 offen offset:1024 sc1\n\t" \
    "buffer_load_dwordx4 v[80:83], v243, %[rsb], s80 offen offset:2048 sc1\n\t" \
    "buffer_load_dwordx4 v[84:87], v243, %[rsb], s80 offen offset:3072 sc1\n\t" \
    "s_add_u32 s80, s80, 0x4000\n\t" \
    "s_and_b32 s80, s80, 0xc000\n\t" \
    "ds_read_b64 v[196:197], v246\n\t" \
    "ds_read_b64 v[200:201], v1\n\t" \
    "ds_read_b64 v[204:205], v2\n\t" \
    "ds_read_b64 v[208:209], v3\n\t" \
    "ds_read_b64 v[212:213], v246 offset:2048\n\t" \
    "ds_read_b64 v[216:217], v1 offset:2048\n\t" \
    "ds_read_b64 v[220:221], v2 offset:2048\n\t" \
    "ds_read_b64 v[224:225], v3 offset:2048\n\t" \
    "ds_read_b64_tr_b16 v[96:97], v226\n\t" \
    "ds_read_b64_tr_b16 v[98:99], v226 offset:2048\n\t" \
    "ds_read_b64_tr_b16 v[100:101], v227\n\t" \
    "ds_read_b64_tr_b16 v[102:103], v227 offset:2048\n\t" \
    "ds_read_b64_tr_b16 v[104:105], v228\n\t" \
    "ds_read_b64_tr_b16 v[106:107], v228 offset:2048\n\t" \
    "ds_read_b64_tr_b16 v[108:109], v229\n\t" \
    "ds_read_b64_tr_b16 v[110:111], v229 offset:2048\n\t" \
    "ds_read_b64_tr_b16 v[112:113], v226 offset:4096\n\t" \
    "ds_read_b64_tr_b16 v[114:115], v226 offset:6144\n\t" \
    "ds_read_b64_tr_b16 v[116:117], v227 offset:4096\n\t" \
    "ds_read_b64_tr_b16 v[118:119], v227 offset:6144\n\t" \
    "ds_read_b64_tr_b16 v[120:121], v228 offset:4096\n\t" \
    "ds_read_b64_tr_b16 v[122:123], v228 offset:6144\n\t" \
    "ds_read_b64_tr_b16 v[124:125], v229 offset:4096\n\t" \
    "ds_read_b64_tr_b16 v[126:127], v229 offset:6144\n\t" \
    "v_mov_b32 v0, 0\n\t" \
    "v_mov_b32 v1, 0\n\t" \
    "v_mov_b32 v2, 0\n\t" \
    "v_mov_b32 v3, 0\n\t" \
    "v_mov_b32 v4, 0\n\t" \
    "v_mov_b32 v5, 0\n\t" \
    "v_mov_b32 v6, 0\n\t" \
    "v_mov_b32 v7, 0\n\t" \
    "v_mov_b32 v8, 0\n\t" \
    "v_mov_b32 v9, 0\n\t" \
    "v_mov_b32 v10, 0\n\t" \
    "v_mov_b32 v11, 0\n\t" \
    "v_mov_b32 v12, 0\n\t" \
    "v_mov_b32 v13, 0\n\t" \
    "v_mov_b32 v14, 0\n\t" \
    "v_mov_b32 v15, 0\n\t" \
    "v_mov_b32 v16, 0\n\t" \
    "v_mov_b32 v17, 0\n\t" \
    "v_mov_b32 v18, 0\n\t" \
    "v_mov_b32 v19, 0\n\t" \
    "v_mov_b32 v20, 0\n\t" \
    "v_mov_b32 v21, 0\n\t" \
    "v_mov_b32 v22, 0\n\t" \
    "v_mov_b32 v23, 0\n\t" \
    "v_mov_b32 v24, 0\n\t" \
    "v_mov_b32 v25, 0\n\t" \
    "v_mov_b32 v26, 0\n\t" \
    "v_mov_b32 v27, 0\n\t" \
    "v_mov_b32 v28, 0\n\t" \
    "v_mov_b32 v29, 0\n\t" \
    "v_mov_b32 v30, 0\n\t" \
    "v_mov_b32 v31, 0\n\t" \
    "s_waitcnt lgkmcnt(0)\n\t" \
    "v_lshlrev_b32 v194, 16, v196\n\t" \
    "v_and_b32 v195, 0xffff0000, v196\n\t" \
    "v_lshlrev_b32 v196, 16, v197\n\t" \
    "v_and_b32 v197, 0xffff0000, v197\n\t" \
    "v_lshlrev_b32 v198, 16, v200\n\t" \
    "v_and_b32 v199, 0xffff0000, v200\n\t" \
    "v_lshlrev_b32 v200, 16, v201\n\t" \
    "v_and_b32 v201, 0xffff0000, v201\n\t" \
    "v_lshlrev_b32 v202, 16, v204\n\t" \
    "v_and_b32 v203, 0xffff0000, v204\n\t" \
    "v_lshlrev_b32 v204, 16, v205\n\t" \
    "v_and_b32 v205, 0xffff0000, v205\n\t" \
    "v_lshlrev_b32 v206, 16, v208\n\t" \
    "v_and_b32 v207, 0xffff0000, v208\n\t" \
    "v_lshlrev_b32 v208, 16, v209\n\t" \
    "v_and_b32 v209, 0xffff0000, v209\n\t" \
    "v_lshlrev_b32 v210, 16, v212\n\t" \
    "v_and_b32 v211, 0xffff0000, v212\n\t" \
    "v_lshlrev_b32 v212, 16, v213\n\t" \
    "v_and_b32 v213, 0xffff0000, v213\n\t" \
    "v_lshlrev_b32 v214, 16, v216\n\t" \
    "v_and_b32 v215, 0xffff0000, v216\n\t" \
    "v_lshlrev_b32 v216, 16, v217\n\t" \
    "v_and_b32 v217, 0xffff0000, v217\n\t" \
    "v_lshlrev_b32 v218, 16, v220\n\t" \
    "v_and_b32 v219, 0xffff0000, v220\n\t" \
    "v_lshlrev_b32 v220, 16, v221\n\t" \
    "v_and_b32 v221, 0xffff0000, v221\n\t" \
    "v_lshlrev_b32 v222, 16, v224\n\t" \
    "v_and_b32 v223, 0xffff0000, v224\n\t" \
    "v_lshlrev_b32 v224, 16, v225\n\t" \
    "v_and_b32 v225, 0xffff0000, v225\n\t" \
    "v_pk_mul_f32 v[194:195], v[194:195], v[254:255] op_sel_hi:[1,0]\n\t" \
    "v_pk_mul_f32 v[196:197], v[196:197], v[254:255] op_sel_hi:[1,0]\n\t" \
    "v_pk_mul_f32 v[198:199], v[198:199], v[254:255] op_sel_hi:[1,0]\n\t" \
    "v_pk_mul_f32 v[200:201], v[200:201], v[254:255] op_sel_hi:[1,0]\n\t" \
    "v_pk_mul_f32 v[202:203], v[202:203], v[254:255] op_sel_hi:[1,0]\n\t" \
    "v_pk_mul_f32 v[204:205], v[204:205], v[254:255] op_sel_hi:[1,0]\n\t" \
    "v_pk_mul_f32 v[206:207], v[206:207], v[254:255] op_sel_hi:[1,0]\n\t" \
    "v_pk_mul_f32 v[208:209], v[208:209], v[254:255] op_sel_hi:[1,0]\n\t" \
    "v_pk_mul_f32 v[210:211], v[210:211], v[254:255] op_sel:[0,1] op_sel_hi:[1,1]\n\t" \
    "v_pk_mul_f32 v[212:213], v[212:213], v[254:255] op_sel:[0,1] op_sel_hi:[1,1]\n\t" \
    "v_pk_mul_f32 v[214:215], v[214:215], v[254:255] op_sel:[0,1] op_sel_hi:[1,1]\n\t" \
    "v_pk_mul_f32 v[216:217], v[216:217], v[254:255] op_sel:[0,1] op_sel_hi:[1,1]\n\t" \
    "v_pk_mul_f32 v[218:219], v[218:219], v[254:255] op_sel:[0,1] op_sel_hi:[1,1]\n\t" \
    "v_pk_mul_f32 v[220:221], v[220:221], v[254:255] op_sel:[0,1] op_sel_hi:[1,1]\n\t" \
    "v_pk_mul_f32 v[222:223], v[222:223], v[254:255] op_sel:[0,1] op_sel_hi:[1,1]\n\t" \
    "v_pk_mul_f32 v[224:225], v[224:225], v[254:255] op_sel:[0,1] op_sel_hi:[1,1]\n\t" \
    "v_mov_b32 v230, 0\n\t" \
    "s_mov_b32 s81, 2\n\t" \
    "s_mov_b32 s82, 1\n\t" \
    "s_mov_b32 %[st], 1\n\t" \
    "s_waitcnt vmcnt(14)\n\t" \
    "v_cmp_ne_u32 vcc, v236, v161\n\t" \
    "s_nop 1\n\t" \
    "s_and_b32 vcc_lo, vcc_lo, s84\n\t" \
    "s_cbranch_scc1 .Lc_x\n\t" \
    ".Lc_l:\n\t" \
    "s_waitcnt vmcnt(12) lgkmcnt(8)\n\t" \
    "v_mfma_f32_16x16x32_bf16 v[0:3], v[96:99], v[32:35], v[0:3]\n\t" \
    "ds_read_b64_tr_b16 v[128:129], v226 offset:8192\n\t" \
    "buffer_load_dwordx4 v[88:91], v240, %[rsb], s80 offen sc1\n\t" \
    "v_mfma_f32_16x16x32_bf16 v[16:19], v[96:99], v[36:39], v[16:19]\n\t" \
    "ds_read_b64_tr_b16 v[130:131], v226 offset:10240\n\t" \
    "v_add3_u32 v230, v32, v33, v230\n\t" \
    "v_mfma_f32_16x16x32_bf16 v[4:7], v[100:103], v[32:35], v[4:7]\n\t" \
    "ds_read_b64_tr_b16 v[132:133], v227 offset:8192\n\t" \
    "buffer_load_dwordx4 v[92:95], v240, %[rsb], s80 offen offset:1024 sc1\n\t" \
    "v_mfma_f32_16x16x32_bf16 v[20:23], v[100:103], v[36:39], v[20:23]\n\t" \
    "ds_read_b64_tr_b16 v[134:135], v227 offset:10240\n\t" \
    "v_add3_u32 v230, v34, v35, v230\n\t" \
    "v_mfma_f32_16x16x32_bf16 v[8:11], v[104:107], v[32:35], v[8:11]\n\t" \
    "ds_read_b64_tr_b16 v[136:137], v228 offset:8192\n\t" \
    "v_add3_u32 v230, v36, v37, v230\n\t" \
    "v_mfma_f32_16x16x32_bf16 v[24:27], v[104:107], v[36:39], v[24:27]\n\t" \
    "ds_read_b64_tr_b16 v[138:139], v228 offset:10240\n\t" \
    "v_add3_u32 v230, v38, v39, v230\n\t" \
    "v_mfma_f32_16x16x32_bf16 v[12:15], v[108:111], v[32:35], v[12:15]\n\t" \
    "ds_read_b64_tr_b16 v[140:141], v229 offset:8192\n\t" \
    "v_mfma_f32_16x16x32_bf16 v[28:31], v[108:111], v[36:39], v[28:31]\n\t" \
    "ds_read_b64_tr_b16 v[142:143], v229 offset:10240\n\t" \
    "s_waitcnt vmcnt(12) lgkmcnt(8)\n\t" \
    "v_mfma_f32_16x16x32_bf16 v[0:3], v[112:115], v[40:43], v[0:3]\n\t" \
    "ds_read_b64_tr_b16 v[144:145], v226 offset:12288\n\t" \
    "buffer_load_dwordx4 v[32:35], v240, %[rsb], s80 offen offset:2048 sc1\n\t" \
    "v_mfma_f32_16x16x32_bf16 v[16:19], v[112:115], v[44:47], v[16:19]\n\t" \
    "ds_read_b64_tr_b16 v[146:147], v226 offset:14336\n\t" \
    "v_add3_u32 v230, v40, v41, v230\n\t" \
    "v_mfma_f32_16x16x32_bf16 v[4:7], v[116:119], v[40:43], v[4:7]\n\t" \
    "ds_read_b64_tr_b16 v[148:149], v227 offset:12288\n\t" \
    "buffer_load_dwordx4 v[36:39], v240, %[rsb], s80 offen offset:3072 sc1\n\t" \
    "v_mfma_f32_16x16x32_bf16 v[20:23], v[116:119], v[44:47], v[20:23]\n\t" \
    "ds_read_b64_tr_b16 v[150:151], v227 offset:14336\n\t" \
    "v_add3_u32 v230, v42, v43, v230\n\t" \
    "v_mfma_f32_16x16x32_bf16 v[8:11], v[120:123], v[40:43], v[8:11]\n\t" \
    "ds_read_b64_tr_b16 v[152:153], v228 offset:12288\n\t" \
    "v_add3_u32 v230, v44, v45, v230\n\t" \
    "v_mfma_f32_16x16x32_bf16 v[24:27], v[120:123], v[44:47], v[24:27]\n\t" \
    "ds_read_b64_tr_b16 v[154:155], v228 offset:14336\n\t" \
    "v_add3_u32 v230, v46, v47, v230\n\t" \
    "v_mfma_f32_16x16x32_bf16 v[12:15], v[124:127], v[40:43], v[12:15]\n\t" \
    "ds_read_b64_tr_b16 v[156:157], v229 offset:12288\n\t" \
    "v_mfma_f32_16x16x32_bf16 v[28:31], v[124:127], v[44:47], v[28:31]\n\t" \
    "ds_read_b64_tr_b16 v[158:159], v229 offset:14336\n\t" \
    "s_waitcnt vmcnt(12) lgkmcnt(8)\n\t" \
    "v_mfma_f32_16x16x32_bf16 v[0:3], v[128:131], v[48:51], v[0:3]\n\t" \
    "ds_read_b64_tr_b16 v[96:97], v226 offset:16384\n\t" \
    "buffer_load_dwordx4 v[40:43], v241, %[rsb], s80 offen sc1\n\t" \
    "v_mfma_f32_16x16x32_bf16 v[16:19], v[128:131], v[52:55], v[16:19]\n\t" \
    "ds_read_b64_tr_b16 v[98:99], v226 offset:18432\n\t" \
    "v_add3_u32 v230, v48, v49, v230\n\t" \
    "v_mfma_f32_16x16x32_bf16 v[4:7], v[132:135], v[48:51], v[4:7]\n\t" \
    "ds_read_b64_tr_b16 v[100:101], v227 offset:16384\n\t" \
    "buffer_load_dwordx4 v[44:47], v241, %[rsb], s80 offen offset:1024 sc1\n\t" \
    "v_mfma_f32_16x16x32_bf16 v[20:23], v[132:135], v[52:55], v[20:23]\n\t" \
    "ds_read_b64_tr_b16 v[102:103], v227 offset:18432\n\t" \
    "v_add3_u32 v230, v50, v51, v230\n\t" \
    "v_mfma_f32_16x16x32_bf16 v[8:11], v[136:139], v[48:51], v[8:11]\n\t" \
    "ds_read_b64_tr_b16 v[104:105], v228 offset:16384\n\t" \
    "v_add3_u32 v230, v52, v53, v230\n\t" \
    "v_mfma_f32_16x16x32_bf16 v[24:27], v[136:139], v[52:55], v[24:27]\n\t" \
    "ds_read_b64_tr_b16 v[106:107], v228 offset:18432\n\t" \
    "v_add3_u32 v230, v54, v55, v230\n\t" \
    "v_mfma_f32_16x16x32_bf16 v[12:15], v[140:143], v[48:51], v[12:15]\n\t" \
    "ds_read_b64_tr_b16 v[108:109], v229 offset:16384\n\t" \
    "v_mfma_f32_16x16x32_bf16 v[28:31], v[140:143], v[52:55], v[28:31]\n\t" \
    "ds_read_b64_tr_b16 v[110:111], v229 offset:18432\n\t" \
    "s_waitcnt vmcnt(12) lgkmcnt(8)\n\t" \
    "v_mfma_f32_16x16x32_bf16 v[0:3], v[144:147], v[56:59], v[0:3]\n\t" \
    "ds_read_b64_tr_b16 v[112:113], v226 offset:20480\n\t" \
    "buffer_load_dwordx4 v[48:51], v241, %[rsb], s80 offen offset:2048 sc1\n\t" \
    "v_mfma_f32_16x16x32_bf16 v[16:19], v[144:147], v[60:63], v[16:19]\n\t" \
    "ds_read_b64_tr_b16 v[114:115], v226 offset:22528\n\t" \
    "v_add3_u32 v230, v56, v57, v230\n\t" \
    "v_mfma_f32_16x16x32_bf16 v[4:7], v[148:151], v[56:59], v[4:7]\n\t" \
    "ds_read_b64_tr_b16 v[116:117], v227 offset:20480\n\t" \
    "buffer_load_dwordx4 v[52:55], v241, %[rsb], s80 offen offset:3072 sc1\n\t" \
    "v_mfma_f32_16x16x32_bf16 v[20:23], v[148:151], v[60:63], v[20:23]\n\t" \
    "ds_read_b64_tr_b16 v[118:119], v227 offset:22528\n\t" \
    "v_add3_u32 v230, v58, v59, v230\n\t" \
    "v_mfma_f32_16x16x32_bf16 v[8:11], v[152:155], v[56:59], v[8:11]\n\t" \
    "ds_read_b64_tr_b16 v[120:121], v228 offset:20480\n\t" \
    "v_add3_u32 v230, v60, v61, v230\n\t" \
    "v_mfma_f32_16x16x32_bf16 v[24:27], v[152:155], v[60:63], v[24:27]\n\t" \
    "ds_read_b64_tr_b16 v[122:123], v228 offset:22528\n\t" \
    "v_add3_u32 v230, v62, v63, v230\n\t" \
    "v_mfma_f32_16x16x32_bf16 v[12:15], v[156:159], v[56:59], v[12:15]\n\t" \
    "ds_read_b64_tr_b16 v[124:125], v229 offset:20480\n\t" \
    "v_mfma_f32_16x16x32_bf16 v[28:31], v[156:159], v[60:63], v[28:31]\n\t" \
    "ds_read_b64_tr_b16 v[126:127], v229 offset:22528\n\t" \
    "s_waitcnt vmcnt(12) lgkmcnt(8)\n\t" \
    "ds_read_b32 v160, v249 offset:56\n\t" \
    "v_mfma_f32_16x16x32_bf16 v[0:3], v[96:99], v[64:67], v[0:3]\n\t" \
    "ds_read_b64_tr_b16 v[128:129], v226 offset:24576\n\t" \
    "buffer_load_dwordx4 v[56:59], v242, %[rsb], s80 offen sc1\n\t" \
    "v_mfma_f32_16x16x32_bf16 v[16:19], v[96:99], v[68:71], v[16:19]\n\t" \
    "ds_read_b64_tr_b16 v[130:131], v226 offset:26624\n\t" \
    "v_add3_u32 v230, v64, v65, v230\n\t" \
    "v_mfma_f32_16x16x32_bf16 v[4:7], v[100:103], v[64:67], v[4:7]\n\t" \
    "ds_read_b64_tr_b16 v[132:133], v227 offset:24576\n\t" \
    "buffer_load_dwordx4 v[60:63], v242, %[rsb], s80 offen offset:1024 sc1\n\t" \
    "v_mfma_f32_16x16x32_bf16 v[20:23], v[100:103], v[68:71], v[20:23]\n\t" \
    "ds_read_b64_tr_b16 v[134:135], v227 offset:26624\n\t" \
    "v_add3_u32 v230, v66, v67, v230\n\t" \
    "v_mfma_f32_16x16x32_bf16 v[8:11], v[104:107], v[64:67], v[8:11]\n\t" \
    "ds_read_b64_tr_b16 v[136:137], v228 offset:24576\n\t" \
    "v_add3_u32 v230, v68, v69, v230\n\t" \
    "v_mfma_f32_16x16x32_bf16 v[24:27], v[104:107], v[68:71], v[24:27]\n\t" \
    "ds_read_b64_tr_b16 v[138:139], v228 offset:26624\n\t" \
    "v_add3_u32 v230, v70, v71, v230\n\t" \
    "v_mfma_f32_16x16x32_bf16 v[12:15], v[108:111], v[64:67], v[12:15]\n\t" \
    "ds_read_b64_tr_b16 v[140:141], v229 offset:24576\n\t" \
    "v_mfma_f32_16x16x32_bf16 v[28:31], v[108:111], v[68:71], v[28:31]\n\t" \
    "ds_read_b64_tr_b16 v[142:143], v229 offset:26624\n\t" \
    "s_waitcnt vmcnt(12) lgkmcnt(8)\n\t" \
    "v_mfma_f32_16x16x32_bf16 v[0:3], v[112:115], v[72:75], v[0:3]\n\t" \
    "ds_read_b64_tr_b16 v[144:145], v226 offset:28672\n\t" \
    "buffer_load_dwordx4 v[64:67], v242, %[rsb], s80 offen offset:2048 sc1\n\t" \
    "v_mfma_f32_16x16x32_bf16 v[16:19], v[112:115], v[76:79], v[16:19]\n\t" \
    "ds_read_b64_tr_b16 v[146:147], v226 offset:30720\n\t" \
    "v_add3_u32 v230, v72, v73, v230\n\t" \
    "v_mfma_f32_16x16x32_bf16 v[4:7], v[116:119], v[72:75], v[4:7]\n\t" \
    "ds_read_b64_tr_b16 v[148:149], v227 offset:28672\n\t" \
    "buffer_load_dwordx4 v[68:71], v242, %[rsb], s80 offen offset:3072 sc1\n\t" \
    "v_add_u32 v226, 0x8000, v226\n\t" \
    "v_and_b32 v226, 0x1ffff, v226\n\t" \
    "v_mfma_f32_16x16x32_bf16 v[20:23], v[116:119], v[76:79], v[20:23]\n\t" \
    "ds_read_b64_tr_b16 v[150:151], v227 offset:30720\n\t" \
    "v_add3_u32 v230, v74, v75, v230\n\t" \
    "v_mfma_f32_16x16x32_bf16 v[8:11], v[120:123], v[72:75], v[8:11]\n\t" \
    "ds_read_b64_tr_b16 v[152:153], v228 offset:28672\n\t" \
    "v_add3_u32 v230, v76, v77, v230\n\t" \
    "v_add_u32 v227, 0x8000, v227\n\t" \
    "v_and_b32 v227, 0x1ffff, v227\n\t" \
    "v_mfma_f32_16x16x32_bf16 v[24:27], v[120:123], v[76:79], v[24:27]\n\t" \
    "ds_read_b64_tr_b16 v[154:155], v228 offset:30720\n\t" \
    "v_add3_u32 v230, v78, v79, v230\n\t" \
    "v_mfma_f32_16x16x32_bf16 v[12:15], v[124:127], v[72:75], v[12:15]\n\t" \
    "v_readfirstlane_b32 s83, v160\n\t" \
    "s_add_u32 s82, s82, 1\n\t" \
    "ds_read_b64_tr_b16 v[156:157], v229 offset:28672\n\t" \
    "v_add_u32 v228, 0x8000, v228\n\t" \
    "v_and_b32 v228, 0x1ffff, v228\n\t" \
    "v_mfma_f32_16x16x32_bf16 v[28:31], v[124:127], v[76:79], v[28:31]\n\t" \
    "s_cmp_lt_u32 s83, s82\n\t" \
    "s_cbranch_scc1 .Lc_w0\n\t" \
    ".Lc_r0:\n\t" \
    "ds_read_b64_tr_b16 v[158:159], v229 offset:30720\n\t" \
    "v_add_u32 v229, 0x8000, v229\n\t" \
    "v_and_b32 v229, 0x1ffff, v229\n\t" \
    "s_waitcnt vmcnt(12) lgkmcnt(8)\n\t" \
    "v_mfma_f32_16x16x32_bf16 v[0:3], v[128:131], v[80:83], v[0:3]\n\t" \
    "ds_read_b64_tr_b16 v[96:97], v226\n\t" \
    "buffer_load_dwordx4 v[72:75], v243, %[rsb], s80 offen sc1\n\t" \
    "v_mfma_f32_16x16x32_bf16 v[16:19], v[128:131], v[84:87], v[16:19]\n\t" \
    "ds_read_b64_tr_b16 v[98:99], v226 offset:2048\n\t" \
    "v_add3_u32 v230, v80, v81, v230\n\t" \
    "v_mfma_f32_16x16x32_bf16 v[4:7], v[132:135], v[80:83], v[4:7]\n\t" \
    "ds_read_b64_tr_b16 v[100:101], v227\n\t" \
    "buffer_load_dwordx4 v[76:79], v243, %[rsb], s80 offen offset:1024 sc1\n\t" \
    "v_mfma_f32_16x16x32_bf16 v[20:23], v[132:135], v[84:87], v[20:23]\n\t" \
    "ds_read_b64_tr_b16 v[102:103], v227 offset:2048\n\t" \
    "v_add3_u32 v230, v82, v83, v230\n\t" \
    "v_mfma_f32_16x16x32_bf16 v[8:11], v[136:139], v[80:83], v[8:11]\n\t" \
    "ds_read_b64_tr_b16 v[104:105], v228\n\t" \
    "v_add3_u32 v230, v84, v85, v230\n\t" \
    "v_mfma_f32_16x16x32_bf16 v[24:27], v[136:139], v[84:87], v[24:27]\n\t" \
    "ds_read_b64_tr_b16 v[106:107], v228 offset:2048\n\t" \
    "v_add3_u32 v230, v86, v87, v230\n\t" \
    "v_mfma_f32_16x16x32_bf16 v[12:15], v[140:143], v[80:83], v[12:15]\n\t" \
    "ds_read_b64_tr_b16 v[108:109], v229\n\t" \
    "v_mfma_f32_16x16x32_bf16 v[28:31], v[140:143], v[84:87], v[28:31]\n\t" \
    "ds_read_b64_tr_b16 v[110:111], v229 offset:2048\n\t" \
    "s_waitcnt vmcnt(12) lgkmcnt(8)\n\t" \
    "v_mfma_f32_16x16x32_bf16 v[0:3], v[144:147], v[88:91], v[0:3]\n\t" \
    "ds_read_b64_tr_b16 v[112:113], v226 offset:4096\n\t" \
    "buffer_load_dwordx4 v[80:83], v243, %[rsb], s80 offen offset:2048 sc1\n\t" \
    "v_mfma_f32_16x16x32_bf16 v[16:19], v[144:147], v[92:95], v[16:19]\n\t" \
    "ds_read_b64_tr_b16 v[114:115], v226 offset:6144\n\t" \
    "v_add3_u32 v230, v88, v89, v230\n\t" \
    "v_mfma_f32_16x16x32_bf16 v[4:7], v[148:151], v[88:91], v[4:7]\n\t" \
    "ds_read_b64_tr_b16 v[116:117], v227 offset:4096\n\t" \
    "buffer_load_dwordx4 v[84:87], v243, %[rsb], s80 offen offset:3072 sc1\n\t" \
    "v_mfma_f32_16x16x32_bf16 v[20:23], v[148:151], v[92:95], v[20:23]\n\t" \
    "ds_read_b64_tr_b16 v[118:119], v227 offset:6144\n\t" \
    "v_add3_u32 v230, v90, v91, v230\n\t" \
    "v_mfma_f32_16x16x32_bf16 v[8:11], v[152:155], v[88:91], v[8:11]\n\t" \
    "ds_read_b64_tr_b16 v[120:121], v228 offset:4096\n\t" \
    "v_add3_u32 v230, v92, v93, v230\n\t" \
    "v_mfma_f32_16x16x32_bf16 v[24:27], v[152:155], v[92:95], v[24:27]\n\t" \
    "ds_read_b64_tr_b16 v[122:123], v228 offset:6144\n\t" \
    "v_add3_u32 v230, v94, v95, v230\n\t" \
    "v_mfma_f32_16x16x32_bf16 v[12:15], v[156:159], v[88:91], v[12:15]\n\t" \
    "ds_read_b64_tr_b16 v[124:125], v229 offset:4096\n\t" \
    "s_add_u32 s80, s80, 0x4000\n\t" \
    "s_and_b32 s80, s80, 0xc000\n\t" \
    "v_mfma_f32_16x16x32_bf16 v[28:31], v[156:159], v[92:95], v[28:31]\n\t" \
    "ds_read_b64_tr_b16 v[126:127], v229 offset:6144\n\t" \
    "s_sub_u32 s81, s81, 1\n\t" \
    "s_cmp_lg_u32 s81, 0\n\t" \
    "s_cbranch_scc1 .Lc_l\n\t" \
    "s_waitcnt vmcnt(12) lgkmcnt(8)\n\t" \
    "v_mfma_f32_16x16x32_bf16 v[162:165], v[96:99], v[32:35], 0\n\t" \
    "ds_read_b64_tr_b16 v[128:129], v226 offset:8192\n\t" \
    "buffer_load_dwordx4 v[88:91], v240, %[rsb], s80 offen sc1\n\t" \
    "v_mfma_f32_16x16x32_bf16 v[178:181], v[96:99], v[36:39], 0\n\t" \
    "ds_read_b64_tr_b16 v[130:131], v226 offset:10240\n\t" \
    "v_add3_u32 v230, v32, v33, v230\n\t" \
    "v_mfma_f32_16x16x32_bf16 v[166:169], v[100:103], v[32:35], 0\n\t" \
    "ds_read_b64_tr_b16 v[132:133], v227 offset:8192\n\t" \
    "buffer_load_dwordx4 v[92:95], v240, %[rsb], s80 offen offset:1024 sc1\n\t" \
    "v_mfma_f32_16x16x32_bf16 v[182:185], v[100:103], v[36:39], 0\n\t" \
    "ds_read_b64_tr_b16 v[134:135], v227 offset:10240\n\t" \
    "v_add3_u32 v230, v34, v35, v230\n\t" \
    "v_mfma_f32_16x16x32_bf16 v[170:173], v[104:107], v[32:35], 0\n\t" \
    "ds_read_b64_tr_b16 v[136:137], v228 offset:8192\n\t" \
    "v_add3_u32 v230, v36, v37, v230\n\t" \
    "v_mfma_f32_16x16x32_bf16 v[186:189], v[104:107], v[36:39], 0\n\t" \
    "ds_read_b64_tr_b16 v[138:139], v228 offset:10240\n\t" \
    "v_add3_u32 v230, v38, v39, v230\n\t" \
    "v_mfma_f32_16x16x32_bf16 v[174:177], v[108:111], v[32:35], 0\n\t" \
    "ds_read_b64_tr_b16 v[140:141], v229 offset:8192\n\t" \
    "v_mfma_f32_16x16x32_bf16 v[190:193], v[108:111], v[36:39], 0\n\t" \
    "ds_read_b64_tr_b16 v[142:143], v229 offset:10240\n\t" \
    "s_waitcnt vmcnt(12) lgkmcnt(8)\n\t" \
    "v_mfma_f32_16x16x32_bf16 v[162:165], v[112:115], v[40:43], v[162:165]\n\t" \
    "ds_read_b64_tr_b16 v[144:145], v226 offset:12288\n\t" \
    "buffer_load_dwordx4 v[32:35], v240, %[rsb], s80 offen offset:2048 sc1\n\t" \
    "v_mfma_f32_16x16x32_bf16 v[178:181], v[112:115], v[44:47], v[178:181]\n\t" \
    "ds_read_b64_tr_b16 v[146:147], v226 offset:14336\n\t" \
    "v_add3_u32 v230, v40, v41, v230\n\t" \
    "v_mfma_f32_16x16x32_bf16 v[166:169], v[116:119], v[40:43], v[166:169]\n\t" \
    "ds_read_b64_tr_b16 v[148:149], v227 offset:12288\n\t" \
    "buffer_load_dwordx4 v[36:39], v240, %[rsb], s80 offen offset:3072 sc1\n\t" \
    "v_mfma_f32_16x16x32_bf16 v[182:185], v[116:119], v[44:47], v[182:185]\n\t" \
    "ds_read_b64_tr_b16 v[150:151], v227 offset:14336\n\t" \
    "v_add3_u32 v230, v42, v43, v230\n\t" \
    "v_mfma_f32_16x16x32_bf16 v[170:173], v[120:123], v[40:43], v[170:173]\n\t" \
    "ds_read_b64_tr_b16 v[152:153], v228 offset:12288\n\t" \
    "v_add3_u32 v230, v44, v45, v230\n\t" \
    "v_mfma_f32_16x16x32_bf16 v[186:189], v[120:123], v[44:47], v[186:189]\n\t" \
    "ds_read_b64_tr_b16 v[154:155], v228 offset:14336\n\t" \
    "v_add3_u32 v230, v46, v47, v230\n\t" \
    "v_mfma_f32_16x16x32_bf16 v[174:177], v[124:127], v[40:43], v[174:177]\n\t" \
    "ds_read_b64_tr_b16 v[156:157], v229 offset:12288\n\t" \
    "v_mfma_f32_16x16x32_bf16 v[190:193], v[124:127], v[44:47], v[190:193]\n\t" \
    "ds_read_b64_tr_b16 v[158:159], v229 offset:14336\n\t" \
    "s_waitcnt vmcnt(12) lgkmcnt(8)\n\t" \
    "v_mfma_f32_16x16x32_bf16 v[162:165], v[128:131], v[48:51], v[162:165]\n\t" \
    "ds_read_b64_tr_b16 v[96:97], v226 offset:16384\n\t" \
    "buffer_load_dwordx4 v[40:43], v241, %[rsb], s80 offen sc1\n\t" \
    "v_mfma_f32_16x16x32_bf16 v[178:181], v[128:131], v[52:55], v[178:181]\n\t" \
    "ds_read_b64_tr_b16 v[98:99], v226 offset:18432\n\t" \
    "v_add3_u32 v230, v48, v49, v230\n\t" \
    "v_mfma_f32_16x16x32_bf16 v[166:169], v[132:135], v[48:51], v[166:169]\n\t" \
    "ds_read_b64_tr_b16 v[100:101], v227 offset:16384\n\t" \
    "buffer_load_dwordx4 v[44:47], v241, %[rsb], s80 offen offset:1024 sc1\n\t" \
    "v_mfma_f32_16x16x32_bf16 v[182:185], v[132:135], v[52:55], v[182:185]\n\t" \
    "ds_read_b64_tr_b16 v[102:103], v227 offset:18432\n\t" \
    "v_add3_u32 v230, v50, v51, v230\n\t" \
    "v_mfma_f32_16x16x32_bf16 v[170:173], v[136:139], v[48:51], v[170:173]\n\t" \
    "ds_read_b64_tr_b16 v[104:105], v228 offset:16384\n\t" \
    "v_add3_u32 v230, v52, v53, v230\n\t" \
    "v_mfma_f32_16x16x32_bf16 v[186:189], v[136:139], v[52:55], v[186:189]\n\t" \
    "ds_read_b64_tr_b16 v[106:107], v228 offset:18432\n\t" \
    "v_add3_u32 v230, v54, v55, v230\n\t" \
    "v_mfma_f32_16x16x32_bf16 v[174:177], v[140:143], v[48:51], v[174:177]\n\t" \
    "ds_read_b64_tr_b16 v[108:109], v229 offset:16384\n\t" \
    "v_mfma_f32_16x16x32_bf16 v[190:193], v[140:143], v[52:55], v[190:193]\n\t" \
    "ds_read_b64_tr_b16 v[110:111], v229 offset:18432\n\t" \
    "s_waitcnt vmcnt(12) lgkmcnt(8)\n\t" \
    "v_mfma_f32_16x16x32_bf16 v[162:165], v[144:147], v[56:59], v[162:165]\n\t" \
    "ds_read_b64_tr_b16 v[112:113], v226 offset:20480\n\t" \
    "buffer_load_dwordx4 v[48:51], v241, %[rsb], s80 offen offset:2048 sc1\n\t" \
    "v_mfma_f32_16x16x32_bf16 v[178:181], v[144:147], v[60:63], v[178:181]\n\t" \
    "ds_read_b64_tr_b16 v[114:115], v226 offset:22528\n\t" \
    "v_add3_u32 v230, v56, v57, v230\n\t" \
    "v_mfma_f32_16x16x32_bf16 v[166:169], v[148:151], v[56:59], v[166:169]\n\t" \
    "ds_read_b64_tr_b16 v[116:117], v227 offset:20480\n\t" \
    "buffer_load_dwordx4 v[52:55], v241, %[rsb], s80 offen offset:3072 sc1\n\t" \
    "v_mfma_f32_16x16x32_bf16 v[182:185], v[148:151], v[60:63], v[182:185]\n\t" \
    "ds_read_b64_tr_b16 v[118:119], v227 offset:22528\n\t" \
    "v_add3_u32 v230, v58, v59, v230\n\t" \
    "v_mfma_f32_16x16x32_bf16 v[170:173], v[152:155], v[56:59], v[170:173]\n\t" \
    "ds_read_b64_tr_b16 v[120:121], v228 offset:20480\n\t" \
    "v_add3_u32 v230, v60, v61, v230\n\t" \
    "v_mfma_f32_16x16x32_bf16 v[186:189], v[152:155], v[60:63], v[186:189]\n\t" \
    "ds_read_b64_tr_b16 v[122:123], v228 offset:22528\n\t" \
    "v_add3_u32 v230, v62, v63, v230\n\t" \
    "v_mfma_f32_16x16x32_bf16 v[174:177], v[156:159], v[56:59], v[174:177]\n\t" \
    "ds_read_b64_tr_b16 v[124:125], v229 offset:20480\n\t" \
    "v_mfma_f32_16x16x32_bf16 v[190:193], v[156:159], v[60:63], v[190:193]\n\t" \
    "ds_read_b64_tr_b16 v[126:127], v229 offset:22528\n\t" \
    "s_waitcnt vmcnt(12) lgkmcnt(8)\n\t" \
    "ds_read_b32 v160, v249 offset:56\n\t" \
    "v_mfma_f32_16x16x32_bf16 v[162:165], v[96:99], v[64:67], v[162:165]\n\t" \
    "ds_read_b64_tr_b16 v[128:129], v226 offset:24576\n\t" \
    "buffer_load_dwordx4 v[56:59], v242, %[rsb], s80 offen sc1\n\t" \
    "v_mfma_f32_16x16x32_bf16 v[178:181], v[96:99], v[68:71], v[178:181]\n\t" \
    "ds_read_b64_tr_b16 v[130:131], v226 offset:26624\n\t" \
    "v_add3_u32 v230, v64, v65, v230\n\t" \
    "v_mfma_f32_16x16x32_bf16 v[166:169], v[100:103], v[64:67], v[166:169]\n\t" \
    "ds_read_b64_tr_b16 v[132:133], v227 offset:24576\n\t" \
    "buffer_load_dwordx4 v[60:63], v242, %[rsb], s80 offen offset:1024 sc1\n\t" \
    "v_mfma_f32_16x16x32_bf16 v[182:185], v[100:103], v[68:71], v[182:185]\n\t" \
    "ds_read_b64_tr_b16 v[134:135], v227 offset:26624\n\t" \
    "v_add3_u32 v230, v66, v67, v230\n\t" \
    "v_mfma_f32_16x16x32_bf16 v[170:173], v[104:107], v[64:67], v[170:173]\n\t" \
    "ds_read_b64_tr_b16 v[136:137], v228 offset:24576\n\t" \
    "v_add3_u32 v230, v68, v69, v230\n\t" \
    "v_mfma_f32_16x16x32_bf16 v[186:189], v[104:107], v[68:71], v[186:189]\n\t" \
    "ds_read_b64_tr_b16 v[138:139], v228 offset:26624\n\t" \
    "v_add3_u32 v230, v70, v71, v230\n\t" \
    "v_mfma_f32_16x16x32_bf16 v[174:177], v[108:111], v[64:67], v[174:177]\n\t" \
    "ds_read_b64_tr_b16 v[140:141], v229 offset:24576\n\t" \
    "v_mfma_f32_16x16x32_bf16 v[190:193], v[108:111], v[68:71], v[190:193]\n\t" \
    "ds_read_b64_tr_b16 v[142:143], v229 offset:26624\n\t" \
    "s_waitcnt vmcnt(12) lgkmcnt(8)\n\t" \
    "v_mfma_f32_16x16x32_bf16 v[162:165], v[112:115], v[72:75], v[162:165]\n\t" \
    "ds_read_b64_tr_b16 v[144:145], v226 offset:28672\n\t" \
    "buffer_load_dwordx4 v[64:67], v242, %[rsb], s80 offen offset:2048 sc1\n\t" \
    "v_mfma_f32_16x16x32_bf16 v[178:181], v[112:115], v[76:79], v[178:181]\n\t" \
    "ds_read_b64_tr_b16 v[146:147], v226 offset:30720\n\t" \
    "v_add3_u32 v230, v72, v73, v230\n\t" \
    "v_mfma_f32_16x16x32_bf16 v[166:169], v[116:119], v[72:75], v[166:169]\n\t" \
    "ds_read_b64_tr_b16 v[148:149], v227 offset:28672\n\t" \
    "buffer_load_dwordx4 v[68:71], v242, %[rsb], s80 offen offset:3072 sc1\n\t" \
    "v_add_u32 v226, 0x8000, v226\n\t" \
    "v_and_b32 v226, 0x1ffff, v226\n\t" \
    "v_mfma_f32_16x16x32_bf16 v[182:185], v[116:119], v[76:79], v[182:185]\n\t" \
    "ds_read_b64_tr_b16 v[150:151], v227 offset:30720\n\t" \
    "v_add3_u32 v230, v74, v75, v230\n\t" \
    "v_mfma_f32_16x16x32_bf16 v[170:173], v[120:123], v[72:75], v[170:173]\n\t" \
    "ds_read_b64_tr_b16 v[152:153], v228 offset:28672\n\t" \
    "v_add3_u32 v230, v76, v77, v230\n\t" \
    "v_add_u32 v227, 0x8000, v227\n\t" \
    "v_and_b32 v227, 0x1ffff, v227\n\t" \
    "v_mfma_f32_16x16x32_bf16 v[186:189], v[120:123], v[76:79], v[186:189]\n\t" \
    "ds_read_b64_tr_b16 v[154:155], v228 offset:30720\n\t" \
    "v_add3_u32 v230, v78, v79, v230\n\t" \
    "v_mfma_f32_16x16x32_bf16 v[174:177], v[124:127], v[72:75], v[174:177]\n\t" \
    "v_readfirstlane_b32 s83, v160\n\t" \
    "s_add_u32 s82, s82, 1\n\t" \
    "ds_read_b64_tr_b16 v[156:157], v229 offset:28672\n\t" \
    "v_add_u32 v228, 0x8000, v228\n\t" \
    "v_and_b32 v228, 0x1ffff, v228\n\t" \
    "v_mfma_f32_16x16x32_bf16 v[190:193], v[124:127], v[76:79], v[190:193]\n\t" \
    "s_cmp_lt_u32 s83, s82\n\t" \
    "s_cbranch_scc1 .Lc_w1\n\t" \
    ".Lc_r1:\n\t" \
    "ds_read_b64_tr_b16 v[158:159], v229 offset:30720\n\t" \
    "v_add_u32 v229, 0x8000, v229\n\t" \
    "v_and_b32 v229, 0x1ffff, v229\n\t" \
    "s_waitcnt vmcnt(12) lgkmcnt(8)\n\t" \
    "v_mfma_f32_16x16x32_bf16 v[162:165], v[128:131], v[80:83], v[162:165]\n\t" \
    "ds_read_b64_tr_b16 v[96:97], v226\n\t" \
    "buffer_load_dwordx4 v[72:75], v243, %[rsb], s80 offen sc1\n\t" \
    "v_mfma_f32_16x16x32_bf16 v[178:181], v[128:131], v[84:87], v[178:181]\n\t" \
    "ds_read_b64_tr_b16 v[98:99], v226 offset:2048\n\t" \
    "v_add3_u32 v230, v80, v81, v230\n\t" \
    "v_mfma_f32_16x16x32_bf16 v[166:169], v[132:135], v[80:83], v[166:169]\n\t" \
    "ds_read_b64_tr_b16 v[100:101], v227\n\t" \
    "buffer_load_dwordx4 v[76:79], v243, %[rsb], s80 offen offset:1024 sc1\n\t" \
    "v_mfma_f32_16x16x32_bf16 v[182:185], v[132:135], v[84:87], v[182:185]\n\t" \
    "ds_read_b64_tr_b16 v[102:103], v227 offset:2048\n\t" \
    "v_add3_u32 v230, v82, v83, v230\n\t" \
    "v_mfma_f32_16x16x32_bf16 v[170:173], v[136:139], v[80:83], v[170:173]\n\t" \
    "ds_read_b64_tr_b16 v[104:105], v228\n\t" \
    "v_add3_u32 v230, v84, v85, v230\n\t" \
    "v_mfma_f32_16x16x32_bf16 v[186:189], v[136:139], v[84:87], v[186:189]\n\t" \
    "ds_read_b64_tr_b16 v[106:107], v228 offset:2048\n\t" \
    "v_add3_u32 v230, v86, v87, v230\n\t" \
    "v_mfma_f32_16x16x32_bf16 v[174:177], v[140:143], v[80:83], v[174:177]\n\t" \
    "ds_read_b64_tr_b16 v[108:109], v229\n\t" \
    "v_mfma_f32_16x16x32_bf16 v[190:193], v[140:143], v[84:87], v[190:193]\n\t" \
    "ds_read_b64_tr_b16 v[110:111], v229 offset:2048\n\t" \
    "s_waitcnt vmcnt(12) lgkmcnt(8)\n\t" \
    "v_mfma_f32_16x16x32_bf16 v[162:165], v[144:147], v[88:91], v[162:165]\n\t" \
    "ds_read_b64_tr_b16 v[112:113], v226 offset:4096\n\t" \
    "buffer_load_dwordx4 v[80:83], v243, %[rsb], s80 offen offset:2048 sc1\n\t" \
    "v_mfma_f32_16x16x32_bf16 v[178:181], v[144:147], v[92:95], v[178:181]\n\t" \
    "ds_read_b64_tr_b16 v[114:115], v226 offset:6144\n\t" \
    "v_add3_u32 v230, v88, v89, v230\n\t" \
    "v_mfma_f32_16x16x32_bf16 v[166:169], v[148:151], v[88:91], v[166:169]\n\t" \
    "ds_read_b64_tr_b16 v[116:117], v227 offset:4096\n\t" \
    "buffer_load_dwordx4 v[84:87], v243, %[rsb], s80 offen offset:3072 sc1\n\t" \
    "v_mfma_f32_16x16x32_bf16 v[182:185], v[148:151], v[92:95], v[182:185]\n\t" \
    "ds_read_b64_tr_b16 v[118:119], v227 offset:6144\n\t" \
    "v_add3_u32 v230, v90, v91, v230\n\t" \
    "v_mfma_f32_16x16x32_bf16 v[170:173], v[152:155], v[88:91], v[170:173]\n\t" \
    "ds_read_b64_tr_b16 v[120:121], v228 offset:4096\n\t" \
    "v_add3_u32 v230, v92, v93, v230\n\t" \
    "v_mfma_f32_16x16x32_bf16 v[186:189], v[152:155], v[92:95], v[186:189]\n\t" \
    "ds_read_b64_tr_b16 v[122:123], v228 offset:6144\n\t" \
    "v_add3_u32 v230, v94, v95, v230\n\t" \
    "v_mfma_f32_16x16x32_bf16 v[174:177], v[156:159], v[88:91], v[174:177]\n\t" \
    "ds_read_b64_tr_b16 v[124:125], v229 offset:4096\n\t" \
    "s_add_u32 s80, s80, 0x4000\n\t" \
    "s_and_b32 s80, s80, 0xc000\n\t" \
    "v_mfma_f32_16x16x32_bf16 v[190:193], v[156:159], v[92:95], v[190:193]\n\t" \
    "ds_read_b64_tr_b16 v[126:127], v229 offset:6144\n\t" \
    "s_waitcnt vmcnt(12) lgkmcnt(8)\n\t" \
    "v_mfma_f32_16x16x32_bf16 v[162:165], v[96:99], v[32:35], v[162:165]\n\t" \
    "ds_read_b64_tr_b16 v[128:129], v226 offset:8192\n\t" \
    "buffer_load_dwordx4 v[88:91], v240, %[rsb], s80 offen sc1\n\t" \
    "v_mfma_f32_16x16x32_bf16 v[178:181], v[96:99], v[36:39], v[178:181]\n\t" \
    "ds_read_b64_tr_b16 v[130:131], v226 offset:10240\n\t" \
    "v_add3_u32 v230, v32, v33, v230\n\t" \
    ".Lpad_c1:\n\t" \
    "s_cbranch_execz .Lpad_ret\n\t" \
    "v_mfma_f32_16x16x32_bf16 v[166:169], v[100:103], v[32:35], v[166:169]\n\t" \
    "ds_read_b64_tr_b16 v[132:133], v227 offset:8192\n\t" \
    "buffer_load_dwordx4 v[92:95], v240, %[rsb], s80 offen offset:1024 sc1\n\t" \
    "v_mfma_f32_16x16x32_bf16 v[182:185], v[100:103], v[36:39], v[182:185]\n\t" \
    "ds_read_b64_tr_b16 v[134:135], v227 offset:10240\n\t" \
    "v_add3_u32 v230, v34, v35, v230\n\t" \
    "v_mfma_f32_16x16x32_bf16 v[170:173], v[104:107], v[32:35], v[170:173]\n\t" \
    "ds_read_b64_tr_b16 v[136:137], v228 offset:8192\n\t" \
    "v_add3_u32 v230, v36, v37, v230\n\t" \
    "v_mfma_f32_16x16x32_bf16 v[186:189], v[104:107], v[36:39], v[186:189]\n\t" \
    "ds_read_b64_tr_b16 v[138:139], v228 offset:10240\n\t" \
    "v_add3_u32 v230, v38, v39, v230\n\t" \
    "v_mfma_f32_16x16x32_bf16 v[174:177], v[108:111], v[32:35], v[174:177]\n\t" \
    "ds_read_b64_tr_b16 v[140:141], v229 offset:8192\n\t" \
    "buffer_load_dwordx2 v[160:161], v244, %[rsb], %[sog] offen sc1\n\t" \
    "v_mfma_f32_16x16x32_bf16 v[190:193], v[108:111], v[36:39], v[190:193]\n\t" \
    "ds_read_b64_tr_b16 v[142:143], v229 offset:10240\n\t" \
    "s_waitcnt vmcnt(13) lgkmcnt(8)\n\t" \
    "v_mfma_f32_16x16x32_bf16 v[162:165], v[112:115], v[40:43], v[162:165]\n\t" \
    "ds_read_b64_tr_b16 v[144:145], v226 offset:12288\n\t" \
    "v_mfma_f32_16x16x32_bf16 v[178:181], v[112:115], v[44:47], v[178:181]\n\t" \
    "ds_read_b64_tr_b16 v[146:147], v226 offset:14336\n\t" \
    "v_add3_u32 v230, v40, v41, v230\n\t" \
    "v_mfma_f32_16x16x32_bf16 v[166:169], v[116:119], v[40:43], v[166:169]\n\t" \
    "ds_read_b64_tr_b16 v[148:149], v227 offset:12288\n\t" \
    "v_mfma_f32_16x16x32_bf16 v[182:185], v[116:119], v[44:47], v[182:185]\n\t" \
    "ds_read_b64_tr_b16 v[150:151], v227 offset:14336\n\t" \
    "v_add3_u32 v230, v42, v43, v230\n\t" \
    "v_mfma_f32_16x16x32_bf16 v[170:173], v[120:123], v[40:43], v[170:173]\n\t" \
    "ds_read_b64_tr_b16 v[152:153], v228 offset:12288\n\t" \
    "v_add3_u32 v230, v44, v45, v230\n\t" \
    "v_mfma_f32_16x16x32_bf16 v[186:189], v[120:123], v[44:47], v[186:189]\n\t" \
    "ds_read_b64_tr_b16 v[154:155], v228 offset:14336\n\t" \
    "v_add3_u32 v230, v46, v47, v230\n\t" \
    "v_mfma_f32_16x16x32_bf16 v[174:177], v[124:127], v[40:43], v[174:177]\n\t" \
    "ds_read_b64_tr_b16 v[156:157], v229 offset:12288\n\t" \
    "v_mfma_f32_16x16x32_bf16 v[190:193], v[124:127], v[44:47], v[190:193]\n\t" \
    "ds_read_b64_tr_b16 v[158:159], v229 offset:14336\n\t" \
    "s_waitcnt vmcnt(11) lgkmcnt(8)\n\t" \
    "v_mfma_f32_16x16x32_bf16 v[162:165], v[128:131], v[48:51], v[162:165]\n\t" \
    "ds_read_b64_tr_b16 v[96:97], v226 offset:16384\n\t" \
    "v_mfma_f32_16x16x32_bf16 v[178:181], v[128:131], v[52:55], v[178:181]\n\t" \
    "ds_read_b64_tr_b16 v[98:99], v226 offset:18432\n\t" \
    "v_add3_u32 v230, v48, v49, v230\n\t" \
    "v_mfma_f32_16x16x32_bf16 v[166:169], v[132:135], v[48:51], v[166:169]\n\t" \
    "ds_read_b64_tr_b16 v[100:101], v227 offset:16384\n\t" \
    "v_mfma_f32_16x16x32_bf16 v[182:185], v[132:135], v[52:55], v[182:185]\n\t" \
    "ds_read_b64_tr_b16 v[102:103], v227 offset:18432\n\t" \
    "v_add3_u32 v230, v50, v51, v230\n\t" \
    "v_mfma_f32_16x16x32_bf16 v[170:173], v[136:139], v[48:51], v[170:173]\n\t" \
    "ds_read_b64_tr_b16 v[104:105], v228 offset:16384\n\t" \
    "v_add3_u32 v230, v52, v53, v230\n\t" \
    "v_mfma_f32_16x16x32_bf16 v[186:189], v[136:139], v[52:55], v[186:189]\n\t" \
    "ds_read_b64_tr_b16 v[106:107], v228 offset:18432\n\t" \
    "v_add3_u32 v230, v54, v55, v230\n\t" \
    "v_mfma_f32_16x16x32_bf16 v[174:177], v[140:143], v[48:51], v[174:177]\n\t" \
    "ds_read_b64_tr_b16 v[108:109], v229 offset:16384\n\t" \
    "v_mfma_f32_16x16x32_bf16 v[190:193], v[140:143], v[52:55], v[190:193]\n\t" \
    "ds_read_b64_tr_b16 v[110:111], v229 offset:18432\n\t" \
    "s_waitcnt vmcnt(9) lgkmcnt(8)\n\t" \
    "v_mfma_f32_16x16x32_bf16 v[162:165], v[144:147], v[56:59], v[162:165]\n\t" \
    "ds_read_b64_tr_b16 v[112:113], v226 offset:20480\n\t" \
    "v_mfma_f32_16x16x32_bf16 v[178:181], v[144:147], v[60:63], v[178:181]\n\t" \
    "ds_read_b64_tr_b16 v[114:115], v226 offset:22528\n\t" \
    "v_add3_u32 v230, v56, v57, v230\n\t" \
    "v_mfma_f32_16x16x32_bf16 v[166:169], v[148:151], v[56:59], v[166:169]\n\t" \
    "ds_read_b64_tr_b16 v[116:117], v227 offset:20480\n\t" \
    "v_mfma_f32_16x16x32_bf16 v[182:185], v[148:151], v[60:63], v[182:185]\n\t" \
    "ds_read_b64_tr_b16 v[118:119], v227 offset:22528\n\t" \
    "v_add3_u32 v230, v58, v59, v230\n\t" \
    "v_mfma_f32_16x16x32_bf16 v[170:173], v[152:155], v[56:59], v[170:173]\n\t" \
    "ds_read_b64_tr_b16 v[120:121], v228 offset:20480\n\t" \
    "v_add3_u32 v230, v60, v61, v230\n\t" \
    "v_mfma_f32_16x16x32_bf16 v[186:189], v[152:155], v[60:63], v[186:189]\n\t" \
    "ds_read_b64_tr_b16 v[122:123], v228 offset:22528\n\t" \
    "v_add3_u32 v230, v62, v63, v230\n\t" \
    "v_mfma_f32_16x16x32_bf16 v[174:177], v[156:159], v[56:59], v[174:177]\n\t" \
    "ds_read_b64_tr_b16 v[124:125], v229 offset:20480\n\t" \
    "v_mfma_f32_16x16x32_bf16 v[190:193], v[156:159], v[60:63], v[190:193]\n\t" \
    "ds_read_b64_tr_b16 v[126:127], v229 offset:22528\n\t" \
    "s_waitcnt vmcnt(7) lgkmcnt(8)\n\t" \
    "v_mfma_f32_16x16x32_bf16 v[162:165], v[96:99], v[64:67], v[162:165]\n\t" \
    "ds_read_b64_tr_b16 v[128:129], v226 offset:24576\n\t" \
    "v_mfma_f32_16x16x32_bf16 v[178:181], v[96:99], v[68:71], v[178:181]\n\t" \
    "ds_read_b64_tr_b16 v[130:131], v226 offset:26624\n\t" \
    "v_add3_u32 v230, v64, v65, v230\n\t" \
    "v_mfma_f32_16x16x32_bf16 v[166:169], v[100:103], v[64:67], v[166:169]\n\t" \
    "ds_read_b64_tr_b16 v[132:133], v227 offset:24576\n\t" \
    "v_mfma_f32_16x16x32_bf16 v[182:185], v[100:103], v[68:71], v[182:185]\n\t" \
    "ds_read_b64_tr_b16 v[134:135], v227 offset:26624\n\t" \
    "v_add3_u32 v230, v66, v67, v230\n\t" \
    "v_mfma_f32_16x16x32_bf16 v[170:173], v[104:107], v[64:67], v[170:173]\n\t" \
    "ds_read_b64_tr_b16 v[136:137], v228 offset:24576\n\t" \
    "v_add3_u32 v230, v68, v69, v230\n\t" \
    "v_mfma_f32_16x16x32_bf16 v[186:189], v[104:107], v[68:71], v[186:189]\n\t" \
    "ds_read_b64_tr_b16 v[138:139], v228 offset:26624\n\t" \
    "v_add3_u32 v230, v70, v71, v230\n\t" \
    "v_mfma_f32_16x16x32_bf16 v[174:177], v[108:111], v[64:67], v[174:177]\n\t" \
    "ds_read_b64_tr_b16 v[140:141], v229 offset:24576\n\t" \
    "v_mfma_f32_16x16x32_bf16 v[190:193], v[108:111], v[68:71], v[190:193]\n\t" \
    "ds_read_b64_tr_b16 v[142:143], v229 offset:26624\n\t" \
    "s_waitcnt vmcnt(5) lgkmcnt(8)\n\t" \
    "v_mfma_f32_16x16x32_bf16 v[162:165], v[112:115], v[72:75], v[162:165]\n\t" \
    "ds_read_b64_tr_b16 v[144:145], v226 offset:28672\n\t" \
    "v_mfma_f32_16x16x32_bf16 v[178:181], v[112:115], v[76:79], v[178:181]\n\t" \
    "ds_read_b64_tr_b16 v[146:147], v226 offset:30720\n\t" \
    "v_add3_u32 v230, v72, v73, v230\n\t" \
    "v_mfma_f32_16x16x32_bf16 v[166:169], v[116:119], v[72:75], v[166:169]\n\t" \
    "ds_read_b64_tr_b16 v[148:149], v227 offset:28672\n\t" \
    "v_mfma_f32_16x16x32_bf16 v[182:185], v[116:119], v[76:79], v[182:185]\n\t" \
    "ds_read_b64_tr_b16 v[150:151], v227 offset:30720\n\t" \
    "v_add3_u32 v230, v74, v75, v230\n\t" \
    "v_mfma_f32_16x16x32_bf16 v[170:173], v[120:123], v[72:75], v[170:173]\n\t" \
    "ds_read_b64_tr_b16 v[152:153], v228 offset:28672\n\t" \
    "v_add3_u32 v230, v76, v77, v230\n\t" \
    "v_mfma_f32_16x16x32_bf16 v[186:189], v[120:123], v[76:79], v[186:189]\n\t" \
    "ds_read_b64_tr_b16 v[154:155], v228 offset:30720\n\t" \
    "v_add3_u32 v230, v78, v79, v230\n\t" \
    "v_mfma_f32_16x16x32_bf16 v[174:177], v[124:127], v[72:75], v[174:177]\n\t" \
    "ds_read_b64_tr_b16 v[156:157], v229 offset:28672\n\t" \
    "v_mfma_f32_16x16x32_bf16 v[190:193], v[124:127], v[76:79], v[190:193]\n\t" \
    "ds_read_b64_tr_b16 v[158:159], v229 offset:30720\n\t" \
    "s_waitcnt vmcnt(3) lgkmcnt(8)\n\t" \
    "v_mfma_f32_16x16x32_bf16 v[162:165], v[128:131], v[80:83], v[162:165]\n\t" \
    "ds_read_b32 v231, v249 offset:68\n\t" \
    "v_mfma_f32_16x16x32_bf16 v[178:181], v[128:131], v[84:87], v[178:181]\n\t" \
    "v_add3_u32 v230, v80, v81, v230\n\t" \
    "v_mfma_f32_16x16x32_bf16 v[166:169], v[132:135], v[80:83], v[166:169]\n\t" \
    "ds_read_b128 v[96:99], v252\n\t" \
    "v_mfma_f32_16x16x32_bf16 v[182:185], v[132:135], v[84:87], v[182:185]\n\t" \
    "ds_read_b128 v[100:103], v252 offset:64\n\t" \
    "v_add3_u32 v230, v82, v83, v230\n\t" \
    "v_mfma_f32_16x16x32_bf16 v[170:173], v[136:139], v[80:83], v[170:173]\n\t" \
    "ds_read_b128 v[104:107], v252 offset:128\n\t" \
    "v_add3_u32 v230, v84, v85, v230\n\t" \
    "v_mfma_f32_16x16x32_bf16 v[186:189], v[136:139], v[84:87], v[186:189]\n\t" \
    "ds_read_b128 v[108:111], v252 offset:192\n\t" \
    "v_add3_u32 v230, v86, v87, v230\n\t" \
    "v_mfma_f32_16x16x32_bf16 v[174:177], v[140:143], v[80:83], v[174:177]\n\t" \
    "v_mfma_f32_16x16x32_bf16 v[190:193], v[140:143], v[84:87], v[190:193]\n\t" \
    "s_waitcnt vmcnt(1) lgkmcnt(5)\n\t" \
    "v_mfma_f32_16x16x32_bf16 v[162:165], v[144:147], v[88:91], v[162:165]\n\t" \
    "v_mfma_f32_16x16x32_bf16 v[166:169], v[148:151], v[88:91], v[166:169]\n\t" \
    "v_add3_u32 v230, v88, v89, v230\n\t" \
    "v_mfma_f32_16x16x32_bf16 v[170:173], v[152:155], v[88:91], v[170:173]\n\t" \
    "v_mfma_f32_16x16x32_bf16 v[174:177], v[156:159], v[88:91], v[174:177]\n\t" \
    "v_add3_u32 v230, v90, v91, v230\n\t" \
    "v_mfma_f32_16x16x32_bf16 v[178:181], v[144:147], v[92:95], v[178:181]\n\t" \
    "v_add3_u32 v230, v92, v93, v230\n\t" \
    "v_mfma_f32_16x16x32_bf16 v[182:185], v[148:151], v[92:95], v[182:185]\n\t" \
    "v_add3_u32 v230, v94, v95, v230\n\t" \
    "v_mfma_f32_16x16x32_bf16 v[186:189], v[152:155], v[92:95], v[186:189]\n\t" \
    "s_cmp_lg_u64 %[rm], 0\n\t" \
    "v_mfma_f32_16x16x32_bf16 v[190:193], v[156:159], v[92:95], v[190:193]\n\t" \
    "s_cbranch_scc1 .Lc_y\n\t" \
    ".Lc_yr:\n\t" \
    "s_add_u32 s85, %[sout], 0x8000\n\t" \
    "v_pk_add_f32 v[0:1], v[0:1], v[162:163]\n\t" \
    "v_pk_add_f32 v[2:3], v[2:3], v[164:165]\n\t" \
    "v_pk_add_f32 v[4:5], v[4:5], v[166:167]\n\t" \
    "v_pk_add_f32 v[6:7], v[6:7], v[168:169]\n\t" \
    "v_pk_fma_f32 v[0:1], v[232:233], v[0:1], v[194:195] op_sel_hi:[0,1,1]\n\t" \
    "v_pk_fma_f32 v[2:3], v[232:233], v[2:3], v[196:197] op_sel_hi:[0,1,1]\n\t" \
    "v_pk_fma_f32 v[4:5], v[232:233], v[4:5], v[198:199] op_sel_hi:[0,1,1]\n\t" \
    "v_pk_fma_f32 v[6:7], v[232:233], v[6:7], v[200:201] op_sel_hi:[0,1,1]\n\t" \
    "v_cvt_pk_bf16_f32 v32, v0, v1\n\t" \
    "v_cvt_pk_bf16_f32 v34, v4, v5\n\t" \
    "v_cvt_pk_bf16_f32 v33, v2, v3\n\t" \
    "v_cvt_pk_bf16_f32 v35, v6, v7\n\t" \
    "s_nop 0\n\t" \
    "v_permlane16_swap_b32 v32, v34\n\t" \
    "v_permlane16_swap_b32 v33, v35\n\t" \
    "buffer_store_dwordx4 v[32:35], v247, %[rso], %[sout] offen sc1\n\t" \
    "v_pk_add_f32 v[8:9], v[8:9], v[170:171]\n\t" \
    "v_pk_add_f32 v[10:11], v[10:11], v[172:173]\n\t" \
    "v_pk_add_f32 v[12:13], v[12:13], v[174:175]\n\t" \
    "v_pk_add_f32 v[14:15], v[14:15], v[176:177]\n\t" \
    "v_pk_fma_f32 v[8:9], v[232:233], v[8:9], v[202:203] op_sel_hi:[0,1,1]\n\t" \
    "v_pk_fma_f32 v[10:11], v[232:233], v[10:11], v[204:205] op_sel_hi:[0,1,1]\n\t" \
    "v_pk_fma_f32 v[12:13], v[232:233], v[12:13], v[206:207] op_sel_hi:[0,1,1]\n\t" \
    "v_pk_fma_f32 v[14:15], v[232:233], v[14:15], v[208:209] op_sel_hi:[0,1,1]\n\t" \
    "v_cvt_pk_bf16_f32 v36, v8, v9\n\t" \
    "v_cvt_pk_bf16_f32 v38, v12, v13\n\t" \
    "v_cvt_pk_bf16_f32 v37, v10, v11\n\t" \
    "v_cvt_pk_bf16_f32 v39, v14, v15\n\t" \
    "v_cmp_gt_u32 vcc, 32, v250\n\t" \
    "v_permlane16_swap_b32 v36, v38\n\t" \
    "v_permlane16_swap_b32 v37, v39\n\t" \
    "buffer_store_dwordx4 v[36:39], v247, %[rso], %[sout] offen offset:64 sc1\n\t" \
    "v_pk_add_f32 v[16:17], v[16:17], v[178:179]\n\t" \
    "v_pk_add_f32 v[18:19], v[18:19], v[180:181]\n\t" \
    "v_pk_add_f32 v[20:21], v[20:21], v[182:183]\n\t" \
    "v_pk_add_f32 v[22:23], v[22:23], v[184:185]\n\t" \
    "v_pk_fma_f32 v[16:17], v[234:235], v[16:17], v[210:211] op_sel_hi:[0,1,1]\n\t" \
    "v_pk_fma_f32 v[18:19], v[234:235], v[18:19], v[212:213] op_sel_hi:[0,1,1]\n\t" \
    "v_pk_fma_f32 v[20:21], v[234:235], v[20:21], v[214:215] op_sel_hi:[0,1,1]\n\t" \
    "v_pk_fma_f32 v[22:23], v[234:235], v[22:23], v[216:217] op_sel_hi:[0,1,1]\n\t" \
    "v_cvt_pk_bf16_f32 v40, v16, v17\n\t" \
    "v_cvt_pk_bf16_f32 v42, v20, v21\n\t" \
    "v_cvt_pk_bf16_f32 v41, v18, v19\n\t" \
    "v_cvt_pk_bf16_f32 v43, v22, v23\n\t" \
    "s_waitcnt vmcnt(2)\n\t" \
    "v_permlane16_swap_b32 v40, v42\n\t" \
    "v_permlane16_swap_b32 v41, v43\n\t" \
    "buffer_store_dwordx4 v[40:43], v247, %[rso], s85 offen sc1\n\t" \
    "v_pk_add_f32 v[24:25], v[24:25], v[186:187]\n\t" \
    "v_pk_add_f32 v[26:27], v[26:27], v[188:189]\n\t" \
    "v_pk_add_f32 v[28:29], v[28:29], v[190:191]\n\t" \
    "v_pk_add_f32 v[30:31], v[30:31], v[192:193]\n\t" \
    "v_pk_fma_f32 v[24:25], v[234:235], v[24:25], v[218:219] op_sel_hi:[0,1,1]\n\t" \
    "v_pk_fma_f32 v[26:27], v[234:235], v[26:27], v[220:221] op_sel_hi:[0,1,1]\n\t" \
    "v_pk_fma_f32 v[28:29], v[234:235], v[28:29], v[222:223] op_sel_hi:[0,1,1]\n\t" \
    "v_pk_fma_f32 v[30:31], v[234:235], v[30:31], v[224:225] op_sel_hi:[0,1,1]\n\t" \
    "v_cvt_pk_bf16_f32 v44, v24, v25\n\t" \
    "v_cvt_pk_bf16_f32 v46, v28, v29\n\t" \
    "v_cvt_pk_bf16_f32 v45, v26, v27\n\t" \
    "v_cvt_pk_bf16_f32 v47, v30, v31\n\t" \
    "s_waitcnt lgkmcnt(4)\n\t" \
    "v_cndmask_b32 v231, 0, v160, vcc\n\t" \
    "v_permlane16_swap_b32 v44, v46\n\t" \
    "v_permlane16_swap_b32 v45, v47\n\t" \
    "buffer_store_dwordx4 v[44:47], v247, %[rso], s85 offen offset:64 sc1\n\t" \
    "v_sub_u32 v231, v230, v231\n\t" \
    "v_cmp_ne_u32 vcc, v236, v161\n\t" \
    "v_mov_b32 v0, v231\n\t" \
    "s_nop 1\n\t" \
    "v_permlane32_swap_b32 v231, v0\n\t" \
    "v_add_u32 v231, v231, v0\n\t" \
    "v_mov_b32 v0, v231\n\t" \
    "s_nop 1\n\t" \
    "v_permlane16_swap_b32 v231, v0\n\t" \
    "v_add_u32 v231, v231, v0\n\t" \
    "s_nop 1\n\t" \
    "v_add_u32_dpp v231, v231, v231 row_ror:8 row_mask:0xf bank_mask:0xf bound_ctrl:1\n\t" \
    "s_nop 1\n\t" \
    "v_add_u32_dpp v231, v231, v231 row_ror:4 row_mask:0xf bank_mask:0xf bound_ctrl:1\n\t" \
    "s_nop 1\n\t" \
    "v_add_u32_dpp v231, v231, v231 row_ror:2 row_mask:0xf bank_mask:0xf bound_ctrl:1\n\t" \
    "s_nop 1\n\t" \
    "v_add_u32_dpp v231, v231, v231 row_ror:1 row_mask:0xf bank_mask:0xf bound_ctrl:1\n\t" \
    "s_nop 0\n\t" \
    "v_readfirstlane_b32 s83, v231\n\t" \
    "s_or_b32 s83, s83, vcc_lo\n\t" \
    "s_cselect_b32 %[st], 2, 0\n\t" \
    "s_waitcnt lgkmcnt(0)\n\t" \
    ".Lc_end:\n\t"
#define RED1_ASM \
    "s_mov_b32 s80, 256\n\t" \
    "s_add_u32 s81, %[s0], 0x1000\n\t" \
    "v_mov_b32 v41, 1\n\t" \
    ".Lr_t:\n\t" \
    "s_memrealtime s[82:83]\n\t" \
    "s_sub_u32 s80, s80, 1\n\t" \
    "s_cbranch_scc1 .Lr_go\n\t" \
    "s_waitcnt lgkmcnt(0)\n\t" \
    "s_sub_u32 s82, %[t1], s82\n\t" \
    "s_cmp_lt_i32 s82, 5\n\t" \
    "s_cbranch_scc1 .Lr_go\n\t" \
    "s_cmp_lt_i32 s82, 13\n\t" \
    "s_cbranch_scc1 .Lr_t\n\t" \
    "s_sleep 2\n\t" \
    "s_branch .Lr_t\n\t" \
    ".Lr_go:\n\t" \
    "buffer_load_dwordx4 v[0:3], %[v16], %[rsb], %[sm] offen sc1\n\t" \
    "buffer_load_dwordx4 v[4:7], %[v16], %[rsb], %[s0] offen sc1\n\t" \
    "buffer_load_dwordx4 v[8:11], %[v16], %[rsb], %[s0] offen offset:1024 sc1\n\t" \
    "buffer_load_dwordx4 v[12:15], %[v16], %[rsb], %[s0] offen offset:2048 sc1\n\t" \
    "buffer_load_dwordx4 v[16:19], %[v16], %[rsb], %[s0] offen offset:3072 sc1\n\t" \
    "buffer_load_dwordx4 v[20:23], %[v16], %[rsb], s81 offen sc1\n\t" \
    "buffer_load_dwordx4 v[24:27], %[v16], %[rsb], s81 offen offset:1024 sc1\n\t" \
    "buffer_load_dwordx4 v[28:31], %[v16], %[rsb], s81 offen offset:2048 sc1\n\t" \
    "buffer_load_dwordx4 v[32:35], %[v16], %[rsb], s81 offen offset:3072 sc1\n\t" \
    "ds_write_b32 %[ra], v41\n\t" \
    "s_waitcnt vmcnt(8)\n\t" \
    "v_add3_u32 v40, v0, v1, v2\n\t" \
    "v_xor_b32 v41, %[tag], v1\n\t" \
    "v_and_b32 v42, %[mk2], v0\n\t" \
    "v_add_u32 v40, v40, v3\n\t" \
    "v_add_f32_dpp v0, v0, v0 row_ror:8 row_mask:0xf bank_mask:0xf bound_ctrl:1\n\t" \
    "v_add_f32_dpp v1, v1, v1 row_ror:8 row_mask:0xf bank_mask:0xf bound_ctrl:1\n\t" \
    "v_add_f32_dpp v2, v2, v2 row_ror:8 row_mask:0xf bank_mask:0xf bound_ctrl:1\n\t" \
    "v_add_f32_dpp v3, v3, v3 row_ror:8 row_mask:0xf bank_mask:0xf bound_ctrl:1\n\t" \
    "v_and_b32 v40, %[mk01], v40\n\t" \
    "v_sub_u32 v40, v40, v42\n\t" \
    "s_waitcnt vmcnt(7)\n\t" \
    "v_mfma_f32_16x16x32_bf16 v[36:39], %[sel], v[4:7], 0\n\t" \
    "v_add3_u32 v40, v4, v5, v40\n\t" \
    "v_add3_u32 v40, v6, v7, v40\n\t" \
    "v_add_f32_dpp v0, v0, v0 row_ror:4 row_mask:0xf bank_mask:0xf bound_ctrl:1\n\t" \
    "v_add_f32_dpp v1, v1, v1 row_ror:4 row_mask:0xf bank_mask:0xf bound_ctrl:1\n\t" \
    "s_waitcnt vmcnt(6)\n\t" \
    "v_mfma_f32_16x16x32_bf16 v[36:39], %[sel], v[8:11], v[36:39]\n\t" \
    "v_add3_u32 v40, v8, v9, v40\n\t" \
    "v_add3_u32 v40, v10, v11, v40\n\t" \
    "v_add_f32_dpp v2, v2, v2 row_ror:4 row_mask:0xf bank_mask:0xf bound_ctrl:1\n\t" \
    "v_add_f32_dpp v3, v3, v3 row_ror:4 row_mask:0xf bank_mask:0xf bound_ctrl:1\n\t" \
    "s_waitcnt vmcnt(5)\n\t" \
    "v_mfma_f32_16x16x32_bf16 v[36:39], %[sel], v[12:15], v[36:39]\n\t" \
    "v_add3_u32 v40, v12, v13, v40\n\t" \
    "v_add3_u32 v40, v14, v15, v40\n\t" \
    "v_and_b32 v41, %[mk2], v41\n\t" \
    "v_cmp_ne_u32 vcc, 0, v41\n\t" \
    "s_waitcnt vmcnt(4)\n\t" \
    "v_mfma_f32_16x16x32_bf16 v[36:39], %[sel], v[16:19], v[36:39]\n\t" \
    "v_add3_u32 v40, v16, v17, v40\n\t" \
    "v_add3_u32 v40, v18, v19, v40\n\t" \
    "v_permlane32_swap_b32 v0, v2\n\t" \
    "v_permlane32_swap_b32 v1, v3\n\t" \
    "s_waitcnt vmcnt(3)\n\t" \
    "v_mfma_f32_16x16x32_bf16 v[36:39], %[sel], v[20:23], v[36:39]\n\t" \
    "v_add3_u32 v40, v20, v21, v40\n\t" \
    "v_add3_u32 v40, v22, v23, v40\n\t" \
    "v_add_f32 v0, v0, v2\n\t" \
    "v_add_f32 v1, v1, v3\n\t" \
    "s_waitcnt vmcnt(2)\n\t" \
    "v_mfma_f32_16x16x32_bf16 v[36:39], %[sel], v[24:27], v[36:39]\n\t" \
    "v_add3_u32 v40, v24, v25, v40\n\t" \
    "v_add3_u32 v40, v26, v27, v40\n\t" \
    "v_mov_b32 v2, v0\n\t" \
    "v_mov_b32 v3, v1\n\t" \
    "s_waitcnt vmcnt(1)\n\t" \
    "v_mfma_f32_16x16x32_bf16 v[36:39], %[sel], v[28:31], v[36:39]\n\t" \
    "v_add3_u32 v40, v28, v29, v40\n\t" \
    "v_add3_u32 v40, v30, v31, v40\n\t" \
    "s_or_b32 s80, vcc_lo, vcc_hi\n\t" \
    "s_waitcnt vmcnt(0)\n\t" \
    "v_mfma_f32_16x16x32_bf16 v[36:39], %[sel], v[32:35], v[36:39]\n\t" \
    "v_add3_u32 v40, v32, v33, v40\n\t" \
    "v_add3_u32 v40, v34, v35, v40\n\t" \
    "v_permlane16_swap_b32 v0, v2\n\t" \
    "v_permlane16_swap_b32 v1, v3\n\t" \
    "v_add_f32 v0, v0, v2\n\t" \
    "v_add_f32 v1, v1, v3\n\t" \
    "v_cvt_pk_bf16_f32 v44, v0, v1\n\t" \
    "v_mov_b32 v45, v44\n\t" \
    "v_cvt_pk_bf16_f32 v48, v36, v37\n\t" \
    "v_cvt_pk_bf16_f32 v49, v38, v39\n\t" \
    "v_and_b32 v46, %[mkl], v44\n\t" \
    "v_add3_u32 v46, v46, v48, v49\n\t" \
    "v_permlane32_swap_b32 v44, v45\n\t" \
    "buffer_store_dwordx2 v[48:49], %[vkf], %[rsb], %[sf] offen sc1\n\t" \
    "v_permlane32_swap_b32 v40, v46\n\t" \
    "v_add_u32 v40, v40, v46\n\t" \
    "v_mov_b32 v46, v40\n\t" \
    "v_cndmask_b32_e64 v45, v45, %[tag], %[hi]\n\t" \
    "s_nop 0\n\t" \
    "v_permlane16_swap_b32 v40, v46\n\t" \
    "v_add_u32 v40, v40, v46\n\t" \
    "s_nop 1\n\t" \
    "v_add_u32_dpp v40, v40, v40 row_ror:8 row_mask:0xf bank_mask:0xf bound_ctrl:1\n\t" \
    "s_nop 1\n\t" \
    "v_add_u32_dpp v40, v40, v40 row_ror:4 row_mask:0xf bank_mask:0xf bound_ctrl:1\n\t" \
    "s_nop 1\n\t" \
    "v_add_u32_dpp v40, v40, v40 row_ror:2 row_mask:0xf bank_mask:0xf bound_ctrl:1\n\t" \
    "s_nop 1\n\t" \
    "v_add_u32_dpp v40, v40, v40 row_ror:1 row_mask:0xf bank_mask:0xf bound_ctrl:1\n\t" \
    "s_nop 0\n\t" \
    "v_readfirstlane_b32 s81, v40\n\t" \
    "v_cndmask_b32_e64 v44, v44, v40, %[hi]\n\t" \
    "s_or_b32 %[st], s80, s81\n\t" \
    "s_cselect_b32 s82, 0x70000000, %[sg]\n\t" \
    "buffer_store_dwordx2 v[44:45], %[vfm], %[rsb], s82 offen sc1\n\t"
#define RED2_ASM \
    "s_mov_b32 s80, 256\n\t" \
    "s_add_u32 s81, %[s0], 0x1000\n\t" \
    "s_add_u32 s84, %[s0], 0x4000\n\t" \
    "s_mov_b32 m0, 160256\n\t" \
    ".Lr_u:\n\t" \
    "s_memrealtime s[82:83]\n\t" \
    "s_sub_u32 s80, s80, 1\n\t" \
    "s_cbranch_scc1 .Lr_g2\n\t" \
    "s_waitcnt lgkmcnt(0)\n\t" \
    "s_sub_u32 s82, %[t2], s82\n\t" \
    "s_cmp_lt_i32 s82, 5\n\t" \
    "s_cbranch_scc1 .Lr_g2\n\t" \
    "s_cmp_lt_i32 s82, 13\n\t" \
    "s_cbranch_scc1 .Lr_u\n\t" \
    "s_sleep 2\n\t" \
    "s_branch .Lr_u\n\t" \
    ".Lr_g2:\n\t" \
    "buffer_load_dwordx4 %[v16], %[rsb], s84 offen sc1 lds\n\t" \
    "s_mov_b32 m0, 151552\n\t" \
    "s_nop 0\n\t" \
    "buffer_load_dwordx4 %[v16], %[rsb], %[s0] offen sc1 lds\n\t" \
    "buffer_load_dwordx4 %[v16], %[rsb], %[s0] offen offset:1024 sc1 lds\n\t" \
    "buffer_load_dwordx4 %[v16], %[rsb], %[s0] offen offset:2048 sc1 lds\n\t" \
    "buffer_load_dwordx4 %[v16], %[rsb], %[s0] offen offset:3072 sc1 lds\n\t" \
    "s_mov_b32 m0, 155648\n\t" \
    "s_nop 0\n\t" \
    "buffer_load_dwordx4 %[v16], %[rsb], s81 offen sc1 lds\n\t" \
    "buffer_load_dwordx4 %[v16], %[rsb], s81 offen offset:1024 sc1 lds\n\t" \
    "buffer_load_dwordx4 %[v16], %[rsb], s81 offen offset:2048 sc1 lds\n\t" \
    "buffer_load_dwordx4 %[v16], %[rsb], s81 offen offset:3072 sc1 lds\n\t" \
    "ds_write_b32 %[ra], %[pv]\n\t" \
    "v_or_b32 %[pv], 1, %[pv]\n\t" \
    "v_add_u32 %[pv], 1, %[pv]\n\t" \
    "s_waitcnt vmcnt(0)\n\t" \
    "ds_write_b32 %[ra], %[pv]\n\t" \
    "ds_read_b128 v[0:3], %[kwa] offset:8704\n\t" \
    "ds_read_b128 v[4:7], %[kwa]\n\t" \
    "ds_read_b128 v[8:11], %[kwa] offset:1024\n\t" \
    "ds_read_b128 v[12:15], %[kwa] offset:2048\n\t" \
    "ds_read_b128 v[16:19], %[kwa] offset:3072\n\t" \
    "ds_read_b128 v[20:23], %[kwa] offset:4096\n\t" \
    "ds_read_b128 v[24:27], %[kwa] offset:5120\n\t" \
    "ds_read_b128 v[28:31], %[kwa] offset:6144\n\t" \
    "ds_read_b128 v[32:35], %[kwa] offset:7168\n\t" \
    "s_waitcnt lgkmcnt(8)\n\t" \
    "v_add3_u32 v40, v0, v1, v2\n\t" \
    "v_xor_b32 v41, %[tag], v1\n\t" \
    "v_and_b32 v42, %[mk1], v0\n\t" \
    "v_add_u32 v40, v40, v3\n\t" \
    "v_and_b32 v41, %[mk1], v41\n\t" \
    "v_and_b32 v40, %[mk0], v40\n\t" \
    "v_cmp_ne_u32 vcc, 0, v41\n\t" \
    "v_sub_u32 v40, v40, v42\n\t" \
    "v_mov_b32 v42, %[frc]\n\t" \
    "s_waitcnt lgkmcnt(4)\n\t" \
    "v_add3_u32 v40, v4, v5, v40\n\t" \
    "v_add3_u32 v42, v6, v7, v42\n\t" \
    "v_add3_u32 v40, v8, v9, v40\n\t" \
    "v_add3_u32 v42, v10, v11, v42\n\t" \
    "v_add3_u32 v40, v12, v13, v40\n\t" \
    "v_add3_u32 v42, v14, v15, v42\n\t" \
    "v_add3_u32 v40, v16, v17, v40\n\t" \
    "v_add3_u32 v42, v18, v19, v42\n\t" \
    "s_waitcnt lgkmcnt(0)\n\t" \
    "v_add3_u32 v40, v20, v21, v40\n\t" \
    "v_add3_u32 v42, v22, v23, v42\n\t" \
    "v_add3_u32 v40, v24, v25, v40\n\t" \
    "v_add3_u32 v42, v26, v27, v42\n\t" \
    "v_add3_u32 v40, v28, v29, v40\n\t" \
    "v_add3_u32 v42, v30, v31, v42\n\t" \
    "v_add3_u32 v40, v32, v33, v40\n\t" \
    "v_add3_u32 v42, v34, v35, v42\n\t" \
    "s_or_b32 s80, vcc_lo, vcc_hi\n\t" \
    "s_nop 0\n\t" \
    "v_permlane32_swap_b32 v40, v42\n\t" \
    "v_add_u32 v40, v40, v42\n\t" \
    "s_nop 1\n\t" \
    "v_add_u32_dpp v40, v40, v40 row_ror:8 row_mask:0xf bank_mask:0xf bound_ctrl:1\n\t" \
    "s_nop 1\n\t" \
    "v_add_u32_dpp v40, v40, v40 row_ror:4 row_mask:0xf bank_mask:0xf bound_ctrl:1\n\t" \
    "s_nop 1\n\t" \
    "v_add_u32_dpp v40, v40, v40 row_ror:2 row_mask:0xf bank_mask:0xf bound_ctrl:1\n\t" \
    "s_nop 1\n\t" \
    "v_add_u32_dpp v40, v40, v40 row_ror:1 row_mask:0xf bank_mask:0xf bound_ctrl:1\n\t" \
    "s_nop 0\n\t" \
    "v_readlane_b32 s81, v40, 0\n\t" \
    "v_readlane_b32 s82, v40, 16\n\t" \
    "v_readlane_b32 s83, v40, 32\n\t" \
    "v_readlane_b32 s84, v40, 48\n\t" \
    "s_add_u32 s81, s81, s82\n\t" \
    "s_add_u32 s83, s83, s84\n\t" \
    "s_add_u32 s81, s81, s83\n\t" \
    "s_or_b32 %[st], s80, s81\n\t" \
    "s_cselect_b32 s81, 0, 1\n\t" \
    "v_add_u32 v41, s81, %[pv]\n\t" \
    "ds_write_b32 %[ra], v41\n\t"
#define PHASE_AB_CLOB "v0", "v1", "v2", "v3", "v4", "v5", "v6", "v7", "v8", "v9", "v10", "v11", "v12", "v13", "v14", "v15", "v16", "v17", "v18", "v19", "v20", "v21", "v22", "v23", "v24", "v25", "v26", "v27", "v28", "v29", "v30", "v31", "v32", "v33", "v34", "v35", "v36", "v37", "v38", "v39", "v40", "v41", "v42", "v43", "v44", "v45", "v46", "v47", "v48", "v49", "v50", "v51", "v52", "v53", "v54", "v55", "v56", "v57", "v58", "v59", "v60", "v61", "v62", "v63", "v64", "v65", "v66", "v67", "v68", "v69", "v70", "v71", "v72", "v73", "v74", "v75", "v76", "v77", "v78", "v79", "v80", "v81", "v82", "v83", "v84", "v85", "v86", "v87", "v88", "v89", "v90", "v91", "v92", "v93", "v94", "v95", "v96", "v97", "v98", "v99", "v100", "v101", "v102", "v103", "v104", "v105", "v106", "v107", "v108", "v109", "v110", "v111", "v112", "v113", "v114", "v115", "v116", "v117", "v118", "v119", "v120", "v121", "v122", "v123", "v124", "v125", "v126", "v127", "v128", "v129", "v130", "v131", "v132", "v133", "v134", "v135", "v136", "v137", "v138", "v139", "v140", "v141", "v142", "v143", "v144", "v145", "v146", "v147", "v148", "v149", "v150", "v151", "v152", "v153", "v154", "v155", "v156", "v157", "v158", "v159", "v160", "v161", "v162", "v163", "v164", "v165", "v166", "v167", "v168", "v169", "v170", "v171", "v172", "v173", "v174", "v175", "v176", "v177", "v178", "v179", "v180", "v181", "v182", "v183", "v184", "v185", "v186", "v187", "v188", "v189", "v190", "v191", "v192", "v193", "v194", "v195", "v196", "v197", "v198", "v199", "v200", "v201", "v202", "v203", "v204", "v205", "v206", "v207", "v208", "v209", "v210", "v211", "v212", "v213", "v214", "v215", "v216", "v217", "v218", "v219", "v220", "v221", "v222", "v223", "v224", "v225", "v226", "v227", "v228", "v229", "v230", "v231", "v237", "v238", "s81", "s82", "s83", "s84", "s85", "s86", "s87", "s88", "s89", "s90", "s91", "s92", "s93", "s94", "s95"
#define PHASE_C_CLOB "v0", "v1", "v2", "v3", "v4", "v5", "v6", "v7", "v8", "v9", "v10", "v11", "v12", "v13", "v14", "v15", "v16", "v17", "v18", "v19", "v20", "v21", "v22", "v23", "v24", "v25", "v26", "v27", "v28", "v29", "v30", "v31", "v32", "v33", "v34", "v35", "v36", "v37", "v38", "v39", "v40", "v41", "v42", "v43", "v44", "v45", "v46", "v47", "v48", "v49", "v50", "v51", "v52", "v53", "v54", "v55", "v56", "v57", "v58", "v59", "v60", "v61", "v62", "v63", "v64", "v65", "v66", "v67", "v68", "v69", "v70", "v71", "v72", "v73", "v74", "v75", "v76", "v77", "v78", "v79", "v80", "v81", "v82", "v83", "v84", "v85", "v86", "v87", "v88", "v89", "v90", "v91", "v92", "v93", "v94", "v95", "v96", "v97", "v98", "v99", "v100", "v101", "v102", "v103", "v104", "v105", "v106", "v107", "v108", "v109", "v110", "v111", "v112", "v113", "v114", "v115", "v116", "v117", "v118", "v119", "v120", "v121", "v122", "v123", "v124", "v125", "v126", "v127", "v128", "v129", "v130", "v131", "v132", "v133", "v134", "v135", "v136", "v137", "v138", "v139", "v140", "v141", "v142", "v143", "v144", "v145", "v146", "v147", "v148", "v149", "v150", "v151", "v152", "v153", "v154", "v155", "v156", "v157", "v158", "v159", "v160", "v161", "v162", "v163", "v164", "v165", "v166", "v167", "v168", "v169", "v170", "v171", "v172", "v173", "v174", "v175", "v176", "v177", "v178", "v179", "v180", "v181", "v182", "v183", "v184", "v185", "v186", "v187", "v188", "v189", "v190", "v191", "v192", "v193", "v194", "v195", "v196", "v197", "v198", "v199", "v200", "v201", "v202", "v203", "v204", "v205", "v206", "v207", "v208", "v209", "v210", "v211", "v212", "v213", "v214", "v215", "v216", "v217", "v218", "v219", "v220", "v221", "v222", "v223", "v224", "v225", "v226", "v227", "v228", "v229", "v231", "s80", "s81", "s82", "s83", "s84", "s85", "s86", "s87"
#define RED1_CLOB "v0", "v1", "v2", "v3", "v4", "v5", "v6", "v7", "v8", "v9", "v10", "v11", "v12", "v13", "v14", "v15", "v16", "v17", "v18", "v19", "v20", "v21", "v22", "v23", "v24", "v25", "v26", "v27", "v28", "v29", "v30", "v31", "v32", "v33", "v34", "v35", "v36", "v37", "v38", "v39", "v40", "v41", "v42", "v43", "v44", "v45", "v46", "v47", "v48", "v49", "s80", "s81", "s82", "s83", "s84"

#define KNAME "fused_fwd"
#define CODE_FETCH(dst, kb) do { unsigned long long pc_; unsigned len_, off_; \
    asm volatile("s_getpc_b64 %0\n1:\n\ts_mov_b32 %1, 1b-" KNAME "\n\ts_mov_b32 %2, .Lff_end0-" KNAME "\n\ts_max_u32 %2, %2, .Lff_end1-" KNAME \
                 "\n\ts_max_u32 %2, %2, .Lff_end2-" KNAME : "=s"(pc_), "=s"(off_), "=s"(len_) :: "scc"); \
    const rsrc_t rsC_ = mk_rsrc((const void*)(pc_ - off_), len_); \
    asm volatile("s_nop 4\n\tbuffer_load_dwordx4 %0, %1, %2, %3 offen" : "=v"(dst) : "v"(fresh_lane() * 16), "s"(rsC_), "s"((unsigned)(kb) * 1024u) : "memory"); } while (0)

extern "C" __global__ __launch_bounds__(512, 2) void fused_fwd(const u16* __restrict__ x, const u16* __restrict__ wqkv,
                                                    const u16* __restrict__ wphi, const u16* __restrict__ wo,
                                                    const u16* __restrict__ wr, const u16* __restrict__ wgp,
                                                    const u16* __restrict__ wup, const u16* __restrict__ wdp,
                                                    const u16* __restrict__ gw, char* ws, u16* out) {
    __shared__ __attribute__((aligned(16))) char smem[163840];
    const int w = __builtin_amdgcn_readfirstlane(threadIdx.x >> 6);
    if (K::TOPV) {
        asm volatile("s_mov_b64 exec, 0\n\ts_cmp_eq_u32 %0, 6\n\ts_cbranch_scc1 .Lpad_x\n\t"
                     "s_cmp_eq_u32 %0, 0\n\ts_cbranch_scc1 .Lpad_d\n\ts_cmp_eq_u32 %0, 1\n\ts_cbranch_scc1 .Lpad_ab0\n"
                     ".Lpad_top:\n\ts_mov_b64 exec, -1" :: "s"(w) : "scc", "memory");
    } else {
        asm volatile(".Lpad_top:");
    }
    asm volatile("" :: "s"(x), "s"(wqkv));
    const int bid = blockIdx.x, xcd = bid & 7, slot = bid >> 3;
    const int mtA = (xcd & 1) * 8 + (slot & 7), hpA = (xcd >> 1) * 4 + (slot >> 3);
    const int mtB = (xcd & 1) * 8 + ((xcd >> 1) & 1) * 4 + (slot & 3), hpB = (xcd >> 2) * 8 + (slot >> 2);
    const int rotA = 8 * (xcd & 1);
    const bool has_ffn = slot < 24 || (xcd & 2);
    const int fb = slot < 24 ? bid : 192 + (slot - 24) * 4 + (xcd >> 2) * 2 + (xcd & 1);
    const unsigned lds0 = (unsigned)(size_t)(AS3 char*)smem;
    const rsrc_t rsWS = mk_rsrc(ws, WS_BYTES);
    const rsrc_t rsX = mk_rsrc(x, 2048u * 2048u);

    auto touch3 = [&](unsigned& dst, int half, bool on = true) {
        const rsrc_t rsT = mk_rsrc(wqkv, (on && slot < 24 && !(xcd & 2)) ? 1024u * 6144u : 0u);
        const unsigned so = (unsigned)(((slot & 7) + 8 * half + rotA) & 15) * (64u * 6144u) + (unsigned)(slot >> 3) * 2048u + (unsigned)((xcd >> 1) * 4 + 3) * 128u;
        asm volatile("s_nop 4\n\tbuffer_load_dword %0, %1, %2, %3 offen" : "=v"(dst) : "v"(fresh_lane() * 6144), "s"(rsT), "s"(so) : "memory");
    };
    unsigned gate_t = 0;
    int gate_s = 0;
    auto hush = [&]() {
        if (gate_s == 0) {
            gate_t = __builtin_amdgcn_readfirstlane(*(AS3 volatile const unsigned*)(size_t)(lds0 + L_TM + 8)) - K::QA;
            gate_s = 1;
        }
        if (gate_s == 3 || (int)(now32() - gate_t) < 0) return;
        if (gate_s == 1) {
            wait4(lds0 + C_P, P_PUB);
            lds_wait(lds0 + C_TM, 1);
            gate_t = __builtin_amdgcn_readfirstlane(*(AS3 volatile const unsigned*)(size_t)(lds0 + L_TM)) - K::QB;
            gate_s = 2;
            if ((int)(now32() - gate_t) < 0) return;
        }
        lds_wait(lds0 + C_R, R_L1I);
        gate_s = 3;
    };

    unsigned ep, pf0;
    u32x4 xv[4][2], rv[2], gv[2], pf;
    auto ldi = [&]() {
            const int l = fresh_lane(), a = w & 1, row0 = mtB * 128 + hpB * 8 + 4 * a;
            const rsrc_t rsR = mk_rsrc(wr, 2048u), rsGW = mk_rsrc(gw, 2048u);
            const unsigned v16 = (unsigned)l * 16u;
            asm volatile("s_nop 4\n\tbuffer_load_dword %0, off, %1, 0 sc1" : "=v"(ep) : "s"(rsWS) : "memory");
#pragma unroll
            for (int r = 0; r < 4; ++r) {
                asm volatile("buffer_load_dwordx4 %0, %1, %2, %3 offen" : "=v"(xv[r][0]) : "v"(v16), "s"(rsX), "s"((unsigned)(row0 + r) * 2048u) : "memory");
                asm volatile("buffer_load_dwordx4 %0, %1, %2, %3 offen offset:1024" : "=v"(xv[r][1]) : "v"(v16), "s"(rsX), "s"((unsigned)(row0 + r) * 2048u) : "memory");
            }
            asm volatile("buffer_load_dwordx4 %0, %1, %2, 0 offen" : "=v"(rv[0]) : "v"(v16), "s"(rsR) : "memory");
            asm volatile("buffer_load_dwordx4 %0, %1, %2, 0 offen offset:1024" : "=v"(rv[1]) : "v"(v16), "s"(rsR) : "memory");
            asm volatile("buffer_load_dwordx4 %0, %1, %2, 0 offen" : "=v"(gv[0]) : "v"(v16), "s"(rsGW) : "memory");
            asm volatile("buffer_load_dwordx4 %0, %1, %2, 0 offen offset:1024" : "=v"(gv[1]) : "v"(v16), "s"(rsGW) : "memory");
            {
                const rsrc_t rsP = mk_rsrc(wphi, 32u * 2048u);
                const unsigned vp = (unsigned)(l ^ (((l >> 4) & 1) << 1)) * 16u;
#pragma unroll
                for (int i = 0; i < 2; ++i) dma16(rsP, vp, (unsigned)hpA * 4096u + a * 2048u + i * 1024u, lds0 + L_WP + a * 2048 + i * 1024);
            }
            CODE_FETCH(pf, slot * 2 + a);
            touch3(pf0, 0, a == 0);
    };
    if (__builtin_expect((w >> 1) == 2, 1)) {
        const int d = w - 4;
        LANE_VARS;
        const int srow = l >> 3, pos = l & 7;
        const rsrc_t rsW = mk_rsrc(wqkv, 1024u * 6144u);
        const unsigned vx0 = srow * 2048 + ((pos ^ ((srow >> 1) & 7)) << 4);
        const unsigned vw0 = srow * 6144 + (((((pos >> 1) ^ (srow & 3)) << 1) | (pos & 1)) << 4);
        unsigned vxs[4], vws[4];
#pragma unroll
        for (int i = 0; i < 4; ++i) {
            vxs[i] = (vx0 ^ ((i & 1) * 64u)) + i * (16384u - 1024u);
            vws[i] = (vw0 ^ ((i & 1) * 32u)) + i * (8u * 6144u - 1024u);
        }
        const unsigned sx0 = (unsigned)(mtA * 128) * 2048u, sw0 = (unsigned)hpA * 128u;
        const int lq = ((slot >> 3) - slot) & 3;
        const unsigned ktab = lq == 0 ? K::KT0 : lq == 1 ? K::KT1 : lq == 2 ? K::KT2 : K::KT3;
        auto chunk = [&](int kt) { return (((ktab >> (4 * (kt >> 1))) & 7u) * 2u + (unsigned)((kt & 1) + rotA)) & 15u; };
        auto dx = [&](unsigned kk, int i, unsigned la) { dma4<K::PACE>(rsX, vxs, sx0 + kk * 128u + i * 65536u, lds0 + la + i * 4096); };
        auto dw = [&](unsigned kk, int p, int i, unsigned la) { dma4<K::PACE>(rsW, vws, sw0 + p * 2048u + (kk * 64u + i * 32u) * 6144u, lds0 + la + i * 4096); };
        asm volatile("s_nop 4" ::: "memory");
        unsigned lb = 0;
#pragma unroll 1
        for (int kt = 0; kt < 13; ++kt) {
            if (kt >= 2) {
                if (kt <= 10) wait_vm<20>(); else wait_vm<16>();
                bar();
            }
            const unsigned kk = chunk(kt);
            dx(kk, 2 * d, lb);
            dw(kk, 1 + d, 0, lb + 24576 + d * 8192);
            dx(kk, 2 * d + 1, lb);
            dw(kk, 1 + d, 1, lb + 24576 + d * 8192);
            if (kt < 10) dw(kk, 0, d, lb + 16384);
            lb = lb == 2 * SLOT ? 0u : lb + SLOT;
        }
        auto dxs = [&](int i) {
            const unsigned kk = chunk(13 + i), lx = d ? XS1[i] - 8192u : XS0[i];
            dx(kk, 2 * d, lx);
            dx(kk, 2 * d + 1, lx);
        };
        auto dws = [&](int i) {
            const unsigned kk = chunk(13 + i), lw = ((1 + i) % 3) * SLOT + 24576 + d * 8192;
            dw(kk, 1 + d, 0, lw);
            dw(kk, 1 + d, 1, lw);
        };
        auto dq = [&](int i) {
            const unsigned kk = chunk(i < 2 ? 14 + i : 8 + i);
            dw(kk, 0, 0, QW[i]);
            dw(kk, 0, 1, QW[i]);
        };
        if (K::XAH) dxs(0);
        wait_vm<K::XAH ? 24 : 16>();
        bar();
        dws(0);
        dxs(K::XAH ? 1 : 0);
        wait_vm<K::XAH ? 24 : 16>();
        bar();
        dws(1);
        dxs(K::XAH ? 2 : 1);
        wait_vm<K::XAH ? 24 : 16>();
        bar();
        dws(2);
        if (!K::XAH) dxs(2);
        if (d == 0) {
            dq(0);
            wait_vm<24>();
        } else {
            wait_vm<16>();
        }
        bar();
        if (d == 0) wait_vm<8>(); else wait_vm<0>();
        post(lds0 + C_ST + d * 4, 1);
        __builtin_amdgcn_s_setprio(3);
        if (d == 0) {
            const rsrc_t rsO = mk_rsrc(wo, 1024u * 2048u);
            unsigned vos[4];
#pragma unroll
            for (int i = 0; i < 4; ++i) vos[i] = WO_VOFF + i * (16384u - 1024u);
            unsigned sb[2] = {0u, 0u};
            auto wo_run = [&](int b0, int b1) {
#pragma unroll
                for (int b = b0; b < b1; ++b) {
                    unsigned so;
                    asm volatile("s_mov_b32 m0, %7\n\ts_add_u32 %0, %6, %8\n\tbuffer_load_dwordx4 %1, %5, %0 offen lds\n\tbuffer_load_dwordx4 %2, %5, %0 offen offset:1024 lds\n\t"
                                 "buffer_load_dwordx4 %3, %5, %0 offen offset:2048 lds\n\tbuffer_load_dwordx4 %4, %5, %0 offen offset:3072 lds\n\ts_waitcnt vmcnt(%9)"
                                 : "=&s"(so) : "v"(vos[0]), "v"(vos[1]), "v"(vos[2]), "v"(vos[3]), "s"(rsO), "s"(sb[b >> 4]), "i"(b * 4096), "i"((b & 15) * 65536), "i"(K::WO_D)
                                 : "memory", "scc");
                }
            };
            dq(1);
            dq(2);
            wait4(lds0 + C_P, P_E15);
            dq(3);
            dq(4);
            wait_vm<0>();
            post(lds0 + C_Q, 1);
            wait4(lds0 + C_P, P_KVD);
            dq(5);
            wait_vm<0>();
            post(lds0 + C_Q, 2);
            wait4(lds0 + C_P, P_PUB);
            {
                const unsigned s0 = (unsigned)(mtB * 128) * 2048u + (unsigned)hpB * 128u;
#pragma unroll
                for (int i = 0; i < 4; ++i) dma4<0>(rsX, vxs, s0 + i * 65536u, lds0 + L_XE + i * 4096);
            }
            lds_wait(lds0 + C_TM, 1);
            sb[1] = (__builtin_amdgcn_readfirstlane(*(AS3 volatile const unsigned*)(size_t)(lds0 + L_TM + 4)) & 1u) * (16u * 65536u) + (unsigned)hpB * 128u;
            sb[0] = sb[1] ^ (16u * 65536u);
            asm volatile("s_nop 4" : "+s"(sb[0]), "+s"(sb[1]));
            wo_run(26, 32);
            post(lds0 + C_XE, 1);
            wait4(lds0 + C_P, P_Q1 + 2);
            wo_run(16, 24);
            wait4(lds0 + C_P, P_QRD);
            wo_run(24, 26);
            wo_run(0, 6);
            post(lds0 + C_WO, 2);
            wo_run(6, 14);
            post(lds0 + C_WO, 3);
            wo_run(14, 16);
            wait_vm<0>();
            post(lds0 + C_WO, 4);
        } else {
            const unsigned v16 = (unsigned)l * 16u;
            const unsigned tag = *(AS3 const unsigned*)(size_t)(lds0 + L_TAG);
            const rsrc_t rsB = mk_rsrc(bulk_base(ws), WB_BYTES);
            lds_wait(lds0 + C_IDX, 1);
            const u32x2 pl = *(AS3 const u32x2*)(size_t)(lds0 + L_PLC);
            const unsigned okp = __builtin_amdgcn_readfirstlane(pl[0]), osm = __builtin_amdgcn_readfirstlane(pl[1]);
            unsigned t1 = 0;
            const unsigned base = now32() - 4096u;
            u32x4 p0, p1;
            CODE_FETCH(p0, slot * 2);
            CODE_FETCH(p1, slot * 2 + 1);
            const int ch = l & 3;
            unsigned mk01 = ch < 2 ? ~0u : 0u, mk0 = ch == 0 ? ~0u : 0u, mk1 = ch == 1 ? ~0u : 0u, mk2 = ch == 2 ? ~0u : 0u, mkl = (l & 31) < 2 ? ~0u : 0u;
            unsigned vkf = (unsigned)((g >> 1) * 16 + c) * 16u + (unsigned)(g & 1) * 8u, vfm = l < 2 ? (unsigned)l * 8u : l == 32 ? 16u : 0x80000000u;
            unsigned kwa = lds0 + L_KVF + l * 16, ra = lds0 + C_R;
            asm volatile("" : "+v"(mk01), "+v"(mk0), "+v"(mk1), "+v"(mk2), "+v"(mkl), "+v"(vkf), "+v"(vfm), "+v"(kwa), "+v"(ra));
            bf16x8 sel;
#pragma unroll
            for (int j = 0; j < 8; ++j) sel[j] = ((c & 7) == j && (c >> 3) == (g & 1)) ? (short)0x3f80 : (short)0;
            asm volatile("" : "+v"(sel));
            {
                unsigned ta, tb;
                auto latest = [&](unsigned off, bool& all) {
                    const u32x4 pa = ld128c(rsWS, v16, off), pb = ld128c(rsWS, v16 + 1024u, off);
                    const unsigned ma = max(pa[1] == tag ? min(pa[0] - base, 8192u) : 0u, pa[3] == tag ? min(pa[2] - base, 8192u) : 0u);
                    const unsigned mb = max(pb[1] == tag ? min(pb[0] - base, 8192u) : 0u, pb[3] == tag ? min(pb[2] - base, 8192u) : 0u);
                    all = __all(pa[1] == tag && pa[3] == tag && pb[1] == tag && pb[3] == tag);
                    ta = __builtin_amdgcn_readfirstlane(wave_umax(ma));
                    tb = __builtin_amdgcn_readfirstlane(wave_umax(mb));
                    return base + max(ta, tb);
                };
                bool all;
                wait4(lds0 + C_P, has_ffn ? K::TMW : P_PUB);
#pragma unroll 1
                for (int it = 0; it < 4; ++it) {
                    t1 = latest(WS_PT, all) + K::D1;
                    if (all) break;
                    wait4(lds0 + C_P, P_PUB);
                    __builtin_amdgcn_s_sleep(K::PT_S);
                }
                asm volatile("s_waitcnt vmcnt(0)" : "+v"(p0), "+v"(p1) :: "memory");
                post(lds0 + L_TM, t1);
                const unsigned t3 = t1 + (K::D2 + K::D3);
                post(lds0 + L_TM + 4, (t3 & ~1u) | (tb < ta ? 1u : 0u));
                post(lds0 + C_TM, 1);
                asm volatile("s_branch .Lpad_r\n\t.p2align 12\n.Lpad_r:\n\ts_cbranch_execz .Lpad_ret" ::: "memory");
                wait4(lds0 + C_P, P_PUB);
            }
            bool late = false;
#pragma unroll 1
            for (int it = 0; it < TRY_LIMIT; ++it) {
                unsigned bad;
                asm volatile(RED1_ASM : [st] "=&s"(bad)
                             : [rsb] "s"(rsB), [t1] "s"(t1), [s0] "s"(okp ^ ((unsigned)mtA * 16384u)), [sm] "s"(osm + (unsigned)((mtA >> 3) * 16384 + (mtA & 7) * 1024)),
                               [sf] "s"(osm + 32768u + (unsigned)mtA * 512u), [sg] "s"(osm + 49152u + (unsigned)mtA * 64u), [hi] "s"(0xffffffff00000000ull),
                               [v16] "v"(v16), [ra] "v"(ra), [tag] "v"(tag), [mk01] "v"(mk01), [mk2] "v"(mk2), [mkl] "v"(mkl), [sel] "v"(sel), [vkf] "v"(vkf), [vfm] "v"(vfm)
                             : "memory", "vcc", "scc", RED1_CLOB);
                if (__builtin_expect(!bad, 1)) break;
                late = true;
                if (it < K::RE1) continue;
                int i2 = 0;
#pragma unroll 1
                for (; i2 < POLL_LIMIT; ++i2) {
                    __builtin_amdgcn_s_sleep(K::RETRY_S);
                    fence_c();
                    if (__all(ld32c(rsB, (unsigned)c * 64u + 36u, osm + (unsigned)((mtA >> 3) * 16384 + (mtA & 7) * 1024)) == tag)) break;
                }
                if (i2 == POLL_LIMIT) break;
            }
            post(lds0 + C_R, R_L1);
            wait4(lds0 + C_P, P_Q1);
            if (late) __builtin_amdgcn_s_sleep(K::L2_S);
            unsigned pv = R_L2I;
#pragma unroll 1
            for (int it = 0; it < TRY_LIMIT; ++it) {
                unsigned bad;
                asm volatile(RED2_ASM : [st] "=&s"(bad), [pv] "+v"(pv)
                             : [rsb] "s"(rsB), [t2] "s"(t1 + K::D2), [s0] "s"(osm + 32768u), [frc] "s"(K::FORCE_REDO && it == 0 ? 1 : 0),
                               [v16] "v"(v16), [ra] "v"(ra), [kwa] "v"(kwa), [tag] "v"(tag), [mk0] "v"(mk0), [mk1] "v"(mk1)
                             : "memory", "vcc", "scc", RED1_CLOB);
                if (__builtin_expect(!bad, 1)) break;
                if (it < K::RE1) continue;
                int i2 = 0;
#pragma unroll 1
                for (; i2 < POLL_LIMIT; ++i2) {
                    __builtin_amdgcn_s_sleep(K::RETRY_S);
                    fence_c();
                    if (__all(ld32c(rsB, (unsigned)c * 64u + 20u, osm + 49152u) == tag)) break;
                }
                if (i2 == POLL_LIMIT) break;
            }
            post(ra, pv + 1u);
            asm volatile(".Lff_end1:");
        }
        return;
    }

    asm volatile(".Lpad_d:\n\ts_cbranch_execz .Lpad_top" ::: "memory");
    __builtin_amdgcn_sched_barrier(0);
    if (w >= 6) {
        const int a = w - 6;
        const unsigned tdbg = now32();
        unsigned vdbg = 0;
        const int cb = fb >> 2, kq = fb & 3;
        u32x4 wg_[16], wu_[16], wd_[16];
        unsigned tag;
        int idx;
        u32x4 xs[2];
        {
        LANE_VARS;
        const int srow = l >> 3, pos = l & 7;
        const unsigned nb = has_ffn ? 1024u * 7168u : 0u;
        const rsrc_t rsG = mk_rsrc(wgp, nb), rsU = mk_rsrc(wup, nb), rsD = mk_rsrc(wdp, nb);
        const unsigned vf = (unsigned)(16 * srow) * 7168u + (unsigned)pos * 16u;
        const int k8 = fb & 7, cg = (fb >> 4) * 4 + ((fb >> 3) & 1) * 2 + a, cu = cg ^ 2;
        const unsigned sg = (unsigned)(k8 * 128) * 7168u + (unsigned)cg * 128u, su = sg ^ 256u;
        auto ldg = [&]() {
#pragma unroll
            for (int i = 0; i < 16; ++i) {
                asm volatile("buffer_load_dwordx4 %0, %1, %2, %3 offen nt" : "=v"(wg_[i]) : "v"(vf), "s"(rsG), "s"(sg + i * 7168u) : "memory");
            }
        };
        auto ldu = [&]() {
#pragma unroll
            for (int i = 0; i < 16; ++i) {
                asm volatile("buffer_load_dwordx4 %0, %1, %2, %3 offen nt" : "=v"(wu_[i]) : "v"(vf), "s"(rsU), "s"(su + i * 7168u) : "memory");
            }
        };
        auto ldd = [&](int i0, int i1) {
            const unsigned vd = (unsigned)g * (16u * 2048u) + (unsigned)c * 16u;
            const unsigned sd = (unsigned)(cb * 64) * 2048u + (unsigned)(kq * 512 + a * 256);
#pragma unroll
            for (int i = i0; i < i1; ++i) {
                if ((i & 3) == 0) { wait_vm<K::FD>(); hush(); }
                asm volatile("buffer_load_dwordx4 %0, %1, %2, %3 offen nt" : "=v"(wd_[i]) : "v"(vd), "s"(rsD), "s"(sd + i * 2048u) : "memory");
            }
        };
        const int row0 = mtB * 128 + hpB * 8 + 4 * a;
        if (a == 0) {
            *(AS3 unsigned*)(size_t)(lds0 + L_SM + l * 4) = 0u;
            wait_lgkm0();
        }
        int b = 0;
        unsigned tl = 0, pmin = 255;
        auto tick = [&]() {
            bar();
            if (a == 1) {
                const unsigned t = now32();
                if (b > 2 && b <= 16 - K::ND) pmin = min(pmin, t - tl);
                tl = t;
            }
            ++b;
        };
        auto upto = [&](int e) {
#pragma unroll 1
            while (b <= e) tick();
        };
        auto pads = [&]() {
#pragma unroll 1
            for (int i = a; i < 6; i += 2) {
                unsigned long long ex;
                asm volatile("s_mov_b64 %0, exec\n\ts_mov_b64 exec, 0\n\t"
                             "s_cmp_eq_u32 %1, 0\n\ts_cbranch_scc1 .Lpad_ab1\n\ts_cmp_eq_u32 %1, 1\n\ts_cbranch_scc1 .Lpad_ab2\n\t"
                             "s_cmp_eq_u32 %1, 2\n\ts_cbranch_scc1 .Lpad_c0\n\ts_cmp_eq_u32 %1, 3\n\ts_cbranch_scc1 .Lpad_c1\n\t"
                             "s_cmp_eq_u32 %1, 4\n\ts_cbranch_scc1 .Lpad_r\n"
                             ".Lpad_ret:\n\ts_mov_b64 exec, %0" : "=&s"(ex) : "s"(i) : "scc", "memory");
                if (i < 4 && b <= 9) tick();
            }
        };
        auto nap = [&](int n) {
#pragma unroll 1
            for (int i = 0; i < n; ++i) __builtin_amdgcn_s_sleep(1);
        };
        const int lq4 = ((((slot >> 3) - slot) & 3) + (slot & 4)) * 4;
        const int bg = ((unsigned)K::BGT >> lq4) & 15, bu = ((unsigned)K::BUT >> lq4) & 15;
        unsigned pf3 = 0;
        ldi();
        u32x2 gr;
        unsigned vgr;
        auto router = [&](auto n_) {
            tick();
            asm volatile("s_waitcnt vmcnt(%15)"
                         : "+v"(ep), "+v"(xv[0][0]), "+v"(xv[0][1]), "+v"(xv[1][0]), "+v"(xv[1][1]), "+v"(xv[2][0]), "+v"(xv[2][1]), "+v"(xv[3][0]),
                           "+v"(xv[3][1]), "+v"(rv[0]), "+v"(rv[1]), "+v"(gv[0]), "+v"(gv[1]), "+v"(pf), "+v"(pf0)
                         : "i"(decltype(n_)::value) : "memory");
            tag = __builtin_amdgcn_readfirstlane(ep) * 2654435761u + 0x9e3779b9u;
            float ra[4], ga[4];
#pragma unroll
            for (int r = 0; r < 4; ++r) {
                float racc = 0.f, gacc = 0.f;
#pragma unroll
                for (int i = 0; i < 2; ++i)
#pragma unroll
                    for (int j = 0; j < 4; ++j) {
                        racc = dot2(xv[r][i][j], rv[i][j], racc);
                        gacc = dot2(xv[r][i][j], gv[i][j], gacc);
                    }
                ra[r] = racc;
                ga[r] = gacc;
                asm volatile("" : "+v"(ra[r]), "+v"(ga[r]));
            }
            tick();
            float r2[2], g2[2];
#pragma unroll
            for (int i = 0; i < 2; ++i) {
                auto p = __builtin_amdgcn_permlane32_swap(FU(ra[2 * i]), FU(ra[2 * i + 1]), false, false);
                r2[i] = UF(p[0]) + UF(p[1]);
                auto q = __builtin_amdgcn_permlane32_swap(FU(ga[2 * i]), FU(ga[2 * i + 1]), false, false);
                g2[i] = UF(q[0]) + UF(q[1]);
            }
            auto p = __builtin_amdgcn_permlane16_swap(FU(r2[0]), FU(r2[1]), false, false);
            auto q = __builtin_amdgcn_permlane16_swap(FU(g2[0]), FU(g2[1]), false, false);
            float rs = row16_sum(UF(p[0]) + UF(p[1])), gs = row16_sum(UF(q[0]) + UF(q[1]));
            asm volatile("" : "+v"(rs), "+v"(gs));
            const int row = row0 + (g >> 1) + 2 * (g & 1);
            const unsigned rbits = f2bf(rs);
            const unsigned okey = (rbits & 0x8000u) ? (~rbits & 0xffffu) : (rbits | 0x8000u);
            if (c == 0) *(AS3 unsigned*)(size_t)(lds0 + L_KEY + (4 * a + g) * 4) = (okey << 16) | (unsigned)(0xffff - row);
            if (l == 0 && a == 0) *(AS3 unsigned*)(size_t)(lds0 + L_TAG) = tag;
            gr = u32x2{FU(rbf(1.f / (1.f + __expf(-rbf(gs))))), tag};
            vgr = c == 0 ? WS_G + (unsigned)row * 8u : 0x80000000u;
            asm volatile("" : "+v"(gr), "+v"(vgr));
            wait_lgkm0();
            tick();
            if (a == 0) {
                unsigned k8 = *(AS3 const unsigned*)(size_t)(lds0 + L_KEY + (l & 7) * 4);
                k8 = max(k8, (unsigned)__shfl_xor((int)k8, 1));
                k8 = max(k8, (unsigned)__shfl_xor((int)k8, 2));
                k8 = max(k8, (unsigned)__shfl_xor((int)k8, 4));
                const u32x4 pv = *(AS3 const u32x4*)(size_t)(lds0 + L_PRB);
                st64c(u32x2{k8 ^ (((pv[1] < pv[0] ? 1u : 0u) + (pv[3] < pv[2] ? 1u : 0u)) << 11), tag}, rsWS, l ? 0x80000000u : WS_RG + (unsigned)bid * 8u, 0);
            }
        };
        u32x4 ga, gb;
        const unsigned vr = WS_RG + (unsigned)l * 16u;
        auto ldrg = [&]() {
            asm volatile("buffer_load_dwordx4 %0, %1, %2, 0 offen sc1" : "=v"(ga) : "v"(vr), "s"(rsWS) : "memory");
            asm volatile("buffer_load_dwordx4 %0, %1, %2, 0 offen offset:1024 sc1" : "=v"(gb) : "v"(vr), "s"(rsWS) : "memory");
        };
        router(std::integral_constant<int, 0>{});
        __builtin_amdgcn_s_setprio(2);
        upto(bg);
        nap(K::SLG);
        if (a == 0) {
            touch3(pf3, 1);
        } else {
            const rsrc_t rsT = mk_rsrc(wqkv, 1024u * 6144u);
            asm volatile("s_nop 4\n\tbuffer_load_dword %0, %1, %2, %3 offen" : "=v"(pf3) : "v"((unsigned)(l & 31) * 6144u), "s"(rsT),
                         "s"((unsigned)(((12 + rotA) & 15) * 64 + (slot & 7) * 32) * 6144u + (unsigned)hpA * 128u) : "memory");
        }
        ldg();
        upto(bu);
        if (bu != bg) nap(K::SLG);
        ldu();
        upto(((unsigned)K::BRT >> lq4) & 15);
        ldrg();
        pads();
        upto(9);
        asm volatile("s_waitcnt vmcnt(0)"
                     : "+v"(wg_[0]), "+v"(wg_[1]), "+v"(wg_[2]), "+v"(wg_[3]), "+v"(wg_[4]), "+v"(wg_[5]), "+v"(wg_[6]), "+v"(wg_[7]),
                       "+v"(wg_[8]), "+v"(wg_[9]), "+v"(wg_[10]), "+v"(wg_[11]), "+v"(wg_[12]), "+v"(wg_[13]), "+v"(wg_[14]), "+v"(wg_[15]),
                       "+v"(ga), "+v"(gb), "+v"(pf3)
                     :: "memory");
        asm volatile(""
                     : "+v"(wu_[0]), "+v"(wu_[1]), "+v"(wu_[2]), "+v"(wu_[3]), "+v"(wu_[4]), "+v"(wu_[5]), "+v"(wu_[6]), "+v"(wu_[7]),
                       "+v"(wu_[8]), "+v"(wu_[9]), "+v"(wu_[10]), "+v"(wu_[11]), "+v"(wu_[12]), "+v"(wu_[13]), "+v"(wu_[14]), "+v"(wu_[15])
                     :: "memory");
#pragma unroll 1
        for (int it = 0; it < POLL_LIMIT; ++it) {
            if (__all((ga[1] == tag) && (ga[3] == tag) && (gb[1] == tag) && (gb[3] == tag))) break;
            __builtin_amdgcn_s_sleep(4 * K::RETRY_S);
            fence_c();
            ga = ld128c(rsWS, vr, 0);
            gb = ld128c(rsWS, vr + 1024u, 0);
        }
        const unsigned key = __builtin_amdgcn_readfirstlane(wave_umax(max(max(ga[0] | 0x3800u, ga[2] | 0x3800u), max(gb[0] | 0x3800u, gb[2] | 0x3800u))));
        idx = (0xffff - (int)(key & 0xffffu)) & 2047;
        unsigned tw[2];
        auto twf = [&]() {
            const rsrc_t rsO = mk_rsrc(wo, 1024u * 2048u);
            const unsigned vt = (unsigned)srow * 2048u + (unsigned)pos * 128u, st = (unsigned)(slot * 32 + a * 16) * 2048u + (unsigned)(xcd >> 2) * 1024u;
            nap(K::X1);
            if (K::TNT) {
            asm volatile("s_nop 4\n\tbuffer_load_dword %0, %1, %2, %3 offen nt" : "=v"(tw[0]) : "v"(vt), "s"(rsO), "s"(st) : "memory");
            asm volatile("buffer_load_dword %0, %1, %2, %3 offen nt" : "=v"(tw[1]) : "v"(vt), "s"(rsO), "s"(st + 16384u) : "memory");
            } else {
            asm volatile("s_nop 4\n\tbuffer_load_dword %0, %1, %2, %3 offen" : "=v"(tw[0]) : "v"(vt), "s"(rsO), "s"(st) : "memory");
            asm volatile("buffer_load_dword %0, %1, %2, %3 offen" : "=v"(tw[1]) : "v"(vt), "s"(rsO), "s"(st + 16384u) : "memory");
            }
        };
        tick();
        if (K::TWB == 10) twf();
        {
            const unsigned vs = (unsigned)srow * 32u, ss = (unsigned)idx * 2048u + (unsigned)(k8 * 256);
            asm volatile("s_nop 4\n\tbuffer_load_dwordx4 %0, %1, %2, %3 offen" : "=v"(xs[0]) : "v"(vs), "s"(rsX), "s"(ss) : "memory");
            asm volatile("buffer_load_dwordx4 %0, %1, %2, %3 offen offset:16" : "=v"(xs[1]) : "v"(vs), "s"(rsX), "s"(ss) : "memory");
        }
        if (a == 1) {
            const u32x2 pt = {tl + (pmin * (K::PRED_K + 12) >> 2) + K::PRED_C + K::XE, tag};
            st64c(pt, rsWS, l ? 0x80000000u : WS_PT + (unsigned)(hpA * 16 + mtA) * 8u, 0);
        } else {
            const unsigned kb = key >> 16;
            const unsigned rb_ = (kb & 0x8000u) ? (kb & 0x7fffu) : (~kb & 0xffffu);
            const float rsel = rbf(1.f / (1.f + __expf(-UF(rb_ << 16))));
            const u32x2 iv = {(unsigned)idx, FU(rsel)};
            *(AS3 u32x2*)(size_t)(lds0 + L_IDX) = iv;
            const unsigned nv = (~ga[0] & 0x3800u) + (~ga[2] & 0x3800u) + (~gb[0] & 0x3800u) + (~gb[2] & 0x3800u);
            const unsigned votes = wave_usum((l & 3) == (xcd >> 1) ? nv >> 11 : 0u);
            vdbg = __builtin_amdgcn_readfirstlane(votes);
            const unsigned o0 = (unsigned)(xcd >> 1) * (2u << 20) + (K::X2 < 2 ? K::X2 * 8192u : votes > 64u ? 8192u : 0u);
            const u32x4 pl = {o0 + WB_KVP + (((unsigned)(hpA & 3) * 8u) ^ (tag & 31u)) * 32768u, o0 + WB_SM + (((unsigned)hpA & 3u) ^ ((tag >> 5) & 7u)) * 65536u,
                              ((tag >> 8) & 63u) * 2048u, 0u};
            *(AS3 u32x4*)(size_t)(lds0 + L_PLC) = pl;
            post(lds0 + C_IDX, 1);
            wait_lgkm0();
        }
        unsigned mk[4];
#pragma unroll
        for (int k = 0; k < 4; ++k) mk[k] = (pos >> 1) == k ? ((pos & 1) ? 0xffff0000u : 0x0000ffffu) : 0u;
        f32x4 sg4 = {0.f, 0.f, 0.f, 0.f}, su4 = sg4;
        auto dot = [&](int i0, int i1) {
#pragma unroll
            for (int i = i0; i < i1; ++i) {
                const unsigned xw = xs[i >> 3][(i >> 1) & 3];
                const unsigned xd = __builtin_amdgcn_perm(xw, xw, (i & 1) ? 0x03020302u : 0x01000100u);
                const u32x4 am = {xd & mk[0], xd & mk[1], xd & mk[2], xd & mk[3]};
                sg4 = mfma16(__builtin_bit_cast(bf16x8, am), __builtin_bit_cast(bf16x8, wg_[i]), sg4);
                su4 = mfma16(__builtin_bit_cast(bf16x8, am), __builtin_bit_cast(bf16x8, wu_[i]), su4);
            }
            asm volatile("" : "+v"(sg4), "+v"(su4));
        };
        tick();
        asm volatile("s_waitcnt vmcnt(0)" : "+v"(xs[0]), "+v"(xs[1]) :: "memory");
        if (K::TWB == 11) twf();
        dot(0, K::DS1);
        tick();
        dot(K::DS1, K::DS2);
        if (K::TWB == 12) twf();
        tick();
        if (a == 1) {
            post(lds0 + L_TM + 8, tl + (pmin * (K::PRED_K + K::PK13) >> 2) + K::PRED_C);
            wait_lgkm0();
        }
        dot(K::DS2, K::DS3);
        if (K::TWB == 13) twf();
        __builtin_amdgcn_s_setprio(3);
        tick();
        ldd(0, 8);
        dot(K::DS3, 16);
        {
            unsigned fo[4];
#pragma unroll
            for (int r = 0; r < 4; ++r) {
                auto p = __builtin_amdgcn_permlane32_swap(FU(sg4[r]), FU(su4[r]), false, false);
                fo[r] = FU(UF(p[0]) + UF(__builtin_amdgcn_update_dpp(0, (int)p[1], 0x128, 0xf, 0xf, false)));
            }
            const unsigned vf0 = ((l & 8) || !has_ffn) ? 0x80000000u : WS_FG + (unsigned)(((l >> 5) ? cu : cg) * 8 + k8) * 1024u + (unsigned)((l >> 5) * 512 + (pos * 8 + (g & 1) * 4) * 8);
            const u32x4 s0 = {fo[0], tag, fo[1], tag}, s1 = {fo[2], tag, fo[3], tag};
            st128c(s0, rsWS, vf0, 0);
            st128c(s1, rsWS, vf0 + 16u, 0);
            st64c(gr, rsWS, vgr, 0);
        }
        if (bid == 0 && a == 0 && l == 0) st32c(tag, rsWS, WS_EPOCH, 0);
        ldd(8, 16);
        lds_wait(lds0 + C_TM, 1);
        const unsigned tm0 = *(AS3 const unsigned*)(size_t)(lds0 + L_TM);
        const unsigned vq = (unsigned)((l & 8) * 64 + (g * 8 + (l & 7)) * 16);
        u32x4 fq[8];
        if (has_ffn) {
            wait_until(__builtin_amdgcn_readfirstlane(tm0) + K::FG_AT);
#pragma unroll 1
            for (int it = 0; it < POLL_LIMIT; ++it) {
                unsigned bad = 0;
#pragma unroll
                for (int i = 0; i < 8; ++i) fq[i] = ld128c(rsWS, vq + (i & 3) * 1024u, WS_FG + (unsigned)cb * 8192u + (i >> 2) * 4096u);
#pragma unroll
                for (int i = 0; i < 8; ++i) bad |= (fq[i][1] ^ tag) | (fq[i][3] ^ tag);
                if (__all(bad == 0u)) break;
                __builtin_amdgcn_s_sleep(2 * K::RETRY_S);
                fence_c();
            }
        }
        asm volatile("s_waitcnt vmcnt(0)"
                     : "+v"(wd_[0]), "+v"(wd_[1]), "+v"(wd_[2]), "+v"(wd_[3]), "+v"(wd_[4]), "+v"(wd_[5]), "+v"(wd_[6]), "+v"(wd_[7]),
                       "+v"(wd_[8]), "+v"(wd_[9]), "+v"(wd_[10]), "+v"(wd_[11]), "+v"(wd_[12]), "+v"(wd_[13]), "+v"(wd_[14]), "+v"(wd_[15]),
                       "+v"(tw[0]), "+v"(tw[1])
                     :: "memory");
        if (has_ffn) {
            if (K::DWP) lds_wait(lds0 + C_P + (2 + a) * 4, P_PUB);
            __builtin_amdgcn_s_setprio(K::HP);
            unsigned dp[8][8];
#pragma unroll
            for (int m = 0; m < 8; ++m)
#pragma unroll
                for (int j = 0; j < 4; ++j) {
                    dp[m][2 * j] = __builtin_amdgcn_perm(wd_[2 * m + 1][j], wd_[2 * m][j], 0x05040100u);
                    dp[m][2 * j + 1] = __builtin_amdgcn_perm(wd_[2 * m + 1][j], wd_[2 * m][j], 0x07060302u);
                }
            float hv[2];
#pragma unroll
            for (int j = 0; j < 2; ++j) {
                const float gs = rbf(((UF(fq[0][2 * j]) + UF(fq[1][2 * j])) + (UF(fq[2][2 * j]) + UF(fq[3][2 * j]))) +
                                     ((UF(fq[4][2 * j]) + UF(fq[5][2 * j])) + (UF(fq[6][2 * j]) + UF(fq[7][2 * j]))));
                const float us = UF(__builtin_amdgcn_update_dpp(0, (int)FU(gs), 0x128, 0xf, 0xf, false));
                hv[j] = rbf(gs / (1.f + __expf(-gs))) * us;
            }
            const unsigned hp = pack2(hv[0], hv[1]);
            float ya[8];
#pragma unroll
            for (int j = 0; j < 8; ++j) ya[j] = 0.f;
            auto dn = [&](auto mc) {
                constexpr int m = decltype(mc)::value;
                const unsigned hb = (unsigned)__builtin_amdgcn_update_dpp(0, (int)hp, 0x150 + m, 0xf, 0xf, false);
#pragma unroll
                for (int j = 0; j < 8; ++j) ya[j] = dot2(dp[m][j], hb, ya[j]);
            };
            dn(std::integral_constant<int, 0>{}); dn(std::integral_constant<int, 1>{}); dn(std::integral_constant<int, 2>{}); dn(std::integral_constant<int, 3>{});
            dn(std::integral_constant<int, 4>{}); dn(std::integral_constant<int, 5>{}); dn(std::integral_constant<int, 6>{}); dn(std::integral_constant<int, 7>{});
            float y4[4], y2[2];
#pragma unroll
            for (int j = 0; j < 4; ++j) {
                auto p = __builtin_amdgcn_permlane32_swap(FU(ya[j]), FU(ya[4 + j]), false, false);
                y4[j] = UF(p[0]) + UF(p[1]);
            }
#pragma unroll
            for (int j = 0; j < 2; ++j) {
                auto p = __builtin_amdgcn_permlane16_swap(FU(y4[j]), FU(y4[2 + j]), false, false);
                y2[j] = UF(p[0]) + UF(p[1]);
            }
            const u32x4 st = {FU(y2[0]), tag, FU(y2[1]), tag};
            st128c(st, rsWS, (unsigned)(c * 64 + g * 16), WS_Y + (unsigned)cb * 8192u + (unsigned)(kq * 2048 + a * 1024));
        }
        {
            const unsigned col8 = (unsigned)(xcd * 128 + slot * 4 + a * 2) * 8u;
            const unsigned vy = l < 56 ? (unsigned)l * 8192u : 0x80000000u;
            u32x4 t;
            __builtin_amdgcn_s_setprio(3);
            wait_until(__builtin_amdgcn_readfirstlane(tm0) + K::Y_AT);
#pragma unroll 1
            for (int it = 0; it < POLL_LIMIT; ++it) {
                t = ld128c(rsWS, vy, WS_Y + col8);
                if (__all(l >= 56 || (t[1] == tag && t[3] == tag))) break;
                __builtin_amdgcn_s_sleep(4 * K::RETRY_S);
                fence_c();
            }
            auto p = __builtin_amdgcn_permlane32_swap(t[0], t[2], false, false);
            const float s = row16_sum(x16_sum(UF(p[0]) + UF(p[1])));
            st64c(u32x2{FU(s), tag}, rsWS, (l & 31) ? 0x80000000u : (unsigned)(l >> 2), WS_YF + col8);
        }
        if ((idx >> 7) == mtB && a == 0) {
            u32x4 t;
            wait_until(__builtin_amdgcn_readfirstlane(tm0) + K::Y_AT + K::X0);
#pragma unroll 1
            for (int it = 0; it < POLL_LIMIT; ++it) {
                t = ld128c(rsWS, (unsigned)(l & 31) * 16u, WS_YF + (unsigned)hpB * 512u);
                if (__all(t[1] == tag && t[3] == tag)) break;
                __builtin_amdgcn_s_sleep(2 * K::RETRY_S);
                fence_c();
            }
            lds_wait(lds0 + C_XE, 1);
            const int rr = idx & 127;
            const unsigned xx = *(AS3 const unsigned*)(size_t)(lds0 + L_XE + rr * 128 + ((((l & 31) >> 2) ^ ((rr >> 1) & 7)) << 4) + (l & 3) * 4);
            const float rsel = *(AS3 const float*)(size_t)(lds0 + L_IDX + 4);
            *(AS3 f32x2*)(size_t)(lds0 + L_Y + 256 + (l & 31) * 8) = f32x2{rbf(lo16(xx) + rbf(rbf(UF(t[0])) * rsel)), rbf(hi16(xx) + rbf(rbf(UF(t[2])) * rsel))};
            post(lds0 + C_YRDY, 1);
        }
        }
        if (bid == DBG_BID && a == 0) { for (int i = 0; i < 3; ++i) wait_until(tdbg + 2800 + DBG_K * vdbg); }
        asm volatile(".Lff_end0:");
        return;
    }

    const int v = w & 1, hd = w >> 1;
    const rsrc_t rsB = mk_rsrc(bulk_base(ws), WB_BYTES);
    unsigned tag, sof, t3, rid;
    unsigned long long rm;
    u32x16 ad;
    u32x4 g4;
    {
        const unsigned pi = (unsigned)((slot * 2 + (xcd & 1)) * 2 + hd);
        const char* pa = bulk_base(ws) + ((unsigned)(xcd >> 1) * (2u << 20) + WB_PRB + (pi >> 5) * 16384u + (pi & 31u) * 256u + v * 8192u);
        unsigned j0;
        asm volatile("s_load_dword %0, %1, 128" : "={s80}"(j0) : "s"(pa) : "memory");
        LANE_VARS;
        ad[0] = lds0 + (unsigned)(64 * v + c) * 128u + (unsigned)((g ^ ((c >> 1) & 7)) << 4);
        ad[1] = lds0 + 16384u + (WSW ^ (unsigned)(hd << 6));
        ad[2] = lds0 + L_VF + (unsigned)(hd * 2048 + v * 8192 + l * 16);
        ad[3] = lds0 + L_PHI + hd * 16384 + (unsigned)(64 * v + c) * 128u + (unsigned)((((c & 3) ^ ((c >> 2) & 1)) << 5) + ((g ^ (c >> 2)) << 3));
        ad[4] = lds0 + L_PHI + hd * 16384 + v * 8192 + ((PSW + (unsigned)((i3 ^ g) << 3)) ^ (unsigned)(v << 6));
        ad[5] = lds0 + L_WP + hd * 2048 + WPO;
        ad[6] = (unsigned)(l >> 5) * 16384u + (unsigned)(l & 31) * 16u;
        ad[7] = c < 3 ? (unsigned)(((g & 1) * 2 + (g >> 1)) * 1024 + c * 16) : 0x80000000u;
        ad[8] = lds0 + C_P + w * 4;
        ad[9] = lds0 + L_SM;
        ad[10] = c;
#pragma unroll
        for (int i = 11; i < 16; ++i) ad[i] = 0u;
        asm volatile(PHASE_AB_ASM
                     : "+{v[240:255]}"(ad), "={v[232:235]}"(g4), "={v236}"(tag), [sof] "=&s"(sof), [t3] "=&s"(t3), [rid] "=&s"(rid), [rm] "=&s"(rm), "+{s80}"(j0)
                     : [rsb] "s"(rsB), [rsw] "s"(rsWS), [xs0] "s"(v ? XS1[0] - 8192u : XS0[0]), [xs1] "s"(v ? XS1[1] - 8192u : XS0[1]), [kpx] "s"(w * 65536),
                       [kpa] "s"(mtA * 512), [kma] "s"(hd * 16384 + v * 4096 + mtA * 64), [sg] "s"(WS_G + (unsigned)(mtB * 128 + 32 * w) * 8u),
                       [ofc] "s"(WB_OF + (unsigned)(mtB * 4 + w) * 65536u), [ofp] "s"(WB_OF + (unsigned)((mtA * 4 + 2 * v) * 32 + ((2 * hpA + hd + 1) & 31)) * 2048u),
                       [ogp] "s"(WB_OG + (unsigned)((mtA * 4 + 2 * v) * 32 + 2 * hpA + hd) * 8u), [kac] "s"(L_KVF + hd * 4096), [zac] "s"(L_ZF + hd * 512),
                       [xec] "s"(L_XE + w * 4096), [own] "s"(mtB * 4 + w), [prb] "s"(pa)
                     : "memory", "vcc", "scc", PHASE_AB_CLOB);
    }
    {
        const rsrc_t rsOut = mk_rsrc(out, 2048u * 2048u);
        const unsigned sog = WB_OG + sof + (unsigned)(mtB * 4 + w) * 256u;
        unsigned grp = (t3 & 1u) * 2u, flip = 0;
#pragma unroll 1
        for (int att = 0; att < TRY_LIMIT; ++att) {
            unsigned cs, st;
            asm volatile(PHASE_C_ASM
                         : "={v230}"(cs), [st] "=&s"(st)
                         : "{v[240:255]}"(ad), "{v[232:235]}"(g4), "{v236}"(tag), [rsb] "s"(rsB), [rso] "s"(rsOut), [sog] "s"(sog), [grp] "s"(grp), [flip] "s"(flip),
                           [t3] "s"(t3), [rid] "s"(rid), [rm] "s"(rm), [sout] "s"((unsigned)(mtB * 128 + 32 * w) * 2048u + (unsigned)hpB * 128u)
                         : "memory", "vcc", "scc", PHASE_C_CLOB);
            if (K::FORCE_REDO && att < 2) st = 2 - att, cs += 1u;
            if (__builtin_expect(st == 0u, 1)) break;
            u32x2 og = ld64c(rsB, ad[4], sog);
            const unsigned miss = (unsigned)__builtin_amdgcn_ballot_w64(og[1] != tag);
            if (st == 1u && ((miss && !(miss & ~(0xffffu << (8u * grp)))) || K::FORCE_REDO)) {
                grp ^= 2u;
                flip ^= 65536u;
                lds_wait(lds0 + C_WO, 4);
                continue;
            }
            int it = 0;
#pragma unroll 1
            for (; it < POLL_LIMIT && !__all(og[1] == tag); ++it) {
                __builtin_amdgcn_s_sleep(K::RETRY_S);
                fence_c();
                og = ld64c(rsB, ad[4], sog);
            }
            if (it == POLL_LIMIT) break;
            if (st == 2u) {
                if (__all(wave_usum(cs - (ad[10] < 32u ? og[0] : 0u)) == 0u)) break;
            }
        }
    }
    asm volatile(".Lff_end2:\n.Lpad_x:\n\ts_cbranch_execz .Lpad_top");
}

extern "C" void kernel_launch(void* const* d_in, const int* in_sizes, int n_in, void* d_out, int out_size,
                              void* d_ws, size_t ws_size, hipStream_t stream) {
    const u16* x = (const u16*)d_in[0];
    const u16* wqkv = (const u16*)d_in[1];
    const u16* wphi = (const u16*)d_in[2];
    const u16* wo = (const u16*)d_in[3];
    const u16* wr = (const u16*)d_in[5];
    const u16* wg = (const u16*)d_in[6];
    const u16* wu = (const u16*)d_in[7];
    const u16* wd = (const u16*)d_in[8];
    const u16* gw = (const u16*)d_in[9];
    fused_fwd<<<256, 512, 0, stream>>>(x, wqkv, wphi, wo, wr, wg, wu, wd, gw, (char*)d_ws, (u16*)d_out);
}
```

```cpp
#include <hip/hip_runtime.h>
#include <stdint.h>
#include <type_traits>

typedef __attribute__((ext_vector_type(8))) short bf16x8;
typedef __attribute__((ext_vector_type(4))) short s16x4;
typedef __attribute__((ext_vector_type(4))) float f32x4;
typedef __attribute__((ext_vector_type(2))) float f32x2;
typedef __attribute__((ext_vector_type(32))) float f32x32;
typedef __attribute__((ext_vector_type(2))) unsigned u32x2;
typedef __attribute__((ext_vector_type(4))) unsigned u32x4;
typedef __attribute__((ext_vector_type(8))) unsigned u32x8;
typedef __attribute__((ext_vector_type(16))) unsigned u32x16;
typedef __attribute__((ext_vector_type(32))) unsigned u32x32;
typedef __attribute__((ext_vector_type(2))) __bf16 bf16x2_t;
typedef unsigned short u16;
typedef __amdgpu_buffer_rsrc_t rsrc_t;

#define DI __device__ __forceinline__
#define AS3 __attribute__((address_space(3)))
#define RLX_WG __ATOMIC_RELAXED, __HIP_MEMORY_SCOPE_WORKGROUP

constexpr int AUX_SC1 = 16;

DI float bf2f(u16 v) { return __uint_as_float(((unsigned)v) << 16); }
DI u16 f2bf(float f) { __bf16 b = (__bf16)f; return __builtin_bit_cast(u16, b); }
DI float rbf(float f) { return bf2f(f2bf(f)); }
DI unsigned pack2(float lo, float hi) {
    f32x2 v = {lo, hi};
    bf16x2_t b = __builtin_convertvector(v, bf16x2_t);
    return __builtin_bit_cast(unsigned, b);
}
DI float lo16(unsigned u) { return __uint_as_float(u << 16); }
DI float hi16(unsigned u) { return __uint_as_float(u & 0xffff0000u); }
DI float dot2(unsigned a, unsigned b, float c) {
    return __builtin_amdgcn_fdot2_f32_bf16(__builtin_bit_cast(bf16x2_t, a), __builtin_bit_cast(bf16x2_t, b), c, false);
}
DI s16x4 tr_read(unsigned a) { return __builtin_amdgcn_ds_read_tr16_b64_v4i16((AS3 s16x4*)(size_t)a); }
DI bf16x8 cat8(s16x4 a, s16x4 b) { return __builtin_shufflevector(a, b, 0, 1, 2, 3, 4, 5, 6, 7); }
DI bf16x8 lds_rd128(unsigned a) { return *(AS3 const bf16x8*)(size_t)a; }
DI void lds_wr128(unsigned a, bf16x8 v) { *(AS3 bf16x8*)(size_t)a = v; }
DI f32x4 mfma16(bf16x8 a, bf16x8 b, f32x4 c) { return __builtin_amdgcn_mfma_f32_16x16x32_bf16(a, b, c, 0, 0, 0); }
DI bf16x8 pack44(f32x4 a, f32x4 b) {
    u32x4 p = {pack2(a[0], a[1]), pack2(a[2], a[3]), pack2(b[0], b[1]), pack2(b[2], b[3])};
    return __builtin_bit_cast(bf16x8, p);
}
#define FU(x) __float_as_uint(x)
#define UF(x) __uint_as_float(x)
DI float x32_sum(float v) { auto r = __builtin_amdgcn_permlane32_swap(FU(v), FU(v), false, false); return UF(r[0]) + UF(r[1]); }
DI float x16_sum(float v) { auto r = __builtin_amdgcn_permlane16_swap(FU(v), FU(v), false, false); return UF(r[0]) + UF(r[1]); }
DI float xg_sum(float v) { return x16_sum(x32_sum(v)); }
template <int CTRL> DI float dpp_add(float v) { return v + UF(__builtin_amdgcn_update_dpp(0, (int)FU(v), CTRL, 0xf, 0xf, false)); }
DI float row16_sum(float v) { return dpp_add<0x121>(dpp_add<0x122>(dpp_add<0x124>(dpp_add<0x128>(v)))); }
template <int CTRL> DI unsigned dpp_umax(unsigned v) { return max(v, (unsigned)__builtin_amdgcn_update_dpp(0, (int)v, CTRL, 0xf, 0xf, false)); }
DI unsigned wave_umax(unsigned v) {
    auto r = __builtin_amdgcn_permlane32_swap(v, v, false, false);
    v = max(r[0], r[1]);
    auto q = __builtin_amdgcn_permlane16_swap(v, v, false, false);
    v = max(q[0], q[1]);
    return dpp_umax<0x121>(dpp_umax<0x122>(dpp_umax<0x124>(dpp_umax<0x128>(v))));
}
DI unsigned add4(u32x4 v, unsigned s) { return (v[0] + v[1] + v[2]) + (v[3] + s); }
template <int CTRL> DI unsigned dpp_uadd(unsigned v) { return v + (unsigned)__builtin_amdgcn_update_dpp(0, (int)v, CTRL, 0xf, 0xf, false); }
DI unsigned row16_usum(unsigned v) { return dpp_uadd<0x121>(dpp_uadd<0x122>(dpp_uadd<0x124>(dpp_uadd<0x128>(v)))); }
DI rsrc_t mk_rsrc(const void* p, unsigned bytes) { return __builtin_amdgcn_make_buffer_rsrc((void*)p, (short)0, (int)bytes, 0x00020000); }
DI u32x4 ld128c(rsrc_t rs, unsigned vo, unsigned so) { return __builtin_amdgcn_raw_buffer_load_b128(rs, vo, so, AUX_SC1); }
DI u32x2 ld64c(rsrc_t rs, unsigned vo, unsigned so) { return __builtin_amdgcn_raw_buffer_load_b64(rs, vo, so, AUX_SC1); }
DI unsigned ld32c(rsrc_t rs, unsigned vo, unsigned so) { return __builtin_amdgcn_raw_buffer_load_b32(rs, vo, so, AUX_SC1); }
DI void st128c(u32x4 v, rsrc_t rs, unsigned vo, unsigned so) {
    __builtin_amdgcn_raw_buffer_store_b128(v, rs, vo, so, AUX_SC1);
    asm volatile("s_nop 1" :: "v"(v) : "memory");
}
DI void st64c(u32x2 v, rsrc_t rs, unsigned vo, unsigned so) { __builtin_amdgcn_raw_buffer_store_b64(v, rs, vo, so, AUX_SC1); }
DI void st32c(unsigned v, rsrc_t rs, unsigned vo, unsigned so) { __builtin_amdgcn_raw_buffer_store_b32(v, rs, vo, so, AUX_SC1); }

DI void dma16(rsrc_t rs, unsigned voff, unsigned soff, unsigned lds_addr) {
    asm volatile("s_mov_b32 m0, %0\n\ts_nop 0\n\tbuffer_load_dwordx4 %1, %2, %3 offen lds"
                 :: "s"(lds_addr), "v"(voff), "s"(rs), "s"(soff) : "memory");
}
template <int PC> DI void dma4(rsrc_t rs, const unsigned* v, unsigned soff, unsigned lds_addr) {
#define D4(p) asm volatile("s_mov_b32 m0, %0\n\ts_nop 0\n\tbuffer_load_dwordx4 %1, %5, %6 offen lds\n\t" p "buffer_load_dwordx4 %2, %5, %6 offen offset:1024 lds\n\t" p \
                 "buffer_load_dwordx4 %3, %5, %6 offen offset:2048 lds\n\t" p "buffer_load_dwordx4 %4, %5, %6 offen offset:3072 lds" \
                 :: "s"(lds_addr), "v"(v[0]), "v"(v[1]), "v"(v[2]), "v"(v[3]), "s"(rs), "s"(soff), "i"(PC ? PC - 1 : 0) : "memory")
    if (PC) D4("s_nop %7\n\t"); else D4("");
}
template <int N> DI void wait_vm() { asm volatile("s_waitcnt vmcnt(%0)" :: "i"(N) : "memory"); }
DI void wait_lgkm0() { asm volatile("" ::: "memory"); __builtin_amdgcn_s_waitcnt(0xC07F); asm volatile("" ::: "memory"); }
DI void bar() { asm volatile("" ::: "memory"); __builtin_amdgcn_s_barrier(); asm volatile("" ::: "memory"); }
DI void fence_c() { asm volatile("" ::: "memory"); }
DI int fresh_lane() {
    int v;
    asm volatile("v_mbcnt_lo_u32_b32 %0, -1, 0\n\tv_mbcnt_hi_u32_b32 %0, -1, %0" : "=v"(v));
    return v;
}
#define LANE_VARS const int l = fresh_lane(), c = l & 15, g = l >> 4, i4 = (l & 15) >> 2, i3 = l & 3; (void)c; (void)g; (void)i4; (void)i3
#define PSW ((unsigned)(4 * g + i4) * 128u + (unsigned)((i4 ^ (g & 1)) << 5))
#define WPO ((unsigned)(4 * g + i4) * 64u + (unsigned)((g & 1) << 5) + (unsigned)(8 * i3))
#define WSW ((unsigned)(8 * g + i4) * 128u + (unsigned)((i4 ^ (g & 1)) << 5) + (unsigned)(8 * i3))

#define WO_VOFF ((unsigned)(l >> 3) * 2048u + (unsigned)(((((((l & 7) >> 1) ^ ((l >> 3) & 3) ^ (l >> 5)) << 1) | (l & 1))) << 4))

DI void lds_wait(unsigned addr, unsigned target) {
#pragma unroll 1
    for (int it = 0; it < (1 << 22); ++it) {
        unsigned v = __hip_atomic_load((AS3 unsigned*)(size_t)addr, RLX_WG);
        if (__builtin_amdgcn_readfirstlane(v) >= target) break;
        __builtin_amdgcn_s_sleep(1);
    }
    fence_c();
}
DI unsigned now32() { unsigned long long t; asm volatile("s_memrealtime %0\n\ts_waitcnt lgkmcnt(0)" : "=s"(t) :: "memory"); return (unsigned)t; }
DI void wait_until(unsigned t) {
#pragma unroll 1
    for (int it = 0; it < 256; ++it) {
        const int rem = (int)(t - now32());
        if (rem <= 4) break;
        if (rem > 12) __builtin_amdgcn_s_sleep(2);
    }
    fence_c();
}
DI unsigned wave_usum(unsigned m) {
    auto p = __builtin_amdgcn_permlane32_swap(m, m, false, false);
    m = p[0] + p[1];
    auto q = __builtin_amdgcn_permlane16_swap(m, m, false, false);
    return row16_usum(q[0] + q[1]);
}
DI void post(unsigned addr, unsigned v) {
    fence_c();
    *(AS3 volatile unsigned*)(size_t)addr = v;
    fence_c();
}
DI void wait4(unsigned addr, unsigned target) {
#pragma unroll 1
    for (int it = 0; it < (1 << 22); ++it) {
        const u32x4 v = *(AS3 volatile const u32x4*)(size_t)addr;
        if (__builtin_amdgcn_readfirstlane(min(min(v[0], v[1]), min(v[2], v[3]))) >= target) break;
        __builtin_amdgcn_s_sleep(1);
    }
    fence_c();
}
constexpr int POLL_LIMIT = 1 << 16, TRY_LIMIT = 1 << 12;

#define KNOB(n, v) static constexpr int n = v
#define KNOBS
struct Dflt {
    KNOB(KT0, 0x76534210); KNOB(KT1, 0x76453201); KNOB(KT2, 0x75643120); KNOB(KT3, 0x67542310);
    KNOB(BGT, 0x76227622); KNOB(BUT, 0x77327732); KNOB(BRT, 0x77777777); KNOB(BST, 0x33333333); KNOB(PBT, (int)0xaaaaaaaa);
    KNOB(SLG, 0); KNOB(SLS, 20); KNOB(X0, 130); KNOB(X1, 2); KNOB(X2, 2); KNOB(TOPV, 1);
    KNOB(PRED_K, 16); KNOB(PRED_C, 90);
    KNOB(D1, 40); KNOB(D2, 120); KNOB(D3, 140); KNOB(FG_AT, -130); KNOB(Y_AT, 260); KNOB(QA, 30); KNOB(QB, 40);
    KNOB(RETRY_S, 8); KNOB(PT_S, 12); KNOB(KF_S, 10); KNOB(WO_D, 24); KNOB(FD, 12);
    KNOB(PT_B, 10); KNOB(XE, 20);
    KNOB(RE1, 1); KNOB(L2_S, 12);
    KNOB(FORCE_REDO, 0);
    KNOB(ND, 6); KNOB(XAH, 0); KNOB(QG, 2); KNOB(HP, 2); KNOB(Q5E, 1); KNOB(PADS, 1); KNOB(WOSK, 0); KNOB(WOE, 0); KNOB(TNT, 0); KNOB(TWB, 13); KNOB(TMW, 0); KNOB(DWP, 1); KNOB(WOG, 0); KNOB(PK13, 3); KNOB(DS1, 2); KNOB(DS2, 9); KNOB(DS3, 16); KNOB(PACE, 0);
};
struct K : Dflt { KNOBS };

constexpr unsigned WS_EPOCH = 0;
constexpr unsigned WS_RG = 4096;
constexpr unsigned WS_PT = 6144;
constexpr unsigned WS_G = 8192;
constexpr unsigned WS_YF = 24576;
constexpr unsigned WS_FG = 65536;
constexpr unsigned WS_Y = 1u << 19;
constexpr unsigned WS_BYTES = 1u << 20;
constexpr unsigned WB_KVP = 0;
constexpr unsigned WB_SM = 1u << 20;
constexpr unsigned WB_PRB = 3u << 19;
constexpr unsigned WB_OG = 7u << 18;
constexpr unsigned WB_OF = 8u << 20;
constexpr unsigned WB_BYTES = 13u << 20;
DI char* bulk_base(char* ws) { return (char*)(((size_t)ws + (3u << 20)) & ~(size_t)((2u << 20) - 1)); }

constexpr unsigned SLOT = 40960;
constexpr unsigned L_PHI = 98304;
constexpr unsigned L_VF = 131072;
constexpr unsigned L_MISC = 147456;
constexpr unsigned L_WP = L_MISC, L_KVF = L_MISC + 4096, L_SM = L_MISC + 12288;
constexpr unsigned C_ST = L_SM + 0  , C_R = L_SM + 8  , C_P = L_SM + 16  ;
constexpr unsigned C_WO = L_SM + 56, C_YRDY = L_SM + 68, C_IDX = L_SM + 76, C_TM = L_SM + 80, C_XE = L_SM + 84, C_Y1 = L_SM + 88;
constexpr unsigned P_E14 = 1, P_E15 = 2, P_PHA = 3, P_PHB = 4, P_KVD = 5, P_PUB = 6, P_Q1 = 7  , P_QRD = 11, P_O = 12;
constexpr unsigned C_Q = L_SM + 92, C_HOLD = L_SM + 96;
constexpr unsigned XS0[3] = {16384, 98304, L_VF}, XS1[3] = {57344, 122880, L_VF + 8192};
constexpr unsigned QX[3] = {40960, 81920, 0}, QW[6] = {L_KVF, 65536, 73728, 24576, 32768, L_PHI};
constexpr unsigned R_L1I = 1, R_L1 = 2, R_L2I = 3, R_KVF = 4;
constexpr unsigned L_XE = L_VF;
constexpr unsigned L_KEY = L_SM + 256, L_TAG = L_SM + 288, L_IDX = L_SM + 296, L_TM = L_SM + 304  , L_PLC = L_SM + 320  , L_PRB = L_SM + 336  , L_ZF = L_SM + 512, L_Y = L_SM + 1536;

constexpr unsigned long long L2E2 = 0x3fb8aa3b3fb8aa3bull;

#define PHASE_AB_ASM \
    "s_branch .Lpad_ab0\n\t" \
    ".Lab_s1:\n\t" \
    "s_mov_b32 s89, 0x400000\n\t" \
    ".Lab_s1a:\n\t" \
    "s_waitcnt lgkmcnt(0)\n\t" \
    "ds_read_b32 v237, v249\n\t" \
    "ds_read_b32 v238, v249 offset:4\n\t" \
    "s_waitcnt lgkmcnt(0)\n\t" \
    "v_min_u32 v237, v237, v238\n\t" \
    "s_nop 1\n\t" \
    "v_readfirstlane_b32 s85, v237\n\t" \
    "s_sub_u32 s89, s89, 1\n\t" \
    "s_cbranch_scc1 .Lab_s1b\n\t" \
    "s_cmp_lt_u32 s85, 1\n\t" \
    "s_cbranch_scc1 .Lab_s1a\n\t" \
    ".Lab_s1b:\n\t" \
    "s_nop 7\n\t" \
    "s_waitcnt lgkmcnt(0)\n\t" \
    "s_branch .Lab_s1r\n\t" \
    ".Lab_s2:\n\t" \
    "s_mov_b32 s89, 0x400000\n\t" \
    ".Lab_s2a:\n\t" \
    "s_waitcnt lgkmcnt(0)\n\t" \
    "ds_read_b32 v237, v249 offset:16\n\t" \
    "ds_read_b32 v238, v249 offset:20\n\t" \
    "ds_read_b32 v255, v249 offset:24\n\t" \
    "s_waitcnt lgkmcnt(0)\n\t" \
    "v_min3_u32 v237, v237, v238, v255\n\t" \
    "ds_read_b32 v238, v249 offset:28\n\t" \
    "s_waitcnt lgkmcnt(0)\n\t" \
    "v_min_u32 v237, v237, v238\n\t" \
    "s_nop 1\n\t" \
    "v_readfirstlane_b32 s85, v237\n\t" \
    "s_sub_u32 s89, s89, 1\n\t" \
    "s_cbranch_scc1 .Lab_s2b\n\t" \
    "s_cmp_lt_u32 s85, 2\n\t" \
    "s_cbranch_scc1 .Lab_s2a\n\t" \
    ".Lab_s2b:\n\t" \
    "s_nop 7\n\t" \
    "s_waitcnt lgkmcnt(0)\n\t" \
    "s_branch .Lab_s2r\n\t" \
    ".Lab_s3:\n\t" \
    "s_mov_b32 s89, 0x400000\n\t" \
    ".Lab_s3a:\n\t" \
    "s_waitcnt lgkmcnt(0)\n\t" \
    "ds_read_b32 v237, v250\n\t" \
    "s_waitcnt lgkmcnt(0)\n\t" \
    "s_nop 1\n\t" \
    "v_readfirstlane_b32 s85, v237\n\t" \
    "s_sub_u32 s89, s89, 1\n\t" \
    "s_cbranch_scc1 .Lab_s3b\n\t" \
    "s_cmp_lt_u32 s85, 3\n\t" \
    "s_cbranch_scc1 .Lab_s3a\n\t" \
    ".Lab_s3b:\n\t" \
    "s_nop 7\n\t" \
    "ds_read_b64_tr_b16 v[104:105], v245\n\t" \
    "ds_read_b64_tr_b16 v[106:107], v245 offset:2048\n\t" \
    "ds_read_b64_tr_b16 v[108:109], v255\n\t" \
    "ds_read_b64_tr_b16 v[110:111], v255 offset:2048\n\t" \
    "ds_read_b128 v[80:83], v242\n\t" \
    "ds_read_b128 v[84:87], v242 offset:1024\n\t" \
    "ds_read_b128 v[88:91], v242 offset:4096\n\t" \
    "ds_read_b128 v[92:95], v242 offset:5120\n\t" \
    "s_waitcnt lgkmcnt(0)\n\t" \
    "s_branch .Lab_s3r\n\t" \
    ".Lab_s4:\n\t" \
    "s_mov_b32 s89, 0x400000\n\t" \
    ".Lab_s4a:\n\t" \
    "s_waitcnt lgkmcnt(0)\n\t" \
    "ds_read_b32 v237, v250\n\t" \
    "s_waitcnt lgkmcnt(0)\n\t" \
    "s_nop 1\n\t" \
    "v_readfirstlane_b32 s85, v237\n\t" \
    "s_sub_u32 s89, s89, 1\n\t" \
    "s_cbranch_scc1 .Lab_s4b\n\t" \
    "s_cmp_lt_u32 s85, 4\n\t" \
    "s_cbranch_scc1 .Lab_s4a\n\t" \
    ".Lab_s4b:\n\t" \
    "s_nop 7\n\t" \
    "ds_read_b64_tr_b16 v[8:9], v245 offset:4096\n\t" \
    "ds_read_b64_tr_b16 v[10:11], v245 offset:6144\n\t" \
    "ds_read_b64_tr_b16 v[12:13], v255 offset:4096\n\t" \
    "ds_read_b64_tr_b16 v[14:15], v255 offset:6144\n\t" \
    "v_mov_b32 v237, 5\n\t" \
    "s_waitcnt lgkmcnt(0)\n\t" \
    "s_branch .Lab_s4r\n\t" \
    ".Lab_s5:\n\t" \
    "s_mov_b32 s89, 0x400000\n\t" \
    ".Lab_s5a:\n\t" \
    "s_waitcnt lgkmcnt(0)\n\t" \
    "ds_read_b32 v237, v249 offset:92\n\t" \
    "s_waitcnt lgkmcnt(0)\n\t" \
    "s_nop 1\n\t" \
    "v_readfirstlane_b32 s85, v237\n\t" \
    "s_sub_u32 s89, s89, 1\n\t" \
    "s_cbranch_scc1 .Lab_s5b\n\t" \
    "s_cmp_lt_u32 s85, 1\n\t" \
    "s_cbranch_scc1 .Lab_s5a\n\t" \
    ".Lab_s5b:\n\t" \
    "s_nop 7\n\t" \
    "ds_read_b64_tr_b16 v[80:81], v253\n\t" \
    "ds_read_b64_tr_b16 v[82:83], v253 offset:512\n\t" \
    "ds_read_b64_tr_b16 v[84:85], v251\n\t" \
    "ds_read_b64_tr_b16 v[86:87], v251 offset:512\n\t" \
    "ds_read_b64_tr_b16 v[88:89], v253 offset:4096\n\t" \
    "ds_read_b64_tr_b16 v[90:91], v253 offset:4608\n\t" \
    "ds_read_b64_tr_b16 v[92:93], v251 offset:4096\n\t" \
    "ds_read_b64_tr_b16 v[94:95], v251 offset:4608\n\t" \
    "s_waitcnt lgkmcnt(0)\n\t" \
    "s_branch .Lab_s5r\n\t" \
    ".Lab_s6:\n\t" \
    "s_mov_b32 s89, 0x400000\n\t" \
    ".Lab_s6a:\n\t" \
    "s_waitcnt lgkmcnt(0)\n\t" \
    "ds_read_b32 v237, v249 offset:92\n\t" \
    "s_waitcnt lgkmcnt(0)\n\t" \
    "s_nop 1\n\t" \
    "v_readfirstlane_b32 s85, v237\n\t" \
    "s_sub_u32 s89, s89, 1\n\t" \
    "s_cbranch_scc1 .Lab_s6b\n\t" \
    "s_cmp_lt_u32 s85, 2\n\t" \
    "s_cbranch_scc1 .Lab_s6a\n\t" \
    ".Lab_s6b:\n\t" \
    "s_nop 7\n\t" \
    "v_mov_b32 v237, 10\n\t" \
    "s_waitcnt lgkmcnt(0)\n\t" \
    "s_branch .Lab_s6r\n\t" \
    ".Lab_s7:\n\t" \
    "s_mov_b32 s89, 0x400000\n\t" \
    ".Lab_s7a:\n\t" \
    "s_sleep 1\n\t" \
    "ds_read_b32 v237, v249 offset:8\n\t" \
    "s_waitcnt lgkmcnt(0)\n\t" \
    "v_readfirstlane_b32 s85, v237\n\t" \
    "s_sub_u32 s89, s89, 1\n\t" \
    "s_cbranch_scc1 .Lab_s7r\n\t" \
    "s_cmp_lt_u32 s85, 3\n\t" \
    "s_cbranch_scc1 .Lab_s7a\n\t" \
    "s_sleep 5\n\t" \
    "s_branch .Lab_s7r\n\t" \
    ".Lab_sa:\n\t" \
    "ds_read_b32 v36, v249 offset:8\n\t" \
    "s_waitcnt lgkmcnt(0)\n\t" \
    "v_readfirstlane_b32 s80, v36\n\t" \
    "s_sub_u32 s89, s89, 1\n\t" \
    "s_cbranch_scc1 .Lab_sar\n\t" \
    "s_cmp_lt_u32 s80, s82\n\t" \
    "s_cbranch_scc1 .Lab_sa\n\t" \
    "s_cmp_eq_u32 s80, s82\n\t" \
    "s_cbranch_scc1 .Lab_sar\n\t" \
    "s_waitcnt vmcnt(0)\n\t" \
    "s_branch .Lab_k\n\t" \
    ".Lab_s8:\n\t" \
    "s_mov_b32 s89, 0x10000\n\t" \
    "v_lshlrev_b32 v35, 3, v34\n\t" \
    ".Lab_s8a:\n\t" \
    "s_sleep 8\n\t" \
    "buffer_load_dwordx2 v[232:233], v35, %[rsw], %[sg] offen sc1\n\t" \
    "buffer_load_dwordx2 v[234:235], v35, %[rsw], %[sg] offen offset:128 sc1\n\t" \
    "s_sub_u32 s89, s89, 1\n\t" \
    "s_waitcnt vmcnt(0)\n\t" \
    "s_cbranch_scc1 .Lab_s8r\n\t" \
    "v_cmp_ne_u32 vcc, v236, v233\n\t" \
    "v_cmp_ne_u32_e64 s[92:93], v236, v235\n\t" \
    "s_nop 0\n\t" \
    "s_or_b64 vcc, vcc, s[92:93]\n\t" \
    "s_cbranch_scc1 .Lab_s8a\n\t" \
    "s_branch .Lab_s8r\n\t" \
    ".Lab_s9:\n\t" \
    "s_mov_b32 s89, 0x400000\n\t" \
    ".Lab_s9a:\n\t" \
    "s_waitcnt lgkmcnt(0)\n\t" \
    "ds_read_b32 v237, v249 offset:56\n\t" \
    "s_waitcnt lgkmcnt(0)\n\t" \
    "s_nop 1\n\t" \
    "v_readfirstlane_b32 s85, v237\n\t" \
    "s_sub_u32 s89, s89, 1\n\t" \
    "s_cbranch_scc1 .Lab_s9b\n\t" \
    "s_cmp_lt_u32 s85, 2\n\t" \
    "s_cbranch_scc1 .Lab_s9a\n\t" \
    ".Lab_s9b:\n\t" \
    "s_nop 7\n\t" \
    "s_waitcnt lgkmcnt(0)\n\t" \
    "s_branch .Lab_s9r\n\t" \
    ".p2align 12\n\t" \
    ".Lpad_ab0:\n\t" \
    "s_cbranch_execz .Lpad_top\n\t" \
    "v_mov_b32 v32, 0\n\t" \
    "v_mov_b32 v33, 0\n\t" \
    "v_mov_b32 v34, 0\n\t" \
    "v_mov_b32 v35, 0\n\t" \
    "v_mov_b32 v36, 0\n\t" \
    "v_mov_b32 v37, 0\n\t" \
    "v_mov_b32 v38, 0\n\t" \
    "v_mov_b32 v39, 0\n\t" \
    "v_mov_b32 v40, 0\n\t" \
    "v_mov_b32 v41, 0\n\t" \
    "v_mov_b32 v42, 0\n\t" \
    "v_mov_b32 v43, 0\n\t" \
    "v_mov_b32 v44, 0\n\t" \
    "v_mov_b32 v45, 0\n\t" \
    "v_mov_b32 v46, 0\n\t" \
    "v_mov_b32 v47, 0\n\t" \
    "v_mov_b32 v48, 0\n\t" \
    "v_mov_b32 v49, 0\n\t" \
    "v_mov_b32 v50, 0\n\t" \
    "v_mov_b32 v51, 0\n\t" \
    "v_mov_b32 v52, 0\n\t" \
    "v_mov_b32 v53, 0\n\t" \
    "v_mov_b32 v54, 0\n\t" \
    "v_mov_b32 v55, 0\n\t" \
    "v_mov_b32 v56, 0\n\t" \
    "v_mov_b32 v57, 0\n\t" \
    "v_mov_b32 v58, 0\n\t" \
    "v_mov_b32 v59, 0\n\t" \
    "v_mov_b32 v60, 0\n\t" \
    "v_mov_b32 v61, 0\n\t" \
    "v_mov_b32 v62, 0\n\t" \
    "v_mov_b32 v63, 0\n\t" \
    "v_mov_b32 v80, 0\n\t" \
    "v_mov_b32 v81, 0\n\t" \
    "v_mov_b32 v82, 0\n\t" \
    "v_mov_b32 v83, 0\n\t" \
    "v_mov_b32 v84, 0\n\t" \
    "v_mov_b32 v85, 0\n\t" \
    "v_mov_b32 v86, 0\n\t" \
    "v_mov_b32 v87, 0\n\t" \
    "s_waitcnt lgkmcnt(0)\n\t" \
    "s_memtime s[82:83]\n\t" \
    "s_load_dword s80, %[prb], 0\n\t" \
    "v_mov_b32 v88, 0\n\t" \
    "v_mov_b32 v89, 0\n\t" \
    "v_mov_b32 v90, 0\n\t" \
    "v_mov_b32 v91, 0\n\t" \
    "v_mov_b32 v92, 0\n\t" \
    "v_mov_b32 v93, 0\n\t" \
    "v_mov_b32 v94, 0\n\t" \
    "v_mov_b32 v95, 0\n\t" \
    "v_mov_b32 v96, 0\n\t" \
    "v_mov_b32 v97, 0\n\t" \
    "v_mov_b32 v98, 0\n\t" \
    "v_mov_b32 v99, 0\n\t" \
    "v_mov_b32 v100, 0\n\t" \
    "v_mov_b32 v101, 0\n\t" \
    "v_mov_b32 v102, 0\n\t" \
    "v_mov_b32 v103, 0\n\t" \
    "v_mov_b32 v104, 0\n\t" \
    "v_mov_b32 v105, 0\n\t" \
    "v_mov_b32 v106, 0\n\t" \
    "v_mov_b32 v107, 0\n\t" \
    "v_mov_b32 v108, 0\n\t" \
    "v_mov_b32 v109, 0\n\t" \
    "v_mov_b32 v110, 0\n\t" \
    "v_mov_b32 v111, 0\n\t" \
    "v_mov_b32 v192, 0\n\t" \
    "v_mov_b32 v193, 0\n\t" \
    "v_mov_b32 v194, 0\n\t" \
    "v_mov_b32 v195, 0\n\t" \
    "v_mov_b32 v196, 0\n\t" \
    "v_mov_b32 v197, 0\n\t" \
    "v_mov_b32 v198, 0\n\t" \
    "v_mov_b32 v199, 0\n\t" \
    "v_mov_b32 v200, 0\n\t" \
    "v_mov_b32 v201, 0\n\t" \
    "v_mov_b32 v202, 0\n\t" \
    "v_mov_b32 v203, 0\n\t" \
    "v_mov_b32 v204, 0\n\t" \
    "v_mov_b32 v205, 0\n\t" \
    "v_mov_b32 v206, 0\n\t" \
    "v_mov_b32 v207, 0\n\t" \
    "v_mov_b32 v208, 0\n\t" \
    "v_mov_b32 v209, 0\n\t" \
    "v_mov_b32 v210, 0\n\t" \
    "v_mov_b32 v211, 0\n\t" \
    "v_mov_b32 v212, 0\n\t" \
    "v_mov_b32 v213, 0\n\t" \
    "v_mov_b32 v214, 0\n\t" \
    "v_mov_b32 v215, 0\n\t" \
    "v_mov_b32 v216, 0\n\t" \
    "v_mov_b32 v217, 0\n\t" \
    "v_mov_b32 v218, 0\n\t" \
    "v_mov_b32 v219, 0\n\t" \
    "v_mov_b32 v220, 0\n\t" \
    "v_mov_b32 v221, 0\n\t" \
    "v_mov_b32 v222, 0\n\t" \
    "v_mov_b32 v223, 0\n\t" \
    "s_waitcnt lgkmcnt(0)\n\t" \
    "s_memtime s[84:85]\n\t" \
    "s_waitcnt lgkmcnt(0)\n\t" \
    "s_sub_u32 s82, s84, s82\n\t" \
    "v_mov_b32 v237, s82\n\t" \
    "ds_write_b32 v248, v237 offset:320\n\t" \
    "v_mov_b32 v232, 0x3f803f80\n\t" \
    "v_mov_b32 v233, 0x3f803f80\n\t" \
    "v_mov_b32 v234, 0x3f803f80\n\t" \
    "v_mov_b32 v235, 0x3f803f80\n\t" \
    "v_mov_b32 v251, v240\n\t" \
    "v_xor_b32 v252, 64, v240\n\t" \
    "v_mov_b32 v253, v241\n\t" \
    "v_xor_b32 v254, 32, v241\n\t" \
    "s_mov_b32 s80, 0\n\t" \
    "s_mov_b32 s81, 10\n\t" \
    "s_mov_b32 s90, 0x3fb8aa3b\n\t" \
    "s_mov_b32 s91, 0x3fb8aa3b\n\t" \
    "v_cmp_eq_u32_e64 s[92:93], 2, v250\n\t" \
    "v_cmp_gt_u32_e64 s[94:95], 2, v250\n\t" \
    "v_cmp_eq_u32 vcc, 0, v250\n\t" \
    "v_xor_b32 v250, 4, v248\n\t" \
    "s_mov_b32 s84, 0x140a0014\n\t" \
    "s_lshl_b32 s83, %[xs0], 4\n\t" \
    "s_lshl_b32 s85, %[xs1], 12\n\t" \
    "s_or_b32 s83, s83, s85\n\t" \
    "s_or_b32 s83, s83, 0x14\n\t" \
    "s_waitcnt lgkmcnt(0)\n\t" \
    "s_barrier\n\t" \
    "ds_read_b128 v[128:131], v251\n\t" \
    "ds_read_b128 v[132:135], v251 offset:2048\n\t" \
    "ds_read_b128 v[136:139], v251 offset:4096\n\t" \
    "ds_read_b128 v[140:143], v251 offset:6144\n\t" \
    "ds_read_b64_tr_b16 v[0:1], v253 offset:8192\n\t" \
    "ds_read_b64_tr_b16 v[2:3], v253 offset:8704\n\t" \
    "ds_read_b64_tr_b16 v[4:5], v254 offset:8192\n\t" \
    "ds_read_b64_tr_b16 v[6:7], v254 offset:8704\n\t" \
    "ds_read_b64_tr_b16 v[8:9], v253 offset:16384\n\t" \
    "ds_read_b64_tr_b16 v[10:11], v253 offset:16896\n\t" \
    "ds_read_b64_tr_b16 v[12:13], v254 offset:16384\n\t" \
    "ds_read_b64_tr_b16 v[14:15], v254 offset:16896\n\t" \
    "ds_read_b64_tr_b16 v[16:17], v253\n\t" \
    "ds_read_b64_tr_b16 v[18:19], v253 offset:512\n\t" \
    "ds_read_b64_tr_b16 v[20:21], v254\n\t" \
    "ds_read_b64_tr_b16 v[22:23], v254 offset:512\n\t" \
    ".Lab_l1:\n\t" \
    "s_waitcnt lgkmcnt(0)\n\t" \
    "v_mfma_f32_16x16x32_bf16 v[32:35], v[0:3], v[128:131], v[32:35]\n\t" \
    "ds_read_b128 v[144:147], v252\n\t" \
    "v_mfma_f32_16x16x32_bf16 v[40:43], v[0:3], v[132:135], v[40:43]\n\t" \
    "ds_read_b128 v[148:151], v252 offset:2048\n\t" \
    "v_mfma_f32_16x16x32_bf16 v[48:51], v[0:3], v[136:139], v[48:51]\n\t" \
    "ds_read_b128 v[152:155], v252 offset:4096\n\t" \
    "v_mfma_f32_16x16x32_bf16 v[56:59], v[0:3], v[140:143], v[56:59]\n\t" \
    "ds_read_b128 v[156:159], v252 offset:6144\n\t" \
    "v_mfma_f32_16x16x32_bf16 v[36:39], v[4:7], v[128:131], v[36:39]\n\t" \
    "ds_read_b64_tr_b16 v[112:113], v253 offset:12288\n\t" \
    "v_mfma_f32_16x16x32_bf16 v[44:47], v[4:7], v[132:135], v[44:47]\n\t" \
    "ds_read_b64_tr_b16 v[114:115], v253 offset:12800\n\t" \
    "v_mfma_f32_16x16x32_bf16 v[52:55], v[4:7], v[136:139], v[52:55]\n\t" \
    "ds_read_b64_tr_b16 v[116:117], v254 offset:12288\n\t" \
    "v_mfma_f32_16x16x32_bf16 v[60:63], v[4:7], v[140:143], v[60:63]\n\t" \
    "ds_read_b64_tr_b16 v[118:119], v254 offset:12800\n\t" \
    "v_mfma_f32_16x16x32_bf16 v[80:83], v[128:131], v[8:11], v[80:83]\n\t" \
    "ds_read_b64_tr_b16 v[120:121], v253 offset:20480\n\t" \
    "v_mfma_f32_16x16x32_bf16 v[88:91], v[132:135], v[8:11], v[88:91]\n\t" \
    "ds_read_b64_tr_b16 v[122:123], v253 offset:20992\n\t" \
    "v_mfma_f32_16x16x32_bf16 v[96:99], v[136:139], v[8:11], v[96:99]\n\t" \
    "ds_read_b64_tr_b16 v[124:125], v254 offset:20480\n\t" \
    "v_mfma_f32_16x16x32_bf16 v[104:107], v[140:143], v[8:11], v[104:107]\n\t" \
    "ds_read_b64_tr_b16 v[126:127], v254 offset:20992\n\t" \
    "v_mfma_f32_16x16x32_bf16 v[84:87], v[128:131], v[12:15], v[84:87]\n\t" \
    "ds_read_b64_tr_b16 v[24:25], v253 offset:4096\n\t" \
    "v_mfma_f32_16x16x32_bf16 v[92:95], v[132:135], v[12:15], v[92:95]\n\t" \
    "ds_read_b64_tr_b16 v[26:27], v253 offset:4608\n\t" \
    "v_mfma_f32_16x16x32_bf16 v[100:103], v[136:139], v[12:15], v[100:103]\n\t" \
    "ds_read_b64_tr_b16 v[28:29], v254 offset:4096\n\t" \
    "v_mfma_f32_16x16x32_bf16 v[108:111], v[140:143], v[12:15], v[108:111]\n\t" \
    "ds_read_b64_tr_b16 v[30:31], v254 offset:4608\n\t" \
    "v_mfma_f32_16x16x32_bf16 v[192:195], v[16:19], v[128:131], v[192:195]\n\t" \
    "s_add_u32 s85, s80, 40960\n\t" \
    "s_cmp_lg_u32 s80, 81920\n\t" \
    "v_mfma_f32_16x16x32_bf16 v[200:203], v[16:19], v[132:135], v[200:203]\n\t" \
    "s_cselect_b32 s80, s85, 0\n\t" \
    "v_mfma_f32_16x16x32_bf16 v[208:211], v[16:19], v[136:139], v[208:211]\n\t" \
    "v_add_u32 v251, s80, v240\n\t" \
    "v_mfma_f32_16x16x32_bf16 v[216:219], v[16:19], v[140:143], v[216:219]\n\t" \
    "v_add_u32 v253, s80, v241\n\t" \
    "v_mfma_f32_16x16x32_bf16 v[196:199], v[20:23], v[128:131], v[196:199]\n\t" \
    "v_xor_b32 v254, 32, v253\n\t" \
    "v_mfma_f32_16x16x32_bf16 v[204:207], v[20:23], v[132:135], v[204:207]\n\t" \
    "v_mfma_f32_16x16x32_bf16 v[212:215], v[20:23], v[136:139], v[212:215]\n\t" \
    "v_mfma_f32_16x16x32_bf16 v[220:223], v[20:23], v[140:143], v[220:223]\n\t" \
    "s_waitcnt lgkmcnt(0)\n\t" \
    "s_barrier\n\t" \
    "v_mfma_f32_16x16x32_bf16 v[32:35], v[112:115], v[144:147], v[32:35]\n\t" \
    "ds_read_b128 v[128:131], v251\n\t" \
    "v_mfma_f32_16x16x32_bf16 v[40:43], v[112:115], v[148:151], v[40:43]\n\t" \
    "ds_read_b128 v[132:135], v251 offset:2048\n\t" \
    "v_mfma_f32_16x16x32_bf16 v[48:51], v[112:115], v[152:155], v[48:51]\n\t" \
    "ds_read_b128 v[136:139], v251 offset:4096\n\t" \
    "v_mfma_f32_16x16x32_bf16 v[56:59], v[112:115], v[156:159], v[56:59]\n\t" \
    "ds_read_b128 v[140:143], v251 offset:6144\n\t" \
    "v_mfma_f32_16x16x32_bf16 v[36:39], v[116:119], v[144:147], v[36:39]\n\t" \
    "ds_read_b64_tr_b16 v[0:1], v253 offset:8192\n\t" \
    "v_mfma_f32_16x16x32_bf16 v[44:47], v[116:119], v[148:151], v[44:47]\n\t" \
    "ds_read_b64_tr_b16 v[2:3], v253 offset:8704\n\t" \
    "v_mfma_f32_16x16x32_bf16 v[52:55], v[116:119], v[152:155], v[52:55]\n\t" \
    "ds_read_b64_tr_b16 v[4:5], v254 offset:8192\n\t" \
    "v_mfma_f32_16x16x32_bf16 v[60:63], v[116:119], v[156:159], v[60:63]\n\t" \
    "ds_read_b64_tr_b16 v[6:7], v254 offset:8704\n\t" \
    "v_mfma_f32_16x16x32_bf16 v[80:83], v[144:147], v[120:123], v[80:83]\n\t" \
    "ds_read_b64_tr_b16 v[8:9], v253 offset:16384\n\t" \
    "v_mfma_f32_16x16x32_bf16 v[88:91], v[148:151], v[120:123], v[88:91]\n\t" \
    "ds_read_b64_tr_b16 v[10:11], v253 offset:16896\n\t" \
    "v_mfma_f32_16x16x32_bf16 v[96:99], v[152:155], v[120:123], v[96:99]\n\t" \
    "ds_read_b64_tr_b16 v[12:13], v254 offset:16384\n\t" \
    "v_mfma_f32_16x16x32_bf16 v[104:107], v[156:159], v[120:123], v[104:107]\n\t" \
    "ds_read_b64_tr_b16 v[14:15], v254 offset:16896\n\t" \
    "v_mfma_f32_16x16x32_bf16 v[84:87], v[144:147], v[124:127], v[84:87]\n\t" \
    "ds_read_b64_tr_b16 v[16:17], v253\n\t" \
    "v_mfma_f32_16x16x32_bf16 v[92:95], v[148:151], v[124:127], v[92:95]\n\t" \
    "ds_read_b64_tr_b16 v[18:19], v253 offset:512\n\t" \
    "v_mfma_f32_16x16x32_bf16 v[100:103], v[152:155], v[124:127], v[100:103]\n\t" \
    "ds_read_b64_tr_b16 v[20:21], v254\n\t" \
    "v_mfma_f32_16x16x32_bf16 v[108:111], v[156:159], v[124:127], v[108:111]\n\t" \
    "ds_read_b64_tr_b16 v[22:23], v254 offset:512\n\t" \
    "v_mfma_f32_16x16x32_bf16 v[192:195], v[24:27], v[144:147], v[192:195]\n\t" \
    "v_xor_b32 v252, 64, v251\n\t" \
    "v_mfma_f32_16x16x32_bf16 v[200:203], v[24:27], v[148:151], v[200:203]\n\t" \
    "v_mfma_f32_16x16x32_bf16 v[208:211], v[24:27], v[152:155], v[208:211]\n\t" \
    "s_sub_u32 s81, s81, 1\n\t" \
    "v_mfma_f32_16x16x32_bf16 v[216:219], v[24:27], v[156:159], v[216:219]\n\t" \
    "s_cmp_lg_u32 s81, 0\n\t" \
    "v_mfma_f32_16x16x32_bf16 v[196:199], v[28:31], v[144:147], v[196:199]\n\t" \
    "v_mfma_f32_16x16x32_bf16 v[204:207], v[28:31], v[148:151], v[204:207]\n\t" \
    "v_mfma_f32_16x16x32_bf16 v[212:215], v[28:31], v[152:155], v[212:215]\n\t" \
    "v_mfma_f32_16x16x32_bf16 v[220:223], v[28:31], v[156:159], v[220:223]\n\t" \
    "s_cbranch_scc1 .Lab_l1\n\t" \
    ".Lab_l2:\n\t" \
    "s_waitcnt lgkmcnt(0)\n\t" \
    "v_mfma_f32_16x16x32_bf16 v[32:35], v[0:3], v[128:131], v[32:35]\n\t" \
    "ds_read_b128 v[144:147], v252\n\t" \
    "ds_read_b64_tr_b16 v[112:113], v253 offset:12288\n\t" \
    "v_mfma_f32_16x16x32_bf16 v[40:43], v[0:3], v[132:135], v[40:43]\n\t" \
    "ds_read_b128 v[148:151], v252 offset:2048\n\t" \
    "ds_read_b64_tr_b16 v[114:115], v253 offset:12800\n\t" \
    "v_mfma_f32_16x16x32_bf16 v[48:51], v[0:3], v[136:139], v[48:51]\n\t" \
    "ds_read_b128 v[152:155], v252 offset:4096\n\t" \
    "s_and_b32 s80, s84, 255\n\t" \
    "v_mfma_f32_16x16x32_bf16 v[56:59], v[0:3], v[140:143], v[56:59]\n\t" \
    "ds_read_b128 v[156:159], v252 offset:6144\n\t" \
    "s_and_b32 s82, s83, 255\n\t" \
    "v_mfma_f32_16x16x32_bf16 v[36:39], v[4:7], v[128:131], v[36:39]\n\t" \
    "ds_read_b64_tr_b16 v[116:117], v254 offset:12288\n\t" \
    "s_lshl_b32 s80, s80, 12\n\t" \
    "v_mfma_f32_16x16x32_bf16 v[44:47], v[4:7], v[132:135], v[44:47]\n\t" \
    "ds_read_b64_tr_b16 v[118:119], v254 offset:12800\n\t" \
    "s_lshl_b32 s82, s82, 12\n\t" \
    "v_mfma_f32_16x16x32_bf16 v[52:55], v[4:7], v[136:139], v[52:55]\n\t" \
    "ds_read_b64_tr_b16 v[120:121], v253 offset:20480\n\t" \
    "v_mfma_f32_16x16x32_bf16 v[60:63], v[4:7], v[140:143], v[60:63]\n\t" \
    "ds_read_b64_tr_b16 v[122:123], v253 offset:20992\n\t" \
    "v_mfma_f32_16x16x32_bf16 v[80:83], v[128:131], v[8:11], v[80:83]\n\t" \
    "ds_read_b64_tr_b16 v[124:125], v254 offset:20480\n\t" \
    "v_mfma_f32_16x16x32_bf16 v[88:91], v[132:135], v[8:11], v[88:91]\n\t" \
    "ds_read_b64_tr_b16 v[126:127], v254 offset:20992\n\t" \
    "v_mfma_f32_16x16x32_bf16 v[96:99], v[136:139], v[8:11], v[96:99]\n\t" \
    "v_add_u32 v251, s82, v240\n\t" \
    "v_mfma_f32_16x16x32_bf16 v[104:107], v[140:143], v[8:11], v[104:107]\n\t" \
    "v_add_u32 v253, s80, v241\n\t" \
    "v_mfma_f32_16x16x32_bf16 v[84:87], v[128:131], v[12:15], v[84:87]\n\t" \
    "v_xor_b32 v254, 32, v253\n\t" \
    "v_mfma_f32_16x16x32_bf16 v[92:95], v[132:135], v[12:15], v[92:95]\n\t" \
    "v_mfma_f32_16x16x32_bf16 v[100:103], v[136:139], v[12:15], v[100:103]\n\t" \
    "v_mfma_f32_16x16x32_bf16 v[108:111], v[140:143], v[12:15], v[108:111]\n\t" \
    "s_waitcnt lgkmcnt(0)\n\t" \
    "s_barrier\n\t" \
    "v_mfma_f32_16x16x32_bf16 v[32:35], v[112:115], v[144:147], v[32:35]\n\t" \
    "ds_read_b128 v[128:131], v251\n\t" \
    "ds_read_b64_tr_b16 v[0:1], v253 offset:8192\n\t" \
    "v_mfma_f32_16x16x32_bf16 v[40:43], v[112:115], v[148:151], v[40:43]\n\t" \
    "ds_read_b128 v[132:135], v251 offset:2048\n\t" \
    "ds_read_b64_tr_b16 v[2:3], v253 offset:8704\n\t" \
    "v_mfma_f32_16x16x32_bf16 v[48:51], v[112:115], v[152:155], v[48:51]\n\t" \
    "ds_read_b128 v[136:139], v251 offset:4096\n\t" \
    "v_mfma_f32_16x16x32_bf16 v[56:59], v[112:115], v[156:159], v[56:59]\n\t" \
    "ds_read_b128 v[140:143], v251 offset:6144\n\t" \
    "v_mfma_f32_16x16x32_bf16 v[36:39], v[116:119], v[144:147], v[36:39]\n\t" \
    "ds_read_b64_tr_b16 v[4:5], v254 offset:8192\n\t" \
    "v_mfma_f32_16x16x32_bf16 v[44:47], v[116:119], v[148:151], v[44:47]\n\t" \
    "ds_read_b64_tr_b16 v[6:7], v254 offset:8704\n\t" \
    "v_mfma_f32_16x16x32_bf16 v[52:55], v[116:119], v[152:155], v[52:55]\n\t" \
    "ds_read_b64_tr_b16 v[8:9], v253 offset:16384\n\t" \
    "v_mfma_f32_16x16x32_bf16 v[60:63], v[116:119], v[156:159], v[60:63]\n\t" \
    "ds_read_b64_tr_b16 v[10:11], v253 offset:16896\n\t" \
    "v_mfma_f32_16x16x32_bf16 v[80:83], v[144:147], v[120:123], v[80:83]\n\t" \
    "ds_read_b64_tr_b16 v[12:13], v254 offset:16384\n\t" \
    "v_mfma_f32_16x16x32_bf16 v[88:91], v[148:151], v[120:123], v[88:91]\n\t" \
    "ds_read_b64_tr_b16 v[14:15], v254 offset:16896\n\t" \
    "v_mfma_f32_16x16x32_bf16 v[96:99], v[152:155], v[120:123], v[96:99]\n\t" \
    "v_xor_b32 v252, 64, v251\n\t" \
    "v_mfma_f32_16x16x32_bf16 v[104:107], v[156:159], v[120:123], v[104:107]\n\t" \
    "s_lshr_b32 s83, s83, 8\n\t" \
    "v_mfma_f32_16x16x32_bf16 v[84:87], v[144:147], v[124:127], v[84:87]\n\t" \
    "s_lshr_b32 s84, s84, 8\n\t" \
    "v_mfma_f32_16x16x32_bf16 v[92:95], v[148:151], v[124:127], v[92:95]\n\t" \
    "v_mfma_f32_16x16x32_bf16 v[100:103], v[152:155], v[124:127], v[100:103]\n\t" \
    "v_mfma_f32_16x16x32_bf16 v[108:111], v[156:159], v[124:127], v[108:111]\n\t" \
    "s_cbranch_scc1 .Lab_l2\n\t" \
    "s_waitcnt lgkmcnt(0)\n\t" \
    "v_mfma_f32_16x16x32_bf16 v[32:35], v[0:3], v[128:131], v[32:35]\n\t" \
    "ds_read_b128 v[144:147], v252\n\t" \
    "ds_read_b64_tr_b16 v[112:113], v253 offset:12288\n\t" \
    "v_mfma_f32_16x16x32_bf16 v[40:43], v[0:3], v[132:135], v[40:43]\n\t" \
    "ds_read_b128 v[148:151], v252 offset:2048\n\t" \
    "ds_read_b64_tr_b16 v[114:115], v253 offset:12800\n\t" \
    "v_mfma_f32_16x16x32_bf16 v[48:51], v[0:3], v[136:139], v[48:51]\n\t" \
    "ds_read_b128 v[152:155], v252 offset:4096\n\t" \
    "ds_read_b64_tr_b16 v[116:117], v254 offset:12288\n\t" \
    "v_mfma_f32_16x16x32_bf16 v[56:59], v[0:3], v[140:143], v[56:59]\n\t" \
    "ds_read_b128 v[156:159], v252 offset:6144\n\t" \
    "ds_read_b64_tr_b16 v[118:119], v254 offset:12800\n\t" \
    "v_mfma_f32_16x16x32_bf16 v[36:39], v[4:7], v[128:131], v[36:39]\n\t" \
    "ds_read_b64_tr_b16 v[120:121], v253 offset:20480\n\t" \
    "ds_read_b64_tr_b16 v[122:123], v253 offset:20992\n\t" \
    "v_mfma_f32_16x16x32_bf16 v[44:47], v[4:7], v[132:135], v[44:47]\n\t" \
    "ds_read_b64_tr_b16 v[124:125], v254 offset:20480\n\t" \
    "ds_read_b64_tr_b16 v[126:127], v254 offset:20992\n\t" \
    "v_mfma_f32_16x16x32_bf16 v[52:55], v[4:7], v[136:139], v[52:55]\n\t" \
    "ds_read_b64_tr_b16 v[224:225], v245\n\t" \
    "ds_read_b64_tr_b16 v[226:227], v245 offset:1024\n\t" \
    "v_mfma_f32_16x16x32_bf16 v[60:63], v[4:7], v[140:143], v[60:63]\n\t" \
    "v_xor_b32 v245, 32, v245\n\t" \
    "ds_read_b32 v236, v249 offset:288\n\t" \
    "v_mfma_f32_16x16x32_bf16 v[80:83], v[128:131], v[8:11], v[80:83]\n\t" \
    "ds_read_b64_tr_b16 v[228:229], v245\n\t" \
    "ds_read_b64_tr_b16 v[230:231], v245 offset:1024\n\t" \
    "v_mfma_f32_16x16x32_bf16 v[88:91], v[132:135], v[8:11], v[88:91]\n\t" \
    "ds_read_b128 v[68:71], v249 offset:320\n\t" \
    "ds_read_b64 v[64:65], v249 offset:0\n\t" \
    "v_mfma_f32_16x16x32_bf16 v[96:99], v[136:139], v[8:11], v[96:99]\n\t" \
    "v_add_u32 v251, 131072, v240\n\t" \
    "v_mfma_f32_16x16x32_bf16 v[104:107], v[140:143], v[8:11], v[104:107]\n\t" \
    "v_xor_b32 v254, 32, v241\n\t" \
    "v_mfma_f32_16x16x32_bf16 v[84:87], v[128:131], v[12:15], v[84:87]\n\t" \
    "v_mov_b32 v253, v241\n\t" \
    "v_mfma_f32_16x16x32_bf16 v[92:95], v[132:135], v[12:15], v[92:95]\n\t" \
    "v_xor_b32 v252, 64, v251\n\t" \
    "v_mfma_f32_16x16x32_bf16 v[100:103], v[136:139], v[12:15], v[100:103]\n\t" \
    "v_mfma_f32_16x16x32_bf16 v[108:111], v[140:143], v[12:15], v[108:111]\n\t" \
    "s_waitcnt lgkmcnt(0)\n\t" \
    "v_mfma_f32_16x16x32_bf16 v[32:35], v[112:115], v[144:147], v[32:35]\n\t" \
    "v_min_u32 v64, v64, v65\n\t" \
    "v_mfma_f32_16x16x32_bf16 v[40:43], v[112:115], v[148:151], v[40:43]\n\t" \
    "v_readfirstlane_b32 s85, v64\n\t" \
    "s_cmp_lt_u32 s85, 1\n\t" \
    "s_cbranch_scc1 .Lab_s1\n\t" \
    ".Lab_s1r:\n\t" \
    "v_mfma_f32_16x16x32_bf16 v[48:51], v[112:115], v[152:155], v[48:51]\n\t" \
    "ds_read_b128 v[160:163], v251\n\t" \
    "ds_read_b64_tr_b16 v[0:1], v253 offset:8192\n\t" \
    "v_mfma_f32_16x16x32_bf16 v[56:59], v[112:115], v[156:159], v[56:59]\n\t" \
    "ds_read_b128 v[164:167], v251 offset:2048\n\t" \
    "ds_read_b64_tr_b16 v[2:3], v253 offset:8704\n\t" \
    "v_mfma_f32_16x16x32_bf16 v[36:39], v[116:119], v[144:147], v[36:39]\n\t" \
    "ds_read_b128 v[168:171], v251 offset:4096\n\t" \
    "ds_read_b64_tr_b16 v[4:5], v254 offset:8192\n\t" \
    "v_mfma_f32_16x16x32_bf16 v[44:47], v[116:119], v[148:151], v[44:47]\n\t" \
    "ds_read_b128 v[172:175], v251 offset:6144\n\t" \
    "ds_read_b64_tr_b16 v[6:7], v254 offset:8704\n\t" \
    "v_mfma_f32_16x16x32_bf16 v[52:55], v[116:119], v[152:155], v[52:55]\n\t" \
    "ds_read_b64_tr_b16 v[8:9], v253 offset:16384\n\t" \
    "ds_read_b64_tr_b16 v[10:11], v253 offset:16896\n\t" \
    "v_mfma_f32_16x16x32_bf16 v[60:63], v[116:119], v[156:159], v[60:63]\n\t" \
    "ds_read_b64_tr_b16 v[12:13], v254 offset:16384\n\t" \
    "ds_read_b64_tr_b16 v[14:15], v254 offset:16896\n\t" \
    "v_mfma_f32_16x16x32_bf16 v[80:83], v[144:147], v[120:123], v[80:83]\n\t" \
    "v_readfirstlane_b32 s87, v68\n\t" \
    "v_mfma_f32_16x16x32_bf16 v[88:91], v[148:151], v[120:123], v[88:91]\n\t" \
    "v_readfirstlane_b32 s88, v69\n\t" \
    "v_mfma_f32_16x16x32_bf16 v[96:99], v[152:155], v[120:123], v[96:99]\n\t" \
    "v_readfirstlane_b32 %[sof], v70\n\t" \
    "v_mfma_f32_16x16x32_bf16 v[104:107], v[156:159], v[120:123], v[104:107]\n\t" \
    "s_xor_b32 s87, s87, %[kpx]\n\t" \
    "s_add_u32 s88, s88, %[kma]\n\t" \
    "v_mfma_f32_16x16x32_bf16 v[84:87], v[144:147], v[124:127], v[84:87]\n\t" \
    "s_add_u32 s87, s87, %[kpa]\n\t" \
    "v_mfma_f32_16x16x32_bf16 v[92:95], v[148:151], v[124:127], v[92:95]\n\t" \
    "s_xor_b32 s86, s87, 0x8000\n\t" \
    "v_mfma_f32_16x16x32_bf16 v[100:103], v[152:155], v[124:127], v[100:103]\n\t" \
    "v_mfma_f32_16x16x32_bf16 v[108:111], v[156:159], v[124:127], v[108:111]\n\t" \
    "s_waitcnt lgkmcnt(0)\n\t" \
    "v_mfma_f32_16x16x32_bf16 v[32:35], v[0:3], v[160:163], v[32:35]\n\t" \
    "ds_read_b128 v[176:179], v252\n\t" \
    "ds_read_b64_tr_b16 v[112:113], v253 offset:12288\n\t" \
    "v_mfma_f32_16x16x32_bf16 v[40:43], v[0:3], v[164:167], v[40:43]\n\t" \
    "ds_read_b128 v[180:183], v252 offset:2048\n\t" \
    "ds_read_b64_tr_b16 v[114:115], v253 offset:12800\n\t" \
    "v_mfma_f32_16x16x32_bf16 v[48:51], v[0:3], v[168:171], v[48:51]\n\t" \
    "ds_read_b128 v[184:187], v252 offset:4096\n\t" \
    "v_mov_b32 v237, 2\n\t" \
    "v_mfma_f32_16x16x32_bf16 v[56:59], v[0:3], v[172:175], v[56:59]\n\t" \
    "ds_read_b128 v[188:191], v252 offset:6144\n\t" \
    "v_mfma_f32_16x16x32_bf16 v[36:39], v[4:7], v[160:163], v[36:39]\n\t" \
    "ds_read_b64_tr_b16 v[116:117], v254 offset:12288\n\t" \
    "v_mfma_f32_16x16x32_bf16 v[44:47], v[4:7], v[164:167], v[44:47]\n\t" \
    "ds_read_b64_tr_b16 v[118:119], v254 offset:12800\n\t" \
    "v_mfma_f32_16x16x32_bf16 v[52:55], v[4:7], v[168:171], v[52:55]\n\t" \
    "ds_read_b64_tr_b16 v[120:121], v253 offset:20480\n\t" \
    "v_mfma_f32_16x16x32_bf16 v[60:63], v[4:7], v[172:175], v[60:63]\n\t" \
    "ds_read_b64_tr_b16 v[122:123], v253 offset:20992\n\t" \
    "v_mfma_f32_16x16x32_bf16 v[80:83], v[160:163], v[8:11], v[80:83]\n\t" \
    "ds_read_b64_tr_b16 v[124:125], v254 offset:20480\n\t" \
    "v_mfma_f32_16x16x32_bf16 v[88:91], v[164:167], v[8:11], v[88:91]\n\t" \
    "ds_read_b64_tr_b16 v[126:127], v254 offset:20992\n\t" \
    "v_mfma_f32_16x16x32_bf16 v[96:99], v[168:171], v[8:11], v[96:99]\n\t" \
    "ds_write_b32 v248, v237\n\t" \
    "v_mfma_f32_16x16x32_bf16 v[104:107], v[172:175], v[8:11], v[104:107]\n\t" \
    "v_xor_b32 v251, 32, v243\n\t" \
    "v_mfma_f32_16x16x32_bf16 v[84:87], v[160:163], v[12:15], v[84:87]\n\t" \
    "v_xor_b32 v252, 64, v243\n\t" \
    "v_mfma_f32_16x16x32_bf16 v[92:95], v[164:167], v[12:15], v[92:95]\n\t" \
    "v_xor_b32 v253, 0x60, v243\n\t" \
    "v_mfma_f32_16x16x32_bf16 v[100:103], v[168:171], v[12:15], v[100:103]\n\t" \
    "s_setprio 3\n\t" \
    "v_mfma_f32_16x16x32_bf16 v[108:111], v[172:175], v[12:15], v[108:111]\n\t" \
    "s_waitcnt lgkmcnt(0)\n\t" \
    "v_mfma_f32_16x16x32_bf16 v[32:35], v[112:115], v[176:179], v[32:35]\n\t" \
    "v_mfma_f32_16x16x32_bf16 v[36:39], v[116:119], v[176:179], v[36:39]\n\t" \
    "v_mfma_f32_16x16x32_bf16 v[40:43], v[112:115], v[180:183], v[40:43]\n\t" \
    "v_mfma_f32_16x16x32_bf16 v[44:47], v[116:119], v[180:183], v[44:47]\n\t" \
    "ds_read_b128 v[28:31], v249 offset:16\n\t" \
    "v_mfma_f32_16x16x32_bf16 v[48:51], v[112:115], v[184:187], v[48:51]\n\t" \
    "v_mfma_f32_16x16x32_bf16 v[52:55], v[116:119], v[184:187], v[52:55]\n\t" \
    "v_mfma_f32_16x16x32_bf16 v[56:59], v[112:115], v[188:191], v[56:59]\n\t" \
    "v_mfma_f32_16x16x32_bf16 v[60:63], v[116:119], v[188:191], v[60:63]\n\t" \
    "v_cvt_pk_bf16_f32 v64, v32, v33\n\t" \
    "v_cvt_pk_bf16_f32 v65, v34, v35\n\t" \
    "v_mfma_f32_16x16x32_bf16 v[80:83], v[176:179], v[120:123], v[80:83]\n\t" \
    "v_cvt_pk_bf16_f32 v66, v36, v37\n\t" \
    "v_cvt_pk_bf16_f32 v67, v38, v39\n\t" \
    "v_mfma_f32_16x16x32_bf16 v[88:91], v[180:183], v[120:123], v[88:91]\n\t" \
    "v_cvt_pk_bf16_f32 v68, v40, v41\n\t" \
    "v_cvt_pk_bf16_f32 v69, v42, v43\n\t" \
    "v_mfma_f32_16x16x32_bf16 v[84:87], v[176:179], v[124:127], v[84:87]\n\t" \
    "v_cvt_pk_bf16_f32 v70, v44, v45\n\t" \
    "v_cvt_pk_bf16_f32 v71, v46, v47\n\t" \
    "v_mfma_f32_16x16x32_bf16 v[92:95], v[180:183], v[124:127], v[92:95]\n\t" \
    "v_cvt_pk_bf16_f32 v72, v48, v49\n\t" \
    "v_cvt_pk_bf16_f32 v73, v50, v51\n\t" \
    "v_mfma_f32_16x16x32_bf16 v[96:99], v[184:187], v[120:123], v[96:99]\n\t" \
    "v_cvt_pk_bf16_f32 v74, v52, v53\n\t" \
    "v_cvt_pk_bf16_f32 v75, v54, v55\n\t" \
    "v_mfma_f32_16x16x32_bf16 v[104:107], v[188:191], v[120:123], v[104:107]\n\t" \
    "v_cvt_pk_bf16_f32 v76, v56, v57\n\t" \
    "v_cvt_pk_bf16_f32 v77, v58, v59\n\t" \
    "v_mfma_f32_16x16x32_bf16 v[100:103], v[184:187], v[124:127], v[100:103]\n\t" \
    "v_cvt_pk_bf16_f32 v78, v60, v61\n\t" \
    "v_cvt_pk_bf16_f32 v79, v62, v63\n\t" \
    "v_mfma_f32_16x16x32_bf16 v[108:111], v[188:191], v[124:127], v[108:111]\n\t" \
    "s_waitcnt lgkmcnt(0)\n\t" \
    "v_min_u32 v238, v28, v29\n\t" \
    "v_min3_u32 v238, v238, v30, v31\n\t" \
    "v_mfma_f32_16x16x32_bf16 v[0:3], v[224:227], v[64:67], 0\n\t" \
    "v_cvt_pk_bf16_f32 v112, v80, v81\n\t" \
    "v_cvt_pk_bf16_f32 v113, v82, v83\n\t" \
    "v_mfma_f32_16x16x32_bf16 v[4:7], v[228:231], v[64:67], 0\n\t" \
    "v_cvt_pk_bf16_f32 v114, v88, v89\n\t" \
    "v_cvt_pk_bf16_f32 v115, v90, v91\n\t" \
    "v_mfma_f32_16x16x32_bf16 v[8:11], v[224:227], v[68:71], 0\n\t" \
    "v_cvt_pk_bf16_f32 v116, v84, v85\n\t" \
    "v_cvt_pk_bf16_f32 v117, v86, v87\n\t" \
    "v_mfma_f32_16x16x32_bf16 v[12:15], v[228:231], v[68:71], 0\n\t" \
    "v_cvt_pk_bf16_f32 v118, v92, v93\n\t" \
    "v_cvt_pk_bf16_f32 v119, v94, v95\n\t" \
    "v_mfma_f32_16x16x32_bf16 v[16:19], v[224:227], v[72:75], 0\n\t" \
    "v_cvt_pk_bf16_f32 v120, v96, v97\n\t" \
    "v_cvt_pk_bf16_f32 v121, v98, v99\n\t" \
    "v_mfma_f32_16x16x32_bf16 v[20:23], v[228:231], v[72:75], 0\n\t" \
    "v_cvt_pk_bf16_f32 v122, v104, v105\n\t" \
    "v_cvt_pk_bf16_f32 v123, v106, v107\n\t" \
    "v_mfma_f32_16x16x32_bf16 v[24:27], v[224:227], v[76:79], 0\n\t" \
    "v_cvt_pk_bf16_f32 v124, v100, v101\n\t" \
    "v_cvt_pk_bf16_f32 v125, v102, v103\n\t" \
    "v_mfma_f32_16x16x32_bf16 v[28:31], v[228:231], v[76:79], 0\n\t" \
    "v_cvt_pk_bf16_f32 v126, v108, v109\n\t" \
    "v_cvt_pk_bf16_f32 v127, v110, v111\n\t" \
    "v_readfirstlane_b32 s85, v238\n\t" \
    "s_cmp_lt_u32 s85, 2\n\t" \
    "s_cbranch_scc1 .Lab_s2\n\t" \
    ".Lab_s2r:\n\t" \
    "ds_write_b128 v242, v[112:115]\n\t" \
    "v_max3_f32 v64, v0, v1, v2\n\t" \
    "v_max3_f32 v66, -v0, -v1, -v2\n\t" \
    "v_max3_f32 v68, v8, v9, v10\n\t" \
    "v_max3_f32 v64, v64, v3, v4\n\t" \
    "v_max3_f32 v66, v66, -v3, -v4\n\t" \
    "v_max3_f32 v68, v68, v11, v12\n\t" \
    "v_max3_f32 v64, v64, v5, v6\n\t" \
    "v_max3_f32 v66, v66, -v5, -v6\n\t" \
    "ds_write_b128 v242, v[116:119] offset:1024\n\t" \
    "v_max3_f32 v68, v68, v13, v14\n\t" \
    "v_max_f32_e64 v64, v64, v7\n\t" \
    "v_max_f32_e64 v66, v66, -v7\n\t" \
    "v_max_f32_e64 v68, v68, v15\n\t" \
    "v_max3_f32 v70, -v8, -v9, -v10\n\t" \
    "v_permlane32_swap_b32 v64, v66\n\t" \
    "v_max3_f32 v72, v16, v17, v18\n\t" \
    "v_max3_f32 v70, v70, -v11, -v12\n\t" \
    "ds_write_b128 v242, v[120:123] offset:4096\n\t" \
    "v_max_f32 v64, v64, v66\n\t" \
    "v_max3_f32 v72, v72, v19, v20\n\t" \
    "v_max3_f32 v70, v70, -v13, -v14\n\t" \
    "v_max3_f32 v74, -v16, -v17, -v18\n\t" \
    "v_max3_f32 v72, v72, v21, v22\n\t" \
    "v_max_f32_e64 v70, v70, -v15\n\t" \
    "v_max3_f32 v74, v74, -v19, -v20\n\t" \
    "v_max_f32_e64 v72, v72, v23\n\t" \
    "ds_write_b128 v242, v[124:127] offset:5120\n\t" \
    "v_permlane32_swap_b32 v68, v70\n\t" \
    "v_max3_f32 v74, v74, -v21, -v22\n\t" \
    "v_max3_f32 v76, v24, v25, v26\n\t" \
    "v_max_f32 v68, v68, v70\n\t" \
    "v_max_f32_e64 v74, v74, -v23\n\t" \
    "v_max3_f32 v76, v76, v27, v28\n\t" \
    "v_permlane16_swap_b32 v64, v68\n\t" \
    "v_permlane32_swap_b32 v72, v74\n\t" \
    "v_max3_f32 v76, v76, v29, v30\n\t" \
    "v_max_f32 v66, v64, v68\n\t" \
    "v_max_f32 v72, v72, v74\n\t" \
    "v_max_f32_e64 v76, v76, v31\n\t" \
    "v_xor_b32 v254, 32, v244\n\t" \
    "v_mul_f32 v64, 0xbfb8aa3b, v66\n\t" \
    "v_mul_f32 v68, 0xbfb8aa3b, v66\n\t" \
    "v_xor_b32 v245, 0x2000, v244\n\t" \
    "v_max3_f32 v78, -v24, -v25, -v26\n\t" \
    "v_max3_f32 v78, v78, -v27, -v28\n\t" \
    "v_xor_b32 v255, 0x2020, v244\n\t" \
    "v_permlane16_swap_b32 v64, v68\n\t" \
    "v_mov_b32 v66, v64\n\t" \
    "v_xor_b32 v242, 0x2000, v242\n\t" \
    "v_max3_f32 v78, v78, -v29, -v30\n\t" \
    "v_mov_b32 v70, v68\n\t" \
    "v_permlane32_swap_b32 v64, v66\n\t" \
    "v_max_f32_e64 v78, v78, -v31\n\t" \
    "v_permlane32_swap_b32 v68, v70\n\t" \
    "v_pk_fma_f32 v[32:33], v[0:1], s[90:91], v[64:65] op_sel_hi:[1,1,0]\n\t" \
    "v_permlane32_swap_b32 v76, v78\n\t" \
    "v_pk_fma_f32 v[0:1], v[0:1], s[90:91], v[66:67] op_sel_hi:[1,1,0] neg_lo:[1,0,0] neg_hi:[1,0,0]\n\t" \
    "v_pk_fma_f32 v[34:35], v[2:3], s[90:91], v[64:65] op_sel_hi:[1,1,0]\n\t" \
    "v_max_f32 v76, v76, v78\n\t" \
    "v_pk_fma_f32 v[2:3], v[2:3], s[90:91], v[66:67] op_sel_hi:[1,1,0] neg_lo:[1,0,0] neg_hi:[1,0,0]\n\t" \
    "v_pk_fma_f32 v[36:37], v[4:5], s[90:91], v[64:65] op_sel_hi:[1,1,0]\n\t" \
    "v_permlane16_swap_b32 v72, v76\n\t" \
    "v_pk_fma_f32 v[4:5], v[4:5], s[90:91], v[66:67] op_sel_hi:[1,1,0] neg_lo:[1,0,0] neg_hi:[1,0,0]\n\t" \
    "v_pk_fma_f32 v[38:39], v[6:7], s[90:91], v[64:65] op_sel_hi:[1,1,0]\n\t" \
    "v_max_f32 v74, v72, v76\n\t" \
    "v_pk_fma_f32 v[6:7], v[6:7], s[90:91], v[66:67] op_sel_hi:[1,1,0] neg_lo:[1,0,0] neg_hi:[1,0,0]\n\t" \
    "v_exp_f32 v32, v32\n\t" \
    "v_mul_f32 v72, 0xbfb8aa3b, v74\n\t" \
    "v_mul_f32 v76, 0xbfb8aa3b, v74\n\t" \
    "v_exp_f32 v0, v0\n\t" \
    "v_exp_f32 v33, v33\n\t" \
    "v_permlane16_swap_b32 v72, v76\n\t" \
    "v_exp_f32 v1, v1\n\t" \
    "v_exp_f32 v34, v34\n\t" \
    "v_mov_b32 v74, v72\n\t" \
    "v_mov_b32 v78, v76\n\t" \
    "v_exp_f32 v2, v2\n\t" \
    "v_permlane32_swap_b32 v72, v74\n\t" \
    "v_permlane32_swap_b32 v76, v78\n\t" \
    "v_exp_f32 v35, v35\n\t" \
    "v_exp_f32 v3, v3\n\t" \
    "v_exp_f32 v36, v36\n\t" \
    "v_pk_add_f32 v[64:65], v[32:33], v[34:35]\n\t" \
    "v_pk_add_f32 v[66:67], v[0:1], v[2:3]\n\t" \
    "v_exp_f32 v4, v4\n\t" \
    "v_exp_f32 v37, v37\n\t" \
    "v_exp_f32 v5, v5\n\t" \
    "v_exp_f32 v38, v38\n\t" \
    "v_pk_add_f32 v[64:65], v[64:65], v[36:37]\n\t" \
    "v_pk_add_f32 v[66:67], v[66:67], v[4:5]\n\t" \
    "v_exp_f32 v6, v6\n\t" \
    "v_exp_f32 v39, v39\n\t" \
    "v_exp_f32 v7, v7\n\t" \
    "v_pk_fma_f32 v[40:41], v[8:9], s[90:91], v[68:69] op_sel_hi:[1,1,0]\n\t" \
    "v_pk_add_f32 v[64:65], v[64:65], v[38:39]\n\t" \
    "v_pk_add_f32 v[66:67], v[66:67], v[6:7]\n\t" \
    "v_pk_fma_f32 v[8:9], v[8:9], s[90:91], v[70:71] op_sel_hi:[1,1,0] neg_lo:[1,0,0] neg_hi:[1,0,0]\n\t" \
    "v_add_f32 v64, v64, v65\n\t" \
    "v_add_f32 v66, v66, v67\n\t" \
    "v_pk_fma_f32 v[42:43], v[10:11], s[90:91], v[68:69] op_sel_hi:[1,1,0]\n\t" \
    "v_pk_fma_f32 v[10:11], v[10:11], s[90:91], v[70:71] op_sel_hi:[1,1,0] neg_lo:[1,0,0] neg_hi:[1,0,0]\n\t" \
    "v_permlane32_swap_b32 v64, v66\n\t" \
    "v_pk_fma_f32 v[44:45], v[12:13], s[90:91], v[68:69] op_sel_hi:[1,1,0]\n\t" \
    "v_pk_fma_f32 v[12:13], v[12:13], s[90:91], v[70:71] op_sel_hi:[1,1,0] neg_lo:[1,0,0] neg_hi:[1,0,0]\n\t" \
    "v_add_f32 v64, v64, v66\n\t" \
    "v_pk_fma_f32 v[46:47], v[14:15], s[90:91], v[68:69] op_sel_hi:[1,1,0]\n\t" \
    "v_pk_fma_f32 v[14:15], v[14:15], s[90:91], v[70:71] op_sel_hi:[1,1,0] neg_lo:[1,0,0] neg_hi:[1,0,0]\n\t" \
    "v_exp_f32 v40, v40\n\t" \
    "v_exp_f32 v8, v8\n\t" \
    "v_exp_f32 v41, v41\n\t" \
    "v_exp_f32 v9, v9\n\t" \
    "v_exp_f32 v42, v42\n\t" \
    "v_exp_f32 v10, v10\n\t" \
    "v_exp_f32 v43, v43\n\t" \
    "v_exp_f32 v11, v11\n\t" \
    "v_exp_f32 v44, v44\n\t" \
    "v_pk_add_f32 v[68:69], v[40:41], v[42:43]\n\t" \
    "v_pk_add_f32 v[70:71], v[8:9], v[10:11]\n\t" \
    "v_exp_f32 v12, v12\n\t" \
    "v_exp_f32 v45, v45\n\t" \
    "v_exp_f32 v13, v13\n\t" \
    "v_exp_f32 v46, v46\n\t" \
    "v_pk_add_f32 v[68:69], v[68:69], v[44:45]\n\t" \
    "v_pk_add_f32 v[70:71], v[70:71], v[12:13]\n\t" \
    "v_exp_f32 v14, v14\n\t" \
    "v_exp_f32 v47, v47\n\t" \
    "v_exp_f32 v15, v15\n\t" \
    "v_pk_fma_f32 v[48:49], v[16:17], s[90:91], v[72:73] op_sel_hi:[1,1,0]\n\t" \
    "v_pk_add_f32 v[68:69], v[68:69], v[46:47]\n\t" \
    "v_pk_add_f32 v[70:71], v[70:71], v[14:15]\n\t" \
    "v_pk_fma_f32 v[16:17], v[16:17], s[90:91], v[74:75] op_sel_hi:[1,1,0] neg_lo:[1,0,0] neg_hi:[1,0,0]\n\t" \
    "v_add_f32 v68, v68, v69\n\t" \
    "v_add_f32 v70, v70, v71\n\t" \
    "v_pk_fma_f32 v[50:51], v[18:19], s[90:91], v[72:73] op_sel_hi:[1,1,0]\n\t" \
    "v_pk_fma_f32 v[18:19], v[18:19], s[90:91], v[74:75] op_sel_hi:[1,1,0] neg_lo:[1,0,0] neg_hi:[1,0,0]\n\t" \
    "v_permlane32_swap_b32 v68, v70\n\t" \
    "v_pk_fma_f32 v[52:53], v[20:21], s[90:91], v[72:73] op_sel_hi:[1,1,0]\n\t" \
    ".Lpad_ab1:\n\t" \
    "s_cbranch_execz .Lpad_ret\n\t" \
    "v_pk_fma_f32 v[20:21], v[20:21], s[90:91], v[74:75] op_sel_hi:[1,1,0] neg_lo:[1,0,0] neg_hi:[1,0,0]\n\t" \
    "v_add_f32 v68, v68, v70\n\t" \
    "v_pk_fma_f32 v[54:55], v[22:23], s[90:91], v[72:73] op_sel_hi:[1,1,0]\n\t" \
    "v_pk_fma_f32 v[22:23], v[22:23], s[90:91], v[74:75] op_sel_hi:[1,1,0] neg_lo:[1,0,0] neg_hi:[1,0,0]\n\t" \
    "v_permlane16_swap_b32 v64, v68\n\t" \
    "v_exp_f32 v48, v48\n\t" \
    "v_exp_f32 v16, v16\n\t" \
    "v_add_f32 v66, v64, v68\n\t" \
    "v_exp_f32 v49, v49\n\t" \
    "v_exp_f32 v17, v17\n\t" \
    "v_rcp_f32 v64, v66\n\t" \
    "v_exp_f32 v50, v50\n\t" \
    "v_exp_f32 v18, v18\n\t" \
    "v_mov_b32 v68, v64\n\t" \
    "v_exp_f32 v51, v51\n\t" \
    "v_exp_f32 v19, v19\n\t" \
    "v_permlane16_swap_b32 v64, v68\n\t" \
    "v_pk_add_f32 v[72:73], v[48:49], v[50:51]\n\t" \
    "v_pk_add_f32 v[74:75], v[16:17], v[18:19]\n\t" \
    "v_mov_b32 v66, v64\n\t" \
    "v_mov_b32 v70, v68\n\t" \
    "v_exp_f32 v52, v52\n\t" \
    "v_permlane32_swap_b32 v64, v66\n\t" \
    "v_permlane32_swap_b32 v68, v70\n\t" \
    "v_exp_f32 v20, v20\n\t" \
    "v_pk_mul_f32 v[0:1], v[0:1], v[66:67] op_sel_hi:[1,0]\n\t" \
    "v_pk_mul_f32 v[32:33], v[32:33], v[64:65] op_sel_hi:[1,0]\n\t" \
    "v_pk_mul_f32 v[2:3], v[2:3], v[66:67] op_sel_hi:[1,0]\n\t" \
    "v_pk_mul_f32 v[34:35], v[34:35], v[64:65] op_sel_hi:[1,0]\n\t" \
    "v_pk_mul_f32 v[4:5], v[4:5], v[66:67] op_sel_hi:[1,0]\n\t" \
    "v_pk_mul_f32 v[36:37], v[36:37], v[64:65] op_sel_hi:[1,0]\n\t" \
    "v_pk_mul_f32 v[6:7], v[6:7], v[66:67] op_sel_hi:[1,0]\n\t" \
    "v_pk_mul_f32 v[38:39], v[38:39], v[64:65] op_sel_hi:[1,0]\n\t" \
    "v_exp_f32 v53, v53\n\t" \
    "v_cvt_pk_bf16_f32 v7, v6, v7\n\t" \
    "v_cvt_pk_bf16_f32 v6, v4, v5\n\t" \
    "v_cvt_pk_bf16_f32 v5, v2, v3\n\t" \
    "v_cvt_pk_bf16_f32 v4, v0, v1\n\t" \
    "ds_write_b64 v253, v[6:7] offset:0\n\t" \
    "v_cvt_pk_bf16_f32 v0, v32, v33\n\t" \
    "v_cvt_pk_bf16_f32 v1, v34, v35\n\t" \
    "v_cvt_pk_bf16_f32 v2, v36, v37\n\t" \
    "v_cvt_pk_bf16_f32 v3, v38, v39\n\t" \
    "v_pk_add_f32 v[72:73], v[72:73], v[52:53]\n\t" \
    "ds_write_b64 v243, v[0:1] offset:0\n\t" \
    "v_exp_f32 v21, v21\n\t" \
    "v_exp_f32 v54, v54\n\t" \
    "v_exp_f32 v22, v22\n\t" \
    "v_pk_add_f32 v[74:75], v[74:75], v[20:21]\n\t" \
    "v_exp_f32 v55, v55\n\t" \
    "ds_write_b64 v251, v[2:3] offset:0\n\t" \
    "v_exp_f32 v23, v23\n\t" \
    "v_pk_add_f32 v[72:73], v[72:73], v[54:55]\n\t" \
    "v_pk_fma_f32 v[56:57], v[24:25], s[90:91], v[76:77] op_sel_hi:[1,1,0]\n\t" \
    "v_pk_add_f32 v[74:75], v[74:75], v[22:23]\n\t" \
    "v_add_f32 v72, v72, v73\n\t" \
    "ds_write_b64 v252, v[4:5] offset:0\n\t" \
    "v_add_f32 v74, v74, v75\n\t" \
    "v_pk_fma_f32 v[24:25], v[24:25], s[90:91], v[78:79] op_sel_hi:[1,1,0] neg_lo:[1,0,0] neg_hi:[1,0,0]\n\t" \
    "v_pk_fma_f32 v[58:59], v[26:27], s[90:91], v[76:77] op_sel_hi:[1,1,0]\n\t" \
    "v_permlane32_swap_b32 v72, v74\n\t" \
    "v_pk_fma_f32 v[26:27], v[26:27], s[90:91], v[78:79] op_sel_hi:[1,1,0] neg_lo:[1,0,0] neg_hi:[1,0,0]\n\t" \
    "v_pk_fma_f32 v[60:61], v[28:29], s[90:91], v[76:77] op_sel_hi:[1,1,0]\n\t" \
    "v_add_f32 v72, v72, v74\n\t" \
    "v_pk_fma_f32 v[28:29], v[28:29], s[90:91], v[78:79] op_sel_hi:[1,1,0] neg_lo:[1,0,0] neg_hi:[1,0,0]\n\t" \
    "v_pk_fma_f32 v[62:63], v[30:31], s[90:91], v[76:77] op_sel_hi:[1,1,0]\n\t" \
    "v_pk_fma_f32 v[30:31], v[30:31], s[90:91], v[78:79] op_sel_hi:[1,1,0] neg_lo:[1,0,0] neg_hi:[1,0,0]\n\t" \
    "v_exp_f32 v56, v56\n\t" \
    "v_exp_f32 v24, v24\n\t" \
    "v_exp_f32 v57, v57\n\t" \
    "v_exp_f32 v25, v25\n\t" \
    "v_exp_f32 v58, v58\n\t" \
    "v_exp_f32 v26, v26\n\t" \
    "v_exp_f32 v59, v59\n\t" \
    "v_exp_f32 v27, v27\n\t" \
    "v_exp_f32 v60, v60\n\t" \
    "v_pk_add_f32 v[76:77], v[56:57], v[58:59]\n\t" \
    "v_pk_add_f32 v[78:79], v[24:25], v[26:27]\n\t" \
    "v_exp_f32 v28, v28\n\t" \
    "v_exp_f32 v61, v61\n\t" \
    "v_exp_f32 v29, v29\n\t" \
    "v_exp_f32 v62, v62\n\t" \
    "v_pk_add_f32 v[76:77], v[76:77], v[60:61]\n\t" \
    "v_pk_add_f32 v[78:79], v[78:79], v[28:29]\n\t" \
    "v_exp_f32 v30, v30\n\t" \
    "v_exp_f32 v63, v63\n\t" \
    "v_exp_f32 v31, v31\n\t" \
    "v_pk_mul_f32 v[8:9], v[8:9], v[70:71] op_sel_hi:[1,0]\n\t" \
    "v_pk_add_f32 v[76:77], v[76:77], v[62:63]\n\t" \
    "v_pk_add_f32 v[78:79], v[78:79], v[30:31]\n\t" \
    "v_pk_mul_f32 v[40:41], v[40:41], v[68:69] op_sel_hi:[1,0]\n\t" \
    "v_add_f32 v76, v76, v77\n\t" \
    "v_add_f32 v78, v78, v79\n\t" \
    "v_pk_mul_f32 v[10:11], v[10:11], v[70:71] op_sel_hi:[1,0]\n\t" \
    "v_pk_mul_f32 v[42:43], v[42:43], v[68:69] op_sel_hi:[1,0]\n\t" \
    "v_permlane32_swap_b32 v76, v78\n\t" \
    "v_pk_mul_f32 v[12:13], v[12:13], v[70:71] op_sel_hi:[1,0]\n\t" \
    "v_pk_mul_f32 v[44:45], v[44:45], v[68:69] op_sel_hi:[1,0]\n\t" \
    "v_add_f32 v76, v76, v78\n\t" \
    "v_pk_mul_f32 v[14:15], v[14:15], v[70:71] op_sel_hi:[1,0]\n\t" \
    "v_pk_mul_f32 v[46:47], v[46:47], v[68:69] op_sel_hi:[1,0]\n\t" \
    "v_permlane16_swap_b32 v72, v76\n\t" \
    "v_cvt_pk_bf16_f32 v15, v14, v15\n\t" \
    "v_cvt_pk_bf16_f32 v14, v12, v13\n\t" \
    "v_add_f32 v74, v72, v76\n\t" \
    "v_cvt_pk_bf16_f32 v13, v10, v11\n\t" \
    "ds_write_b64 v253, v[14:15] offset:2048\n\t" \
    "v_rcp_f32 v72, v74\n\t" \
    "v_cvt_pk_bf16_f32 v12, v8, v9\n\t" \
    "v_cvt_pk_bf16_f32 v8, v40, v41\n\t" \
    "v_mov_b32 v76, v72\n\t" \
    "v_cvt_pk_bf16_f32 v9, v42, v43\n\t" \
    "ds_write_b64 v252, v[12:13] offset:2048\n\t" \
    "v_permlane16_swap_b32 v72, v76\n\t" \
    "v_cvt_pk_bf16_f32 v10, v44, v45\n\t" \
    "v_cvt_pk_bf16_f32 v11, v46, v47\n\t" \
    "v_mov_b32 v74, v72\n\t" \
    "v_mov_b32 v78, v76\n\t" \
    "ds_write_b64 v243, v[8:9] offset:2048\n\t" \
    "v_permlane32_swap_b32 v72, v74\n\t" \
    "v_permlane32_swap_b32 v76, v78\n\t" \
    "ds_write_b64 v251, v[10:11] offset:2048\n\t" \
    "v_mov_b32 v237, 3\n\t" \
    "v_pk_mul_f32 v[16:17], v[16:17], v[74:75] op_sel_hi:[1,0]\n\t" \
    "ds_write_b32 v248, v237\n\t" \
    "v_pk_mul_f32 v[48:49], v[48:49], v[72:73] op_sel_hi:[1,0]\n\t" \
    "ds_read_b64_tr_b16 v[96:97], v244\n\t" \
    "v_pk_mul_f32 v[18:19], v[18:19], v[74:75] op_sel_hi:[1,0]\n\t" \
    "ds_read_b64_tr_b16 v[98:99], v244 offset:2048\n\t" \
    "v_pk_mul_f32 v[50:51], v[50:51], v[72:73] op_sel_hi:[1,0]\n\t" \
    "ds_read_b64_tr_b16 v[100:101], v254\n\t" \
    "v_pk_mul_f32 v[20:21], v[20:21], v[74:75] op_sel_hi:[1,0]\n\t" \
    "ds_read_b64_tr_b16 v[102:103], v254 offset:2048\n\t" \
    "v_pk_mul_f32 v[52:53], v[52:53], v[72:73] op_sel_hi:[1,0]\n\t" \
    "v_pk_mul_f32 v[22:23], v[22:23], v[74:75] op_sel_hi:[1,0]\n\t" \
    "v_pk_mul_f32 v[54:55], v[54:55], v[72:73] op_sel_hi:[1,0]\n\t" \
    "v_pk_mul_f32 v[24:25], v[24:25], v[78:79] op_sel_hi:[1,0]\n\t" \
    "v_cvt_pk_bf16_f32 v23, v22, v23\n\t" \
    "v_cvt_pk_bf16_f32 v22, v20, v21\n\t" \
    "v_cvt_pk_bf16_f32 v21, v18, v19\n\t" \
    "v_cvt_pk_bf16_f32 v20, v16, v17\n\t" \
    "ds_write_b64 v253, v[22:23] offset:4096\n\t" \
    "v_cvt_pk_bf16_f32 v16, v48, v49\n\t" \
    "v_cvt_pk_bf16_f32 v17, v50, v51\n\t" \
    "v_cvt_pk_bf16_f32 v18, v52, v53\n\t" \
    "v_cvt_pk_bf16_f32 v19, v54, v55\n\t" \
    "v_pk_mul_f32 v[56:57], v[56:57], v[76:77] op_sel_hi:[1,0]\n\t" \
    "ds_write_b64 v243, v[16:17] offset:4096\n\t" \
    "v_pk_mul_f32 v[26:27], v[26:27], v[78:79] op_sel_hi:[1,0]\n\t" \
    "v_pk_mul_f32 v[58:59], v[58:59], v[76:77] op_sel_hi:[1,0]\n\t" \
    "v_pk_mul_f32 v[28:29], v[28:29], v[78:79] op_sel_hi:[1,0]\n\t" \
    "v_pk_mul_f32 v[60:61], v[60:61], v[76:77] op_sel_hi:[1,0]\n\t" \
    "v_pk_mul_f32 v[30:31], v[30:31], v[78:79] op_sel_hi:[1,0]\n\t" \
    "ds_write_b64 v251, v[18:19] offset:4096\n\t" \
    "v_pk_mul_f32 v[62:63], v[62:63], v[76:77] op_sel_hi:[1,0]\n\t" \
    "v_cvt_pk_bf16_f32 v31, v30, v31\n\t" \
    "v_cvt_pk_bf16_f32 v30, v28, v29\n\t" \
    "v_cvt_pk_bf16_f32 v29, v26, v27\n\t" \
    "v_cvt_pk_bf16_f32 v28, v24, v25\n\t" \
    "ds_write_b64 v252, v[20:21] offset:4096\n\t" \
    "ds_read_b32 v238, v250\n\t" \
    "v_cvt_pk_bf16_f32 v24, v56, v57\n\t" \
    "ds_read_b64_tr_b16 v[104:105], v245\n\t" \
    "v_cvt_pk_bf16_f32 v25, v58, v59\n\t" \
    "ds_read_b64_tr_b16 v[106:107], v245 offset:2048\n\t" \
    "v_cvt_pk_bf16_f32 v26, v60, v61\n\t" \
    "ds_read_b64_tr_b16 v[108:109], v255\n\t" \
    "v_cvt_pk_bf16_f32 v27, v62, v63\n\t" \
    "ds_read_b64_tr_b16 v[110:111], v255 offset:2048\n\t" \
    "ds_write_b64 v243, v[24:25] offset:6144\n\t" \
    "ds_read_b128 v[80:83], v242\n\t" \
    "ds_write_b64 v251, v[26:27] offset:6144\n\t" \
    "ds_read_b128 v[84:87], v242 offset:1024\n\t" \
    "ds_write_b64 v252, v[28:29] offset:6144\n\t" \
    "ds_read_b128 v[88:91], v242 offset:4096\n\t" \
    "ds_write_b64 v253, v[30:31] offset:6144\n\t" \
    "ds_read_b128 v[92:95], v242 offset:5120\n\t" \
    "v_mov_b32 v237, 4\n\t" \
    "ds_write_b32 v248, v237\n\t" \
    "s_waitcnt lgkmcnt(14)\n\t" \
    "v_mfma_f32_16x16x32_bf16 v[64:67], v[96:99], v[232:235], 0\n\t" \
    "ds_read_b64_tr_b16 v[0:1], v244 offset:4096\n\t" \
    "ds_read_b64_tr_b16 v[2:3], v244 offset:6144\n\t" \
    "v_mfma_f32_16x16x32_bf16 v[68:71], v[100:103], v[232:235], 0\n\t" \
    "ds_read_b64_tr_b16 v[4:5], v254 offset:4096\n\t" \
    "ds_read_b64_tr_b16 v[6:7], v254 offset:6144\n\t" \
    "v_mfma_f32_16x16x32_bf16 v[32:35], v[96:99], v[112:115], 0\n\t" \
    "v_mfma_f32_16x16x32_bf16 v[40:43], v[100:103], v[112:115], 0\n\t" \
    "s_waitcnt lgkmcnt(14)\n\t" \
    "v_readfirstlane_b32 s85, v238\n\t" \
    "s_cmp_lt_u32 s85, 3\n\t" \
    "s_cbranch_scc1 .Lab_s3\n\t" \
    ".Lab_s3r:\n\t" \
    "v_mfma_f32_16x16x32_bf16 v[36:39], v[96:99], v[116:119], 0\n\t" \
    "v_mfma_f32_16x16x32_bf16 v[44:47], v[100:103], v[116:119], 0\n\t" \
    "ds_read_b32 v238, v250\n\t" \
    "ds_read_b64_tr_b16 v[8:9], v245 offset:4096\n\t" \
    "v_mfma_f32_16x16x32_bf16 v[64:67], v[104:107], v[232:235], v[64:67]\n\t" \
    "ds_read_b64_tr_b16 v[10:11], v245 offset:6144\n\t" \
    "ds_read_b64_tr_b16 v[12:13], v255 offset:4096\n\t" \
    "s_waitcnt lgkmcnt(14)\n\t" \
    "v_mfma_f32_16x16x32_bf16 v[68:71], v[108:111], v[232:235], v[68:71]\n\t" \
    "ds_read_b64_tr_b16 v[14:15], v255 offset:6144\n\t" \
    "v_mfma_f32_16x16x32_bf16 v[32:35], v[104:107], v[80:83], v[32:35]\n\t" \
    "v_mfma_f32_16x16x32_bf16 v[40:43], v[108:111], v[80:83], v[40:43]\n\t" \
    "s_waitcnt lgkmcnt(14)\n\t" \
    "v_mfma_f32_16x16x32_bf16 v[36:39], v[104:107], v[84:87], v[36:39]\n\t" \
    "v_add_u32 v253, 135168, v241\n\t" \
    "v_mfma_f32_16x16x32_bf16 v[44:47], v[108:111], v[84:87], v[44:47]\n\t" \
    "v_xor_b32 v251, 32, v253\n\t" \
    "s_waitcnt lgkmcnt(7)\n\t" \
    "v_mfma_f32_16x16x32_bf16 v[64:67], v[0:3], v[232:235], v[64:67]\n\t" \
    "v_mov_b32 v237, 5\n\t" \
    "s_waitcnt lgkmcnt(5)\n\t" \
    "v_mfma_f32_16x16x32_bf16 v[68:71], v[4:7], v[232:235], v[68:71]\n\t" \
    "v_mfma_f32_16x16x32_bf16 v[32:35], v[0:3], v[120:123], v[32:35]\n\t" \
    "v_mfma_f32_16x16x32_bf16 v[40:43], v[4:7], v[120:123], v[40:43]\n\t" \
    "v_mfma_f32_16x16x32_bf16 v[36:39], v[0:3], v[124:127], v[36:39]\n\t" \
    "s_waitcnt lgkmcnt(4)\n\t" \
    "v_readfirstlane_b32 s85, v238\n\t" \
    "s_cmp_lt_u32 s85, 4\n\t" \
    "s_cbranch_scc1 .Lab_s4\n\t" \
    ".Lab_s4r:\n\t" \
    "v_mfma_f32_16x16x32_bf16 v[44:47], v[4:7], v[124:127], v[44:47]\n\t" \
    "ds_write_b32 v248, v237\n\t" \
    "s_waitcnt lgkmcnt(3)\n\t" \
    "v_mfma_f32_16x16x32_bf16 v[64:67], v[8:11], v[232:235], v[64:67]\n\t" \
    "ds_read_b32 v238, v249 offset:92\n\t" \
    "s_waitcnt lgkmcnt(2)\n\t" \
    "v_mfma_f32_16x16x32_bf16 v[68:71], v[12:15], v[232:235], v[68:71]\n\t" \
    "v_mfma_f32_16x16x32_bf16 v[32:35], v[8:11], v[88:91], v[32:35]\n\t" \
    "v_mfma_f32_16x16x32_bf16 v[40:43], v[12:15], v[88:91], v[40:43]\n\t" \
    "v_mfma_f32_16x16x32_bf16 v[36:39], v[8:11], v[92:95], v[36:39]\n\t" \
    "v_mfma_f32_16x16x32_bf16 v[44:47], v[12:15], v[92:95], v[44:47]\n\t" \
    "ds_read_b64_tr_b16 v[80:81], v253\n\t" \
    "ds_read_b64_tr_b16 v[82:83], v253 offset:512\n\t" \
    "ds_read_b64_tr_b16 v[84:85], v251\n\t" \
    "s_nop 0\n\t" \
    "v_cndmask_b32 v24, v68, v64, vcc\n\t" \
    "v_cndmask_b32 v25, v69, v65, vcc\n\t" \
    "v_cndmask_b32 v26, v70, v66, vcc\n\t" \
    "v_cndmask_b32 v27, v71, v67, vcc\n\t" \
    "v_add3_u32 v30, v24, v25, v26\n\t" \
    "v_add_u32 v30, v30, v27\n\t" \
    "v_cvt_pk_bf16_f32 v16, v32, v33\n\t" \
    "v_cvt_pk_bf16_f32 v17, v34, v35\n\t" \
    "v_cvt_pk_bf16_f32 v18, v40, v41\n\t" \
    "v_cvt_pk_bf16_f32 v19, v42, v43\n\t" \
    "v_cvt_pk_bf16_f32 v20, v36, v37\n\t" \
    "v_cvt_pk_bf16_f32 v21, v38, v39\n\t" \
    "v_cvt_pk_bf16_f32 v22, v44, v45\n\t" \
    "v_cvt_pk_bf16_f32 v23, v46, v47\n\t" \
    "v_add3_u32 v28, v16, v17, v18\n\t" \
    "v_add3_u32 v29, v20, v21, v22\n\t" \
    "v_add_u32 v28, v28, v19\n\t" \
    "v_add_u32 v29, v29, v23\n\t" \
    "v_cndmask_b32_e64 v30, 0, v30, s[94:95]\n\t" \
    "buffer_store_dwordx4 v[16:19], v246, %[rsb], s87 offen sc1\n\t" \
    "v_permlane16_swap_b32 v28, v29\n\t" \
    "v_add3_u32 v28, v28, v29, v30\n\t" \
    "buffer_store_dwordx4 v[20:23], v246, %[rsb], s86 offen sc1\n\t" \
    "v_cndmask_b32_e64 v25, v25, v236, s[92:93]\n\t" \
    "v_add_u32_dpp v28, v28, v28 row_ror:8 row_mask:0xf bank_mask:0xf bound_ctrl:1\n\t" \
    "v_mov_b32 v237, 6\n\t" \
    "ds_read_b64_tr_b16 v[86:87], v251 offset:512\n\t" \
    "v_add_u32_dpp v28, v28, v28 row_ror:4 row_mask:0xf bank_mask:0xf bound_ctrl:1\n\t" \
    "ds_read_b64_tr_b16 v[88:89], v253 offset:4096\n\t" \
    "ds_read_b64_tr_b16 v[90:91], v253 offset:4608\n\t" \
    "v_add_u32_dpp v28, v28, v28 row_ror:2 row_mask:0xf bank_mask:0xf bound_ctrl:1\n\t" \
    "ds_read_b64_tr_b16 v[92:93], v251 offset:4096\n\t" \
    "ds_read_b64_tr_b16 v[94:95], v251 offset:4608\n\t" \
    "v_add_u32_dpp v28, v28, v28 row_ror:1 row_mask:0xf bank_mask:0xf bound_ctrl:1\n\t" \
    "v_cndmask_b32_e64 v24, v24, v28, s[92:93]\n\t" \
    "buffer_store_dwordx4 v[24:27], v247, %[rsb], s88 offen sc1\n\t" \
    "ds_write_b32 v248, v237\n\t" \
    "s_setprio 1\n\t" \
    "s_waitcnt lgkmcnt(9)\n\t" \
    "v_readfirstlane_b32 s85, v238\n\t" \
    "s_cmp_lt_u32 s85, 1\n\t" \
    "s_cbranch_scc1 .Lab_s5\n\t" \
    ".Lab_s5r:\n\t" \
    "s_waitcnt lgkmcnt(7)\n\t" \
    "v_mfma_f32_16x16x32_bf16 v[192:195], v[80:83], v[128:131], v[192:195]\n\t" \
    "v_add_u32 v244, 49152, v241\n\t" \
    "v_xor_b32 v245, 32, v244\n\t" \
    "v_mfma_f32_16x16x32_bf16 v[200:203], v[80:83], v[132:135], v[200:203]\n\t" \
    "ds_read_b64_tr_b16 v[96:97], v244\n\t" \
    "ds_read_b64_tr_b16 v[98:99], v244 offset:512\n\t" \
    "v_mfma_f32_16x16x32_bf16 v[208:211], v[80:83], v[136:139], v[208:211]\n\t" \
    "ds_read_b64_tr_b16 v[100:101], v245\n\t" \
    "ds_read_b64_tr_b16 v[102:103], v245 offset:512\n\t" \
    "v_mfma_f32_16x16x32_bf16 v[216:219], v[80:83], v[140:143], v[216:219]\n\t" \
    "ds_read_b64_tr_b16 v[104:105], v244 offset:4096\n\t" \
    "ds_read_b64_tr_b16 v[106:107], v244 offset:4608\n\t" \
    "s_waitcnt lgkmcnt(11)\n\t" \
    "v_mfma_f32_16x16x32_bf16 v[196:199], v[84:87], v[128:131], v[196:199]\n\t" \
    "ds_read_b64_tr_b16 v[108:109], v245 offset:4096\n\t" \
    "ds_read_b64_tr_b16 v[110:111], v245 offset:4608\n\t" \
    "v_mfma_f32_16x16x32_bf16 v[204:207], v[84:87], v[132:135], v[204:207]\n\t" \
    "v_xor_b32 v246, 64, v240\n\t" \
    "v_subrev_u32 v242, 16384, v241\n\t" \
    "v_mfma_f32_16x16x32_bf16 v[212:215], v[84:87], v[136:139], v[212:215]\n\t" \
    "v_xor_b32 v243, 32, v242\n\t" \
    "v_add_u32 v247, 65536, v240\n\t" \
    "v_mfma_f32_16x16x32_bf16 v[220:223], v[84:87], v[140:143], v[220:223]\n\t" \
    "v_mov_b32 v237, 7\n\t" \
    "ds_write_b32 v248, v237\n\t" \
    "s_waitcnt lgkmcnt(12)\n\t" \
    "v_mfma_f32_16x16x32_bf16 v[192:195], v[88:91], v[144:147], v[192:195]\n\t" \
    "ds_read_b64_tr_b16 v[80:81], v244 offset:8192\n\t" \
    "v_mfma_f32_16x16x32_bf16 v[200:203], v[88:91], v[148:151], v[200:203]\n\t" \
    "ds_read_b128 v[128:131], v240 offset:40960\n\t" \
    "v_mfma_f32_16x16x32_bf16 v[208:211], v[88:91], v[152:155], v[208:211]\n\t" \
    "ds_read_b64_tr_b16 v[82:83], v244 offset:8704\n\t" \
    "v_add_u32 v237, 1, v237\n\t" \
    "v_mfma_f32_16x16x32_bf16 v[216:219], v[88:91], v[156:159], v[216:219]\n\t" \
    "ds_read_b128 v[132:135], v240 offset:43008\n\t" \
    "s_waitcnt lgkmcnt(14)\n\t" \
    "v_mfma_f32_16x16x32_bf16 v[196:199], v[92:95], v[144:147], v[196:199]\n\t" \
    "ds_read_b64_tr_b16 v[84:85], v245 offset:8192\n\t" \
    "v_xor_b32 v255, 64, v247\n\t" \
    "v_mfma_f32_16x16x32_bf16 v[204:207], v[92:95], v[148:151], v[204:207]\n\t" \
    "ds_read_b128 v[136:139], v240 offset:45056\n\t" \
    "v_mfma_f32_16x16x32_bf16 v[212:215], v[92:95], v[152:155], v[212:215]\n\t" \
    "ds_read_b64_tr_b16 v[86:87], v245 offset:8704\n\t" \
    "v_mfma_f32_16x16x32_bf16 v[220:223], v[92:95], v[156:159], v[220:223]\n\t" \
    "ds_read_b128 v[140:143], v240 offset:47104\n\t" \
    "s_waitcnt lgkmcnt(14)\n\t" \
    "v_mfma_f32_16x16x32_bf16 v[192:195], v[96:99], v[160:163], v[192:195]\n\t" \
    "ds_read_b64_tr_b16 v[88:89], v244 offset:12288\n\t" \
    "v_mfma_f32_16x16x32_bf16 v[200:203], v[96:99], v[164:167], v[200:203]\n\t" \
    "ds_read_b128 v[144:147], v246 offset:40960\n\t" \
    "v_mfma_f32_16x16x32_bf16 v[208:211], v[96:99], v[168:171], v[208:211]\n\t" \
    "ds_read_b64_tr_b16 v[90:91], v244 offset:12800\n\t" \
    "v_mfma_f32_16x16x32_bf16 v[216:219], v[96:99], v[172:175], v[216:219]\n\t" \
    "ds_read_b128 v[148:151], v246 offset:43008\n\t" \
    "s_waitcnt lgkmcnt(14)\n\t" \
    "v_mfma_f32_16x16x32_bf16 v[196:199], v[100:103], v[160:163], v[196:199]\n\t" \
    "ds_read_b64_tr_b16 v[92:93], v245 offset:12288\n\t" \
    "v_mfma_f32_16x16x32_bf16 v[204:207], v[100:103], v[164:167], v[204:207]\n\t" \
    "ds_read_b128 v[152:155], v246 offset:45056\n\t" \
    "v_mfma_f32_16x16x32_bf16 v[212:215], v[100:103], v[168:171], v[212:215]\n\t" \
    "ds_read_b64_tr_b16 v[94:95], v245 offset:12800\n\t" \
    "v_mfma_f32_16x16x32_bf16 v[220:223], v[100:103], v[172:175], v[220:223]\n\t" \
    "ds_read_b128 v[156:159], v246 offset:47104\n\t" \
    "ds_write_b32 v248, v237\n\t" \
    "v_mfma_f32_16x16x32_bf16 v[192:195], v[104:107], v[176:179], v[192:195]\n\t" \
    "ds_read_b64_tr_b16 v[96:97], v242 offset:24576\n\t" \
    "v_mfma_f32_16x16x32_bf16 v[200:203], v[104:107], v[180:183], v[200:203]\n\t" \
    "ds_read_b128 v[160:163], v247 offset:16384\n\t" \
    "v_mfma_f32_16x16x32_bf16 v[208:211], v[104:107], v[184:187], v[208:211]\n\t" \
    "ds_read_b64_tr_b16 v[98:99], v242 offset:25088\n\t" \
    "v_add_u32 v237, 1, v237\n\t" \
    "v_mfma_f32_16x16x32_bf16 v[216:219], v[104:107], v[188:191], v[216:219]\n\t" \
    "ds_read_b128 v[164:167], v247 offset:18432\n\t" \
    "s_waitcnt lgkmcnt(14)\n\t" \
    "v_mfma_f32_16x16x32_bf16 v[196:199], v[108:111], v[176:179], v[196:199]\n\t" \
    "ds_read_b64_tr_b16 v[100:101], v243 offset:24576\n\t" \
    "v_mfma_f32_16x16x32_bf16 v[204:207], v[108:111], v[180:183], v[204:207]\n\t" \
    "ds_read_b128 v[168:171], v247 offset:20480\n\t" \
    "v_mfma_f32_16x16x32_bf16 v[212:215], v[108:111], v[184:187], v[212:215]\n\t" \
    "ds_read_b64_tr_b16 v[102:103], v243 offset:25088\n\t" \
    "v_mfma_f32_16x16x32_bf16 v[220:223], v[108:111], v[188:191], v[220:223]\n\t" \
    "ds_read_b128 v[172:175], v247 offset:22528\n\t" \
    "v_mfma_f32_16x16x32_bf16 v[192:195], v[80:83], v[128:131], v[192:195]\n\t" \
    "ds_read_b64_tr_b16 v[104:105], v242 offset:28672\n\t" \
    "v_mfma_f32_16x16x32_bf16 v[200:203], v[80:83], v[132:135], v[200:203]\n\t" \
    "ds_read_b128 v[176:179], v255 offset:16384\n\t" \
    "v_mfma_f32_16x16x32_bf16 v[208:211], v[80:83], v[136:139], v[208:211]\n\t" \
    "ds_read_b64_tr_b16 v[106:107], v242 offset:29184\n\t" \
    "s_waitcnt lgkmcnt(14)\n\t" \
    "v_mfma_f32_16x16x32_bf16 v[216:219], v[80:83], v[140:143], v[216:219]\n\t" \
    "ds_read_b128 v[180:183], v255 offset:18432\n\t" \
    "v_mfma_f32_16x16x32_bf16 v[196:199], v[84:87], v[128:131], v[196:199]\n\t" \
    "ds_read_b64_tr_b16 v[108:109], v243 offset:28672\n\t" \
    "v_mfma_f32_16x16x32_bf16 v[204:207], v[84:87], v[132:135], v[204:207]\n\t" \
    "ds_read_b128 v[184:187], v255 offset:20480\n\t" \
    "v_mfma_f32_16x16x32_bf16 v[212:215], v[84:87], v[136:139], v[212:215]\n\t" \
    "ds_read_b64_tr_b16 v[110:111], v243 offset:29184\n\t" \
    "v_mfma_f32_16x16x32_bf16 v[220:223], v[84:87], v[140:143], v[220:223]\n\t" \
    "ds_read_b128 v[188:191], v255 offset:22528\n\t" \
    "ds_write_b32 v248, v237\n\t" \
    "v_mfma_f32_16x16x32_bf16 v[192:195], v[88:91], v[144:147], v[192:195]\n\t" \
    "ds_read_b64_tr_b16 v[80:81], v242 offset:32768\n\t" \
    "v_mfma_f32_16x16x32_bf16 v[200:203], v[88:91], v[148:151], v[200:203]\n\t" \
    "ds_read_b128 v[128:131], v240\n\t" \
    "v_mfma_f32_16x16x32_bf16 v[208:211], v[88:91], v[152:155], v[208:211]\n\t" \
    "ds_read_b64_tr_b16 v[82:83], v242 offset:33280\n\t" \
    "v_add_u32 v237, 1, v237\n\t" \
    "s_waitcnt lgkmcnt(14)\n\t" \
    "v_mfma_f32_16x16x32_bf16 v[216:219], v[88:91], v[156:159], v[216:219]\n\t" \
    "ds_read_b128 v[132:135], v240 offset:2048\n\t" \
    "v_mfma_f32_16x16x32_bf16 v[196:199], v[92:95], v[144:147], v[196:199]\n\t" \
    "ds_read_b64_tr_b16 v[84:85], v243 offset:32768\n\t" \
    "v_mfma_f32_16x16x32_bf16 v[204:207], v[92:95], v[148:151], v[204:207]\n\t" \
    "ds_read_b128 v[136:139], v240 offset:4096\n\t" \
    "v_mfma_f32_16x16x32_bf16 v[212:215], v[92:95], v[152:155], v[212:215]\n\t" \
    "ds_read_b64_tr_b16 v[86:87], v243 offset:33280\n\t" \
    "ds_read_b32 v238, v249 offset:92\n\t" \
    "v_mfma_f32_16x16x32_bf16 v[220:223], v[92:95], v[156:159], v[220:223]\n\t" \
    "ds_read_b128 v[140:143], v240 offset:6144\n\t" \
    "v_mfma_f32_16x16x32_bf16 v[192:195], v[96:99], v[160:163], v[192:195]\n\t" \
    "ds_read_b64_tr_b16 v[88:89], v242 offset:36864\n\t" \
    "v_add_u32 v247, %[xs0], v240\n\t" \
    "v_mfma_f32_16x16x32_bf16 v[200:203], v[96:99], v[164:167], v[200:203]\n\t" \
    "ds_read_b128 v[144:147], v246\n\t" \
    "v_mfma_f32_16x16x32_bf16 v[208:211], v[96:99], v[168:171], v[208:211]\n\t" \
    "ds_read_b64_tr_b16 v[90:91], v242 offset:37376\n\t" \
    "v_xor_b32 v255, 64, v247\n\t" \
    "s_waitcnt lgkmcnt(14)\n\t" \
    "v_mfma_f32_16x16x32_bf16 v[216:219], v[96:99], v[172:175], v[216:219]\n\t" \
    "ds_read_b128 v[148:151], v246 offset:2048\n\t" \
    "v_mfma_f32_16x16x32_bf16 v[196:199], v[100:103], v[160:163], v[196:199]\n\t" \
    "ds_read_b64_tr_b16 v[92:93], v243 offset:36864\n\t" \
    "v_mfma_f32_16x16x32_bf16 v[204:207], v[100:103], v[164:167], v[204:207]\n\t" \
    "ds_read_b128 v[152:155], v246 offset:4096\n\t" \
    "v_mfma_f32_16x16x32_bf16 v[212:215], v[100:103], v[168:171], v[212:215]\n\t" \
    "ds_read_b64_tr_b16 v[94:95], v243 offset:37376\n\t" \
    "v_mfma_f32_16x16x32_bf16 v[220:223], v[100:103], v[172:175], v[220:223]\n\t" \
    "ds_read_b128 v[156:159], v246 offset:6144\n\t" \
    "ds_write_b32 v248, v237\n\t" \
    "v_mfma_f32_16x16x32_bf16 v[192:195], v[104:107], v[176:179], v[192:195]\n\t" \
    "s_waitcnt lgkmcnt(10)\n\t" \
    "v_readfirstlane_b32 s85, v238\n\t" \
    "s_cmp_lt_u32 s85, 2\n\t" \
    "s_cbranch_scc1 .Lab_s6\n\t" \
    ".Lab_s6r:\n\t" \
    "ds_read_b64_tr_b16 v[96:97], v244 offset:32768\n\t" \
    "v_mfma_f32_16x16x32_bf16 v[200:203], v[104:107], v[180:183], v[200:203]\n\t" \
    "ds_read_b128 v[160:163], v247\n\t" \
    "v_mfma_f32_16x16x32_bf16 v[208:211], v[104:107], v[184:187], v[208:211]\n\t" \
    "ds_read_b64_tr_b16 v[98:99], v244 offset:33280\n\t" \
    "v_add_u32 v237, 1, v237\n\t" \
    "v_mfma_f32_16x16x32_bf16 v[216:219], v[104:107], v[188:191], v[216:219]\n\t" \
    "ds_read_b128 v[164:167], v247 offset:2048\n\t" \
    "v_mfma_f32_16x16x32_bf16 v[196:199], v[108:111], v[176:179], v[196:199]\n\t" \
    "ds_read_b64_tr_b16 v[100:101], v245 offset:32768\n\t" \
    "v_mfma_f32_16x16x32_bf16 v[204:207], v[108:111], v[180:183], v[204:207]\n\t" \
    "ds_read_b128 v[168:171], v247 offset:4096\n\t" \
    "v_mfma_f32_16x16x32_bf16 v[212:215], v[108:111], v[184:187], v[212:215]\n\t" \
    "ds_read_b64_tr_b16 v[102:103], v245 offset:33280\n\t" \
    "v_mfma_f32_16x16x32_bf16 v[220:223], v[108:111], v[188:191], v[220:223]\n\t" \
    "ds_read_b128 v[172:175], v247 offset:6144\n\t" \
    "v_mfma_f32_16x16x32_bf16 v[192:195], v[80:83], v[128:131], v[192:195]\n\t" \
    "ds_read_b64_tr_b16 v[104:105], v244 offset:36864\n\t" \
    "v_mfma_f32_16x16x32_bf16 v[200:203], v[80:83], v[132:135], v[200:203]\n\t" \
    "ds_read_b128 v[176:179], v255\n\t" \
    "v_mfma_f32_16x16x32_bf16 v[208:211], v[80:83], v[136:139], v[208:211]\n\t" \
    "ds_read_b64_tr_b16 v[106:107], v244 offset:37376\n\t" \
    "s_waitcnt lgkmcnt(14)\n\t" \
    "v_mfma_f32_16x16x32_bf16 v[216:219], v[80:83], v[140:143], v[216:219]\n\t" \
    "ds_read_b128 v[180:183], v255 offset:2048\n\t" \
    "v_mfma_f32_16x16x32_bf16 v[196:199], v[84:87], v[128:131], v[196:199]\n\t" \
    "ds_read_b64_tr_b16 v[108:109], v245 offset:36864\n\t" \
    "v_mfma_f32_16x16x32_bf16 v[204:207], v[84:87], v[132:135], v[204:207]\n\t" \
    "ds_read_b128 v[184:187], v255 offset:4096\n\t" \
    "v_mfma_f32_16x16x32_bf16 v[212:215], v[84:87], v[136:139], v[212:215]\n\t" \
    "ds_read_b64_tr_b16 v[110:111], v245 offset:37376\n\t" \
    "v_mfma_f32_16x16x32_bf16 v[220:223], v[84:87], v[140:143], v[220:223]\n\t" \
    "ds_read_b128 v[188:191], v255 offset:6144\n\t" \
    "ds_write_b32 v248, v237\n\t" \
    "v_mfma_f32_16x16x32_bf16 v[192:195], v[88:91], v[144:147], v[192:195]\n\t" \
    "v_mbcnt_lo_u32_b32 v250, -1, 0\n\t" \
    "v_mbcnt_hi_u32_b32 v250, -1, v250\n\t" \
    "v_mfma_f32_16x16x32_bf16 v[200:203], v[88:91], v[148:151], v[200:203]\n\t" \
    "v_and_b32 v238, 15, v250\n\t" \
    "v_lshlrev_b32 v238, 3, v238\n\t" \
    "v_mfma_f32_16x16x32_bf16 v[208:211], v[88:91], v[152:155], v[208:211]\n\t" \
    "v_add_u32 v237, 1, v237\n\t" \
    "s_waitcnt lgkmcnt(14)\n\t" \
    "v_mfma_f32_16x16x32_bf16 v[216:219], v[88:91], v[156:159], v[216:219]\n\t" \
    "buffer_load_dwordx2 v[232:233], v238, %[rsw], %[sg] offen sc1\n\t" \
    "v_mfma_f32_16x16x32_bf16 v[196:199], v[92:95], v[144:147], v[196:199]\n\t" \
    "v_lshlrev_b32 v251, 4, v250\n\t" \
    "s_add_u32 s85, %[sof], %[ofc]\n\t" \
    "v_mfma_f32_16x16x32_bf16 v[204:207], v[92:95], v[148:151], v[204:207]\n\t" \
    "v_add_u32 v240, s85, v251\n\t" \
    "s_add_u32 s87, %[sof], %[ofp]\n\t" \
    "v_mfma_f32_16x16x32_bf16 v[212:215], v[92:95], v[152:155], v[212:215]\n\t" \
    "buffer_load_dwordx2 v[234:235], v238, %[rsw], %[sg] offen offset:128 sc1\n\t" \
    "v_mfma_f32_16x16x32_bf16 v[220:223], v[92:95], v[156:159], v[220:223]\n\t" \
    "v_add_u32 v241, 0x1000, v240\n\t" \
    "s_add_u32 s88, %[sof], %[ogp]\n\t" \
    "v_mfma_f32_16x16x32_bf16 v[192:195], v[96:99], v[160:163], v[192:195]\n\t" \
    "s_mov_b32 s86, 0x10000\n\t" \
    "v_add_u32 v242, 0x2000, v240\n\t" \
    "v_add_u32 v243, 0x3000, v240\n\t" \
    "s_waitcnt lgkmcnt(10)\n\t" \
    "v_mfma_f32_16x16x32_bf16 v[196:199], v[100:103], v[160:163], v[196:199]\n\t" \
    "v_bfe_u32 v244, v251, 1, 8\n\t" \
    "v_add_u32 v253, %[kac], v251\n\t" \
    "s_waitcnt lgkmcnt(6)\n\t" \
    "v_mfma_f32_16x16x32_bf16 v[192:195], v[104:107], v[176:179], v[192:195]\n\t" \
    "v_lshrrev_b32 v112, 2, v250\n\t" \
    "v_bfe_u32 v113, v250, 4, 1\n\t" \
    "s_waitcnt lgkmcnt(2)\n\t" \
    "v_mfma_f32_16x16x32_bf16 v[196:199], v[108:111], v[176:179], v[196:199]\n\t" \
    "v_lshlrev_b32 v245, 7, v112\n\t" \
    "v_bitop3_b32 v112, v112, v113, 3 bitop3:0x28\n\t" \
    "v_mfma_f32_16x16x32_bf16 v[200:203], v[96:99], v[164:167], v[200:203]\n\t" \
    "v_lshl_or_b32 v245, v112, 5, v245\n\t" \
    "v_and_b32 v112, 24, v238\n\t" \
    "v_mfma_f32_16x16x32_bf16 v[204:207], v[100:103], v[164:167], v[204:207]\n\t" \
    "v_or3_b32 v245, v245, v112, s86\n\t" \
    "v_lshlrev_b32 v247, 8, v238\n\t" \
    "v_mfma_f32_16x16x32_bf16 v[200:203], v[104:107], v[180:183], v[200:203]\n\t" \
    "v_lshl_or_b32 v247, v113, 5, v247\n\t" \
    "v_lshrrev_b32 v112, 5, v250\n\t" \
    "v_mfma_f32_16x16x32_bf16 v[204:207], v[108:111], v[180:183], v[204:207]\n\t" \
    "v_lshl_or_b32 v247, v112, 4, v247\n\t" \
    "v_and_b32 v252, 48, v247\n\t" \
    "v_mfma_f32_16x16x32_bf16 v[208:211], v[96:99], v[168:171], v[208:211]\n\t" \
    "v_lshl_add_u32 v252, v252, 2, %[zac]\n\t" \
    "v_lshlrev_b32 v246, 4, v238\n\t" \
    "v_mfma_f32_16x16x32_bf16 v[212:215], v[100:103], v[168:171], v[212:215]\n\t" \
    "v_cvt_pk_bf16_f32 v64, v192, v193\n\t" \
    "v_cvt_pk_bf16_f32 v65, v194, v195\n\t" \
    "v_mfma_f32_16x16x32_bf16 v[208:211], v[104:107], v[184:187], v[208:211]\n\t" \
    "v_cvt_pk_bf16_f32 v66, v196, v197\n\t" \
    "v_cvt_pk_bf16_f32 v67, v198, v199\n\t" \
    "v_mfma_f32_16x16x32_bf16 v[212:215], v[108:111], v[184:187], v[212:215]\n\t" \
    "v_lshl_or_b32 v246, v113, 3, v246\n\t" \
    "v_lshrrev_b32 v113, 4, v238\n\t" \
    "v_mfma_f32_16x16x32_bf16 v[216:219], v[96:99], v[172:175], v[216:219]\n\t" \
    "v_cvt_pk_bf16_f32 v68, v200, v201\n\t" \
    "v_cvt_pk_bf16_f32 v69, v202, v203\n\t" \
    "v_mfma_f32_16x16x32_bf16 v[220:223], v[100:103], v[172:175], v[220:223]\n\t" \
    "v_cvt_pk_bf16_f32 v70, v204, v205\n\t" \
    "v_cvt_pk_bf16_f32 v71, v206, v207\n\t" \
    "s_waitcnt lgkmcnt(1)\n\t" \
    "v_mfma_f32_16x16x32_bf16 v[216:219], v[104:107], v[188:191], v[216:219]\n\t" \
    "v_xor_b32 v112, v112, v113\n\t" \
    "v_lshl_or_b32 v246, v112, 4, v246\n\t" \
    "v_mfma_f32_16x16x32_bf16 v[220:223], v[108:111], v[188:191], v[220:223]\n\t" \
    "v_add_u32 v246, %[xec], v246\n\t" \
    "v_mfma_f32_16x16x32_bf16 v[0:3], v[224:227], v[64:67], 0\n\t" \
    "v_cvt_pk_bf16_f32 v72, v208, v209\n\t" \
    "v_cvt_pk_bf16_f32 v73, v210, v211\n\t" \
    "v_mfma_f32_16x16x32_bf16 v[4:7], v[228:231], v[64:67], 0\n\t" \
    "v_cvt_pk_bf16_f32 v74, v212, v213\n\t" \
    "v_cvt_pk_bf16_f32 v75, v214, v215\n\t" \
    "v_mfma_f32_16x16x32_bf16 v[8:11], v[224:227], v[68:71], 0\n\t" \
    "v_mov_b32 v120, 0x358637bd\n\t" \
    "v_mov_b32 v121, 0x358637bd\n\t" \
    "v_mfma_f32_16x16x32_bf16 v[12:15], v[228:231], v[68:71], 0\n\t" \
    "v_mov_b32 v122, 0x358637bd\n\t" \
    "v_mov_b32 v123, 0x358637bd\n\t" \
    "v_mfma_f32_16x16x32_bf16 v[16:19], v[224:227], v[72:75], 0\n\t" \
    "v_cvt_pk_bf16_f32 v76, v216, v217\n\t" \
    "v_cvt_pk_bf16_f32 v77, v218, v219\n\t" \
    "v_mfma_f32_16x16x32_bf16 v[20:23], v[228:231], v[72:75], 0\n\t" \
    "v_cvt_pk_bf16_f32 v78, v220, v221\n\t" \
    "v_cvt_pk_bf16_f32 v79, v222, v223\n\t" \
    "s_nop 1\n\t" \
    "v_mfma_f32_16x16x32_bf16 v[24:27], v[224:227], v[76:79], 0\n\t" \
    "v_mfma_f32_16x16x32_bf16 v[28:31], v[228:231], v[76:79], 0\n\t" \
    "v_max3_f32 v64, v0, v1, v2\n\t" \
    "v_max3_f32 v66, -v0, -v1, -v2\n\t" \
    "v_max3_f32 v68, v8, v9, v10\n\t" \
    "v_max3_f32 v64, v64, v3, v4\n\t" \
    "v_max3_f32 v66, v66, -v3, -v4\n\t" \
    "v_max3_f32 v68, v68, v11, v12\n\t" \
    "v_max3_f32 v64, v64, v5, v6\n\t" \
    "v_max3_f32 v66, v66, -v5, -v6\n\t" \
    "v_max3_f32 v68, v68, v13, v14\n\t" \
    "v_max_f32_e64 v64, v64, v7\n\t" \
    "v_max_f32_e64 v66, v66, -v7\n\t" \
    "v_max_f32_e64 v68, v68, v15\n\t" \
    "v_max3_f32 v70, -v8, -v9, -v10\n\t" \
    "v_permlane32_swap_b32 v64, v66\n\t" \
    "v_max3_f32 v72, v16, v17, v18\n\t" \
    "v_max3_f32 v70, v70, -v11, -v12\n\t" \
    "v_max_f32 v64, v64, v66\n\t" \
    "v_max3_f32 v72, v72, v19, v20\n\t" \
    "v_max3_f32 v70, v70, -v13, -v14\n\t" \
    "v_max3_f32 v74, -v16, -v17, -v18\n\t" \
    "v_max3_f32 v72, v72, v21, v22\n\t" \
    "v_max_f32_e64 v70, v70, -v15\n\t" \
    "v_max3_f32 v74, v74, -v19, -v20\n\t" \
    "v_max_f32_e64 v72, v72, v23\n\t" \
    "v_permlane32_swap_b32 v68, v70\n\t" \
    "v_max3_f32 v74, v74, -v21, -v22\n\t" \
    "v_max3_f32 v76, v24, v25, v26\n\t" \
    "v_max_f32 v68, v68, v70\n\t" \
    "v_max_f32_e64 v74, v74, -v23\n\t" \
    "v_max3_f32 v76, v76, v27, v28\n\t" \
    "v_permlane16_swap_b32 v64, v68\n\t" \
    "v_permlane32_swap_b32 v72, v74\n\t" \
    "v_max3_f32 v76, v76, v29, v30\n\t" \
    "v_max_f32 v66, v64, v68\n\t" \
    "v_max_f32 v72, v72, v74\n\t" \
    "v_max_f32_e64 v76, v76, v31\n\t" \
    ".Lpad_ab2:\n\t" \
    "s_cbranch_execz .Lpad_ret\n\t" \
    "v_mul_f32 v64, 0xbfb8aa3b, v66\n\t" \
    "v_mul_f32 v68, 0xbfb8aa3b, v66\n\t" \
    "v_max3_f32 v78, -v24, -v25, -v26\n\t" \
    "v_max3_f32 v78, v78, -v27, -v28\n\t" \
    "v_permlane16_swap_b32 v64, v68\n\t" \
    "v_mov_b32 v66, v64\n\t" \
    "v_max3_f32 v78, v78, -v29, -v30\n\t" \
    "v_mov_b32 v70, v68\n\t" \
    "v_permlane32_swap_b32 v64, v66\n\t" \
    "v_max_f32_e64 v78, v78, -v31\n\t" \
    "v_permlane32_swap_b32 v68, v70\n\t" \
    "v_pk_fma_f32 v[32:33], v[0:1], s[90:91], v[64:65] op_sel_hi:[1,1,0]\n\t" \
    "v_permlane32_swap_b32 v76, v78\n\t" \
    "v_pk_fma_f32 v[0:1], v[0:1], s[90:91], v[66:67] op_sel_hi:[1,1,0] neg_lo:[1,0,0] neg_hi:[1,0,0]\n\t" \
    "v_pk_fma_f32 v[34:35], v[2:3], s[90:91], v[64:65] op_sel_hi:[1,1,0]\n\t" \
    "v_max_f32 v76, v76, v78\n\t" \
    "v_pk_fma_f32 v[2:3], v[2:3], s[90:91], v[66:67] op_sel_hi:[1,1,0] neg_lo:[1,0,0] neg_hi:[1,0,0]\n\t" \
    "v_pk_fma_f32 v[36:37], v[4:5], s[90:91], v[64:65] op_sel_hi:[1,1,0]\n\t" \
    "v_permlane16_swap_b32 v72, v76\n\t" \
    "v_pk_fma_f32 v[4:5], v[4:5], s[90:91], v[66:67] op_sel_hi:[1,1,0] neg_lo:[1,0,0] neg_hi:[1,0,0]\n\t" \
    "v_pk_fma_f32 v[38:39], v[6:7], s[90:91], v[64:65] op_sel_hi:[1,1,0]\n\t" \
    "v_max_f32 v74, v72, v76\n\t" \
    "v_pk_fma_f32 v[6:7], v[6:7], s[90:91], v[66:67] op_sel_hi:[1,1,0] neg_lo:[1,0,0] neg_hi:[1,0,0]\n\t" \
    "v_exp_f32 v32, v32\n\t" \
    "v_mul_f32 v72, 0xbfb8aa3b, v74\n\t" \
    "v_mul_f32 v76, 0xbfb8aa3b, v74\n\t" \
    "v_exp_f32 v0, v0\n\t" \
    "v_exp_f32 v33, v33\n\t" \
    "v_permlane16_swap_b32 v72, v76\n\t" \
    "v_exp_f32 v1, v1\n\t" \
    "v_exp_f32 v34, v34\n\t" \
    "v_mov_b32 v74, v72\n\t" \
    "v_mov_b32 v78, v76\n\t" \
    "v_exp_f32 v2, v2\n\t" \
    "v_permlane32_swap_b32 v72, v74\n\t" \
    "v_permlane32_swap_b32 v76, v78\n\t" \
    "v_exp_f32 v35, v35\n\t" \
    "v_exp_f32 v3, v3\n\t" \
    "v_exp_f32 v36, v36\n\t" \
    "v_pk_add_f32 v[64:65], v[32:33], v[34:35]\n\t" \
    "v_pk_add_f32 v[66:67], v[0:1], v[2:3]\n\t" \
    "v_exp_f32 v4, v4\n\t" \
    "v_exp_f32 v37, v37\n\t" \
    "v_exp_f32 v5, v5\n\t" \
    "v_exp_f32 v38, v38\n\t" \
    "v_pk_add_f32 v[64:65], v[64:65], v[36:37]\n\t" \
    "v_pk_add_f32 v[66:67], v[66:67], v[4:5]\n\t" \
    "v_exp_f32 v6, v6\n\t" \
    "v_exp_f32 v39, v39\n\t" \
    "v_exp_f32 v7, v7\n\t" \
    "v_pk_fma_f32 v[40:41], v[8:9], s[90:91], v[68:69] op_sel_hi:[1,1,0]\n\t" \
    "v_pk_add_f32 v[64:65], v[64:65], v[38:39]\n\t" \
    "v_pk_add_f32 v[66:67], v[66:67], v[6:7]\n\t" \
    "v_pk_fma_f32 v[8:9], v[8:9], s[90:91], v[70:71] op_sel_hi:[1,1,0] neg_lo:[1,0,0] neg_hi:[1,0,0]\n\t" \
    "v_add_f32 v64, v64, v65\n\t" \
    "v_add_f32 v66, v66, v67\n\t" \
    "v_pk_fma_f32 v[42:43], v[10:11], s[90:91], v[68:69] op_sel_hi:[1,1,0]\n\t" \
    "v_pk_fma_f32 v[10:11], v[10:11], s[90:91], v[70:71] op_sel_hi:[1,1,0] neg_lo:[1,0,0] neg_hi:[1,0,0]\n\t" \
    "v_permlane32_swap_b32 v64, v66\n\t" \
    "v_pk_fma_f32 v[44:45], v[12:13], s[90:91], v[68:69] op_sel_hi:[1,1,0]\n\t" \
    "v_pk_fma_f32 v[12:13], v[12:13], s[90:91], v[70:71] op_sel_hi:[1,1,0] neg_lo:[1,0,0] neg_hi:[1,0,0]\n\t" \
    "v_add_f32 v64, v64, v66\n\t" \
    "v_pk_fma_f32 v[46:47], v[14:15], s[90:91], v[68:69] op_sel_hi:[1,1,0]\n\t" \
    "v_pk_fma_f32 v[14:15], v[14:15], s[90:91], v[70:71] op_sel_hi:[1,1,0] neg_lo:[1,0,0] neg_hi:[1,0,0]\n\t" \
    "v_exp_f32 v40, v40\n\t" \
    "v_exp_f32 v8, v8\n\t" \
    "v_exp_f32 v41, v41\n\t" \
    "v_exp_f32 v9, v9\n\t" \
    "v_exp_f32 v42, v42\n\t" \
    "v_exp_f32 v10, v10\n\t" \
    "v_exp_f32 v43, v43\n\t" \
    "v_exp_f32 v11, v11\n\t" \
    "v_exp_f32 v44, v44\n\t" \
    "v_pk_add_f32 v[68:69], v[40:41], v[42:43]\n\t" \
    "v_pk_add_f32 v[70:71], v[8:9], v[10:11]\n\t" \
    "v_exp_f32 v12, v12\n\t" \
    "v_exp_f32 v45, v45\n\t" \
    "v_exp_f32 v13, v13\n\t" \
    "v_exp_f32 v46, v46\n\t" \
    "v_pk_add_f32 v[68:69], v[68:69], v[44:45]\n\t" \
    "v_pk_add_f32 v[70:71], v[70:71], v[12:13]\n\t" \
    "v_exp_f32 v14, v14\n\t" \
    "v_exp_f32 v47, v47\n\t" \
    "v_exp_f32 v15, v15\n\t" \
    "v_pk_fma_f32 v[48:49], v[16:17], s[90:91], v[72:73] op_sel_hi:[1,1,0]\n\t" \
    "v_pk_add_f32 v[68:69], v[68:69], v[46:47]\n\t" \
    "v_pk_add_f32 v[70:71], v[70:71], v[14:15]\n\t" \
    "v_pk_fma_f32 v[16:17], v[16:17], s[90:91], v[74:75] op_sel_hi:[1,1,0] neg_lo:[1,0,0] neg_hi:[1,0,0]\n\t" \
    "v_add_f32 v68, v68, v69\n\t" \
    "v_add_f32 v70, v70, v71\n\t" \
    "v_pk_fma_f32 v[50:51], v[18:19], s[90:91], v[72:73] op_sel_hi:[1,1,0]\n\t" \
    "v_pk_fma_f32 v[18:19], v[18:19], s[90:91], v[74:75] op_sel_hi:[1,1,0] neg_lo:[1,0,0] neg_hi:[1,0,0]\n\t" \
    "v_permlane32_swap_b32 v68, v70\n\t" \
    "v_pk_fma_f32 v[52:53], v[20:21], s[90:91], v[72:73] op_sel_hi:[1,1,0]\n\t" \
    "v_pk_fma_f32 v[20:21], v[20:21], s[90:91], v[74:75] op_sel_hi:[1,1,0] neg_lo:[1,0,0] neg_hi:[1,0,0]\n\t" \
    "v_add_f32 v68, v68, v70\n\t" \
    "v_pk_fma_f32 v[54:55], v[22:23], s[90:91], v[72:73] op_sel_hi:[1,1,0]\n\t" \
    "v_pk_fma_f32 v[22:23], v[22:23], s[90:91], v[74:75] op_sel_hi:[1,1,0] neg_lo:[1,0,0] neg_hi:[1,0,0]\n\t" \
    "v_permlane16_swap_b32 v64, v68\n\t" \
    "v_exp_f32 v48, v48\n\t" \
    "v_exp_f32 v16, v16\n\t" \
    "v_add_f32 v66, v64, v68\n\t" \
    "v_exp_f32 v49, v49\n\t" \
    "v_exp_f32 v17, v17\n\t" \
    "v_rcp_f32 v64, v66\n\t" \
    "v_exp_f32 v50, v50\n\t" \
    "v_exp_f32 v18, v18\n\t" \
    "v_mov_b32 v68, v64\n\t" \
    "v_exp_f32 v51, v51\n\t" \
    "v_exp_f32 v19, v19\n\t" \
    "v_permlane16_swap_b32 v64, v68\n\t" \
    "v_pk_add_f32 v[72:73], v[48:49], v[50:51]\n\t" \
    "v_pk_add_f32 v[74:75], v[16:17], v[18:19]\n\t" \
    "v_mov_b32 v66, v64\n\t" \
    "v_mov_b32 v70, v68\n\t" \
    "v_exp_f32 v52, v52\n\t" \
    "v_permlane32_swap_b32 v64, v66\n\t" \
    "v_permlane32_swap_b32 v68, v70\n\t" \
    "v_exp_f32 v20, v20\n\t" \
    "v_exp_f32 v53, v53\n\t" \
    "v_exp_f32 v21, v21\n\t" \
    "v_exp_f32 v54, v54\n\t" \
    "v_pk_add_f32 v[72:73], v[72:73], v[52:53]\n\t" \
    "v_pk_add_f32 v[74:75], v[74:75], v[20:21]\n\t" \
    "v_exp_f32 v22, v22\n\t" \
    "v_exp_f32 v55, v55\n\t" \
    "v_exp_f32 v23, v23\n\t" \
    "v_pk_fma_f32 v[56:57], v[24:25], s[90:91], v[76:77] op_sel_hi:[1,1,0]\n\t" \
    "v_pk_add_f32 v[72:73], v[72:73], v[54:55]\n\t" \
    "v_pk_add_f32 v[74:75], v[74:75], v[22:23]\n\t" \
    "v_pk_fma_f32 v[24:25], v[24:25], s[90:91], v[78:79] op_sel_hi:[1,1,0] neg_lo:[1,0,0] neg_hi:[1,0,0]\n\t" \
    "v_add_f32 v72, v72, v73\n\t" \
    "v_add_f32 v74, v74, v75\n\t" \
    "v_pk_fma_f32 v[58:59], v[26:27], s[90:91], v[76:77] op_sel_hi:[1,1,0]\n\t" \
    "v_pk_fma_f32 v[26:27], v[26:27], s[90:91], v[78:79] op_sel_hi:[1,1,0] neg_lo:[1,0,0] neg_hi:[1,0,0]\n\t" \
    "v_permlane32_swap_b32 v72, v74\n\t" \
    "v_pk_fma_f32 v[60:61], v[28:29], s[90:91], v[76:77] op_sel_hi:[1,1,0]\n\t" \
    "v_pk_fma_f32 v[28:29], v[28:29], s[90:91], v[78:79] op_sel_hi:[1,1,0] neg_lo:[1,0,0] neg_hi:[1,0,0]\n\t" \
    "v_add_f32 v72, v72, v74\n\t" \
    "v_pk_fma_f32 v[62:63], v[30:31], s[90:91], v[76:77] op_sel_hi:[1,1,0]\n\t" \
    "v_pk_fma_f32 v[30:31], v[30:31], s[90:91], v[78:79] op_sel_hi:[1,1,0] neg_lo:[1,0,0] neg_hi:[1,0,0]\n\t" \
    "v_exp_f32 v56, v56\n\t" \
    "v_exp_f32 v24, v24\n\t" \
    "v_exp_f32 v57, v57\n\t" \
    "v_exp_f32 v25, v25\n\t" \
    "v_exp_f32 v58, v58\n\t" \
    "v_exp_f32 v26, v26\n\t" \
    "v_exp_f32 v59, v59\n\t" \
    "v_exp_f32 v27, v27\n\t" \
    "v_exp_f32 v60, v60\n\t" \
    "v_pk_add_f32 v[76:77], v[56:57], v[58:59]\n\t" \
    "v_pk_add_f32 v[78:79], v[24:25], v[26:27]\n\t" \
    "v_exp_f32 v28, v28\n\t" \
    "v_exp_f32 v61, v61\n\t" \
    "v_exp_f32 v29, v29\n\t" \
    "v_exp_f32 v62, v62\n\t" \
    "v_pk_add_f32 v[76:77], v[76:77], v[60:61]\n\t" \
    "v_pk_add_f32 v[78:79], v[78:79], v[28:29]\n\t" \
    "v_exp_f32 v30, v30\n\t" \
    "v_exp_f32 v63, v63\n\t" \
    "v_exp_f32 v31, v31\n\t" \
    "v_pk_mul_f32 v[0:1], v[0:1], v[66:67] op_sel_hi:[1,0]\n\t" \
    "v_pk_add_f32 v[76:77], v[76:77], v[62:63]\n\t" \
    "v_pk_add_f32 v[78:79], v[78:79], v[30:31]\n\t" \
    "v_pk_mul_f32 v[32:33], v[32:33], v[64:65] op_sel_hi:[1,0]\n\t" \
    "v_add_f32 v76, v76, v77\n\t" \
    "v_add_f32 v78, v78, v79\n\t" \
    "v_pk_mul_f32 v[2:3], v[2:3], v[66:67] op_sel_hi:[1,0]\n\t" \
    "v_pk_mul_f32 v[34:35], v[34:35], v[64:65] op_sel_hi:[1,0]\n\t" \
    "v_permlane32_swap_b32 v76, v78\n\t" \
    "v_pk_mul_f32 v[4:5], v[4:5], v[66:67] op_sel_hi:[1,0]\n\t" \
    "v_pk_mul_f32 v[36:37], v[36:37], v[64:65] op_sel_hi:[1,0]\n\t" \
    "v_add_f32 v76, v76, v78\n\t" \
    "v_pk_mul_f32 v[6:7], v[6:7], v[66:67] op_sel_hi:[1,0]\n\t" \
    "v_pk_mul_f32 v[38:39], v[38:39], v[64:65] op_sel_hi:[1,0]\n\t" \
    "v_permlane16_swap_b32 v72, v76\n\t" \
    "v_cvt_pk_bf16_f32 v7, v6, v7\n\t" \
    "v_cvt_pk_bf16_f32 v6, v4, v5\n\t" \
    "v_add_f32 v74, v72, v76\n\t" \
    "v_cvt_pk_bf16_f32 v5, v2, v3\n\t" \
    "v_cvt_pk_bf16_f32 v4, v0, v1\n\t" \
    "v_rcp_f32 v72, v74\n\t" \
    "v_cvt_pk_bf16_f32 v0, v32, v33\n\t" \
    "v_cvt_pk_bf16_f32 v1, v34, v35\n\t" \
    "v_mov_b32 v76, v72\n\t" \
    "v_cvt_pk_bf16_f32 v2, v36, v37\n\t" \
    "v_cvt_pk_bf16_f32 v3, v38, v39\n\t" \
    "v_permlane16_swap_b32 v72, v76\n\t" \
    "v_pk_mul_f32 v[8:9], v[8:9], v[70:71] op_sel_hi:[1,0]\n\t" \
    "v_pk_mul_f32 v[40:41], v[40:41], v[68:69] op_sel_hi:[1,0]\n\t" \
    "v_mov_b32 v74, v72\n\t" \
    "v_mov_b32 v78, v76\n\t" \
    "v_pk_mul_f32 v[10:11], v[10:11], v[70:71] op_sel_hi:[1,0]\n\t" \
    "v_permlane32_swap_b32 v72, v74\n\t" \
    "v_permlane32_swap_b32 v76, v78\n\t" \
    "v_pk_mul_f32 v[42:43], v[42:43], v[68:69] op_sel_hi:[1,0]\n\t" \
    "v_pk_mul_f32 v[12:13], v[12:13], v[70:71] op_sel_hi:[1,0]\n\t" \
    "v_pk_mul_f32 v[44:45], v[44:45], v[68:69] op_sel_hi:[1,0]\n\t" \
    "v_pk_mul_f32 v[14:15], v[14:15], v[70:71] op_sel_hi:[1,0]\n\t" \
    "v_pk_mul_f32 v[46:47], v[46:47], v[68:69] op_sel_hi:[1,0]\n\t" \
    "v_pk_mul_f32 v[16:17], v[16:17], v[74:75] op_sel_hi:[1,0]\n\t" \
    "v_cvt_pk_bf16_f32 v15, v14, v15\n\t" \
    "v_cvt_pk_bf16_f32 v14, v12, v13\n\t" \
    "v_cvt_pk_bf16_f32 v13, v10, v11\n\t" \
    "v_cvt_pk_bf16_f32 v12, v8, v9\n\t" \
    "v_cvt_pk_bf16_f32 v8, v40, v41\n\t" \
    "v_cvt_pk_bf16_f32 v9, v42, v43\n\t" \
    "v_cvt_pk_bf16_f32 v10, v44, v45\n\t" \
    "v_cvt_pk_bf16_f32 v11, v46, v47\n\t" \
    "v_pk_mul_f32 v[48:49], v[48:49], v[72:73] op_sel_hi:[1,0]\n\t" \
    "v_pk_mul_f32 v[18:19], v[18:19], v[74:75] op_sel_hi:[1,0]\n\t" \
    "v_pk_mul_f32 v[50:51], v[50:51], v[72:73] op_sel_hi:[1,0]\n\t" \
    "v_pk_mul_f32 v[20:21], v[20:21], v[74:75] op_sel_hi:[1,0]\n\t" \
    "v_pk_mul_f32 v[52:53], v[52:53], v[72:73] op_sel_hi:[1,0]\n\t" \
    "v_pk_mul_f32 v[22:23], v[22:23], v[74:75] op_sel_hi:[1,0]\n\t" \
    "v_pk_mul_f32 v[54:55], v[54:55], v[72:73] op_sel_hi:[1,0]\n\t" \
    "ds_read_b32 v237, v249 offset:8\n\t" \
    "v_pk_mul_f32 v[24:25], v[24:25], v[78:79] op_sel_hi:[1,0]\n\t" \
    "v_cvt_pk_bf16_f32 v23, v22, v23\n\t" \
    "v_cvt_pk_bf16_f32 v22, v20, v21\n\t" \
    "v_cvt_pk_bf16_f32 v21, v18, v19\n\t" \
    "v_cvt_pk_bf16_f32 v20, v16, v17\n\t" \
    "v_cvt_pk_bf16_f32 v16, v48, v49\n\t" \
    "v_cvt_pk_bf16_f32 v17, v50, v51\n\t" \
    "v_cvt_pk_bf16_f32 v18, v52, v53\n\t" \
    "v_cvt_pk_bf16_f32 v19, v54, v55\n\t" \
    "v_pk_mul_f32 v[56:57], v[56:57], v[76:77] op_sel_hi:[1,0]\n\t" \
    "v_pk_mul_f32 v[26:27], v[26:27], v[78:79] op_sel_hi:[1,0]\n\t" \
    "v_pk_mul_f32 v[58:59], v[58:59], v[76:77] op_sel_hi:[1,0]\n\t" \
    "v_pk_mul_f32 v[28:29], v[28:29], v[78:79] op_sel_hi:[1,0]\n\t" \
    "v_pk_mul_f32 v[60:61], v[60:61], v[76:77] op_sel_hi:[1,0]\n\t" \
    "v_pk_mul_f32 v[30:31], v[30:31], v[78:79] op_sel_hi:[1,0]\n\t" \
    "v_pk_mul_f32 v[62:63], v[62:63], v[76:77] op_sel_hi:[1,0]\n\t" \
    "v_cvt_pk_bf16_f32 v31, v30, v31\n\t" \
    "v_cvt_pk_bf16_f32 v30, v28, v29\n\t" \
    "v_cvt_pk_bf16_f32 v29, v26, v27\n\t" \
    "v_cvt_pk_bf16_f32 v28, v24, v25\n\t" \
    "v_cvt_pk_bf16_f32 v24, v56, v57\n\t" \
    "v_cvt_pk_bf16_f32 v25, v58, v59\n\t" \
    "v_cvt_pk_bf16_f32 v26, v60, v61\n\t" \
    "v_cvt_pk_bf16_f32 v27, v62, v63\n\t" \
    "s_setprio 2\n\t" \
    "s_waitcnt lgkmcnt(0)\n\t" \
    "v_readfirstlane_b32 s85, v237\n\t" \
    "s_add_u32 s86, s87, 0x10000\n\t" \
    "s_cmp_lt_u32 s85, 3\n\t" \
    "s_cbranch_scc1 .Lab_s7\n\t" \
    ".Lab_s7r:\n\t" \
    "s_mov_b32 s89, 0x400000\n\t" \
    "s_mov_b32 s82, 4\n\t" \
    ".Lab_k:\n\t" \
    "ds_read_b32 v237, v249 offset:8\n\t" \
    "ds_read_b128 v[32:35], v252\n\t" \
    "ds_read_b128 v[36:39], v252 offset:256\n\t" \
    "ds_read_b128 v[40:43], v253\n\t" \
    "ds_read_b128 v[44:47], v253 offset:1024\n\t" \
    "ds_read_b128 v[48:51], v253 offset:2048\n\t" \
    "ds_read_b128 v[52:55], v253 offset:3072\n\t" \
    "s_sub_u32 s89, s89, 1\n\t" \
    "s_cbranch_scc1 .Lab_kr\n\t" \
    "s_waitcnt lgkmcnt(6)\n\t" \
    "v_readfirstlane_b32 s85, v237\n\t" \
    "s_cmp_lt_u32 s85, s82\n\t" \
    "s_cbranch_scc1 .Lab_k\n\t" \
    ".Lab_kr:\n\t" \
    "s_waitcnt lgkmcnt(5)\n\t" \
    "v_mfma_f32_16x16x32_bf16 v[56:59], v[32:35], v[0:3], v[120:123]\n\t" \
    "ds_read_b32 v126, v249 offset:296\n\t" \
    "v_mfma_f32_16x16x32_bf16 v[60:63], v[32:35], v[8:11], v[120:123]\n\t" \
    "ds_read_b32 v127, v249 offset:308\n\t" \
    "v_mfma_f32_16x16x32_bf16 v[64:67], v[32:35], v[16:19], v[120:123]\n\t" \
    "v_cmp_eq_u32 vcc, 0, v244\n\t" \
    "v_mfma_f32_16x16x32_bf16 v[68:71], v[32:35], v[24:27], v[120:123]\n\t" \
    "v_lshrrev_b32 v237, 1, v251\n\t" \
    "v_bfrev_b32 v238, 1\n\t" \
    "s_waitcnt lgkmcnt(6)\n\t" \
    "v_mfma_f32_16x16x32_bf16 v[56:59], v[36:39], v[4:7], v[56:59]\n\t" \
    "v_mfma_f32_16x16x32_bf16 v[60:63], v[36:39], v[12:15], v[60:63]\n\t" \
    "v_cndmask_b32 v254, v238, v237, vcc\n\t" \
    "v_mfma_f32_16x16x32_bf16 v[64:67], v[36:39], v[20:23], v[64:67]\n\t" \
    "s_or_b32 s82, s85, 1\n\t" \
    "v_mfma_f32_16x16x32_bf16 v[68:71], v[36:39], v[28:31], v[68:71]\n\t" \
    "s_waitcnt lgkmcnt(5)\n\t" \
    "v_mfma_f32_16x16x32_bf16 v[72:75], v[40:43], v[0:3], 0\n\t" \
    "s_waitcnt lgkmcnt(4)\n\t" \
    "v_mfma_f32_16x16x32_bf16 v[76:79], v[44:47], v[0:3], 0\n\t" \
    "s_waitcnt lgkmcnt(3)\n\t" \
    "v_mfma_f32_16x16x32_bf16 v[72:75], v[48:51], v[4:7], v[72:75]\n\t" \
    "s_waitcnt lgkmcnt(2)\n\t" \
    "v_mfma_f32_16x16x32_bf16 v[76:79], v[52:55], v[4:7], v[76:79]\n\t" \
    "v_mfma_f32_16x16x32_bf16 v[80:83], v[40:43], v[8:11], 0\n\t" \
    "v_rcp_f32 v56, v56\n\t" \
    "v_mfma_f32_16x16x32_bf16 v[84:87], v[44:47], v[8:11], 0\n\t" \
    "v_rcp_f32 v60, v60\n\t" \
    "v_mfma_f32_16x16x32_bf16 v[80:83], v[48:51], v[12:15], v[80:83]\n\t" \
    "v_rcp_f32 v64, v64\n\t" \
    "v_mfma_f32_16x16x32_bf16 v[84:87], v[52:55], v[12:15], v[84:87]\n\t" \
    "v_rcp_f32 v68, v68\n\t" \
    "v_mfma_f32_16x16x32_bf16 v[88:91], v[40:43], v[16:19], 0\n\t" \
    "v_mfma_f32_16x16x32_bf16 v[92:95], v[44:47], v[16:19], 0\n\t" \
    "v_mul_f32 v72, v72, v56\n\t" \
    "v_mul_f32 v73, v73, v56\n\t" \
    "v_mfma_f32_16x16x32_bf16 v[88:91], v[48:51], v[20:23], v[88:91]\n\t" \
    "v_mul_f32 v74, v74, v56\n\t" \
    "v_mul_f32 v75, v75, v56\n\t" \
    "v_mfma_f32_16x16x32_bf16 v[92:95], v[52:55], v[20:23], v[92:95]\n\t" \
    "v_mul_f32 v76, v76, v56\n\t" \
    "v_mul_f32 v77, v77, v56\n\t" \
    "v_mfma_f32_16x16x32_bf16 v[96:99], v[40:43], v[24:27], 0\n\t" \
    "v_mul_f32 v78, v78, v56\n\t" \
    "v_mul_f32 v79, v79, v56\n\t" \
    "v_mfma_f32_16x16x32_bf16 v[100:103], v[44:47], v[24:27], 0\n\t" \
    "v_cvt_pk_bf16_f32 v104, v72, v73\n\t" \
    "v_cvt_pk_bf16_f32 v105, v74, v75\n\t" \
    "v_mfma_f32_16x16x32_bf16 v[96:99], v[48:51], v[28:31], v[96:99]\n\t" \
    "v_cvt_pk_bf16_f32 v106, v76, v77\n\t" \
    "v_cvt_pk_bf16_f32 v107, v78, v79\n\t" \
    "v_mfma_f32_16x16x32_bf16 v[100:103], v[52:55], v[28:31], v[100:103]\n\t" \
    "buffer_store_dwordx4 v[104:107], v251, %[rsb], s87 offen sc1\n\t" \
    "v_pk_mul_f32 v[80:81], v[80:81], v[60:61] op_sel_hi:[1,0]\n\t" \
    "v_pk_mul_f32 v[82:83], v[82:83], v[60:61] op_sel_hi:[1,0]\n\t" \
    "v_pk_mul_f32 v[84:85], v[84:85], v[60:61] op_sel_hi:[1,0]\n\t" \
    "v_pk_mul_f32 v[86:87], v[86:87], v[60:61] op_sel_hi:[1,0]\n\t" \
    "v_cvt_pk_bf16_f32 v108, v80, v81\n\t" \
    "v_cvt_pk_bf16_f32 v109, v82, v83\n\t" \
    "v_cvt_pk_bf16_f32 v110, v84, v85\n\t" \
    "v_cvt_pk_bf16_f32 v111, v86, v87\n\t" \
    "buffer_store_dwordx4 v[108:111], v251, %[rsb], s87 offen offset:1024 sc1\n\t" \
    "v_pk_mul_f32 v[88:89], v[88:89], v[64:65] op_sel_hi:[1,0]\n\t" \
    "v_pk_mul_f32 v[90:91], v[90:91], v[64:65] op_sel_hi:[1,0]\n\t" \
    "v_pk_mul_f32 v[92:93], v[92:93], v[64:65] op_sel_hi:[1,0]\n\t" \
    "v_pk_mul_f32 v[94:95], v[94:95], v[64:65] op_sel_hi:[1,0]\n\t" \
    "v_cvt_pk_bf16_f32 v112, v88, v89\n\t" \
    "v_cvt_pk_bf16_f32 v113, v90, v91\n\t" \
    "v_cvt_pk_bf16_f32 v114, v92, v93\n\t" \
    "v_cvt_pk_bf16_f32 v115, v94, v95\n\t" \
    "buffer_store_dwordx4 v[112:115], v251, %[rsb], s86 offen sc1\n\t" \
    "v_pk_mul_f32 v[96:97], v[96:97], v[68:69] op_sel_hi:[1,0]\n\t" \
    "v_pk_mul_f32 v[98:99], v[98:99], v[68:69] op_sel_hi:[1,0]\n\t" \
    "v_pk_mul_f32 v[100:101], v[100:101], v[68:69] op_sel_hi:[1,0]\n\t" \
    "v_pk_mul_f32 v[102:103], v[102:103], v[68:69] op_sel_hi:[1,0]\n\t" \
    "v_cvt_pk_bf16_f32 v116, v96, v97\n\t" \
    "v_cvt_pk_bf16_f32 v117, v98, v99\n\t" \
    "v_cvt_pk_bf16_f32 v118, v100, v101\n\t" \
    "v_cvt_pk_bf16_f32 v119, v102, v103\n\t" \
    "v_add3_u32 v32, v104, v105, 0\n\t" \
    "v_add3_u32 v32, v106, v107, v32\n\t" \
    "v_add3_u32 v33, v112, v113, 0\n\t" \
    "v_add3_u32 v33, v114, v115, v33\n\t" \
    "v_add3_u32 v32, v108, v109, v32\n\t" \
    "v_add3_u32 v32, v110, v111, v32\n\t" \
    "v_add3_u32 v33, v116, v117, v33\n\t" \
    "v_add3_u32 v33, v118, v119, v33\n\t" \
    "buffer_store_dwordx4 v[116:119], v251, %[rsb], s86 offen offset:1024 sc1\n\t" \
    "ds_read_b32 v36, v249 offset:8\n\t" \
    "v_permlane32_swap_b32 v32, v33\n\t" \
    "v_add_u32 v32, v32, v33\n\t" \
    "v_mov_b32 v33, v32\n\t" \
    "s_waitcnt lgkmcnt(2)\n\t" \
    "v_readfirstlane_b32 %[rid], v126\n\t" \
    "s_waitcnt lgkmcnt(1)\n\t" \
    "v_readfirstlane_b32 %[t3], v127\n\t" \
    "v_permlane16_swap_b32 v32, v33\n\t" \
    "v_add_u32 v32, v32, v33\n\t" \
    "v_mov_b32 v33, v236\n\t" \
    "ds_read_b32 v238, v249 offset:56\n\t" \
    "v_add_u32_dpp v32, v32, v32 row_ror:8 row_mask:0xf bank_mask:0xf bound_ctrl:1\n\t" \
    "v_mov_b32 v237, 12\n\t" \
    "v_and_b32 v34, 15, v250\n\t" \
    "v_add_u32_dpp v32, v32, v32 row_ror:4 row_mask:0xf bank_mask:0xf bound_ctrl:1\n\t" \
    "s_waitcnt lgkmcnt(1)\n\t" \
    "v_readfirstlane_b32 s80, v36\n\t" \
    "s_and_b32 s85, %[rid], 15\n\t" \
    "v_add_u32_dpp v32, v32, v32 row_ror:2 row_mask:0xf bank_mask:0xf bound_ctrl:1\n\t" \
    "v_cmp_eq_u32_e64 %[rm], s85, v34\n\t" \
    "s_cmp_lg_u32 s80, s82\n\t" \
    "v_add_u32_dpp v32, v32, v32 row_ror:1 row_mask:0xf bank_mask:0xf bound_ctrl:1\n\t" \
    "s_cbranch_scc1 .Lab_sa\n\t" \
    ".Lab_sar:\n\t" \
    "buffer_store_dwordx2 v[32:33], v254, %[rsb], s88 offen sc1\n\t" \
    "ds_write_b32 v248, v237\n\t" \
    "v_and_b32 v252, 48, v250\n\t" \
    "s_lshr_b32 s85, %[rid], 5\n\t" \
    "v_add_u32 v252, 161536, v252\n\t" \
    "s_cmp_eq_u32 s85, %[own]\n\t" \
    "s_cselect_b64 %[rm], %[rm], 0\n\t" \
    "s_waitcnt vmcnt(5)\n\t" \
    "v_cmp_ne_u32 vcc, v236, v233\n\t" \
    "v_cmp_ne_u32_e64 s[92:93], v236, v235\n\t" \
    "s_nop 0\n\t" \
    "s_or_b64 vcc, vcc, s[92:93]\n\t" \
    "s_cbranch_scc1 .Lab_s8\n\t" \
    ".Lab_s8r:\n\t" \
    "v_sub_f32 v254, 1.0, v232\n\t" \
    "v_sub_f32 v255, 1.0, v234\n\t" \
    "v_cvt_pk_bf16_f32 v255, v254, v255\n\t" \
    "v_lshlrev_b32 v254, 16, v255\n\t" \
    "v_and_b32 v255, 0xffff0000, v255\n\t" \
    "s_waitcnt lgkmcnt(1)\n\t" \
    "v_readfirstlane_b32 s85, v238\n\t" \
    "s_cmp_lt_u32 s85, 2\n\t" \
    "s_cbranch_scc1 .Lab_s9\n\t" \
    ".Lab_s9r:\n\t" \
    "s_waitcnt lgkmcnt(0)\n\t"
#define PHASE_C_ASM \
    "s_branch .Lpad_c0\n\t" \
    ".Lc_x:\n\t" \
    "s_waitcnt vmcnt(0) lgkmcnt(0)\n\t" \
    "s_branch .Lc_end\n\t" \
    ".Lc_w0:\n\t" \
    "s_mov_b32 s86, 0x400000\n\t" \
    ".Lc_a0:\n\t" \
    "ds_read_b32 v160, v249 offset:56\n\t" \
    "s_waitcnt lgkmcnt(0)\n\t" \
    "v_readfirstlane_b32 s83, v160\n\t" \
    "s_sub_u32 s86, s86, 1\n\t" \
    "s_cbranch_scc1 .Lc_r0\n\t" \
    "s_cmp_lt_u32 s83, s82\n\t" \
    "s_cbranch_scc1 .Lc_a0\n\t" \
    "s_branch .Lc_r0\n\t" \
    ".Lc_w1:\n\t" \
    "s_mov_b32 s86, 0x400000\n\t" \
    ".Lc_a1:\n\t" \
    "ds_read_b32 v160, v249 offset:56\n\t" \
    "s_waitcnt lgkmcnt(0)\n\t" \
    "v_readfirstlane_b32 s83, v160\n\t" \
    "s_sub_u32 s86, s86, 1\n\t" \
    "s_cbranch_scc1 .Lc_r1\n\t" \
    "s_cmp_lt_u32 s83, s82\n\t" \
    "s_cbranch_scc1 .Lc_a1\n\t" \
    "s_branch .Lc_r1\n\t" \
    ".Lc_y:\n\t" \
    "s_mov_b32 s86, 0x400000\n\t" \
    ".Lc_ya:\n\t" \
    "s_waitcnt lgkmcnt(0)\n\t" \
    "v_readfirstlane_b32 s83, v231\n\t" \
    "s_sub_u32 s86, s86, 1\n\t" \
    "s_cbranch_scc1 .Lc_yb\n\t" \
    "s_cmp_lg_u32 s83, 0\n\t" \
    "s_cbranch_scc1 .Lc_yb\n\t" \
    "ds_read_b32 v231, v249 offset:68\n\t" \
    "ds_read_b128 v[96:99], v252\n\t" \
    "ds_read_b128 v[100:103], v252 offset:64\n\t" \
    "ds_read_b128 v[104:107], v252 offset:128\n\t" \
    "ds_read_b128 v[108:111], v252 offset:192\n\t" \
    "s_branch .Lc_ya\n\t" \
    ".Lc_yb:\n\t" \
    "s_mov_b64 exec, %[rm]\n\t" \
    "s_bitcmp1_b32 %[rid], 4\n\t" \
    "s_cbranch_scc1 .Lc_yc\n\t" \
    "v_pk_mul_f32 v[194:195], v[96:97], v[254:255] op_sel_hi:[1,0]\n\t" \
    "v_pk_mul_f32 v[196:197], v[98:99], v[254:255] op_sel_hi:[1,0]\n\t" \
    "v_pk_mul_f32 v[198:199], v[100:101], v[254:255] op_sel_hi:[1,0]\n\t" \
    "v_pk_mul_f32 v[200:201], v[102:103], v[254:255] op_sel_hi:[1,0]\n\t" \
    "v_pk_mul_f32 v[202:203], v[104:105], v[254:255] op_sel_hi:[1,0]\n\t" \
    "v_pk_mul_f32 v[204:205], v[106:107], v[254:255] op_sel_hi:[1,0]\n\t" \
    "v_pk_mul_f32 v[206:207], v[108:109], v[254:255] op_sel_hi:[1,0]\n\t" \
    "v_pk_mul_f32 v[208:209], v[110:111], v[254:255] op_sel_hi:[1,0]\n\t" \
    "s_mov_b64 exec, -1\n\t" \
    "s_branch .Lc_yr\n\t" \
    ".Lc_yc:\n\t" \
    "v_pk_mul_f32 v[210:211], v[96:97], v[254:255] op_sel:[0,1] op_sel_hi:[1,1]\n\t" \
    "v_pk_mul_f32 v[212:213], v[98:99], v[254:255] op_sel:[0,1] op_sel_hi:[1,1]\n\t" \
    "v_pk_mul_f32 v[214:215], v[100:101], v[254:255] op_sel:[0,1] op_sel_hi:[1,1]\n\t" \
    "v_pk_mul_f32 v[216:217], v[102:103], v[254:255] op_sel:[0,1] op_sel_hi:[1,1]\n\t" \
    "v_pk_mul_f32 v[218:219], v[104:105], v[254:255] op_sel:[0,1] op_sel_hi:[1,1]\n\t" \
    "v_pk_mul_f32 v[220:221], v[106:107], v[254:255] op_sel:[0,1] op_sel_hi:[1,1]\n\t" \
    "v_pk_mul_f32 v[222:223], v[108:109], v[254:255] op_sel:[0,1] op_sel_hi:[1,1]\n\t" \
    "v_pk_mul_f32 v[224:225], v[110:111], v[254:255] op_sel:[0,1] op_sel_hi:[1,1]\n\t" \
    "s_mov_b64 exec, -1\n\t" \
    "s_branch .Lc_yr\n\t" \
    ".p2align 12\n\t" \
    ".Lpad_c0:\n\t" \
    "s_cbranch_execz .Lpad_ret\n\t" \
    "s_setprio 2\n\t" \
    "v_xor_b32 v226, %[flip], v245\n\t" \
    "v_xor_b32 v227, 32, v226\n\t" \
    "v_xor_b32 v228, 64, v226\n\t" \
    "v_xor_b32 v229, 96, v226\n\t" \
    "v_and_b32 v231, 0x7f, v244\n\t" \
    "s_lshl_b32 s80, %[grp], 14\n\t" \
    "s_lshl_b32 s83, %[grp], 3\n\t" \
    "s_lshl_b32 s84, 0xffff, s83\n\t" \
    "s_lshl_b32 s85, %[grp], 6\n\t" \
    "s_add_u32 s85, s85, %[sog]\n\t" \
    "s_mov_b32 s81, 256\n\t" \
    "v_xor_b32 v1, 32, v246\n\t" \
    "v_xor_b32 v2, 64, v246\n\t" \
    "v_xor_b32 v3, 96, v246\n\t" \
    ".Lc_t:\n\t" \
    "s_memrealtime s[86:87]\n\t" \
    "s_sub_u32 s81, s81, 1\n\t" \
    "s_cbranch_scc1 .Lc_go\n\t" \
    "s_waitcnt lgkmcnt(0)\n\t" \
    "s_sub_u32 s86, %[t3], s86\n\t" \
    "s_cmp_lt_i32 s86, 5\n\t" \
    "s_cbranch_scc1 .Lc_go\n\t" \
    "s_cmp_lt_i32 s86, 13\n\t" \
    "s_cbranch_scc1 .Lc_t\n\t" \
    "s_sleep 2\n\t" \
    "s_branch .Lc_t\n\t" \
    ".Lc_go:\n\t" \
    "buffer_load_dwordx2 v[160:161], v231, %[rsb], s85 offen sc1\n\t" \
    "buffer_load_dwordx4 v[32:35], v240, %[rsb], s80 offen offset:2048 sc1\n\t" \
    "buffer_load_dwordx4 v[36:39], v240, %[rsb], s80 offen offset:3072 sc1\n\t" \
    "buffer_load_dwordx4 v[40:43], v241, %[rsb], s80 offen sc1\n\t" \
    "buffer_load_dwordx4 v[44:47], v241, %[rsb], s80 offen offset:1024 sc1\n\t" \
    "buffer_load_dwordx4 v[48:51], v241, %[rsb], s80 offen offset:2048 sc1\n\t" \
    "buffer_load_dwordx4 v[52:55], v241, %[rsb], s80 offen offset:3072 sc1\n\t" \
    "buffer_load_dwordx4 v[56:59], v242, %[rsb], s80 offen sc1\n\t" \
    "buffer_load_dwordx4 v[60:63], v242, %[rsb], s80 offen offset:1024 sc1\n\t" \
    "buffer_load_dwordx4 v[64:67], v242, %[rsb], s80 offen offset:2048 sc1\n\t" \
    "buffer_load_dwordx4 v[68:71], v242, %[rsb], s80 offen offset:3072 sc1\n\t" \
    "buffer_load_dwordx4 v[72:75], v243, %[rsb], s80 offen sc1\n\t" \
    "buffer_load_dwordx4 v[76:79], v243, %[rsb], s80 offen offset:1024 sc1\n\t" \
    "buffer_load_dwordx4 v[80:83], v243, %[rsb], s80 offen offset:2048 sc1\n\t" \
    "buffer_load_dwordx4 v[84:87], v243, %[rsb], s80 offen offset:3072 sc1\n\t" \
    "s_add_u32 s80, s80, 0x4000\n\t" \
    "s_and_b32 s80, s80, 0xc000\n\t" \
    "ds_read_b64 v[196:197], v246\n\t" \
    "ds_read_b64 v[200:201], v1\n\t" \
    "ds_read_b64 v[204:205], v2\n\t" \
    "ds_read_b64 v[208:209], v3\n\t" \
    "ds_read_b64 v[212:213], v246 offset:2048\n\t" \
    "ds_read_b64 v[216:217], v1 offset:2048\n\t" \
    "ds_read_b64 v[220:221], v2 offset:2048\n\t" \
    "ds_read_b64 v[224:225], v3 offset:2048\n\t" \
    "ds_read_b64_tr_b16 v[96:97], v226\n\t" \
    "ds_read_b64_tr_b16 v[98:99], v226 offset:2048\n\t" \
    "ds_read_b64_tr_b16 v[100:101], v227\n\t" \
    "ds_read_b64_tr_b16 v[102:103], v227 offset:2048\n\t" \
    "ds_read_b64_tr_b16 v[104:105], v228\n\t" \
    "ds_read_b64_tr_b16 v[106:107], v228 offset:2048\n\t" \
    "ds_read_b64_tr_b16 v[108:109], v229\n\t" \
    "ds_read_b64_tr_b16 v[110:111], v229 offset:2048\n\t" \
    "ds_read_b64_tr_b16 v[112:113], v226 offset:4096\n\t" \
    "ds_read_b64_tr_b16 v[114:115], v226 offset:6144\n\t" \
    "ds_read_b64_tr_b16 v[116:117], v227 offset:4096\n\t" \
    "ds_read_b64_tr_b16 v[118:119], v227 offset:6144\n\t" \
    "ds_read_b64_tr_b16 v[120:121], v228 offset:4096\n\t" \
    "ds_read_b64_tr_b16 v[122:123], v228 offset:6144\n\t" \
    "ds_read_b64_tr_b16 v[124:125], v229 offset:4096\n\t" \
    "ds_read_b64_tr_b16 v[126:127], v229 offset:6144\n\t" \
    "v_mov_b32 v0, 0\n\t" \
    "v_mov_b32 v1, 0\n\t" \
    "v_mov_b32 v2, 0\n\t" \
    "v_mov_b32 v3, 0\n\t" \
    "v_mov_b32 v4, 0\n\t" \
    "v_mov_b32 v5, 0\n\t" \
    "v_mov_b32 v6, 0\n\t" \
    "v_mov_b32 v7, 0\n\t" \
    "v_mov_b32 v8, 0\n\t" \
    "v_mov_b32 v9, 0\n\t" \
    "v_mov_b32 v10, 0\n\t" \
    "v_mov_b32 v11, 0\n\t" \
    "v_mov_b32 v12, 0\n\t" \
    "v_mov_b32 v13, 0\n\t" \
    "v_mov_b32 v14, 0\n\t" \
    "v_mov_b32 v15, 0\n\t" \
    "v_mov_b32 v16, 0\n\t" \
    "v_mov_b32 v17, 0\n\t" \
    "v_mov_b32 v18, 0\n\t" \
    "v_mov_b32 v19, 0\n\t" \
    "v_mov_b32 v20, 0\n\t" \
    "v_mov_b32 v21, 0\n\t" \
    "v_mov_b32 v22, 0\n\t" \
    "v_mov_b32 v23, 0\n\t" \
    "v_mov_b32 v24, 0\n\t" \
    "v_mov_b32 v25, 0\n\t" \
    "v_mov_b32 v26, 0\n\t" \
    "v_mov_b32 v27, 0\n\t" \
    "v_mov_b32 v28, 0\n\t" \
    "v_mov_b32 v29, 0\n\t" \
    "v_mov_b32 v30, 0\n\t" \
    "v_mov_b32 v31, 0\n\t" \
    "s_waitcnt lgkmcnt(0)\n\t" \
    "v_lshlrev_b32 v194, 16, v196\n\t" \
    "v_and_b32 v195, 0xffff0000, v196\n\t" \
    "v_lshlrev_b32 v196, 16, v197\n\t" \
    "v_and_b32 v197, 0xffff0000, v197\n\t" \
    "v_lshlrev_b32 v198, 16, v200\n\t" \
    "v_and_b32 v199, 0xffff0000, v200\n\t" \
    "v_lshlrev_b32 v200, 16, v201\n\t" \
    "v_and_b32 v201, 0xffff0000, v201\n\t" \
    "v_lshlrev_b32 v202, 16, v204\n\t" \
    "v_and_b32 v203, 0xffff0000, v204\n\t" \
    "v_lshlrev_b32 v204, 16, v205\n\t" \
    "v_and_b32 v205, 0xffff0000, v205\n\t" \
    "v_lshlrev_b32 v206, 16, v208\n\t" \
    "v_and_b32 v207, 0xffff0000, v208\n\t" \
    "v_lshlrev_b32 v208, 16, v209\n\t" \
    "v_and_b32 v209, 0xffff0000, v209\n\t" \
    "v_lshlrev_b32 v210, 16, v212\n\t" \
    "v_and_b32 v211, 0xffff0000, v212\n\t" \
    "v_lshlrev_b32 v212, 16, v213\n\t" \
    "v_and_b32 v213, 0xffff0000, v213\n\t" \
    "v_lshlrev_b32 v214, 16, v216\n\t" \
    "v_and_b32 v215, 0xffff0000, v216\n\t" \
    "v_lshlrev_b32 v216, 16, v217\n\t" \
    "v_and_b32 v217, 0xffff0000, v217\n\t" \
    "v_lshlrev_b32 v218, 16, v220\n\t" \
    "v_and_b32 v219, 0xffff0000, v220\n\t" \
    "v_lshlrev_b32 v220, 16, v221\n\t" \
    "v_and_b32 v221, 0xffff0000, v221\n\t" \
    "v_lshlrev_b32 v222, 16, v224\n\t" \
    "v_and_b32 v223, 0xffff0000, v224\n\t" \
    "v_lshlrev_b32 v224, 16, v225\n\t" \
    "v_and_b32 v225, 0xffff0000, v225\n\t" \
    "v_pk_mul_f32 v[194:195], v[194:195], v[254:255] op_sel_hi:[1,0]\n\t" \
    "v_pk_mul_f32 v[196:197], v[196:197], v[254:255] op_sel_hi:[1,0]\n\t" \
    "v_pk_mul_f32 v[198:199], v[198:199], v[254:255] op_sel_hi:[1,0]\n\t" \
    "v_pk_mul_f32 v[200:201], v[200:201], v[254:255] op_sel_hi:[1,0]\n\t" \
    "v_pk_mul_f32 v[202:203], v[202:203], v[254:255] op_sel_hi:[1,0]\n\t" \
    "v_pk_mul_f32 v[204:205], v[204:205], v[254:255] op_sel_hi:[1,0]\n\t" \
    "v_pk_mul_f32 v[206:207], v[206:207], v[254:255] op_sel_hi:[1,0]\n\t" \
    "v_pk_mul_f32 v[208:209], v[208:209], v[254:255] op_sel_hi:[1,0]\n\t" \
    "v_pk_mul_f32 v[210:211], v[210:211], v[254:255] op_sel:[0,1] op_sel_hi:[1,1]\n\t" \
    "v_pk_mul_f32 v[212:213], v[212:213], v[254:255] op_sel:[0,1] op_sel_hi:[1,1]\n\t" \
    "v_pk_mul_f32 v[214:215], v[214:215], v[254:255] op_sel:[0,1] op_sel_hi:[1,1]\n\t" \
    "v_pk_mul_f32 v[216:217], v[216:217], v[254:255] op_sel:[0,1] op_sel_hi:[1,1]\n\t" \
    "v_pk_mul_f32 v[218:219], v[218:219], v[254:255] op_sel:[0,1] op_sel_hi:[1,1]\n\t" \
    "v_pk_mul_f32 v[220:221], v[220:221], v[254:255] op_sel:[0,1] op_sel_hi:[1,1]\n\t" \
    "v_pk_mul_f32 v[222:223], v[222:223], v[254:255] op_sel:[0,1] op_sel_hi:[1,1]\n\t" \
    "v_pk_mul_f32 v[224:225], v[224:225], v[254:255] op_sel:[0,1] op_sel_hi:[1,1]\n\t" \
    "v_mov_b32 v230, 0\n\t" \
    "s_mov_b32 s81, 2\n\t" \
    "s_mov_b32 s82, 1\n\t" \
    "s_mov_b32 %[st], 1\n\t" \
    "s_waitcnt vmcnt(14)\n\t" \
    "v_cmp_ne_u32 vcc, v236, v161\n\t" \
    "s_nop 1\n\t" \
    "s_and_b32 vcc_lo, vcc_lo, s84\n\t" \
    "s_cbranch_scc1 .Lc_x\n\t" \
    ".Lc_l:\n\t" \
    "s_waitcnt vmcnt(12) lgkmcnt(8)\n\t" \
    "v_mfma_f32_16x16x32_bf16 v[0:3], v[96:99], v[32:35], v[0:3]\n\t" \
    "ds_read_b64_tr_b16 v[128:129], v226 offset:8192\n\t" \
    "buffer_load_dwordx4 v[88:91], v240, %[rsb], s80 offen sc1\n\t" \
    "v_mfma_f32_16x16x32_bf16 v[16:19], v[96:99], v[36:39], v[16:19]\n\t" \
    "ds_read_b64_tr_b16 v[130:131], v226 offset:10240\n\t" \
    "v_add3_u32 v230, v32, v33, v230\n\t" \
    "v_mfma_f32_16x16x32_bf16 v[4:7], v[100:103], v[32:35], v[4:7]\n\t" \
    "ds_read_b64_tr_b16 v[132:133], v227 offset:8192\n\t" \
    "buffer_load_dwordx4 v[92:95], v240, %[rsb], s80 offen offset:1024 sc1\n\t" \
    "v_mfma_f32_16x16x32_bf16 v[20:23], v[100:103], v[36:39], v[20:23]\n\t" \
    "ds_read_b64_tr_b16 v[134:135], v227 offset:10240\n\t" \
    "v_add3_u32 v230, v34, v35, v230\n\t" \
    "v_mfma_f32_16x16x32_bf16 v[8:11], v[104:107], v[32:35], v[8:11]\n\t" \
    "ds_read_b64_tr_b16 v[136:137], v228 offset:8192\n\t" \
    "v_add3_u32 v230, v36, v37, v230\n\t" \
    "v_mfma_f32_16x16x32_bf16 v[24:27], v[104:107], v[36:39], v[24:27]\n\t" \
    "ds_read_b64_tr_b16 v[138:139], v228 offset:10240\n\t" \
    "v_add3_u32 v230, v38, v39, v230\n\t" \
    "v_mfma_f32_16x16x32_bf16 v[12:15], v[108:111], v[32:35], v[12:15]\n\t" \
    "ds_read_b64_tr_b16 v[140:141], v229 offset:8192\n\t" \
    "v_mfma_f32_16x16x32_bf16 v[28:31], v[108:111], v[36:39], v[28:31]\n\t" \
    "ds_read_b64_tr_b16 v[142:143], v229 offset:10240\n\t" \
    "s_waitcnt vmcnt(12) lgkmcnt(8)\n\t" \
    "v_mfma_f32_16x16x32_bf16 v[0:3], v[112:115], v[40:43], v[0:3]\n\t" \
    "ds_read_b64_tr_b16 v[144:145], v226 offset:12288\n\t" \
    "buffer_load_dwordx4 v[32:35], v240, %[rsb], s80 offen offset:2048 sc1\n\t" \
    "v_mfma_f32_16x16x32_bf16 v[16:19], v[112:115], v[44:47], v[16:19]\n\t" \
    "ds_read_b64_tr_b16 v[146:147], v226 offset:14336\n\t" \
    "v_add3_u32 v230, v40, v41, v230\n\t" \
    "v_mfma_f32_16x16x32_bf16 v[4:7], v[116:119], v[40:43], v[4:7]\n\t" \
    "ds_read_b64_tr_b16 v[148:149], v227 offset:12288\n\t" \
    "buffer_load_dwordx4 v[36:39], v240, %[rsb], s80 offen offset:3072 sc1\n\t" \
    "v_mfma_f32_16x16x32_bf16 v[20:23], v[116:119], v[44:47], v[20:23]\n\t" \
    "ds_read_b64_tr_b16 v[150:151], v227 offset:14336\n\t" \
    "v_add3_u32 v230, v42, v43, v230\n\t" \
    "v_mfma_f32_16x16x32_bf16 v[8:11], v[120:123], v[40:43], v[8:11]\n\t" \
    "ds_read_b64_tr_b16 v[152:153], v228 offset:12288\n\t" \
    "v_add3_u32 v230, v44, v45, v230\n\t" \
    "v_mfma_f32_16x16x32_bf16 v[24:27], v[120:123], v[44:47], v[24:27]\n\t" \
    "ds_read_b64_tr_b16 v[154:155], v228 offset:14336\n\t" \
    "v_add3_u32 v230, v46, v47, v230\n\t" \
    "v_mfma_f32_16x16x32_bf16 v[12:15], v[124:127], v[40:43], v[12:15]\n\t" \
    "ds_read_b64_tr_b16 v[156:157], v229 offset:12288\n\t" \
    "v_mfma_f32_16x16x32_bf16 v[28:31], v[124:127], v[44:47], v[28:31]\n\t" \
    "ds_read_b64_tr_b16 v[158:159], v229 offset:14336\n\t" \
    "s_waitcnt vmcnt(12) lgkmcnt(8)\n\t" \
    "v_mfma_f32_16x16x32_bf16 v[0:3], v[128:131], v[48:51], v[0:3]\n\t" \
    "ds_read_b64_tr_b16 v[96:97], v226 offset:16384\n\t" \
    "buffer_load_dwordx4 v[40:43], v241, %[rsb], s80 offen sc1\n\t" \
    "v_mfma_f32_16x16x32_bf16 v[16:19], v[128:131], v[52:55], v[16:19]\n\t" \
    "ds_read_b64_tr_b16 v[98:99], v226 offset:18432\n\t" \
    "v_add3_u32 v230, v48, v49, v230\n\t" \
    "v_mfma_f32_16x16x32_bf16 v[4:7], v[132:135], v[48:51], v[4:7]\n\t" \
    "ds_read_b64_tr_b16 v[100:101], v227 offset:16384\n\t" \
    "buffer_load_dwordx4 v[44:47], v241, %[rsb], s80 offen offset:1024 sc1\n\t" \
    "v_mfma_f32_16x16x32_bf16 v[20:23], v[132:135], v[52:55], v[20:23]\n\t" \
    "ds_read_b64_tr_b16 v[102:103], v227 offset:18432\n\t" \
    "v_add3_u32 v230, v50, v51, v230\n\t" \
    "v_mfma_f32_16x16x32_bf16 v[8:11], v[136:139], v[48:51], v[8:11]\n\t" \
    "ds_read_b64_tr_b16 v[104:105], v228 offset:16384\n\t" \
    "v_add3_u32 v230, v52, v53, v230\n\t" \
    "v_mfma_f32_16x16x32_bf16 v[24:27], v[136:139], v[52:55], v[24:27]\n\t" \
    "ds_read_b64_tr_b16 v[106:107], v228 offset:18432\n\t" \
    "v_add3_u32 v230, v54, v55, v230\n\t" \
    "v_mfma_f32_16x16x32_bf16 v[12:15], v[140:143], v[48:51], v[12:15]\n\t" \
    "ds_read_b64_tr_b16 v[108:109], v229 offset:16384\n\t" \
    "v_mfma_f32_16x16x32_bf16 v[28:31], v[140:143], v[52:55], v[28:31]\n\t" \
    "ds_read_b64_tr_b16 v[110:111], v229 offset:18432\n\t" \
    "s_waitcnt vmcnt(12) lgkmcnt(8)\n\t" \
    "v_mfma_f32_16x16x32_bf16 v[0:3], v[144:147], v[56:59], v[0:3]\n\t" \
    "ds_read_b64_tr_b16 v[112:113], v226 offset:20480\n\t" \
    "buffer_load_dwordx4 v[48:51], v241, %[rsb], s80 offen offset:2048 sc1\n\t" \
    "v_mfma_f32_16x16x32_bf16 v[16:19], v[144:147], v[60:63], v[16:19]\n\t" \
    "ds_read_b64_tr_b16 v[114:115], v226 offset:22528\n\t" \
    "v_add3_u32 v230, v56, v57, v230\n\t" \
    "v_mfma_f32_16x16x32_bf16 v[4:7], v[148:151], v[56:59], v[4:7]\n\t" \
    "ds_read_b64_tr_b16 v[116:117], v227 offset:20480\n\t" \
    "buffer_load_dwordx4 v[52:55], v241, %[rsb], s80 offen offset:3072 sc1\n\t" \
    "v_mfma_f32_16x16x32_bf16 v[20:23], v[148:151], v[60:63], v[20:23]\n\t" \
    "ds_read_b64_tr_b16 v[118:119], v227 offset:22528\n\t" \
    "v_add3_u32 v230, v58, v59, v230\n\t" \
    "v_mfma_f32_16x16x32_bf16 v[8:11], v[152:155], v[56:59], v[8:11]\n\t" \
    "ds_read_b64_tr_b16 v[120:121], v228 offset:20480\n\t" \
    "v_add3_u32 v230, v60, v61, v230\n\t" \
    "v_mfma_f32_16x16x32_bf16 v[24:27], v[152:155], v[60:63], v[24:27]\n\t" \
    "ds_read_b64_tr_b16 v[122:123], v228 offset:22528\n\t" \
    "v_add3_u32 v230, v62, v63, v230\n\t" \
    "v_mfma_f32_16x16x32_bf16 v[12:15], v[156:159], v[56:59], v[12:15]\n\t" \
    "ds_read_b64_tr_b16 v[124:125], v229 offset:20480\n\t" \
    "v_mfma_f32_16x16x32_bf16 v[28:31], v[156:159], v[60:63], v[28:31]\n\t" \
    "ds_read_b64_tr_b16 v[126:127], v229 offset:22528\n\t" \
    "s_waitcnt vmcnt(12) lgkmcnt(8)\n\t" \
    "ds_read_b32 v160, v249 offset:56\n\t" \
    "v_mfma_f32_16x16x32_bf16 v[0:3], v[96:99], v[64:67], v[0:3]\n\t" \
    "ds_read_b64_tr_b16 v[128:129], v226 offset:24576\n\t" \
    "buffer_load_dwordx4 v[56:59], v242, %[rsb], s80 offen sc1\n\t" \
    "v_mfma_f32_16x16x32_bf16 v[16:19], v[96:99], v[68:71], v[16:19]\n\t" \
    "ds_read_b64_tr_b16 v[130:131], v226 offset:26624\n\t" \
    "v_add3_u32 v230, v64, v65, v230\n\t" \
    "v_mfma_f32_16x16x32_bf16 v[4:7], v[100:103], v[64:67], v[4:7]\n\t" \
    "ds_read_b64_tr_b16 v[132:133], v227 offset:24576\n\t" \
    "buffer_load_dwordx4 v[60:63], v242, %[rsb], s80 offen offset:1024 sc1\n\t" \
    "v_mfma_f32_16x16x32_bf16 v[20:23], v[100:103], v[68:71], v[20:23]\n\t" \
    "ds_read_b64_tr_b16 v[134:135], v227 offset:26624\n\t" \
    "v_add3_u32 v230, v66, v67, v230\n\t" \
    "v_mfma_f32_16x16x32_bf16 v[8:11], v[104:107], v[64:67], v[8:11]\n\t" \
    "ds_read_b64_tr_b16 v[136:137], v228 offset:24576\n\t" \
    "v_add3_u32 v230, v68, v69, v230\n\t" \
    "v_mfma_f32_16x16x32_bf16 v[24:27], v[104:107], v[68:71], v[24:27]\n\t" \
    "ds_read_b64_tr_b16 v[138:139], v228 offset:26624\n\t" \
    "v_add3_u32 v230, v70, v71, v230\n\t" \
    "v_mfma_f32_16x16x32_bf16 v[12:15], v[108:111], v[64:67], v[12:15]\n\t" \
    "ds_read_b64_tr_b16 v[140:141], v229 offset:24576\n\t" \
    "v_mfma_f32_16x16x32_bf16 v[28:31], v[108:111], v[68:71], v[28:31]\n\t" \
    "ds_read_b64_tr_b16 v[142:143], v229 offset:26624\n\t" \
    "s_waitcnt vmcnt(12) lgkmcnt(8)\n\t" \
    "v_mfma_f32_16x16x32_bf16 v[0:3], v[112:115], v[72:75], v[0:3]\n\t" \
    "ds_read_b64_tr_b16 v[144:145], v226 offset:28672\n\t" \
    "buffer_load_dwordx4 v[64:67], v242, %[rsb], s80 offen offset:2048 sc1\n\t" \
    "v_mfma_f32_16x16x32_bf16 v[16:19], v[112:115], v[76:79], v[16:19]\n\t" \
    "ds_read_b64_tr_b16 v[146:147], v226 offset:30720\n\t" \
    "v_add3_u32 v230, v72, v73, v230\n\t" \
    "v_mfma_f32_16x16x32_bf16 v[4:7], v[116:119], v[72:75], v[4:7]\n\t" \
    "ds_read_b64_tr_b16 v[148:149], v227 offset:28672\n\t" \
    "buffer_load_dwordx4 v[68:71], v242, %[rsb], s80 offen offset:3072 sc1\n\t" \
    "v_add_u32 v226, 0x8000, v226\n\t" \
    "v_and_b32 v226, 0x1ffff, v226\n\t" \
    "v_mfma_f32_16x16x32_bf16 v[20:23], v[116:119], v[76:79], v[20:23]\n\t" \
    "ds_read_b64_tr_b16 v[150:151], v227 offset:30720\n\t" \
    "v_add3_u32 v230, v74, v75, v230\n\t" \
    "v_mfma_f32_16x16x32_bf16 v[8:11], v[120:123], v[72:75], v[8:11]\n\t" \
    "ds_read_b64_tr_b16 v[152:153], v228 offset:28672\n\t" \
    "v_add3_u32 v230, v76, v77, v230\n\t" \
    "v_add_u32 v227, 0x8000, v227\n\t" \
    "v_and_b32 v227, 0x1ffff, v227\n\t" \
    "v_mfma_f32_16x16x32_bf16 v[24:27], v[120:123], v[76:79], v[24:27]\n\t" \
    "ds_read_b64_tr_b16 v[154:155], v228 offset:30720\n\t" \
    "v_add3_u32 v230, v78, v79, v230\n\t" \
    "v_mfma_f32_16x16x32_bf16 v[12:15], v[124:127], v[72:75], v[12:15]\n\t" \
    "v_readfirstlane_b32 s83, v160\n\t" \
    "s_add_u32 s82, s82, 1\n\t" \
    "ds_read_b64_tr_b16 v[156:157], v229 offset:28672\n\t" \
    "v_add_u32 v228, 0x8000, v228\n\t" \
    "v_and_b32 v228, 0x1ffff, v228\n\t" \
    "v_mfma_f32_16x16x32_bf16 v[28:31], v[124:127], v[76:79], v[28:31]\n\t" \
    "s_cmp_lt_u32 s83, s82\n\t" \
    "s_cbranch_scc1 .Lc_w0\n\t" \
    ".Lc_r0:\n\t" \
    "ds_read_b64_tr_b16 v[158:159], v229 offset:30720\n\t" \
    "v_add_u32 v229, 0x8000, v229\n\t" \
    "v_and_b32 v229, 0x1ffff, v229\n\t" \
    "s_waitcnt vmcnt(12) lgkmcnt(8)\n\t" \
    "v_mfma_f32_16x16x32_bf16 v[0:3], v[128:131], v[80:83], v[0:3]\n\t" \
    "ds_read_b64_tr_b16 v[96:97], v226\n\t" \
    "buffer_load_dwordx4 v[72:75], v243, %[rsb], s80 offen sc1\n\t" \
    "v_mfma_f32_16x16x32_bf16 v[16:19], v[128:131], v[84:87], v[16:19]\n\t" \
    "ds_read_b64_tr_b16 v[98:99], v226 offset:2048\n\t" \
    "v_add3_u32 v230, v80, v81, v230\n\t" \
    "v_mfma_f32_16x16x32_bf16 v[4:7], v[132:135], v[80:83], v[4:7]\n\t" \
    "ds_read_b64_tr_b16 v[100:101], v227\n\t" \
    "buffer_load_dwordx4 v[76:79], v243, %[rsb], s80 offen offset:1024 sc1\n\t" \
    "v_mfma_f32_16x16x32_bf16 v[20:23], v[132:135], v[84:87], v[20:23]\n\t" \
    "ds_read_b64_tr_b16 v[102:103], v227 offset:2048\n\t" \
    "v_add3_u32 v230, v82, v83, v230\n\t" \
    "v_mfma_f32_16x16x32_bf16 v[8:11], v[136:139], v[80:83], v[8:11]\n\t" \
    "ds_read_b64_tr_b16 v[104:105], v228\n\t" \
    "v_add3_u32 v230, v84, v85, v230\n\t" \
    "v_mfma_f32_16x16x32_bf16 v[24:27], v[136:139], v[84:87], v[24:27]\n\t" \
    "ds_read_b64_tr_b16 v[106:107], v228 offset:2048\n\t" \
    "v_add3_u32 v230, v86, v87, v230\n\t" \
    "v_mfma_f32_16x16x32_bf16 v[12:15], v[140:143], v[80:83], v[12:15]\n\t" \
    "ds_read_b64_tr_b16 v[108:109], v229\n\t" \
    "v_mfma_f32_16x16x32_bf16 v[28:31], v[140:143], v[84:87], v[28:31]\n\t" \
    "ds_read_b64_tr_b16 v[110:111], v229 offset:2048\n\t" \
    "s_waitcnt vmcnt(12) lgkmcnt(8)\n\t" \
    "v_mfma_f32_16x16x32_bf16 v[0:3], v[144:147], v[88:91], v[0:3]\n\t" \
    "ds_read_b64_tr_b16 v[112:113], v226 offset:4096\n\t" \
    "buffer_load_dwordx4 v[80:83], v243, %[rsb], s80 offen offset:2048 sc1\n\t" \
    "v_mfma_f32_16x16x32_bf16 v[16:19], v[144:147], v[92:95], v[16:19]\n\t" \
    "ds_read_b64_tr_b16 v[114:115], v226 offset:6144\n\t" \
    "v_add3_u32 v230, v88, v89, v230\n\t" \
    "v_mfma_f32_16x16x32_bf16 v[4:7], v[148:151], v[88:91], v[4:7]\n\t" \
    "ds_read_b64_tr_b16 v[116:117], v227 offset:4096\n\t" \
    "buffer_load_dwordx4 v[84:87], v243, %[rsb], s80 offen offset:3072 sc1\n\t" \
    "v_mfma_f32_16x16x32_bf16 v[20:23], v[148:151], v[92:95], v[20:23]\n\t" \
    "ds_read_b64_tr_b16 v[118:119], v227 offset:6144\n\t" \
    "v_add3_u32 v230, v90, v91, v230\n\t" \
    "v_mfma_f32_16x16x32_bf16 v[8:11], v[152:155], v[88:91], v[8:11]\n\t" \
    "ds_read_b64_tr_b16 v[120:121], v228 offset:4096\n\t" \
    "v_add3_u32 v230, v92, v93, v230\n\t" \
    "v_mfma_f32_16x16x32_bf16 v[24:27], v[152:155], v[92:95], v[24:27]\n\t" \
    "ds_read_b64_tr_b16 v[122:123], v228 offset:6144\n\t" \
    "v_add3_u32 v230, v94, v95, v230\n\t" \
    "v_mfma_f32_16x16x32_bf16 v[12:15], v[156:159], v[88:91], v[12:15]\n\t" \
    "ds_read_b64_tr_b16 v[124:125], v229 offset:4096\n\t" \
    "s_add_u32 s80, s80, 0x4000\n\t" \
    "s_and_b32 s80, s80, 0xc000\n\t" \
    "v_mfma_f32_16x16x32_bf16 v[28:31], v[156:159], v[92:95], v[28:31]\n\t" \
    "ds_read_b64_tr_b16 v[126:127], v229 offset:6144\n\t" \
    "s_sub_u32 s81, s81, 1\n\t" \
    "s_cmp_lg_u32 s81, 0\n\t" \
    "s_cbranch_scc1 .Lc_l\n\t" \
    "s_waitcnt vmcnt(12) lgkmcnt(8)\n\t" \
    "v_mfma_f32_16x16x32_bf16 v[162:165], v[96:99], v[32:35], 0\n\t" \
    "ds_read_b64_tr_b16 v[128:129], v226 offset:8192\n\t" \
    "buffer_load_dwordx4 v[88:91], v240, %[rsb], s80 offen sc1\n\t" \
    "v_mfma_f32_16x16x32_bf16 v[178:181], v[96:99], v[36:39], 0\n\t" \
    "ds_read_b64_tr_b16 v[130:131], v226 offset:10240\n\t" \
    "v_add3_u32 v230, v32, v33, v230\n\t" \
    "v_mfma_f32_16x16x32_bf16 v[166:169], v[100:103], v[32:35], 0\n\t" \
    "ds_read_b64_tr_b16 v[132:133], v227 offset:8192\n\t" \
    "buffer_load_dwordx4 v[92:95], v240, %[rsb], s80 offen offset:1024 sc1\n\t" \
    "v_mfma_f32_16x16x32_bf16 v[182:185], v[100:103], v[36:39], 0\n\t" \
    "ds_read_b64_tr_b16 v[134:135], v227 offset:10240\n\t" \
    "v_add3_u32 v230, v34, v35, v230\n\t" \
    "v_mfma_f32_16x16x32_bf16 v[170:173], v[104:107], v[32:35], 0\n\t" \
    "ds_read_b64_tr_b16 v[136:137], v228 offset:8192\n\t" \
    "v_add3_u32 v230, v36, v37, v230\n\t" \
    "v_mfma_f32_16x16x32_bf16 v[186:189], v[104:107], v[36:39], 0\n\t" \
    "ds_read_b64_tr_b16 v[138:139], v228 offset:10240\n\t" \
    "v_add3_u32 v230, v38, v39, v230\n\t" \
    "v_mfma_f32_16x16x32_bf16 v[174:177], v[108:111], v[32:35], 0\n\t" \
    "ds_read_b64_tr_b16 v[140:141], v229 offset:8192\n\t" \
    "v_mfma_f32_16x16x32_bf16 v[190:193], v[108:111], v[36:39], 0\n\t" \
    "ds_read_b64_tr_b16 v[142:143], v229 offset:10240\n\t" \
    "s_waitcnt vmcnt(12) lgkmcnt(8)\n\t" \
    "v_mfma_f32_16x16x32_bf16 v[162:165], v[112:115], v[40:43], v[162:165]\n\t" \
    "ds_read_b64_tr_b16 v[144:145], v226 offset:12288\n\t" \
    "buffer_load_dwordx4 v[32:35], v240, %[rsb], s80 offen offset:2048 sc1\n\t" \
    "v_mfma_f32_16x16x32_bf16 v[178:181], v[112:115], v[44:47], v[178:181]\n\t" \
    "ds_read_b64_tr_b16 v[146:147], v226 offset:14336\n\t" \
    "v_add3_u32 v230, v40, v41, v230\n\t" \
    "v_mfma_f32_16x16x32_bf16 v[166:169], v[116:119], v[40:43], v[166:169]\n\t" \
    "ds_read_b64_tr_b16 v[148:149], v227 offset:12288\n\t" \
    "buffer_load_dwordx4 v[36:39], v240, %[rsb], s80 offen offset:3072 sc1\n\t" \
    "v_mfma_f32_16x16x32_bf16 v[182:185], v[116:119], v[44:47], v[182:185]\n\t" \
    "ds_read_b64_tr_b16 v[150:151], v227 offset:14336\n\t" \
    "v_add3_u32 v230, v42, v43, v230\n\t" \
    "v_mfma_f32_16x16x32_bf16 v[170:173], v[120:123], v[40:43], v[170:173]\n\t" \
    "ds_read_b64_tr_b16 v[152:153], v228 offset:12288\n\t" \
    "v_add3_u32 v230, v44, v45, v230\n\t" \
    "v_mfma_f32_16x16x32_bf16 v[186:189], v[120:123], v[44:47], v[186:189]\n\t" \
    "ds_read_b64_tr_b16 v[154:155], v228 offset:14336\n\t" \
    "v_add3_u32 v230, v46, v47, v230\n\t" \
    "v_mfma_f32_16x16x32_bf16 v[174:177], v[124:127], v[40:43], v[174:177]\n\t" \
    "ds_read_b64_tr_b16 v[156:157], v229 offset:12288\n\t" \
    "v_mfma_f32_16x16x32_bf16 v[190:193], v[124:127], v[44:47], v[190:193]\n\t" \
    "ds_read_b64_tr_b16 v[158:159], v229 offset:14336\n\t" \
    "s_waitcnt vmcnt(12) lgkmcnt(8)\n\t" \
    "v_mfma_f32_16x16x32_bf16 v[162:165], v[128:131], v[48:51], v[162:165]\n\t" \
    "ds_read_b64_tr_b16 v[96:97], v226 offset:16384\n\t" \
    "buffer_load_dwordx4 v[40:43], v241, %[rsb], s80 offen sc1\n\t" \
    "v_mfma_f32_16x16x32_bf16 v[178:181], v[128:131], v[52:55], v[178:181]\n\t" \
    "ds_read_b64_tr_b16 v[98:99], v226 offset:18432\n\t" \
    "v_add3_u32 v230, v48, v49, v230\n\t" \
    "v_mfma_f32_16x16x32_bf16 v[166:169], v[132:135], v[48:51], v[166:169]\n\t" \
    "ds_read_b64_tr_b16 v[100:101], v227 offset:16384\n\t" \
    "buffer_load_dwordx4 v[44:47], v241, %[rsb], s80 offen offset:1024 sc1\n\t" \
    "v_mfma_f32_16x16x32_bf16 v[182:185], v[132:135], v[52:55], v[182:185]\n\t" \
    "ds_read_b64_tr_b16 v[102:103], v227 offset:18432\n\t" \
    "v_add3_u32 v230, v50, v51, v230\n\t" \
    "v_mfma_f32_16x16x32_bf16 v[170:173], v[136:139], v[48:51], v[170:173]\n\t" \
    "ds_read_b64_tr_b16 v[104:105], v228 offset:16384\n\t" \
    "v_add3_u32 v230, v52, v53, v230\n\t" \
    "v_mfma_f32_16x16x32_bf16 v[186:189], v[136:139], v[52:55], v[186:189]\n\t" \
    "ds_read_b64_tr_b16 v[106:107], v228 offset:18432\n\t" \
    "v_add3_u32 v230, v54, v55, v230\n\t" \
    "v_mfma_f32_16x16x32_bf16 v[174:177], v[140:143], v[48:51], v[174:177]\n\t" \
    "ds_read_b64_tr_b16 v[108:109], v229 offset:16384\n\t" \
    "v_mfma_f32_16x16x32_bf16 v[190:193], v[140:143], v[52:55], v[190:193]\n\t" \
    "ds_read_b64_tr_b16 v[110:111], v229 offset:18432\n\t" \
    "s_waitcnt vmcnt(12) lgkmcnt(8)\n\t" \
    "v_mfma_f32_16x16x32_bf16 v[162:165], v[144:147], v[56:59], v[162:165]\n\t" \
    "ds_read_b64_tr_b16 v[112:113], v226 offset:20480\n\t" \
    "buffer_load_dwordx4 v[48:51], v241, %[rsb], s80 offen offset:2048 sc1\n\t" \
    "v_mfma_f32_16x16x32_bf16 v[178:181], v[144:147], v[60:63], v[178:181]\n\t" \
    "ds_read_b64_tr_b16 v[114:115], v226 offset:22528\n\t" \
    "v_add3_u32 v230, v56, v57, v230\n\t" \
    "v_mfma_f32_16x16x32_bf16 v[166:169], v[148:151], v[56:59], v[166:169]\n\t" \
    "ds_read_b64_tr_b16 v[116:117], v227 offset:20480\n\t" \
    "buffer_load_dwordx4 v[52:55], v241, %[rsb], s80 offen offset:3072 sc1\n\t" \
    "v_mfma_f32_16x16x32_bf16 v[182:185], v[148:151], v[60:63], v[182:185]\n\t" \
    "ds_read_b64_tr_b16 v[118:119], v227 offset:22528\n\t" \
    "v_add3_u32 v230, v58, v59, v230\n\t" \
    "v_mfma_f32_16x16x32_bf16 v[170:173], v[152:155], v[56:59], v[170:173]\n\t" \
    "ds_read_b64_tr_b16 v[120:121], v228 offset:20480\n\t" \
    "v_add3_u32 v230, v60, v61, v230\n\t" \
    "v_mfma_f32_16x16x32_bf16 v[186:189], v[152:155], v[60:63], v[186:189]\n\t" \
    "ds_read_b64_tr_b16 v[122:123], v228 offset:22528\n\t" \
    "v_add3_u32 v230, v62, v63, v230\n\t" \
    "v_mfma_f32_16x16x32_bf16 v[174:177], v[156:159], v[56:59], v[174:177]\n\t" \
    "ds_read_b64_tr_b16 v[124:125], v229 offset:20480\n\t" \
    "v_mfma_f32_16x16x32_bf16 v[190:193], v[156:159], v[60:63], v[190:193]\n\t" \
    "ds_read_b64_tr_b16 v[126:127], v229 offset:22528\n\t" \
    "s_waitcnt vmcnt(12) lgkmcnt(8)\n\t" \
    "ds_read_b32 v160, v249 offset:56\n\t" \
    "v_mfma_f32_16x16x32_bf16 v[162:165], v[96:99], v[64:67], v[162:165]\n\t" \
    "ds_read_b64_tr_b16 v[128:129], v226 offset:24576\n\t" \
    "buffer_load_dwordx4 v[56:59], v242, %[rsb], s80 offen sc1\n\t" \
    "v_mfma_f32_16x16x32_bf16 v[178:181], v[96:99], v[68:71], v[178:181]\n\t" \
    "ds_read_b64_tr_b16 v[130:131], v226 offset:26624\n\t" \
    "v_add3_u32 v230, v64, v65, v230\n\t" \
    "v_mfma_f32_16x16x32_bf16 v[166:169], v[100:103], v[64:67], v[166:169]\n\t" \
    "ds_read_b64_tr_b16 v[132:133], v227 offset:24576\n\t" \
    "buffer_load_dwordx4 v[60:63], v242, %[rsb], s80 offen offset:1024 sc1\n\t" \
    "v_mfma_f32_16x16x32_bf16 v[182:185], v[100:103], v[68:71], v[182:185]\n\t" \
    "ds_read_b64_tr_b16 v[134:135], v227 offset:26624\n\t" \
    "v_add3_u32 v230, v66, v67, v230\n\t" \
    "v_mfma_f32_16x16x32_bf16 v[170:173], v[104:107], v[64:67], v[170:173]\n\t" \
    "ds_read_b64_tr_b16 v[136:137], v228 offset:24576\n\t" \
    "v_add3_u32 v230, v68, v69, v230\n\t" \
    "v_mfma_f32_16x16x32_bf16 v[186:189], v[104:107], v[68:71], v[186:189]\n\t" \
    "ds_read_b64_tr_b16 v[138:139], v228 offset:26624\n\t" \
    "v_add3_u32 v230, v70, v71, v230\n\t" \
    "v_mfma_f32_16x16x32_bf16 v[174:177], v[108:111], v[64:67], v[174:177]\n\t" \
    "ds_read_b64_tr_b16 v[140:141], v229 offset:24576\n\t" \
    "v_mfma_f32_16x16x32_bf16 v[190:193], v[108:111], v[68:71], v[190:193]\n\t" \
    "ds_read_b64_tr_b16 v[142:143], v229 offset:26624\n\t" \
    "s_waitcnt vmcnt(12) lgkmcnt(8)\n\t" \
    "v_mfma_f32_16x16x32_bf16 v[162:165], v[112:115], v[72:75], v[162:165]\n\t" \
    "ds_read_b64_tr_b16 v[144:145], v226 offset:28672\n\t" \
    "buffer_load_dwordx4 v[64:67], v242, %[rsb], s80 offen offset:2048 sc1\n\t" \
    "v_mfma_f32_16x16x32_bf16 v[178:181], v[112:115], v[76:79], v[178:181]\n\t" \
    "ds_read_b64_tr_b16 v[146:147], v226 offset:30720\n\t" \
    "v_add3_u32 v230, v72, v73, v230\n\t" \
    "v_mfma_f32_16x16x32_bf16 v[166:169], v[116:119], v[72:75], v[166:169]\n\t" \
    "ds_read_b64_tr_b16 v[148:149], v227 offset:28672\n\t" \
    "buffer_load_dwordx4 v[68:71], v242, %[rsb], s80 offen offset:3072 sc1\n\t" \
    "v_add_u32 v226, 0x8000, v226\n\t" \
    "v_and_b32 v226, 0x1ffff, v226\n\t" \
    "v_mfma_f32_16x16x32_bf16 v[182:185], v[116:119], v[76:79], v[182:185]\n\t" \
    "ds_read_b64_tr_b16 v[150:151], v227 offset:30720\n\t" \
    "v_add3_u32 v230, v74, v75, v230\n\t" \
    "v_mfma_f32_16x16x32_bf16 v[170:173], v[120:123], v[72:75], v[170:173]\n\t" \
    "ds_read_b64_tr_b16 v[152:153], v228 offset:28672\n\t" \
    "v_add3_u32 v230, v76, v77, v230\n\t" \
    "v_add_u32 v227, 0x8000, v227\n\t" \
    "v_and_b32 v227, 0x1ffff, v227\n\t" \
    "v_mfma_f32_16x16x32_bf16 v[186:189], v[120:123], v[76:79], v[186:189]\n\t" \
    "ds_read_b64_tr_b16 v[154:155], v228 offset:30720\n\t" \
    "v_add3_u32 v230, v78, v79, v230\n\t" \
    "v_mfma_f32_16x16x32_bf16 v[174:177], v[124:127], v[72:75], v[174:177]\n\t" \
    "v_readfirstlane_b32 s83, v160\n\t" \
    "s_add_u32 s82, s82, 1\n\t" \
    "ds_read_b64_tr_b16 v[156:157], v229 offset:28672\n\t" \
    "v_add_u32 v228, 0x8000, v228\n\t" \
    "v_and_b32 v228, 0x1ffff, v228\n\t" \
    "v_mfma_f32_16x16x32_bf16 v[190:193], v[124:127], v[76:79], v[190:193]\n\t" \
    "s_cmp_lt_u32 s83, s82\n\t" \
    "s_cbranch_scc1 .Lc_w1\n\t" \
    ".Lc_r1:\n\t" \
    "ds_read_b64_tr_b16 v[158:159], v229 offset:30720\n\t" \
    "v_add_u32 v229, 0x8000, v229\n\t" \
    "v_and_b32 v229, 0x1ffff, v229\n\t" \
    "s_waitcnt vmcnt(12) lgkmcnt(8)\n\t" \
    "v_mfma_f32_16x16x32_bf16 v[162:165], v[128:131], v[80:83], v[162:165]\n\t" \
    "ds_read_b64_tr_b16 v[96:97], v226\n\t" \
    "buffer_load_dwordx4 v[72:75], v243, %[rsb], s80 offen sc1\n\t" \
    "v_mfma_f32_16x16x32_bf16 v[178:181], v[128:131], v[84:87], v[178:181]\n\t" \
    "ds_read_b64_tr_b16 v[98:99], v226 offset:2048\n\t" \
    "v_add3_u32 v230, v80, v81, v230\n\t" \
    "v_mfma_f32_16x16x32_bf16 v[166:169], v[132:135], v[80:83], v[166:169]\n\t" \
    "ds_read_b64_tr_b16 v[100:101], v227\n\t" \
    "buffer_load_dwordx4 v[76:79], v243, %[rsb], s80 offen offset:1024 sc1\n\t" \
    "v_mfma_f32_16x16x32_bf16 v[182:185], v[132:135], v[84:87], v[182:185]\n\t" \
    "ds_read_b64_tr_b16 v[102:103], v227 offset:2048\n\t" \
    "v_add3_u32 v230, v82, v83, v230\n\t" \
    "v_mfma_f32_16x16x32_bf16 v[170:173], v[136:139], v[80:83], v[170:173]\n\t" \
    "ds_read_b64_tr_b16 v[104:105], v228\n\t" \
    "v_add3_u32 v230, v84, v85, v230\n\t" \
    "v_mfma_f32_16x16x32_bf16 v[186:189], v[136:139], v[84:87], v[186:189]\n\t" \
    "ds_read_b64_tr_b16 v[106:107], v228 offset:2048\n\t" \
    "v_add3_u32 v230, v86, v87, v230\n\t" \
    "v_mfma_f32_16x16x32_bf16 v[174:177], v[140:143], v[80:83], v[174:177]\n\t" \
    "ds_read_b64_tr_b16 v[108:109], v229\n\t" \
    "v_mfma_f32_16x16x32_bf16 v[190:193], v[140:143], v[84:87], v[190:193]\n\t" \
    "ds_read_b64_tr_b16 v[110:111], v229 offset:2048\n\t" \
    "s_waitcnt vmcnt(12) lgkmcnt(8)\n\t" \
    "v_mfma_f32_16x16x32_bf16 v[162:165], v[144:147], v[88:91], v[162:165]\n\t" \
    "ds_read_b64_tr_b16 v[112:113], v226 offset:4096\n\t" \
    "buffer_load_dwordx4 v[80:83], v243, %[rsb], s80 offen offset:2048 sc1\n\t" \
    "v_mfma_f32_16x16x32_bf16 v[178:181], v[144:147], v[92:95], v[178:181]\n\t" \
    "ds_read_b64_tr_b16 v[114:115], v226 offset:6144\n\t" \
    "v_add3_u32 v230, v88, v89, v230\n\t" \
    "v_mfma_f32_16x16x32_bf16 v[166:169], v[148:151], v[88:91], v[166:169]\n\t" \
    "ds_read_b64_tr_b16 v[116:117], v227 offset:4096\n\t" \
    "buffer_load_dwordx4 v[84:87], v243, %[rsb], s80 offen offset:3072 sc1\n\t" \
    "v_mfma_f32_16x16x32_bf16 v[182:185], v[148:151], v[92:95], v[182:185]\n\t" \
    "ds_read_b64_tr_b16 v[118:119], v227 offset:6144\n\t" \
    "v_add3_u32 v230, v90, v91, v230\n\t" \
    "v_mfma_f32_16x16x32_bf16 v[170:173], v[152:155], v[88:91], v[170:173]\n\t" \
    "ds_read_b64_tr_b16 v[120:121], v228 offset:4096\n\t" \
    "v_add3_u32 v230, v92, v93, v230\n\t" \
    "v_mfma_f32_16x16x32_bf16 v[186:189], v[152:155], v[92:95], v[186:189]\n\t" \
    "ds_read_b64_tr_b16 v[122:123], v228 offset:6144\n\t" \
    "v_add3_u32 v230, v94, v95, v230\n\t" \
    "v_mfma_f32_16x16x32_bf16 v[174:177], v[156:159], v[88:91], v[174:177]\n\t" \
    "ds_read_b64_tr_b16 v[124:125], v229 offset:4096\n\t" \
    "s_add_u32 s80, s80, 0x4000\n\t" \
    "s_and_b32 s80, s80, 0xc000\n\t" \
    "v_mfma_f32_16x16x32_bf16 v[190:193], v[156:159], v[92:95], v[190:193]\n\t" \
    "ds_read_b64_tr_b16 v[126:127], v229 offset:6144\n\t" \
    "s_waitcnt vmcnt(12) lgkmcnt(8)\n\t" \
    "v_mfma_f32_16x16x32_bf16 v[162:165], v[96:99], v[32:35], v[162:165]\n\t" \
    "ds_read_b64_tr_b16 v[128:129], v226 offset:8192\n\t" \
    "buffer_load_dwordx4 v[88:91], v240, %[rsb], s80 offen sc1\n\t" \
    "v_mfma_f32_16x16x32_bf16 v[178:181], v[96:99], v[36:39], v[178:181]\n\t" \
    "ds_read_b64_tr_b16 v[130:131], v226 offset:10240\n\t" \
    "v_add3_u32 v230, v32, v33, v230\n\t" \
    ".Lpad_c1:\n\t" \
    "s_cbranch_execz .Lpad_ret\n\t" \
    "v_mfma_f32_16x16x32_bf16 v[166:169], v[100:103], v[32:35], v[166:169]\n\t" \
    "ds_read_b64_tr_b16 v[132:133], v227 offset:8192\n\t" \
    "buffer_load_dwordx4 v[92:95], v240, %[rsb], s80 offen offset:1024 sc1\n\t" \
    "v_mfma_f32_16x16x32_bf16 v[182:185], v[100:103], v[36:39], v[182:185]\n\t" \
    "ds_read_b64_tr_b16 v[134:135], v227 offset:10240\n\t" \
    "v_add3_u32 v230, v34, v35, v230\n\t" \
    "v_mfma_f32_16x16x32_bf16 v[170:173], v[104:107], v[32:35], v[170:173]\n\t" \
    "ds_read_b64_tr_b16 v[136:137], v228 offset:8192\n\t" \
    "v_add3_u32 v230, v36, v37, v230\n\t" \
    "v_mfma_f32_16x16x32_bf16 v[186:189], v[104:107], v[36:39], v[186:189]\n\t" \
    "ds_read_b64_tr_b16 v[138:139], v228 offset:10240\n\t" \
    "v_add3_u32 v230, v38, v39, v230\n\t" \
    "v_mfma_f32_16x16x32_bf16 v[174:177], v[108:111], v[32:35], v[174:177]\n\t" \
    "ds_read_b64_tr_b16 v[140:141], v229 offset:8192\n\t" \
    "buffer_load_dwordx2 v[160:161], v244, %[rsb], %[sog] offen sc1\n\t" \
    "v_mfma_f32_16x16x32_bf16 v[190:193], v[108:111], v[36:39], v[190:193]\n\t" \
    "ds_read_b64_tr_b16 v[142:143], v229 offset:10240\n\t" \
    "s_waitcnt vmcnt(13) lgkmcnt(8)\n\t" \
    "v_mfma_f32_16x16x32_bf16 v[162:165], v[112:115], v[40:43], v[162:165]\n\t" \
    "ds_read_b64_tr_b16 v[144:145], v226 offset:12288\n\t" \
    "v_mfma_f32_16x16x32_bf16 v[178:181], v[112:115], v[44:47], v[178:181]\n\t" \
    "ds_read_b64_tr_b16 v[146:147], v226 offset:14336\n\t" \
    "v_add3_u32 v230, v40, v41, v230\n\t" \
    "v_mfma_f32_16x16x32_bf16 v[166:169], v[116:119], v[40:43], v[166:169]\n\t" \
    "ds_read_b64_tr_b16 v[148:149], v227 offset:12288\n\t" \
    "v_mfma_f32_16x16x32_bf16 v[182:185], v[116:119], v[44:47], v[182:185]\n\t" \
    "ds_read_b64_tr_b16 v[150:151], v227 offset:14336\n\t" \
    "v_add3_u32 v230, v42, v43, v230\n\t" \
    "v_mfma_f32_16x16x32_bf16 v[170:173], v[120:123], v[40:43], v[170:173]\n\t" \
    "ds_read_b64_tr_b16 v[152:153], v228 offset:12288\n\t" \
    "v_add3_u32 v230, v44, v45, v230\n\t" \
    "v_mfma_f32_16x16x32_bf16 v[186:189], v[120:123], v[44:47], v[186:189]\n\t" \
    "ds_read_b64_tr_b16 v[154:155], v228 offset:14336\n\t" \
    "v_add3_u32 v230, v46, v47, v230\n\t" \
    "v_mfma_f32_16x16x32_bf16 v[174:177], v[124:127], v[40:43], v[174:177]\n\t" \
    "ds_read_b64_tr_b16 v[156:157], v229 offset:12288\n\t" \
    "v_mfma_f32_16x16x32_bf16 v[190:193], v[124:127], v[44:47], v[190:193]\n\t" \
    "ds_read_b64_tr_b16 v[158:159], v229 offset:14336\n\t" \
    "s_waitcnt vmcnt(11) lgkmcnt(8)\n\t" \
    "v_mfma_f32_16x16x32_bf16 v[162:165], v[128:131], v[48:51], v[162:165]\n\t" \
    "ds_read_b64_tr_b16 v[96:97], v226 offset:16384\n\t" \
    "v_mfma_f32_16x16x32_bf16 v[178:181], v[128:131], v[52:55], v[178:181]\n\t" \
    "ds_read_b64_tr_b16 v[98:99], v226 offset:18432\n\t" \
    "v_add3_u32 v230, v48, v49, v230\n\t" \
    "v_mfma_f32_16x16x32_bf16 v[166:169], v[132:135], v[48:51], v[166:169]\n\t" \
    "ds_read_b64_tr_b16 v[100:101], v227 offset:16384\n\t" \
    "v_mfma_f32_16x16x32_bf16 v[182:185], v[132:135], v[52:55], v[182:185]\n\t" \
    "ds_read_b64_tr_b16 v[102:103], v227 offset:18432\n\t" \
    "v_add3_u32 v230, v50, v51, v230\n\t" \
    "v_mfma_f32_16x16x32_bf16 v[170:173], v[136:139], v[48:51], v[170:173]\n\t" \
    "ds_read_b64_tr_b16 v[104:105], v228 offset:16384\n\t" \
    "v_add3_u32 v230, v52, v53, v230\n\t" \
    "v_mfma_f32_16x16x32_bf16 v[186:189], v[136:139], v[52:55], v[186:189]\n\t" \
    "ds_read_b64_tr_b16 v[106:107], v228 offset:18432\n\t" \
    "v_add3_u32 v230, v54, v55, v230\n\t" \
    "v_mfma_f32_16x16x32_bf16 v[174:177], v[140:143], v[48:51], v[174:177]\n\t" \
    "ds_read_b64_tr_b16 v[108:109], v229 offset:16384\n\t" \
    "v_mfma_f32_16x16x32_bf16 v[190:193], v[140:143], v[52:55], v[190:193]\n\t" \
    "ds_read_b64_tr_b16 v[110:111], v229 offset:18432\n\t" \
    "s_waitcnt vmcnt(9) lgkmcnt(8)\n\t" \
    "v_mfma_f32_16x16x32_bf16 v[162:165], v[144:147], v[56:59], v[162:165]\n\t" \
    "ds_read_b64_tr_b16 v[112:113], v226 offset:20480\n\t" \
    "v_mfma_f32_16x16x32_bf16 v[178:181], v[144:147], v[60:63], v[178:181]\n\t" \
    "ds_read_b64_tr_b16 v[114:115], v226 offset:22528\n\t" \
    "v_add3_u32 v230, v56, v57, v230\n\t" \
    "v_mfma_f32_16x16x32_bf16 v[166:169], v[148:151], v[56:59], v[166:169]\n\t" \
    "ds_read_b64_tr_b16 v[116:117], v227 offset:20480\n\t" \
    "v_mfma_f32_16x16x32_bf16 v[182:185], v[148:151], v[60:63], v[182:185]\n\t" \
    "ds_read_b64_tr_b16 v[118:119], v227 offset:22528\n\t" \
    "v_add3_u32 v230, v58, v59, v230\n\t" \
    "v_mfma_f32_16x16x32_bf16 v[170:173], v[152:155], v[56:59], v[170:173]\n\t" \
    "ds_read_b64_tr_b16 v[120:121], v228 offset:20480\n\t" \
    "v_add3_u32 v230, v60, v61, v230\n\t" \
    "v_mfma_f32_16x16x32_bf16 v[186:189], v[152:155], v[60:63], v[186:189]\n\t" \
    "ds_read_b64_tr_b16 v[122:123], v228 offset:22528\n\t" \
    "v_add3_u32 v230, v62, v63, v230\n\t" \
    "v_mfma_f32_16x16x32_bf16 v[174:177], v[156:159], v[56:59], v[174:177]\n\t" \
    "ds_read_b64_tr_b16 v[124:125], v229 offset:20480\n\t" \
    "v_mfma_f32_16x16x32_bf16 v[190:193], v[156:159], v[60:63], v[190:193]\n\t" \
    "ds_read_b64_tr_b16 v[126:127], v229 offset:22528\n\t" \
    "s_waitcnt vmcnt(7) lgkmcnt(8)\n\t" \
    "v_mfma_f32_16x16x32_bf16 v[162:165], v[96:99], v[64:67], v[162:165]\n\t" \
    "ds_read_b64_tr_b16 v[128:129], v226 offset:24576\n\t" \
    "v_mfma_f32_16x16x32_bf16 v[178:181], v[96:99], v[68:71], v[178:181]\n\t" \
    "ds_read_b64_tr_b16 v[130:131], v226 offset:26624\n\t" \
    "v_add3_u32 v230, v64, v65, v230\n\t" \
    "v_mfma_f32_16x16x32_bf16 v[166:169], v[100:103], v[64:67], v[166:169]\n\t" \
    "ds_read_b64_tr_b16 v[132:133], v227 offset:24576\n\t" \
    "v_mfma_f32_16x16x32_bf16 v[182:185], v[100:103], v[68:71], v[182:185]\n\t" \
    "ds_read_b64_tr_b16 v[134:135], v227 offset:26624\n\t" \
    "v_add3_u32 v230, v66, v67, v230\n\t" \
    "v_mfma_f32_16x16x32_bf16 v[170:173], v[104:107], v[64:67], v[170:173]\n\t" \
    "ds_read_b64_tr_b16 v[136:137], v228 offset:24576\n\t" \
    "v_add3_u32 v230, v68, v69, v230\n\t" \
    "v_mfma_f32_16x16x32_bf16 v[186:189], v[104:107], v[68:71], v[186:189]\n\t" \
    "ds_read_b64_tr_b16 v[138:139], v228 offset:26624\n\t" \
    "v_add3_u32 v230, v70, v71, v230\n\t" \
    "v_mfma_f32_16x16x32_bf16 v[174:177], v[108:111], v[64:67], v[174:177]\n\t" \
    "ds_read_b64_tr_b16 v[140:141], v229 offset:24576\n\t" \
    "v_mfma_f32_16x16x32_bf16 v[190:193], v[108:111], v[68:71], v[190:193]\n\t" \
    "ds_read_b64_tr_b16 v[142:143], v229 offset:26624\n\t" \
    "s_waitcnt vmcnt(5) lgkmcnt(8)\n\t" \
    "v_mfma_f32_16x16x32_bf16 v[162:165], v[112:115], v[72:75], v[162:165]\n\t" \
    "ds_read_b64_tr_b16 v[144:145], v226 offset:28672\n\t" \
    "v_mfma_f32_16x16x32_bf16 v[178:181], v[112:115], v[76:79], v[178:181]\n\t" \
    "ds_read_b64_tr_b16 v[146:147], v226 offset:30720\n\t" \
    "v_add3_u32 v230, v72, v73, v230\n\t" \
    "v_mfma_f32_16x16x32_bf16 v[166:169], v[116:119], v[72:75], v[166:169]\n\t" \
    "ds_read_b64_tr_b16 v[148:149], v227 offset:28672\n\t" \
    "v_mfma_f32_16x16x32_bf16 v[182:185], v[116:119], v[76:79], v[182:185]\n\t" \
    "ds_read_b64_tr_b16 v[150:151], v227 offset:30720\n\t" \
    "v_add3_u32 v230, v74, v75, v230\n\t" \
    "v_mfma_f32_16x16x32_bf16 v[170:173], v[120:123], v[72:75], v[170:173]\n\t" \
    "ds_read_b64_tr_b16 v[152:153], v228 offset:28672\n\t" \
    "v_add3_u32 v230, v76, v77, v230\n\t" \
    "v_mfma_f32_16x16x32_bf16 v[186:189], v[120:123], v[76:79], v[186:189]\n\t" \
    "ds_read_b64_tr_b16 v[154:155], v228 offset:30720\n\t" \
    "v_add3_u32 v230, v78, v79, v230\n\t" \
    "v_mfma_f32_16x16x32_bf16 v[174:177], v[124:127], v[72:75], v[174:177]\n\t" \
    "ds_read_b64_tr_b16 v[156:157], v229 offset:28672\n\t" \
    "v_mfma_f32_16x16x32_bf16 v[190:193], v[124:127], v[76:79], v[190:193]\n\t" \
    "ds_read_b64_tr_b16 v[158:159], v229 offset:30720\n\t" \
    "s_waitcnt vmcnt(3) lgkmcnt(8)\n\t" \
    "v_mfma_f32_16x16x32_bf16 v[162:165], v[128:131], v[80:83], v[162:165]\n\t" \
    "ds_read_b32 v231, v249 offset:68\n\t" \
    "v_mfma_f32_16x16x32_bf16 v[178:181], v[128:131], v[84:87], v[178:181]\n\t" \
    "v_add3_u32 v230, v80, v81, v230\n\t" \
    "v_mfma_f32_16x16x32_bf16 v[166:169], v[132:135], v[80:83], v[166:169]\n\t" \
    "ds_read_b128 v[96:99], v252\n\t" \
    "v_mfma_f32_16x16x32_bf16 v[182:185], v[132:135], v[84:87], v[182:185]\n\t" \
    "ds_read_b128 v[100:103], v252 offset:64\n\t" \
    "v_add3_u32 v230, v82, v83, v230\n\t" \
    "v_mfma_f32_16x16x32_bf16 v[170:173], v[136:139], v[80:83], v[170:173]\n\t" \
    "ds_read_b128 v[104:107], v252 offset:128\n\t" \
    "v_add3_u32 v230, v84, v85, v230\n\t" \
    "v_mfma_f32_16x16x32_bf16 v[186:189], v[136:139], v[84:87], v[186:189]\n\t" \
    "ds_read_b128 v[108:111], v252 offset:192\n\t" \
    "v_add3_u32 v230, v86, v87, v230\n\t" \
    "v_mfma_f32_16x16x32_bf16 v[174:177], v[140:143], v[80:83], v[174:177]\n\t" \
    "v_mfma_f32_16x16x32_bf16 v[190:193], v[140:143], v[84:87], v[190:193]\n\t" \
    "s_waitcnt vmcnt(1) lgkmcnt(5)\n\t" \
    "v_mfma_f32_16x16x32_bf16 v[162:165], v[144:147], v[88:91], v[162:165]\n\t" \
    "v_mfma_f32_16x16x32_bf16 v[166:169], v[148:151], v[88:91], v[166:169]\n\t" \
    "v_add3_u32 v230, v88, v89, v230\n\t" \
    "v_mfma_f32_16x16x32_bf16 v[170:173], v[152:155], v[88:91], v[170:173]\n\t" \
    "v_mfma_f32_16x16x32_bf16 v[174:177], v[156:159], v[88:91], v[174:177]\n\t" \
    "v_add3_u32 v230, v90, v91, v230\n\t" \
    "v_mfma_f32_16x16x32_bf16 v[178:181], v[144:147], v[92:95], v[178:181]\n\t" \
    "v_add3_u32 v230, v92, v93, v230\n\t" \
    "v_mfma_f32_16x16x32_bf16 v[182:185], v[148:151], v[92:95], v[182:185]\n\t" \
    "v_add3_u32 v230, v94, v95, v230\n\t" \
    "v_mfma_f32_16x16x32_bf16 v[186:189], v[152:155], v[92:95], v[186:189]\n\t" \
    "s_cmp_lg_u64 %[rm], 0\n\t" \
    "v_mfma_f32_16x16x32_bf16 v[190:193], v[156:159], v[92:95], v[190:193]\n\t" \
    "s_cbranch_scc1 .Lc_y\n\t" \
    ".Lc_yr:\n\t" \
    "s_add_u32 s85, %[sout], 0x8000\n\t" \
    "v_pk_add_f32 v[0:1], v[0:1], v[162:163]\n\t" \
    "v_pk_add_f32 v[2:3], v[2:3], v[164:165]\n\t" \
    "v_pk_add_f32 v[4:5], v[4:5], v[166:167]\n\t" \
    "v_pk_add_f32 v[6:7], v[6:7], v[168:169]\n\t" \
    "v_pk_fma_f32 v[0:1], v[232:233], v[0:1], v[194:195] op_sel_hi:[0,1,1]\n\t" \
    "v_pk_fma_f32 v[2:3], v[232:233], v[2:3], v[196:197] op_sel_hi:[0,1,1]\n\t" \
    "v_pk_fma_f32 v[4:5], v[232:233], v[4:5], v[198:199] op_sel_hi:[0,1,1]\n\t" \
    "v_pk_fma_f32 v[6:7], v[232:233], v[6:7], v[200:201] op_sel_hi:[0,1,1]\n\t" \
    "v_cvt_pk_bf16_f32 v32, v0, v1\n\t" \
    "v_cvt_pk_bf16_f32 v34, v4, v5\n\t" \
    "v_cvt_pk_bf16_f32 v33, v2, v3\n\t" \
    "v_cvt_pk_bf16_f32 v35, v6, v7\n\t" \
    "s_nop 0\n\t" \
    "v_permlane16_swap_b32 v32, v34\n\t" \
    "v_permlane16_swap_b32 v33, v35\n\t" \
    "buffer_store_dwordx4 v[32:35], v247, %[rso], %[sout] offen sc1\n\t" \
    "v_pk_add_f32 v[8:9], v[8:9], v[170:171]\n\t" \
    "v_pk_add_f32 v[10:11], v[10:11], v[172:173]\n\t" \
    "v_pk_add_f32 v[12:13], v[12:13], v[174:175]\n\t" \
    "v_pk_add_f32 v[14:15], v[14:15], v[176:177]\n\t" \
    "v_pk_fma_f32 v[8:9], v[232:233], v[8:9], v[202:203] op_sel_hi:[0,1,1]\n\t" \
    "v_pk_fma_f32 v[10:11], v[232:233], v[10:11], v[204:205] op_sel_hi:[0,1,1]\n\t" \
    "v_pk_fma_f32 v[12:13], v[232:233], v[12:13], v[206:207] op_sel_hi:[0,1,1]\n\t" \
    "v_pk_fma_f32 v[14:15], v[232:233], v[14:15], v[208:209] op_sel_hi:[0,1,1]\n\t" \
    "v_cvt_pk_bf16_f32 v36, v8, v9\n\t" \
    "v_cvt_pk_bf16_f32 v38, v12, v13\n\t" \
    "v_cvt_pk_bf16_f32 v37, v10, v11\n\t" \
    "v_cvt_pk_bf16_f32 v39, v14, v15\n\t" \
    "v_cmp_gt_u32 vcc, 32, v250\n\t" \
    "v_permlane16_swap_b32 v36, v38\n\t" \
    "v_permlane16_swap_b32 v37, v39\n\t" \
    "buffer_store_dwordx4 v[36:39], v247, %[rso], %[sout] offen offset:64 sc1\n\t" \
    "v_pk_add_f32 v[16:17], v[16:17], v[178:179]\n\t" \
    "v_pk_add_f32 v[18:19], v[18:19], v[180:181]\n\t" \
    "v_pk_add_f32 v[20:21], v[20:21], v[182:183]\n\t" \
    "v_pk_add_f32 v[22:23], v[22:23], v[184:185]\n\t" \
    "v_pk_fma_f32 v[16:17], v[234:235], v[16:17], v[210:211] op_sel_hi:[0,1,1]\n\t" \
    "v_pk_fma_f32 v[18:19], v[234:235], v[18:19], v[212:213] op_sel_hi:[0,1,1]\n\t" \
    "v_pk_fma_f32 v[20:21], v[234:235], v[20:21], v[214:215] op_sel_hi:[0,1,1]\n\t" \
    "v_pk_fma_f32 v[22:23], v[234:235], v[22:23], v[216:217] op_sel_hi:[0,1,1]\n\t" \
    "v_cvt_pk_bf16_f32 v40, v16, v17\n\t" \
    "v_cvt_pk_bf16_f32 v42, v20, v21\n\t" \
    "v_cvt_pk_bf16_f32 v41, v18, v19\n\t" \
    "v_cvt_pk_bf16_f32 v43, v22, v23\n\t" \
    "s_waitcnt vmcnt(2)\n\t" \
    "v_permlane16_swap_b32 v40, v42\n\t" \
    "v_permlane16_swap_b32 v41, v43\n\t" \
    "buffer_store_dwordx4 v[40:43], v247, %[rso], s85 offen sc1\n\t" \
    "v_pk_add_f32 v[24:25], v[24:25], v[186:187]\n\t" \
    "v_pk_add_f32 v[26:27], v[26:27], v[188:189]\n\t" \
    "v_pk_add_f32 v[28:29], v[28:29], v[190:191]\n\t" \
    "v_pk_add_f32 v[30:31], v[30:31], v[192:193]\n\t" \
    "v_pk_fma_f32 v[24:25], v[234:235], v[24:25], v[218:219] op_sel_hi:[0,1,1]\n\t" \
    "v_pk_fma_f32 v[26:27], v[234:235], v[26:27], v[220:221] op_sel_hi:[0,1,1]\n\t" \
    "v_pk_fma_f32 v[28:29], v[234:235], v[28:29], v[222:223] op_sel_hi:[0,1,1]\n\t" \
    "v_pk_fma_f32 v[30:31], v[234:235], v[30:31], v[224:225] op_sel_hi:[0,1,1]\n\t" \
    "v_cvt_pk_bf16_f32 v44, v24, v25\n\t" \
    "v_cvt_pk_bf16_f32 v46, v28, v29\n\t" \
    "v_cvt_pk_bf16_f32 v45, v26, v27\n\t" \
    "v_cvt_pk_bf16_f32 v47, v30, v31\n\t" \
    "s_waitcnt lgkmcnt(4)\n\t" \
    "v_cndmask_b32 v231, 0, v160, vcc\n\t" \
    "v_permlane16_swap_b32 v44, v46\n\t" \
    "v_permlane16_swap_b32 v45, v47\n\t" \
    "buffer_store_dwordx4 v[44:47], v247, %[rso], s85 offen offset:64 sc1\n\t" \
    "v_sub_u32 v231, v230, v231\n\t" \
    "v_cmp_ne_u32 vcc, v236, v161\n\t" \
    "v_mov_b32 v0, v231\n\t" \
    "s_nop 1\n\t" \
    "v_permlane32_swap_b32 v231, v0\n\t" \
    "v_add_u32 v231, v231, v0\n\t" \
    "v_mov_b32 v0, v231\n\t" \
    "s_nop 1\n\t" \
    "v_permlane16_swap_b32 v231, v0\n\t" \
    "v_add_u32 v231, v231, v0\n\t" \
    "s_nop 1\n\t" \
    "v_add_u32_dpp v231, v231, v231 row_ror:8 row_mask:0xf bank_mask:0xf bound_ctrl:1\n\t" \
    "s_nop 1\n\t" \
    "v_add_u32_dpp v231, v231, v231 row_ror:4 row_mask:0xf bank_mask:0xf bound_ctrl:1\n\t" \
    "s_nop 1\n\t" \
    "v_add_u32_dpp v231, v231, v231 row_ror:2 row_mask:0xf bank_mask:0xf bound_ctrl:1\n\t" \
    "s_nop 1\n\t" \
    "v_add_u32_dpp v231, v231, v231 row_ror:1 row_mask:0xf bank_mask:0xf bound_ctrl:1\n\t" \
    "s_nop 0\n\t" \
    "v_readfirstlane_b32 s83, v231\n\t" \
    "s_or_b32 s83, s83, vcc_lo\n\t" \
    "s_cselect_b32 %[st], 2, 0\n\t" \
    "s_waitcnt lgkmcnt(0)\n\t" \
    ".Lc_end:\n\t"
#define RED1_ASM \
    "s_mov_b32 s80, 256\n\t" \
    "s_add_u32 s81, %[s0], 0x1000\n\t" \
    "v_mov_b32 v41, 1\n\t" \
    ".Lr_t:\n\t" \
    "s_memrealtime s[82:83]\n\t" \
    "s_sub_u32 s80, s80, 1\n\t" \
    "s_cbranch_scc1 .Lr_go\n\t" \
    "s_waitcnt lgkmcnt(0)\n\t" \
    "s_sub_u32 s82, %[t1], s82\n\t" \
    "s_cmp_lt_i32 s82, 5\n\t" \
    "s_cbranch_scc1 .Lr_go\n\t" \
    "s_cmp_lt_i32 s82, 13\n\t" \
    "s_cbranch_scc1 .Lr_t\n\t" \
    "s_sleep 2\n\t" \
    "s_branch .Lr_t\n\t" \
    ".Lr_go:\n\t" \
    "buffer_load_dwordx4 v[0:3], %[v16], %[rsb], %[sm] offen sc1\n\t" \
    "buffer_load_dwordx4 v[4:7], %[v16], %[rsb], %[s0] offen sc1\n\t" \
    "buffer_load_dwordx4 v[8:11], %[v16], %[rsb], %[s0] offen offset:1024 sc1\n\t" \
    "buffer_load_dwordx4 v[12:15], %[v16], %[rsb], %[s0] offen offset:2048 sc1\n\t" \
    "buffer_load_dwordx4 v[16:19], %[v16], %[rsb], %[s0] offen offset:3072 sc1\n\t" \
    "buffer_load_dwordx4 v[20:23], %[v16], %[rsb], s81 offen sc1\n\t" \
    "buffer_load_dwordx4 v[24:27], %[v16], %[rsb], s81 offen offset:1024 sc1\n\t" \
    "buffer_load_dwordx4 v[28:31], %[v16], %[rsb], s81 offen offset:2048 sc1\n\t" \
    "buffer_load_dwordx4 v[32:35], %[v16], %[rsb], s81 offen offset:3072 sc1\n\t" \
    "ds_write_b32 %[ra], v41\n\t" \
    "s_waitcnt vmcnt(8)\n\t" \
    "v_add3_u32 v40, v0, v1, v2\n\t" \
    "v_xor_b32 v41, %[tag], v1\n\t" \
    "v_and_b32 v42, %[mk2], v0\n\t" \
    "v_add_u32 v40, v40, v3\n\t" \
    "v_add_f32_dpp v0, v0, v0 row_ror:8 row_mask:0xf bank_mask:0xf bound_ctrl:1\n\t" \
    "v_add_f32_dpp v1, v1, v1 row_ror:8 row_mask:0xf bank_mask:0xf bound_ctrl:1\n\t" \
    "v_add_f32_dpp v2, v2, v2 row_ror:8 row_mask:0xf bank_mask:0xf bound_ctrl:1\n\t" \
    "v_add_f32_dpp v3, v3, v3 row_ror:8 row_mask:0xf bank_mask:0xf bound_ctrl:1\n\t" \
    "v_and_b32 v40, %[mk01], v40\n\t" \
    "v_sub_u32 v40, v40, v42\n\t" \
    "s_waitcnt vmcnt(7)\n\t" \
    "v_mfma_f32_16x16x32_bf16 v[36:39], %[sel], v[4:7], 0\n\t" \
    "v_add3_u32 v40, v4, v5, v40\n\t" \
    "v_add3_u32 v40, v6, v7, v40\n\t" \
    "v_add_f32_dpp v0, v0, v0 row_ror:4 row_mask:0xf bank_mask:0xf bound_ctrl:1\n\t" \
    "v_add_f32_dpp v1, v1, v1 row_ror:4 row_mask:0xf bank_mask:0xf bound_ctrl:1\n\t" \
    "s_waitcnt vmcnt(6)\n\t" \
    "v_mfma_f32_16x16x32_bf16 v[36:39], %[sel], v[8:11], v[36:39]\n\t" \
    "v_add3_u32 v40, v8, v9, v40\n\t" \
    "v_add3_u32 v40, v10, v11, v40\n\t" \
    "v_add_f32_dpp v2, v2, v2 row_ror:4 row_mask:0xf bank_mask:0xf bound_ctrl:1\n\t" \
    "v_add_f32_dpp v3, v3, v3 row_ror:4 row_mask:0xf bank_mask:0xf bound_ctrl:1\n\t" \
    "s_waitcnt vmcnt(5)\n\t" \
    "v_mfma_f32_16x16x32_bf16 v[36:39], %[sel], v[12:15], v[36:39]\n\t" \
    "v_add3_u32 v40, v12, v13, v40\n\t" \
    "v_add3_u32 v40, v14, v15, v40\n\t" \
    "v_and_b32 v41, %[mk2], v41\n\t" \
    "v_cmp_ne_u32 vcc, 0, v41\n\t" \
    "s_waitcnt vmcnt(4)\n\t" \
    "v_mfma_f32_16x16x32_bf16 v[36:39], %[sel], v[16:19], v[36:39]\n\t" \
    "v_add3_u32 v40, v16, v17, v40\n\t" \
    "v_add3_u32 v40, v18, v19, v40\n\t" \
    "v_permlane32_swap_b32 v0, v2\n\t" \
    "v_permlane32_swap_b32 v1, v3\n\t" \
    "s_waitcnt vmcnt(3)\n\t" \
    "v_mfma_f32_16x16x32_bf16 v[36:39], %[sel], v[20:23], v[36:39]\n\t" \
    "v_add3_u32 v40, v20, v21, v40\n\t" \
    "v_add3_u32 v40, v22, v23, v40\n\t" \
    "v_add_f32 v0, v0, v2\n\t" \
    "v_add_f32 v1, v1, v3\n\t" \
    "s_waitcnt vmcnt(2)\n\t" \
    "v_mfma_f32_16x16x32_bf16 v[36:39], %[sel], v[24:27], v[36:39]\n\t" \
    "v_add3_u32 v40, v24, v25, v40\n\t" \
    "v_add3_u32 v40, v26, v27, v40\n\t" \
    "v_mov_b32 v2, v0\n\t" \
    "v_mov_b32 v3, v1\n\t" \
    "s_waitcnt vmcnt(1)\n\t" \
    "v_mfma_f32_16x16x32_bf16 v[36:39], %[sel], v[28:31], v[36:39]\n\t" \
    "v_add3_u32 v40, v28, v29, v40\n\t" \
    "v_add3_u32 v40, v30, v31, v40\n\t" \
    "s_or_b32 s80, vcc_lo, vcc_hi\n\t" \
    "s_waitcnt vmcnt(0)\n\t" \
    "v_mfma_f32_16x16x32_bf16 v[36:39], %[sel], v[32:35], v[36:39]\n\t" \
    "v_add3_u32 v40, v32, v33, v40\n\t" \
    "v_add3_u32 v40, v34, v35, v40\n\t" \
    "v_permlane16_swap_b32 v0, v2\n\t" \
    "v_permlane16_swap_b32 v1, v3\n\t" \
    "v_add_f32 v0, v0, v2\n\t" \
    "v_add_f32 v1, v1, v3\n\t" \
    "v_cvt_pk_bf16_f32 v44, v0, v1\n\t" \
    "v_mov_b32 v45, v44\n\t" \
    "v_cvt_pk_bf16_f32 v48, v36, v37\n\t" \
    "v_cvt_pk_bf16_f32 v49, v38, v39\n\t" \
    "v_and_b32 v46, %[mkl], v44\n\t" \
    "v_add3_u32 v46, v46, v48, v49\n\t" \
    "v_permlane32_swap_b32 v44, v45\n\t" \
    "buffer_store_dwordx2 v[48:49], %[vkf], %[rsb], %[sf] offen sc1\n\t" \
    "v_permlane32_swap_b32 v40, v46\n\t" \
    "v_add_u32 v40, v40, v46\n\t" \
    "v_mov_b32 v46, v40\n\t" \
    "v_cndmask_b32_e64 v45, v45, %[tag], %[hi]\n\t" \
    "s_nop 0\n\t" \
    "v_permlane16_swap_b32 v40, v46\n\t" \
    "v_add_u32 v40, v40, v46\n\t" \
    "s_nop 1\n\t" \
    "v_add_u32_dpp v40, v40, v40 row_ror:8 row_mask:0xf bank_mask:0xf bound_ctrl:1\n\t" \
    "s_nop 1\n\t" \
    "v_add_u32_dpp v40, v40, v40 row_ror:4 row_mask:0xf bank_mask:0xf bound_ctrl:1\n\t" \
    "s_nop 1\n\t" \
    "v_add_u32_dpp v40, v40, v40 row_ror:2 row_mask:0xf bank_mask:0xf bound_ctrl:1\n\t" \
    "s_nop 1\n\t" \
    "v_add_u32_dpp v40, v40, v40 row_ror:1 row_mask:0xf bank_mask:0xf bound_ctrl:1\n\t" \
    "s_nop 0\n\t" \
    "v_readfirstlane_b32 s81, v40\n\t" \
    "v_cndmask_b32_e64 v44, v44, v40, %[hi]\n\t" \
    "s_or_b32 %[st], s80, s81\n\t" \
    "s_cselect_b32 s82, 0x70000000, %[sg]\n\t" \
    "buffer_store_dwordx2 v[44:45], %[vfm], %[rsb], s82 offen sc1\n\t"
#define RED2_ASM \
    "s_mov_b32 s80, 256\n\t" \
    "s_add_u32 s81, %[s0], 0x1000\n\t" \
    "s_add_u32 s84, %[s0], 0x4000\n\t" \
    "s_mov_b32 m0, 160256\n\t" \
    ".Lr_u:\n\t" \
    "s_memrealtime s[82:83]\n\t" \
    "s_sub_u32 s80, s80, 1\n\t" \
    "s_cbranch_scc1 .Lr_g2\n\t" \
    "s_waitcnt lgkmcnt(0)\n\t" \
    "s_sub_u32 s82, %[t2], s82\n\t" \
    "s_cmp_lt_i32 s82, 5\n\t" \
    "s_cbranch_scc1 .Lr_g2\n\t" \
    "s_cmp_lt_i32 s82, 13\n\t" \
    "s_cbranch_scc1 .Lr_u\n\t" \
    "s_sleep 2\n\t" \
    "s_branch .Lr_u\n\t" \
    ".Lr_g2:\n\t" \
    "buffer_load_dwordx4 %[v16], %[rsb], s84 offen sc1 lds\n\t" \
    "s_mov_b32 m0, 151552\n\t" \
    "s_nop 0\n\t" \
    "buffer_load_dwordx4 %[v16], %[rsb], %[s0] offen sc1 lds\n\t" \
    "buffer_load_dwordx4 %[v16], %[rsb], %[s0] offen offset:1024 sc1 lds\n\t" \
    "buffer_load_dwordx4 %[v16], %[rsb], %[s0] offen offset:2048 sc1 lds\n\t" \
    "buffer_load_dwordx4 %[v16], %[rsb], %[s0] offen offset:3072 sc1 lds\n\t" \
    "s_mov_b32 m0, 155648\n\t" \
    "s_nop 0\n\t" \
    "buffer_load_dwordx4 %[v16], %[rsb], s81 offen sc1 lds\n\t" \
    "buffer_load_dwordx4 %[v16], %[rsb], s81 offen offset:1024 sc1 lds\n\t" \
    "buffer_load_dwordx4 %[v16], %[rsb], s81 offen offset:2048 sc1 lds\n\t" \
    "buffer_load_dwordx4 %[v16], %[rsb], s81 offen offset:3072 sc1 lds\n\t" \
    "ds_write_b32 %[ra], %[pv]\n\t" \
    "v_or_b32 %[pv], 1, %[pv]\n\t" \
    "v_add_u32 %[pv], 1, %[pv]\n\t" \
    "s_waitcnt vmcnt(0)\n\t" \
    "ds_write_b32 %[ra], %[pv]\n\t" \
    "ds_read_b128 v[0:3], %[kwa] offset:8704\n\t" \
    "ds_read_b128 v[4:7], %[kwa]\n\t" \
    "ds_read_b128 v[8:11], %[kwa] offset:1024\n\t" \
    "ds_read_b128 v[12:15], %[kwa] offset:2048\n\t" \
    "ds_read_b128 v[16:19], %[kwa] offset:3072\n\t" \
    "ds_read_b128 v[20:23], %[kwa] offset:4096\n\t" \
    "ds_read_b128 v[24:27], %[kwa] offset:5120\n\t" \
    "ds_read_b128 v[28:31], %[kwa] offset:6144\n\t" \
    "ds_read_b128 v[32:35], %[kwa] offset:7168\n\t" \
    "s_waitcnt lgkmcnt(8)\n\t" \
    "v_add3_u32 v40, v0, v1, v2\n\t" \
    "v_xor_b32 v41, %[tag], v1\n\t" \
    "v_and_b32 v42, %[mk1], v0\n\t" \
    "v_add_u32 v40, v40, v3\n\t" \
    "v_and_b32 v41, %[mk1], v41\n\t" \
    "v_and_b32 v40, %[mk0], v40\n\t" \
    "v_cmp_ne_u32 vcc, 0, v41\n\t" \
    "v_sub_u32 v40, v40, v42\n\t" \
    "v_mov_b32 v42, %[frc]\n\t" \
    "s_waitcnt lgkmcnt(4)\n\t" \
    "v_add3_u32 v40, v4, v5, v40\n\t" \
    "v_add3_u32 v42, v6, v7, v42\n\t" \
    "v_add3_u32 v40, v8, v9, v40\n\t" \
    "v_add3_u32 v42, v10, v11, v42\n\t" \
    "v_add3_u32 v40, v12, v13, v40\n\t" \
    "v_add3_u32 v42, v14, v15, v42\n\t" \
    "v_add3_u32 v40, v16, v17, v40\n\t" \
    "v_add3_u32 v42, v18, v19, v42\n\t" \
    "s_waitcnt lgkmcnt(0)\n\t" \
    "v_add3_u32 v40, v20, v21, v40\n\t" \
    "v_add3_u32 v42, v22, v23, v42\n\t" \
    "v_add3_u32 v40, v24, v25, v40\n\t" \
    "v_add3_u32 v42, v26, v27, v42\n\t" \
    "v_add3_u32 v40, v28, v29, v40\n\t" \
    "v_add3_u32 v42, v30, v31, v42\n\t" \
    "v_add3_u32 v40, v32, v33, v40\n\t" \
    "v_add3_u32 v42, v34, v35, v42\n\t" \
    "s_or_b32 s80, vcc_lo, vcc_hi\n\t" \
    "s_nop 0\n\t" \
    "v_permlane32_swap_b32 v40, v42\n\t" \
    "v_add_u32 v40, v40, v42\n\t" \
    "s_nop 1\n\t" \
    "v_add_u32_dpp v40, v40, v40 row_ror:8 row_mask:0xf bank_mask:0xf bound_ctrl:1\n\t" \
    "s_nop 1\n\t" \
    "v_add_u32_dpp v40, v40, v40 row_ror:4 row_mask:0xf bank_mask:0xf bound_ctrl:1\n\t" \
    "s_nop 1\n\t" \
    "v_add_u32_dpp v40, v40, v40 row_ror:2 row_mask:0xf bank_mask:0xf bound_ctrl:1\n\t" \
    "s_nop 1\n\t" \
    "v_add_u32_dpp v40, v40, v40 row_ror:1 row_mask:0xf bank_mask:0xf bound_ctrl:1\n\t" \
    "s_nop 0\n\t" \
    "v_readlane_b32 s81, v40, 0\n\t" \
    "v_readlane_b32 s82, v40, 16\n\t" \
    "v_readlane_b32 s83, v40, 32\n\t" \
    "v_readlane_b32 s84, v40, 48\n\t" \
    "s_add_u32 s81, s81, s82\n\t" \
    "s_add_u32 s83, s83, s84\n\t" \
    "s_add_u32 s81, s81, s83\n\t" \
    "s_or_b32 %[st], s80, s81\n\t" \
    "s_cselect_b32 s81, 0, 1\n\t" \
    "v_add_u32 v41, s81, %[pv]\n\t" \
    "ds_write_b32 %[ra], v41\n\t"
#define PHASE_AB_CLOB "v0", "v1", "v2", "v3", "v4", "v5", "v6", "v7", "v8", "v9", "v10", "v11", "v12", "v13", "v14", "v15", "v16", "v17", "v18", "v19", "v20", "v21", "v22", "v23", "v24", "v25", "v26", "v27", "v28", "v29", "v30", "v31", "v32", "v33", "v34", "v35", "v36", "v37", "v38", "v39", "v40", "v41", "v42", "v43", "v44", "v45", "v46", "v47", "v48", "v49", "v50", "v51", "v52", "v53", "v54", "v55", "v56", "v57", "v58", "v59", "v60", "v61", "v62", "v63", "v64", "v65", "v66", "v67", "v68", "v69", "v70", "v71", "v72", "v73", "v74", "v75", "v76", "v77", "v78", "v79", "v80", "v81", "v82", "v83", "v84", "v85", "v86", "v87", "v88", "v89", "v90", "v91", "v92", "v93", "v94", "v95", "v96", "v97", "v98", "v99", "v100", "v101", "v102", "v103", "v104", "v105", "v106", "v107", "v108", "v109", "v110", "v111", "v112", "v113", "v114", "v115", "v116", "v117", "v118", "v119", "v120", "v121", "v122", "v123", "v124", "v125", "v126", "v127", "v128", "v129", "v130", "v131", "v132", "v133", "v134", "v135", "v136", "v137", "v138", "v139", "v140", "v141", "v142", "v143", "v144", "v145", "v146", "v147", "v148", "v149", "v150", "v151", "v152", "v153", "v154", "v155", "v156", "v157", "v158", "v159", "v160", "v161", "v162", "v163", "v164", "v165", "v166", "v167", "v168", "v169", "v170", "v171", "v172", "v173", "v174", "v175", "v176", "v177", "v178", "v179", "v180", "v181", "v182", "v183", "v184", "v185", "v186", "v187", "v188", "v189", "v190", "v191", "v192", "v193", "v194", "v195", "v196", "v197", "v198", "v199", "v200", "v201", "v202", "v203", "v204", "v205", "v206", "v207", "v208", "v209", "v210", "v211", "v212", "v213", "v214", "v215", "v216", "v217", "v218", "v219", "v220", "v221", "v222", "v223", "v224", "v225", "v226", "v227", "v228", "v229", "v230", "v231", "v237", "v238", "s81", "s82", "s83", "s84", "s85", "s86", "s87", "s88", "s89", "s90", "s91", "s92", "s93", "s94", "s95"
#define PHASE_C_CLOB "v0", "v1", "v2", "v3", "v4", "v5", "v6", "v7", "v8", "v9", "v10", "v11", "v12", "v13", "v14", "v15", "v16", "v17", "v18", "v19", "v20", "v21", "v22", "v23", "v24", "v25", "v26", "v27", "v28", "v29", "v30", "v31", "v32", "v33", "v34", "v35", "v36", "v37", "v38", "v39", "v40", "v41", "v42", "v43", "v44", "v45", "v46", "v47", "v48", "v49", "v50", "v51", "v52", "v53", "v54", "v55", "v56", "v57", "v58", "v59", "v60", "v61", "v62", "v63", "v64", "v65", "v66", "v67", "v68", "v69", "v70", "v71", "v72", "v73", "v74", "v75", "v76", "v77", "v78", "v79", "v80", "v81", "v82", "v83", "v84", "v85", "v86", "v87", "v88", "v89", "v90", "v91", "v92", "v93", "v94", "v95", "v96", "v97", "v98", "v99", "v100", "v101", "v102", "v103", "v104", "v105", "v106", "v107", "v108", "v109", "v110", "v111", "v112", "v113", "v114", "v115", "v116", "v117", "v118", "v119", "v120", "v121", "v122", "v123", "v124", "v125", "v126", "v127", "v128", "v129", "v130", "v131", "v132", "v133", "v134", "v135", "v136", "v137", "v138", "v139", "v140", "v141", "v142", "v143", "v144", "v145", "v146", "v147", "v148", "v149", "v150", "v151", "v152", "v153", "v154", "v155", "v156", "v157", "v158", "v159", "v160", "v161", "v162", "v163", "v164", "v165", "v166", "v167", "v168", "v169", "v170", "v171", "v172", "v173", "v174", "v175", "v176", "v177", "v178", "v179", "v180", "v181", "v182", "v183", "v184", "v185", "v186", "v187", "v188", "v189", "v190", "v191", "v192", "v193", "v194", "v195", "v196", "v197", "v198", "v199", "v200", "v201", "v202", "v203", "v204", "v205", "v206", "v207", "v208", "v209", "v210", "v211", "v212", "v213", "v214", "v215", "v216", "v217", "v218", "v219", "v220", "v221", "v222", "v223", "v224", "v225", "v226", "v227", "v228", "v229", "v231", "s80", "s81", "s82", "s83", "s84", "s85", "s86", "s87"
#define RED1_CLOB "v0", "v1", "v2", "v3", "v4", "v5", "v6", "v7", "v8", "v9", "v10", "v11", "v12", "v13", "v14", "v15", "v16", "v17", "v18", "v19", "v20", "v21", "v22", "v23", "v24", "v25", "v26", "v27", "v28", "v29", "v30", "v31", "v32", "v33", "v34", "v35", "v36", "v37", "v38", "v39", "v40", "v41", "v42", "v43", "v44", "v45", "v46", "v47", "v48", "v49", "s80", "s81", "s82", "s83", "s84"

#define KNAME "fused_fwd"
#define CODE_FETCH(dst, kb) do { unsigned long long pc_; unsigned len_, off_; \
    asm volatile("s_getpc_b64 %0\n1:\n\ts_mov_b32 %1, 1b-" KNAME "\n\ts_mov_b32 %2, .Lff_end0-" KNAME "\n\ts_max_u32 %2, %2, .Lff_end1-" KNAME \
                 "\n\ts_max_u32 %2, %2, .Lff_end2-" KNAME : "=s"(pc_), "=s"(off_), "=s"(len_) :: "scc"); \
    const rsrc_t rsC_ = mk_rsrc((const void*)(pc_ - off_), len_); \
    asm volatile("s_nop 4\n\tbuffer_load_dwordx4 %0, %1, %2, %3 offen" : "=v"(dst) : "v"(fresh_lane() * 16), "s"(rsC_), "s"((unsigned)(kb) * 1024u) : "memory"); } while (0)

extern "C" __global__ __launch_bounds__(512, 2) void fused_fwd(const u16* __restrict__ x, const u16* __restrict__ wqkv,
                                                    const u16* __restrict__ wphi, const u16* __restrict__ wo,
                                                    const u16* __restrict__ wr, const u16* __restrict__ wgp,
                                                    const u16* __restrict__ wup, const u16* __restrict__ wdp,
                                                    const u16* __restrict__ gw, char* ws, u16* out) {
    __shared__ __attribute__((aligned(16))) char smem[163840];
    const int w = __builtin_amdgcn_readfirstlane(threadIdx.x >> 6);
    if (K::TOPV) {
        asm volatile("s_mov_b64 exec, 0\n\ts_cmp_eq_u32 %0, 6\n\ts_cbranch_scc1 .Lpad_x\n\t"
                     "s_cmp_eq_u32 %0, 0\n\ts_cbranch_scc1 .Lpad_d\n\ts_cmp_eq_u32 %0, 1\n\ts_cbranch_scc1 .Lpad_ab0\n"
                     ".Lpad_top:\n\ts_mov_b64 exec, -1" :: "s"(w) : "scc", "memory");
    } else {
        asm volatile(".Lpad_top:");
    }
    asm volatile("" :: "s"(x), "s"(wqkv));
    const int bid = blockIdx.x, xcd = bid & 7, slot = bid >> 3;
    const int mtA = (xcd & 1) * 8 + (slot & 7), hpA = (xcd >> 1) * 4 + (slot >> 3);
    const int mtB = (xcd & 1) * 8 + ((xcd >> 1) & 1) * 4 + (slot & 3), hpB = (xcd >> 2) * 8 + (slot >> 2);
    const int rotA = 8 * (xcd & 1);
    const bool has_ffn = slot < 24 || (xcd & 2);
    const int fb = slot < 24 ? bid : 192 + (slot - 24) * 4 + (xcd >> 2) * 2 + (xcd & 1);
    const unsigned lds0 = (unsigned)(size_t)(AS3 char*)smem;
    const rsrc_t rsWS = mk_rsrc(ws, WS_BYTES);
    const rsrc_t rsX = mk_rsrc(x, 2048u * 2048u);

    auto touch3 = [&](unsigned& dst, int half, bool on = true) {
        const rsrc_t rsT = mk_rsrc(wqkv, (on && slot < 24 && !(xcd & 2)) ? 1024u * 6144u : 0u);
        const unsigned so = (unsigned)(((slot & 7) + 8 * half + rotA) & 15) * (64u * 6144u) + (unsigned)(slot >> 3) * 2048u + (unsigned)((xcd >> 1) * 4 + 3) * 128u;
        asm volatile("s_nop 4\n\tbuffer_load_dword %0, %1, %2, %3 offen" : "=v"(dst) : "v"(fresh_lane() * 6144), "s"(rsT), "s"(so) : "memory");
    };
    unsigned gate_t = 0;
    int gate_s = 0;
    auto hush = [&]() {
        if (gate_s == 0) {
            gate_t = __builtin_amdgcn_readfirstlane(*(AS3 volatile const unsigned*)(size_t)(lds0 + L_TM + 8)) - K::QA;
            gate_s = 1;
        }
        if (gate_s == 3 || (int)(now32() - gate_t) < 0) return;
        if (gate_s == 1) {
            wait4(lds0 + C_P, P_PUB);
            lds_wait(lds0 + C_TM, 1);
            gate_t = __builtin_amdgcn_readfirstlane(*(AS3 volatile const unsigned*)(size_t)(lds0 + L_TM)) - K::QB;
            gate_s = 2;
            if ((int)(now32() - gate_t) < 0) return;
        }
        lds_wait(lds0 + C_R, R_L1I);
        gate_s = 3;
    };

    unsigned ep, pf0;
    u32x4 xv[4][2], rv[2], gv[2], pf;
    auto ldi = [&]() {
            const int l = fresh_lane(), a = w & 1, row0 = mtB * 128 + hpB * 8 + 4 * a;
            const rsrc_t rsR = mk_rsrc(wr, 2048u), rsGW = mk_rsrc(gw, 2048u);
            const unsigned v16 = (unsigned)l * 16u;
            asm volatile("s_nop 4\n\tbuffer_load_dword %0, off, %1, 0 sc1" : "=v"(ep) : "s"(rsWS) : "memory");
#pragma unroll
            for (int r = 0; r < 4; ++r) {
                asm volatile("buffer_load_dwordx4 %0, %1, %2, %3 offen" : "=v"(xv[r][0]) : "v"(v16), "s"(rsX), "s"((unsigned)(row0 + r) * 2048u) : "memory");
                asm volatile("buffer_load_dwordx4 %0, %1, %2, %3 offen offset:1024" : "=v"(xv[r][1]) : "v"(v16), "s"(rsX), "s"((unsigned)(row0 + r) * 2048u) : "memory");
            }
            asm volatile("buffer_load_dwordx4 %0, %1, %2, 0 offen" : "=v"(rv[0]) : "v"(v16), "s"(rsR) : "memory");
            asm volatile("buffer_load_dwordx4 %0, %1, %2, 0 offen offset:1024" : "=v"(rv[1]) : "v"(v16), "s"(rsR) : "memory");
            asm volatile("buffer_load_dwordx4 %0, %1, %2, 0 offen" : "=v"(gv[0]) : "v"(v16), "s"(rsGW) : "memory");
            asm volatile("buffer_load_dwordx4 %0, %1, %2, 0 offen offset:1024" : "=v"(gv[1]) : "v"(v16), "s"(rsGW) : "memory");
            {
                const rsrc_t rsP = mk_rsrc(wphi, 32u * 2048u);
                const unsigned vp = (unsigned)(l ^ (((l >> 4) & 1) << 1)) * 16u;
#pragma unroll
                for (int i = 0; i < 2; ++i) dma16(rsP, vp, (unsigned)hpA * 4096u + a * 2048u + i * 1024u, lds0 + L_WP + a * 2048 + i * 1024);
            }
            CODE_FETCH(pf, slot * 2 + a);
            touch3(pf0, 0, a == 0);
    };
    if (__builtin_expect((w >> 1) == 2, 1)) {
        const int d = w - 4;
        LANE_VARS;
        const int srow = l >> 3, pos = l & 7;
        const rsrc_t rsW = mk_rsrc(wqkv, 1024u * 6144u);
        const unsigned vx0 = srow * 2048 + ((pos ^ ((srow >> 1) & 7)) << 4);
        const unsigned vw0 = srow * 6144 + (((((pos >> 1) ^ (srow & 3)) << 1) | (pos & 1)) << 4);
        unsigned vxs[4], vws[4];
#pragma unroll
        for (int i = 0; i < 4; ++i) {
            vxs[i] = (vx0 ^ ((i & 1) * 64u)) + i * (16384u - 1024u);
            vws[i] = (vw0 ^ ((i & 1) * 32u)) + i * (8u * 6144u - 1024u);
        }
        const unsigned sx0 = (unsigned)(mtA * 128) * 2048u, sw0 = (unsigned)hpA * 128u;
        const int lq = ((slot >> 3) - slot) & 3;
        const unsigned ktab = lq == 0 ? K::KT0 : lq == 1 ? K::KT1 : lq == 2 ? K::KT2 : K::KT3;
        auto chunk = [&](int kt) { return (((ktab >> (4 * (kt >> 1))) & 7u) * 2u + (unsigned)((kt & 1) + rotA)) & 15u; };
        auto dx = [&](unsigned kk, int i, unsigned la) { dma4<K::PACE>(rsX, vxs, sx0 + kk * 128u + i * 65536u, lds0 + la + i * 4096); };
        auto dw = [&](unsigned kk, int p, int i, unsigned la) { dma4<K::PACE>(rsW, vws, sw0 + p * 2048u + (kk * 64u + i * 32u) * 6144u, lds0 + la + i * 4096); };
        asm volatile("s_nop 4" ::: "memory");
        unsigned lb = 0;
#pragma unroll 1
        for (int kt = 0; kt < 13; ++kt) {
            if (kt >= 2) {
                if (kt <= 10) wait_vm<20>(); else wait_vm<16>();
                bar();
            }
            const unsigned kk = chunk(kt);
            dx(kk, 2 * d, lb);
            dw(kk, 1 + d, 0, lb + 24576 + d * 8192);
            dx(kk, 2 * d + 1, lb);
            dw(kk, 1 + d, 1, lb + 24576 + d * 8192);
            if (kt < 10) dw(kk, 0, d, lb + 16384);
            lb = lb == 2 * SLOT ? 0u : lb + SLOT;
        }
        auto dxs = [&](int i) {
            const unsigned kk = chunk(13 + i), lx = d ? XS1[i] - 8192u : XS0[i];
            dx(kk, 2 * d, lx);
            dx(kk, 2 * d + 1, lx);
        };
        auto dws = [&](int i) {
            const unsigned kk = chunk(13 + i), lw = ((1 + i) % 3) * SLOT + 24576 + d * 8192;
            dw(kk, 1 + d, 0, lw);
            dw(kk, 1 + d, 1, lw);
        };
        auto dq = [&](int i) {
            const unsigned kk = chunk(i < 2 ? 14 + i : 8 + i);
            dw(kk, 0, 0, QW[i]);
            dw(kk, 0, 1, QW[i]);
        };
        if (K::XAH) dxs(0);
        wait_vm<K::XAH ? 24 : 16>();
        bar();
        dws(0);
        dxs(K::XAH ? 1 : 0);
        wait_vm<K::XAH ? 24 : 16>();
        bar();
        dws(1);
        dxs(K::XAH ? 2 : 1);
        wait_vm<K::XAH ? 24 : 16>();
        bar();
        dws(2);
        if (!K::XAH) dxs(2);
        if (d == 0) {
            dq(0);
            wait_vm<24>();
        } else {
            wait_vm<16>();
        }
        bar();
        if (d == 0) wait_vm<8>(); else wait_vm<0>();
        post(lds0 + C_ST + d * 4, 1);
        __builtin_amdgcn_s_setprio(3);
        if (d == 0) {
            const rsrc_t rsO = mk_rsrc(wo, 1024u * 2048u);
            unsigned vos[4];
#pragma unroll
            for (int i = 0; i < 4; ++i) vos[i] = WO_VOFF + i * (16384u - 1024u);
            unsigned sb[2] = {0u, 0u};
            auto wo_run = [&](int b0, int b1) {
#pragma unroll
                for (int b = b0; b < b1; ++b) {
                    unsigned so;
                    asm volatile("s_mov_b32 m0, %7\n\ts_add_u32 %0, %6, %8\n\tbuffer_load_dwordx4 %1, %5, %0 offen lds\n\tbuffer_load_dwordx4 %2, %5, %0 offen offset:1024 lds\n\t"
                                 "buffer_load_dwordx4 %3, %5, %0 offen offset:2048 lds\n\tbuffer_load_dwordx4 %4, %5, %0 offen offset:3072 lds\n\ts_waitcnt vmcnt(%9)"
                                 : "=&s"(so) : "v"(vos[0]), "v"(vos[1]), "v"(vos[2]), "v"(vos[3]), "s"(rsO), "s"(sb[b >> 4]), "i"(b * 4096), "i"((b & 15) * 65536), "i"(K::WO_D)
                                 : "memory", "scc");
                }
            };
            dq(1);
            dq(2);
            wait4(lds0 + C_P, P_E15);
            dq(3);
            dq(4);
            wait_vm<0>();
            post(lds0 + C_Q, 1);
            wait4(lds0 + C_P, P_KVD);
            dq(5);
            wait_vm<0>();
            post(lds0 + C_Q, 2);
            wait4(lds0 + C_P, P_PUB);
            {
                const unsigned s0 = (unsigned)(mtB * 128) * 2048u + (unsigned)hpB * 128u;
#pragma unroll
                for (int i = 0; i < 4; ++i) dma4<0>(rsX, vxs, s0 + i * 65536u, lds0 + L_XE + i * 4096);
            }
            lds_wait(lds0 + C_TM, 1);
            sb[1] = (__builtin_amdgcn_readfirstlane(*(AS3 volatile const unsigned*)(size_t)(lds0 + L_TM + 4)) & 1u) * (16u * 65536u) + (unsigned)hpB * 128u;
            sb[0] = sb[1] ^ (16u * 65536u);
            asm volatile("s_nop 4" : "+s"(sb[0]), "+s"(sb[1]));
            wo_run(26, 32);
            post(lds0 + C_XE, 1);
            wait4(lds0 + C_P, P_Q1 + 2);
            wo_run(16, 24);
            wait4(lds0 + C_P, P_QRD);
            wo_run(24, 26);
            wo_run(0, 6);
            post(lds0 + C_WO, 2);
            wo_run(6, 14);
            post(lds0 + C_WO, 3);
            wo_run(14, 16);
            wait_vm<0>();
            post(lds0 + C_WO, 4);
        } else {
            const unsigned v16 = (unsigned)l * 16u;
            const unsigned tag = *(AS3 const unsigned*)(size_t)(lds0 + L_TAG);
            const rsrc_t rsB = mk_rsrc(bulk_base(ws), WB_BYTES);
            lds_wait(lds0 + C_IDX, 1);
            const u32x2 pl = *(AS3 const u32x2*)(size_t)(lds0 + L_PLC);
            const unsigned okp = __builtin_amdgcn_readfirstlane(pl[0]), osm = __builtin_amdgcn_readfirstlane(pl[1]);
            unsigned t1 = 0;
            const unsigned base = now32() - 4096u;
            u32x4 p0, p1;
            CODE_FETCH(p0, slot * 2);
            CODE_FETCH(p1, slot * 2 + 1);
            const int ch = l & 3;
            unsigned mk01 = ch < 2 ? ~0u : 0u, mk0 = ch == 0 ? ~0u : 0u, mk1 = ch == 1 ? ~0u : 0u, mk2 = ch == 2 ? ~0u : 0u, mkl = (l & 31) < 2 ? ~0u : 0u;
            unsigned vkf = (unsigned)((g >> 1) * 16 + c) * 16u + (unsigned)(g & 1) * 8u, vfm = l < 2 ? (unsigned)l * 8u : l == 32 ? 16u : 0x80000000u;
            unsigned kwa = lds0 + L_KVF + l * 16, ra = lds0 + C_R;
            asm volatile("" : "+v"(mk01), "+v"(mk0), "+v"(mk1), "+v"(mk2), "+v"(mkl), "+v"(vkf), "+v"(vfm), "+v"(kwa), "+v"(ra));
            bf16x8 sel;
#pragma unroll
            for (int j = 0; j < 8; ++j) sel[j] = ((c & 7) == j && (c >> 3) == (g & 1)) ? (short)0x3f80 : (short)0;
            asm volatile("" : "+v"(sel));
            {
                unsigned ta, tb;
                auto latest = [&](unsigned off, bool& all) {
                    const u32x4 pa = ld128c(rsWS, v16, off), pb = ld128c(rsWS, v16 + 1024u, off);
                    const unsigned ma = max(pa[1] == tag ? min(pa[0] - base, 8192u) : 0u, pa[3] == tag ? min(pa[2] - base, 8192u) : 0u);
                    const unsigned mb = max(pb[1] == tag ? min(pb[0] - base, 8192u) : 0u, pb[3] == tag ? min(pb[2] - base, 8192u) : 0u);
                    all = __all(pa[1] == tag && pa[3] == tag && pb[1] == tag && pb[3] == tag);
                    ta = __builtin_amdgcn_readfirstlane(wave_umax(ma));
                    tb = __builtin_amdgcn_readfirstlane(wave_umax(mb));
                    return base + max(ta, tb);
                };
                bool all;
                wait4(lds0 + C_P, has_ffn ? K::TMW : P_PUB);
#pragma unroll 1
                for (int it = 0; it < 4; ++it) {
                    t1 = latest(WS_PT, all) + K::D1;
                    if (all) break;
                    wait4(lds0 + C_P, P_PUB);
                    __builtin_amdgcn_s_sleep(K::PT_S);
                }
                asm volatile("s_waitcnt vmcnt(0)" : "+v"(p0), "+v"(p1) :: "memory");
                post(lds0 + L_TM, t1);
                const unsigned t3 = t1 + (K::D2 + K::D3);
                post(lds0 + L_TM + 4, (t3 & ~1u) | (tb < ta ? 1u : 0u));
                post(lds0 + C_TM, 1);
                asm volatile("s_branch .Lpad_r\n\t.p2align 12\n.Lpad_r:\n\ts_cbranch_execz .Lpad_ret" ::: "memory");
                wait4(lds0 + C_P, P_PUB);
            }
            bool late = false;
#pragma unroll 1
            for (int it = 0; it < TRY_LIMIT; ++it) {
                unsigned bad;
                asm volatile(RED1_ASM : [st] "=&s"(bad)
                             : [rsb] "s"(rsB), [t1] "s"(t1), [s0] "s"(okp ^ ((unsigned)mtA * 16384u)), [sm] "s"(osm + (unsigned)((mtA >> 3) * 16384 + (mtA & 7) * 1024)),
                               [sf] "s"(osm + 32768u + (unsigned)mtA * 512u), [sg] "s"(osm + 49152u + (unsigned)mtA * 64u), [hi] "s"(0xffffffff00000000ull),
                               [v16] "v"(v16), [ra] "v"(ra), [tag] "v"(tag), [mk01] "v"(mk01), [mk2] "v"(mk2), [mkl] "v"(mkl), [sel] "v"(sel), [vkf] "v"(vkf), [vfm] "v"(vfm)
                             : "memory", "vcc", "scc", RED1_CLOB);
                if (__builtin_expect(!bad, 1)) break;
                late = true;
                if (it < K::RE1) continue;
                int i2 = 0;
#pragma unroll 1
                for (; i2 < POLL_LIMIT; ++i2) {
                    __builtin_amdgcn_s_sleep(K::RETRY_S);
                    fence_c();
                    if (__all(ld32c(rsB, (unsigned)c * 64u + 36u, osm + (unsigned)((mtA >> 3) * 16384 + (mtA & 7) * 1024)) == tag)) break;
                }
                if (i2 == POLL_LIMIT) break;
            }
            post(lds0 + C_R, R_L1);
            wait4(lds0 + C_P, P_Q1);
            if (late) __builtin_amdgcn_s_sleep(K::L2_S);
            unsigned pv = R_L2I;
#pragma unroll 1
            for (int it = 0; it < TRY_LIMIT; ++it) {
                unsigned bad;
                asm volatile(RED2_ASM : [st] "=&s"(bad), [pv] "+v"(pv)
                             : [rsb] "s"(rsB), [t2] "s"(t1 + K::D2), [s0] "s"(osm + 32768u), [frc] "s"(K::FORCE_REDO && it == 0 ? 1 : 0),
                               [v16] "v"(v16), [ra] "v"(ra), [kwa] "v"(kwa), [tag] "v"(tag), [mk0] "v"(mk0), [mk1] "v"(mk1)
                             : "memory", "vcc", "scc", RED1_CLOB);
                if (__builtin_expect(!bad, 1)) break;
                if (it < K::RE1) continue;
                int i2 = 0;
#pragma unroll 1
                for (; i2 < POLL_LIMIT; ++i2) {
                    __builtin_amdgcn_s_sleep(K::RETRY_S);
                    fence_c();
                    if (__all(ld32c(rsB, (unsigned)c * 64u + 20u, osm + 49152u) == tag)) break;
                }
                if (i2 == POLL_LIMIT) break;
            }
            post(ra, pv + 1u);
            asm volatile(".Lff_end1:");
        }
        return;
    }

    asm volatile(".Lpad_d:\n\ts_cbranch_execz .Lpad_top" ::: "memory");
    __builtin_amdgcn_sched_barrier(0);
    if (w >= 6) {
        const int a = w - 6;
        const int cb = fb >> 2, kq = fb & 3;
        u32x4 wg_[16], wu_[16], wd_[16];
        unsigned tag;
        int idx;
        u32x4 xs[2];
        {
        LANE_VARS;
        const int srow = l >> 3, pos = l & 7;
        const unsigned nb = has_ffn ? 1024u * 7168u : 0u;
        const rsrc_t rsG = mk_rsrc(wgp, nb), rsU = mk_rsrc(wup, nb), rsD = mk_rsrc(wdp, nb);
        const unsigned vf = (unsigned)(16 * srow) * 7168u + (unsigned)pos * 16u;
        const int k8 = fb & 7, cg = (fb >> 4) * 4 + ((fb >> 3) & 1) * 2 + a, cu = cg ^ 2;
        const unsigned sg = (unsigned)(k8 * 128) * 7168u + (unsigned)cg * 128u, su = sg ^ 256u;
        auto ldg = [&]() {
#pragma unroll
            for (int i = 0; i < 16; ++i) {
                asm volatile("buffer_load_dwordx4 %0, %1, %2, %3 offen nt" : "=v"(wg_[i]) : "v"(vf), "s"(rsG), "s"(sg + i * 7168u) : "memory");
            }
        };
        auto ldu = [&]() {
#pragma unroll
            for (int i = 0; i < 16; ++i) {
                asm volatile("buffer_load_dwordx4 %0, %1, %2, %3 offen nt" : "=v"(wu_[i]) : "v"(vf), "s"(rsU), "s"(su + i * 7168u) : "memory");
            }
        };
        auto ldd = [&](int i0, int i1) {
            const unsigned vd = (unsigned)g * (16u * 2048u) + (unsigned)c * 16u;
            const unsigned sd = (unsigned)(cb * 64) * 2048u + (unsigned)(kq * 512 + a * 256);
#pragma unroll
            for (int i = i0; i < i1; ++i) {
                if ((i & 3) == 0) { wait_vm<K::FD>(); hush(); }
                asm volatile("buffer_load_dwordx4 %0, %1, %2, %3 offen nt" : "=v"(wd_[i]) : "v"(vd), "s"(rsD), "s"(sd + i * 2048u) : "memory");
            }
        };
        const int row0 = mtB * 128 + hpB * 8 + 4 * a;
        if (a == 0) {
            *(AS3 unsigned*)(size_t)(lds0 + L_SM + l * 4) = 0u;
            wait_lgkm0();
        }
        int b = 0;
        unsigned tl = 0, pmin = 255;
        auto tick = [&]() {
            bar();
            if (a == 1) {
                const unsigned t = now32();
                if (b > 2 && b <= 16 - K::ND) pmin = min(pmin, t - tl);
                tl = t;
            }
            ++b;
        };
        auto upto = [&](int e) {
#pragma unroll 1
            while (b <= e) tick();
        };
        auto pads = [&]() {
#pragma unroll 1
            for (int i = a; i < 6; i += 2) {
                unsigned long long ex;
                asm volatile("s_mov_b64 %0, exec\n\ts_mov_b64 exec, 0\n\t"
                             "s_cmp_eq_u32 %1, 0\n\ts_cbranch_scc1 .Lpad_ab1\n\ts_cmp_eq_u32 %1, 1\n\ts_cbranch_scc1 .Lpad_ab2\n\t"
                             "s_cmp_eq_u32 %1, 2\n\ts_cbranch_scc1 .Lpad_c0\n\ts_cmp_eq_u32 %1, 3\n\ts_cbranch_scc1 .Lpad_c1\n\t"
                             "s_cmp_eq_u32 %1, 4\n\ts_cbranch_scc1 .Lpad_r\n"
                             ".Lpad_ret:\n\ts_mov_b64 exec, %0" : "=&s"(ex) : "s"(i) : "scc", "memory");
                if (i < 4 && b <= 9) tick();
            }
        };
        auto nap = [&](int n) {
#pragma unroll 1
            for (int i = 0; i < n; ++i) __builtin_amdgcn_s_sleep(1);
        };
        const int lq4 = ((((slot >> 3) - slot) & 3) + (slot & 4)) * 4;
        const int bg = ((unsigned)K::BGT >> lq4) & 15, bu = ((unsigned)K::BUT >> lq4) & 15;
        unsigned pf3 = 0;
        ldi();
        u32x2 gr;
        unsigned vgr;
        auto router = [&](auto n_) {
            tick();
            asm volatile("s_waitcnt vmcnt(%15)"
                         : "+v"(ep), "+v"(xv[0][0]), "+v"(xv[0][1]), "+v"(xv[1][0]), "+v"(xv[1][1]), "+v"(xv[2][0]), "+v"(xv[2][1]), "+v"(xv[3][0]),
                           "+v"(xv[3][1]), "+v"(rv[0]), "+v"(rv[1]), "+v"(gv[0]), "+v"(gv[1]), "+v"(pf), "+v"(pf0)
                         : "i"(decltype(n_)::value) : "memory");
            tag = __builtin_amdgcn_readfirstlane(ep) * 2654435761u + 0x9e3779b9u;
            float ra[4], ga[4];
#pragma unroll
            for (int r = 0; r < 4; ++r) {
                float racc = 0.f, gacc = 0.f;
#pragma unroll
                for (int i = 0; i < 2; ++i)
#pragma unroll
                    for (int j = 0; j < 4; ++j) {
                        racc = dot2(xv[r][i][j], rv[i][j], racc);
                        gacc = dot2(xv[r][i][j], gv[i][j], gacc);
                    }
                ra[r] = racc;
                ga[r] = gacc;
                asm volatile("" : "+v"(ra[r]), "+v"(ga[r]));
            }
            tick();
            float r2[2], g2[2];
#pragma unroll
            for (int i = 0; i < 2; ++i) {
                auto p = __builtin_amdgcn_permlane32_swap(FU(ra[2 * i]), FU(ra[2 * i + 1]), false, false);
                r2[i] = UF(p[0]) + UF(p[1]);
                auto q = __builtin_amdgcn_permlane32_swap(FU(ga[2 * i]), FU(ga[2 * i + 1]), false, false);
                g2[i] = UF(q[0]) + UF(q[1]);
            }
            auto p = __builtin_amdgcn_permlane16_swap(FU(r2[0]), FU(r2[1]), false, false);
            auto q = __builtin_amdgcn_permlane16_swap(FU(g2[0]), FU(g2[1]), false, false);
            float rs = row16_sum(UF(p[0]) + UF(p[1])), gs = row16_sum(UF(q[0]) + UF(q[1]));
            asm volatile("" : "+v"(rs), "+v"(gs));
            const int row = row0 + (g >> 1) + 2 * (g & 1);
            const unsigned rbits = f2bf(rs);
            const unsigned okey = (rbits & 0x8000u) ? (~rbits & 0xffffu) : (rbits | 0x8000u);
            if (c == 0) *(AS3 unsigned*)(size_t)(lds0 + L_KEY + (4 * a + g) * 4) = (okey << 16) | (unsigned)(0xffff - row);
            if (l == 0 && a == 0) *(AS3 unsigned*)(size_t)(lds0 + L_TAG) = tag;
            gr = u32x2{FU(rbf(1.f / (1.f + __expf(-rbf(gs))))), tag};
            vgr = c == 0 ? WS_G + (unsigned)row * 8u : 0x80000000u;
            asm volatile("" : "+v"(gr), "+v"(vgr));
            wait_lgkm0();
            tick();
            if (a == 0) {
                unsigned k8 = *(AS3 const unsigned*)(size_t)(lds0 + L_KEY + (l & 7) * 4);
                k8 = max(k8, (unsigned)__shfl_xor((int)k8, 1));
                k8 = max(k8, (unsigned)__shfl_xor((int)k8, 2));
                k8 = max(k8, (unsigned)__shfl_xor((int)k8, 4));
                const u32x4 pv = *(AS3 const u32x4*)(size_t)(lds0 + L_PRB);
                st64c(u32x2{k8 ^ (((((pv[1] < pv[0] ? 1u : 0u) ^ slot) & 1u) + (((pv[3] < pv[2] ? 1u : 0u) ^ slot) & 1u)) << 11), tag}, rsWS, l ? 0x80000000u : WS_RG + (unsigned)bid * 8u, 0);
            }
        };
        u32x4 ga, gb;
        const unsigned vr = WS_RG + (unsigned)l * 16u;
        auto ldrg = [&]() {
            asm volatile("buffer_load_dwordx4 %0, %1, %2, 0 offen sc1" : "=v"(ga) : "v"(vr), "s"(rsWS) : "memory");
            asm volatile("buffer_load_dwordx4 %0, %1, %2, 0 offen offset:1024 sc1" : "=v"(gb) : "v"(vr), "s"(rsWS) : "memory");
        };
        router(std::integral_constant<int, 0>{});
        __builtin_amdgcn_s_setprio(2);
        upto(bg);
        nap(K::SLG);
        if (a == 0) {
            touch3(pf3, 1);
        } else {
            const rsrc_t rsT = mk_rsrc(wqkv, 1024u * 6144u);
            asm volatile("s_nop 4\n\tbuffer_load_dword %0, %1, %2, %3 offen" : "=v"(pf3) : "v"((unsigned)(l & 31) * 6144u), "s"(rsT),
                         "s"((unsigned)(((12 + rotA) & 15) * 64 + (slot & 7) * 32) * 6144u + (unsigned)hpA * 128u) : "memory");
        }
        ldg();
        upto(bu);
        if (bu != bg) nap(K::SLG);
        ldu();
        upto(((unsigned)K::BRT >> lq4) & 15);
        ldrg();
        pads();
        upto(9);
        asm volatile("s_waitcnt vmcnt(0)"
                     : "+v"(wg_[0]), "+v"(wg_[1]), "+v"(wg_[2]), "+v"(wg_[3]), "+v"(wg_[4]), "+v"(wg_[5]), "+v"(wg_[6]), "+v"(wg_[7]),
                       "+v"(wg_[8]), "+v"(wg_[9]), "+v"(wg_[10]), "+v"(wg_[11]), "+v"(wg_[12]), "+v"(wg_[13]), "+v"(wg_[14]), "+v"(wg_[15]),
                       "+v"(ga), "+v"(gb), "+v"(pf3)
                     :: "memory");
        asm volatile(""
                     : "+v"(wu_[0]), "+v"(wu_[1]), "+v"(wu_[2]), "+v"(wu_[3]), "+v"(wu_[4]), "+v"(wu_[5]), "+v"(wu_[6]), "+v"(wu_[7]),
                       "+v"(wu_[8]), "+v"(wu_[9]), "+v"(wu_[10]), "+v"(wu_[11]), "+v"(wu_[12]), "+v"(wu_[13]), "+v"(wu_[14]), "+v"(wu_[15])
                     :: "memory");
#pragma unroll 1
        for (int it = 0; it < POLL_LIMIT; ++it) {
            if (__all((ga[1] == tag) && (ga[3] == tag) && (gb[1] == tag) && (gb[3] == tag))) break;
            __builtin_amdgcn_s_sleep(4 * K::RETRY_S);
            fence_c();
            ga = ld128c(rsWS, vr, 0);
            gb = ld128c(rsWS, vr + 1024u, 0);
        }
        const unsigned key = __builtin_amdgcn_readfirstlane(wave_umax(max(max(ga[0] | 0x3800u, ga[2] | 0x3800u), max(gb[0] | 0x3800u, gb[2] | 0x3800u))));
        idx = (0xffff - (int)(key & 0xffffu)) & 2047;
        unsigned tw[2];
        auto twf = [&]() {
            const rsrc_t rsO = mk_rsrc(wo, 1024u * 2048u);
            const unsigned vt = (unsigned)srow * 2048u + (unsigned)pos * 128u, st = (unsigned)(slot * 32 + a * 16) * 2048u + (unsigned)(xcd >> 2) * 1024u;
            nap(K::X1);
            if (K::TNT) {
            asm volatile("s_nop 4\n\tbuffer_load_dword %0, %1, %2, %3 offen nt" : "=v"(tw[0]) : "v"(vt), "s"(rsO), "s"(st) : "memory");
            asm volatile("buffer_load_dword %0, %1, %2, %3 offen nt" : "=v"(tw[1]) : "v"(vt), "s"(rsO), "s"(st + 16384u) : "memory");
            } else {
            asm volatile("s_nop 4\n\tbuffer_load_dword %0, %1, %2, %3 offen" : "=v"(tw[0]) : "v"(vt), "s"(rsO), "s"(st) : "memory");
            asm volatile("buffer_load_dword %0, %1, %2, %3 offen" : "=v"(tw[1]) : "v"(vt), "s"(rsO), "s"(st + 16384u) : "memory");
            }
        };
        tick();
        if (K::TWB == 10) twf();
        {
            const unsigned vs = (unsigned)srow * 32u, ss = (unsigned)idx * 2048u + (unsigned)(k8 * 256);
            asm volatile("s_nop 4\n\tbuffer_load_dwordx4 %0, %1, %2, %3 offen" : "=v"(xs[0]) : "v"(vs), "s"(rsX), "s"(ss) : "memory");
            asm volatile("buffer_load_dwordx4 %0, %1, %2, %3 offen offset:16" : "=v"(xs[1]) : "v"(vs), "s"(rsX), "s"(ss) : "memory");
        }
        if (a == 1) {
            const u32x2 pt = {tl + (pmin * (K::PRED_K + 12) >> 2) + K::PRED_C + K::XE, tag};
            st64c(pt, rsWS, l ? 0x80000000u : WS_PT + (unsigned)(hpA * 16 + mtA) * 8u, 0);
        } else {
            const unsigned kb = key >> 16;
            const unsigned rb_ = (kb & 0x8000u) ? (kb & 0x7fffu) : (~kb & 0xffffu);
            const float rsel = rbf(1.f / (1.f + __expf(-UF(rb_ << 16))));
            const u32x2 iv = {(unsigned)idx, FU(rsel)};
            *(AS3 u32x2*)(size_t)(lds0 + L_IDX) = iv;
            const unsigned nv = (~ga[0] & 0x3800u) + (~ga[2] & 0x3800u) + (~gb[0] & 0x3800u) + (~gb[2] & 0x3800u);
            const unsigned votes = wave_usum((l & 3) == (xcd >> 1) ? nv >> 11 : 0u);
            const unsigned o0 = (unsigned)(xcd >> 1) * (2u << 20) + (K::X2 < 2 ? K::X2 * 8192u : votes > 64u ? 8192u : 0u);
            const u32x4 pl = {o0 + WB_KVP + (((unsigned)(hpA & 3) * 8u) ^ (tag & 31u)) * 32768u, o0 + WB_SM + (((unsigned)hpA & 3u) ^ ((tag >> 5) & 7u)) * 65536u,
                              ((tag >> 8) & 63u) * 2048u, 0u};
            *(AS3 u32x4*)(size_t)(lds0 + L_PLC) = pl;
            post(lds0 + C_IDX, 1);
            wait_lgkm0();
        }
        unsigned mk[4];
#pragma unroll
        for (int k = 0; k < 4; ++k) mk[k] = (pos >> 1) == k ? ((pos & 1) ? 0xffff0000u : 0x0000ffffu) : 0u;
        f32x4 sg4 = {0.f, 0.f, 0.f, 0.f}, su4 = sg4;
        auto dot = [&](int i0, int i1) {
#pragma unroll
            for (int i = i0; i < i1; ++i) {
                const unsigned xw = xs[i >> 3][(i >> 1) & 3];
                const unsigned xd = __builtin_amdgcn_perm(xw, xw, (i & 1) ? 0x03020302u : 0x01000100u);
                const u32x4 am = {xd & mk[0], xd & mk[1], xd & mk[2], xd & mk[3]};
                sg4 = mfma16(__builtin_bit_cast(bf16x8, am), __builtin_bit_cast(bf16x8, wg_[i]), sg4);
                su4 = mfma16(__builtin_bit_cast(bf16x8, am), __builtin_bit_cast(bf16x8, wu_[i]), su4);
            }
            asm volatile("" : "+v"(sg4), "+v"(su4));
        };
        tick();
        asm volatile("s_waitcnt vmcnt(0)" : "+v"(xs[0]), "+v"(xs[1]) :: "memory");
        if (K::TWB == 11) twf();
        dot(0, K::DS1);
        tick();
        dot(K::DS1, K::DS2);
        if (K::TWB == 12) twf();
        tick();
        if (a == 1) {
            post(lds0 + L_TM + 8, tl + (pmin * (K::PRED_K + K::PK13) >> 2) + K::PRED_C);
            wait_lgkm0();
        }
        dot(K::DS2, K::DS3);
        if (K::TWB == 13) twf();
        __builtin_amdgcn_s_setprio(3);
        tick();
        ldd(0, 8);
        dot(K::DS3, 16);
        {
            unsigned fo[4];
#pragma unroll
            for (int r = 0; r < 4; ++r) {
                auto p = __builtin_amdgcn_permlane32_swap(FU(sg4[r]), FU(su4[r]), false, false);
                fo[r] = FU(UF(p[0]) + UF(__builtin_amdgcn_update_dpp(0, (int)p[1], 0x128, 0xf, 0xf, false)));
            }
            const unsigned vf0 = ((l & 8) || !has_ffn) ? 0x80000000u : WS_FG + (unsigned)(((l >> 5) ? cu : cg) * 8 + k8) * 1024u + (unsigned)((l >> 5) * 512 + (pos * 8 + (g & 1) * 4) * 8);
            const u32x4 s0 = {fo[0], tag, fo[1], tag}, s1 = {fo[2], tag, fo[3], tag};
            st128c(s0, rsWS, vf0, 0);
            st128c(s1, rsWS, vf0 + 16u, 0);
            st64c(gr, rsWS, vgr, 0);
        }
        if (bid == 0 && a == 0 && l == 0) st32c(tag, rsWS, WS_EPOCH, 0);
        ldd(8, 16);
        lds_wait(lds0 + C_TM, 1);
        const unsigned tm0 = *(AS3 const unsigned*)(size_t)(lds0 + L_TM);
        const unsigned vq = (unsigned)((l & 8) * 64 + (g * 8 + (l & 7)) * 16);
        u32x4 fq[8];
        if (has_ffn) {
            wait_until(__builtin_amdgcn_readfirstlane(tm0) + K::FG_AT);
#pragma unroll 1
            for (int it = 0; it < POLL_LIMIT; ++it) {
                unsigned bad = 0;
#pragma unroll
                for (int i = 0; i < 8; ++i) fq[i] = ld128c(rsWS, vq + (i & 3) * 1024u, WS_FG + (unsigned)cb * 8192u + (i >> 2) * 4096u);
#pragma unroll
                for (int i = 0; i < 8; ++i) bad |= (fq[i][1] ^ tag) | (fq[i][3] ^ tag);
                if (__all(bad == 0u)) break;
                __builtin_amdgcn_s_sleep(2 * K::RETRY_S);
                fence_c();
            }
        }
        asm volatile("s_waitcnt vmcnt(0)"
                     : "+v"(wd_[0]), "+v"(wd_[1]), "+v"(wd_[2]), "+v"(wd_[3]), "+v"(wd_[4]), "+v"(wd_[5]), "+v"(wd_[6]), "+v"(wd_[7]),
                       "+v"(wd_[8]), "+v"(wd_[9]), "+v"(wd_[10]), "+v"(wd_[11]), "+v"(wd_[12]), "+v"(wd_[13]), "+v"(wd_[14]), "+v"(wd_[15]),
                       "+v"(tw[0]), "+v"(tw[1])
                     :: "memory");
        if (has_ffn) {
            if (K::DWP) lds_wait(lds0 + C_P + (2 + a) * 4, P_PUB);
            __builtin_amdgcn_s_setprio(K::HP);
            unsigned dp[8][8];
#pragma unroll
            for (int m = 0; m < 8; ++m)
#pragma unroll
                for (int j = 0; j < 4; ++j) {
                    dp[m][2 * j] = __builtin_amdgcn_perm(wd_[2 * m + 1][j], wd_[2 * m][j], 0x05040100u);
                    dp[m][2 * j + 1] = __builtin_amdgcn_perm(wd_[2 * m + 1][j], wd_[2 * m][j], 0x07060302u);
                }
            float hv[2];
#pragma unroll
            for (int j = 0; j < 2; ++j) {
                const float gs = rbf(((UF(fq[0][2 * j]) + UF(fq[1][2 * j])) + (UF(fq[2][2 * j]) + UF(fq[3][2 * j]))) +
                                     ((UF(fq[4][2 * j]) + UF(fq[5][2 * j])) + (UF(fq[6][2 * j]) + UF(fq[7][2 * j]))));
                const float us = UF(__builtin_amdgcn_update_dpp(0, (int)FU(gs), 0x128, 0xf, 0xf, false));
                hv[j] = rbf(gs / (1.f + __expf(-gs))) * us;
            }
            const unsigned hp = pack2(hv[0], hv[1]);
            float ya[8];
#pragma unroll
            for (int j = 0; j < 8; ++j) ya[j] = 0.f;
            auto dn = [&](auto mc) {
                constexpr int m = decltype(mc)::value;
                const unsigned hb = (unsigned)__builtin_amdgcn_update_dpp(0, (int)hp, 0x150 + m, 0xf, 0xf, false);
#pragma unroll
                for (int j = 0; j < 8; ++j) ya[j] = dot2(dp[m][j], hb, ya[j]);
            };
            dn(std::integral_constant<int, 0>{}); dn(std::integral_constant<int, 1>{}); dn(std::integral_constant<int, 2>{}); dn(std::integral_constant<int, 3>{});
            dn(std::integral_constant<int, 4>{}); dn(std::integral_constant<int, 5>{}); dn(std::integral_constant<int, 6>{}); dn(std::integral_constant<int, 7>{});
            float y4[4], y2[2];
#pragma unroll
            for (int j = 0; j < 4; ++j) {
                auto p = __builtin_amdgcn_permlane32_swap(FU(ya[j]), FU(ya[4 + j]), false, false);
                y4[j] = UF(p[0]) + UF(p[1]);
            }
#pragma unroll
            for (int j = 0; j < 2; ++j) {
                auto p = __builtin_amdgcn_permlane16_swap(FU(y4[j]), FU(y4[2 + j]), false, false);
                y2[j] = UF(p[0]) + UF(p[1]);
            }
            const u32x4 st = {FU(y2[0]), tag, FU(y2[1]), tag};
            st128c(st, rsWS, (unsigned)(c * 64 + g * 16), WS_Y + (unsigned)cb * 8192u + (unsigned)(kq * 2048 + a * 1024));
        }
        {
            const unsigned col8 = (unsigned)(xcd * 128 + slot * 4 + a * 2) * 8u;
            const unsigned vy = l < 56 ? (unsigned)l * 8192u : 0x80000000u;
            u32x4 t;
            __builtin_amdgcn_s_setprio(3);
            wait_until(__builtin_amdgcn_readfirstlane(tm0) + K::Y_AT);
#pragma unroll 1
            for (int it = 0; it < POLL_LIMIT; ++it) {
                t = ld128c(rsWS, vy, WS_Y + col8);
                if (__all(l >= 56 || (t[1] == tag && t[3] == tag))) break;
                __builtin_amdgcn_s_sleep(4 * K::RETRY_S);
                fence_c();
            }
            auto p = __builtin_amdgcn_permlane32_swap(t[0], t[2], false, false);
            const float s = row16_sum(x16_sum(UF(p[0]) + UF(p[1])));
            st64c(u32x2{FU(s), tag}, rsWS, (l & 31) ? 0x80000000u : (unsigned)(l >> 2), WS_YF + col8);
        }
        if ((idx >> 7) == mtB && a == 0) {
            u32x4 t;
            wait_until(__builtin_amdgcn_readfirstlane(tm0) + K::Y_AT + K::X0);
#pragma unroll 1
            for (int it = 0; it < POLL_LIMIT; ++it) {
                t = ld128c(rsWS, (unsigned)(l & 31) * 16u, WS_YF + (unsigned)hpB * 512u);
                if (__all(t[1] == tag && t[3] == tag)) break;
                __builtin_amdgcn_s_sleep(2 * K::RETRY_S);
                fence_c();
            }
            lds_wait(lds0 + C_XE, 1);
            const int rr = idx & 127;
            const unsigned xx = *(AS3 const unsigned*)(size_t)(lds0 + L_XE + rr * 128 + ((((l & 31) >> 2) ^ ((rr >> 1) & 7)) << 4) + (l & 3) * 4);
            const float rsel = *(AS3 const float*)(size_t)(lds0 + L_IDX + 4);
            *(AS3 f32x2*)(size_t)(lds0 + L_Y + 256 + (l & 31) * 8) = f32x2{rbf(lo16(xx) + rbf(rbf(UF(t[0])) * rsel)), rbf(hi16(xx) + rbf(rbf(UF(t[2])) * rsel))};
            post(lds0 + C_YRDY, 1);
        }
        }
        asm volatile(".Lff_end0:");
        return;
    }

    const int v = w & 1, hd = w >> 1;
    const rsrc_t rsB = mk_rsrc(bulk_base(ws), WB_BYTES);
    unsigned tag, sof, t3, rid;
    unsigned long long rm;
    u32x16 ad;
    u32x4 g4;
    {
        const unsigned pi = (unsigned)((slot * 2 + (xcd & 1)) * 2 + hd);
        const char* pa = bulk_base(ws) + ((unsigned)(xcd >> 1) * (2u << 20) + WB_PRB + (pi >> 5) * 16384u + (pi & 31u) * 256u + ((v ^ slot) & 1) * 8192u);
        unsigned j0;
        asm volatile("s_load_dword %0, %1, 64" : "={s80}"(j0) : "s"(pa) : "memory");
        LANE_VARS;
        ad[0] = lds0 + (unsigned)(64 * v + c) * 128u + (unsigned)((g ^ ((c >> 1) & 7)) << 4);
        ad[1] = lds0 + 16384u + (WSW ^ (unsigned)(hd << 6));
        ad[2] = lds0 + L_VF + (unsigned)(hd * 2048 + v * 8192 + l * 16);
        ad[3] = lds0 + L_PHI + hd * 16384 + (unsigned)(64 * v + c) * 128u + (unsigned)((((c & 3) ^ ((c >> 2) & 1)) << 5) + ((g ^ (c >> 2)) << 3));
        ad[4] = lds0 + L_PHI + hd * 16384 + v * 8192 + ((PSW + (unsigned)((i3 ^ g) << 3)) ^ (unsigned)(v << 6));
        ad[5] = lds0 + L_WP + hd * 2048 + WPO;
        ad[6] = (unsigned)(l >> 5) * 16384u + (unsigned)(l & 31) * 16u;
        ad[7] = c < 3 ? (unsigned)(((g & 1) * 2 + (g >> 1)) * 1024 + c * 16) : 0x80000000u;
        ad[8] = lds0 + C_P + w * 4;
        ad[9] = lds0 + L_SM;
        ad[10] = c;
#pragma unroll
        for (int i = 11; i < 16; ++i) ad[i] = 0u;
        asm volatile(PHASE_AB_ASM
                     : "+{v[240:255]}"(ad), "={v[232:235]}"(g4), "={v236}"(tag), [sof] "=&s"(sof), [t3] "=&s"(t3), [rid] "=&s"(rid), [rm] "=&s"(rm), "+{s80}"(j0)
                     : [rsb] "s"(rsB), [rsw] "s"(rsWS), [xs0] "s"(v ? XS1[0] - 8192u : XS0[0]), [xs1] "s"(v ? XS1[1] - 8192u : XS0[1]), [kpx] "s"(w * 65536),
                       [kpa] "s"(mtA * 512), [kma] "s"(hd * 16384 + v * 4096 + mtA * 64), [sg] "s"(WS_G + (unsigned)(mtB * 128 + 32 * w) * 8u),
                       [ofc] "s"(WB_OF + (unsigned)(mtB * 4 + w) * 65536u), [ofp] "s"(WB_OF + (unsigned)((mtA * 4 + 2 * v) * 32 + ((2 * hpA + hd + 1) & 31)) * 2048u),
                       [ogp] "s"(WB_OG + (unsigned)((mtA * 4 + 2 * v) * 32 + 2 * hpA + hd) * 8u), [kac] "s"(L_KVF + hd * 4096), [zac] "s"(L_ZF + hd * 512),
                       [xec] "s"(L_XE + w * 4096), [own] "s"(mtB * 4 + w), [prb] "s"(pa)
                     : "memory", "vcc", "scc", PHASE_AB_CLOB);
    }
    {
        const rsrc_t rsOut = mk_rsrc(out, 2048u * 2048u);
        const unsigned sog = WB_OG + sof + (unsigned)(mtB * 4 + w) * 256u;
        unsigned grp = (t3 & 1u) * 2u, flip = 0;
#pragma unroll 1
        for (int att = 0; att < TRY_LIMIT; ++att) {
            unsigned cs, st;
            asm volatile(PHASE_C_ASM
                         : "={v230}"(cs), [st] "=&s"(st)
                         : "{v[240:255]}"(ad), "{v[232:235]}"(g4), "{v236}"(tag), [rsb] "s"(rsB), [rso] "s"(rsOut), [sog] "s"(sog), [grp] "s"(grp), [flip] "s"(flip),
                           [t3] "s"(t3), [rid] "s"(rid), [rm] "s"(rm), [sout] "s"((unsigned)(mtB * 128 + 32 * w) * 2048u + (unsigned)hpB * 128u)
                         : "memory", "vcc", "scc", PHASE_C_CLOB);
            if (K::FORCE_REDO && att < 2) st = 2 - att, cs += 1u;
            if (__builtin_expect(st == 0u, 1)) break;
            u32x2 og = ld64c(rsB, ad[4], sog);
            const unsigned miss = (unsigned)__builtin_amdgcn_ballot_w64(og[1] != tag);
            if (st == 1u && ((miss && !(miss & ~(0xffffu << (8u * grp)))) || K::FORCE_REDO)) {
                grp ^= 2u;
                flip ^= 65536u;
                lds_wait(lds0 + C_WO, 4);
                continue;
            }
            int it = 0;
#pragma unroll 1
            for (; it < POLL_LIMIT && !__all(og[1] == tag); ++it) {
                __builtin_amdgcn_s_sleep(K::RETRY_S);
                fence_c();
                og = ld64c(rsB, ad[4], sog);
            }
            if (it == POLL_LIMIT) break;
            if (st == 2u) {
                if (__all(wave_usum(cs - (ad[10] < 32u ? og[0] : 0u)) == 0u)) break;
            }
        }
    }
    asm volatile(".Lff_end2:\n.Lpad_x:\n\ts_cbranch_execz .Lpad_top");
}

extern "C" void kernel_launch(void* const* d_in, const int* in_sizes, int n_in, void* d_out, int out_size,
                              void* d_ws, size_t ws_size, hipStream_t stream) {
    const u16* x = (const u16*)d_in[0];
    const u16* wqkv = (const u16*)d_in[1];
    const u16* wphi = (const u16*)d_in[2];
    const u16* wo = (const u16*)d_in[3];
    const u16* wr = (const u16*)d_in[5];
    const u16* wg = (const u16*)d_in[6];
    const u16* wu = (const u16*)d_in[7];
    const u16* wd = (const u16*)d_in[8];
    const u16* gw = (const u16*)d_in[9];
    fused_fwd<<<256, 512, 0, stream>>>(x, wqkv, wphi, wo, wr, wg, wu, wd, gw, (char*)d_ws, (u16*)d_out);
}
```

```cpp
#include <hip/hip_runtime.h>
#include <stdint.h>
#include <type_traits>

typedef __attribute__((ext_vector_type(8))) short bf16x8;
typedef __attribute__((ext_vector_type(4))) short s16x4;
typedef __attribute__((ext_vector_type(4))) float f32x4;
typedef __attribute__((ext_vector_type(2))) float f32x2;
typedef __attribute__((ext_vector_type(32))) float f32x32;
typedef __attribute__((ext_vector_type(2))) unsigned u32x2;
typedef __attribute__((ext_vector_type(4))) unsigned u32x4;
typedef __attribute__((ext_vector_type(8))) unsigned u32x8;
typedef __attribute__((ext_vector_type(16))) unsigned u32x16;
typedef __attribute__((ext_vector_type(32))) unsigned u32x32;
typedef __attribute__((ext_vector_type(2))) __bf16 bf16x2_t;
typedef unsigned short u16;
typedef __amdgpu_buffer_rsrc_t rsrc_t;

#define DI __device__ __forceinline__
#define AS3 __attribute__((address_space(3)))
#define RLX_WG __ATOMIC_RELAXED, __HIP_MEMORY_SCOPE_WORKGROUP

constexpr int AUX_SC1 = 16;

DI float bf2f(u16 v) { return __uint_as_float(((unsigned)v) << 16); }
DI u16 f2bf(float f) { __bf16 b = (__bf16)f; return __builtin_bit_cast(u16, b); }
DI float rbf(float f) { return bf2f(f2bf(f)); }
DI unsigned pack2(float lo, float hi) {
    f32x2 v = {lo, hi};
    bf16x2_t b = __builtin_convertvector(v, bf16x2_t);
    return __builtin_bit_cast(unsigned, b);
}
DI float lo16(unsigned u) { return __uint_as_float(u << 16); }
DI float hi16(unsigned u) { return __uint_as_float(u & 0xffff0000u); }
DI float dot2(unsigned a, unsigned b, float c) {
    return __builtin_amdgcn_fdot2_f32_bf16(__builtin_bit_cast(bf16x2_t, a), __builtin_bit_cast(bf16x2_t, b), c, false);
}
DI s16x4 tr_read(unsigned a) { return __builtin_amdgcn_ds_read_tr16_b64_v4i16((AS3 s16x4*)(size_t)a); }
DI bf16x8 cat8(s16x4 a, s16x4 b) { return __builtin_shufflevector(a, b, 0, 1, 2, 3, 4, 5, 6, 7); }
DI bf16x8 lds_rd128(unsigned a) { return *(AS3 const bf16x8*)(size_t)a; }
DI void lds_wr128(unsigned a, bf16x8 v) { *(AS3 bf16x8*)(size_t)a = v; }
DI f32x4 mfma16(bf16x8 a, bf16x8 b, f32x4 c) { return __builtin_amdgcn_mfma_f32_16x16x32_bf16(a, b, c, 0, 0, 0); }
DI bf16x8 pack44(f32x4 a, f32x4 b) {
    u32x4 p = {pack2(a[0], a[1]), pack2(a[2], a[3]), pack2(b[0], b[1]), pack2(b[2], b[3])};
    return __builtin_bit_cast(bf16x8, p);
}
#define FU(x) __float_as_uint(x)
#define UF(x) __uint_as_float(x)
DI float x32_sum(float v) { auto r = __builtin_amdgcn_permlane32_swap(FU(v), FU(v), false, false); return UF(r[0]) + UF(r[1]); }
DI float x16_sum(float v) { auto r = __builtin_amdgcn_permlane16_swap(FU(v), FU(v), false, false); return UF(r[0]) + UF(r[1]); }
DI float xg_sum(float v) { return x16_sum(x32_sum(v)); }
template <int CTRL> DI float dpp_add(float v) { return v + UF(__builtin_amdgcn_update_dpp(0, (int)FU(v), CTRL, 0xf, 0xf, false)); }
DI float row16_sum(float v) { return dpp_add<0x121>(dpp_add<0x122>(dpp_add<0x124>(dpp_add<0x128>(v)))); }
template <int CTRL> DI unsigned dpp_umax(unsigned v) { return max(v, (unsigned)__builtin_amdgcn_update_dpp(0, (int)v, CTRL, 0xf, 0xf, false)); }
DI unsigned wave_umax(unsigned v) {
    auto r = __builtin_amdgcn_permlane32_swap(v, v, false, false);
    v = max(r[0], r[1]);
    auto q = __builtin_amdgcn_permlane16_swap(v, v, false, false);
    v = max(q[0], q[1]);
    return dpp_umax<0x121>(dpp_umax<0x122>(dpp_umax<0x124>(dpp_umax<0x128>(v))));
}
DI unsigned add4(u32x4 v, unsigned s) { return (v[0] + v[1] + v[2]) + (v[3] + s); }
template <int CTRL> DI unsigned dpp_uadd(unsigned v) { return v + (unsigned)__builtin_amdgcn_update_dpp(0, (int)v, CTRL, 0xf, 0xf, false); }
DI unsigned row16_usum(unsigned v) { return dpp_uadd<0x121>(dpp_uadd<0x122>(dpp_uadd<0x124>(dpp_uadd<0x128>(v)))); }
DI rsrc_t mk_rsrc(const void* p, unsigned bytes) { return __builtin_amdgcn_make_buffer_rsrc((void*)p, (short)0, (int)bytes, 0x00020000); }
DI u32x4 ld128c(rsrc_t rs, unsigned vo, unsigned so) { return __builtin_amdgcn_raw_buffer_load_b128(rs, vo, so, AUX_SC1); }
DI u32x2 ld64c(rsrc_t rs, unsigned vo, unsigned so) { return __builtin_amdgcn_raw_buffer_load_b64(rs, vo, so, AUX_SC1); }
DI unsigned ld32c(rsrc_t rs, unsigned vo, unsigned so) { return __builtin_amdgcn_raw_buffer_load_b32(rs, vo, so, AUX_SC1); }
DI void st128c(u32x4 v, rsrc_t rs, unsigned vo, unsigned so) {
    __builtin_amdgcn_raw_buffer_store_b128(v, rs, vo, so, AUX_SC1);
    asm volatile("s_nop 1" :: "v"(v) : "memory");
}
DI void st64c(u32x2 v, rsrc_t rs, unsigned vo, unsigned so) { __builtin_amdgcn_raw_buffer_store_b64(v, rs, vo, so, AUX_SC1); }
DI void st32c(unsigned v, rsrc_t rs, unsigned vo, unsigned so) { __builtin_amdgcn_raw_buffer_store_b32(v, rs, vo, so, AUX_SC1); }

DI void dma16(rsrc_t rs, unsigned voff, unsigned soff, unsigned lds_addr) {
    asm volatile("s_mov_b32 m0, %0\n\ts_nop 0\n\tbuffer_load_dwordx4 %1, %2, %3 offen lds"
                 :: "s"(lds_addr), "v"(voff), "s"(rs), "s"(soff) : "memory");
}
template <int PC> DI void dma4(rsrc_t rs, const unsigned* v, unsigned soff, unsigned lds_addr) {
#define D4(p) asm volatile("s_mov_b32 m0, %0\n\ts_nop 0\n\tbuffer_load_dwordx4 %1, %5, %6 offen lds\n\t" p "buffer_load_dwordx4 %2, %5, %6 offen offset:1024 lds\n\t" p \
                 "buffer_load_dwordx4 %3, %5, %6 offen offset:2048 lds\n\t" p "buffer_load_dwordx4 %4, %5, %6 offen offset:3072 lds" \
                 :: "s"(lds_addr), "v"(v[0]), "v"(v[1]), "v"(v[2]), "v"(v[3]), "s"(rs), "s"(soff), "i"(PC ? PC - 1 : 0) : "memory")
    if (PC) D4("s_nop %7\n\t"); else D4("");
}
template <int N> DI void wait_vm() { asm volatile("s_waitcnt vmcnt(%0)" :: "i"(N) : "memory"); }
DI void wait_lgkm0() { asm volatile("" ::: "memory"); __builtin_amdgcn_s_waitcnt(0xC07F); asm volatile("" ::: "memory"); }
DI void bar() { asm volatile("" ::: "memory"); __builtin_amdgcn_s_barrier(); asm volatile("" ::: "memory"); }
DI void fence_c() { asm volatile("" ::: "memory"); }
DI int fresh_lane() {
    int v;
    asm volatile("v_mbcnt_lo_u32_b32 %0, -1, 0\n\tv_mbcnt_hi_u32_b32 %0, -1, %0" : "=v"(v));
    return v;
}
#define LANE_VARS const int l = fresh_lane(), c = l & 15, g = l >> 4, i4 = (l & 15) >> 2, i3 = l & 3; (void)c; (void)g; (void)i4; (void)i3
#define PSW ((unsigned)(4 * g + i4) * 128u + (unsigned)((i4 ^ (g & 1)) << 5))
#define WPO ((unsigned)(4 * g + i4) * 64u + (unsigned)((g & 1) << 5) + (unsigned)(8 * i3))
#define WSW ((unsigned)(8 * g + i4) * 128u + (unsigned)((i4 ^ (g & 1)) << 5) + (unsigned)(8 * i3))

#define WO_VOFF ((unsigned)(l >> 3) * 2048u + (unsigned)(((((((l & 7) >> 1) ^ ((l >> 3) & 3) ^ (l >> 5)) << 1) | (l & 1))) << 4))

DI void lds_wait(unsigned addr, unsigned target) {
#pragma unroll 1
    for (int it = 0; it < (1 << 22); ++it) {
        unsigned v = __hip_atomic_load((AS3 unsigned*)(size_t)addr, RLX_WG);
        if (__builtin_amdgcn_readfirstlane(v) >= target) break;
        __builtin_amdgcn_s_sleep(1);
    }
    fence_c();
}
DI unsigned now32() { unsigned long long t; asm volatile("s_memrealtime %0\n\ts_waitcnt lgkmcnt(0)" : "=s"(t) :: "memory"); return (unsigned)t; }
DI void wait_until(unsigned t) {
#pragma unroll 1
    for (int it = 0; it < 256; ++it) {
        const int rem = (int)(t - now32());
        if (rem <= 4) break;
        if (rem > 12) __builtin_amdgcn_s_sleep(2);
    }
    fence_c();
}
DI unsigned wave_usum(unsigned m) {
    auto p = __builtin_amdgcn_permlane32_swap(m, m, false, false);
    m = p[0] + p[1];
    auto q = __builtin_amdgcn_permlane16_swap(m, m, false, false);
    return row16_usum(q[0] + q[1]);
}
DI void post(unsigned addr, unsigned v) {
    fence_c();
    *(AS3 volatile unsigned*)(size_t)addr = v;
    fence_c();
}
DI void wait4(unsigned addr, unsigned target) {
#pragma unroll 1
    for (int it = 0; it < (1 << 22); ++it) {
        const u32x4 v = *(AS3 volatile const u32x4*)(size_t)addr;
        if (__builtin_amdgcn_readfirstlane(min(min(v[0], v[1]), min(v[2], v[3]))) >= target) break;
        __builtin_amdgcn_s_sleep(1);
    }
    fence_c();
}
constexpr int POLL_LIMIT = 1 << 16, TRY_LIMIT = 1 << 12;

#define KNOB(n, v) static constexpr int n = v
#define KNOBS
struct Dflt {
    KNOB(KT0, 0x76534210); KNOB(KT1, 0x76453201); KNOB(KT2, 0x75643120); KNOB(KT3, 0x67542310);
    KNOB(BGT, 0x76227622); KNOB(BUT, 0x77327732); KNOB(BRT, 0x77777777); KNOB(BST, 0x33333333); KNOB(PBT, (int)0xaaaaaaaa);
    KNOB(SLG, 0); KNOB(SLS, 20); KNOB(X0, 130); KNOB(X1, 2); KNOB(X2, 2); KNOB(TOPV, 1);
    KNOB(PRED_K, 16); KNOB(PRED_C, 90);
    KNOB(D1, 40); KNOB(D2, 120); KNOB(D3, 140); KNOB(FG_AT, -130); KNOB(Y_AT, 260); KNOB(QA, 30); KNOB(QB, 40);
    KNOB(RETRY_S, 8); KNOB(PT_S, 12); KNOB(KF_S, 10); KNOB(WO_D, 24); KNOB(FD, 12);
    KNOB(PT_B, 10); KNOB(XE, 20);
    KNOB(RE1, 1); KNOB(L2_S, 12);
    KNOB(FORCE_REDO, 0);
    KNOB(ND, 6); KNOB(XAH, 0); KNOB(QG, 2); KNOB(HP, 2); KNOB(Q5E, 1); KNOB(PADS, 1); KNOB(WOSK, 0); KNOB(WOE, 0); KNOB(TNT, 0); KNOB(TWB, 13); KNOB(TMW, 0); KNOB(DWP, 1); KNOB(WOG, 0); KNOB(PK13, 3); KNOB(DS1, 2); KNOB(DS2, 9); KNOB(DS3, 16); KNOB(PACE, 0);
};
struct K : Dflt { KNOBS };

constexpr unsigned WS_EPOCH = 0;
constexpr unsigned WS_RG = 4096;
constexpr unsigned WS_PT = 6144;
constexpr unsigned WS_G = 8192;
constexpr unsigned WS_YF = 24576;
constexpr unsigned WS_FG = 65536;
constexpr unsigned WS_Y = 1u << 19;
constexpr unsigned WS_BYTES = 1u << 20;
constexpr unsigned WB_KVP = 0;
constexpr unsigned WB_SM = 1u << 20;
constexpr unsigned WB_PRB = 3u << 19;
constexpr unsigned WB_OG = 7u << 18;
constexpr unsigned WB_OF = 8u << 20;
constexpr unsigned WB_BYTES = 13u << 20;
DI char* bulk_base(char* ws) { return (char*)(((size_t)ws + (3u << 20)) & ~(size_t)((2u << 20) - 1)); }

constexpr unsigned SLOT = 40960;
constexpr unsigned L_PHI = 98304;
constexpr unsigned L_VF = 131072;
constexpr unsigned L_MISC = 147456;
constexpr unsigned L_WP = L_MISC, L_KVF = L_MISC + 4096, L_SM = L_MISC + 12288;
constexpr unsigned C_ST = L_SM + 0  , C_R = L_SM + 8  , C_P = L_SM + 16  ;
constexpr unsigned C_WO = L_SM + 56, C_YRDY = L_SM + 68, C_IDX = L_SM + 76, C_TM = L_SM + 80, C_XE = L_SM + 84, C_Y1 = L_SM + 88;
constexpr unsigned P_E14 = 1, P_E15 = 2, P_PHA = 3, P_PHB = 4, P_KVD = 5, P_PUB = 6, P_Q1 = 7  , P_QRD = 11, P_O = 12;
constexpr unsigned C_Q = L_SM + 92, C_HOLD = L_SM + 96;
constexpr unsigned XS0[3] = {16384, 98304, L_VF}, XS1[3] = {57344, 122880, L_VF + 8192};
constexpr unsigned QX[3] = {40960, 81920, 0}, QW[6] = {L_KVF, 65536, 73728, 24576, 32768, L_PHI};
constexpr unsigned R_L1I = 1, R_L1 = 2, R_L2I = 3, R_KVF = 4;
constexpr unsigned L_XE = L_VF;
constexpr unsigned L_KEY = L_SM + 256, L_TAG = L_SM + 288, L_IDX = L_SM + 296, L_TM = L_SM + 304  , L_PLC = L_SM + 320  , L_PRB = L_SM + 336  , L_ZF = L_SM + 512, L_Y = L_SM + 1536;

constexpr unsigned long long L2E2 = 0x3fb8aa3b3fb8aa3bull;

#define PHASE_AB_ASM \
    "s_branch .Lpad_ab0\n\t" \
    ".Lab_s1:\n\t" \
    "s_mov_b32 s89, 0x400000\n\t" \
    ".Lab_s1a:\n\t" \
    "s_waitcnt lgkmcnt(0)\n\t" \
    "ds_read_b32 v237, v249\n\t" \
    "ds_read_b32 v238, v249 offset:4\n\t" \
    "s_waitcnt lgkmcnt(0)\n\t" \
    "v_min_u32 v237, v237, v238\n\t" \
    "s_nop 1\n\t" \
    "v_readfirstlane_b32 s85, v237\n\t" \
    "s_sub_u32 s89, s89, 1\n\t" \
    "s_cbranch_scc1 .Lab_s1b\n\t" \
    "s_cmp_lt_u32 s85, 1\n\t" \
    "s_cbranch_scc1 .Lab_s1a\n\t" \
    ".Lab_s1b:\n\t" \
    "s_nop 7\n\t" \
    "s_waitcnt lgkmcnt(0)\n\t" \
    "s_branch .Lab_s1r\n\t" \
    ".Lab_s2:\n\t" \
    "s_mov_b32 s89, 0x400000\n\t" \
    ".Lab_s2a:\n\t" \
    "s_waitcnt lgkmcnt(0)\n\t" \
    "ds_read_b32 v237, v249 offset:16\n\t" \
    "ds_read_b32 v238, v249 offset:20\n\t" \
    "ds_read_b32 v255, v249 offset:24\n\t" \
    "s_waitcnt lgkmcnt(0)\n\t" \
    "v_min3_u32 v237, v237, v238, v255\n\t" \
    "ds_read_b32 v238, v249 offset:28\n\t" \
    "s_waitcnt lgkmcnt(0)\n\t" \
    "v_min_u32 v237, v237, v238\n\t" \
    "s_nop 1\n\t" \
    "v_readfirstlane_b32 s85, v237\n\t" \
    "s_sub_u32 s89, s89, 1\n\t" \
    "s_cbranch_scc1 .Lab_s2b\n\t" \
    "s_cmp_lt_u32 s85, 2\n\t" \
    "s_cbranch_scc1 .Lab_s2a\n\t" \
    ".Lab_s2b:\n\t" \
    "s_nop 7\n\t" \
    "s_waitcnt lgkmcnt(0)\n\t" \
    "s_branch .Lab_s2r\n\t" \
    ".Lab_s3:\n\t" \
    "s_mov_b32 s89, 0x400000\n\t" \
    ".Lab_s3a:\n\t" \
    "s_waitcnt lgkmcnt(0)\n\t" \
    "ds_read_b32 v237, v250\n\t" \
    "s_waitcnt lgkmcnt(0)\n\t" \
    "s_nop 1\n\t" \
    "v_readfirstlane_b32 s85, v237\n\t" \
    "s_sub_u32 s89, s89, 1\n\t" \
    "s_cbranch_scc1 .Lab_s3b\n\t" \
    "s_cmp_lt_u32 s85, 3\n\t" \
    "s_cbranch_scc1 .Lab_s3a\n\t" \
    ".Lab_s3b:\n\t" \
    "s_nop 7\n\t" \
    "ds_read_b64_tr_b16 v[104:105], v245\n\t" \
    "ds_read_b64_tr_b16 v[106:107], v245 offset:2048\n\t" \
    "ds_read_b64_tr_b16 v[108:109], v255\n\t" \
    "ds_read_b64_tr_b16 v[110:111], v255 offset:2048\n\t" \
    "ds_read_b128 v[80:83], v242\n\t" \
    "ds_read_b128 v[84:87], v242 offset:1024\n\t" \
    "ds_read_b128 v[88:91], v242 offset:4096\n\t" \
    "ds_read_b128 v[92:95], v242 offset:5120\n\t" \
    "s_waitcnt lgkmcnt(0)\n\t" \
    "s_branch .Lab_s3r\n\t" \
    ".Lab_s4:\n\t" \
    "s_mov_b32 s89, 0x400000\n\t" \
    ".Lab_s4a:\n\t" \
    "s_waitcnt lgkmcnt(0)\n\t" \
    "ds_read_b32 v237, v250\n\t" \
    "s_waitcnt lgkmcnt(0)\n\t" \
    "s_nop 1\n\t" \
    "v_readfirstlane_b32 s85, v237\n\t" \
    "s_sub_u32 s89, s89, 1\n\t" \
    "s_cbranch_scc1 .Lab_s4b\n\t" \
    "s_cmp_lt_u32 s85, 4\n\t" \
    "s_cbranch_scc1 .Lab_s4a\n\t" \
    ".Lab_s4b:\n\t" \
    "s_nop 7\n\t" \
    "ds_read_b64_tr_b16 v[8:9], v245 offset:4096\n\t" \
    "ds_read_b64_tr_b16 v[10:11], v245 offset:6144\n\t" \
    "ds_read_b64_tr_b16 v[12:13], v255 offset:4096\n\t" \
    "ds_read_b64_tr_b16 v[14:15], v255 offset:6144\n\t" \
    "v_mov_b32 v237, 5\n\t" \
    "s_waitcnt lgkmcnt(0)\n\t" \
    "s_branch .Lab_s4r\n\t" \
    ".Lab_s5:\n\t" \
    "s_mov_b32 s89, 0x400000\n\t" \
    ".Lab_s5a:\n\t" \
    "s_waitcnt lgkmcnt(0)\n\t" \
    "ds_read_b32 v237, v249 offset:92\n\t" \
    "s_waitcnt lgkmcnt(0)\n\t" \
    "s_nop 1\n\t" \
    "v_readfirstlane_b32 s85, v237\n\t" \
    "s_sub_u32 s89, s89, 1\n\t" \
    "s_cbranch_scc1 .Lab_s5b\n\t" \
    "s_cmp_lt_u32 s85, 1\n\t" \
    "s_cbranch_scc1 .Lab_s5a\n\t" \
    ".Lab_s5b:\n\t" \
    "s_nop 7\n\t" \
    "ds_read_b64_tr_b16 v[80:81], v253\n\t" \
    "ds_read_b64_tr_b16 v[82:83], v253 offset:512\n\t" \
    "ds_read_b64_tr_b16 v[84:85], v251\n\t" \
    "ds_read_b64_tr_b16 v[86:87], v251 offset:512\n\t" \
    "ds_read_b64_tr_b16 v[88:89], v253 offset:4096\n\t" \
    "ds_read_b64_tr_b16 v[90:91], v253 offset:4608\n\t" \
    "ds_read_b64_tr_b16 v[92:93], v251 offset:4096\n\t" \
    "ds_read_b64_tr_b16 v[94:95], v251 offset:4608\n\t" \
    "s_waitcnt lgkmcnt(0)\n\t" \
    "s_branch .Lab_s5r\n\t" \
    ".Lab_s6:\n\t" \
    "s_mov_b32 s89, 0x400000\n\t" \
    ".Lab_s6a:\n\t" \
    "s_waitcnt lgkmcnt(0)\n\t" \
    "ds_read_b32 v237, v249 offset:92\n\t" \
    "s_waitcnt lgkmcnt(0)\n\t" \
    "s_nop 1\n\t" \
    "v_readfirstlane_b32 s85, v237\n\t" \
    "s_sub_u32 s89, s89, 1\n\t" \
    "s_cbranch_scc1 .Lab_s6b\n\t" \
    "s_cmp_lt_u32 s85, 2\n\t" \
    "s_cbranch_scc1 .Lab_s6a\n\t" \
    ".Lab_s6b:\n\t" \
    "s_nop 7\n\t" \
    "v_mov_b32 v237, 10\n\t" \
    "s_waitcnt lgkmcnt(0)\n\t" \
    "s_branch .Lab_s6r\n\t" \
    ".Lab_s7:\n\t" \
    "s_mov_b32 s89, 0x400000\n\t" \
    ".Lab_s7a:\n\t" \
    "s_sleep 1\n\t" \
    "ds_read_b32 v237, v249 offset:8\n\t" \
    "s_waitcnt lgkmcnt(0)\n\t" \
    "v_readfirstlane_b32 s85, v237\n\t" \
    "s_sub_u32 s89, s89, 1\n\t" \
    "s_cbranch_scc1 .Lab_s7r\n\t" \
    "s_cmp_lt_u32 s85, 3\n\t" \
    "s_cbranch_scc1 .Lab_s7a\n\t" \
    "s_sleep 5\n\t" \
    "s_branch .Lab_s7r\n\t" \
    ".Lab_sa:\n\t" \
    "ds_read_b32 v36, v249 offset:8\n\t" \
    "s_waitcnt lgkmcnt(0)\n\t" \
    "v_readfirstlane_b32 s80, v36\n\t" \
    "s_sub_u32 s89, s89, 1\n\t" \
    "s_cbranch_scc1 .Lab_sar\n\t" \
    "s_cmp_lt_u32 s80, s82\n\t" \
    "s_cbranch_scc1 .Lab_sa\n\t" \
    "s_cmp_eq_u32 s80, s82\n\t" \
    "s_cbranch_scc1 .Lab_sar\n\t" \
    "s_waitcnt vmcnt(0)\n\t" \
    "s_branch .Lab_k\n\t" \
    ".Lab_s8:\n\t" \
    "s_mov_b32 s89, 0x10000\n\t" \
    "v_lshlrev_b32 v35, 3, v34\n\t" \
    ".Lab_s8a:\n\t" \
    "s_sleep 8\n\t" \
    "buffer_load_dwordx2 v[232:233], v35, %[rsw], %[sg] offen sc1\n\t" \
    "buffer_load_dwordx2 v[234:235], v35, %[rsw], %[sg] offen offset:128 sc1\n\t" \
    "s_sub_u32 s89, s89, 1\n\t" \
    "s_waitcnt vmcnt(0)\n\t" \
    "s_cbranch_scc1 .Lab_s8r\n\t" \
    "v_cmp_ne_u32 vcc, v236, v233\n\t" \
    "v_cmp_ne_u32_e64 s[92:93], v236, v235\n\t" \
    "s_nop 0\n\t" \
    "s_or_b64 vcc, vcc, s[92:93]\n\t" \
    "s_cbranch_scc1 .Lab_s8a\n\t" \
    "s_branch .Lab_s8r\n\t" \
    ".Lab_s9:\n\t" \
    "s_mov_b32 s89, 0x400000\n\t" \
    ".Lab_s9a:\n\t" \
    "s_waitcnt lgkmcnt(0)\n\t" \
    "ds_read_b32 v237, v249 offset:56\n\t" \
    "s_waitcnt lgkmcnt(0)\n\t" \
    "s_nop 1\n\t" \
    "v_readfirstlane_b32 s85, v237\n\t" \
    "s_sub_u32 s89, s89, 1\n\t" \
    "s_cbranch_scc1 .Lab_s9b\n\t" \
    "s_cmp_lt_u32 s85, 2\n\t" \
    "s_cbranch_scc1 .Lab_s9a\n\t" \
    ".Lab_s9b:\n\t" \
    "s_nop 7\n\t" \
    "s_waitcnt lgkmcnt(0)\n\t" \
    "s_branch .Lab_s9r\n\t" \
    ".p2align 12\n\t" \
    ".Lpad_ab0:\n\t" \
    "s_cbranch_execz .Lpad_top\n\t" \
    "v_mov_b32 v32, 0\n\t" \
    "v_mov_b32 v33, 0\n\t" \
    "v_mov_b32 v34, 0\n\t" \
    "v_mov_b32 v35, 0\n\t" \
    "v_mov_b32 v36, 0\n\t" \
    "v_mov_b32 v37, 0\n\t" \
    "v_mov_b32 v38, 0\n\t" \
    "v_mov_b32 v39, 0\n\t" \
    "v_mov_b32 v40, 0\n\t" \
    "v_mov_b32 v41, 0\n\t" \
    "v_mov_b32 v42, 0\n\t" \
    "v_mov_b32 v43, 0\n\t" \
    "v_mov_b32 v44, 0\n\t" \
    "v_mov_b32 v45, 0\n\t" \
    "v_mov_b32 v46, 0\n\t" \
    "v_mov_b32 v47, 0\n\t" \
    "v_mov_b32 v48, 0\n\t" \
    "v_mov_b32 v49, 0\n\t" \
    "v_mov_b32 v50, 0\n\t" \
    "v_mov_b32 v51, 0\n\t" \
    "v_mov_b32 v52, 0\n\t" \
    "v_mov_b32 v53, 0\n\t" \
    "v_mov_b32 v54, 0\n\t" \
    "v_mov_b32 v55, 0\n\t" \
    "v_mov_b32 v56, 0\n\t" \
    "v_mov_b32 v57, 0\n\t" \
    "v_mov_b32 v58, 0\n\t" \
    "v_mov_b32 v59, 0\n\t" \
    "v_mov_b32 v60, 0\n\t" \
    "v_mov_b32 v61, 0\n\t" \
    "v_mov_b32 v62, 0\n\t" \
    "v_mov_b32 v63, 0\n\t" \
    "v_mov_b32 v80, 0\n\t" \
    "v_mov_b32 v81, 0\n\t" \
    "v_mov_b32 v82, 0\n\t" \
    "v_mov_b32 v83, 0\n\t" \
    "v_mov_b32 v84, 0\n\t" \
    "v_mov_b32 v85, 0\n\t" \
    "v_mov_b32 v86, 0\n\t" \
    "v_mov_b32 v87, 0\n\t" \
    "v_mov_b32 v88, 0\n\t" \
    "v_mov_b32 v89, 0\n\t" \
    "v_mov_b32 v90, 0\n\t" \
    "v_mov_b32 v91, 0\n\t" \
    "v_mov_b32 v92, 0\n\t" \
    "v_mov_b32 v93, 0\n\t" \
    "v_mov_b32 v94, 0\n\t" \
    "v_mov_b32 v95, 0\n\t" \
    "v_mov_b32 v96, 0\n\t" \
    "v_mov_b32 v97, 0\n\t" \
    "v_mov_b32 v98, 0\n\t" \
    "v_mov_b32 v99, 0\n\t" \
    "v_mov_b32 v100, 0\n\t" \
    "v_mov_b32 v101, 0\n\t" \
    "v_mov_b32 v102, 0\n\t" \
    "v_mov_b32 v103, 0\n\t" \
    "v_mov_b32 v104, 0\n\t" \
    "v_mov_b32 v105, 0\n\t" \
    "v_mov_b32 v106, 0\n\t" \
    "v_mov_b32 v107, 0\n\t" \
    "v_mov_b32 v108, 0\n\t" \
    "v_mov_b32 v109, 0\n\t" \
    "v_mov_b32 v110, 0\n\t" \
    "v_mov_b32 v111, 0\n\t" \
    "v_mov_b32 v192, 0\n\t" \
    "v_mov_b32 v193, 0\n\t" \
    "v_mov_b32 v194, 0\n\t" \
    "v_mov_b32 v195, 0\n\t" \
    "v_mov_b32 v196, 0\n\t" \
    "v_mov_b32 v197, 0\n\t" \
    "v_mov_b32 v198, 0\n\t" \
    "v_mov_b32 v199, 0\n\t" \
    "v_mov_b32 v200, 0\n\t" \
    "v_mov_b32 v201, 0\n\t" \
    "v_mov_b32 v202, 0\n\t" \
    "v_mov_b32 v203, 0\n\t" \
    "v_mov_b32 v204, 0\n\t" \
    "v_mov_b32 v205, 0\n\t" \
    "v_mov_b32 v206, 0\n\t" \
    "v_mov_b32 v207, 0\n\t" \
    "v_mov_b32 v208, 0\n\t" \
    "v_mov_b32 v209, 0\n\t" \
    "v_mov_b32 v210, 0\n\t" \
    "v_mov_b32 v211, 0\n\t" \
    "v_mov_b32 v212, 0\n\t" \
    "v_mov_b32 v213, 0\n\t" \
    "v_mov_b32 v214, 0\n\t" \
    "v_mov_b32 v215, 0\n\t" \
    "v_mov_b32 v216, 0\n\t" \
    "v_mov_b32 v217, 0\n\t" \
    "v_mov_b32 v218, 0\n\t" \
    "v_mov_b32 v219, 0\n\t" \
    "v_mov_b32 v220, 0\n\t" \
    "v_mov_b32 v221, 0\n\t" \
    "v_mov_b32 v222, 0\n\t" \
    "v_mov_b32 v223, 0\n\t" \
    "v_mov_b32 v232, 0x3f803f80\n\t" \
    "v_mov_b32 v233, 0x3f803f80\n\t" \
    "v_mov_b32 v234, 0x3f803f80\n\t" \
    "v_mov_b32 v235, 0x3f803f80\n\t" \
    "v_mov_b32 v251, v240\n\t" \
    "v_xor_b32 v252, 64, v240\n\t" \
    "v_mov_b32 v253, v241\n\t" \
    "v_xor_b32 v254, 32, v241\n\t" \
    "s_mov_b32 s90, 0x3fb8aa3b\n\t" \
    "s_mov_b32 s91, 0x3fb8aa3b\n\t" \
    "v_cmp_eq_u32_e64 s[92:93], 2, v250\n\t" \
    "v_cmp_gt_u32_e64 s[94:95], 2, v250\n\t" \
    "v_cmp_eq_u32 vcc, 0, v250\n\t" \
    "v_xor_b32 v250, 4, v248\n\t" \
    "s_mov_b32 s84, 0x140a0014\n\t" \
    "s_lshl_b32 s83, %[xs0], 4\n\t" \
    "s_lshl_b32 s85, %[xs1], 12\n\t" \
    "s_or_b32 s83, s83, s85\n\t" \
    "s_or_b32 s83, s83, 0x14\n\t" \
    "s_waitcnt lgkmcnt(0)\n\t" \
    "s_mov_b32 s80, 64\n\t" \
    "s_add_u32 s81, %[pro], 64\n\t" \
    ".Lab_p:\n\t" \
    "s_memtime s[86:87]\n\t" \
    "s_load_dword s85, %[prb], s80\n\t" \
    "s_waitcnt lgkmcnt(0)\n\t" \
    "s_memtime s[88:89]\n\t" \
    "s_load_dword s85, %[prb], s81\n\t" \
    "s_waitcnt lgkmcnt(0)\n\t" \
    "s_memtime s[96:97]\n\t" \
    "s_mov_b32 s81, %[pro]\n\t" \
    "s_sub_u32 s80, s80, 64\n\t" \
    "s_waitcnt lgkmcnt(0)\n\t" \
    "s_cbranch_scc0 .Lab_p\n\t" \
    "s_sub_u32 s86, s88, s86\n\t" \
    "s_sub_u32 s88, s96, s88\n\t" \
    "s_mov_b32 s80, 0\n\t" \
    "s_mov_b32 s81, 10\n\t" \
    "s_pack_ll_b32_b16 s86, s86, s88\n\t" \
    "v_mov_b32 v237, s86\n\t" \
    "ds_write_b32 v248, v237 offset:320\n\t" \
    "s_waitcnt lgkmcnt(0)\n\t" \
    "s_barrier\n\t" \
    "ds_read_b128 v[128:131], v251\n\t" \
    "ds_read_b128 v[132:135], v251 offset:2048\n\t" \
    "ds_read_b128 v[136:139], v251 offset:4096\n\t" \
    "ds_read_b128 v[140:143], v251 offset:6144\n\t" \
    "ds_read_b64_tr_b16 v[0:1], v253 offset:8192\n\t" \
    "ds_read_b64_tr_b16 v[2:3], v253 offset:8704\n\t" \
    "ds_read_b64_tr_b16 v[4:5], v254 offset:8192\n\t" \
    "ds_read_b64_tr_b16 v[6:7], v254 offset:8704\n\t" \
    "ds_read_b64_tr_b16 v[8:9], v253 offset:16384\n\t" \
    "ds_read_b64_tr_b16 v[10:11], v253 offset:16896\n\t" \
    "ds_read_b64_tr_b16 v[12:13], v254 offset:16384\n\t" \
    "ds_read_b64_tr_b16 v[14:15], v254 offset:16896\n\t" \
    "ds_read_b64_tr_b16 v[16:17], v253\n\t" \
    "ds_read_b64_tr_b16 v[18:19], v253 offset:512\n\t" \
    "ds_read_b64_tr_b16 v[20:21], v254\n\t" \
    "ds_read_b64_tr_b16 v[22:23], v254 offset:512\n\t" \
    ".Lab_l1:\n\t" \
    "s_waitcnt lgkmcnt(0)\n\t" \
    "v_mfma_f32_16x16x32_bf16 v[32:35], v[0:3], v[128:131], v[32:35]\n\t" \
    "ds_read_b128 v[144:147], v252\n\t" \
    "v_mfma_f32_16x16x32_bf16 v[40:43], v[0:3], v[132:135], v[40:43]\n\t" \
    "ds_read_b128 v[148:151], v252 offset:2048\n\t" \
    "v_mfma_f32_16x16x32_bf16 v[48:51], v[0:3], v[136:139], v[48:51]\n\t" \
    "ds_read_b128 v[152:155], v252 offset:4096\n\t" \
    "v_mfma_f32_16x16x32_bf16 v[56:59], v[0:3], v[140:143], v[56:59]\n\t" \
    "ds_read_b128 v[156:159], v252 offset:6144\n\t" \
    "v_mfma_f32_16x16x32_bf16 v[36:39], v[4:7], v[128:131], v[36:39]\n\t" \
    "ds_read_b64_tr_b16 v[112:113], v253 offset:12288\n\t" \
    "v_mfma_f32_16x16x32_bf16 v[44:47], v[4:7], v[132:135], v[44:47]\n\t" \
    "ds_read_b64_tr_b16 v[114:115], v253 offset:12800\n\t" \
    "v_mfma_f32_16x16x32_bf16 v[52:55], v[4:7], v[136:139], v[52:55]\n\t" \
    "ds_read_b64_tr_b16 v[116:117], v254 offset:12288\n\t" \
    "v_mfma_f32_16x16x32_bf16 v[60:63], v[4:7], v[140:143], v[60:63]\n\t" \
    "ds_read_b64_tr_b16 v[118:119], v254 offset:12800\n\t" \
    "v_mfma_f32_16x16x32_bf16 v[80:83], v[128:131], v[8:11], v[80:83]\n\t" \
    "ds_read_b64_tr_b16 v[120:121], v253 offset:20480\n\t" \
    "v_mfma_f32_16x16x32_bf16 v[88:91], v[132:135], v[8:11], v[88:91]\n\t" \
    "ds_read_b64_tr_b16 v[122:123], v253 offset:20992\n\t" \
    "v_mfma_f32_16x16x32_bf16 v[96:99], v[136:139], v[8:11], v[96:99]\n\t" \
    "ds_read_b64_tr_b16 v[124:125], v254 offset:20480\n\t" \
    "v_mfma_f32_16x16x32_bf16 v[104:107], v[140:143], v[8:11], v[104:107]\n\t" \
    "ds_read_b64_tr_b16 v[126:127], v254 offset:20992\n\t" \
    "v_mfma_f32_16x16x32_bf16 v[84:87], v[128:131], v[12:15], v[84:87]\n\t" \
    "ds_read_b64_tr_b16 v[24:25], v253 offset:4096\n\t" \
    "v_mfma_f32_16x16x32_bf16 v[92:95], v[132:135], v[12:15], v[92:95]\n\t" \
    "ds_read_b64_tr_b16 v[26:27], v253 offset:4608\n\t" \
    "v_mfma_f32_16x16x32_bf16 v[100:103], v[136:139], v[12:15], v[100:103]\n\t" \
    "ds_read_b64_tr_b16 v[28:29], v254 offset:4096\n\t" \
    "v_mfma_f32_16x16x32_bf16 v[108:111], v[140:143], v[12:15], v[108:111]\n\t" \
    "ds_read_b64_tr_b16 v[30:31], v254 offset:4608\n\t" \
    "v_mfma_f32_16x16x32_bf16 v[192:195], v[16:19], v[128:131], v[192:195]\n\t" \
    "s_add_u32 s85, s80, 40960\n\t" \
    "s_cmp_lg_u32 s80, 81920\n\t" \
    "v_mfma_f32_16x16x32_bf16 v[200:203], v[16:19], v[132:135], v[200:203]\n\t" \
    "s_cselect_b32 s80, s85, 0\n\t" \
    "v_mfma_f32_16x16x32_bf16 v[208:211], v[16:19], v[136:139], v[208:211]\n\t" \
    "v_add_u32 v251, s80, v240\n\t" \
    "v_mfma_f32_16x16x32_bf16 v[216:219], v[16:19], v[140:143], v[216:219]\n\t" \
    "v_add_u32 v253, s80, v241\n\t" \
    "v_mfma_f32_16x16x32_bf16 v[196:199], v[20:23], v[128:131], v[196:199]\n\t" \
    "v_xor_b32 v254, 32, v253\n\t" \
    "v_mfma_f32_16x16x32_bf16 v[204:207], v[20:23], v[132:135], v[204:207]\n\t" \
    "v_mfma_f32_16x16x32_bf16 v[212:215], v[20:23], v[136:139], v[212:215]\n\t" \
    "v_mfma_f32_16x16x32_bf16 v[220:223], v[20:23], v[140:143], v[220:223]\n\t" \
    "s_waitcnt lgkmcnt(0)\n\t" \
    "s_barrier\n\t" \
    "v_mfma_f32_16x16x32_bf16 v[32:35], v[112:115], v[144:147], v[32:35]\n\t" \
    "ds_read_b128 v[128:131], v251\n\t" \
    "v_mfma_f32_16x16x32_bf16 v[40:43], v[112:115], v[148:151], v[40:43]\n\t" \
    "ds_read_b128 v[132:135], v251 offset:2048\n\t" \
    "v_mfma_f32_16x16x32_bf16 v[48:51], v[112:115], v[152:155], v[48:51]\n\t" \
    "ds_read_b128 v[136:139], v251 offset:4096\n\t" \
    "v_mfma_f32_16x16x32_bf16 v[56:59], v[112:115], v[156:159], v[56:59]\n\t" \
    "ds_read_b128 v[140:143], v251 offset:6144\n\t" \
    "v_mfma_f32_16x16x32_bf16 v[36:39], v[116:119], v[144:147], v[36:39]\n\t" \
    "ds_read_b64_tr_b16 v[0:1], v253 offset:8192\n\t" \
    "v_mfma_f32_16x16x32_bf16 v[44:47], v[116:119], v[148:151], v[44:47]\n\t" \
    "ds_read_b64_tr_b16 v[2:3], v253 offset:8704\n\t" \
    "v_mfma_f32_16x16x32_bf16 v[52:55], v[116:119], v[152:155], v[52:55]\n\t" \
    "ds_read_b64_tr_b16 v[4:5], v254 offset:8192\n\t" \
    "v_mfma_f32_16x16x32_bf16 v[60:63], v[116:119], v[156:159], v[60:63]\n\t" \
    "ds_read_b64_tr_b16 v[6:7], v254 offset:8704\n\t" \
    "v_mfma_f32_16x16x32_bf16 v[80:83], v[144:147], v[120:123], v[80:83]\n\t" \
    "ds_read_b64_tr_b16 v[8:9], v253 offset:16384\n\t" \
    "v_mfma_f32_16x16x32_bf16 v[88:91], v[148:151], v[120:123], v[88:91]\n\t" \
    "ds_read_b64_tr_b16 v[10:11], v253 offset:16896\n\t" \
    "v_mfma_f32_16x16x32_bf16 v[96:99], v[152:155], v[120:123], v[96:99]\n\t" \
    "ds_read_b64_tr_b16 v[12:13], v254 offset:16384\n\t" \
    "v_mfma_f32_16x16x32_bf16 v[104:107], v[156:159], v[120:123], v[104:107]\n\t" \
    "ds_read_b64_tr_b16 v[14:15], v254 offset:16896\n\t" \
    "v_mfma_f32_16x16x32_bf16 v[84:87], v[144:147], v[124:127], v[84:87]\n\t" \
    "ds_read_b64_tr_b16 v[16:17], v253\n\t" \
    "v_mfma_f32_16x16x32_bf16 v[92:95], v[148:151], v[124:127], v[92:95]\n\t" \
    "ds_read_b64_tr_b16 v[18:19], v253 offset:512\n\t" \
    "v_mfma_f32_16x16x32_bf16 v[100:103], v[152:155], v[124:127], v[100:103]\n\t" \
    "ds_read_b64_tr_b16 v[20:21], v254\n\t" \
    "v_mfma_f32_16x16x32_bf16 v[108:111], v[156:159], v[124:127], v[108:111]\n\t" \
    "ds_read_b64_tr_b16 v[22:23], v254 offset:512\n\t" \
    "v_mfma_f32_16x16x32_bf16 v[192:195], v[24:27], v[144:147], v[192:195]\n\t" \
    "v_xor_b32 v252, 64, v251\n\t" \
    "v_mfma_f32_16x16x32_bf16 v[200:203], v[24:27], v[148:151], v[200:203]\n\t" \
    "v_mfma_f32_16x16x32_bf16 v[208:211], v[24:27], v[152:155], v[208:211]\n\t" \
    "s_sub_u32 s81, s81, 1\n\t" \
    "v_mfma_f32_16x16x32_bf16 v[216:219], v[24:27], v[156:159], v[216:219]\n\t" \
    "s_cmp_lg_u32 s81, 0\n\t" \
    "v_mfma_f32_16x16x32_bf16 v[196:199], v[28:31], v[144:147], v[196:199]\n\t" \
    "v_mfma_f32_16x16x32_bf16 v[204:207], v[28:31], v[148:151], v[204:207]\n\t" \
    "v_mfma_f32_16x16x32_bf16 v[212:215], v[28:31], v[152:155], v[212:215]\n\t" \
    "v_mfma_f32_16x16x32_bf16 v[220:223], v[28:31], v[156:159], v[220:223]\n\t" \
    "s_cbranch_scc1 .Lab_l1\n\t" \
    ".Lab_l2:\n\t" \
    "s_waitcnt lgkmcnt(0)\n\t" \
    "v_mfma_f32_16x16x32_bf16 v[32:35], v[0:3], v[128:131], v[32:35]\n\t" \
    "ds_read_b128 v[144:147], v252\n\t" \
    "ds_read_b64_tr_b16 v[112:113], v253 offset:12288\n\t" \
    "v_mfma_f32_16x16x32_bf16 v[40:43], v[0:3], v[132:135], v[40:43]\n\t" \
    "ds_read_b128 v[148:151], v252 offset:2048\n\t" \
    "ds_read_b64_tr_b16 v[114:115], v253 offset:12800\n\t" \
    "v_mfma_f32_16x16x32_bf16 v[48:51], v[0:3], v[136:139], v[48:51]\n\t" \
    "ds_read_b128 v[152:155], v252 offset:4096\n\t" \
    "s_and_b32 s80, s84, 255\n\t" \
    "v_mfma_f32_16x16x32_bf16 v[56:59], v[0:3], v[140:143], v[56:59]\n\t" \
    "ds_read_b128 v[156:159], v252 offset:6144\n\t" \
    "s_and_b32 s82, s83, 255\n\t" \
    "v_mfma_f32_16x16x32_bf16 v[36:39], v[4:7], v[128:131], v[36:39]\n\t" \
    "ds_read_b64_tr_b16 v[116:117], v254 offset:12288\n\t" \
    "s_lshl_b32 s80, s80, 12\n\t" \
    "v_mfma_f32_16x16x32_bf16 v[44:47], v[4:7], v[132:135], v[44:47]\n\t" \
    "ds_read_b64_tr_b16 v[118:119], v254 offset:12800\n\t" \
    "s_lshl_b32 s82, s82, 12\n\t" \
    "v_mfma_f32_16x16x32_bf16 v[52:55], v[4:7], v[136:139], v[52:55]\n\t" \
    "ds_read_b64_tr_b16 v[120:121], v253 offset:20480\n\t" \
    "v_mfma_f32_16x16x32_bf16 v[60:63], v[4:7], v[140:143], v[60:63]\n\t" \
    "ds_read_b64_tr_b16 v[122:123], v253 offset:20992\n\t" \
    "v_mfma_f32_16x16x32_bf16 v[80:83], v[128:131], v[8:11], v[80:83]\n\t" \
    "ds_read_b64_tr_b16 v[124:125], v254 offset:20480\n\t" \
    "v_mfma_f32_16x16x32_bf16 v[88:91], v[132:135], v[8:11], v[88:91]\n\t" \
    "ds_read_b64_tr_b16 v[126:127], v254 offset:20992\n\t" \
    "v_mfma_f32_16x16x32_bf16 v[96:99], v[136:139], v[8:11], v[96:99]\n\t" \
    "v_add_u32 v251, s82, v240\n\t" \
    "v_mfma_f32_16x16x32_bf16 v[104:107], v[140:143], v[8:11], v[104:107]\n\t" \
    "v_add_u32 v253, s80, v241\n\t" \
    "v_mfma_f32_16x16x32_bf16 v[84:87], v[128:131], v[12:15], v[84:87]\n\t" \
    "v_xor_b32 v254, 32, v253\n\t" \
    "v_mfma_f32_16x16x32_bf16 v[92:95], v[132:135], v[12:15], v[92:95]\n\t" \
    "v_mfma_f32_16x16x32_bf16 v[100:103], v[136:139], v[12:15], v[100:103]\n\t" \
    "v_mfma_f32_16x16x32_bf16 v[108:111], v[140:143], v[12:15], v[108:111]\n\t" \
    "s_waitcnt lgkmcnt(0)\n\t" \
    "s_barrier\n\t" \
    "v_mfma_f32_16x16x32_bf16 v[32:35], v[112:115], v[144:147], v[32:35]\n\t" \
    "ds_read_b128 v[128:131], v251\n\t" \
    "ds_read_b64_tr_b16 v[0:1], v253 offset:8192\n\t" \
    "v_mfma_f32_16x16x32_bf16 v[40:43], v[112:115], v[148:151], v[40:43]\n\t" \
    "ds_read_b128 v[132:135], v251 offset:2048\n\t" \
    "ds_read_b64_tr_b16 v[2:3], v253 offset:8704\n\t" \
    "v_mfma_f32_16x16x32_bf16 v[48:51], v[112:115], v[152:155], v[48:51]\n\t" \
    "ds_read_b128 v[136:139], v251 offset:4096\n\t" \
    "v_mfma_f32_16x16x32_bf16 v[56:59], v[112:115], v[156:159], v[56:59]\n\t" \
    "ds_read_b128 v[140:143], v251 offset:6144\n\t" \
    "v_mfma_f32_16x16x32_bf16 v[36:39], v[116:119], v[144:147], v[36:39]\n\t" \
    "ds_read_b64_tr_b16 v[4:5], v254 offset:8192\n\t" \
    "v_mfma_f32_16x16x32_bf16 v[44:47], v[116:119], v[148:151], v[44:47]\n\t" \
    "ds_read_b64_tr_b16 v[6:7], v254 offset:8704\n\t" \
    "v_mfma_f32_16x16x32_bf16 v[52:55], v[116:119], v[152:155], v[52:55]\n\t" \
    "ds_read_b64_tr_b16 v[8:9], v253 offset:16384\n\t" \
    "v_mfma_f32_16x16x32_bf16 v[60:63], v[116:119], v[156:159], v[60:63]\n\t" \
    "ds_read_b64_tr_b16 v[10:11], v253 offset:16896\n\t" \
    "v_mfma_f32_16x16x32_bf16 v[80:83], v[144:147], v[120:123], v[80:83]\n\t" \
    "ds_read_b64_tr_b16 v[12:13], v254 offset:16384\n\t" \
    "v_mfma_f32_16x16x32_bf16 v[88:91], v[148:151], v[120:123], v[88:91]\n\t" \
    "ds_read_b64_tr_b16 v[14:15], v254 offset:16896\n\t" \
    "v_mfma_f32_16x16x32_bf16 v[96:99], v[152:155], v[120:123], v[96:99]\n\t" \
    "v_xor_b32 v252, 64, v251\n\t" \
    "v_mfma_f32_16x16x32_bf16 v[104:107], v[156:159], v[120:123], v[104:107]\n\t" \
    "s_lshr_b32 s83, s83, 8\n\t" \
    "v_mfma_f32_16x16x32_bf16 v[84:87], v[144:147], v[124:127], v[84:87]\n\t" \
    "s_lshr_b32 s84, s84, 8\n\t" \
    "v_mfma_f32_16x16x32_bf16 v[92:95], v[148:151], v[124:127], v[92:95]\n\t" \
    "v_mfma_f32_16x16x32_bf16 v[100:103], v[152:155], v[124:127], v[100:103]\n\t" \
    "v_mfma_f32_16x16x32_bf16 v[108:111], v[156:159], v[124:127], v[108:111]\n\t" \
    "s_cbranch_scc1 .Lab_l2\n\t" \
    "s_waitcnt lgkmcnt(0)\n\t" \
    "v_mfma_f32_16x16x32_bf16 v[32:35], v[0:3], v[128:131], v[32:35]\n\t" \
    "ds_read_b128 v[144:147], v252\n\t" \
    "ds_read_b64_tr_b16 v[112:113], v253 offset:12288\n\t" \
    "v_mfma_f32_16x16x32_bf16 v[40:43], v[0:3], v[132:135], v[40:43]\n\t" \
    "ds_read_b128 v[148:151], v252 offset:2048\n\t" \
    "ds_read_b64_tr_b16 v[114:115], v253 offset:12800\n\t" \
    "v_mfma_f32_16x16x32_bf16 v[48:51], v[0:3], v[136:139], v[48:51]\n\t" \
    "ds_read_b128 v[152:155], v252 offset:4096\n\t" \
    "ds_read_b64_tr_b16 v[116:117], v254 offset:12288\n\t" \
    "v_mfma_f32_16x16x32_bf16 v[56:59], v[0:3], v[140:143], v[56:59]\n\t" \
    "ds_read_b128 v[156:159], v252 offset:6144\n\t" \
    "ds_read_b64_tr_b16 v[118:119], v254 offset:12800\n\t" \
    "v_mfma_f32_16x16x32_bf16 v[36:39], v[4:7], v[128:131], v[36:39]\n\t" \
    "ds_read_b64_tr_b16 v[120:121], v253 offset:20480\n\t" \
    "ds_read_b64_tr_b16 v[122:123], v253 offset:20992\n\t" \
    "v_mfma_f32_16x16x32_bf16 v[44:47], v[4:7], v[132:135], v[44:47]\n\t" \
    "ds_read_b64_tr_b16 v[124:125], v254 offset:20480\n\t" \
    "ds_read_b64_tr_b16 v[126:127], v254 offset:20992\n\t" \
    "v_mfma_f32_16x16x32_bf16 v[52:55], v[4:7], v[136:139], v[52:55]\n\t" \
    "ds_read_b64_tr_b16 v[224:225], v245\n\t" \
    "ds_read_b64_tr_b16 v[226:227], v245 offset:1024\n\t" \
    "v_mfma_f32_16x16x32_bf16 v[60:63], v[4:7], v[140:143], v[60:63]\n\t" \
    "v_xor_b32 v245, 32, v245\n\t" \
    "ds_read_b32 v236, v249 offset:288\n\t" \
    "v_mfma_f32_16x16x32_bf16 v[80:83], v[128:131], v[8:11], v[80:83]\n\t" \
    "ds_read_b64_tr_b16 v[228:229], v245\n\t" \
    "ds_read_b64_tr_b16 v[230:231], v245 offset:1024\n\t" \
    "v_mfma_f32_16x16x32_bf16 v[88:91], v[132:135], v[8:11], v[88:91]\n\t" \
    "ds_read_b128 v[68:71], v249 offset:320\n\t" \
    "ds_read_b64 v[64:65], v249 offset:0\n\t" \
    "v_mfma_f32_16x16x32_bf16 v[96:99], v[136:139], v[8:11], v[96:99]\n\t" \
    "v_add_u32 v251, 131072, v240\n\t" \
    "v_mfma_f32_16x16x32_bf16 v[104:107], v[140:143], v[8:11], v[104:107]\n\t" \
    "v_xor_b32 v254, 32, v241\n\t" \
    "v_mfma_f32_16x16x32_bf16 v[84:87], v[128:131], v[12:15], v[84:87]\n\t" \
    "v_mov_b32 v253, v241\n\t" \
    "v_mfma_f32_16x16x32_bf16 v[92:95], v[132:135], v[12:15], v[92:95]\n\t" \
    "v_xor_b32 v252, 64, v251\n\t" \
    "v_mfma_f32_16x16x32_bf16 v[100:103], v[136:139], v[12:15], v[100:103]\n\t" \
    "v_mfma_f32_16x16x32_bf16 v[108:111], v[140:143], v[12:15], v[108:111]\n\t" \
    "s_waitcnt lgkmcnt(0)\n\t" \
    "v_mfma_f32_16x16x32_bf16 v[32:35], v[112:115], v[144:147], v[32:35]\n\t" \
    "v_min_u32 v64, v64, v65\n\t" \
    "v_mfma_f32_16x16x32_bf16 v[40:43], v[112:115], v[148:151], v[40:43]\n\t" \
    "v_readfirstlane_b32 s85, v64\n\t" \
    "s_cmp_lt_u32 s85, 1\n\t" \
    "s_cbranch_scc1 .Lab_s1\n\t" \
    ".Lab_s1r:\n\t" \
    "v_mfma_f32_16x16x32_bf16 v[48:51], v[112:115], v[152:155], v[48:51]\n\t" \
    "ds_read_b128 v[160:163], v251\n\t" \
    "ds_read_b64_tr_b16 v[0:1], v253 offset:8192\n\t" \
    "v_mfma_f32_16x16x32_bf16 v[56:59], v[112:115], v[156:159], v[56:59]\n\t" \
    "ds_read_b128 v[164:167], v251 offset:2048\n\t" \
    "ds_read_b64_tr_b16 v[2:3], v253 offset:8704\n\t" \
    "v_mfma_f32_16x16x32_bf16 v[36:39], v[116:119], v[144:147], v[36:39]\n\t" \
    "ds_read_b128 v[168:171], v251 offset:4096\n\t" \
    "ds_read_b64_tr_b16 v[4:5], v254 offset:8192\n\t" \
    "v_mfma_f32_16x16x32_bf16 v[44:47], v[116:119], v[148:151], v[44:47]\n\t" \
    "ds_read_b128 v[172:175], v251 offset:6144\n\t" \
    "ds_read_b64_tr_b16 v[6:7], v254 offset:8704\n\t" \
    "v_mfma_f32_16x16x32_bf16 v[52:55], v[116:119], v[152:155], v[52:55]\n\t" \
    "ds_read_b64_tr_b16 v[8:9], v253 offset:16384\n\t" \
    "ds_read_b64_tr_b16 v[10:11], v253 offset:16896\n\t" \
    "v_mfma_f32_16x16x32_bf16 v[60:63], v[116:119], v[156:159], v[60:63]\n\t" \
    "ds_read_b64_tr_b16 v[12:13], v254 offset:16384\n\t" \
    "ds_read_b64_tr_b16 v[14:15], v254 offset:16896\n\t" \
    "v_mfma_f32_16x16x32_bf16 v[80:83], v[144:147], v[120:123], v[80:83]\n\t" \
    "v_readfirstlane_b32 s87, v68\n\t" \
    "v_mfma_f32_16x16x32_bf16 v[88:91], v[148:151], v[120:123], v[88:91]\n\t" \
    "v_readfirstlane_b32 s88, v69\n\t" \
    "v_mfma_f32_16x16x32_bf16 v[96:99], v[152:155], v[120:123], v[96:99]\n\t" \
    "v_readfirstlane_b32 %[sof], v70\n\t" \
    "v_mfma_f32_16x16x32_bf16 v[104:107], v[156:159], v[120:123], v[104:107]\n\t" \
    "s_xor_b32 s87, s87, %[kpx]\n\t" \
    "s_add_u32 s88, s88, %[kma]\n\t" \
    "v_mfma_f32_16x16x32_bf16 v[84:87], v[144:147], v[124:127], v[84:87]\n\t" \
    "s_add_u32 s87, s87, %[kpa]\n\t" \
    "v_mfma_f32_16x16x32_bf16 v[92:95], v[148:151], v[124:127], v[92:95]\n\t" \
    "s_xor_b32 s86, s87, 0x8000\n\t" \
    "v_mfma_f32_16x16x32_bf16 v[100:103], v[152:155], v[124:127], v[100:103]\n\t" \
    "v_mfma_f32_16x16x32_bf16 v[108:111], v[156:159], v[124:127], v[108:111]\n\t" \
    "s_waitcnt lgkmcnt(0)\n\t" \
    "v_mfma_f32_16x16x32_bf16 v[32:35], v[0:3], v[160:163], v[32:35]\n\t" \
    "ds_read_b128 v[176:179], v252\n\t" \
    "ds_read_b64_tr_b16 v[112:113], v253 offset:12288\n\t" \
    "v_mfma_f32_16x16x32_bf16 v[40:43], v[0:3], v[164:167], v[40:43]\n\t" \
    "ds_read_b128 v[180:183], v252 offset:2048\n\t" \
    "ds_read_b64_tr_b16 v[114:115], v253 offset:12800\n\t" \
    "v_mfma_f32_16x16x32_bf16 v[48:51], v[0:3], v[168:171], v[48:51]\n\t" \
    "ds_read_b128 v[184:187], v252 offset:4096\n\t" \
    "v_mov_b32 v237, 2\n\t" \
    "v_mfma_f32_16x16x32_bf16 v[56:59], v[0:3], v[172:175], v[56:59]\n\t" \
    "ds_read_b128 v[188:191], v252 offset:6144\n\t" \
    "v_mfma_f32_16x16x32_bf16 v[36:39], v[4:7], v[160:163], v[36:39]\n\t" \
    "ds_read_b64_tr_b16 v[116:117], v254 offset:12288\n\t" \
    "v_mfma_f32_16x16x32_bf16 v[44:47], v[4:7], v[164:167], v[44:47]\n\t" \
    "ds_read_b64_tr_b16 v[118:119], v254 offset:12800\n\t" \
    "v_mfma_f32_16x16x32_bf16 v[52:55], v[4:7], v[168:171], v[52:55]\n\t" \
    "ds_read_b64_tr_b16 v[120:121], v253 offset:20480\n\t" \
    "v_mfma_f32_16x16x32_bf16 v[60:63], v[4:7], v[172:175], v[60:63]\n\t" \
    "ds_read_b64_tr_b16 v[122:123], v253 offset:20992\n\t" \
    "v_mfma_f32_16x16x32_bf16 v[80:83], v[160:163], v[8:11], v[80:83]\n\t" \
    "ds_read_b64_tr_b16 v[124:125], v254 offset:20480\n\t" \
    "v_mfma_f32_16x16x32_bf16 v[88:91], v[164:167], v[8:11], v[88:91]\n\t" \
    "ds_read_b64_tr_b16 v[126:127], v254 offset:20992\n\t" \
    "v_mfma_f32_16x16x32_bf16 v[96:99], v[168:171], v[8:11], v[96:99]\n\t" \
    "ds_write_b32 v248, v237\n\t" \
    "v_mfma_f32_16x16x32_bf16 v[104:107], v[172:175], v[8:11], v[104:107]\n\t" \
    "v_xor_b32 v251, 32, v243\n\t" \
    "v_mfma_f32_16x16x32_bf16 v[84:87], v[160:163], v[12:15], v[84:87]\n\t" \
    "v_xor_b32 v252, 64, v243\n\t" \
    "v_mfma_f32_16x16x32_bf16 v[92:95], v[164:167], v[12:15], v[92:95]\n\t" \
    "v_xor_b32 v253, 0x60, v243\n\t" \
    "v_mfma_f32_16x16x32_bf16 v[100:103], v[168:171], v[12:15], v[100:103]\n\t" \
    "s_setprio 3\n\t" \
    "v_mfma_f32_16x16x32_bf16 v[108:111], v[172:175], v[12:15], v[108:111]\n\t" \
    "s_waitcnt lgkmcnt(0)\n\t" \
    "v_mfma_f32_16x16x32_bf16 v[32:35], v[112:115], v[176:179], v[32:35]\n\t" \
    "v_mfma_f32_16x16x32_bf16 v[36:39], v[116:119], v[176:179], v[36:39]\n\t" \
    "v_mfma_f32_16x16x32_bf16 v[40:43], v[112:115], v[180:183], v[40:43]\n\t" \
    "v_mfma_f32_16x16x32_bf16 v[44:47], v[116:119], v[180:183], v[44:47]\n\t" \
    "ds_read_b128 v[28:31], v249 offset:16\n\t" \
    "v_mfma_f32_16x16x32_bf16 v[48:51], v[112:115], v[184:187], v[48:51]\n\t" \
    "v_mfma_f32_16x16x32_bf16 v[52:55], v[116:119], v[184:187], v[52:55]\n\t" \
    "v_mfma_f32_16x16x32_bf16 v[56:59], v[112:115], v[188:191], v[56:59]\n\t" \
    "v_mfma_f32_16x16x32_bf16 v[60:63], v[116:119], v[188:191], v[60:63]\n\t" \
    "v_cvt_pk_bf16_f32 v64, v32, v33\n\t" \
    "v_cvt_pk_bf16_f32 v65, v34, v35\n\t" \
    "v_mfma_f32_16x16x32_bf16 v[80:83], v[176:179], v[120:123], v[80:83]\n\t" \
    "v_cvt_pk_bf16_f32 v66, v36, v37\n\t" \
    "v_cvt_pk_bf16_f32 v67, v38, v39\n\t" \
    "v_mfma_f32_16x16x32_bf16 v[88:91], v[180:183], v[120:123], v[88:91]\n\t" \
    "v_cvt_pk_bf16_f32 v68, v40, v41\n\t" \
    "v_cvt_pk_bf16_f32 v69, v42, v43\n\t" \
    "v_mfma_f32_16x16x32_bf16 v[84:87], v[176:179], v[124:127], v[84:87]\n\t" \
    "v_cvt_pk_bf16_f32 v70, v44, v45\n\t" \
    "v_cvt_pk_bf16_f32 v71, v46, v47\n\t" \
    "v_mfma_f32_16x16x32_bf16 v[92:95], v[180:183], v[124:127], v[92:95]\n\t" \
    "v_cvt_pk_bf16_f32 v72, v48, v49\n\t" \
    "v_cvt_pk_bf16_f32 v73, v50, v51\n\t" \
    "v_mfma_f32_16x16x32_bf16 v[96:99], v[184:187], v[120:123], v[96:99]\n\t" \
    "v_cvt_pk_bf16_f32 v74, v52, v53\n\t" \
    "v_cvt_pk_bf16_f32 v75, v54, v55\n\t" \
    "v_mfma_f32_16x16x32_bf16 v[104:107], v[188:191], v[120:123], v[104:107]\n\t" \
    "v_cvt_pk_bf16_f32 v76, v56, v57\n\t" \
    "v_cvt_pk_bf16_f32 v77, v58, v59\n\t" \
    "v_mfma_f32_16x16x32_bf16 v[100:103], v[184:187], v[124:127], v[100:103]\n\t" \
    "v_cvt_pk_bf16_f32 v78, v60, v61\n\t" \
    "v_cvt_pk_bf16_f32 v79, v62, v63\n\t" \
    "v_mfma_f32_16x16x32_bf16 v[108:111], v[188:191], v[124:127], v[108:111]\n\t" \
    "s_waitcnt lgkmcnt(0)\n\t" \
    "v_min_u32 v238, v28, v29\n\t" \
    "v_min3_u32 v238, v238, v30, v31\n\t" \
    "v_mfma_f32_16x16x32_bf16 v[0:3], v[224:227], v[64:67], 0\n\t" \
    "v_cvt_pk_bf16_f32 v112, v80, v81\n\t" \
    "v_cvt_pk_bf16_f32 v113, v82, v83\n\t" \
    "v_mfma_f32_16x16x32_bf16 v[4:7], v[228:231], v[64:67], 0\n\t" \
    "v_cvt_pk_bf16_f32 v114, v88, v89\n\t" \
    "v_cvt_pk_bf16_f32 v115, v90, v91\n\t" \
    "v_mfma_f32_16x16x32_bf16 v[8:11], v[224:227], v[68:71], 0\n\t" \
    "v_cvt_pk_bf16_f32 v116, v84, v85\n\t" \
    "v_cvt_pk_bf16_f32 v117, v86, v87\n\t" \
    "v_mfma_f32_16x16x32_bf16 v[12:15], v[228:231], v[68:71], 0\n\t" \
    "v_cvt_pk_bf16_f32 v118, v92, v93\n\t" \
    "v_cvt_pk_bf16_f32 v119, v94, v95\n\t" \
    "v_mfma_f32_16x16x32_bf16 v[16:19], v[224:227], v[72:75], 0\n\t" \
    "v_cvt_pk_bf16_f32 v120, v96, v97\n\t" \
    "v_cvt_pk_bf16_f32 v121, v98, v99\n\t" \
    "v_mfma_f32_16x16x32_bf16 v[20:23], v[228:231], v[72:75], 0\n\t" \
    "v_cvt_pk_bf16_f32 v122, v104, v105\n\t" \
    "v_cvt_pk_bf16_f32 v123, v106, v107\n\t" \
    "v_mfma_f32_16x16x32_bf16 v[24:27], v[224:227], v[76:79], 0\n\t" \
    "v_cvt_pk_bf16_f32 v124, v100, v101\n\t" \
    "v_cvt_pk_bf16_f32 v125, v102, v103\n\t" \
    "v_mfma_f32_16x16x32_bf16 v[28:31], v[228:231], v[76:79], 0\n\t" \
    "v_cvt_pk_bf16_f32 v126, v108, v109\n\t" \
    "v_cvt_pk_bf16_f32 v127, v110, v111\n\t" \
    "v_readfirstlane_b32 s85, v238\n\t" \
    "s_cmp_lt_u32 s85, 2\n\t" \
    "s_cbranch_scc1 .Lab_s2\n\t" \
    ".Lab_s2r:\n\t" \
    "ds_write_b128 v242, v[112:115]\n\t" \
    "v_max3_f32 v64, v0, v1, v2\n\t" \
    "v_max3_f32 v66, -v0, -v1, -v2\n\t" \
    "v_max3_f32 v68, v8, v9, v10\n\t" \
    "v_max3_f32 v64, v64, v3, v4\n\t" \
    "v_max3_f32 v66, v66, -v3, -v4\n\t" \
    "v_max3_f32 v68, v68, v11, v12\n\t" \
    "v_max3_f32 v64, v64, v5, v6\n\t" \
    "v_max3_f32 v66, v66, -v5, -v6\n\t" \
    "ds_write_b128 v242, v[116:119] offset:1024\n\t" \
    "v_max3_f32 v68, v68, v13, v14\n\t" \
    "v_max_f32_e64 v64, v64, v7\n\t" \
    "v_max_f32_e64 v66, v66, -v7\n\t" \
    "v_max_f32_e64 v68, v68, v15\n\t" \
    "v_max3_f32 v70, -v8, -v9, -v10\n\t" \
    "v_permlane32_swap_b32 v64, v66\n\t" \
    "v_max3_f32 v72, v16, v17, v18\n\t" \
    "v_max3_f32 v70, v70, -v11, -v12\n\t" \
    "ds_write_b128 v242, v[120:123] offset:4096\n\t" \
    "v_max_f32 v64, v64, v66\n\t" \
    "v_max3_f32 v72, v72, v19, v20\n\t" \
    "v_max3_f32 v70, v70, -v13, -v14\n\t" \
    "v_max3_f32 v74, -v16, -v17, -v18\n\t" \
    "v_max3_f32 v72, v72, v21, v22\n\t" \
    "v_max_f32_e64 v70, v70, -v15\n\t" \
    "v_max3_f32 v74, v74, -v19, -v20\n\t" \
    "v_max_f32_e64 v72, v72, v23\n\t" \
    "ds_write_b128 v242, v[124:127] offset:5120\n\t" \
    "v_permlane32_swap_b32 v68, v70\n\t" \
    "v_max3_f32 v74, v74, -v21, -v22\n\t" \
    "v_max3_f32 v76, v24, v25, v26\n\t" \
    "v_max_f32 v68, v68, v70\n\t" \
    "v_max_f32_e64 v74, v74, -v23\n\t" \
    "v_max3_f32 v76, v76, v27, v28\n\t" \
    "v_permlane16_swap_b32 v64, v68\n\t" \
    "v_permlane32_swap_b32 v72, v74\n\t" \
    "v_max3_f32 v76, v76, v29, v30\n\t" \
    "v_max_f32 v66, v64, v68\n\t" \
    "v_max_f32 v72, v72, v74\n\t" \
    "v_max_f32_e64 v76, v76, v31\n\t" \
    "v_xor_b32 v254, 32, v244\n\t" \
    "v_mul_f32 v64, 0xbfb8aa3b, v66\n\t" \
    "v_mul_f32 v68, 0xbfb8aa3b, v66\n\t" \
    "v_xor_b32 v245, 0x2000, v244\n\t" \
    "v_max3_f32 v78, -v24, -v25, -v26\n\t" \
    "v_max3_f32 v78, v78, -v27, -v28\n\t" \
    "v_xor_b32 v255, 0x2020, v244\n\t" \
    "v_permlane16_swap_b32 v64, v68\n\t" \
    "v_mov_b32 v66, v64\n\t" \
    "v_xor_b32 v242, 0x2000, v242\n\t" \
    "v_max3_f32 v78, v78, -v29, -v30\n\t" \
    "v_mov_b32 v70, v68\n\t" \
    "v_permlane32_swap_b32 v64, v66\n\t" \
    "v_max_f32_e64 v78, v78, -v31\n\t" \
    "v_permlane32_swap_b32 v68, v70\n\t" \
    "v_pk_fma_f32 v[32:33], v[0:1], s[90:91], v[64:65] op_sel_hi:[1,1,0]\n\t" \
    "v_permlane32_swap_b32 v76, v78\n\t" \
    "v_pk_fma_f32 v[0:1], v[0:1], s[90:91], v[66:67] op_sel_hi:[1,1,0] neg_lo:[1,0,0] neg_hi:[1,0,0]\n\t" \
    "v_pk_fma_f32 v[34:35], v[2:3], s[90:91], v[64:65] op_sel_hi:[1,1,0]\n\t" \
    "v_max_f32 v76, v76, v78\n\t" \
    "v_pk_fma_f32 v[2:3], v[2:3], s[90:91], v[66:67] op_sel_hi:[1,1,0] neg_lo:[1,0,0] neg_hi:[1,0,0]\n\t" \
    "v_pk_fma_f32 v[36:37], v[4:5], s[90:91], v[64:65] op_sel_hi:[1,1,0]\n\t" \
    "v_permlane16_swap_b32 v72, v76\n\t" \
    "v_pk_fma_f32 v[4:5], v[4:5], s[90:91], v[66:67] op_sel_hi:[1,1,0] neg_lo:[1,0,0] neg_hi:[1,0,0]\n\t" \
    "v_pk_fma_f32 v[38:39], v[6:7], s[90:91], v[64:65] op_sel_hi:[1,1,0]\n\t" \
    "v_max_f32 v74, v72, v76\n\t" \
    "v_pk_fma_f32 v[6:7], v[6:7], s[90:91], v[66:67] op_sel_hi:[1,1,0] neg_lo:[1,0,0] neg_hi:[1,0,0]\n\t" \
    "v_exp_f32 v32, v32\n\t" \
    "v_mul_f32 v72, 0xbfb8aa3b, v74\n\t" \
    "v_mul_f32 v76, 0xbfb8aa3b, v74\n\t" \
    "v_exp_f32 v0, v0\n\t" \
    "v_exp_f32 v33, v33\n\t" \
    "v_permlane16_swap_b32 v72, v76\n\t" \
    "v_exp_f32 v1, v1\n\t" \
    "v_exp_f32 v34, v34\n\t" \
    "v_mov_b32 v74, v72\n\t" \
    "v_mov_b32 v78, v76\n\t" \
    "v_exp_f32 v2, v2\n\t" \
    "v_permlane32_swap_b32 v72, v74\n\t" \
    "v_permlane32_swap_b32 v76, v78\n\t" \
    "v_exp_f32 v35, v35\n\t" \
    "v_exp_f32 v3, v3\n\t" \
    "v_exp_f32 v36, v36\n\t" \
    "v_pk_add_f32 v[64:65], v[32:33], v[34:35]\n\t" \
    "v_pk_add_f32 v[66:67], v[0:1], v[2:3]\n\t" \
    "v_exp_f32 v4, v4\n\t" \
    "v_exp_f32 v37, v37\n\t" \
    "v_exp_f32 v5, v5\n\t" \
    "v_exp_f32 v38, v38\n\t" \
    "v_pk_add_f32 v[64:65], v[64:65], v[36:37]\n\t" \
    "v_pk_add_f32 v[66:67], v[66:67], v[4:5]\n\t" \
    "v_exp_f32 v6, v6\n\t" \
    "v_exp_f32 v39, v39\n\t" \
    "v_exp_f32 v7, v7\n\t" \
    "v_pk_fma_f32 v[40:41], v[8:9], s[90:91], v[68:69] op_sel_hi:[1,1,0]\n\t" \
    "v_pk_add_f32 v[64:65], v[64:65], v[38:39]\n\t" \
    "v_pk_add_f32 v[66:67], v[66:67], v[6:7]\n\t" \
    "v_pk_fma_f32 v[8:9], v[8:9], s[90:91], v[70:71] op_sel_hi:[1,1,0] neg_lo:[1,0,0] neg_hi:[1,0,0]\n\t" \
    "v_add_f32 v64, v64, v65\n\t" \
    "v_add_f32 v66, v66, v67\n\t" \
    "v_pk_fma_f32 v[42:43], v[10:11], s[90:91], v[68:69] op_sel_hi:[1,1,0]\n\t" \
    "v_pk_fma_f32 v[10:11], v[10:11], s[90:91], v[70:71] op_sel_hi:[1,1,0] neg_lo:[1,0,0] neg_hi:[1,0,0]\n\t" \
    "v_permlane32_swap_b32 v64, v66\n\t" \
    "v_pk_fma_f32 v[44:45], v[12:13], s[90:91], v[68:69] op_sel_hi:[1,1,0]\n\t" \
    "v_pk_fma_f32 v[12:13], v[12:13], s[90:91], v[70:71] op_sel_hi:[1,1,0] neg_lo:[1,0,0] neg_hi:[1,0,0]\n\t" \
    "v_add_f32 v64, v64, v66\n\t" \
    "v_pk_fma_f32 v[46:47], v[14:15], s[90:91], v[68:69] op_sel_hi:[1,1,0]\n\t" \
    "v_pk_fma_f32 v[14:15], v[14:15], s[90:91], v[70:71] op_sel_hi:[1,1,0] neg_lo:[1,0,0] neg_hi:[1,0,0]\n\t" \
    "v_exp_f32 v40, v40\n\t" \
    "v_exp_f32 v8, v8\n\t" \
    "v_exp_f32 v41, v41\n\t" \
    "v_exp_f32 v9, v9\n\t" \
    "v_exp_f32 v42, v42\n\t" \
    "v_exp_f32 v10, v10\n\t" \
    "v_exp_f32 v43, v43\n\t" \
    "v_exp_f32 v11, v11\n\t" \
    "v_exp_f32 v44, v44\n\t" \
    "v_pk_add_f32 v[68:69], v[40:41], v[42:43]\n\t" \
    "v_pk_add_f32 v[70:71], v[8:9], v[10:11]\n\t" \
    "v_exp_f32 v12, v12\n\t" \
    "v_exp_f32 v45, v45\n\t" \
    "v_exp_f32 v13, v13\n\t" \
    "v_exp_f32 v46, v46\n\t" \
    "v_pk_add_f32 v[68:69], v[68:69], v[44:45]\n\t" \
    "v_pk_add_f32 v[70:71], v[70:71], v[12:13]\n\t" \
    "v_exp_f32 v14, v14\n\t" \
    "v_exp_f32 v47, v47\n\t" \
    "v_exp_f32 v15, v15\n\t" \
    "v_pk_fma_f32 v[48:49], v[16:17], s[90:91], v[72:73] op_sel_hi:[1,1,0]\n\t" \
    "v_pk_add_f32 v[68:69], v[68:69], v[46:47]\n\t" \
    "v_pk_add_f32 v[70:71], v[70:71], v[14:15]\n\t" \
    ".Lpad_ab1:\n\t" \
    "s_cbranch_execz .Lpad_ret\n\t" \
    "v_pk_fma_f32 v[16:17], v[16:17], s[90:91], v[74:75] op_sel_hi:[1,1,0] neg_lo:[1,0,0] neg_hi:[1,0,0]\n\t" \
    "v_add_f32 v68, v68, v69\n\t" \
    "v_add_f32 v70, v70, v71\n\t" \
    "v_pk_fma_f32 v[50:51], v[18:19], s[90:91], v[72:73] op_sel_hi:[1,1,0]\n\t" \
    "v_pk_fma_f32 v[18:19], v[18:19], s[90:91], v[74:75] op_sel_hi:[1,1,0] neg_lo:[1,0,0] neg_hi:[1,0,0]\n\t" \
    "v_permlane32_swap_b32 v68, v70\n\t" \
    "v_pk_fma_f32 v[52:53], v[20:21], s[90:91], v[72:73] op_sel_hi:[1,1,0]\n\t" \
    "v_pk_fma_f32 v[20:21], v[20:21], s[90:91], v[74:75] op_sel_hi:[1,1,0] neg_lo:[1,0,0] neg_hi:[1,0,0]\n\t" \
    "v_add_f32 v68, v68, v70\n\t" \
    "v_pk_fma_f32 v[54:55], v[22:23], s[90:91], v[72:73] op_sel_hi:[1,1,0]\n\t" \
    "v_pk_fma_f32 v[22:23], v[22:23], s[90:91], v[74:75] op_sel_hi:[1,1,0] neg_lo:[1,0,0] neg_hi:[1,0,0]\n\t" \
    "v_permlane16_swap_b32 v64, v68\n\t" \
    "v_exp_f32 v48, v48\n\t" \
    "v_exp_f32 v16, v16\n\t" \
    "v_add_f32 v66, v64, v68\n\t" \
    "v_exp_f32 v49, v49\n\t" \
    "v_exp_f32 v17, v17\n\t" \
    "v_rcp_f32 v64, v66\n\t" \
    "v_exp_f32 v50, v50\n\t" \
    "v_exp_f32 v18, v18\n\t" \
    "v_mov_b32 v68, v64\n\t" \
    "v_exp_f32 v51, v51\n\t" \
    "v_exp_f32 v19, v19\n\t" \
    "v_permlane16_swap_b32 v64, v68\n\t" \
    "v_pk_add_f32 v[72:73], v[48:49], v[50:51]\n\t" \
    "v_pk_add_f32 v[74:75], v[16:17], v[18:19]\n\t" \
    "v_mov_b32 v66, v64\n\t" \
    "v_mov_b32 v70, v68\n\t" \
    "v_exp_f32 v52, v52\n\t" \
    "v_permlane32_swap_b32 v64, v66\n\t" \
    "v_permlane32_swap_b32 v68, v70\n\t" \
    "v_exp_f32 v20, v20\n\t" \
    "v_pk_mul_f32 v[0:1], v[0:1], v[66:67] op_sel_hi:[1,0]\n\t" \
    "v_pk_mul_f32 v[32:33], v[32:33], v[64:65] op_sel_hi:[1,0]\n\t" \
    "v_pk_mul_f32 v[2:3], v[2:3], v[66:67] op_sel_hi:[1,0]\n\t" \
    "v_pk_mul_f32 v[34:35], v[34:35], v[64:65] op_sel_hi:[1,0]\n\t" \
    "v_pk_mul_f32 v[4:5], v[4:5], v[66:67] op_sel_hi:[1,0]\n\t" \
    "v_pk_mul_f32 v[36:37], v[36:37], v[64:65] op_sel_hi:[1,0]\n\t" \
    "v_pk_mul_f32 v[6:7], v[6:7], v[66:67] op_sel_hi:[1,0]\n\t" \
    "v_pk_mul_f32 v[38:39], v[38:39], v[64:65] op_sel_hi:[1,0]\n\t" \
    "v_exp_f32 v53, v53\n\t" \
    "v_cvt_pk_bf16_f32 v7, v6, v7\n\t" \
    "v_cvt_pk_bf16_f32 v6, v4, v5\n\t" \
    "v_cvt_pk_bf16_f32 v5, v2, v3\n\t" \
    "v_cvt_pk_bf16_f32 v4, v0, v1\n\t" \
    "ds_write_b64 v253, v[6:7] offset:0\n\t" \
    "v_cvt_pk_bf16_f32 v0, v32, v33\n\t" \
    "v_cvt_pk_bf16_f32 v1, v34, v35\n\t" \
    "v_cvt_pk_bf16_f32 v2, v36, v37\n\t" \
    "v_cvt_pk_bf16_f32 v3, v38, v39\n\t" \
    "v_pk_add_f32 v[72:73], v[72:73], v[52:53]\n\t" \
    "ds_write_b64 v243, v[0:1] offset:0\n\t" \
    "v_exp_f32 v21, v21\n\t" \
    "v_exp_f32 v54, v54\n\t" \
    "v_exp_f32 v22, v22\n\t" \
    "v_pk_add_f32 v[74:75], v[74:75], v[20:21]\n\t" \
    "v_exp_f32 v55, v55\n\t" \
    "ds_write_b64 v251, v[2:3] offset:0\n\t" \
    "v_exp_f32 v23, v23\n\t" \
    "v_pk_add_f32 v[72:73], v[72:73], v[54:55]\n\t" \
    "v_pk_fma_f32 v[56:57], v[24:25], s[90:91], v[76:77] op_sel_hi:[1,1,0]\n\t" \
    "v_pk_add_f32 v[74:75], v[74:75], v[22:23]\n\t" \
    "v_add_f32 v72, v72, v73\n\t" \
    "ds_write_b64 v252, v[4:5] offset:0\n\t" \
    "v_add_f32 v74, v74, v75\n\t" \
    "v_pk_fma_f32 v[24:25], v[24:25], s[90:91], v[78:79] op_sel_hi:[1,1,0] neg_lo:[1,0,0] neg_hi:[1,0,0]\n\t" \
    "v_pk_fma_f32 v[58:59], v[26:27], s[90:91], v[76:77] op_sel_hi:[1,1,0]\n\t" \
    "v_permlane32_swap_b32 v72, v74\n\t" \
    "v_pk_fma_f32 v[26:27], v[26:27], s[90:91], v[78:79] op_sel_hi:[1,1,0] neg_lo:[1,0,0] neg_hi:[1,0,0]\n\t" \
    "v_pk_fma_f32 v[60:61], v[28:29], s[90:91], v[76:77] op_sel_hi:[1,1,0]\n\t" \
    "v_add_f32 v72, v72, v74\n\t" \
    "v_pk_fma_f32 v[28:29], v[28:29], s[90:91], v[78:79] op_sel_hi:[1,1,0] neg_lo:[1,0,0] neg_hi:[1,0,0]\n\t" \
    "v_pk_fma_f32 v[62:63], v[30:31], s[90:91], v[76:77] op_sel_hi:[1,1,0]\n\t" \
    "v_pk_fma_f32 v[30:31], v[30:31], s[90:91], v[78:79] op_sel_hi:[1,1,0] neg_lo:[1,0,0] neg_hi:[1,0,0]\n\t" \
    "v_exp_f32 v56, v56\n\t" \
    "v_exp_f32 v24, v24\n\t" \
    "v_exp_f32 v57, v57\n\t" \
    "v_exp_f32 v25, v25\n\t" \
    "v_exp_f32 v58, v58\n\t" \
    "v_exp_f32 v26, v26\n\t" \
    "v_exp_f32 v59, v59\n\t" \
    "v_exp_f32 v27, v27\n\t" \
    "v_exp_f32 v60, v60\n\t" \
    "v_pk_add_f32 v[76:77], v[56:57], v[58:59]\n\t" \
    "v_pk_add_f32 v[78:79], v[24:25], v[26:27]\n\t" \
    "v_exp_f32 v28, v28\n\t" \
    "v_exp_f32 v61, v61\n\t" \
    "v_exp_f32 v29, v29\n\t" \
    "v_exp_f32 v62, v62\n\t" \
    "v_pk_add_f32 v[76:77], v[76:77], v[60:61]\n\t" \
    "v_pk_add_f32 v[78:79], v[78:79], v[28:29]\n\t" \
    "v_exp_f32 v30, v30\n\t" \
    "v_exp_f32 v63, v63\n\t" \
    "v_exp_f32 v31, v31\n\t" \
    "v_pk_mul_f32 v[8:9], v[8:9], v[70:71] op_sel_hi:[1,0]\n\t" \
    "v_pk_add_f32 v[76:77], v[76:77], v[62:63]\n\t" \
    "v_pk_add_f32 v[78:79], v[78:79], v[30:31]\n\t" \
    "v_pk_mul_f32 v[40:41], v[40:41], v[68:69] op_sel_hi:[1,0]\n\t" \
    "v_add_f32 v76, v76, v77\n\t" \
    "v_add_f32 v78, v78, v79\n\t" \
    "v_pk_mul_f32 v[10:11], v[10:11], v[70:71] op_sel_hi:[1,0]\n\t" \
    "v_pk_mul_f32 v[42:43], v[42:43], v[68:69] op_sel_hi:[1,0]\n\t" \
    "v_permlane32_swap_b32 v76, v78\n\t" \
    "v_pk_mul_f32 v[12:13], v[12:13], v[70:71] op_sel_hi:[1,0]\n\t" \
    "v_pk_mul_f32 v[44:45], v[44:45], v[68:69] op_sel_hi:[1,0]\n\t" \
    "v_add_f32 v76, v76, v78\n\t" \
    "v_pk_mul_f32 v[14:15], v[14:15], v[70:71] op_sel_hi:[1,0]\n\t" \
    "v_pk_mul_f32 v[46:47], v[46:47], v[68:69] op_sel_hi:[1,0]\n\t" \
    "v_permlane16_swap_b32 v72, v76\n\t" \
    "v_cvt_pk_bf16_f32 v15, v14, v15\n\t" \
    "v_cvt_pk_bf16_f32 v14, v12, v13\n\t" \
    "v_add_f32 v74, v72, v76\n\t" \
    "v_cvt_pk_bf16_f32 v13, v10, v11\n\t" \
    "ds_write_b64 v253, v[14:15] offset:2048\n\t" \
    "v_rcp_f32 v72, v74\n\t" \
    "v_cvt_pk_bf16_f32 v12, v8, v9\n\t" \
    "v_cvt_pk_bf16_f32 v8, v40, v41\n\t" \
    "v_mov_b32 v76, v72\n\t" \
    "v_cvt_pk_bf16_f32 v9, v42, v43\n\t" \
    "ds_write_b64 v252, v[12:13] offset:2048\n\t" \
    "v_permlane16_swap_b32 v72, v76\n\t" \
    "v_cvt_pk_bf16_f32 v10, v44, v45\n\t" \
    "v_cvt_pk_bf16_f32 v11, v46, v47\n\t" \
    "v_mov_b32 v74, v72\n\t" \
    "v_mov_b32 v78, v76\n\t" \
    "ds_write_b64 v243, v[8:9] offset:2048\n\t" \
    "v_permlane32_swap_b32 v72, v74\n\t" \
    "v_permlane32_swap_b32 v76, v78\n\t" \
    "ds_write_b64 v251, v[10:11] offset:2048\n\t" \
    "v_mov_b32 v237, 3\n\t" \
    "v_pk_mul_f32 v[16:17], v[16:17], v[74:75] op_sel_hi:[1,0]\n\t" \
    "ds_write_b32 v248, v237\n\t" \
    "v_pk_mul_f32 v[48:49], v[48:49], v[72:73] op_sel_hi:[1,0]\n\t" \
    "ds_read_b64_tr_b16 v[96:97], v244\n\t" \
    "v_pk_mul_f32 v[18:19], v[18:19], v[74:75] op_sel_hi:[1,0]\n\t" \
    "ds_read_b64_tr_b16 v[98:99], v244 offset:2048\n\t" \
    "v_pk_mul_f32 v[50:51], v[50:51], v[72:73] op_sel_hi:[1,0]\n\t" \
    "ds_read_b64_tr_b16 v[100:101], v254\n\t" \
    "v_pk_mul_f32 v[20:21], v[20:21], v[74:75] op_sel_hi:[1,0]\n\t" \
    "ds_read_b64_tr_b16 v[102:103], v254 offset:2048\n\t" \
    "v_pk_mul_f32 v[52:53], v[52:53], v[72:73] op_sel_hi:[1,0]\n\t" \
    "v_pk_mul_f32 v[22:23], v[22:23], v[74:75] op_sel_hi:[1,0]\n\t" \
    "v_pk_mul_f32 v[54:55], v[54:55], v[72:73] op_sel_hi:[1,0]\n\t" \
    "v_pk_mul_f32 v[24:25], v[24:25], v[78:79] op_sel_hi:[1,0]\n\t" \
    "v_cvt_pk_bf16_f32 v23, v22, v23\n\t" \
    "v_cvt_pk_bf16_f32 v22, v20, v21\n\t" \
    "v_cvt_pk_bf16_f32 v21, v18, v19\n\t" \
    "v_cvt_pk_bf16_f32 v20, v16, v17\n\t" \
    "ds_write_b64 v253, v[22:23] offset:4096\n\t" \
    "v_cvt_pk_bf16_f32 v16, v48, v49\n\t" \
    "v_cvt_pk_bf16_f32 v17, v50, v51\n\t" \
    "v_cvt_pk_bf16_f32 v18, v52, v53\n\t" \
    "v_cvt_pk_bf16_f32 v19, v54, v55\n\t" \
    "v_pk_mul_f32 v[56:57], v[56:57], v[76:77] op_sel_hi:[1,0]\n\t" \
    "ds_write_b64 v243, v[16:17] offset:4096\n\t" \
    "v_pk_mul_f32 v[26:27], v[26:27], v[78:79] op_sel_hi:[1,0]\n\t" \
    "v_pk_mul_f32 v[58:59], v[58:59], v[76:77] op_sel_hi:[1,0]\n\t" \
    "v_pk_mul_f32 v[28:29], v[28:29], v[78:79] op_sel_hi:[1,0]\n\t" \
    "v_pk_mul_f32 v[60:61], v[60:61], v[76:77] op_sel_hi:[1,0]\n\t" \
    "v_pk_mul_f32 v[30:31], v[30:31], v[78:79] op_sel_hi:[1,0]\n\t" \
    "ds_write_b64 v251, v[18:19] offset:4096\n\t" \
    "v_pk_mul_f32 v[62:63], v[62:63], v[76:77] op_sel_hi:[1,0]\n\t" \
    "v_cvt_pk_bf16_f32 v31, v30, v31\n\t" \
    "v_cvt_pk_bf16_f32 v30, v28, v29\n\t" \
    "v_cvt_pk_bf16_f32 v29, v26, v27\n\t" \
    "v_cvt_pk_bf16_f32 v28, v24, v25\n\t" \
    "ds_write_b64 v252, v[20:21] offset:4096\n\t" \
    "ds_read_b32 v238, v250\n\t" \
    "v_cvt_pk_bf16_f32 v24, v56, v57\n\t" \
    "ds_read_b64_tr_b16 v[104:105], v245\n\t" \
    "v_cvt_pk_bf16_f32 v25, v58, v59\n\t" \
    "ds_read_b64_tr_b16 v[106:107], v245 offset:2048\n\t" \
    "v_cvt_pk_bf16_f32 v26, v60, v61\n\t" \
    "ds_read_b64_tr_b16 v[108:109], v255\n\t" \
    "v_cvt_pk_bf16_f32 v27, v62, v63\n\t" \
    "ds_read_b64_tr_b16 v[110:111], v255 offset:2048\n\t" \
    "ds_write_b64 v243, v[24:25] offset:6144\n\t" \
    "ds_read_b128 v[80:83], v242\n\t" \
    "ds_write_b64 v251, v[26:27] offset:6144\n\t" \
    "ds_read_b128 v[84:87], v242 offset:1024\n\t" \
    "ds_write_b64 v252, v[28:29] offset:6144\n\t" \
    "ds_read_b128 v[88:91], v242 offset:4096\n\t" \
    "ds_write_b64 v253, v[30:31] offset:6144\n\t" \
    "ds_read_b128 v[92:95], v242 offset:5120\n\t" \
    "v_mov_b32 v237, 4\n\t" \
    "ds_write_b32 v248, v237\n\t" \
    "s_waitcnt lgkmcnt(14)\n\t" \
    "v_mfma_f32_16x16x32_bf16 v[64:67], v[96:99], v[232:235], 0\n\t" \
    "ds_read_b64_tr_b16 v[0:1], v244 offset:4096\n\t" \
    "ds_read_b64_tr_b16 v[2:3], v244 offset:6144\n\t" \
    "v_mfma_f32_16x16x32_bf16 v[68:71], v[100:103], v[232:235], 0\n\t" \
    "ds_read_b64_tr_b16 v[4:5], v254 offset:4096\n\t" \
    "ds_read_b64_tr_b16 v[6:7], v254 offset:6144\n\t" \
    "v_mfma_f32_16x16x32_bf16 v[32:35], v[96:99], v[112:115], 0\n\t" \
    "v_mfma_f32_16x16x32_bf16 v[40:43], v[100:103], v[112:115], 0\n\t" \
    "s_waitcnt lgkmcnt(14)\n\t" \
    "v_readfirstlane_b32 s85, v238\n\t" \
    "s_cmp_lt_u32 s85, 3\n\t" \
    "s_cbranch_scc1 .Lab_s3\n\t" \
    ".Lab_s3r:\n\t" \
    "v_mfma_f32_16x16x32_bf16 v[36:39], v[96:99], v[116:119], 0\n\t" \
    "v_mfma_f32_16x16x32_bf16 v[44:47], v[100:103], v[116:119], 0\n\t" \
    "ds_read_b32 v238, v250\n\t" \
    "ds_read_b64_tr_b16 v[8:9], v245 offset:4096\n\t" \
    "v_mfma_f32_16x16x32_bf16 v[64:67], v[104:107], v[232:235], v[64:67]\n\t" \
    "ds_read_b64_tr_b16 v[10:11], v245 offset:6144\n\t" \
    "ds_read_b64_tr_b16 v[12:13], v255 offset:4096\n\t" \
    "s_waitcnt lgkmcnt(14)\n\t" \
    "v_mfma_f32_16x16x32_bf16 v[68:71], v[108:111], v[232:235], v[68:71]\n\t" \
    "ds_read_b64_tr_b16 v[14:15], v255 offset:6144\n\t" \
    "v_mfma_f32_16x16x32_bf16 v[32:35], v[104:107], v[80:83], v[32:35]\n\t" \
    "v_mfma_f32_16x16x32_bf16 v[40:43], v[108:111], v[80:83], v[40:43]\n\t" \
    "s_waitcnt lgkmcnt(14)\n\t" \
    "v_mfma_f32_16x16x32_bf16 v[36:39], v[104:107], v[84:87], v[36:39]\n\t" \
    "v_add_u32 v253, 135168, v241\n\t" \
    "v_mfma_f32_16x16x32_bf16 v[44:47], v[108:111], v[84:87], v[44:47]\n\t" \
    "v_xor_b32 v251, 32, v253\n\t" \
    "s_waitcnt lgkmcnt(7)\n\t" \
    "v_mfma_f32_16x16x32_bf16 v[64:67], v[0:3], v[232:235], v[64:67]\n\t" \
    "v_mov_b32 v237, 5\n\t" \
    "s_waitcnt lgkmcnt(5)\n\t" \
    "v_mfma_f32_16x16x32_bf16 v[68:71], v[4:7], v[232:235], v[68:71]\n\t" \
    "v_mfma_f32_16x16x32_bf16 v[32:35], v[0:3], v[120:123], v[32:35]\n\t" \
    "v_mfma_f32_16x16x32_bf16 v[40:43], v[4:7], v[120:123], v[40:43]\n\t" \
    "v_mfma_f32_16x16x32_bf16 v[36:39], v[0:3], v[124:127], v[36:39]\n\t" \
    "s_waitcnt lgkmcnt(4)\n\t" \
    "v_readfirstlane_b32 s85, v238\n\t" \
    "s_cmp_lt_u32 s85, 4\n\t" \
    "s_cbranch_scc1 .Lab_s4\n\t" \
    ".Lab_s4r:\n\t" \
    "v_mfma_f32_16x16x32_bf16 v[44:47], v[4:7], v[124:127], v[44:47]\n\t" \
    "ds_write_b32 v248, v237\n\t" \
    "s_waitcnt lgkmcnt(3)\n\t" \
    "v_mfma_f32_16x16x32_bf16 v[64:67], v[8:11], v[232:235], v[64:67]\n\t" \
    "ds_read_b32 v238, v249 offset:92\n\t" \
    "s_waitcnt lgkmcnt(2)\n\t" \
    "v_mfma_f32_16x16x32_bf16 v[68:71], v[12:15], v[232:235], v[68:71]\n\t" \
    "v_mfma_f32_16x16x32_bf16 v[32:35], v[8:11], v[88:91], v[32:35]\n\t" \
    "v_mfma_f32_16x16x32_bf16 v[40:43], v[12:15], v[88:91], v[40:43]\n\t" \
    "v_mfma_f32_16x16x32_bf16 v[36:39], v[8:11], v[92:95], v[36:39]\n\t" \
    "v_mfma_f32_16x16x32_bf16 v[44:47], v[12:15], v[92:95], v[44:47]\n\t" \
    "ds_read_b64_tr_b16 v[80:81], v253\n\t" \
    "ds_read_b64_tr_b16 v[82:83], v253 offset:512\n\t" \
    "ds_read_b64_tr_b16 v[84:85], v251\n\t" \
    "s_nop 0\n\t" \
    "v_cndmask_b32 v24, v68, v64, vcc\n\t" \
    "v_cndmask_b32 v25, v69, v65, vcc\n\t" \
    "v_cndmask_b32 v26, v70, v66, vcc\n\t" \
    "v_cndmask_b32 v27, v71, v67, vcc\n\t" \
    "v_add3_u32 v30, v24, v25, v26\n\t" \
    "v_add_u32 v30, v30, v27\n\t" \
    "v_cvt_pk_bf16_f32 v16, v32, v33\n\t" \
    "v_cvt_pk_bf16_f32 v17, v34, v35\n\t" \
    "v_cvt_pk_bf16_f32 v18, v40, v41\n\t" \
    "v_cvt_pk_bf16_f32 v19, v42, v43\n\t" \
    "v_cvt_pk_bf16_f32 v20, v36, v37\n\t" \
    "v_cvt_pk_bf16_f32 v21, v38, v39\n\t" \
    "v_cvt_pk_bf16_f32 v22, v44, v45\n\t" \
    "v_cvt_pk_bf16_f32 v23, v46, v47\n\t" \
    "v_add3_u32 v28, v16, v17, v18\n\t" \
    "v_add3_u32 v29, v20, v21, v22\n\t" \
    "v_add_u32 v28, v28, v19\n\t" \
    "v_add_u32 v29, v29, v23\n\t" \
    "v_cndmask_b32_e64 v30, 0, v30, s[94:95]\n\t" \
    "buffer_store_dwordx4 v[16:19], v246, %[rsb], s87 offen sc1\n\t" \
    "v_permlane16_swap_b32 v28, v29\n\t" \
    "v_add3_u32 v28, v28, v29, v30\n\t" \
    "buffer_store_dwordx4 v[20:23], v246, %[rsb], s86 offen sc1\n\t" \
    "v_cndmask_b32_e64 v25, v25, v236, s[92:93]\n\t" \
    "v_add_u32_dpp v28, v28, v28 row_ror:8 row_mask:0xf bank_mask:0xf bound_ctrl:1\n\t" \
    "v_mov_b32 v237, 6\n\t" \
    "ds_read_b64_tr_b16 v[86:87], v251 offset:512\n\t" \
    "v_add_u32_dpp v28, v28, v28 row_ror:4 row_mask:0xf bank_mask:0xf bound_ctrl:1\n\t" \
    "ds_read_b64_tr_b16 v[88:89], v253 offset:4096\n\t" \
    "ds_read_b64_tr_b16 v[90:91], v253 offset:4608\n\t" \
    "v_add_u32_dpp v28, v28, v28 row_ror:2 row_mask:0xf bank_mask:0xf bound_ctrl:1\n\t" \
    "ds_read_b64_tr_b16 v[92:93], v251 offset:4096\n\t" \
    "ds_read_b64_tr_b16 v[94:95], v251 offset:4608\n\t" \
    "v_add_u32_dpp v28, v28, v28 row_ror:1 row_mask:0xf bank_mask:0xf bound_ctrl:1\n\t" \
    "v_cndmask_b32_e64 v24, v24, v28, s[92:93]\n\t" \
    "buffer_store_dwordx4 v[24:27], v247, %[rsb], s88 offen sc1\n\t" \
    "ds_write_b32 v248, v237\n\t" \
    "s_setprio 1\n\t" \
    "s_waitcnt lgkmcnt(9)\n\t" \
    "v_readfirstlane_b32 s85, v238\n\t" \
    "s_cmp_lt_u32 s85, 1\n\t" \
    "s_cbranch_scc1 .Lab_s5\n\t" \
    ".Lab_s5r:\n\t" \
    "s_waitcnt lgkmcnt(7)\n\t" \
    "v_mfma_f32_16x16x32_bf16 v[192:195], v[80:83], v[128:131], v[192:195]\n\t" \
    "v_add_u32 v244, 49152, v241\n\t" \
    "v_xor_b32 v245, 32, v244\n\t" \
    "v_mfma_f32_16x16x32_bf16 v[200:203], v[80:83], v[132:135], v[200:203]\n\t" \
    "ds_read_b64_tr_b16 v[96:97], v244\n\t" \
    "ds_read_b64_tr_b16 v[98:99], v244 offset:512\n\t" \
    "v_mfma_f32_16x16x32_bf16 v[208:211], v[80:83], v[136:139], v[208:211]\n\t" \
    "ds_read_b64_tr_b16 v[100:101], v245\n\t" \
    "ds_read_b64_tr_b16 v[102:103], v245 offset:512\n\t" \
    "v_mfma_f32_16x16x32_bf16 v[216:219], v[80:83], v[140:143], v[216:219]\n\t" \
    "ds_read_b64_tr_b16 v[104:105], v244 offset:4096\n\t" \
    "ds_read_b64_tr_b16 v[106:107], v244 offset:4608\n\t" \
    "s_waitcnt lgkmcnt(11)\n\t" \
    "v_mfma_f32_16x16x32_bf16 v[196:199], v[84:87], v[128:131], v[196:199]\n\t" \
    "ds_read_b64_tr_b16 v[108:109], v245 offset:4096\n\t" \
    "ds_read_b64_tr_b16 v[110:111], v245 offset:4608\n\t" \
    "v_mfma_f32_16x16x32_bf16 v[204:207], v[84:87], v[132:135], v[204:207]\n\t" \
    "v_xor_b32 v246, 64, v240\n\t" \
    "v_subrev_u32 v242, 16384, v241\n\t" \
    "v_mfma_f32_16x16x32_bf16 v[212:215], v[84:87], v[136:139], v[212:215]\n\t" \
    "v_xor_b32 v243, 32, v242\n\t" \
    "v_add_u32 v247, 65536, v240\n\t" \
    "v_mfma_f32_16x16x32_bf16 v[220:223], v[84:87], v[140:143], v[220:223]\n\t" \
    "v_mov_b32 v237, 7\n\t" \
    "ds_write_b32 v248, v237\n\t" \
    "s_waitcnt lgkmcnt(12)\n\t" \
    "v_mfma_f32_16x16x32_bf16 v[192:195], v[88:91], v[144:147], v[192:195]\n\t" \
    "ds_read_b64_tr_b16 v[80:81], v244 offset:8192\n\t" \
    "v_mfma_f32_16x16x32_bf16 v[200:203], v[88:91], v[148:151], v[200:203]\n\t" \
    "ds_read_b128 v[128:131], v240 offset:40960\n\t" \
    "v_mfma_f32_16x16x32_bf16 v[208:211], v[88:91], v[152:155], v[208:211]\n\t" \
    "ds_read_b64_tr_b16 v[82:83], v244 offset:8704\n\t" \
    "v_add_u32 v237, 1, v237\n\t" \
    "v_mfma_f32_16x16x32_bf16 v[216:219], v[88:91], v[156:159], v[216:219]\n\t" \
    "ds_read_b128 v[132:135], v240 offset:43008\n\t" \
    "s_waitcnt lgkmcnt(14)\n\t" \
    "v_mfma_f32_16x16x32_bf16 v[196:199], v[92:95], v[144:147], v[196:199]\n\t" \
    "ds_read_b64_tr_b16 v[84:85], v245 offset:8192\n\t" \
    "v_xor_b32 v255, 64, v247\n\t" \
    "v_mfma_f32_16x16x32_bf16 v[204:207], v[92:95], v[148:151], v[204:207]\n\t" \
    "ds_read_b128 v[136:139], v240 offset:45056\n\t" \
    "v_mfma_f32_16x16x32_bf16 v[212:215], v[92:95], v[152:155], v[212:215]\n\t" \
    "ds_read_b64_tr_b16 v[86:87], v245 offset:8704\n\t" \
    "v_mfma_f32_16x16x32_bf16 v[220:223], v[92:95], v[156:159], v[220:223]\n\t" \
    "ds_read_b128 v[140:143], v240 offset:47104\n\t" \
    "s_waitcnt lgkmcnt(14)\n\t" \
    "v_mfma_f32_16x16x32_bf16 v[192:195], v[96:99], v[160:163], v[192:195]\n\t" \
    "ds_read_b64_tr_b16 v[88:89], v244 offset:12288\n\t" \
    "v_mfma_f32_16x16x32_bf16 v[200:203], v[96:99], v[164:167], v[200:203]\n\t" \
    "ds_read_b128 v[144:147], v246 offset:40960\n\t" \
    "v_mfma_f32_16x16x32_bf16 v[208:211], v[96:99], v[168:171], v[208:211]\n\t" \
    "ds_read_b64_tr_b16 v[90:91], v244 offset:12800\n\t" \
    "v_mfma_f32_16x16x32_bf16 v[216:219], v[96:99], v[172:175], v[216:219]\n\t" \
    "ds_read_b128 v[148:151], v246 offset:43008\n\t" \
    "s_waitcnt lgkmcnt(14)\n\t" \
    "v_mfma_f32_16x16x32_bf16 v[196:199], v[100:103], v[160:163], v[196:199]\n\t" \
    "ds_read_b64_tr_b16 v[92:93], v245 offset:12288\n\t" \
    "v_mfma_f32_16x16x32_bf16 v[204:207], v[100:103], v[164:167], v[204:207]\n\t" \
    "ds_read_b128 v[152:155], v246 offset:45056\n\t" \
    "v_mfma_f32_16x16x32_bf16 v[212:215], v[100:103], v[168:171], v[212:215]\n\t" \
    "ds_read_b64_tr_b16 v[94:95], v245 offset:12800\n\t" \
    "v_mfma_f32_16x16x32_bf16 v[220:223], v[100:103], v[172:175], v[220:223]\n\t" \
    "ds_read_b128 v[156:159], v246 offset:47104\n\t" \
    "ds_write_b32 v248, v237\n\t" \
    "v_mfma_f32_16x16x32_bf16 v[192:195], v[104:107], v[176:179], v[192:195]\n\t" \
    "ds_read_b64_tr_b16 v[96:97], v242 offset:24576\n\t" \
    "v_mfma_f32_16x16x32_bf16 v[200:203], v[104:107], v[180:183], v[200:203]\n\t" \
    "ds_read_b128 v[160:163], v247 offset:16384\n\t" \
    "v_mfma_f32_16x16x32_bf16 v[208:211], v[104:107], v[184:187], v[208:211]\n\t" \
    "ds_read_b64_tr_b16 v[98:99], v242 offset:25088\n\t" \
    "v_add_u32 v237, 1, v237\n\t" \
    "v_mfma_f32_16x16x32_bf16 v[216:219], v[104:107], v[188:191], v[216:219]\n\t" \
    "ds_read_b128 v[164:167], v247 offset:18432\n\t" \
    "s_waitcnt lgkmcnt(14)\n\t" \
    "v_mfma_f32_16x16x32_bf16 v[196:199], v[108:111], v[176:179], v[196:199]\n\t" \
    "ds_read_b64_tr_b16 v[100:101], v243 offset:24576\n\t" \
    "v_mfma_f32_16x16x32_bf16 v[204:207], v[108:111], v[180:183], v[204:207]\n\t" \
    "ds_read_b128 v[168:171], v247 offset:20480\n\t" \
    "v_mfma_f32_16x16x32_bf16 v[212:215], v[108:111], v[184:187], v[212:215]\n\t" \
    "ds_read_b64_tr_b16 v[102:103], v243 offset:25088\n\t" \
    "v_mfma_f32_16x16x32_bf16 v[220:223], v[108:111], v[188:191], v[220:223]\n\t" \
    "ds_read_b128 v[172:175], v247 offset:22528\n\t" \
    "v_mfma_f32_16x16x32_bf16 v[192:195], v[80:83], v[128:131], v[192:195]\n\t" \
    "ds_read_b64_tr_b16 v[104:105], v242 offset:28672\n\t" \
    "v_mfma_f32_16x16x32_bf16 v[200:203], v[80:83], v[132:135], v[200:203]\n\t" \
    "ds_read_b128 v[176:179], v255 offset:16384\n\t" \
    "v_mfma_f32_16x16x32_bf16 v[208:211], v[80:83], v[136:139], v[208:211]\n\t" \
    "ds_read_b64_tr_b16 v[106:107], v242 offset:29184\n\t" \
    "s_waitcnt lgkmcnt(14)\n\t" \
    "v_mfma_f32_16x16x32_bf16 v[216:219], v[80:83], v[140:143], v[216:219]\n\t" \
    "ds_read_b128 v[180:183], v255 offset:18432\n\t" \
    "v_mfma_f32_16x16x32_bf16 v[196:199], v[84:87], v[128:131], v[196:199]\n\t" \
    "ds_read_b64_tr_b16 v[108:109], v243 offset:28672\n\t" \
    "v_mfma_f32_16x16x32_bf16 v[204:207], v[84:87], v[132:135], v[204:207]\n\t" \
    "ds_read_b128 v[184:187], v255 offset:20480\n\t" \
    "v_mfma_f32_16x16x32_bf16 v[212:215], v[84:87], v[136:139], v[212:215]\n\t" \
    "ds_read_b64_tr_b16 v[110:111], v243 offset:29184\n\t" \
    "v_mfma_f32_16x16x32_bf16 v[220:223], v[84:87], v[140:143], v[220:223]\n\t" \
    "ds_read_b128 v[188:191], v255 offset:22528\n\t" \
    "ds_write_b32 v248, v237\n\t" \
    "v_mfma_f32_16x16x32_bf16 v[192:195], v[88:91], v[144:147], v[192:195]\n\t" \
    "ds_read_b64_tr_b16 v[80:81], v242 offset:32768\n\t" \
    "v_mfma_f32_16x16x32_bf16 v[200:203], v[88:91], v[148:151], v[200:203]\n\t" \
    "ds_read_b128 v[128:131], v240\n\t" \
    "v_mfma_f32_16x16x32_bf16 v[208:211], v[88:91], v[152:155], v[208:211]\n\t" \
    "ds_read_b64_tr_b16 v[82:83], v242 offset:33280\n\t" \
    "v_add_u32 v237, 1, v237\n\t" \
    "s_waitcnt lgkmcnt(14)\n\t" \
    "v_mfma_f32_16x16x32_bf16 v[216:219], v[88:91], v[156:159], v[216:219]\n\t" \
    "ds_read_b128 v[132:135], v240 offset:2048\n\t" \
    "v_mfma_f32_16x16x32_bf16 v[196:199], v[92:95], v[144:147], v[196:199]\n\t" \
    "ds_read_b64_tr_b16 v[84:85], v243 offset:32768\n\t" \
    "v_mfma_f32_16x16x32_bf16 v[204:207], v[92:95], v[148:151], v[204:207]\n\t" \
    "ds_read_b128 v[136:139], v240 offset:4096\n\t" \
    "v_mfma_f32_16x16x32_bf16 v[212:215], v[92:95], v[152:155], v[212:215]\n\t" \
    "ds_read_b64_tr_b16 v[86:87], v243 offset:33280\n\t" \
    "ds_read_b32 v238, v249 offset:92\n\t" \
    "v_mfma_f32_16x16x32_bf16 v[220:223], v[92:95], v[156:159], v[220:223]\n\t" \
    "ds_read_b128 v[140:143], v240 offset:6144\n\t" \
    "v_mfma_f32_16x16x32_bf16 v[192:195], v[96:99], v[160:163], v[192:195]\n\t" \
    "ds_read_b64_tr_b16 v[88:89], v242 offset:36864\n\t" \
    "v_add_u32 v247, %[xs0], v240\n\t" \
    "v_mfma_f32_16x16x32_bf16 v[200:203], v[96:99], v[164:167], v[200:203]\n\t" \
    "ds_read_b128 v[144:147], v246\n\t" \
    "v_mfma_f32_16x16x32_bf16 v[208:211], v[96:99], v[168:171], v[208:211]\n\t" \
    "ds_read_b64_tr_b16 v[90:91], v242 offset:37376\n\t" \
    "v_xor_b32 v255, 64, v247\n\t" \
    "s_waitcnt lgkmcnt(14)\n\t" \
    "v_mfma_f32_16x16x32_bf16 v[216:219], v[96:99], v[172:175], v[216:219]\n\t" \
    "ds_read_b128 v[148:151], v246 offset:2048\n\t" \
    "v_mfma_f32_16x16x32_bf16 v[196:199], v[100:103], v[160:163], v[196:199]\n\t" \
    "ds_read_b64_tr_b16 v[92:93], v243 offset:36864\n\t" \
    "v_mfma_f32_16x16x32_bf16 v[204:207], v[100:103], v[164:167], v[204:207]\n\t" \
    "ds_read_b128 v[152:155], v246 offset:4096\n\t" \
    "v_mfma_f32_16x16x32_bf16 v[212:215], v[100:103], v[168:171], v[212:215]\n\t" \
    "ds_read_b64_tr_b16 v[94:95], v243 offset:37376\n\t" \
    "v_mfma_f32_16x16x32_bf16 v[220:223], v[100:103], v[172:175], v[220:223]\n\t" \
    "ds_read_b128 v[156:159], v246 offset:6144\n\t" \
    "ds_write_b32 v248, v237\n\t" \
    "v_mfma_f32_16x16x32_bf16 v[192:195], v[104:107], v[176:179], v[192:195]\n\t" \
    "s_waitcnt lgkmcnt(10)\n\t" \
    "v_readfirstlane_b32 s85, v238\n\t" \
    "s_cmp_lt_u32 s85, 2\n\t" \
    "s_cbranch_scc1 .Lab_s6\n\t" \
    ".Lab_s6r:\n\t" \
    "ds_read_b64_tr_b16 v[96:97], v244 offset:32768\n\t" \
    "v_mfma_f32_16x16x32_bf16 v[200:203], v[104:107], v[180:183], v[200:203]\n\t" \
    "ds_read_b128 v[160:163], v247\n\t" \
    "v_mfma_f32_16x16x32_bf16 v[208:211], v[104:107], v[184:187], v[208:211]\n\t" \
    "ds_read_b64_tr_b16 v[98:99], v244 offset:33280\n\t" \
    "v_add_u32 v237, 1, v237\n\t" \
    "v_mfma_f32_16x16x32_bf16 v[216:219], v[104:107], v[188:191], v[216:219]\n\t" \
    "ds_read_b128 v[164:167], v247 offset:2048\n\t" \
    "v_mfma_f32_16x16x32_bf16 v[196:199], v[108:111], v[176:179], v[196:199]\n\t" \
    "ds_read_b64_tr_b16 v[100:101], v245 offset:32768\n\t" \
    "v_mfma_f32_16x16x32_bf16 v[204:207], v[108:111], v[180:183], v[204:207]\n\t" \
    "ds_read_b128 v[168:171], v247 offset:4096\n\t" \
    "v_mfma_f32_16x16x32_bf16 v[212:215], v[108:111], v[184:187], v[212:215]\n\t" \
    "ds_read_b64_tr_b16 v[102:103], v245 offset:33280\n\t" \
    "v_mfma_f32_16x16x32_bf16 v[220:223], v[108:111], v[188:191], v[220:223]\n\t" \
    "ds_read_b128 v[172:175], v247 offset:6144\n\t" \
    "v_mfma_f32_16x16x32_bf16 v[192:195], v[80:83], v[128:131], v[192:195]\n\t" \
    "ds_read_b64_tr_b16 v[104:105], v244 offset:36864\n\t" \
    "v_mfma_f32_16x16x32_bf16 v[200:203], v[80:83], v[132:135], v[200:203]\n\t" \
    "ds_read_b128 v[176:179], v255\n\t" \
    "v_mfma_f32_16x16x32_bf16 v[208:211], v[80:83], v[136:139], v[208:211]\n\t" \
    "ds_read_b64_tr_b16 v[106:107], v244 offset:37376\n\t" \
    "s_waitcnt lgkmcnt(14)\n\t" \
    "v_mfma_f32_16x16x32_bf16 v[216:219], v[80:83], v[140:143], v[216:219]\n\t" \
    "ds_read_b128 v[180:183], v255 offset:2048\n\t" \
    "v_mfma_f32_16x16x32_bf16 v[196:199], v[84:87], v[128:131], v[196:199]\n\t" \
    "ds_read_b64_tr_b16 v[108:109], v245 offset:36864\n\t" \
    "v_mfma_f32_16x16x32_bf16 v[204:207], v[84:87], v[132:135], v[204:207]\n\t" \
    "ds_read_b128 v[184:187], v255 offset:4096\n\t" \
    "v_mfma_f32_16x16x32_bf16 v[212:215], v[84:87], v[136:139], v[212:215]\n\t" \
    "ds_read_b64_tr_b16 v[110:111], v245 offset:37376\n\t" \
    "v_mfma_f32_16x16x32_bf16 v[220:223], v[84:87], v[140:143], v[220:223]\n\t" \
    "ds_read_b128 v[188:191], v255 offset:6144\n\t" \
    "ds_write_b32 v248, v237\n\t" \
    "v_mfma_f32_16x16x32_bf16 v[192:195], v[88:91], v[144:147], v[192:195]\n\t" \
    "v_mbcnt_lo_u32_b32 v250, -1, 0\n\t" \
    "v_mbcnt_hi_u32_b32 v250, -1, v250\n\t" \
    "v_mfma_f32_16x16x32_bf16 v[200:203], v[88:91], v[148:151], v[200:203]\n\t" \
    "v_and_b32 v238, 15, v250\n\t" \
    "v_lshlrev_b32 v238, 3, v238\n\t" \
    "v_mfma_f32_16x16x32_bf16 v[208:211], v[88:91], v[152:155], v[208:211]\n\t" \
    "v_add_u32 v237, 1, v237\n\t" \
    "s_waitcnt lgkmcnt(14)\n\t" \
    "v_mfma_f32_16x16x32_bf16 v[216:219], v[88:91], v[156:159], v[216:219]\n\t" \
    "buffer_load_dwordx2 v[232:233], v238, %[rsw], %[sg] offen sc1\n\t" \
    "v_mfma_f32_16x16x32_bf16 v[196:199], v[92:95], v[144:147], v[196:199]\n\t" \
    "v_lshlrev_b32 v251, 4, v250\n\t" \
    "s_add_u32 s85, %[sof], %[ofc]\n\t" \
    "v_mfma_f32_16x16x32_bf16 v[204:207], v[92:95], v[148:151], v[204:207]\n\t" \
    "v_add_u32 v240, s85, v251\n\t" \
    "s_add_u32 s87, %[sof], %[ofp]\n\t" \
    "v_mfma_f32_16x16x32_bf16 v[212:215], v[92:95], v[152:155], v[212:215]\n\t" \
    "buffer_load_dwordx2 v[234:235], v238, %[rsw], %[sg] offen offset:128 sc1\n\t" \
    "v_mfma_f32_16x16x32_bf16 v[220:223], v[92:95], v[156:159], v[220:223]\n\t" \
    "v_add_u32 v241, 0x1000, v240\n\t" \
    "s_add_u32 s88, %[sof], %[ogp]\n\t" \
    "v_mfma_f32_16x16x32_bf16 v[192:195], v[96:99], v[160:163], v[192:195]\n\t" \
    "s_mov_b32 s86, 0x10000\n\t" \
    "v_add_u32 v242, 0x2000, v240\n\t" \
    "v_add_u32 v243, 0x3000, v240\n\t" \
    "s_waitcnt lgkmcnt(10)\n\t" \
    "v_mfma_f32_16x16x32_bf16 v[196:199], v[100:103], v[160:163], v[196:199]\n\t" \
    "v_bfe_u32 v244, v251, 1, 8\n\t" \
    "v_add_u32 v253, %[kac], v251\n\t" \
    "s_waitcnt lgkmcnt(6)\n\t" \
    "v_mfma_f32_16x16x32_bf16 v[192:195], v[104:107], v[176:179], v[192:195]\n\t" \
    "v_lshrrev_b32 v112, 2, v250\n\t" \
    "v_bfe_u32 v113, v250, 4, 1\n\t" \
    "s_waitcnt lgkmcnt(2)\n\t" \
    "v_mfma_f32_16x16x32_bf16 v[196:199], v[108:111], v[176:179], v[196:199]\n\t" \
    "v_lshlrev_b32 v245, 7, v112\n\t" \
    "v_bitop3_b32 v112, v112, v113, 3 bitop3:0x28\n\t" \
    "v_mfma_f32_16x16x32_bf16 v[200:203], v[96:99], v[164:167], v[200:203]\n\t" \
    "v_lshl_or_b32 v245, v112, 5, v245\n\t" \
    "v_and_b32 v112, 24, v238\n\t" \
    "v_mfma_f32_16x16x32_bf16 v[204:207], v[100:103], v[164:167], v[204:207]\n\t" \
    "v_or3_b32 v245, v245, v112, s86\n\t" \
    "v_lshlrev_b32 v247, 8, v238\n\t" \
    "v_mfma_f32_16x16x32_bf16 v[200:203], v[104:107], v[180:183], v[200:203]\n\t" \
    "v_lshl_or_b32 v247, v113, 5, v247\n\t" \
    "v_lshrrev_b32 v112, 5, v250\n\t" \
    "v_mfma_f32_16x16x32_bf16 v[204:207], v[108:111], v[180:183], v[204:207]\n\t" \
    "v_lshl_or_b32 v247, v112, 4, v247\n\t" \
    "v_and_b32 v252, 48, v247\n\t" \
    "v_mfma_f32_16x16x32_bf16 v[208:211], v[96:99], v[168:171], v[208:211]\n\t" \
    "v_lshl_add_u32 v252, v252, 2, %[zac]\n\t" \
    "v_lshlrev_b32 v246, 4, v238\n\t" \
    "v_mfma_f32_16x16x32_bf16 v[212:215], v[100:103], v[168:171], v[212:215]\n\t" \
    "v_cvt_pk_bf16_f32 v64, v192, v193\n\t" \
    "v_cvt_pk_bf16_f32 v65, v194, v195\n\t" \
    "v_mfma_f32_16x16x32_bf16 v[208:211], v[104:107], v[184:187], v[208:211]\n\t" \
    "v_cvt_pk_bf16_f32 v66, v196, v197\n\t" \
    "v_cvt_pk_bf16_f32 v67, v198, v199\n\t" \
    "v_mfma_f32_16x16x32_bf16 v[212:215], v[108:111], v[184:187], v[212:215]\n\t" \
    "v_lshl_or_b32 v246, v113, 3, v246\n\t" \
    "v_lshrrev_b32 v113, 4, v238\n\t" \
    "v_mfma_f32_16x16x32_bf16 v[216:219], v[96:99], v[172:175], v[216:219]\n\t" \
    "v_cvt_pk_bf16_f32 v68, v200, v201\n\t" \
    "v_cvt_pk_bf16_f32 v69, v202, v203\n\t" \
    "v_mfma_f32_16x16x32_bf16 v[220:223], v[100:103], v[172:175], v[220:223]\n\t" \
    "v_cvt_pk_bf16_f32 v70, v204, v205\n\t" \
    "v_cvt_pk_bf16_f32 v71, v206, v207\n\t" \
    "s_waitcnt lgkmcnt(1)\n\t" \
    "v_mfma_f32_16x16x32_bf16 v[216:219], v[104:107], v[188:191], v[216:219]\n\t" \
    "v_xor_b32 v112, v112, v113\n\t" \
    "v_lshl_or_b32 v246, v112, 4, v246\n\t" \
    "v_mfma_f32_16x16x32_bf16 v[220:223], v[108:111], v[188:191], v[220:223]\n\t" \
    "v_add_u32 v246, %[xec], v246\n\t" \
    "v_mfma_f32_16x16x32_bf16 v[0:3], v[224:227], v[64:67], 0\n\t" \
    "v_cvt_pk_bf16_f32 v72, v208, v209\n\t" \
    "v_cvt_pk_bf16_f32 v73, v210, v211\n\t" \
    "v_mfma_f32_16x16x32_bf16 v[4:7], v[228:231], v[64:67], 0\n\t" \
    "v_cvt_pk_bf16_f32 v74, v212, v213\n\t" \
    "v_cvt_pk_bf16_f32 v75, v214, v215\n\t" \
    "v_mfma_f32_16x16x32_bf16 v[8:11], v[224:227], v[68:71], 0\n\t" \
    "v_mov_b32 v120, 0x358637bd\n\t" \
    "v_mov_b32 v121, 0x358637bd\n\t" \
    "v_mfma_f32_16x16x32_bf16 v[12:15], v[228:231], v[68:71], 0\n\t" \
    "v_mov_b32 v122, 0x358637bd\n\t" \
    "v_mov_b32 v123, 0x358637bd\n\t" \
    "v_mfma_f32_16x16x32_bf16 v[16:19], v[224:227], v[72:75], 0\n\t" \
    "v_cvt_pk_bf16_f32 v76, v216, v217\n\t" \
    "v_cvt_pk_bf16_f32 v77, v218, v219\n\t" \
    "v_mfma_f32_16x16x32_bf16 v[20:23], v[228:231], v[72:75], 0\n\t" \
    "v_cvt_pk_bf16_f32 v78, v220, v221\n\t" \
    "v_cvt_pk_bf16_f32 v79, v222, v223\n\t" \
    "s_nop 1\n\t" \
    "v_mfma_f32_16x16x32_bf16 v[24:27], v[224:227], v[76:79], 0\n\t" \
    "v_mfma_f32_16x16x32_bf16 v[28:31], v[228:231], v[76:79], 0\n\t" \
    "v_max3_f32 v64, v0, v1, v2\n\t" \
    "v_max3_f32 v66, -v0, -v1, -v2\n\t" \
    "v_max3_f32 v68, v8, v9, v10\n\t" \
    "v_max3_f32 v64, v64, v3, v4\n\t" \
    "v_max3_f32 v66, v66, -v3, -v4\n\t" \
    "v_max3_f32 v68, v68, v11, v12\n\t" \
    "v_max3_f32 v64, v64, v5, v6\n\t" \
    "v_max3_f32 v66, v66, -v5, -v6\n\t" \
    "v_max3_f32 v68, v68, v13, v14\n\t" \
    "v_max_f32_e64 v64, v64, v7\n\t" \
    "v_max_f32_e64 v66, v66, -v7\n\t" \
    "v_max_f32_e64 v68, v68, v15\n\t" \
    "v_max3_f32 v70, -v8, -v9, -v10\n\t" \
    "v_permlane32_swap_b32 v64, v66\n\t" \
    "v_max3_f32 v72, v16, v17, v18\n\t" \
    "v_max3_f32 v70, v70, -v11, -v12\n\t" \
    "v_max_f32 v64, v64, v66\n\t" \
    "v_max3_f32 v72, v72, v19, v20\n\t" \
    "v_max3_f32 v70, v70, -v13, -v14\n\t" \
    "v_max3_f32 v74, -v16, -v17, -v18\n\t" \
    "v_max3_f32 v72, v72, v21, v22\n\t" \
    "v_max_f32_e64 v70, v70, -v15\n\t" \
    "v_max3_f32 v74, v74, -v19, -v20\n\t" \
    "v_max_f32_e64 v72, v72, v23\n\t" \
    "v_permlane32_swap_b32 v68, v70\n\t" \
    "v_max3_f32 v74, v74, -v21, -v22\n\t" \
    "v_max3_f32 v76, v24, v25, v26\n\t" \
    ".Lpad_ab2:\n\t" \
    "s_cbranch_execz .Lpad_ret\n\t" \
    "v_max_f32 v68, v68, v70\n\t" \
    "v_max_f32_e64 v74, v74, -v23\n\t" \
    "v_max3_f32 v76, v76, v27, v28\n\t" \
    "v_permlane16_swap_b32 v64, v68\n\t" \
    "v_permlane32_swap_b32 v72, v74\n\t" \
    "v_max3_f32 v76, v76, v29, v30\n\t" \
    "v_max_f32 v66, v64, v68\n\t" \
    "v_max_f32 v72, v72, v74\n\t" \
    "v_max_f32_e64 v76, v76, v31\n\t" \
    "v_mul_f32 v64, 0xbfb8aa3b, v66\n\t" \
    "v_mul_f32 v68, 0xbfb8aa3b, v66\n\t" \
    "v_max3_f32 v78, -v24, -v25, -v26\n\t" \
    "v_max3_f32 v78, v78, -v27, -v28\n\t" \
    "v_permlane16_swap_b32 v64, v68\n\t" \
    "v_mov_b32 v66, v64\n\t" \
    "v_max3_f32 v78, v78, -v29, -v30\n\t" \
    "v_mov_b32 v70, v68\n\t" \
    "v_permlane32_swap_b32 v64, v66\n\t" \
    "v_max_f32_e64 v78, v78, -v31\n\t" \
    "v_permlane32_swap_b32 v68, v70\n\t" \
    "v_pk_fma_f32 v[32:33], v[0:1], s[90:91], v[64:65] op_sel_hi:[1,1,0]\n\t" \
    "v_permlane32_swap_b32 v76, v78\n\t" \
    "v_pk_fma_f32 v[0:1], v[0:1], s[90:91], v[66:67] op_sel_hi:[1,1,0] neg_lo:[1,0,0] neg_hi:[1,0,0]\n\t" \
    "v_pk_fma_f32 v[34:35], v[2:3], s[90:91], v[64:65] op_sel_hi:[1,1,0]\n\t" \
    "v_max_f32 v76, v76, v78\n\t" \
    "v_pk_fma_f32 v[2:3], v[2:3], s[90:91], v[66:67] op_sel_hi:[1,1,0] neg_lo:[1,0,0] neg_hi:[1,0,0]\n\t" \
    "v_pk_fma_f32 v[36:37], v[4:5], s[90:91], v[64:65] op_sel_hi:[1,1,0]\n\t" \
    "v_permlane16_swap_b32 v72, v76\n\t" \
    "v_pk_fma_f32 v[4:5], v[4:5], s[90:91], v[66:67] op_sel_hi:[1,1,0] neg_lo:[1,0,0] neg_hi:[1,0,0]\n\t" \
    "v_pk_fma_f32 v[38:39], v[6:7], s[90:91], v[64:65] op_sel_hi:[1,1,0]\n\t" \
    "v_max_f32 v74, v72, v76\n\t" \
    "v_pk_fma_f32 v[6:7], v[6:7], s[90:91], v[66:67] op_sel_hi:[1,1,0] neg_lo:[1,0,0] neg_hi:[1,0,0]\n\t" \
    "v_exp_f32 v32, v32\n\t" \
    "v_mul_f32 v72, 0xbfb8aa3b, v74\n\t" \
    "v_mul_f32 v76, 0xbfb8aa3b, v74\n\t" \
    "v_exp_f32 v0, v0\n\t" \
    "v_exp_f32 v33, v33\n\t" \
    "v_permlane16_swap_b32 v72, v76\n\t" \
    "v_exp_f32 v1, v1\n\t" \
    "v_exp_f32 v34, v34\n\t" \
    "v_mov_b32 v74, v72\n\t" \
    "v_mov_b32 v78, v76\n\t" \
    "v_exp_f32 v2, v2\n\t" \
    "v_permlane32_swap_b32 v72, v74\n\t" \
    "v_permlane32_swap_b32 v76, v78\n\t" \
    "v_exp_f32 v35, v35\n\t" \
    "v_exp_f32 v3, v3\n\t" \
    "v_exp_f32 v36, v36\n\t" \
    "v_pk_add_f32 v[64:65], v[32:33], v[34:35]\n\t" \
    "v_pk_add_f32 v[66:67], v[0:1], v[2:3]\n\t" \
    "v_exp_f32 v4, v4\n\t" \
    "v_exp_f32 v37, v37\n\t" \
    "v_exp_f32 v5, v5\n\t" \
    "v_exp_f32 v38, v38\n\t" \
    "v_pk_add_f32 v[64:65], v[64:65], v[36:37]\n\t" \
    "v_pk_add_f32 v[66:67], v[66:67], v[4:5]\n\t" \
    "v_exp_f32 v6, v6\n\t" \
    "v_exp_f32 v39, v39\n\t" \
    "v_exp_f32 v7, v7\n\t" \
    "v_pk_fma_f32 v[40:41], v[8:9], s[90:91], v[68:69] op_sel_hi:[1,1,0]\n\t" \
    "v_pk_add_f32 v[64:65], v[64:65], v[38:39]\n\t" \
    "v_pk_add_f32 v[66:67], v[66:67], v[6:7]\n\t" \
    "v_pk_fma_f32 v[8:9], v[8:9], s[90:91], v[70:71] op_sel_hi:[1,1,0] neg_lo:[1,0,0] neg_hi:[1,0,0]\n\t" \
    "v_add_f32 v64, v64, v65\n\t" \
    "v_add_f32 v66, v66, v67\n\t" \
    "v_pk_fma_f32 v[42:43], v[10:11], s[90:91], v[68:69] op_sel_hi:[1,1,0]\n\t" \
    "v_pk_fma_f32 v[10:11], v[10:11], s[90:91], v[70:71] op_sel_hi:[1,1,0] neg_lo:[1,0,0] neg_hi:[1,0,0]\n\t" \
    "v_permlane32_swap_b32 v64, v66\n\t" \
    "v_pk_fma_f32 v[44:45], v[12:13], s[90:91], v[68:69] op_sel_hi:[1,1,0]\n\t" \
    "v_pk_fma_f32 v[12:13], v[12:13], s[90:91], v[70:71] op_sel_hi:[1,1,0] neg_lo:[1,0,0] neg_hi:[1,0,0]\n\t" \
    "v_add_f32 v64, v64, v66\n\t" \
    "v_pk_fma_f32 v[46:47], v[14:15], s[90:91], v[68:69] op_sel_hi:[1,1,0]\n\t" \
    "v_pk_fma_f32 v[14:15], v[14:15], s[90:91], v[70:71] op_sel_hi:[1,1,0] neg_lo:[1,0,0] neg_hi:[1,0,0]\n\t" \
    "v_exp_f32 v40, v40\n\t" \
    "v_exp_f32 v8, v8\n\t" \
    "v_exp_f32 v41, v41\n\t" \
    "v_exp_f32 v9, v9\n\t" \
    "v_exp_f32 v42, v42\n\t" \
    "v_exp_f32 v10, v10\n\t" \
    "v_exp_f32 v43, v43\n\t" \
    "v_exp_f32 v11, v11\n\t" \
    "v_exp_f32 v44, v44\n\t" \
    "v_pk_add_f32 v[68:69], v[40:41], v[42:43]\n\t" \
    "v_pk_add_f32 v[70:71], v[8:9], v[10:11]\n\t" \
    "v_exp_f32 v12, v12\n\t" \
    "v_exp_f32 v45, v45\n\t" \
    "v_exp_f32 v13, v13\n\t" \
    "v_exp_f32 v46, v46\n\t" \
    "v_pk_add_f32 v[68:69], v[68:69], v[44:45]\n\t" \
    "v_pk_add_f32 v[70:71], v[70:71], v[12:13]\n\t" \
    "v_exp_f32 v14, v14\n\t" \
    "v_exp_f32 v47, v47\n\t" \
    "v_exp_f32 v15, v15\n\t" \
    "v_pk_fma_f32 v[48:49], v[16:17], s[90:91], v[72:73] op_sel_hi:[1,1,0]\n\t" \
    "v_pk_add_f32 v[68:69], v[68:69], v[46:47]\n\t" \
    "v_pk_add_f32 v[70:71], v[70:71], v[14:15]\n\t" \
    "v_pk_fma_f32 v[16:17], v[16:17], s[90:91], v[74:75] op_sel_hi:[1,1,0] neg_lo:[1,0,0] neg_hi:[1,0,0]\n\t" \
    "v_add_f32 v68, v68, v69\n\t" \
    "v_add_f32 v70, v70, v71\n\t" \
    "v_pk_fma_f32 v[50:51], v[18:19], s[90:91], v[72:73] op_sel_hi:[1,1,0]\n\t" \
    "v_pk_fma_f32 v[18:19], v[18:19], s[90:91], v[74:75] op_sel_hi:[1,1,0] neg_lo:[1,0,0] neg_hi:[1,0,0]\n\t" \
    "v_permlane32_swap_b32 v68, v70\n\t" \
    "v_pk_fma_f32 v[52:53], v[20:21], s[90:91], v[72:73] op_sel_hi:[1,1,0]\n\t" \
    "v_pk_fma_f32 v[20:21], v[20:21], s[90:91], v[74:75] op_sel_hi:[1,1,0] neg_lo:[1,0,0] neg_hi:[1,0,0]\n\t" \
    "v_add_f32 v68, v68, v70\n\t" \
    "v_pk_fma_f32 v[54:55], v[22:23], s[90:91], v[72:73] op_sel_hi:[1,1,0]\n\t" \
    "v_pk_fma_f32 v[22:23], v[22:23], s[90:91], v[74:75] op_sel_hi:[1,1,0] neg_lo:[1,0,0] neg_hi:[1,0,0]\n\t" \
    "v_permlane16_swap_b32 v64, v68\n\t" \
    "v_exp_f32 v48, v48\n\t" \
    "v_exp_f32 v16, v16\n\t" \
    "v_add_f32 v66, v64, v68\n\t" \
    "v_exp_f32 v49, v49\n\t" \
    "v_exp_f32 v17, v17\n\t" \
    "v_rcp_f32 v64, v66\n\t" \
    "v_exp_f32 v50, v50\n\t" \
    "v_exp_f32 v18, v18\n\t" \
    "v_mov_b32 v68, v64\n\t" \
    "v_exp_f32 v51, v51\n\t" \
    "v_exp_f32 v19, v19\n\t" \
    "v_permlane16_swap_b32 v64, v68\n\t" \
    "v_pk_add_f32 v[72:73], v[48:49], v[50:51]\n\t" \
    "v_pk_add_f32 v[74:75], v[16:17], v[18:19]\n\t" \
    "v_mov_b32 v66, v64\n\t" \
    "v_mov_b32 v70, v68\n\t" \
    "v_exp_f32 v52, v52\n\t" \
    "v_permlane32_swap_b32 v64, v66\n\t" \
    "v_permlane32_swap_b32 v68, v70\n\t" \
    "v_exp_f32 v20, v20\n\t" \
    "v_exp_f32 v53, v53\n\t" \
    "v_exp_f32 v21, v21\n\t" \
    "v_exp_f32 v54, v54\n\t" \
    "v_pk_add_f32 v[72:73], v[72:73], v[52:53]\n\t" \
    "v_pk_add_f32 v[74:75], v[74:75], v[20:21]\n\t" \
    "v_exp_f32 v22, v22\n\t" \
    "v_exp_f32 v55, v55\n\t" \
    "v_exp_f32 v23, v23\n\t" \
    "v_pk_fma_f32 v[56:57], v[24:25], s[90:91], v[76:77] op_sel_hi:[1,1,0]\n\t" \
    "v_pk_add_f32 v[72:73], v[72:73], v[54:55]\n\t" \
    "v_pk_add_f32 v[74:75], v[74:75], v[22:23]\n\t" \
    "v_pk_fma_f32 v[24:25], v[24:25], s[90:91], v[78:79] op_sel_hi:[1,1,0] neg_lo:[1,0,0] neg_hi:[1,0,0]\n\t" \
    "v_add_f32 v72, v72, v73\n\t" \
    "v_add_f32 v74, v74, v75\n\t" \
    "v_pk_fma_f32 v[58:59], v[26:27], s[90:91], v[76:77] op_sel_hi:[1,1,0]\n\t" \
    "v_pk_fma_f32 v[26:27], v[26:27], s[90:91], v[78:79] op_sel_hi:[1,1,0] neg_lo:[1,0,0] neg_hi:[1,0,0]\n\t" \
    "v_permlane32_swap_b32 v72, v74\n\t" \
    "v_pk_fma_f32 v[60:61], v[28:29], s[90:91], v[76:77] op_sel_hi:[1,1,0]\n\t" \
    "v_pk_fma_f32 v[28:29], v[28:29], s[90:91], v[78:79] op_sel_hi:[1,1,0] neg_lo:[1,0,0] neg_hi:[1,0,0]\n\t" \
    "v_add_f32 v72, v72, v74\n\t" \
    "v_pk_fma_f32 v[62:63], v[30:31], s[90:91], v[76:77] op_sel_hi:[1,1,0]\n\t" \
    "v_pk_fma_f32 v[30:31], v[30:31], s[90:91], v[78:79] op_sel_hi:[1,1,0] neg_lo:[1,0,0] neg_hi:[1,0,0]\n\t" \
    "v_exp_f32 v56, v56\n\t" \
    "v_exp_f32 v24, v24\n\t" \
    "v_exp_f32 v57, v57\n\t" \
    "v_exp_f32 v25, v25\n\t" \
    "v_exp_f32 v58, v58\n\t" \
    "v_exp_f32 v26, v26\n\t" \
    "v_exp_f32 v59, v59\n\t" \
    "v_exp_f32 v27, v27\n\t" \
    "v_exp_f32 v60, v60\n\t" \
    "v_pk_add_f32 v[76:77], v[56:57], v[58:59]\n\t" \
    "v_pk_add_f32 v[78:79], v[24:25], v[26:27]\n\t" \
    "v_exp_f32 v28, v28\n\t" \
    "v_exp_f32 v61, v61\n\t" \
    "v_exp_f32 v29, v29\n\t" \
    "v_exp_f32 v62, v62\n\t" \
    "v_pk_add_f32 v[76:77], v[76:77], v[60:61]\n\t" \
    "v_pk_add_f32 v[78:79], v[78:79], v[28:29]\n\t" \
    "v_exp_f32 v30, v30\n\t" \
    "v_exp_f32 v63, v63\n\t" \
    "v_exp_f32 v31, v31\n\t" \
    "v_pk_mul_f32 v[0:1], v[0:1], v[66:67] op_sel_hi:[1,0]\n\t" \
    "v_pk_add_f32 v[76:77], v[76:77], v[62:63]\n\t" \
    "v_pk_add_f32 v[78:79], v[78:79], v[30:31]\n\t" \
    "v_pk_mul_f32 v[32:33], v[32:33], v[64:65] op_sel_hi:[1,0]\n\t" \
    "v_add_f32 v76, v76, v77\n\t" \
    "v_add_f32 v78, v78, v79\n\t" \
    "v_pk_mul_f32 v[2:3], v[2:3], v[66:67] op_sel_hi:[1,0]\n\t" \
    "v_pk_mul_f32 v[34:35], v[34:35], v[64:65] op_sel_hi:[1,0]\n\t" \
    "v_permlane32_swap_b32 v76, v78\n\t" \
    "v_pk_mul_f32 v[4:5], v[4:5], v[66:67] op_sel_hi:[1,0]\n\t" \
    "v_pk_mul_f32 v[36:37], v[36:37], v[64:65] op_sel_hi:[1,0]\n\t" \
    "v_add_f32 v76, v76, v78\n\t" \
    "v_pk_mul_f32 v[6:7], v[6:7], v[66:67] op_sel_hi:[1,0]\n\t" \
    "v_pk_mul_f32 v[38:39], v[38:39], v[64:65] op_sel_hi:[1,0]\n\t" \
    "v_permlane16_swap_b32 v72, v76\n\t" \
    "v_cvt_pk_bf16_f32 v7, v6, v7\n\t" \
    "v_cvt_pk_bf16_f32 v6, v4, v5\n\t" \
    "v_add_f32 v74, v72, v76\n\t" \
    "v_cvt_pk_bf16_f32 v5, v2, v3\n\t" \
    "v_cvt_pk_bf16_f32 v4, v0, v1\n\t" \
    "v_rcp_f32 v72, v74\n\t" \
    "v_cvt_pk_bf16_f32 v0, v32, v33\n\t" \
    "v_cvt_pk_bf16_f32 v1, v34, v35\n\t" \
    "v_mov_b32 v76, v72\n\t" \
    "v_cvt_pk_bf16_f32 v2, v36, v37\n\t" \
    "v_cvt_pk_bf16_f32 v3, v38, v39\n\t" \
    "v_permlane16_swap_b32 v72, v76\n\t" \
    "v_pk_mul_f32 v[8:9], v[8:9], v[70:71] op_sel_hi:[1,0]\n\t" \
    "v_pk_mul_f32 v[40:41], v[40:41], v[68:69] op_sel_hi:[1,0]\n\t" \
    "v_mov_b32 v74, v72\n\t" \
    "v_mov_b32 v78, v76\n\t" \
    "v_pk_mul_f32 v[10:11], v[10:11], v[70:71] op_sel_hi:[1,0]\n\t" \
    "v_permlane32_swap_b32 v72, v74\n\t" \
    "v_permlane32_swap_b32 v76, v78\n\t" \
    "v_pk_mul_f32 v[42:43], v[42:43], v[68:69] op_sel_hi:[1,0]\n\t" \
    "v_pk_mul_f32 v[12:13], v[12:13], v[70:71] op_sel_hi:[1,0]\n\t" \
    "v_pk_mul_f32 v[44:45], v[44:45], v[68:69] op_sel_hi:[1,0]\n\t" \
    "v_pk_mul_f32 v[14:15], v[14:15], v[70:71] op_sel_hi:[1,0]\n\t" \
    "v_pk_mul_f32 v[46:47], v[46:47], v[68:69] op_sel_hi:[1,0]\n\t" \
    "v_pk_mul_f32 v[16:17], v[16:17], v[74:75] op_sel_hi:[1,0]\n\t" \
    "v_cvt_pk_bf16_f32 v15, v14, v15\n\t" \
    "v_cvt_pk_bf16_f32 v14, v12, v13\n\t" \
    "v_cvt_pk_bf16_f32 v13, v10, v11\n\t" \
    "v_cvt_pk_bf16_f32 v12, v8, v9\n\t" \
    "v_cvt_pk_bf16_f32 v8, v40, v41\n\t" \
    "v_cvt_pk_bf16_f32 v9, v42, v43\n\t" \
    "v_cvt_pk_bf16_f32 v10, v44, v45\n\t" \
    "v_cvt_pk_bf16_f32 v11, v46, v47\n\t" \
    "v_pk_mul_f32 v[48:49], v[48:49], v[72:73] op_sel_hi:[1,0]\n\t" \
    "v_pk_mul_f32 v[18:19], v[18:19], v[74:75] op_sel_hi:[1,0]\n\t" \
    "v_pk_mul_f32 v[50:51], v[50:51], v[72:73] op_sel_hi:[1,0]\n\t" \
    "v_pk_mul_f32 v[20:21], v[20:21], v[74:75] op_sel_hi:[1,0]\n\t" \
    "v_pk_mul_f32 v[52:53], v[52:53], v[72:73] op_sel_hi:[1,0]\n\t" \
    "v_pk_mul_f32 v[22:23], v[22:23], v[74:75] op_sel_hi:[1,0]\n\t" \
    "v_pk_mul_f32 v[54:55], v[54:55], v[72:73] op_sel_hi:[1,0]\n\t" \
    "ds_read_b32 v237, v249 offset:8\n\t" \
    "v_pk_mul_f32 v[24:25], v[24:25], v[78:79] op_sel_hi:[1,0]\n\t" \
    "v_cvt_pk_bf16_f32 v23, v22, v23\n\t" \
    "v_cvt_pk_bf16_f32 v22, v20, v21\n\t" \
    "v_cvt_pk_bf16_f32 v21, v18, v19\n\t" \
    "v_cvt_pk_bf16_f32 v20, v16, v17\n\t" \
    "v_cvt_pk_bf16_f32 v16, v48, v49\n\t" \
    "v_cvt_pk_bf16_f32 v17, v50, v51\n\t" \
    "v_cvt_pk_bf16_f32 v18, v52, v53\n\t" \
    "v_cvt_pk_bf16_f32 v19, v54, v55\n\t" \
    "v_pk_mul_f32 v[56:57], v[56:57], v[76:77] op_sel_hi:[1,0]\n\t" \
    "v_pk_mul_f32 v[26:27], v[26:27], v[78:79] op_sel_hi:[1,0]\n\t" \
    "v_pk_mul_f32 v[58:59], v[58:59], v[76:77] op_sel_hi:[1,0]\n\t" \
    "v_pk_mul_f32 v[28:29], v[28:29], v[78:79] op_sel_hi:[1,0]\n\t" \
    "v_pk_mul_f32 v[60:61], v[60:61], v[76:77] op_sel_hi:[1,0]\n\t" \
    "v_pk_mul_f32 v[30:31], v[30:31], v[78:79] op_sel_hi:[1,0]\n\t" \
    "v_pk_mul_f32 v[62:63], v[62:63], v[76:77] op_sel_hi:[1,0]\n\t" \
    "v_cvt_pk_bf16_f32 v31, v30, v31\n\t" \
    "v_cvt_pk_bf16_f32 v30, v28, v29\n\t" \
    "v_cvt_pk_bf16_f32 v29, v26, v27\n\t" \
    "v_cvt_pk_bf16_f32 v28, v24, v25\n\t" \
    "v_cvt_pk_bf16_f32 v24, v56, v57\n\t" \
    "v_cvt_pk_bf16_f32 v25, v58, v59\n\t" \
    "v_cvt_pk_bf16_f32 v26, v60, v61\n\t" \
    "v_cvt_pk_bf16_f32 v27, v62, v63\n\t" \
    "s_setprio 2\n\t" \
    "s_waitcnt lgkmcnt(0)\n\t" \
    "v_readfirstlane_b32 s85, v237\n\t" \
    "s_add_u32 s86, s87, 0x10000\n\t" \
    "s_cmp_lt_u32 s85, 3\n\t" \
    "s_cbranch_scc1 .Lab_s7\n\t" \
    ".Lab_s7r:\n\t" \
    "s_mov_b32 s89, 0x400000\n\t" \
    "s_mov_b32 s82, 4\n\t" \
    ".Lab_k:\n\t" \
    "ds_read_b32 v237, v249 offset:8\n\t" \
    "ds_read_b128 v[32:35], v252\n\t" \
    "ds_read_b128 v[36:39], v252 offset:256\n\t" \
    "ds_read_b128 v[40:43], v253\n\t" \
    "ds_read_b128 v[44:47], v253 offset:1024\n\t" \
    "ds_read_b128 v[48:51], v253 offset:2048\n\t" \
    "ds_read_b128 v[52:55], v253 offset:3072\n\t" \
    "s_sub_u32 s89, s89, 1\n\t" \
    "s_cbranch_scc1 .Lab_kr\n\t" \
    "s_waitcnt lgkmcnt(6)\n\t" \
    "v_readfirstlane_b32 s85, v237\n\t" \
    "s_cmp_lt_u32 s85, s82\n\t" \
    "s_cbranch_scc1 .Lab_k\n\t" \
    ".Lab_kr:\n\t" \
    "s_waitcnt lgkmcnt(5)\n\t" \
    "v_mfma_f32_16x16x32_bf16 v[56:59], v[32:35], v[0:3], v[120:123]\n\t" \
    "ds_read_b32 v126, v249 offset:296\n\t" \
    "v_mfma_f32_16x16x32_bf16 v[60:63], v[32:35], v[8:11], v[120:123]\n\t" \
    "ds_read_b32 v127, v249 offset:308\n\t" \
    "v_mfma_f32_16x16x32_bf16 v[64:67], v[32:35], v[16:19], v[120:123]\n\t" \
    "v_cmp_eq_u32 vcc, 0, v244\n\t" \
    "v_mfma_f32_16x16x32_bf16 v[68:71], v[32:35], v[24:27], v[120:123]\n\t" \
    "v_lshrrev_b32 v237, 1, v251\n\t" \
    "v_bfrev_b32 v238, 1\n\t" \
    "s_waitcnt lgkmcnt(6)\n\t" \
    "v_mfma_f32_16x16x32_bf16 v[56:59], v[36:39], v[4:7], v[56:59]\n\t" \
    "v_mfma_f32_16x16x32_bf16 v[60:63], v[36:39], v[12:15], v[60:63]\n\t" \
    "v_cndmask_b32 v254, v238, v237, vcc\n\t" \
    "v_mfma_f32_16x16x32_bf16 v[64:67], v[36:39], v[20:23], v[64:67]\n\t" \
    "s_or_b32 s82, s85, 1\n\t" \
    "v_mfma_f32_16x16x32_bf16 v[68:71], v[36:39], v[28:31], v[68:71]\n\t" \
    "s_waitcnt lgkmcnt(5)\n\t" \
    "v_mfma_f32_16x16x32_bf16 v[72:75], v[40:43], v[0:3], 0\n\t" \
    "s_waitcnt lgkmcnt(4)\n\t" \
    "v_mfma_f32_16x16x32_bf16 v[76:79], v[44:47], v[0:3], 0\n\t" \
    "s_waitcnt lgkmcnt(3)\n\t" \
    "v_mfma_f32_16x16x32_bf16 v[72:75], v[48:51], v[4:7], v[72:75]\n\t" \
    "s_waitcnt lgkmcnt(2)\n\t" \
    "v_mfma_f32_16x16x32_bf16 v[76:79], v[52:55], v[4:7], v[76:79]\n\t" \
    "v_mfma_f32_16x16x32_bf16 v[80:83], v[40:43], v[8:11], 0\n\t" \
    "v_rcp_f32 v56, v56\n\t" \
    "v_mfma_f32_16x16x32_bf16 v[84:87], v[44:47], v[8:11], 0\n\t" \
    "v_rcp_f32 v60, v60\n\t" \
    "v_mfma_f32_16x16x32_bf16 v[80:83], v[48:51], v[12:15], v[80:83]\n\t" \
    "v_rcp_f32 v64, v64\n\t" \
    "v_mfma_f32_16x16x32_bf16 v[84:87], v[52:55], v[12:15], v[84:87]\n\t" \
    "v_rcp_f32 v68, v68\n\t" \
    "v_mfma_f32_16x16x32_bf16 v[88:91], v[40:43], v[16:19], 0\n\t" \
    "v_mfma_f32_16x16x32_bf16 v[92:95], v[44:47], v[16:19], 0\n\t" \
    "v_mul_f32 v72, v72, v56\n\t" \
    "v_mul_f32 v73, v73, v56\n\t" \
    "v_mfma_f32_16x16x32_bf16 v[88:91], v[48:51], v[20:23], v[88:91]\n\t" \
    "v_mul_f32 v74, v74, v56\n\t" \
    "v_mul_f32 v75, v75, v56\n\t" \
    "v_mfma_f32_16x16x32_bf16 v[92:95], v[52:55], v[20:23], v[92:95]\n\t" \
    "v_mul_f32 v76, v76, v56\n\t" \
    "v_mul_f32 v77, v77, v56\n\t" \
    "v_mfma_f32_16x16x32_bf16 v[96:99], v[40:43], v[24:27], 0\n\t" \
    "v_mul_f32 v78, v78, v56\n\t" \
    "v_mul_f32 v79, v79, v56\n\t" \
    "v_mfma_f32_16x16x32_bf16 v[100:103], v[44:47], v[24:27], 0\n\t" \
    "v_cvt_pk_bf16_f32 v104, v72, v73\n\t" \
    "v_cvt_pk_bf16_f32 v105, v74, v75\n\t" \
    "v_mfma_f32_16x16x32_bf16 v[96:99], v[48:51], v[28:31], v[96:99]\n\t" \
    "v_cvt_pk_bf16_f32 v106, v76, v77\n\t" \
    "v_cvt_pk_bf16_f32 v107, v78, v79\n\t" \
    "v_mfma_f32_16x16x32_bf16 v[100:103], v[52:55], v[28:31], v[100:103]\n\t" \
    "buffer_store_dwordx4 v[104:107], v251, %[rsb], s87 offen sc1\n\t" \
    "v_pk_mul_f32 v[80:81], v[80:81], v[60:61] op_sel_hi:[1,0]\n\t" \
    "v_pk_mul_f32 v[82:83], v[82:83], v[60:61] op_sel_hi:[1,0]\n\t" \
    "v_pk_mul_f32 v[84:85], v[84:85], v[60:61] op_sel_hi:[1,0]\n\t" \
    "v_pk_mul_f32 v[86:87], v[86:87], v[60:61] op_sel_hi:[1,0]\n\t" \
    "v_cvt_pk_bf16_f32 v108, v80, v81\n\t" \
    "v_cvt_pk_bf16_f32 v109, v82, v83\n\t" \
    "v_cvt_pk_bf16_f32 v110, v84, v85\n\t" \
    "v_cvt_pk_bf16_f32 v111, v86, v87\n\t" \
    "buffer_store_dwordx4 v[108:111], v251, %[rsb], s87 offen offset:1024 sc1\n\t" \
    "v_pk_mul_f32 v[88:89], v[88:89], v[64:65] op_sel_hi:[1,0]\n\t" \
    "v_pk_mul_f32 v[90:91], v[90:91], v[64:65] op_sel_hi:[1,0]\n\t" \
    "v_pk_mul_f32 v[92:93], v[92:93], v[64:65] op_sel_hi:[1,0]\n\t" \
    "v_pk_mul_f32 v[94:95], v[94:95], v[64:65] op_sel_hi:[1,0]\n\t" \
    "v_cvt_pk_bf16_f32 v112, v88, v89\n\t" \
    "v_cvt_pk_bf16_f32 v113, v90, v91\n\t" \
    "v_cvt_pk_bf16_f32 v114, v92, v93\n\t" \
    "v_cvt_pk_bf16_f32 v115, v94, v95\n\t" \
    "buffer_store_dwordx4 v[112:115], v251, %[rsb], s86 offen sc1\n\t" \
    "v_pk_mul_f32 v[96:97], v[96:97], v[68:69] op_sel_hi:[1,0]\n\t" \
    "v_pk_mul_f32 v[98:99], v[98:99], v[68:69] op_sel_hi:[1,0]\n\t" \
    "v_pk_mul_f32 v[100:101], v[100:101], v[68:69] op_sel_hi:[1,0]\n\t" \
    "v_pk_mul_f32 v[102:103], v[102:103], v[68:69] op_sel_hi:[1,0]\n\t" \
    "v_cvt_pk_bf16_f32 v116, v96, v97\n\t" \
    "v_cvt_pk_bf16_f32 v117, v98, v99\n\t" \
    "v_cvt_pk_bf16_f32 v118, v100, v101\n\t" \
    "v_cvt_pk_bf16_f32 v119, v102, v103\n\t" \
    "v_add3_u32 v32, v104, v105, 0\n\t" \
    "v_add3_u32 v32, v106, v107, v32\n\t" \
    "v_add3_u32 v33, v112, v113, 0\n\t" \
    "v_add3_u32 v33, v114, v115, v33\n\t" \
    "v_add3_u32 v32, v108, v109, v32\n\t" \
    "v_add3_u32 v32, v110, v111, v32\n\t" \
    "v_add3_u32 v33, v116, v117, v33\n\t" \
    "v_add3_u32 v33, v118, v119, v33\n\t" \
    "buffer_store_dwordx4 v[116:119], v251, %[rsb], s86 offen offset:1024 sc1\n\t" \
    "ds_read_b32 v36, v249 offset:8\n\t" \
    "v_permlane32_swap_b32 v32, v33\n\t" \
    "v_add_u32 v32, v32, v33\n\t" \
    "v_mov_b32 v33, v32\n\t" \
    "s_waitcnt lgkmcnt(2)\n\t" \
    "v_readfirstlane_b32 %[rid], v126\n\t" \
    "s_waitcnt lgkmcnt(1)\n\t" \
    "v_readfirstlane_b32 %[t3], v127\n\t" \
    "v_permlane16_swap_b32 v32, v33\n\t" \
    "v_add_u32 v32, v32, v33\n\t" \
    "v_mov_b32 v33, v236\n\t" \
    "ds_read_b32 v238, v249 offset:56\n\t" \
    "v_add_u32_dpp v32, v32, v32 row_ror:8 row_mask:0xf bank_mask:0xf bound_ctrl:1\n\t" \
    "v_mov_b32 v237, 12\n\t" \
    "v_and_b32 v34, 15, v250\n\t" \
    "v_add_u32_dpp v32, v32, v32 row_ror:4 row_mask:0xf bank_mask:0xf bound_ctrl:1\n\t" \
    "s_waitcnt lgkmcnt(1)\n\t" \
    "v_readfirstlane_b32 s80, v36\n\t" \
    "s_and_b32 s85, %[rid], 15\n\t" \
    "v_add_u32_dpp v32, v32, v32 row_ror:2 row_mask:0xf bank_mask:0xf bound_ctrl:1\n\t" \
    "v_cmp_eq_u32_e64 %[rm], s85, v34\n\t" \
    "s_cmp_lg_u32 s80, s82\n\t" \
    "v_add_u32_dpp v32, v32, v32 row_ror:1 row_mask:0xf bank_mask:0xf bound_ctrl:1\n\t" \
    "s_cbranch_scc1 .Lab_sa\n\t" \
    ".Lab_sar:\n\t" \
    "buffer_store_dwordx2 v[32:33], v254, %[rsb], s88 offen sc1\n\t" \
    "ds_write_b32 v248, v237\n\t" \
    "v_and_b32 v252, 48, v250\n\t" \
    "s_lshr_b32 s85, %[rid], 5\n\t" \
    "v_add_u32 v252, 161536, v252\n\t" \
    "s_cmp_eq_u32 s85, %[own]\n\t" \
    "s_cselect_b64 %[rm], %[rm], 0\n\t" \
    "s_waitcnt vmcnt(5)\n\t" \
    "v_cmp_ne_u32 vcc, v236, v233\n\t" \
    "v_cmp_ne_u32_e64 s[92:93], v236, v235\n\t" \
    "s_nop 0\n\t" \
    "s_or_b64 vcc, vcc, s[92:93]\n\t" \
    "s_cbranch_scc1 .Lab_s8\n\t" \
    ".Lab_s8r:\n\t" \
    "v_sub_f32 v254, 1.0, v232\n\t" \
    "v_sub_f32 v255, 1.0, v234\n\t" \
    "v_cvt_pk_bf16_f32 v255, v254, v255\n\t" \
    "v_lshlrev_b32 v254, 16, v255\n\t" \
    "v_and_b32 v255, 0xffff0000, v255\n\t" \
    "s_waitcnt lgkmcnt(1)\n\t" \
    "v_readfirstlane_b32 s85, v238\n\t" \
    "s_cmp_lt_u32 s85, 2\n\t" \
    "s_cbranch_scc1 .Lab_s9\n\t" \
    ".Lab_s9r:\n\t" \
    "s_waitcnt lgkmcnt(0)\n\t"
#define PHASE_C_ASM \
    "s_branch .Lpad_c0\n\t" \
    ".Lc_x:\n\t" \
    "s_waitcnt vmcnt(0) lgkmcnt(0)\n\t" \
    "s_branch .Lc_end\n\t" \
    ".Lc_w0:\n\t" \
    "s_mov_b32 s86, 0x400000\n\t" \
    ".Lc_a0:\n\t" \
    "ds_read_b32 v160, v249 offset:56\n\t" \
    "s_waitcnt lgkmcnt(0)\n\t" \
    "v_readfirstlane_b32 s83, v160\n\t" \
    "s_sub_u32 s86, s86, 1\n\t" \
    "s_cbranch_scc1 .Lc_r0\n\t" \
    "s_cmp_lt_u32 s83, s82\n\t" \
    "s_cbranch_scc1 .Lc_a0\n\t" \
    "s_branch .Lc_r0\n\t" \
    ".Lc_w1:\n\t" \
    "s_mov_b32 s86, 0x400000\n\t" \
    ".Lc_a1:\n\t" \
    "ds_read_b32 v160, v249 offset:56\n\t" \
    "s_waitcnt lgkmcnt(0)\n\t" \
    "v_readfirstlane_b32 s83, v160\n\t" \
    "s_sub_u32 s86, s86, 1\n\t" \
    "s_cbranch_scc1 .Lc_r1\n\t" \
    "s_cmp_lt_u32 s83, s82\n\t" \
    "s_cbranch_scc1 .Lc_a1\n\t" \
    "s_branch .Lc_r1\n\t" \
    ".Lc_y:\n\t" \
    "s_mov_b32 s86, 0x400000\n\t" \
    ".Lc_ya:\n\t" \
    "s_waitcnt lgkmcnt(0)\n\t" \
    "v_readfirstlane_b32 s83, v231\n\t" \
    "s_sub_u32 s86, s86, 1\n\t" \
    "s_cbranch_scc1 .Lc_yb\n\t" \
    "s_cmp_lg_u32 s83, 0\n\t" \
    "s_cbranch_scc1 .Lc_yb\n\t" \
    "ds_read_b32 v231, v249 offset:68\n\t" \
    "ds_read_b128 v[96:99], v252\n\t" \
    "ds_read_b128 v[100:103], v252 offset:64\n\t" \
    "ds_read_b128 v[104:107], v252 offset:128\n\t" \
    "ds_read_b128 v[108:111], v252 offset:192\n\t" \
    "s_branch .Lc_ya\n\t" \
    ".Lc_yb:\n\t" \
    "s_mov_b64 exec, %[rm]\n\t" \
    "s_bitcmp1_b32 %[rid], 4\n\t" \
    "s_cbranch_scc1 .Lc_yc\n\t" \
    "v_pk_mul_f32 v[194:195], v[96:97], v[254:255] op_sel_hi:[1,0]\n\t" \
    "v_pk_mul_f32 v[196:197], v[98:99], v[254:255] op_sel_hi:[1,0]\n\t" \
    "v_pk_mul_f32 v[198:199], v[100:101], v[254:255] op_sel_hi:[1,0]\n\t" \
    "v_pk_mul_f32 v[200:201], v[102:103], v[254:255] op_sel_hi:[1,0]\n\t" \
    "v_pk_mul_f32 v[202:203], v[104:105], v[254:255] op_sel_hi:[1,0]\n\t" \
    "v_pk_mul_f32 v[204:205], v[106:107], v[254:255] op_sel_hi:[1,0]\n\t" \
    "v_pk_mul_f32 v[206:207], v[108:109], v[254:255] op_sel_hi:[1,0]\n\t" \
    "v_pk_mul_f32 v[208:209], v[110:111], v[254:255] op_sel_hi:[1,0]\n\t" \
    "s_mov_b64 exec, -1\n\t" \
    "s_branch .Lc_yr\n\t" \
    ".Lc_yc:\n\t" \
    "v_pk_mul_f32 v[210:211], v[96:97], v[254:255] op_sel:[0,1] op_sel_hi:[1,1]\n\t" \
    "v_pk_mul_f32 v[212:213], v[98:99], v[254:255] op_sel:[0,1] op_sel_hi:[1,1]\n\t" \
    "v_pk_mul_f32 v[214:215], v[100:101], v[254:255] op_sel:[0,1] op_sel_hi:[1,1]\n\t" \
    "v_pk_mul_f32 v[216:217], v[102:103], v[254:255] op_sel:[0,1] op_sel_hi:[1,1]\n\t" \
    "v_pk_mul_f32 v[218:219], v[104:105], v[254:255] op_sel:[0,1] op_sel_hi:[1,1]\n\t" \
    "v_pk_mul_f32 v[220:221], v[106:107], v[254:255] op_sel:[0,1] op_sel_hi:[1,1]\n\t" \
    "v_pk_mul_f32 v[222:223], v[108:109], v[254:255] op_sel:[0,1] op_sel_hi:[1,1]\n\t" \
    "v_pk_mul_f32 v[224:225], v[110:111], v[254:255] op_sel:[0,1] op_sel_hi:[1,1]\n\t" \
    "s_mov_b64 exec, -1\n\t" \
    "s_branch .Lc_yr\n\t" \
    ".p2align 12\n\t" \
    ".Lpad_c0:\n\t" \
    "s_cbranch_execz .Lpad_ret\n\t" \
    "s_setprio 2\n\t" \
    "v_xor_b32 v226, %[flip], v245\n\t" \
    "v_xor_b32 v227, 32, v226\n\t" \
    "v_xor_b32 v228, 64, v226\n\t" \
    "v_xor_b32 v229, 96, v226\n\t" \
    "v_and_b32 v231, 0x7f, v244\n\t" \
    "s_lshl_b32 s80, %[grp], 14\n\t" \
    "s_lshl_b32 s83, %[grp], 3\n\t" \
    "s_lshl_b32 s84, 0xffff, s83\n\t" \
    "s_lshl_b32 s85, %[grp], 6\n\t" \
    "s_add_u32 s85, s85, %[sog]\n\t" \
    "s_mov_b32 s81, 256\n\t" \
    "v_xor_b32 v1, 32, v246\n\t" \
    "v_xor_b32 v2, 64, v246\n\t" \
    "v_xor_b32 v3, 96, v246\n\t" \
    ".Lc_t:\n\t" \
    "s_memrealtime s[86:87]\n\t" \
    "s_sub_u32 s81, s81, 1\n\t" \
    "s_cbranch_scc1 .Lc_go\n\t" \
    "s_waitcnt lgkmcnt(0)\n\t" \
    "s_sub_u32 s86, %[t3], s86\n\t" \
    "s_cmp_lt_i32 s86, 5\n\t" \
    "s_cbranch_scc1 .Lc_go\n\t" \
    "s_cmp_lt_i32 s86, 13\n\t" \
    "s_cbranch_scc1 .Lc_t\n\t" \
    "s_sleep 2\n\t" \
    "s_branch .Lc_t\n\t" \
    ".Lc_go:\n\t" \
    "buffer_load_dwordx2 v[160:161], v231, %[rsb], s85 offen sc1\n\t" \
    "buffer_load_dwordx4 v[32:35], v240, %[rsb], s80 offen offset:2048 sc1\n\t" \
    "buffer_load_dwordx4 v[36:39], v240, %[rsb], s80 offen offset:3072 sc1\n\t" \
    "buffer_load_dwordx4 v[40:43], v241, %[rsb], s80 offen sc1\n\t" \
    "buffer_load_dwordx4 v[44:47], v241, %[rsb], s80 offen offset:1024 sc1\n\t" \
    "buffer_load_dwordx4 v[48:51], v241, %[rsb], s80 offen offset:2048 sc1\n\t" \
    "buffer_load_dwordx4 v[52:55], v241, %[rsb], s80 offen offset:3072 sc1\n\t" \
    "buffer_load_dwordx4 v[56:59], v242, %[rsb], s80 offen sc1\n\t" \
    "buffer_load_dwordx4 v[60:63], v242, %[rsb], s80 offen offset:1024 sc1\n\t" \
    "buffer_load_dwordx4 v[64:67], v242, %[rsb], s80 offen offset:2048 sc1\n\t" \
    "buffer_load_dwordx4 v[68:71], v242, %[rsb], s80 offen offset:3072 sc1\n\t" \
    "buffer_load_dwordx4 v[72:75], v243, %[rsb], s80 offen sc1\n\t" \
    "buffer_load_dwordx4 v[76:79], v243, %[rsb], s80 offen offset:1024 sc1\n\t" \
    "buffer_load_dwordx4 v[80:83], v243, %[rsb], s80 offen offset:2048 sc1\n\t" \
    "buffer_load_dwordx4 v[84:87], v243, %[rsb], s80 offen offset:3072 sc1\n\t" \
    "s_add_u32 s80, s80, 0x4000\n\t" \
    "s_and_b32 s80, s80, 0xc000\n\t" \
    "ds_read_b64 v[196:197], v246\n\t" \
    "ds_read_b64 v[200:201], v1\n\t" \
    "ds_read_b64 v[204:205], v2\n\t" \
    "ds_read_b64 v[208:209], v3\n\t" \
    "ds_read_b64 v[212:213], v246 offset:2048\n\t" \
    "ds_read_b64 v[216:217], v1 offset:2048\n\t" \
    "ds_read_b64 v[220:221], v2 offset:2048\n\t" \
    "ds_read_b64 v[224:225], v3 offset:2048\n\t" \
    "ds_read_b64_tr_b16 v[96:97], v226\n\t" \
    "ds_read_b64_tr_b16 v[98:99], v226 offset:2048\n\t" \
    "ds_read_b64_tr_b16 v[100:101], v227\n\t" \
    "ds_read_b64_tr_b16 v[102:103], v227 offset:2048\n\t" \
    "ds_read_b64_tr_b16 v[104:105], v228\n\t" \
    "ds_read_b64_tr_b16 v[106:107], v228 offset:2048\n\t" \
    "ds_read_b64_tr_b16 v[108:109], v229\n\t" \
    "ds_read_b64_tr_b16 v[110:111], v229 offset:2048\n\t" \
    "ds_read_b64_tr_b16 v[112:113], v226 offset:4096\n\t" \
    "ds_read_b64_tr_b16 v[114:115], v226 offset:6144\n\t" \
    "ds_read_b64_tr_b16 v[116:117], v227 offset:4096\n\t" \
    "ds_read_b64_tr_b16 v[118:119], v227 offset:6144\n\t" \
    "ds_read_b64_tr_b16 v[120:121], v228 offset:4096\n\t" \
    "ds_read_b64_tr_b16 v[122:123], v228 offset:6144\n\t" \
    "ds_read_b64_tr_b16 v[124:125], v229 offset:4096\n\t" \
    "ds_read_b64_tr_b16 v[126:127], v229 offset:6144\n\t" \
    "v_mov_b32 v0, 0\n\t" \
    "v_mov_b32 v1, 0\n\t" \
    "v_mov_b32 v2, 0\n\t" \
    "v_mov_b32 v3, 0\n\t" \
    "v_mov_b32 v4, 0\n\t" \
    "v_mov_b32 v5, 0\n\t" \
    "v_mov_b32 v6, 0\n\t" \
    "v_mov_b32 v7, 0\n\t" \
    "v_mov_b32 v8, 0\n\t" \
    "v_mov_b32 v9, 0\n\t" \
    "v_mov_b32 v10, 0\n\t" \
    "v_mov_b32 v11, 0\n\t" \
    "v_mov_b32 v12, 0\n\t" \
    "v_mov_b32 v13, 0\n\t" \
    "v_mov_b32 v14, 0\n\t" \
    "v_mov_b32 v15, 0\n\t" \
    "v_mov_b32 v16, 0\n\t" \
    "v_mov_b32 v17, 0\n\t" \
    "v_mov_b32 v18, 0\n\t" \
    "v_mov_b32 v19, 0\n\t" \
    "v_mov_b32 v20, 0\n\t" \
    "v_mov_b32 v21, 0\n\t" \
    "v_mov_b32 v22, 0\n\t" \
    "v_mov_b32 v23, 0\n\t" \
    "v_mov_b32 v24, 0\n\t" \
    "v_mov_b32 v25, 0\n\t" \
    "v_mov_b32 v26, 0\n\t" \
    "v_mov_b32 v27, 0\n\t" \
    "v_mov_b32 v28, 0\n\t" \
    "v_mov_b32 v29, 0\n\t" \
    "v_mov_b32 v30, 0\n\t" \
    "v_mov_b32 v31, 0\n\t" \
    "s_waitcnt lgkmcnt(0)\n\t" \
    "v_lshlrev_b32 v194, 16, v196\n\t" \
    "v_and_b32 v195, 0xffff0000, v196\n\t" \
    "v_lshlrev_b32 v196, 16, v197\n\t" \
    "v_and_b32 v197, 0xffff0000, v197\n\t" \
    "v_lshlrev_b32 v198, 16, v200\n\t" \
    "v_and_b32 v199, 0xffff0000, v200\n\t" \
    "v_lshlrev_b32 v200, 16, v201\n\t" \
    "v_and_b32 v201, 0xffff0000, v201\n\t" \
    "v_lshlrev_b32 v202, 16, v204\n\t" \
    "v_and_b32 v203, 0xffff0000, v204\n\t" \
    "v_lshlrev_b32 v204, 16, v205\n\t" \
    "v_and_b32 v205, 0xffff0000, v205\n\t" \
    "v_lshlrev_b32 v206, 16, v208\n\t" \
    "v_and_b32 v207, 0xffff0000, v208\n\t" \
    "v_lshlrev_b32 v208, 16, v209\n\t" \
    "v_and_b32 v209, 0xffff0000, v209\n\t" \
    "v_lshlrev_b32 v210, 16, v212\n\t" \
    "v_and_b32 v211, 0xffff0000, v212\n\t" \
    "v_lshlrev_b32 v212, 16, v213\n\t" \
    "v_and_b32 v213, 0xffff0000, v213\n\t" \
    "v_lshlrev_b32 v214, 16, v216\n\t" \
    "v_and_b32 v215, 0xffff0000, v216\n\t" \
    "v_lshlrev_b32 v216, 16, v217\n\t" \
    "v_and_b32 v217, 0xffff0000, v217\n\t" \
    "v_lshlrev_b32 v218, 16, v220\n\t" \
    "v_and_b32 v219, 0xffff0000, v220\n\t" \
    "v_lshlrev_b32 v220, 16, v221\n\t" \
    "v_and_b32 v221, 0xffff0000, v221\n\t" \
    "v_lshlrev_b32 v222, 16, v224\n\t" \
    "v_and_b32 v223, 0xffff0000, v224\n\t" \
    "v_lshlrev_b32 v224, 16, v225\n\t" \
    "v_and_b32 v225, 0xffff0000, v225\n\t" \
    "v_pk_mul_f32 v[194:195], v[194:195], v[254:255] op_sel_hi:[1,0]\n\t" \
    "v_pk_mul_f32 v[196:197], v[196:197], v[254:255] op_sel_hi:[1,0]\n\t" \
    "v_pk_mul_f32 v[198:199], v[198:199], v[254:255] op_sel_hi:[1,0]\n\t" \
    "v_pk_mul_f32 v[200:201], v[200:201], v[254:255] op_sel_hi:[1,0]\n\t" \
    "v_pk_mul_f32 v[202:203], v[202:203], v[254:255] op_sel_hi:[1,0]\n\t" \
    "v_pk_mul_f32 v[204:205], v[204:205], v[254:255] op_sel_hi:[1,0]\n\t" \
    "v_pk_mul_f32 v[206:207], v[206:207], v[254:255] op_sel_hi:[1,0]\n\t" \
    "v_pk_mul_f32 v[208:209], v[208:209], v[254:255] op_sel_hi:[1,0]\n\t" \
    "v_pk_mul_f32 v[210:211], v[210:211], v[254:255] op_sel:[0,1] op_sel_hi:[1,1]\n\t" \
    "v_pk_mul_f32 v[212:213], v[212:213], v[254:255] op_sel:[0,1] op_sel_hi:[1,1]\n\t" \
    "v_pk_mul_f32 v[214:215], v[214:215], v[254:255] op_sel:[0,1] op_sel_hi:[1,1]\n\t" \
    "v_pk_mul_f32 v[216:217], v[216:217], v[254:255] op_sel:[0,1] op_sel_hi:[1,1]\n\t" \
    "v_pk_mul_f32 v[218:219], v[218:219], v[254:255] op_sel:[0,1] op_sel_hi:[1,1]\n\t" \
    "v_pk_mul_f32 v[220:221], v[220:221], v[254:255] op_sel:[0,1] op_sel_hi:[1,1]\n\t" \
    "v_pk_mul_f32 v[222:223], v[222:223], v[254:255] op_sel:[0,1] op_sel_hi:[1,1]\n\t" \
    "v_pk_mul_f32 v[224:225], v[224:225], v[254:255] op_sel:[0,1] op_sel_hi:[1,1]\n\t" \
    "v_mov_b32 v230, 0\n\t" \
    "s_mov_b32 s81, 2\n\t" \
    "s_mov_b32 s82, 1\n\t" \
    "s_mov_b32 %[st], 1\n\t" \
    "s_waitcnt vmcnt(14)\n\t" \
    "v_cmp_ne_u32 vcc, v236, v161\n\t" \
    "s_nop 1\n\t" \
    "s_and_b32 vcc_lo, vcc_lo, s84\n\t" \
    "s_cbranch_scc1 .Lc_x\n\t" \
    ".Lc_l:\n\t" \
    "s_waitcnt vmcnt(12) lgkmcnt(8)\n\t" \
    "v_mfma_f32_16x16x32_bf16 v[0:3], v[96:99], v[32:35], v[0:3]\n\t" \
    "ds_read_b64_tr_b16 v[128:129], v226 offset:8192\n\t" \
    "buffer_load_dwordx4 v[88:91], v240, %[rsb], s80 offen sc1\n\t" \
    "v_mfma_f32_16x16x32_bf16 v[16:19], v[96:99], v[36:39], v[16:19]\n\t" \
    "ds_read_b64_tr_b16 v[130:131], v226 offset:10240\n\t" \
    "v_add3_u32 v230, v32, v33, v230\n\t" \
    "v_mfma_f32_16x16x32_bf16 v[4:7], v[100:103], v[32:35], v[4:7]\n\t" \
    "ds_read_b64_tr_b16 v[132:133], v227 offset:8192\n\t" \
    "buffer_load_dwordx4 v[92:95], v240, %[rsb], s80 offen offset:1024 sc1\n\t" \
    "v_mfma_f32_16x16x32_bf16 v[20:23], v[100:103], v[36:39], v[20:23]\n\t" \
    "ds_read_b64_tr_b16 v[134:135], v227 offset:10240\n\t" \
    "v_add3_u32 v230, v34, v35, v230\n\t" \
    "v_mfma_f32_16x16x32_bf16 v[8:11], v[104:107], v[32:35], v[8:11]\n\t" \
    "ds_read_b64_tr_b16 v[136:137], v228 offset:8192\n\t" \
    "v_add3_u32 v230, v36, v37, v230\n\t" \
    "v_mfma_f32_16x16x32_bf16 v[24:27], v[104:107], v[36:39], v[24:27]\n\t" \
    "ds_read_b64_tr_b16 v[138:139], v228 offset:10240\n\t" \
    "v_add3_u32 v230, v38, v39, v230\n\t" \
    "v_mfma_f32_16x16x32_bf16 v[12:15], v[108:111], v[32:35], v[12:15]\n\t" \
    "ds_read_b64_tr_b16 v[140:141], v229 offset:8192\n\t" \
    "v_mfma_f32_16x16x32_bf16 v[28:31], v[108:111], v[36:39], v[28:31]\n\t" \
    "ds_read_b64_tr_b16 v[142:143], v229 offset:10240\n\t" \
    "s_waitcnt vmcnt(12) lgkmcnt(8)\n\t" \
    "v_mfma_f32_16x16x32_bf16 v[0:3], v[112:115], v[40:43], v[0:3]\n\t" \
    "ds_read_b64_tr_b16 v[144:145], v226 offset:12288\n\t" \
    "buffer_load_dwordx4 v[32:35], v240, %[rsb], s80 offen offset:2048 sc1\n\t" \
    "v_mfma_f32_16x16x32_bf16 v[16:19], v[112:115], v[44:47], v[16:19]\n\t" \
    "ds_read_b64_tr_b16 v[146:147], v226 offset:14336\n\t" \
    "v_add3_u32 v230, v40, v41, v230\n\t" \
    "v_mfma_f32_16x16x32_bf16 v[4:7], v[116:119], v[40:43], v[4:7]\n\t" \
    "ds_read_b64_tr_b16 v[148:149], v227 offset:12288\n\t" \
    "buffer_load_dwordx4 v[36:39], v240, %[rsb], s80 offen offset:3072 sc1\n\t" \
    "v_mfma_f32_16x16x32_bf16 v[20:23], v[116:119], v[44:47], v[20:23]\n\t" \
    "ds_read_b64_tr_b16 v[150:151], v227 offset:14336\n\t" \
    "v_add3_u32 v230, v42, v43, v230\n\t" \
    "v_mfma_f32_16x16x32_bf16 v[8:11], v[120:123], v[40:43], v[8:11]\n\t" \
    "ds_read_b64_tr_b16 v[152:153], v228 offset:12288\n\t" \
    "v_add3_u32 v230, v44, v45, v230\n\t" \
    "v_mfma_f32_16x16x32_bf16 v[24:27], v[120:123], v[44:47], v[24:27]\n\t" \
    "ds_read_b64_tr_b16 v[154:155], v228 offset:14336\n\t" \
    "v_add3_u32 v230, v46, v47, v230\n\t" \
    "v_mfma_f32_16x16x32_bf16 v[12:15], v[124:127], v[40:43], v[12:15]\n\t" \
    "ds_read_b64_tr_b16 v[156:157], v229 offset:12288\n\t" \
    "v_mfma_f32_16x16x32_bf16 v[28:31], v[124:127], v[44:47], v[28:31]\n\t" \
    "ds_read_b64_tr_b16 v[158:159], v229 offset:14336\n\t" \
    "s_waitcnt vmcnt(12) lgkmcnt(8)\n\t" \
    "v_mfma_f32_16x16x32_bf16 v[0:3], v[128:131], v[48:51], v[0:3]\n\t" \
    "ds_read_b64_tr_b16 v[96:97], v226 offset:16384\n\t" \
    "buffer_load_dwordx4 v[40:43], v241, %[rsb], s80 offen sc1\n\t" \
    "v_mfma_f32_16x16x32_bf16 v[16:19], v[128:131], v[52:55], v[16:19]\n\t" \
    "ds_read_b64_tr_b16 v[98:99], v226 offset:18432\n\t" \
    "v_add3_u32 v230, v48, v49, v230\n\t" \
    "v_mfma_f32_16x16x32_bf16 v[4:7], v[132:135], v[48:51], v[4:7]\n\t" \
    "ds_read_b64_tr_b16 v[100:101], v227 offset:16384\n\t" \
    "buffer_load_dwordx4 v[44:47], v241, %[rsb], s80 offen offset:1024 sc1\n\t" \
    "v_mfma_f32_16x16x32_bf16 v[20:23], v[132:135], v[52:55], v[20:23]\n\t" \
    "ds_read_b64_tr_b16 v[102:103], v227 offset:18432\n\t" \
    "v_add3_u32 v230, v50, v51, v230\n\t" \
    "v_mfma_f32_16x16x32_bf16 v[8:11], v[136:139], v[48:51], v[8:11]\n\t" \
    "ds_read_b64_tr_b16 v[104:105], v228 offset:16384\n\t" \
    "v_add3_u32 v230, v52, v53, v230\n\t" \
    "v_mfma_f32_16x16x32_bf16 v[24:27], v[136:139], v[52:55], v[24:27]\n\t" \
    "ds_read_b64_tr_b16 v[106:107], v228 offset:18432\n\t" \
    "v_add3_u32 v230, v54, v55, v230\n\t" \
    "v_mfma_f32_16x16x32_bf16 v[12:15], v[140:143], v[48:51], v[12:15]\n\t" \
    "ds_read_b64_tr_b16 v[108:109], v229 offset:16384\n\t" \
    "v_mfma_f32_16x16x32_bf16 v[28:31], v[140:143], v[52:55], v[28:31]\n\t" \
    "ds_read_b64_tr_b16 v[110:111], v229 offset:18432\n\t" \
    "s_waitcnt vmcnt(12) lgkmcnt(8)\n\t" \
    "v_mfma_f32_16x16x32_bf16 v[0:3], v[144:147], v[56:59], v[0:3]\n\t" \
    "ds_read_b64_tr_b16 v[112:113], v226 offset:20480\n\t" \
    "buffer_load_dwordx4 v[48:51], v241, %[rsb], s80 offen offset:2048 sc1\n\t" \
    "v_mfma_f32_16x16x32_bf16 v[16:19], v[144:147], v[60:63], v[16:19]\n\t" \
    "ds_read_b64_tr_b16 v[114:115], v226 offset:22528\n\t" \
    "v_add3_u32 v230, v56, v57, v230\n\t" \
    "v_mfma_f32_16x16x32_bf16 v[4:7], v[148:151], v[56:59], v[4:7]\n\t" \
    "ds_read_b64_tr_b16 v[116:117], v227 offset:20480\n\t" \
    "buffer_load_dwordx4 v[52:55], v241, %[rsb], s80 offen offset:3072 sc1\n\t" \
    "v_mfma_f32_16x16x32_bf16 v[20:23], v[148:151], v[60:63], v[20:23]\n\t" \
    "ds_read_b64_tr_b16 v[118:119], v227 offset:22528\n\t" \
    "v_add3_u32 v230, v58, v59, v230\n\t" \
    "v_mfma_f32_16x16x32_bf16 v[8:11], v[152:155], v[56:59], v[8:11]\n\t" \
    "ds_read_b64_tr_b16 v[120:121], v228 offset:20480\n\t" \
    "v_add3_u32 v230, v60, v61, v230\n\t" \
    "v_mfma_f32_16x16x32_bf16 v[24:27], v[152:155], v[60:63], v[24:27]\n\t" \
    "ds_read_b64_tr_b16 v[122:123], v228 offset:22528\n\t" \
    "v_add3_u32 v230, v62, v63, v230\n\t" \
    "v_mfma_f32_16x16x32_bf16 v[12:15], v[156:159], v[56:59], v[12:15]\n\t" \
    "ds_read_b64_tr_b16 v[124:125], v229 offset:20480\n\t" \
    "v_mfma_f32_16x16x32_bf16 v[28:31], v[156:159], v[60:63], v[28:31]\n\t" \
    "ds_read_b64_tr_b16 v[126:127], v229 offset:22528\n\t" \
    "s_waitcnt vmcnt(12) lgkmcnt(8)\n\t" \
    "ds_read_b32 v160, v249 offset:56\n\t" \
    "v_mfma_f32_16x16x32_bf16 v[0:3], v[96:99], v[64:67], v[0:3]\n\t" \
    "ds_read_b64_tr_b16 v[128:129], v226 offset:24576\n\t" \
    "buffer_load_dwordx4 v[56:59], v242, %[rsb], s80 offen sc1\n\t" \
    "v_mfma_f32_16x16x32_bf16 v[16:19], v[96:99], v[68:71], v[16:19]\n\t" \
    "ds_read_b64_tr_b16 v[130:131], v226 offset:26624\n\t" \
    "v_add3_u32 v230, v64, v65, v230\n\t" \
    "v_mfma_f32_16x16x32_bf16 v[4:7], v[100:103], v[64:67], v[4:7]\n\t" \
    "ds_read_b64_tr_b16 v[132:133], v227 offset:24576\n\t" \
    "buffer_load_dwordx4 v[60:63], v242, %[rsb], s80 offen offset:1024 sc1\n\t" \
    "v_mfma_f32_16x16x32_bf16 v[20:23], v[100:103], v[68:71], v[20:23]\n\t" \
    "ds_read_b64_tr_b16 v[134:135], v227 offset:26624\n\t" \
    "v_add3_u32 v230, v66, v67, v230\n\t" \
    "v_mfma_f32_16x16x32_bf16 v[8:11], v[104:107], v[64:67], v[8:11]\n\t" \
    "ds_read_b64_tr_b16 v[136:137], v228 offset:24576\n\t" \
    "v_add3_u32 v230, v68, v69, v230\n\t" \
    "v_mfma_f32_16x16x32_bf16 v[24:27], v[104:107], v[68:71], v[24:27]\n\t" \
    "ds_read_b64_tr_b16 v[138:139], v228 offset:26624\n\t" \
    "v_add3_u32 v230, v70, v71, v230\n\t" \
    "v_mfma_f32_16x16x32_bf16 v[12:15], v[108:111], v[64:67], v[12:15]\n\t" \
    "ds_read_b64_tr_b16 v[140:141], v229 offset:24576\n\t" \
    "v_mfma_f32_16x16x32_bf16 v[28:31], v[108:111], v[68:71], v[28:31]\n\t" \
    "ds_read_b64_tr_b16 v[142:143], v229 offset:26624\n\t" \
    "s_waitcnt vmcnt(12) lgkmcnt(8)\n\t" \
    "v_mfma_f32_16x16x32_bf16 v[0:3], v[112:115], v[72:75], v[0:3]\n\t" \
    "ds_read_b64_tr_b16 v[144:145], v226 offset:28672\n\t" \
    "buffer_load_dwordx4 v[64:67], v242, %[rsb], s80 offen offset:2048 sc1\n\t" \
    "v_mfma_f32_16x16x32_bf16 v[16:19], v[112:115], v[76:79], v[16:19]\n\t" \
    "ds_read_b64_tr_b16 v[146:147], v226 offset:30720\n\t" \
    "v_add3_u32 v230, v72, v73, v230\n\t" \
    "v_mfma_f32_16x16x32_bf16 v[4:7], v[116:119], v[72:75], v[4:7]\n\t" \
    "ds_read_b64_tr_b16 v[148:149], v227 offset:28672\n\t" \
    "buffer_load_dwordx4 v[68:71], v242, %[rsb], s80 offen offset:3072 sc1\n\t" \
    "v_add_u32 v226, 0x8000, v226\n\t" \
    "v_and_b32 v226, 0x1ffff, v226\n\t" \
    "v_mfma_f32_16x16x32_bf16 v[20:23], v[116:119], v[76:79], v[20:23]\n\t" \
    "ds_read_b64_tr_b16 v[150:151], v227 offset:30720\n\t" \
    "v_add3_u32 v230, v74, v75, v230\n\t" \
    "v_mfma_f32_16x16x32_bf16 v[8:11], v[120:123], v[72:75], v[8:11]\n\t" \
    "ds_read_b64_tr_b16 v[152:153], v228 offset:28672\n\t" \
    "v_add3_u32 v230, v76, v77, v230\n\t" \
    "v_add_u32 v227, 0x8000, v227\n\t" \
    "v_and_b32 v227, 0x1ffff, v227\n\t" \
    "v_mfma_f32_16x16x32_bf16 v[24:27], v[120:123], v[76:79], v[24:27]\n\t" \
    "ds_read_b64_tr_b16 v[154:155], v228 offset:30720\n\t" \
    "v_add3_u32 v230, v78, v79, v230\n\t" \
    "v_mfma_f32_16x16x32_bf16 v[12:15], v[124:127], v[72:75], v[12:15]\n\t" \
    "v_readfirstlane_b32 s83, v160\n\t" \
    "s_add_u32 s82, s82, 1\n\t" \
    "ds_read_b64_tr_b16 v[156:157], v229 offset:28672\n\t" \
    "v_add_u32 v228, 0x8000, v228\n\t" \
    "v_and_b32 v228, 0x1ffff, v228\n\t" \
    "v_mfma_f32_16x16x32_bf16 v[28:31], v[124:127], v[76:79], v[28:31]\n\t" \
    "s_cmp_lt_u32 s83, s82\n\t" \
    "s_cbranch_scc1 .Lc_w0\n\t" \
    ".Lc_r0:\n\t" \
    "ds_read_b64_tr_b16 v[158:159], v229 offset:30720\n\t" \
    "v_add_u32 v229, 0x8000, v229\n\t" \
    "v_and_b32 v229, 0x1ffff, v229\n\t" \
    "s_waitcnt vmcnt(12) lgkmcnt(8)\n\t" \
    "v_mfma_f32_16x16x32_bf16 v[0:3], v[128:131], v[80:83], v[0:3]\n\t" \
    "ds_read_b64_tr_b16 v[96:97], v226\n\t" \
    "buffer_load_dwordx4 v[72:75], v243, %[rsb], s80 offen sc1\n\t" \
    "v_mfma_f32_16x16x32_bf16 v[16:19], v[128:131], v[84:87], v[16:19]\n\t" \
    "ds_read_b64_tr_b16 v[98:99], v226 offset:2048\n\t" \
    "v_add3_u32 v230, v80, v81, v230\n\t" \
    "v_mfma_f32_16x16x32_bf16 v[4:7], v[132:135], v[80:83], v[4:7]\n\t" \
    "ds_read_b64_tr_b16 v[100:101], v227\n\t" \
    "buffer_load_dwordx4 v[76:79], v243, %[rsb], s80 offen offset:1024 sc1\n\t" \
    "v_mfma_f32_16x16x32_bf16 v[20:23], v[132:135], v[84:87], v[20:23]\n\t" \
    "ds_read_b64_tr_b16 v[102:103], v227 offset:2048\n\t" \
    "v_add3_u32 v230, v82, v83, v230\n\t" \
    "v_mfma_f32_16x16x32_bf16 v[8:11], v[136:139], v[80:83], v[8:11]\n\t" \
    "ds_read_b64_tr_b16 v[104:105], v228\n\t" \
    "v_add3_u32 v230, v84, v85, v230\n\t" \
    "v_mfma_f32_16x16x32_bf16 v[24:27], v[136:139], v[84:87], v[24:27]\n\t" \
    "ds_read_b64_tr_b16 v[106:107], v228 offset:2048\n\t" \
    "v_add3_u32 v230, v86, v87, v230\n\t" \
    "v_mfma_f32_16x16x32_bf16 v[12:15], v[140:143], v[80:83], v[12:15]\n\t" \
    "ds_read_b64_tr_b16 v[108:109], v229\n\t" \
    "v_mfma_f32_16x16x32_bf16 v[28:31], v[140:143], v[84:87], v[28:31]\n\t" \
    "ds_read_b64_tr_b16 v[110:111], v229 offset:2048\n\t" \
    "s_waitcnt vmcnt(12) lgkmcnt(8)\n\t" \
    "v_mfma_f32_16x16x32_bf16 v[0:3], v[144:147], v[88:91], v[0:3]\n\t" \
    "ds_read_b64_tr_b16 v[112:113], v226 offset:4096\n\t" \
    "buffer_load_dwordx4 v[80:83], v243, %[rsb], s80 offen offset:2048 sc1\n\t" \
    "v_mfma_f32_16x16x32_bf16 v[16:19], v[144:147], v[92:95], v[16:19]\n\t" \
    "ds_read_b64_tr_b16 v[114:115], v226 offset:6144\n\t" \
    "v_add3_u32 v230, v88, v89, v230\n\t" \
    "v_mfma_f32_16x16x32_bf16 v[4:7], v[148:151], v[88:91], v[4:7]\n\t" \
    "ds_read_b64_tr_b16 v[116:117], v227 offset:4096\n\t" \
    "buffer_load_dwordx4 v[84:87], v243, %[rsb], s80 offen offset:3072 sc1\n\t" \
    "v_mfma_f32_16x16x32_bf16 v[20:23], v[148:151], v[92:95], v[20:23]\n\t" \
    "ds_read_b64_tr_b16 v[118:119], v227 offset:6144\n\t" \
    "v_add3_u32 v230, v90, v91, v230\n\t" \
    "v_mfma_f32_16x16x32_bf16 v[8:11], v[152:155], v[88:91], v[8:11]\n\t" \
    "ds_read_b64_tr_b16 v[120:121], v228 offset:4096\n\t" \
    "v_add3_u32 v230, v92, v93, v230\n\t" \
    "v_mfma_f32_16x16x32_bf16 v[24:27], v[152:155], v[92:95], v[24:27]\n\t" \
    "ds_read_b64_tr_b16 v[122:123], v228 offset:6144\n\t" \
    "v_add3_u32 v230, v94, v95, v230\n\t" \
    "v_mfma_f32_16x16x32_bf16 v[12:15], v[156:159], v[88:91], v[12:15]\n\t" \
    "ds_read_b64_tr_b16 v[124:125], v229 offset:4096\n\t" \
    "s_add_u32 s80, s80, 0x4000\n\t" \
    "s_and_b32 s80, s80, 0xc000\n\t" \
    "v_mfma_f32_16x16x32_bf16 v[28:31], v[156:159], v[92:95], v[28:31]\n\t" \
    "ds_read_b64_tr_b16 v[126:127], v229 offset:6144\n\t" \
    "s_sub_u32 s81, s81, 1\n\t" \
    "s_cmp_lg_u32 s81, 0\n\t" \
    "s_cbranch_scc1 .Lc_l\n\t" \
    "s_waitcnt vmcnt(12) lgkmcnt(8)\n\t" \
    "v_mfma_f32_16x16x32_bf16 v[162:165], v[96:99], v[32:35], 0\n\t" \
    "ds_read_b64_tr_b16 v[128:129], v226 offset:8192\n\t" \
    "buffer_load_dwordx4 v[88:91], v240, %[rsb], s80 offen sc1\n\t" \
    "v_mfma_f32_16x16x32_bf16 v[178:181], v[96:99], v[36:39], 0\n\t" \
    "ds_read_b64_tr_b16 v[130:131], v226 offset:10240\n\t" \
    "v_add3_u32 v230, v32, v33, v230\n\t" \
    "v_mfma_f32_16x16x32_bf16 v[166:169], v[100:103], v[32:35], 0\n\t" \
    "ds_read_b64_tr_b16 v[132:133], v227 offset:8192\n\t" \
    "buffer_load_dwordx4 v[92:95], v240, %[rsb], s80 offen offset:1024 sc1\n\t" \
    "v_mfma_f32_16x16x32_bf16 v[182:185], v[100:103], v[36:39], 0\n\t" \
    "ds_read_b64_tr_b16 v[134:135], v227 offset:10240\n\t" \
    "v_add3_u32 v230, v34, v35, v230\n\t" \
    "v_mfma_f32_16x16x32_bf16 v[170:173], v[104:107], v[32:35], 0\n\t" \
    "ds_read_b64_tr_b16 v[136:137], v228 offset:8192\n\t" \
    "v_add3_u32 v230, v36, v37, v230\n\t" \
    "v_mfma_f32_16x16x32_bf16 v[186:189], v[104:107], v[36:39], 0\n\t" \
    "ds_read_b64_tr_b16 v[138:139], v228 offset:10240\n\t" \
    "v_add3_u32 v230, v38, v39, v230\n\t" \
    "v_mfma_f32_16x16x32_bf16 v[174:177], v[108:111], v[32:35], 0\n\t" \
    "ds_read_b64_tr_b16 v[140:141], v229 offset:8192\n\t" \
    "v_mfma_f32_16x16x32_bf16 v[190:193], v[108:111], v[36:39], 0\n\t" \
    "ds_read_b64_tr_b16 v[142:143], v229 offset:10240\n\t" \
    "s_waitcnt vmcnt(12) lgkmcnt(8)\n\t" \
    "v_mfma_f32_16x16x32_bf16 v[162:165], v[112:115], v[40:43], v[162:165]\n\t" \
    "ds_read_b64_tr_b16 v[144:145], v226 offset:12288\n\t" \
    "buffer_load_dwordx4 v[32:35], v240, %[rsb], s80 offen offset:2048 sc1\n\t" \
    "v_mfma_f32_16x16x32_bf16 v[178:181], v[112:115], v[44:47], v[178:181]\n\t" \
    "ds_read_b64_tr_b16 v[146:147], v226 offset:14336\n\t" \
    "v_add3_u32 v230, v40, v41, v230\n\t" \
    "v_mfma_f32_16x16x32_bf16 v[166:169], v[116:119], v[40:43], v[166:169]\n\t" \
    "ds_read_b64_tr_b16 v[148:149], v227 offset:12288\n\t" \
    "buffer_load_dwordx4 v[36:39], v240, %[rsb], s80 offen offset:3072 sc1\n\t" \
    "v_mfma_f32_16x16x32_bf16 v[182:185], v[116:119], v[44:47], v[182:185]\n\t" \
    "ds_read_b64_tr_b16 v[150:151], v227 offset:14336\n\t" \
    "v_add3_u32 v230, v42, v43, v230\n\t" \
    "v_mfma_f32_16x16x32_bf16 v[170:173], v[120:123], v[40:43], v[170:173]\n\t" \
    "ds_read_b64_tr_b16 v[152:153], v228 offset:12288\n\t" \
    "v_add3_u32 v230, v44, v45, v230\n\t" \
    "v_mfma_f32_16x16x32_bf16 v[186:189], v[120:123], v[44:47], v[186:189]\n\t" \
    "ds_read_b64_tr_b16 v[154:155], v228 offset:14336\n\t" \
    "v_add3_u32 v230, v46, v47, v230\n\t" \
    "v_mfma_f32_16x16x32_bf16 v[174:177], v[124:127], v[40:43], v[174:177]\n\t" \
    "ds_read_b64_tr_b16 v[156:157], v229 offset:12288\n\t" \
    "v_mfma_f32_16x16x32_bf16 v[190:193], v[124:127], v[44:47], v[190:193]\n\t" \
    "ds_read_b64_tr_b16 v[158:159], v229 offset:14336\n\t" \
    "s_waitcnt vmcnt(12) lgkmcnt(8)\n\t" \
    "v_mfma_f32_16x16x32_bf16 v[162:165], v[128:131], v[48:51], v[162:165]\n\t" \
    "ds_read_b64_tr_b16 v[96:97], v226 offset:16384\n\t" \
    "buffer_load_dwordx4 v[40:43], v241, %[rsb], s80 offen sc1\n\t" \
    "v_mfma_f32_16x16x32_bf16 v[178:181], v[128:131], v[52:55], v[178:181]\n\t" \
    "ds_read_b64_tr_b16 v[98:99], v226 offset:18432\n\t" \
    "v_add3_u32 v230, v48, v49, v230\n\t" \
    "v_mfma_f32_16x16x32_bf16 v[166:169], v[132:135], v[48:51], v[166:169]\n\t" \
    "ds_read_b64_tr_b16 v[100:101], v227 offset:16384\n\t" \
    "buffer_load_dwordx4 v[44:47], v241, %[rsb], s80 offen offset:1024 sc1\n\t" \
    "v_mfma_f32_16x16x32_bf16 v[182:185], v[132:135], v[52:55], v[182:185]\n\t" \
    "ds_read_b64_tr_b16 v[102:103], v227 offset:18432\n\t" \
    "v_add3_u32 v230, v50, v51, v230\n\t" \
    "v_mfma_f32_16x16x32_bf16 v[170:173], v[136:139], v[48:51], v[170:173]\n\t" \
    "ds_read_b64_tr_b16 v[104:105], v228 offset:16384\n\t" \
    "v_add3_u32 v230, v52, v53, v230\n\t" \
    "v_mfma_f32_16x16x32_bf16 v[186:189], v[136:139], v[52:55], v[186:189]\n\t" \
    "ds_read_b64_tr_b16 v[106:107], v228 offset:18432\n\t" \
    "v_add3_u32 v230, v54, v55, v230\n\t" \
    "v_mfma_f32_16x16x32_bf16 v[174:177], v[140:143], v[48:51], v[174:177]\n\t" \
    "ds_read_b64_tr_b16 v[108:109], v229 offset:16384\n\t" \
    "v_mfma_f32_16x16x32_bf16 v[190:193], v[140:143], v[52:55], v[190:193]\n\t" \
    "ds_read_b64_tr_b16 v[110:111], v229 offset:18432\n\t" \
    "s_waitcnt vmcnt(12) lgkmcnt(8)\n\t" \
    "v_mfma_f32_16x16x32_bf16 v[162:165], v[144:147], v[56:59], v[162:165]\n\t" \
    "ds_read_b64_tr_b16 v[112:113], v226 offset:20480\n\t" \
    "buffer_load_dwordx4 v[48:51], v241, %[rsb], s80 offen offset:2048 sc1\n\t" \
    "v_mfma_f32_16x16x32_bf16 v[178:181], v[144:147], v[60:63], v[178:181]\n\t" \
    "ds_read_b64_tr_b16 v[114:115], v226 offset:22528\n\t" \
    "v_add3_u32 v230, v56, v57, v230\n\t" \
    "v_mfma_f32_16x16x32_bf16 v[166:169], v[148:151], v[56:59], v[166:169]\n\t" \
    "ds_read_b64_tr_b16 v[116:117], v227 offset:20480\n\t" \
    "buffer_load_dwordx4 v[52:55], v241, %[rsb], s80 offen offset:3072 sc1\n\t" \
    "v_mfma_f32_16x16x32_bf16 v[182:185], v[148:151], v[60:63], v[182:185]\n\t" \
    "ds_read_b64_tr_b16 v[118:119], v227 offset:22528\n\t" \
    "v_add3_u32 v230, v58, v59, v230\n\t" \
    "v_mfma_f32_16x16x32_bf16 v[170:173], v[152:155], v[56:59], v[170:173]\n\t" \
    "ds_read_b64_tr_b16 v[120:121], v228 offset:20480\n\t" \
    "v_add3_u32 v230, v60, v61, v230\n\t" \
    "v_mfma_f32_16x16x32_bf16 v[186:189], v[152:155], v[60:63], v[186:189]\n\t" \
    "ds_read_b64_tr_b16 v[122:123], v228 offset:22528\n\t" \
    "v_add3_u32 v230, v62, v63, v230\n\t" \
    "v_mfma_f32_16x16x32_bf16 v[174:177], v[156:159], v[56:59], v[174:177]\n\t" \
    "ds_read_b64_tr_b16 v[124:125], v229 offset:20480\n\t" \
    "v_mfma_f32_16x16x32_bf16 v[190:193], v[156:159], v[60:63], v[190:193]\n\t" \
    "ds_read_b64_tr_b16 v[126:127], v229 offset:22528\n\t" \
    "s_waitcnt vmcnt(12) lgkmcnt(8)\n\t" \
    "ds_read_b32 v160, v249 offset:56\n\t" \
    "v_mfma_f32_16x16x32_bf16 v[162:165], v[96:99], v[64:67], v[162:165]\n\t" \
    "ds_read_b64_tr_b16 v[128:129], v226 offset:24576\n\t" \
    "buffer_load_dwordx4 v[56:59], v242, %[rsb], s80 offen sc1\n\t" \
    "v_mfma_f32_16x16x32_bf16 v[178:181], v[96:99], v[68:71], v[178:181]\n\t" \
    "ds_read_b64_tr_b16 v[130:131], v226 offset:26624\n\t" \
    "v_add3_u32 v230, v64, v65, v230\n\t" \
    "v_mfma_f32_16x16x32_bf16 v[166:169], v[100:103], v[64:67], v[166:169]\n\t" \
    "ds_read_b64_tr_b16 v[132:133], v227 offset:24576\n\t" \
    "buffer_load_dwordx4 v[60:63], v242, %[rsb], s80 offen offset:1024 sc1\n\t" \
    "v_mfma_f32_16x16x32_bf16 v[182:185], v[100:103], v[68:71], v[182:185]\n\t" \
    "ds_read_b64_tr_b16 v[134:135], v227 offset:26624\n\t" \
    "v_add3_u32 v230, v66, v67, v230\n\t" \
    "v_mfma_f32_16x16x32_bf16 v[170:173], v[104:107], v[64:67], v[170:173]\n\t" \
    "ds_read_b64_tr_b16 v[136:137], v228 offset:24576\n\t" \
    "v_add3_u32 v230, v68, v69, v230\n\t" \
    "v_mfma_f32_16x16x32_bf16 v[186:189], v[104:107], v[68:71], v[186:189]\n\t" \
    "ds_read_b64_tr_b16 v[138:139], v228 offset:26624\n\t" \
    "v_add3_u32 v230, v70, v71, v230\n\t" \
    "v_mfma_f32_16x16x32_bf16 v[174:177], v[108:111], v[64:67], v[174:177]\n\t" \
    "ds_read_b64_tr_b16 v[140:141], v229 offset:24576\n\t" \
    "v_mfma_f32_16x16x32_bf16 v[190:193], v[108:111], v[68:71], v[190:193]\n\t" \
    "ds_read_b64_tr_b16 v[142:143], v229 offset:26624\n\t" \
    "s_waitcnt vmcnt(12) lgkmcnt(8)\n\t" \
    "v_mfma_f32_16x16x32_bf16 v[162:165], v[112:115], v[72:75], v[162:165]\n\t" \
    "ds_read_b64_tr_b16 v[144:145], v226 offset:28672\n\t" \
    "buffer_load_dwordx4 v[64:67], v242, %[rsb], s80 offen offset:2048 sc1\n\t" \
    "v_mfma_f32_16x16x32_bf16 v[178:181], v[112:115], v[76:79], v[178:181]\n\t" \
    "ds_read_b64_tr_b16 v[146:147], v226 offset:30720\n\t" \
    "v_add3_u32 v230, v72, v73, v230\n\t" \
    "v_mfma_f32_16x16x32_bf16 v[166:169], v[116:119], v[72:75], v[166:169]\n\t" \
    "ds_read_b64_tr_b16 v[148:149], v227 offset:28672\n\t" \
    "buffer_load_dwordx4 v[68:71], v242, %[rsb], s80 offen offset:3072 sc1\n\t" \
    "v_add_u32 v226, 0x8000, v226\n\t" \
    "v_and_b32 v226, 0x1ffff, v226\n\t" \
    "v_mfma_f32_16x16x32_bf16 v[182:185], v[116:119], v[76:79], v[182:185]\n\t" \
    "ds_read_b64_tr_b16 v[150:151], v227 offset:30720\n\t" \
    "v_add3_u32 v230, v74, v75, v230\n\t" \
    "v_mfma_f32_16x16x32_bf16 v[170:173], v[120:123], v[72:75], v[170:173]\n\t" \
    "ds_read_b64_tr_b16 v[152:153], v228 offset:28672\n\t" \
    "v_add3_u32 v230, v76, v77, v230\n\t" \
    "v_add_u32 v227, 0x8000, v227\n\t" \
    "v_and_b32 v227, 0x1ffff, v227\n\t" \
    "v_mfma_f32_16x16x32_bf16 v[186:189], v[120:123], v[76:79], v[186:189]\n\t" \
    "ds_read_b64_tr_b16 v[154:155], v228 offset:30720\n\t" \
    "v_add3_u32 v230, v78, v79, v230\n\t" \
    "v_mfma_f32_16x16x32_bf16 v[174:177], v[124:127], v[72:75], v[174:177]\n\t" \
    "v_readfirstlane_b32 s83, v160\n\t" \
    "s_add_u32 s82, s82, 1\n\t" \
    "ds_read_b64_tr_b16 v[156:157], v229 offset:28672\n\t" \
    "v_add_u32 v228, 0x8000, v228\n\t" \
    "v_and_b32 v228, 0x1ffff, v228\n\t" \
    "v_mfma_f32_16x16x32_bf16 v[190:193], v[124:127], v[76:79], v[190:193]\n\t" \
    "s_cmp_lt_u32 s83, s82\n\t" \
    "s_cbranch_scc1 .Lc_w1\n\t" \
    ".Lc_r1:\n\t" \
    "ds_read_b64_tr_b16 v[158:159], v229 offset:30720\n\t" \
    "v_add_u32 v229, 0x8000, v229\n\t" \
    "v_and_b32 v229, 0x1ffff, v229\n\t" \
    "s_waitcnt vmcnt(12) lgkmcnt(8)\n\t" \
    "v_mfma_f32_16x16x32_bf16 v[162:165], v[128:131], v[80:83], v[162:165]\n\t" \
    "ds_read_b64_tr_b16 v[96:97], v226\n\t" \
    "buffer_load_dwordx4 v[72:75], v243, %[rsb], s80 offen sc1\n\t" \
    "v_mfma_f32_16x16x32_bf16 v[178:181], v[128:131], v[84:87], v[178:181]\n\t" \
    "ds_read_b64_tr_b16 v[98:99], v226 offset:2048\n\t" \
    "v_add3_u32 v230, v80, v81, v230\n\t" \
    "v_mfma_f32_16x16x32_bf16 v[166:169], v[132:135], v[80:83], v[166:169]\n\t" \
    "ds_read_b64_tr_b16 v[100:101], v227\n\t" \
    "buffer_load_dwordx4 v[76:79], v243, %[rsb], s80 offen offset:1024 sc1\n\t" \
    "v_mfma_f32_16x16x32_bf16 v[182:185], v[132:135], v[84:87], v[182:185]\n\t" \
    "ds_read_b64_tr_b16 v[102:103], v227 offset:2048\n\t" \
    "v_add3_u32 v230, v82, v83, v230\n\t" \
    "v_mfma_f32_16x16x32_bf16 v[170:173], v[136:139], v[80:83], v[170:173]\n\t" \
    "ds_read_b64_tr_b16 v[104:105], v228\n\t" \
    "v_add3_u32 v230, v84, v85, v230\n\t" \
    "v_mfma_f32_16x16x32_bf16 v[186:189], v[136:139], v[84:87], v[186:189]\n\t" \
    "ds_read_b64_tr_b16 v[106:107], v228 offset:2048\n\t" \
    "v_add3_u32 v230, v86, v87, v230\n\t" \
    "v_mfma_f32_16x16x32_bf16 v[174:177], v[140:143], v[80:83], v[174:177]\n\t" \
    "ds_read_b64_tr_b16 v[108:109], v229\n\t" \
    "v_mfma_f32_16x16x32_bf16 v[190:193], v[140:143], v[84:87], v[190:193]\n\t" \
    "ds_read_b64_tr_b16 v[110:111], v229 offset:2048\n\t" \
    "s_waitcnt vmcnt(12) lgkmcnt(8)\n\t" \
    "v_mfma_f32_16x16x32_bf16 v[162:165], v[144:147], v[88:91], v[162:165]\n\t" \
    "ds_read_b64_tr_b16 v[112:113], v226 offset:4096\n\t" \
    "buffer_load_dwordx4 v[80:83], v243, %[rsb], s80 offen offset:2048 sc1\n\t" \
    "v_mfma_f32_16x16x32_bf16 v[178:181], v[144:147], v[92:95], v[178:181]\n\t" \
    "ds_read_b64_tr_b16 v[114:115], v226 offset:6144\n\t" \
    "v_add3_u32 v230, v88, v89, v230\n\t" \
    "v_mfma_f32_16x16x32_bf16 v[166:169], v[148:151], v[88:91], v[166:169]\n\t" \
    "ds_read_b64_tr_b16 v[116:117], v227 offset:4096\n\t" \
    "buffer_load_dwordx4 v[84:87], v243, %[rsb], s80 offen offset:3072 sc1\n\t" \
    "v_mfma_f32_16x16x32_bf16 v[182:185], v[148:151], v[92:95], v[182:185]\n\t" \
    "ds_read_b64_tr_b16 v[118:119], v227 offset:6144\n\t" \
    "v_add3_u32 v230, v90, v91, v230\n\t" \
    "v_mfma_f32_16x16x32_bf16 v[170:173], v[152:155], v[88:91], v[170:173]\n\t" \
    "ds_read_b64_tr_b16 v[120:121], v228 offset:4096\n\t" \
    "v_add3_u32 v230, v92, v93, v230\n\t" \
    "v_mfma_f32_16x16x32_bf16 v[186:189], v[152:155], v[92:95], v[186:189]\n\t" \
    "ds_read_b64_tr_b16 v[122:123], v228 offset:6144\n\t" \
    "v_add3_u32 v230, v94, v95, v230\n\t" \
    "v_mfma_f32_16x16x32_bf16 v[174:177], v[156:159], v[88:91], v[174:177]\n\t" \
    "ds_read_b64_tr_b16 v[124:125], v229 offset:4096\n\t" \
    "s_add_u32 s80, s80, 0x4000\n\t" \
    "s_and_b32 s80, s80, 0xc000\n\t" \
    "v_mfma_f32_16x16x32_bf16 v[190:193], v[156:159], v[92:95], v[190:193]\n\t" \
    "ds_read_b64_tr_b16 v[126:127], v229 offset:6144\n\t" \
    "s_waitcnt vmcnt(12) lgkmcnt(8)\n\t" \
    "v_mfma_f32_16x16x32_bf16 v[162:165], v[96:99], v[32:35], v[162:165]\n\t" \
    "ds_read_b64_tr_b16 v[128:129], v226 offset:8192\n\t" \
    "buffer_load_dwordx4 v[88:91], v240, %[rsb], s80 offen sc1\n\t" \
    "v_mfma_f32_16x16x32_bf16 v[178:181], v[96:99], v[36:39], v[178:181]\n\t" \
    "ds_read_b64_tr_b16 v[130:131], v226 offset:10240\n\t" \
    "v_add3_u32 v230, v32, v33, v230\n\t" \
    ".Lpad_c1:\n\t" \
    "s_cbranch_execz .Lpad_ret\n\t" \
    "v_mfma_f32_16x16x32_bf16 v[166:169], v[100:103], v[32:35], v[166:169]\n\t" \
    "ds_read_b64_tr_b16 v[132:133], v227 offset:8192\n\t" \
    "buffer_load_dwordx4 v[92:95], v240, %[rsb], s80 offen offset:1024 sc1\n\t" \
    "v_mfma_f32_16x16x32_bf16 v[182:185], v[100:103], v[36:39], v[182:185]\n\t" \
    "ds_read_b64_tr_b16 v[134:135], v227 offset:10240\n\t" \
    "v_add3_u32 v230, v34, v35, v230\n\t" \
    "v_mfma_f32_16x16x32_bf16 v[170:173], v[104:107], v[32:35], v[170:173]\n\t" \
    "ds_read_b64_tr_b16 v[136:137], v228 offset:8192\n\t" \
    "v_add3_u32 v230, v36, v37, v230\n\t" \
    "v_mfma_f32_16x16x32_bf16 v[186:189], v[104:107], v[36:39], v[186:189]\n\t" \
    "ds_read_b64_tr_b16 v[138:139], v228 offset:10240\n\t" \
    "v_add3_u32 v230, v38, v39, v230\n\t" \
    "v_mfma_f32_16x16x32_bf16 v[174:177], v[108:111], v[32:35], v[174:177]\n\t" \
    "ds_read_b64_tr_b16 v[140:141], v229 offset:8192\n\t" \
    "buffer_load_dwordx2 v[160:161], v244, %[rsb], %[sog] offen sc1\n\t" \
    "v_mfma_f32_16x16x32_bf16 v[190:193], v[108:111], v[36:39], v[190:193]\n\t" \
    "ds_read_b64_tr_b16 v[142:143], v229 offset:10240\n\t" \
    "s_waitcnt vmcnt(13) lgkmcnt(8)\n\t" \
    "v_mfma_f32_16x16x32_bf16 v[162:165], v[112:115], v[40:43], v[162:165]\n\t" \
    "ds_read_b64_tr_b16 v[144:145], v226 offset:12288\n\t" \
    "v_mfma_f32_16x16x32_bf16 v[178:181], v[112:115], v[44:47], v[178:181]\n\t" \
    "ds_read_b64_tr_b16 v[146:147], v226 offset:14336\n\t" \
    "v_add3_u32 v230, v40, v41, v230\n\t" \
    "v_mfma_f32_16x16x32_bf16 v[166:169], v[116:119], v[40:43], v[166:169]\n\t" \
    "ds_read_b64_tr_b16 v[148:149], v227 offset:12288\n\t" \
    "v_mfma_f32_16x16x32_bf16 v[182:185], v[116:119], v[44:47], v[182:185]\n\t" \
    "ds_read_b64_tr_b16 v[150:151], v227 offset:14336\n\t" \
    "v_add3_u32 v230, v42, v43, v230\n\t" \
    "v_mfma_f32_16x16x32_bf16 v[170:173], v[120:123], v[40:43], v[170:173]\n\t" \
    "ds_read_b64_tr_b16 v[152:153], v228 offset:12288\n\t" \
    "v_add3_u32 v230, v44, v45, v230\n\t" \
    "v_mfma_f32_16x16x32_bf16 v[186:189], v[120:123], v[44:47], v[186:189]\n\t" \
    "ds_read_b64_tr_b16 v[154:155], v228 offset:14336\n\t" \
    "v_add3_u32 v230, v46, v47, v230\n\t" \
    "v_mfma_f32_16x16x32_bf16 v[174:177], v[124:127], v[40:43], v[174:177]\n\t" \
    "ds_read_b64_tr_b16 v[156:157], v229 offset:12288\n\t" \
    "v_mfma_f32_16x16x32_bf16 v[190:193], v[124:127], v[44:47], v[190:193]\n\t" \
    "ds_read_b64_tr_b16 v[158:159], v229 offset:14336\n\t" \
    "s_waitcnt vmcnt(11) lgkmcnt(8)\n\t" \
    "v_mfma_f32_16x16x32_bf16 v[162:165], v[128:131], v[48:51], v[162:165]\n\t" \
    "ds_read_b64_tr_b16 v[96:97], v226 offset:16384\n\t" \
    "v_mfma_f32_16x16x32_bf16 v[178:181], v[128:131], v[52:55], v[178:181]\n\t" \
    "ds_read_b64_tr_b16 v[98:99], v226 offset:18432\n\t" \
    "v_add3_u32 v230, v48, v49, v230\n\t" \
    "v_mfma_f32_16x16x32_bf16 v[166:169], v[132:135], v[48:51], v[166:169]\n\t" \
    "ds_read_b64_tr_b16 v[100:101], v227 offset:16384\n\t" \
    "v_mfma_f32_16x16x32_bf16 v[182:185], v[132:135], v[52:55], v[182:185]\n\t" \
    "ds_read_b64_tr_b16 v[102:103], v227 offset:18432\n\t" \
    "v_add3_u32 v230, v50, v51, v230\n\t" \
    "v_mfma_f32_16x16x32_bf16 v[170:173], v[136:139], v[48:51], v[170:173]\n\t" \
    "ds_read_b64_tr_b16 v[104:105], v228 offset:16384\n\t" \
    "v_add3_u32 v230, v52, v53, v230\n\t" \
    "v_mfma_f32_16x16x32_bf16 v[186:189], v[136:139], v[52:55], v[186:189]\n\t" \
    "ds_read_b64_tr_b16 v[106:107], v228 offset:18432\n\t" \
    "v_add3_u32 v230, v54, v55, v230\n\t" \
    "v_mfma_f32_16x16x32_bf16 v[174:177], v[140:143], v[48:51], v[174:177]\n\t" \
    "ds_read_b64_tr_b16 v[108:109], v229 offset:16384\n\t" \
    "v_mfma_f32_16x16x32_bf16 v[190:193], v[140:143], v[52:55], v[190:193]\n\t" \
    "ds_read_b64_tr_b16 v[110:111], v229 offset:18432\n\t" \
    "s_waitcnt vmcnt(9) lgkmcnt(8)\n\t" \
    "v_mfma_f32_16x16x32_bf16 v[162:165], v[144:147], v[56:59], v[162:165]\n\t" \
    "ds_read_b64_tr_b16 v[112:113], v226 offset:20480\n\t" \
    "v_mfma_f32_16x16x32_bf16 v[178:181], v[144:147], v[60:63], v[178:181]\n\t" \
    "ds_read_b64_tr_b16 v[114:115], v226 offset:22528\n\t" \
    "v_add3_u32 v230, v56, v57, v230\n\t" \
    "v_mfma_f32_16x16x32_bf16 v[166:169], v[148:151], v[56:59], v[166:169]\n\t" \
    "ds_read_b64_tr_b16 v[116:117], v227 offset:20480\n\t" \
    "v_mfma_f32_16x16x32_bf16 v[182:185], v[148:151], v[60:63], v[182:185]\n\t" \
    "ds_read_b64_tr_b16 v[118:119], v227 offset:22528\n\t" \
    "v_add3_u32 v230, v58, v59, v230\n\t" \
    "v_mfma_f32_16x16x32_bf16 v[170:173], v[152:155], v[56:59], v[170:173]\n\t" \
    "ds_read_b64_tr_b16 v[120:121], v228 offset:20480\n\t" \
    "v_add3_u32 v230, v60, v61, v230\n\t" \
    "v_mfma_f32_16x16x32_bf16 v[186:189], v[152:155], v[60:63], v[186:189]\n\t" \
    "ds_read_b64_tr_b16 v[122:123], v228 offset:22528\n\t" \
    "v_add3_u32 v230, v62, v63, v230\n\t" \
    "v_mfma_f32_16x16x32_bf16 v[174:177], v[156:159], v[56:59], v[174:177]\n\t" \
    "ds_read_b64_tr_b16 v[124:125], v229 offset:20480\n\t" \
    "v_mfma_f32_16x16x32_bf16 v[190:193], v[156:159], v[60:63], v[190:193]\n\t" \
    "ds_read_b64_tr_b16 v[126:127], v229 offset:22528\n\t" \
    "s_waitcnt vmcnt(7) lgkmcnt(8)\n\t" \
    "v_mfma_f32_16x16x32_bf16 v[162:165], v[96:99], v[64:67], v[162:165]\n\t" \
    "ds_read_b64_tr_b16 v[128:129], v226 offset:24576\n\t" \
    "v_mfma_f32_16x16x32_bf16 v[178:181], v[96:99], v[68:71], v[178:181]\n\t" \
    "ds_read_b64_tr_b16 v[130:131], v226 offset:26624\n\t" \
    "v_add3_u32 v230, v64, v65, v230\n\t" \
    "v_mfma_f32_16x16x32_bf16 v[166:169], v[100:103], v[64:67], v[166:169]\n\t" \
    "ds_read_b64_tr_b16 v[132:133], v227 offset:24576\n\t" \
    "v_mfma_f32_16x16x32_bf16 v[182:185], v[100:103], v[68:71], v[182:185]\n\t" \
    "ds_read_b64_tr_b16 v[134:135], v227 offset:26624\n\t" \
    "v_add3_u32 v230, v66, v67, v230\n\t" \
    "v_mfma_f32_16x16x32_bf16 v[170:173], v[104:107], v[64:67], v[170:173]\n\t" \
    "ds_read_b64_tr_b16 v[136:137], v228 offset:24576\n\t" \
    "v_add3_u32 v230, v68, v69, v230\n\t" \
    "v_mfma_f32_16x16x32_bf16 v[186:189], v[104:107], v[68:71], v[186:189]\n\t" \
    "ds_read_b64_tr_b16 v[138:139], v228 offset:26624\n\t" \
    "v_add3_u32 v230, v70, v71, v230\n\t" \
    "v_mfma_f32_16x16x32_bf16 v[174:177], v[108:111], v[64:67], v[174:177]\n\t" \
    "ds_read_b64_tr_b16 v[140:141], v229 offset:24576\n\t" \
    "v_mfma_f32_16x16x32_bf16 v[190:193], v[108:111], v[68:71], v[190:193]\n\t" \
    "ds_read_b64_tr_b16 v[142:143], v229 offset:26624\n\t" \
    "s_waitcnt vmcnt(5) lgkmcnt(8)\n\t" \
    "v_mfma_f32_16x16x32_bf16 v[162:165], v[112:115], v[72:75], v[162:165]\n\t" \
    "ds_read_b64_tr_b16 v[144:145], v226 offset:28672\n\t" \
    "v_mfma_f32_16x16x32_bf16 v[178:181], v[112:115], v[76:79], v[178:181]\n\t" \
    "ds_read_b64_tr_b16 v[146:147], v226 offset:30720\n\t" \
    "v_add3_u32 v230, v72, v73, v230\n\t" \
    "v_mfma_f32_16x16x32_bf16 v[166:169], v[116:119], v[72:75], v[166:169]\n\t" \
    "ds_read_b64_tr_b16 v[148:149], v227 offset:28672\n\t" \
    "v_mfma_f32_16x16x32_bf16 v[182:185], v[116:119], v[76:79], v[182:185]\n\t" \
    "ds_read_b64_tr_b16 v[150:151], v227 offset:30720\n\t" \
    "v_add3_u32 v230, v74, v75, v230\n\t" \
    "v_mfma_f32_16x16x32_bf16 v[170:173], v[120:123], v[72:75], v[170:173]\n\t" \
    "ds_read_b64_tr_b16 v[152:153], v228 offset:28672\n\t" \
    "v_add3_u32 v230, v76, v77, v230\n\t" \
    "v_mfma_f32_16x16x32_bf16 v[186:189], v[120:123], v[76:79], v[186:189]\n\t" \
    "ds_read_b64_tr_b16 v[154:155], v228 offset:30720\n\t" \
    "v_add3_u32 v230, v78, v79, v230\n\t" \
    "v_mfma_f32_16x16x32_bf16 v[174:177], v[124:127], v[72:75], v[174:177]\n\t" \
    "ds_read_b64_tr_b16 v[156:157], v229 offset:28672\n\t" \
    "v_mfma_f32_16x16x32_bf16 v[190:193], v[124:127], v[76:79], v[190:193]\n\t" \
    "ds_read_b64_tr_b16 v[158:159], v229 offset:30720\n\t" \
    "s_waitcnt vmcnt(3) lgkmcnt(8)\n\t" \
    "v_mfma_f32_16x16x32_bf16 v[162:165], v[128:131], v[80:83], v[162:165]\n\t" \
    "ds_read_b32 v231, v249 offset:68\n\t" \
    "v_mfma_f32_16x16x32_bf16 v[178:181], v[128:131], v[84:87], v[178:181]\n\t" \
    "v_add3_u32 v230, v80, v81, v230\n\t" \
    "v_mfma_f32_16x16x32_bf16 v[166:169], v[132:135], v[80:83], v[166:169]\n\t" \
    "ds_read_b128 v[96:99], v252\n\t" \
    "v_mfma_f32_16x16x32_bf16 v[182:185], v[132:135], v[84:87], v[182:185]\n\t" \
    "ds_read_b128 v[100:103], v252 offset:64\n\t" \
    "v_add3_u32 v230, v82, v83, v230\n\t" \
    "v_mfma_f32_16x16x32_bf16 v[170:173], v[136:139], v[80:83], v[170:173]\n\t" \
    "ds_read_b128 v[104:107], v252 offset:128\n\t" \
    "v_add3_u32 v230, v84, v85, v230\n\t" \
    "v_mfma_f32_16x16x32_bf16 v[186:189], v[136:139], v[84:87], v[186:189]\n\t" \
    "ds_read_b128 v[108:111], v252 offset:192\n\t" \
    "v_add3_u32 v230, v86, v87, v230\n\t" \
    "v_mfma_f32_16x16x32_bf16 v[174:177], v[140:143], v[80:83], v[174:177]\n\t" \
    "v_mfma_f32_16x16x32_bf16 v[190:193], v[140:143], v[84:87], v[190:193]\n\t" \
    "s_waitcnt vmcnt(1) lgkmcnt(5)\n\t" \
    "v_mfma_f32_16x16x32_bf16 v[162:165], v[144:147], v[88:91], v[162:165]\n\t" \
    "v_mfma_f32_16x16x32_bf16 v[166:169], v[148:151], v[88:91], v[166:169]\n\t" \
    "v_add3_u32 v230, v88, v89, v230\n\t" \
    "v_mfma_f32_16x16x32_bf16 v[170:173], v[152:155], v[88:91], v[170:173]\n\t" \
    "v_mfma_f32_16x16x32_bf16 v[174:177], v[156:159], v[88:91], v[174:177]\n\t" \
    "v_add3_u32 v230, v90, v91, v230\n\t" \
    "v_mfma_f32_16x16x32_bf16 v[178:181], v[144:147], v[92:95], v[178:181]\n\t" \
    "v_add3_u32 v230, v92, v93, v230\n\t" \
    "v_mfma_f32_16x16x32_bf16 v[182:185], v[148:151], v[92:95], v[182:185]\n\t" \
    "v_add3_u32 v230, v94, v95, v230\n\t" \
    "v_mfma_f32_16x16x32_bf16 v[186:189], v[152:155], v[92:95], v[186:189]\n\t" \
    "s_cmp_lg_u64 %[rm], 0\n\t" \
    "v_mfma_f32_16x16x32_bf16 v[190:193], v[156:159], v[92:95], v[190:193]\n\t" \
    "s_cbranch_scc1 .Lc_y\n\t" \
    ".Lc_yr:\n\t" \
    "s_add_u32 s85, %[sout], 0x8000\n\t" \
    "v_pk_add_f32 v[0:1], v[0:1], v[162:163]\n\t" \
    "v_pk_add_f32 v[2:3], v[2:3], v[164:165]\n\t" \
    "v_pk_add_f32 v[4:5], v[4:5], v[166:167]\n\t" \
    "v_pk_add_f32 v[6:7], v[6:7], v[168:169]\n\t" \
    "v_pk_fma_f32 v[0:1], v[232:233], v[0:1], v[194:195] op_sel_hi:[0,1,1]\n\t" \
    "v_pk_fma_f32 v[2:3], v[232:233], v[2:3], v[196:197] op_sel_hi:[0,1,1]\n\t" \
    "v_pk_fma_f32 v[4:5], v[232:233], v[4:5], v[198:199] op_sel_hi:[0,1,1]\n\t" \
    "v_pk_fma_f32 v[6:7], v[232:233], v[6:7], v[200:201] op_sel_hi:[0,1,1]\n\t" \
    "v_cvt_pk_bf16_f32 v32, v0, v1\n\t" \
    "v_cvt_pk_bf16_f32 v34, v4, v5\n\t" \
    "v_cvt_pk_bf16_f32 v33, v2, v3\n\t" \
    "v_cvt_pk_bf16_f32 v35, v6, v7\n\t" \
    "s_nop 0\n\t" \
    "v_permlane16_swap_b32 v32, v34\n\t" \
    "v_permlane16_swap_b32 v33, v35\n\t" \
    "buffer_store_dwordx4 v[32:35], v247, %[rso], %[sout] offen sc1\n\t" \
    "v_pk_add_f32 v[8:9], v[8:9], v[170:171]\n\t" \
    "v_pk_add_f32 v[10:11], v[10:11], v[172:173]\n\t" \
    "v_pk_add_f32 v[12:13], v[12:13], v[174:175]\n\t" \
    "v_pk_add_f32 v[14:15], v[14:15], v[176:177]\n\t" \
    "v_pk_fma_f32 v[8:9], v[232:233], v[8:9], v[202:203] op_sel_hi:[0,1,1]\n\t" \
    "v_pk_fma_f32 v[10:11], v[232:233], v[10:11], v[204:205] op_sel_hi:[0,1,1]\n\t" \
    "v_pk_fma_f32 v[12:13], v[232:233], v[12:13], v[206:207] op_sel_hi:[0,1,1]\n\t" \
    "v_pk_fma_f32 v[14:15], v[232:233], v[14:15], v[208:209] op_sel_hi:[0,1,1]\n\t" \
    "v_cvt_pk_bf16_f32 v36, v8, v9\n\t" \
    "v_cvt_pk_bf16_f32 v38, v12, v13\n\t" \
    "v_cvt_pk_bf16_f32 v37, v10, v11\n\t" \
    "v_cvt_pk_bf16_f32 v39, v14, v15\n\t" \
    "v_cmp_gt_u32 vcc, 32, v250\n\t" \
    "v_permlane16_swap_b32 v36, v38\n\t" \
    "v_permlane16_swap_b32 v37, v39\n\t" \
    "buffer_store_dwordx4 v[36:39], v247, %[rso], %[sout] offen offset:64 sc1\n\t" \
    "v_pk_add_f32 v[16:17], v[16:17], v[178:179]\n\t" \
    "v_pk_add_f32 v[18:19], v[18:19], v[180:181]\n\t" \
    "v_pk_add_f32 v[20:21], v[20:21], v[182:183]\n\t" \
    "v_pk_add_f32 v[22:23], v[22:23], v[184:185]\n\t" \
    "v_pk_fma_f32 v[16:17], v[234:235], v[16:17], v[210:211] op_sel_hi:[0,1,1]\n\t" \
    "v_pk_fma_f32 v[18:19], v[234:235], v[18:19], v[212:213] op_sel_hi:[0,1,1]\n\t" \
    "v_pk_fma_f32 v[20:21], v[234:235], v[20:21], v[214:215] op_sel_hi:[0,1,1]\n\t" \
    "v_pk_fma_f32 v[22:23], v[234:235], v[22:23], v[216:217] op_sel_hi:[0,1,1]\n\t" \
    "v_cvt_pk_bf16_f32 v40, v16, v17\n\t" \
    "v_cvt_pk_bf16_f32 v42, v20, v21\n\t" \
    "v_cvt_pk_bf16_f32 v41, v18, v19\n\t" \
    "v_cvt_pk_bf16_f32 v43, v22, v23\n\t" \
    "s_waitcnt vmcnt(2)\n\t" \
    "v_permlane16_swap_b32 v40, v42\n\t" \
    "v_permlane16_swap_b32 v41, v43\n\t" \
    "buffer_store_dwordx4 v[40:43], v247, %[rso], s85 offen sc1\n\t" \
    "v_pk_add_f32 v[24:25], v[24:25], v[186:187]\n\t" \
    "v_pk_add_f32 v[26:27], v[26:27], v[188:189]\n\t" \
    "v_pk_add_f32 v[28:29], v[28:29], v[190:191]\n\t" \
    "v_pk_add_f32 v[30:31], v[30:31], v[192:193]\n\t" \
    "v_pk_fma_f32 v[24:25], v[234:235], v[24:25], v[218:219] op_sel_hi:[0,1,1]\n\t" \
    "v_pk_fma_f32 v[26:27], v[234:235], v[26:27], v[220:221] op_sel_hi:[0,1,1]\n\t" \
    "v_pk_fma_f32 v[28:29], v[234:235], v[28:29], v[222:223] op_sel_hi:[0,1,1]\n\t" \
    "v_pk_fma_f32 v[30:31], v[234:235], v[30:31], v[224:225] op_sel_hi:[0,1,1]\n\t" \
    "v_cvt_pk_bf16_f32 v44, v24, v25\n\t" \
    "v_cvt_pk_bf16_f32 v46, v28, v29\n\t" \
    "v_cvt_pk_bf16_f32 v45, v26, v27\n\t" \
    "v_cvt_pk_bf16_f32 v47, v30, v31\n\t" \
    "s_waitcnt lgkmcnt(4)\n\t" \
    "v_cndmask_b32 v231, 0, v160, vcc\n\t" \
    "v_permlane16_swap_b32 v44, v46\n\t" \
    "v_permlane16_swap_b32 v45, v47\n\t" \
    "buffer_store_dwordx4 v[44:47], v247, %[rso], s85 offen offset:64 sc1\n\t" \
    "v_sub_u32 v231, v230, v231\n\t" \
    "v_cmp_ne_u32 vcc, v236, v161\n\t" \
    "v_mov_b32 v0, v231\n\t" \
    "s_nop 1\n\t" \
    "v_permlane32_swap_b32 v231, v0\n\t" \
    "v_add_u32 v231, v231, v0\n\t" \
    "v_mov_b32 v0, v231\n\t" \
    "s_nop 1\n\t" \
    "v_permlane16_swap_b32 v231, v0\n\t" \
    "v_add_u32 v231, v231, v0\n\t" \
    "s_nop 1\n\t" \
    "v_add_u32_dpp v231, v231, v231 row_ror:8 row_mask:0xf bank_mask:0xf bound_ctrl:1\n\t" \
    "s_nop 1\n\t" \
    "v_add_u32_dpp v231, v231, v231 row_ror:4 row_mask:0xf bank_mask:0xf bound_ctrl:1\n\t" \
    "s_nop 1\n\t" \
    "v_add_u32_dpp v231, v231, v231 row_ror:2 row_mask:0xf bank_mask:0xf bound_ctrl:1\n\t" \
    "s_nop 1\n\t" \
    "v_add_u32_dpp v231, v231, v231 row_ror:1 row_mask:0xf bank_mask:0xf bound_ctrl:1\n\t" \
    "s_nop 0\n\t" \
    "v_readfirstlane_b32 s83, v231\n\t" \
    "s_or_b32 s83, s83, vcc_lo\n\t" \
    "s_cselect_b32 %[st], 2, 0\n\t" \
    "s_waitcnt lgkmcnt(0)\n\t" \
    ".Lc_end:\n\t"
#define RED1_ASM \
    "s_mov_b32 s80, 256\n\t" \
    "s_add_u32 s81, %[s0], 0x1000\n\t" \
    "v_mov_b32 v41, 1\n\t" \
    ".Lr_t:\n\t" \
    "s_memrealtime s[82:83]\n\t" \
    "s_sub_u32 s80, s80, 1\n\t" \
    "s_cbranch_scc1 .Lr_go\n\t" \
    "s_waitcnt lgkmcnt(0)\n\t" \
    "s_sub_u32 s82, %[t1], s82\n\t" \
    "s_cmp_lt_i32 s82, 5\n\t" \
    "s_cbranch_scc1 .Lr_go\n\t" \
    "s_cmp_lt_i32 s82, 13\n\t" \
    "s_cbranch_scc1 .Lr_t\n\t" \
    "s_sleep 2\n\t" \
    "s_branch .Lr_t\n\t" \
    ".Lr_go:\n\t" \
    "buffer_load_dwordx4 v[0:3], %[v16], %[rsb], %[sm] offen sc1\n\t" \
    "buffer_load_dwordx4 v[4:7], %[v16], %[rsb], %[s0] offen sc1\n\t" \
    "buffer_load_dwordx4 v[8:11], %[v16], %[rsb], %[s0] offen offset:1024 sc1\n\t" \
    "buffer_load_dwordx4 v[12:15], %[v16], %[rsb], %[s0] offen offset:2048 sc1\n\t" \
    "buffer_load_dwordx4 v[16:19], %[v16], %[rsb], %[s0] offen offset:3072 sc1\n\t" \
    "buffer_load_dwordx4 v[20:23], %[v16], %[rsb], s81 offen sc1\n\t" \
    "buffer_load_dwordx4 v[24:27], %[v16], %[rsb], s81 offen offset:1024 sc1\n\t" \
    "buffer_load_dwordx4 v[28:31], %[v16], %[rsb], s81 offen offset:2048 sc1\n\t" \
    "buffer_load_dwordx4 v[32:35], %[v16], %[rsb], s81 offen offset:3072 sc1\n\t" \
    "ds_write_b32 %[ra], v41\n\t" \
    "s_waitcnt vmcnt(8)\n\t" \
    "v_add3_u32 v40, v0, v1, v2\n\t" \
    "v_xor_b32 v41, %[tag], v1\n\t" \
    "v_and_b32 v42, %[mk2], v0\n\t" \
    "v_add_u32 v40, v40, v3\n\t" \
    "v_add_f32_dpp v0, v0, v0 row_ror:8 row_mask:0xf bank_mask:0xf bound_ctrl:1\n\t" \
    "v_add_f32_dpp v1, v1, v1 row_ror:8 row_mask:0xf bank_mask:0xf bound_ctrl:1\n\t" \
    "v_add_f32_dpp v2, v2, v2 row_ror:8 row_mask:0xf bank_mask:0xf bound_ctrl:1\n\t" \
    "v_add_f32_dpp v3, v3, v3 row_ror:8 row_mask:0xf bank_mask:0xf bound_ctrl:1\n\t" \
    "v_and_b32 v40, %[mk01], v40\n\t" \
    "v_sub_u32 v40, v40, v42\n\t" \
    "s_waitcnt vmcnt(7)\n\t" \
    "v_mfma_f32_16x16x32_bf16 v[36:39], %[sel], v[4:7], 0\n\t" \
    "v_add3_u32 v40, v4, v5, v40\n\t" \
    "v_add3_u32 v40, v6, v7, v40\n\t" \
    "v_add_f32_dpp v0, v0, v0 row_ror:4 row_mask:0xf bank_mask:0xf bound_ctrl:1\n\t" \
    "v_add_f32_dpp v1, v1, v1 row_ror:4 row_mask:0xf bank_mask:0xf bound_ctrl:1\n\t" \
    "s_waitcnt vmcnt(6)\n\t" \
    "v_mfma_f32_16x16x32_bf16 v[36:39], %[sel], v[8:11], v[36:39]\n\t" \
    "v_add3_u32 v40, v8, v9, v40\n\t" \
    "v_add3_u32 v40, v10, v11, v40\n\t" \
    "v_add_f32_dpp v2, v2, v2 row_ror:4 row_mask:0xf bank_mask:0xf bound_ctrl:1\n\t" \
    "v_add_f32_dpp v3, v3, v3 row_ror:4 row_mask:0xf bank_mask:0xf bound_ctrl:1\n\t" \
    "s_waitcnt vmcnt(5)\n\t" \
    "v_mfma_f32_16x16x32_bf16 v[36:39], %[sel], v[12:15], v[36:39]\n\t" \
    "v_add3_u32 v40, v12, v13, v40\n\t" \
    "v_add3_u32 v40, v14, v15, v40\n\t" \
    "v_and_b32 v41, %[mk2], v41\n\t" \
    "v_cmp_ne_u32 vcc, 0, v41\n\t" \
    "s_waitcnt vmcnt(4)\n\t" \
    "v_mfma_f32_16x16x32_bf16 v[36:39], %[sel], v[16:19], v[36:39]\n\t" \
    "v_add3_u32 v40, v16, v17, v40\n\t" \
    "v_add3_u32 v40, v18, v19, v40\n\t" \
    "v_permlane32_swap_b32 v0, v2\n\t" \
    "v_permlane32_swap_b32 v1, v3\n\t" \
    "s_waitcnt vmcnt(3)\n\t" \
    "v_mfma_f32_16x16x32_bf16 v[36:39], %[sel], v[20:23], v[36:39]\n\t" \
    "v_add3_u32 v40, v20, v21, v40\n\t" \
    "v_add3_u32 v40, v22, v23, v40\n\t" \
    "v_add_f32 v0, v0, v2\n\t" \
    "v_add_f32 v1, v1, v3\n\t" \
    "s_waitcnt vmcnt(2)\n\t" \
    "v_mfma_f32_16x16x32_bf16 v[36:39], %[sel], v[24:27], v[36:39]\n\t" \
    "v_add3_u32 v40, v24, v25, v40\n\t" \
    "v_add3_u32 v40, v26, v27, v40\n\t" \
    "v_mov_b32 v2, v0\n\t" \
    "v_mov_b32 v3, v1\n\t" \
    "s_waitcnt vmcnt(1)\n\t" \
    "v_mfma_f32_16x16x32_bf16 v[36:39], %[sel], v[28:31], v[36:39]\n\t" \
    "v_add3_u32 v40, v28, v29, v40\n\t" \
    "v_add3_u32 v40, v30, v31, v40\n\t" \
    "s_or_b32 s80, vcc_lo, vcc_hi\n\t" \
    "s_waitcnt vmcnt(0)\n\t" \
    "v_mfma_f32_16x16x32_bf16 v[36:39], %[sel], v[32:35], v[36:39]\n\t" \
    "v_add3_u32 v40, v32, v33, v40\n\t" \
    "v_add3_u32 v40, v34, v35, v40\n\t" \
    "v_permlane16_swap_b32 v0, v2\n\t" \
    "v_permlane16_swap_b32 v1, v3\n\t" \
    "v_add_f32 v0, v0, v2\n\t" \
    "v_add_f32 v1, v1, v3\n\t" \
    "v_cvt_pk_bf16_f32 v44, v0, v1\n\t" \
    "v_mov_b32 v45, v44\n\t" \
    "v_cvt_pk_bf16_f32 v48, v36, v37\n\t" \
    "v_cvt_pk_bf16_f32 v49, v38, v39\n\t" \
    "v_and_b32 v46, %[mkl], v44\n\t" \
    "v_add3_u32 v46, v46, v48, v49\n\t" \
    "v_permlane32_swap_b32 v44, v45\n\t" \
    "buffer_store_dwordx2 v[48:49], %[vkf], %[rsb], %[sf] offen sc1\n\t" \
    "v_permlane32_swap_b32 v40, v46\n\t" \
    "v_add_u32 v40, v40, v46\n\t" \
    "v_mov_b32 v46, v40\n\t" \
    "v_cndmask_b32_e64 v45, v45, %[tag], %[hi]\n\t" \
    "s_nop 0\n\t" \
    "v_permlane16_swap_b32 v40, v46\n\t" \
    "v_add_u32 v40, v40, v46\n\t" \
    "s_nop 1\n\t" \
    "v_add_u32_dpp v40, v40, v40 row_ror:8 row_mask:0xf bank_mask:0xf bound_ctrl:1\n\t" \
    "s_nop 1\n\t" \
    "v_add_u32_dpp v40, v40, v40 row_ror:4 row_mask:0xf bank_mask:0xf bound_ctrl:1\n\t" \
    "s_nop 1\n\t" \
    "v_add_u32_dpp v40, v40, v40 row_ror:2 row_mask:0xf bank_mask:0xf bound_ctrl:1\n\t" \
    "s_nop 1\n\t" \
    "v_add_u32_dpp v40, v40, v40 row_ror:1 row_mask:0xf bank_mask:0xf bound_ctrl:1\n\t" \
    "s_nop 0\n\t" \
    "v_readfirstlane_b32 s81, v40\n\t" \
    "v_cndmask_b32_e64 v44, v44, v40, %[hi]\n\t" \
    "s_or_b32 %[st], s80, s81\n\t" \
    "s_cselect_b32 s82, 0x70000000, %[sg]\n\t" \
    "buffer_store_dwordx2 v[44:45], %[vfm], %[rsb], s82 offen sc1\n\t"
#define RED2_ASM \
    "s_mov_b32 s80, 256\n\t" \
    "s_add_u32 s81, %[s0], 0x1000\n\t" \
    "s_add_u32 s84, %[s0], 0x4000\n\t" \
    "s_mov_b32 m0, 160256\n\t" \
    ".Lr_u:\n\t" \
    "s_memrealtime s[82:83]\n\t" \
    "s_sub_u32 s80, s80, 1\n\t" \
    "s_cbranch_scc1 .Lr_g2\n\t" \
    "s_waitcnt lgkmcnt(0)\n\t" \
    "s_sub_u32 s82, %[t2], s82\n\t" \
    "s_cmp_lt_i32 s82, 5\n\t" \
    "s_cbranch_scc1 .Lr_g2\n\t" \
    "s_cmp_lt_i32 s82, 13\n\t" \
    "s_cbranch_scc1 .Lr_u\n\t" \
    "s_sleep 2\n\t" \
    "s_branch .Lr_u\n\t" \
    ".Lr_g2:\n\t" \
    "buffer_load_dwordx4 %[v16], %[rsb], s84 offen sc1 lds\n\t" \
    "s_mov_b32 m0, 151552\n\t" \
    "s_nop 0\n\t" \
    "buffer_load_dwordx4 %[v16], %[rsb], %[s0] offen sc1 lds\n\t" \
    "buffer_load_dwordx4 %[v16], %[rsb], %[s0] offen offset:1024 sc1 lds\n\t" \
    "buffer_load_dwordx4 %[v16], %[rsb], %[s0] offen offset:2048 sc1 lds\n\t" \
    "buffer_load_dwordx4 %[v16], %[rsb], %[s0] offen offset:3072 sc1 lds\n\t" \
    "s_mov_b32 m0, 155648\n\t" \
    "s_nop 0\n\t" \
    "buffer_load_dwordx4 %[v16], %[rsb], s81 offen sc1 lds\n\t" \
    "buffer_load_dwordx4 %[v16], %[rsb], s81 offen offset:1024 sc1 lds\n\t" \
    "buffer_load_dwordx4 %[v16], %[rsb], s81 offen offset:2048 sc1 lds\n\t" \
    "buffer_load_dwordx4 %[v16], %[rsb], s81 offen offset:3072 sc1 lds\n\t" \
    "ds_write_b32 %[ra], %[pv]\n\t" \
    "v_or_b32 %[pv], 1, %[pv]\n\t" \
    "v_add_u32 %[pv], 1, %[pv]\n\t" \
    "s_waitcnt vmcnt(0)\n\t" \
    "ds_write_b32 %[ra], %[pv]\n\t" \
    "ds_read_b128 v[0:3], %[kwa] offset:8704\n\t" \
    "ds_read_b128 v[4:7], %[kwa]\n\t" \
    "ds_read_b128 v[8:11], %[kwa] offset:1024\n\t" \
    "ds_read_b128 v[12:15], %[kwa] offset:2048\n\t" \
    "ds_read_b128 v[16:19], %[kwa] offset:3072\n\t" \
    "ds_read_b128 v[20:23], %[kwa] offset:4096\n\t" \
    "ds_read_b128 v[24:27], %[kwa] offset:5120\n\t" \
    "ds_read_b128 v[28:31], %[kwa] offset:6144\n\t" \
    "ds_read_b128 v[32:35], %[kwa] offset:7168\n\t" \
    "s_waitcnt lgkmcnt(8)\n\t" \
    "v_add3_u32 v40, v0, v1, v2\n\t" \
    "v_xor_b32 v41, %[tag], v1\n\t" \
    "v_and_b32 v42, %[mk1], v0\n\t" \
    "v_add_u32 v40, v40, v3\n\t" \
    "v_and_b32 v41, %[mk1], v41\n\t" \
    "v_and_b32 v40, %[mk0], v40\n\t" \
    "v_cmp_ne_u32 vcc, 0, v41\n\t" \
    "v_sub_u32 v40, v40, v42\n\t" \
    "v_mov_b32 v42, %[frc]\n\t" \
    "s_waitcnt lgkmcnt(4)\n\t" \
    "v_add3_u32 v40, v4, v5, v40\n\t" \
    "v_add3_u32 v42, v6, v7, v42\n\t" \
    "v_add3_u32 v40, v8, v9, v40\n\t" \
    "v_add3_u32 v42, v10, v11, v42\n\t" \
    "v_add3_u32 v40, v12, v13, v40\n\t" \
    "v_add3_u32 v42, v14, v15, v42\n\t" \
    "v_add3_u32 v40, v16, v17, v40\n\t" \
    "v_add3_u32 v42, v18, v19, v42\n\t" \
    "s_waitcnt lgkmcnt(0)\n\t" \
    "v_add3_u32 v40, v20, v21, v40\n\t" \
    "v_add3_u32 v42, v22, v23, v42\n\t" \
    "v_add3_u32 v40, v24, v25, v40\n\t" \
    "v_add3_u32 v42, v26, v27, v42\n\t" \
    "v_add3_u32 v40, v28, v29, v40\n\t" \
    "v_add3_u32 v42, v30, v31, v42\n\t" \
    "v_add3_u32 v40, v32, v33, v40\n\t" \
    "v_add3_u32 v42, v34, v35, v42\n\t" \
    "s_or_b32 s80, vcc_lo, vcc_hi\n\t" \
    "s_nop 0\n\t" \
    "v_permlane32_swap_b32 v40, v42\n\t" \
    "v_add_u32 v40, v40, v42\n\t" \
    "s_nop 1\n\t" \
    "v_add_u32_dpp v40, v40, v40 row_ror:8 row_mask:0xf bank_mask:0xf bound_ctrl:1\n\t" \
    "s_nop 1\n\t" \
    "v_add_u32_dpp v40, v40, v40 row_ror:4 row_mask:0xf bank_mask:0xf bound_ctrl:1\n\t" \
    "s_nop 1\n\t" \
    "v_add_u32_dpp v40, v40, v40 row_ror:2 row_mask:0xf bank_mask:0xf bound_ctrl:1\n\t" \
    "s_nop 1\n\t" \
    "v_add_u32_dpp v40, v40, v40 row_ror:1 row_mask:0xf bank_mask:0xf bound_ctrl:1\n\t" \
    "s_nop 0\n\t" \
    "v_readlane_b32 s81, v40, 0\n\t" \
    "v_readlane_b32 s82, v40, 16\n\t" \
    "v_readlane_b32 s83, v40, 32\n\t" \
    "v_readlane_b32 s84, v40, 48\n\t" \
    "s_add_u32 s81, s81, s82\n\t" \
    "s_add_u32 s83, s83, s84\n\t" \
    "s_add_u32 s81, s81, s83\n\t" \
    "s_or_b32 %[st], s80, s81\n\t" \
    "s_cselect_b32 s81, 0, 1\n\t" \
    "v_add_u32 v41, s81, %[pv]\n\t" \
    "ds_write_b32 %[ra], v41\n\t"
#define PHASE_AB_CLOB "v0", "v1", "v2", "v3", "v4", "v5", "v6", "v7", "v8", "v9", "v10", "v11", "v12", "v13", "v14", "v15", "v16", "v17", "v18", "v19", "v20", "v21", "v22", "v23", "v24", "v25", "v26", "v27", "v28", "v29", "v30", "v31", "v32", "v33", "v34", "v35", "v36", "v37", "v38", "v39", "v40", "v41", "v42", "v43", "v44", "v45", "v46", "v47", "v48", "v49", "v50", "v51", "v52", "v53", "v54", "v55", "v56", "v57", "v58", "v59", "v60", "v61", "v62", "v63", "v64", "v65", "v66", "v67", "v68", "v69", "v70", "v71", "v72", "v73", "v74", "v75", "v76", "v77", "v78", "v79", "v80", "v81", "v82", "v83", "v84", "v85", "v86", "v87", "v88", "v89", "v90", "v91", "v92", "v93", "v94", "v95", "v96", "v97", "v98", "v99", "v100", "v101", "v102", "v103", "v104", "v105", "v106", "v107", "v108", "v109", "v110", "v111", "v112", "v113", "v114", "v115", "v116", "v117", "v118", "v119", "v120", "v121", "v122", "v123", "v124", "v125", "v126", "v127", "v128", "v129", "v130", "v131", "v132", "v133", "v134", "v135", "v136", "v137", "v138", "v139", "v140", "v141", "v142", "v143", "v144", "v145", "v146", "v147", "v148", "v149", "v150", "v151", "v152", "v153", "v154", "v155", "v156", "v157", "v158", "v159", "v160", "v161", "v162", "v163", "v164", "v165", "v166", "v167", "v168", "v169", "v170", "v171", "v172", "v173", "v174", "v175", "v176", "v177", "v178", "v179", "v180", "v181", "v182", "v183", "v184", "v185", "v186", "v187", "v188", "v189", "v190", "v191", "v192", "v193", "v194", "v195", "v196", "v197", "v198", "v199", "v200", "v201", "v202", "v203", "v204", "v205", "v206", "v207", "v208", "v209", "v210", "v211", "v212", "v213", "v214", "v215", "v216", "v217", "v218", "v219", "v220", "v221", "v222", "v223", "v224", "v225", "v226", "v227", "v228", "v229", "v230", "v231", "v237", "v238", "s82", "s83", "s84", "s85", "s86", "s87", "s88", "s89", "s90", "s91", "s92", "s93", "s94", "s95", "s96", "s97"
#define PHASE_C_CLOB "v0", "v1", "v2", "v3", "v4", "v5", "v6", "v7", "v8", "v9", "v10", "v11", "v12", "v13", "v14", "v15", "v16", "v17", "v18", "v19", "v20", "v21", "v22", "v23", "v24", "v25", "v26", "v27", "v28", "v29", "v30", "v31", "v32", "v33", "v34", "v35", "v36", "v37", "v38", "v39", "v40", "v41", "v42", "v43", "v44", "v45", "v46", "v47", "v48", "v49", "v50", "v51", "v52", "v53", "v54", "v55", "v56", "v57", "v58", "v59", "v60", "v61", "v62", "v63", "v64", "v65", "v66", "v67", "v68", "v69", "v70", "v71", "v72", "v73", "v74", "v75", "v76", "v77", "v78", "v79", "v80", "v81", "v82", "v83", "v84", "v85", "v86", "v87", "v88", "v89", "v90", "v91", "v92", "v93", "v94", "v95", "v96", "v97", "v98", "v99", "v100", "v101", "v102", "v103", "v104", "v105", "v106", "v107", "v108", "v109", "v110", "v111", "v112", "v113", "v114", "v115", "v116", "v117", "v118", "v119", "v120", "v121", "v122", "v123", "v124", "v125", "v126", "v127", "v128", "v129", "v130", "v131", "v132", "v133", "v134", "v135", "v136", "v137", "v138", "v139", "v140", "v141", "v142", "v143", "v144", "v145", "v146", "v147", "v148", "v149", "v150", "v151", "v152", "v153", "v154", "v155", "v156", "v157", "v158", "v159", "v160", "v161", "v162", "v163", "v164", "v165", "v166", "v167", "v168", "v169", "v170", "v171", "v172", "v173", "v174", "v175", "v176", "v177", "v178", "v179", "v180", "v181", "v182", "v183", "v184", "v185", "v186", "v187", "v188", "v189", "v190", "v191", "v192", "v193", "v194", "v195", "v196", "v197", "v198", "v199", "v200", "v201", "v202", "v203", "v204", "v205", "v206", "v207", "v208", "v209", "v210", "v211", "v212", "v213", "v214", "v215", "v216", "v217", "v218", "v219", "v220", "v221", "v222", "v223", "v224", "v225", "v226", "v227", "v228", "v229", "v231", "s80", "s81", "s82", "s83", "s84", "s85", "s86", "s87"
#define RED1_CLOB "v0", "v1", "v2", "v3", "v4", "v5", "v6", "v7", "v8", "v9", "v10", "v11", "v12", "v13", "v14", "v15", "v16", "v17", "v18", "v19", "v20", "v21", "v22", "v23", "v24", "v25", "v26", "v27", "v28", "v29", "v30", "v31", "v32", "v33", "v34", "v35", "v36", "v37", "v38", "v39", "v40", "v41", "v42", "v43", "v44", "v45", "v46", "v47", "v48", "v49", "s80", "s81", "s82", "s83", "s84"

#define KNAME "fused_fwd"
#define CODE_FETCH(dst, kb) do { unsigned long long pc_; unsigned len_, off_; \
    asm volatile("s_getpc_b64 %0\n1:\n\ts_mov_b32 %1, 1b-" KNAME "\n\ts_mov_b32 %2, .Lff_end0-" KNAME "\n\ts_max_u32 %2, %2, .Lff_end1-" KNAME \
                 "\n\ts_max_u32 %2, %2, .Lff_end2-" KNAME : "=s"(pc_), "=s"(off_), "=s"(len_) :: "scc"); \
    const rsrc_t rsC_ = mk_rsrc((const void*)(pc_ - off_), len_); \
    asm volatile("s_nop 4\n\tbuffer_load_dwordx4 %0, %1, %2, %3 offen" : "=v"(dst) : "v"(fresh_lane() * 16), "s"(rsC_), "s"((unsigned)(kb) * 1024u) : "memory"); } while (0)

extern "C" __global__ __launch_bounds__(512, 2) void fused_fwd(const u16* __restrict__ x, const u16* __restrict__ wqkv,
                                                    const u16* __restrict__ wphi, const u16* __restrict__ wo,
                                                    const u16* __restrict__ wr, const u16* __restrict__ wgp,
                                                    const u16* __restrict__ wup, const u16* __restrict__ wdp,
                                                    const u16* __restrict__ gw, char* ws, u16* out) {
    __shared__ __attribute__((aligned(16))) char smem[163840];
    const int w = __builtin_amdgcn_readfirstlane(threadIdx.x >> 6);
    if (K::TOPV) {
        asm volatile("s_mov_b64 exec, 0\n\ts_cmp_eq_u32 %0, 6\n\ts_cbranch_scc1 .Lpad_x\n\t"
                     "s_cmp_eq_u32 %0, 0\n\ts_cbranch_scc1 .Lpad_d\n\ts_cmp_eq_u32 %0, 1\n\ts_cbranch_scc1 .Lpad_ab0\n"
                     ".Lpad_top:\n\ts_mov_b64 exec, -1" :: "s"(w) : "scc", "memory");
    } else {
        asm volatile(".Lpad_top:");
    }
    asm volatile("" :: "s"(x), "s"(wqkv));
    const int bid = blockIdx.x, xcd = bid & 7, slot = bid >> 3;
    const int mtA = (xcd & 1) * 8 + (slot & 7), hpA = (xcd >> 1) * 4 + (slot >> 3);
    const int mtB = (xcd & 1) * 8 + ((xcd >> 1) & 1) * 4 + (slot & 3), hpB = (xcd >> 2) * 8 + (slot >> 2);
    const int rotA = 8 * (xcd & 1);
    const bool has_ffn = slot < 24 || (xcd & 2);
    const int fb = slot < 24 ? bid : 192 + (slot - 24) * 4 + (xcd >> 2) * 2 + (xcd & 1);
    const unsigned lds0 = (unsigned)(size_t)(AS3 char*)smem;
    const rsrc_t rsWS = mk_rsrc(ws, WS_BYTES);
    const rsrc_t rsX = mk_rsrc(x, 2048u * 2048u);

    auto touch3 = [&](unsigned& dst, int half, bool on = true) {
        const rsrc_t rsT = mk_rsrc(wqkv, (on && slot < 24 && !(xcd & 2)) ? 1024u * 6144u : 0u);
        const unsigned so = (unsigned)(((slot & 7) + 8 * half + rotA) & 15) * (64u * 6144u) + (unsigned)(slot >> 3) * 2048u + (unsigned)((xcd >> 1) * 4 + 3) * 128u;
        asm volatile("s_nop 4\n\tbuffer_load_dword %0, %1, %2, %3 offen" : "=v"(dst) : "v"(fresh_lane() * 6144), "s"(rsT), "s"(so) : "memory");
    };
    unsigned gate_t = 0;
    int gate_s = 0;
    auto hush = [&]() {
        if (gate_s == 0) {
            gate_t = __builtin_amdgcn_readfirstlane(*(AS3 volatile const unsigned*)(size_t)(lds0 + L_TM + 8)) - K::QA;
            gate_s = 1;
        }
        if (gate_s == 3 || (int)(now32() - gate_t) < 0) return;
        if (gate_s == 1) {
            wait4(lds0 + C_P, P_PUB);
            lds_wait(lds0 + C_TM, 1);
            gate_t = __builtin_amdgcn_readfirstlane(*(AS3 volatile const unsigned*)(size_t)(lds0 + L_TM)) - K::QB;
            gate_s = 2;
            if ((int)(now32() - gate_t) < 0) return;
        }
        lds_wait(lds0 + C_R, R_L1I);
        gate_s = 3;
    };

    unsigned ep, pf0;
    u32x4 xv[4][2], rv[2], gv[2], pf;
    auto ldi = [&]() {
            const int l = fresh_lane(), a = w & 1, row0 = mtB * 128 + hpB * 8 + 4 * a;
            const rsrc_t rsR = mk_rsrc(wr, 2048u), rsGW = mk_rsrc(gw, 2048u);
            const unsigned v16 = (unsigned)l * 16u;
            asm volatile("s_nop 4\n\tbuffer_load_dword %0, off, %1, 0 sc1" : "=v"(ep) : "s"(rsWS) : "memory");
#pragma unroll
            for (int r = 0; r < 4; ++r) {
                asm volatile("buffer_load_dwordx4 %0, %1, %2, %3 offen" : "=v"(xv[r][0]) : "v"(v16), "s"(rsX), "s"((unsigned)(row0 + r) * 2048u) : "memory");
                asm volatile("buffer_load_dwordx4 %0, %1, %2, %3 offen offset:1024" : "=v"(xv[r][1]) : "v"(v16), "s"(rsX), "s"((unsigned)(row0 + r) * 2048u) : "memory");
            }
            asm volatile("buffer_load_dwordx4 %0, %1, %2, 0 offen" : "=v"(rv[0]) : "v"(v16), "s"(rsR) : "memory");
            asm volatile("buffer_load_dwordx4 %0, %1, %2, 0 offen offset:1024" : "=v"(rv[1]) : "v"(v16), "s"(rsR) : "memory");
            asm volatile("buffer_load_dwordx4 %0, %1, %2, 0 offen" : "=v"(gv[0]) : "v"(v16), "s"(rsGW) : "memory");
            asm volatile("buffer_load_dwordx4 %0, %1, %2, 0 offen offset:1024" : "=v"(gv[1]) : "v"(v16), "s"(rsGW) : "memory");
            {
                const rsrc_t rsP = mk_rsrc(wphi, 32u * 2048u);
                const unsigned vp = (unsigned)(l ^ (((l >> 4) & 1) << 1)) * 16u;
#pragma unroll
                for (int i = 0; i < 2; ++i) dma16(rsP, vp, (unsigned)hpA * 4096u + a * 2048u + i * 1024u, lds0 + L_WP + a * 2048 + i * 1024);
            }
            CODE_FETCH(pf, slot * 2 + a);
            touch3(pf0, 0, a == 0);
    };
    if (__builtin_expect((w >> 1) == 2, 1)) {
        const int d = w - 4;
        LANE_VARS;
        const int srow = l >> 3, pos = l & 7;
        const rsrc_t rsW = mk_rsrc(wqkv, 1024u * 6144u);
        const unsigned vx0 = srow * 2048 + ((pos ^ ((srow >> 1) & 7)) << 4);
        const unsigned vw0 = srow * 6144 + (((((pos >> 1) ^ (srow & 3)) << 1) | (pos & 1)) << 4);
        unsigned vxs[4], vws[4];
#pragma unroll
        for (int i = 0; i < 4; ++i) {
            vxs[i] = (vx0 ^ ((i & 1) * 64u)) + i * (16384u - 1024u);
            vws[i] = (vw0 ^ ((i & 1) * 32u)) + i * (8u * 6144u - 1024u);
        }
        const unsigned sx0 = (unsigned)(mtA * 128) * 2048u, sw0 = (unsigned)hpA * 128u;
        const int lq = ((slot >> 3) - slot) & 3;
        const unsigned ktab = lq == 0 ? K::KT0 : lq == 1 ? K::KT1 : lq == 2 ? K::KT2 : K::KT3;
        auto chunk = [&](int kt) { return (((ktab >> (4 * (kt >> 1))) & 7u) * 2u + (unsigned)((kt & 1) + rotA)) & 15u; };
        auto dx = [&](unsigned kk, int i, unsigned la) { dma4<K::PACE>(rsX, vxs, sx0 + kk * 128u + i * 65536u, lds0 + la + i * 4096); };
        auto dw = [&](unsigned kk, int p, int i, unsigned la) { dma4<K::PACE>(rsW, vws, sw0 + p * 2048u + (kk * 64u + i * 32u) * 6144u, lds0 + la + i * 4096); };
        asm volatile("s_nop 4" ::: "memory");
        unsigned lb = 0;
#pragma unroll 1
        for (int kt = 0; kt < 13; ++kt) {
            if (kt >= 2) {
                if (kt <= 10) wait_vm<20>(); else wait_vm<16>();
                bar();
            }
            const unsigned kk = chunk(kt);
            dx(kk, 2 * d, lb);
            dw(kk, 1 + d, 0, lb + 24576 + d * 8192);
            dx(kk, 2 * d + 1, lb);
            dw(kk, 1 + d, 1, lb + 24576 + d * 8192);
            if (kt < 10) dw(kk, 0, d, lb + 16384);
            lb = lb == 2 * SLOT ? 0u : lb + SLOT;
        }
        auto dxs = [&](int i) {
            const unsigned kk = chunk(13 + i), lx = d ? XS1[i] - 8192u : XS0[i];
            dx(kk, 2 * d, lx);
            dx(kk, 2 * d + 1, lx);
        };
        auto dws = [&](int i) {
            const unsigned kk = chunk(13 + i), lw = ((1 + i) % 3) * SLOT + 24576 + d * 8192;
            dw(kk, 1 + d, 0, lw);
            dw(kk, 1 + d, 1, lw);
        };
        auto dq = [&](int i) {
            const unsigned kk = chunk(i < 2 ? 14 + i : 8 + i);
            dw(kk, 0, 0, QW[i]);
            dw(kk, 0, 1, QW[i]);
        };
        if (K::XAH) dxs(0);
        wait_vm<K::XAH ? 24 : 16>();
        bar();
        dws(0);
        dxs(K::XAH ? 1 : 0);
        wait_vm<K::XAH ? 24 : 16>();
        bar();
        dws(1);
        dxs(K::XAH ? 2 : 1);
        wait_vm<K::XAH ? 24 : 16>();
        bar();
        dws(2);
        if (!K::XAH) dxs(2);
        if (d == 0) {
            dq(0);
            wait_vm<24>();
        } else {
            wait_vm<16>();
        }
        bar();
        if (d == 0) wait_vm<8>(); else wait_vm<0>();
        post(lds0 + C_ST + d * 4, 1);
        __builtin_amdgcn_s_setprio(3);
        if (d == 0) {
            const rsrc_t rsO = mk_rsrc(wo, 1024u * 2048u);
            unsigned vos[4];
#pragma unroll
            for (int i = 0; i < 4; ++i) vos[i] = WO_VOFF + i * (16384u - 1024u);
            unsigned sb[2] = {0u, 0u};
            auto wo_run = [&](int b0, int b1) {
#pragma unroll
                for (int b = b0; b < b1; ++b) {
                    unsigned so;
                    asm volatile("s_mov_b32 m0, %7\n\ts_add_u32 %0, %6, %8\n\tbuffer_load_dwordx4 %1, %5, %0 offen lds\n\tbuffer_load_dwordx4 %2, %5, %0 offen offset:1024 lds\n\t"
                                 "buffer_load_dwordx4 %3, %5, %0 offen offset:2048 lds\n\tbuffer_load_dwordx4 %4, %5, %0 offen offset:3072 lds\n\ts_waitcnt vmcnt(%9)"
                                 : "=&s"(so) : "v"(vos[0]), "v"(vos[1]), "v"(vos[2]), "v"(vos[3]), "s"(rsO), "s"(sb[b >> 4]), "i"(b * 4096), "i"((b & 15) * 65536), "i"(K::WO_D)
                                 : "memory", "scc");
                }
            };
            dq(1);
            dq(2);
            wait4(lds0 + C_P, P_E15);
            dq(3);
            dq(4);
            wait_vm<0>();
            post(lds0 + C_Q, 1);
            wait4(lds0 + C_P, P_KVD);
            dq(5);
            wait_vm<0>();
            post(lds0 + C_Q, 2);
            wait4(lds0 + C_P, P_PUB);
            {
                const unsigned s0 = (unsigned)(mtB * 128) * 2048u + (unsigned)hpB * 128u;
#pragma unroll
                for (int i = 0; i < 4; ++i) dma4<0>(rsX, vxs, s0 + i * 65536u, lds0 + L_XE + i * 4096);
            }
            lds_wait(lds0 + C_TM, 1);
            sb[1] = (__builtin_amdgcn_readfirstlane(*(AS3 volatile const unsigned*)(size_t)(lds0 + L_TM + 4)) & 1u) * (16u * 65536u) + (unsigned)hpB * 128u;
            sb[0] = sb[1] ^ (16u * 65536u);
            asm volatile("s_nop 4" : "+s"(sb[0]), "+s"(sb[1]));
            wo_run(26, 32);
            post(lds0 + C_XE, 1);
            wait4(lds0 + C_P, P_Q1 + 2);
            wo_run(16, 24);
            wait4(lds0 + C_P, P_QRD);
            wo_run(24, 26);
            wo_run(0, 6);
            post(lds0 + C_WO, 2);
            wo_run(6, 14);
            post(lds0 + C_WO, 3);
            wo_run(14, 16);
            wait_vm<0>();
            post(lds0 + C_WO, 4);
        } else {
            const unsigned v16 = (unsigned)l * 16u;
            const unsigned tag = *(AS3 const unsigned*)(size_t)(lds0 + L_TAG);
            const rsrc_t rsB = mk_rsrc(bulk_base(ws), WB_BYTES);
            lds_wait(lds0 + C_IDX, 1);
            const u32x2 pl = *(AS3 const u32x2*)(size_t)(lds0 + L_PLC);
            const unsigned okp = __builtin_amdgcn_readfirstlane(pl[0]), osm = __builtin_amdgcn_readfirstlane(pl[1]);
            unsigned t1 = 0;
            const unsigned base = now32() - 4096u;
            u32x4 p0, p1;
            CODE_FETCH(p0, slot * 2);
            CODE_FETCH(p1, slot * 2 + 1);
            const int ch = l & 3;
            unsigned mk01 = ch < 2 ? ~0u : 0u, mk0 = ch == 0 ? ~0u : 0u, mk1 = ch == 1 ? ~0u : 0u, mk2 = ch == 2 ? ~0u : 0u, mkl = (l & 31) < 2 ? ~0u : 0u;
            unsigned vkf = (unsigned)((g >> 1) * 16 + c) * 16u + (unsigned)(g & 1) * 8u, vfm = l < 2 ? (unsigned)l * 8u : l == 32 ? 16u : 0x80000000u;
            unsigned kwa = lds0 + L_KVF + l * 16, ra = lds0 + C_R;
            asm volatile("" : "+v"(mk01), "+v"(mk0), "+v"(mk1), "+v"(mk2), "+v"(mkl), "+v"(vkf), "+v"(vfm), "+v"(kwa), "+v"(ra));
            bf16x8 sel;
#pragma unroll
            for (int j = 0; j < 8; ++j) sel[j] = ((c & 7) == j && (c >> 3) == (g & 1)) ? (short)0x3f80 : (short)0;
            asm volatile("" : "+v"(sel));
            {
                unsigned ta, tb;
                auto latest = [&](unsigned off, bool& all) {
                    const u32x4 pa = ld128c(rsWS, v16, off), pb = ld128c(rsWS, v16 + 1024u, off);
                    const unsigned ma = max(pa[1] == tag ? min(pa[0] - base, 8192u) : 0u, pa[3] == tag ? min(pa[2] - base, 8192u) : 0u);
                    const unsigned mb = max(pb[1] == tag ? min(pb[0] - base, 8192u) : 0u, pb[3] == tag ? min(pb[2] - base, 8192u) : 0u);
                    all = __all(pa[1] == tag && pa[3] == tag && pb[1] == tag && pb[3] == tag);
                    ta = __builtin_amdgcn_readfirstlane(wave_umax(ma));
                    tb = __builtin_amdgcn_readfirstlane(wave_umax(mb));
                    return base + max(ta, tb);
                };
                bool all;
                wait4(lds0 + C_P, has_ffn ? K::TMW : P_PUB);
#pragma unroll 1
                for (int it = 0; it < 4; ++it) {
                    t1 = latest(WS_PT, all) + K::D1;
                    if (all) break;
                    wait4(lds0 + C_P, P_PUB);
                    __builtin_amdgcn_s_sleep(K::PT_S);
                }
                asm volatile("s_waitcnt vmcnt(0)" : "+v"(p0), "+v"(p1) :: "memory");
                post(lds0 + L_TM, t1);
                const unsigned t3 = t1 + (K::D2 + K::D3);
                post(lds0 + L_TM + 4, (t3 & ~1u) | (tb < ta ? 1u : 0u));
                post(lds0 + C_TM, 1);
                asm volatile("s_branch .Lpad_r\n\t.p2align 12\n.Lpad_r:\n\ts_cbranch_execz .Lpad_ret" ::: "memory");
                wait4(lds0 + C_P, P_PUB);
            }
            bool late = false;
#pragma unroll 1
            for (int it = 0; it < TRY_LIMIT; ++it) {
                unsigned bad;
                asm volatile(RED1_ASM : [st] "=&s"(bad)
                             : [rsb] "s"(rsB), [t1] "s"(t1), [s0] "s"(okp ^ ((unsigned)mtA * 16384u)), [sm] "s"(osm + (unsigned)((mtA >> 3) * 16384 + (mtA & 7) * 1024)),
                               [sf] "s"(osm + 32768u + (unsigned)mtA * 512u), [sg] "s"(osm + 49152u + (unsigned)mtA * 64u), [hi] "s"(0xffffffff00000000ull),
                               [v16] "v"(v16), [ra] "v"(ra), [tag] "v"(tag), [mk01] "v"(mk01), [mk2] "v"(mk2), [mkl] "v"(mkl), [sel] "v"(sel), [vkf] "v"(vkf), [vfm] "v"(vfm)
                             : "memory", "vcc", "scc", RED1_CLOB);
                if (__builtin_expect(!bad, 1)) break;
                late = true;
                if (it < K::RE1) continue;
                int i2 = 0;
#pragma unroll 1
                for (; i2 < POLL_LIMIT; ++i2) {
                    __builtin_amdgcn_s_sleep(K::RETRY_S);
                    fence_c();
                    if (__all(ld32c(rsB, (unsigned)c * 64u + 36u, osm + (unsigned)((mtA >> 3) * 16384 + (mtA & 7) * 1024)) == tag)) break;
                }
                if (i2 == POLL_LIMIT) break;
            }
            post(lds0 + C_R, R_L1);
            wait4(lds0 + C_P, P_Q1);
            if (late) __builtin_amdgcn_s_sleep(K::L2_S);
            unsigned pv = R_L2I;
#pragma unroll 1
            for (int it = 0; it < TRY_LIMIT; ++it) {
                unsigned bad;
                asm volatile(RED2_ASM : [st] "=&s"(bad), [pv] "+v"(pv)
                             : [rsb] "s"(rsB), [t2] "s"(t1 + K::D2), [s0] "s"(osm + 32768u), [frc] "s"(K::FORCE_REDO && it == 0 ? 1 : 0),
                               [v16] "v"(v16), [ra] "v"(ra), [kwa] "v"(kwa), [tag] "v"(tag), [mk0] "v"(mk0), [mk1] "v"(mk1)
                             : "memory", "vcc", "scc", RED1_CLOB);
                if (__builtin_expect(!bad, 1)) break;
                if (it < K::RE1) continue;
                int i2 = 0;
#pragma unroll 1
                for (; i2 < POLL_LIMIT; ++i2) {
                    __builtin_amdgcn_s_sleep(K::RETRY_S);
                    fence_c();
                    if (__all(ld32c(rsB, (unsigned)c * 64u + 20u, osm + 49152u) == tag)) break;
                }
                if (i2 == POLL_LIMIT) break;
            }
            post(ra, pv + 1u);
            asm volatile(".Lff_end1:");
        }
        return;
    }

    asm volatile(".Lpad_d:\n\ts_cbranch_execz .Lpad_top" ::: "memory");
    __builtin_amdgcn_sched_barrier(0);
    if (w >= 6) {
        const int a = w - 6;
        const int cb = fb >> 2, kq = fb & 3;
        u32x4 wg_[16], wu_[16], wd_[16];
        unsigned tag;
        int idx;
        u32x4 xs[2];
        {
        LANE_VARS;
        const int srow = l >> 3, pos = l & 7;
        const unsigned nb = has_ffn ? 1024u * 7168u : 0u;
        const rsrc_t rsG = mk_rsrc(wgp, nb), rsU = mk_rsrc(wup, nb), rsD = mk_rsrc(wdp, nb);
        const unsigned vf = (unsigned)(16 * srow) * 7168u + (unsigned)pos * 16u;
        const int k8 = fb & 7, cg = (fb >> 4) * 4 + ((fb >> 3) & 1) * 2 + a, cu = cg ^ 2;
        const unsigned sg = (unsigned)(k8 * 128) * 7168u + (unsigned)cg * 128u, su = sg ^ 256u;
        auto ldg = [&]() {
#pragma unroll
            for (int i = 0; i < 16; ++i) {
                asm volatile("buffer_load_dwordx4 %0, %1, %2, %3 offen nt" : "=v"(wg_[i]) : "v"(vf), "s"(rsG), "s"(sg + i * 7168u) : "memory");
            }
        };
        auto ldu = [&]() {
#pragma unroll
            for (int i = 0; i < 16; ++i) {
                asm volatile("buffer_load_dwordx4 %0, %1, %2, %3 offen nt" : "=v"(wu_[i]) : "v"(vf), "s"(rsU), "s"(su + i * 7168u) : "memory");
            }
        };
        auto ldd = [&](int i0, int i1) {
            const unsigned vd = (unsigned)g * (16u * 2048u) + (unsigned)c * 16u;
            const unsigned sd = (unsigned)(cb * 64) * 2048u + (unsigned)(kq * 512 + a * 256);
#pragma unroll
            for (int i = i0; i < i1; ++i) {
                if ((i & 3) == 0) { wait_vm<K::FD>(); hush(); }
                asm volatile("buffer_load_dwordx4 %0, %1, %2, %3 offen nt" : "=v"(wd_[i]) : "v"(vd), "s"(rsD), "s"(sd + i * 2048u) : "memory");
            }
        };
        const int row0 = mtB * 128 + hpB * 8 + 4 * a;
        if (a == 0) {
            *(AS3 unsigned*)(size_t)(lds0 + L_SM + l * 4) = 0u;
            wait_lgkm0();
        }
        int b = 0;
        unsigned tl = 0, pmin = 255;
        auto tick = [&]() {
            bar();
            if (a == 1) {
                const unsigned t = now32();
                if (b > 2 && b <= 16 - K::ND) pmin = min(pmin, t - tl);
                tl = t;
            }
            ++b;
        };
        auto upto = [&](int e) {
#pragma unroll 1
            while (b <= e) tick();
        };
        auto pads = [&]() {
#pragma unroll 1
            for (int i = a; i < 6; i += 2) {
                unsigned long long ex;
                asm volatile("s_mov_b64 %0, exec\n\ts_mov_b64 exec, 0\n\t"
                             "s_cmp_eq_u32 %1, 0\n\ts_cbranch_scc1 .Lpad_ab1\n\ts_cmp_eq_u32 %1, 1\n\ts_cbranch_scc1 .Lpad_ab2\n\t"
                             "s_cmp_eq_u32 %1, 2\n\ts_cbranch_scc1 .Lpad_c0\n\ts_cmp_eq_u32 %1, 3\n\ts_cbranch_scc1 .Lpad_c1\n\t"
                             "s_cmp_eq_u32 %1, 4\n\ts_cbranch_scc1 .Lpad_r\n"
                             ".Lpad_ret:\n\ts_mov_b64 exec, %0" : "=&s"(ex) : "s"(i) : "scc", "memory");
                if (i < 4 && b <= 9) tick();
            }
        };
        auto nap = [&](int n) {
#pragma unroll 1
            for (int i = 0; i < n; ++i) __builtin_amdgcn_s_sleep(1);
        };
        const int lq4 = ((((slot >> 3) - slot) & 3) + (slot & 4)) * 4;
        const int bg = ((unsigned)K::BGT >> lq4) & 15, bu = ((unsigned)K::BUT >> lq4) & 15;
        unsigned pf3 = 0;
        ldi();
        u32x2 gr;
        unsigned vgr;
        auto router = [&](auto n_) {
            tick();
            asm volatile("s_waitcnt vmcnt(%15)"
                         : "+v"(ep), "+v"(xv[0][0]), "+v"(xv[0][1]), "+v"(xv[1][0]), "+v"(xv[1][1]), "+v"(xv[2][0]), "+v"(xv[2][1]), "+v"(xv[3][0]),
                           "+v"(xv[3][1]), "+v"(rv[0]), "+v"(rv[1]), "+v"(gv[0]), "+v"(gv[1]), "+v"(pf), "+v"(pf0)
                         : "i"(decltype(n_)::value) : "memory");
            tag = __builtin_amdgcn_readfirstlane(ep) * 2654435761u + 0x9e3779b9u;
            float ra[4], ga[4];
#pragma unroll
            for (int r = 0; r < 4; ++r) {
                float racc = 0.f, gacc = 0.f;
#pragma unroll
                for (int i = 0; i < 2; ++i)
#pragma unroll
                    for (int j = 0; j < 4; ++j) {
                        racc = dot2(xv[r][i][j], rv[i][j], racc);
                        gacc = dot2(xv[r][i][j], gv[i][j], gacc);
                    }
                ra[r] = racc;
                ga[r] = gacc;
                asm volatile("" : "+v"(ra[r]), "+v"(ga[r]));
            }
            tick();
            float r2[2], g2[2];
#pragma unroll
            for (int i = 0; i < 2; ++i) {
                auto p = __builtin_amdgcn_permlane32_swap(FU(ra[2 * i]), FU(ra[2 * i + 1]), false, false);
                r2[i] = UF(p[0]) + UF(p[1]);
                auto q = __builtin_amdgcn_permlane32_swap(FU(ga[2 * i]), FU(ga[2 * i + 1]), false, false);
                g2[i] = UF(q[0]) + UF(q[1]);
            }
            auto p = __builtin_amdgcn_permlane16_swap(FU(r2[0]), FU(r2[1]), false, false);
            auto q = __builtin_amdgcn_permlane16_swap(FU(g2[0]), FU(g2[1]), false, false);
            float rs = row16_sum(UF(p[0]) + UF(p[1])), gs = row16_sum(UF(q[0]) + UF(q[1]));
            asm volatile("" : "+v"(rs), "+v"(gs));
            const int row = row0 + (g >> 1) + 2 * (g & 1);
            const unsigned rbits = f2bf(rs);
            const unsigned okey = (rbits & 0x8000u) ? (~rbits & 0xffffu) : (rbits | 0x8000u);
            if (c == 0) *(AS3 unsigned*)(size_t)(lds0 + L_KEY + (4 * a + g) * 4) = (okey << 16) | (unsigned)(0xffff - row);
            if (l == 0 && a == 0) *(AS3 unsigned*)(size_t)(lds0 + L_TAG) = tag;
            gr = u32x2{FU(rbf(1.f / (1.f + __expf(-rbf(gs))))), tag};
            vgr = c == 0 ? WS_G + (unsigned)row * 8u : 0x80000000u;
            asm volatile("" : "+v"(gr), "+v"(vgr));
            wait_lgkm0();
            tick();
            if (a == 0) {
                unsigned k8 = *(AS3 const unsigned*)(size_t)(lds0 + L_KEY + (l & 7) * 4);
                k8 = max(k8, (unsigned)__shfl_xor((int)k8, 1));
                k8 = max(k8, (unsigned)__shfl_xor((int)k8, 2));
                k8 = max(k8, (unsigned)__shfl_xor((int)k8, 4));
                const u32x4 pv = *(AS3 const u32x4*)(size_t)(lds0 + L_PRB);
                const unsigned lo = min(min(pv[0] & 0xffffu, pv[2] & 0xffffu), min(pv[1] >> 16, pv[3] >> 16));
                const unsigned up = min(min(pv[0] >> 16, pv[2] >> 16), min(pv[1] & 0xffffu, pv[3] & 0xffffu));
                st64c(u32x2{k8 ^ (up < lo ? 0x2000u : 0u), tag}, rsWS, l ? 0x80000000u : WS_RG + (unsigned)bid * 8u, 0);
            }
        };
        u32x4 ga, gb;
        const unsigned vr = WS_RG + (unsigned)l * 16u;
        auto ldrg = [&]() {
            asm volatile("buffer_load_dwordx4 %0, %1, %2, 0 offen sc1" : "=v"(ga) : "v"(vr), "s"(rsWS) : "memory");
            asm volatile("buffer_load_dwordx4 %0, %1, %2, 0 offen offset:1024 sc1" : "=v"(gb) : "v"(vr), "s"(rsWS) : "memory");
        };
        router(std::integral_constant<int, 0>{});
        __builtin_amdgcn_s_setprio(2);
        upto(bg);
        nap(K::SLG);
        if (a == 0) {
            touch3(pf3, 1);
        } else {
            const rsrc_t rsT = mk_rsrc(wqkv, 1024u * 6144u);
            asm volatile("s_nop 4\n\tbuffer_load_dword %0, %1, %2, %3 offen" : "=v"(pf3) : "v"((unsigned)(l & 31) * 6144u), "s"(rsT),
                         "s"((unsigned)(((12 + rotA) & 15) * 64 + (slot & 7) * 32) * 6144u + (unsigned)hpA * 128u) : "memory");
        }
        ldg();
        upto(bu);
        if (bu != bg) nap(K::SLG);
        ldu();
        upto(((unsigned)K::BRT >> lq4) & 15);
        ldrg();
        pads();
        upto(9);
        asm volatile("s_waitcnt vmcnt(0)"
                     : "+v"(wg_[0]), "+v"(wg_[1]), "+v"(wg_[2]), "+v"(wg_[3]), "+v"(wg_[4]), "+v"(wg_[5]), "+v"(wg_[6]), "+v"(wg_[7]),
                       "+v"(wg_[8]), "+v"(wg_[9]), "+v"(wg_[10]), "+v"(wg_[11]), "+v"(wg_[12]), "+v"(wg_[13]), "+v"(wg_[14]), "+v"(wg_[15]),
                       "+v"(ga), "+v"(gb), "+v"(pf3)
                     :: "memory");
        asm volatile(""
                     : "+v"(wu_[0]), "+v"(wu_[1]), "+v"(wu_[2]), "+v"(wu_[3]), "+v"(wu_[4]), "+v"(wu_[5]), "+v"(wu_[6]), "+v"(wu_[7]),
                       "+v"(wu_[8]), "+v"(wu_[9]), "+v"(wu_[10]), "+v"(wu_[11]), "+v"(wu_[12]), "+v"(wu_[13]), "+v"(wu_[14]), "+v"(wu_[15])
                     :: "memory");
#pragma unroll 1
        for (int it = 0; it < POLL_LIMIT; ++it) {
            if (__all((ga[1] == tag) && (ga[3] == tag) && (gb[1] == tag) && (gb[3] == tag))) break;
            __builtin_amdgcn_s_sleep(4 * K::RETRY_S);
            fence_c();
            ga = ld128c(rsWS, vr, 0);
            gb = ld128c(rsWS, vr + 1024u, 0);
        }
        const unsigned key = __builtin_amdgcn_readfirstlane(wave_umax(max(max(ga[0] | 0x3800u, ga[2] | 0x3800u), max(gb[0] | 0x3800u, gb[2] | 0x3800u))));
        idx = (0xffff - (int)(key & 0xffffu)) & 2047;
        unsigned tw[2];
        auto twf = [&]() {
            const rsrc_t rsO = mk_rsrc(wo, 1024u * 2048u);
            const unsigned vt = (unsigned)srow * 2048u + (unsigned)pos * 128u, st = (unsigned)(slot * 32 + a * 16) * 2048u + (unsigned)(xcd >> 2) * 1024u;
            nap(K::X1);
            if (K::TNT) {
            asm volatile("s_nop 4\n\tbuffer_load_dword %0, %1, %2, %3 offen nt" : "=v"(tw[0]) : "v"(vt), "s"(rsO), "s"(st) : "memory");
            asm volatile("buffer_load_dword %0, %1, %2, %3 offen nt" : "=v"(tw[1]) : "v"(vt), "s"(rsO), "s"(st + 16384u) : "memory");
            } else {
            asm volatile("s_nop 4\n\tbuffer_load_dword %0, %1, %2, %3 offen" : "=v"(tw[0]) : "v"(vt), "s"(rsO), "s"(st) : "memory");
            asm volatile("buffer_load_dword %0, %1, %2, %3 offen" : "=v"(tw[1]) : "v"(vt), "s"(rsO), "s"(st + 16384u) : "memory");
            }
        };
        tick();
        if (K::TWB == 10) twf();
        {
            const unsigned vs = (unsigned)srow * 32u, ss = (unsigned)idx * 2048u + (unsigned)(k8 * 256);
            asm volatile("s_nop 4\n\tbuffer_load_dwordx4 %0, %1, %2, %3 offen" : "=v"(xs[0]) : "v"(vs), "s"(rsX), "s"(ss) : "memory");
            asm volatile("buffer_load_dwordx4 %0, %1, %2, %3 offen offset:16" : "=v"(xs[1]) : "v"(vs), "s"(rsX), "s"(ss) : "memory");
        }
        if (a == 1) {
            const u32x2 pt = {tl + (pmin * (K::PRED_K + 12) >> 2) + K::PRED_C + K::XE, tag};
            st64c(pt, rsWS, l ? 0x80000000u : WS_PT + (unsigned)(hpA * 16 + mtA) * 8u, 0);
        } else {
            const unsigned kb = key >> 16;
            const unsigned rb_ = (kb & 0x8000u) ? (kb & 0x7fffu) : (~kb & 0xffffu);
            const float rsel = rbf(1.f / (1.f + __expf(-UF(rb_ << 16))));
            const u32x2 iv = {(unsigned)idx, FU(rsel)};
            *(AS3 u32x2*)(size_t)(lds0 + L_IDX) = iv;
            const unsigned nv = (~ga[0] & 0x3800u) + (~ga[2] & 0x3800u) + (~gb[0] & 0x3800u) + (~gb[2] & 0x3800u);
            const unsigned votes = wave_usum((l & 3) == (xcd >> 1) ? nv >> 11 : 0u);
            const unsigned o0 = (unsigned)(xcd >> 1) * (2u << 20) + (K::X2 < 2 ? K::X2 * 8192u : votes > 128u ? 8192u : 0u);
            const u32x4 pl = {o0 + WB_KVP + (((unsigned)(hpA & 3) * 8u) ^ (tag & 31u)) * 32768u, o0 + WB_SM + (((unsigned)hpA & 3u) ^ ((tag >> 5) & 7u)) * 65536u,
                              ((tag >> 8) & 63u) * 2048u, 0u};
            *(AS3 u32x4*)(size_t)(lds0 + L_PLC) = pl;
            post(lds0 + C_IDX, 1);
            wait_lgkm0();
        }
        unsigned mk[4];
#pragma unroll
        for (int k = 0; k < 4; ++k) mk[k] = (pos >> 1) == k ? ((pos & 1) ? 0xffff0000u : 0x0000ffffu) : 0u;
        f32x4 sg4 = {0.f, 0.f, 0.f, 0.f}, su4 = sg4;
        auto dot = [&](int i0, int i1) {
#pragma unroll
            for (int i = i0; i < i1; ++i) {
                const unsigned xw = xs[i >> 3][(i >> 1) & 3];
                const unsigned xd = __builtin_amdgcn_perm(xw, xw, (i & 1) ? 0x03020302u : 0x01000100u);
                const u32x4 am = {xd & mk[0], xd & mk[1], xd & mk[2], xd & mk[3]};
                sg4 = mfma16(__builtin_bit_cast(bf16x8, am), __builtin_bit_cast(bf16x8, wg_[i]), sg4);
                su4 = mfma16(__builtin_bit_cast(bf16x8, am), __builtin_bit_cast(bf16x8, wu_[i]), su4);
            }
            asm volatile("" : "+v"(sg4), "+v"(su4));
        };
        tick();
        asm volatile("s_waitcnt vmcnt(0)" : "+v"(xs[0]), "+v"(xs[1]) :: "memory");
        if (K::TWB == 11) twf();
        dot(0, K::DS1);
        tick();
        dot(K::DS1, K::DS2);
        if (K::TWB == 12) twf();
        tick();
        if (a == 1) {
            post(lds0 + L_TM + 8, tl + (pmin * (K::PRED_K + K::PK13) >> 2) + K::PRED_C);
            wait_lgkm0();
        }
        dot(K::DS2, K::DS3);
        if (K::TWB == 13) twf();
        __builtin_amdgcn_s_setprio(3);
        tick();
        ldd(0, 8);
        dot(K::DS3, 16);
        {
            unsigned fo[4];
#pragma unroll
            for (int r = 0; r < 4; ++r) {
                auto p = __builtin_amdgcn_permlane32_swap(FU(sg4[r]), FU(su4[r]), false, false);
                fo[r] = FU(UF(p[0]) + UF(__builtin_amdgcn_update_dpp(0, (int)p[1], 0x128, 0xf, 0xf, false)));
            }
            const unsigned vf0 = ((l & 8) || !has_ffn) ? 0x80000000u : WS_FG + (unsigned)(((l >> 5) ? cu : cg) * 8 + k8) * 1024u + (unsigned)((l >> 5) * 512 + (pos * 8 + (g & 1) * 4) * 8);
            const u32x4 s0 = {fo[0], tag, fo[1], tag}, s1 = {fo[2], tag, fo[3], tag};
            st128c(s0, rsWS, vf0, 0);
            st128c(s1, rsWS, vf0 + 16u, 0);
            st64c(gr, rsWS, vgr, 0);
        }
        if (bid == 0 && a == 0 && l == 0) st32c(tag, rsWS, WS_EPOCH, 0);
        ldd(8, 16);
        lds_wait(lds0 + C_TM, 1);
        const unsigned tm0 = *(AS3 const unsigned*)(size_t)(lds0 + L_TM);
        const unsigned vq = (unsigned)((l & 8) * 64 + (g * 8 + (l & 7)) * 16);
        u32x4 fq[8];
        if (has_ffn) {
            wait_until(__builtin_amdgcn_readfirstlane(tm0) + K::FG_AT);
#pragma unroll 1
            for (int it = 0; it < POLL_LIMIT; ++it) {
                unsigned bad = 0;
#pragma unroll
                for (int i = 0; i < 8; ++i) fq[i] = ld128c(rsWS, vq + (i & 3) * 1024u, WS_FG + (unsigned)cb * 8192u + (i >> 2) * 4096u);
#pragma unroll
                for (int i = 0; i < 8; ++i) bad |= (fq[i][1] ^ tag) | (fq[i][3] ^ tag);
                if (__all(bad == 0u)) break;
                __builtin_amdgcn_s_sleep(2 * K::RETRY_S);
                fence_c();
            }
        }
        asm volatile("s_waitcnt vmcnt(0)"
                     : "+v"(wd_[0]), "+v"(wd_[1]), "+v"(wd_[2]), "+v"(wd_[3]), "+v"(wd_[4]), "+v"(wd_[5]), "+v"(wd_[6]), "+v"(wd_[7]),
                       "+v"(wd_[8]), "+v"(wd_[9]), "+v"(wd_[10]), "+v"(wd_[11]), "+v"(wd_[12]), "+v"(wd_[13]), "+v"(wd_[14]), "+v"(wd_[15]),
                       "+v"(tw[0]), "+v"(tw[1])
                     :: "memory");
        if (has_ffn) {
            if (K::DWP) lds_wait(lds0 + C_P + (2 + a) * 4, P_PUB);
            __builtin_amdgcn_s_setprio(K::HP);
            unsigned dp[8][8];
#pragma unroll
            for (int m = 0; m < 8; ++m)
#pragma unroll
                for (int j = 0; j < 4; ++j) {
                    dp[m][2 * j] = __builtin_amdgcn_perm(wd_[2 * m + 1][j], wd_[2 * m][j], 0x05040100u);
                    dp[m][2 * j + 1] = __builtin_amdgcn_perm(wd_[2 * m + 1][j], wd_[2 * m][j], 0x07060302u);
                }
            float hv[2];
#pragma unroll
            for (int j = 0; j < 2; ++j) {
                const float gs = rbf(((UF(fq[0][2 * j]) + UF(fq[1][2 * j])) + (UF(fq[2][2 * j]) + UF(fq[3][2 * j]))) +
                                     ((UF(fq[4][2 * j]) + UF(fq[5][2 * j])) + (UF(fq[6][2 * j]) + UF(fq[7][2 * j]))));
                const float us = UF(__builtin_amdgcn_update_dpp(0, (int)FU(gs), 0x128, 0xf, 0xf, false));
                hv[j] = rbf(gs / (1.f + __expf(-gs))) * us;
            }
            const unsigned hp = pack2(hv[0], hv[1]);
            float ya[8];
#pragma unroll
            for (int j = 0; j < 8; ++j) ya[j] = 0.f;
            auto dn = [&](auto mc) {
                constexpr int m = decltype(mc)::value;
                const unsigned hb = (unsigned)__builtin_amdgcn_update_dpp(0, (int)hp, 0x150 + m, 0xf, 0xf, false);
#pragma unroll
                for (int j = 0; j < 8; ++j) ya[j] = dot2(dp[m][j], hb, ya[j]);
            };
            dn(std::integral_constant<int, 0>{}); dn(std::integral_constant<int, 1>{}); dn(std::integral_constant<int, 2>{}); dn(std::integral_constant<int, 3>{});
            dn(std::integral_constant<int, 4>{}); dn(std::integral_constant<int, 5>{}); dn(std::integral_constant<int, 6>{}); dn(std::integral_constant<int, 7>{});
            float y4[4], y2[2];
#pragma unroll
            for (int j = 0; j < 4; ++j) {
                auto p = __builtin_amdgcn_permlane32_swap(FU(ya[j]), FU(ya[4 + j]), false, false);
                y4[j] = UF(p[0]) + UF(p[1]);
            }
#pragma unroll
            for (int j = 0; j < 2; ++j) {
                auto p = __builtin_amdgcn_permlane16_swap(FU(y4[j]), FU(y4[2 + j]), false, false);
                y2[j] = UF(p[0]) + UF(p[1]);
            }
            const u32x4 st = {FU(y2[0]), tag, FU(y2[1]), tag};
            st128c(st, rsWS, (unsigned)(c * 64 + g * 16), WS_Y + (unsigned)cb * 8192u + (unsigned)(kq * 2048 + a * 1024));
        }
        {
            const unsigned col8 = (unsigned)(xcd * 128 + slot * 4 + a * 2) * 8u;
            const unsigned vy = l < 56 ? (unsigned)l * 8192u : 0x80000000u;
            u32x4 t;
            __builtin_amdgcn_s_setprio(3);
            wait_until(__builtin_amdgcn_readfirstlane(tm0) + K::Y_AT);
#pragma unroll 1
            for (int it = 0; it < POLL_LIMIT; ++it) {
                t = ld128c(rsWS, vy, WS_Y + col8);
                if (__all(l >= 56 || (t[1] == tag && t[3] == tag))) break;
                __builtin_amdgcn_s_sleep(4 * K::RETRY_S);
                fence_c();
            }
            auto p = __builtin_amdgcn_permlane32_swap(t[0], t[2], false, false);
            const float s = row16_sum(x16_sum(UF(p[0]) + UF(p[1])));
            st64c(u32x2{FU(s), tag}, rsWS, (l & 31) ? 0x80000000u : (unsigned)(l >> 2), WS_YF + col8);
        }
        if ((idx >> 7) == mtB && a == 0) {
            u32x4 t;
            wait_until(__builtin_amdgcn_readfirstlane(tm0) + K::Y_AT + K::X0);
#pragma unroll 1
            for (int it = 0; it < POLL_LIMIT; ++it) {
                t = ld128c(rsWS, (unsigned)(l & 31) * 16u, WS_YF + (unsigned)hpB * 512u);
                if (__all(t[1] == tag && t[3] == tag)) break;
                __builtin_amdgcn_s_sleep(2 * K::RETRY_S);
                fence_c();
            }
            lds_wait(lds0 + C_XE, 1);
            const int rr = idx & 127;
            const unsigned xx = *(AS3 const unsigned*)(size_t)(lds0 + L_XE + rr * 128 + ((((l & 31) >> 2) ^ ((rr >> 1) & 7)) << 4) + (l & 3) * 4);
            const float rsel = *(AS3 const float*)(size_t)(lds0 + L_IDX + 4);
            *(AS3 f32x2*)(size_t)(lds0 + L_Y + 256 + (l & 31) * 8) = f32x2{rbf(lo16(xx) + rbf(rbf(UF(t[0])) * rsel)), rbf(hi16(xx) + rbf(rbf(UF(t[2])) * rsel))};
            post(lds0 + C_YRDY, 1);
        }
        }
        asm volatile(".Lff_end0:");
        return;
    }

    const int v = w & 1, hd = w >> 1;
    const rsrc_t rsB = mk_rsrc(bulk_base(ws), WB_BYTES);
    unsigned tag, sof, t3, rid;
    unsigned long long rm;
    u32x16 ad;
    u32x4 g4;
    {
        const unsigned pi = (unsigned)((slot * 2 + (xcd & 1)) * 4 + w);
        const char* pa = bulk_base(ws) + ((unsigned)(xcd >> 1) * (2u << 20) + WB_PRB + (pi >> 5) * 16384u + (pi & 31u) * 256u + v * 8192u);
        const unsigned po = 8192u + v * 128u;
        unsigned j0, j1;
        asm volatile("s_load_dword %0, %2, 64\n\ts_load_dword %1, %2, %3" : "={s80}"(j0), "={s81}"(j1) : "s"(pa), "s"(po + 64u) : "memory");
        LANE_VARS;
        ad[0] = lds0 + (unsigned)(64 * v + c) * 128u + (unsigned)((g ^ ((c >> 1) & 7)) << 4);
        ad[1] = lds0 + 16384u + (WSW ^ (unsigned)(hd << 6));
        ad[2] = lds0 + L_VF + (unsigned)(hd * 2048 + v * 8192 + l * 16);
        ad[3] = lds0 + L_PHI + hd * 16384 + (unsigned)(64 * v + c) * 128u + (unsigned)((((c & 3) ^ ((c >> 2) & 1)) << 5) + ((g ^ (c >> 2)) << 3));
        ad[4] = lds0 + L_PHI + hd * 16384 + v * 8192 + ((PSW + (unsigned)((i3 ^ g) << 3)) ^ (unsigned)(v << 6));
        ad[5] = lds0 + L_WP + hd * 2048 + WPO;
        ad[6] = (unsigned)(l >> 5) * 16384u + (unsigned)(l & 31) * 16u;
        ad[7] = c < 3 ? (unsigned)(((g & 1) * 2 + (g >> 1)) * 1024 + c * 16) : 0x80000000u;
        ad[8] = lds0 + C_P + w * 4;
        ad[9] = lds0 + L_SM;
        ad[10] = c;
#pragma unroll
        for (int i = 11; i < 16; ++i) ad[i] = 0u;
        asm volatile(PHASE_AB_ASM
                     : "+{v[240:255]}"(ad), "={v[232:235]}"(g4), "={v236}"(tag), [sof] "=&s"(sof), [t3] "=&s"(t3), [rid] "=&s"(rid), [rm] "=&s"(rm), "+{s80}"(j0), "+{s81}"(j1)
                     : [rsb] "s"(rsB), [rsw] "s"(rsWS), [xs0] "s"(v ? XS1[0] - 8192u : XS0[0]), [xs1] "s"(v ? XS1[1] - 8192u : XS0[1]), [kpx] "s"(w * 65536),
                       [kpa] "s"(mtA * 512), [kma] "s"(hd * 16384 + v * 4096 + mtA * 64), [sg] "s"(WS_G + (unsigned)(mtB * 128 + 32 * w) * 8u),
                       [ofc] "s"(WB_OF + (unsigned)(mtB * 4 + w) * 65536u), [ofp] "s"(WB_OF + (unsigned)((mtA * 4 + 2 * v) * 32 + ((2 * hpA + hd + 1) & 31)) * 2048u),
                       [ogp] "s"(WB_OG + (unsigned)((mtA * 4 + 2 * v) * 32 + 2 * hpA + hd) * 8u), [kac] "s"(L_KVF + hd * 4096), [zac] "s"(L_ZF + hd * 512),
                       [xec] "s"(L_XE + w * 4096), [own] "s"(mtB * 4 + w), [prb] "s"(pa), [pro] "s"(po)
                     : "memory", "vcc", "scc", PHASE_AB_CLOB);
    }
    {
        const rsrc_t rsOut = mk_rsrc(out, 2048u * 2048u);
        const unsigned sog = WB_OG + sof + (unsigned)(mtB * 4 + w) * 256u;
        unsigned grp = (t3 & 1u) * 2u, flip = 0;
#pragma unroll 1
        for (int att = 0; att < TRY_LIMIT; ++att) {
            unsigned cs, st;
            asm volatile(PHASE_C_ASM
                         : "={v230}"(cs), [st] "=&s"(st)
                         : "{v[240:255]}"(ad), "{v[232:235]}"(g4), "{v236}"(tag), [rsb] "s"(rsB), [rso] "s"(rsOut), [sog] "s"(sog), [grp] "s"(grp), [flip] "s"(flip),
                           [t3] "s"(t3), [rid] "s"(rid), [rm] "s"(rm), [sout] "s"((unsigned)(mtB * 128 + 32 * w) * 2048u + (unsigned)hpB * 128u)
                         : "memory", "vcc", "scc", PHASE_C_CLOB);
            if (K::FORCE_REDO && att < 2) st = 2 - att, cs += 1u;
            if (__builtin_expect(st == 0u, 1)) break;
            u32x2 og = ld64c(rsB, ad[4], sog);
            const unsigned miss = (unsigned)__builtin_amdgcn_ballot_w64(og[1] != tag);
            if (st == 1u && ((miss && !(miss & ~(0xffffu << (8u * grp)))) || K::FORCE_REDO)) {
                grp ^= 2u;
                flip ^= 65536u;
                lds_wait(lds0 + C_WO, 4);
                continue;
            }
            int it = 0;
#pragma unroll 1
            for (; it < POLL_LIMIT && !__all(og[1] == tag); ++it) {
                __builtin_amdgcn_s_sleep(K::RETRY_S);
                fence_c();
                og = ld64c(rsB, ad[4], sog);
            }
            if (it == POLL_LIMIT) break;
            if (st == 2u) {
                if (__all(wave_usum(cs - (ad[10] < 32u ? og[0] : 0u)) == 0u)) break;
            }
        }
    }
    asm volatile(".Lff_end2:\n.Lpad_x:\n\ts_cbranch_execz .Lpad_top");
}

extern "C" void kernel_launch(void* const* d_in, const int* in_sizes, int n_in, void* d_out, int out_size,
                              void* d_ws, size_t ws_size, hipStream_t stream) {
    const u16* x = (const u16*)d_in[0];
    const u16* wqkv = (const u16*)d_in[1];
    const u16* wphi = (const u16*)d_in[2];
    const u16* wo = (const u16*)d_in[3];
    const u16* wr = (const u16*)d_in[5];
    const u16* wg = (const u16*)d_in[6];
    const u16* wu = (const u16*)d_in[7];
    const u16* wd = (const u16*)d_in[8];
    const u16* gw = (const u16*)d_in[9];
    fused_fwd<<<256, 512, 0, stream>>>(x, wqkv, wphi, wo, wr, wg, wu, wd, gw, (char*)d_ws, (u16*)d_out);
}
```

```cpp
#define DBG_BID 0
#define DBG_K 10
#include <hip/hip_runtime.h>
#include <stdint.h>
#include <type_traits>

typedef __attribute__((ext_vector_type(8))) short bf16x8;
typedef __attribute__((ext_vector_type(4))) short s16x4;
typedef __attribute__((ext_vector_type(4))) float f32x4;
typedef __attribute__((ext_vector_type(2))) float f32x2;
typedef __attribute__((ext_vector_type(32))) float f32x32;
typedef __attribute__((ext_vector_type(2))) unsigned u32x2;
typedef __attribute__((ext_vector_type(4))) unsigned u32x4;
typedef __attribute__((ext_vector_type(8))) unsigned u32x8;
typedef __attribute__((ext_vector_type(16))) unsigned u32x16;
typedef __attribute__((ext_vector_type(32))) unsigned u32x32;
typedef __attribute__((ext_vector_type(2))) __bf16 bf16x2_t;
typedef unsigned short u16;
typedef __amdgpu_buffer_rsrc_t rsrc_t;

#define DI __device__ __forceinline__
#define AS3 __attribute__((address_space(3)))
#define RLX_WG __ATOMIC_RELAXED, __HIP_MEMORY_SCOPE_WORKGROUP

constexpr int AUX_SC1 = 16;

DI float bf2f(u16 v) { return __uint_as_float(((unsigned)v) << 16); }
DI u16 f2bf(float f) { __bf16 b = (__bf16)f; return __builtin_bit_cast(u16, b); }
DI float rbf(float f) { return bf2f(f2bf(f)); }
DI unsigned pack2(float lo, float hi) {
    f32x2 v = {lo, hi};
    bf16x2_t b = __builtin_convertvector(v, bf16x2_t);
    return __builtin_bit_cast(unsigned, b);
}
DI float lo16(unsigned u) { return __uint_as_float(u << 16); }
DI float hi16(unsigned u) { return __uint_as_float(u & 0xffff0000u); }
DI float dot2(unsigned a, unsigned b, float c) {
    return __builtin_amdgcn_fdot2_f32_bf16(__builtin_bit_cast(bf16x2_t, a), __builtin_bit_cast(bf16x2_t, b), c, false);
}
DI s16x4 tr_read(unsigned a) { return __builtin_amdgcn_ds_read_tr16_b64_v4i16((AS3 s16x4*)(size_t)a); }
DI bf16x8 cat8(s16x4 a, s16x4 b) { return __builtin_shufflevector(a, b, 0, 1, 2, 3, 4, 5, 6, 7); }
DI bf16x8 lds_rd128(unsigned a) { return *(AS3 const bf16x8*)(size_t)a; }
DI void lds_wr128(unsigned a, bf16x8 v) { *(AS3 bf16x8*)(size_t)a = v; }
DI f32x4 mfma16(bf16x8 a, bf16x8 b, f32x4 c) { return __builtin_amdgcn_mfma_f32_16x16x32_bf16(a, b, c, 0, 0, 0); }
DI bf16x8 pack44(f32x4 a, f32x4 b) {
    u32x4 p = {pack2(a[0], a[1]), pack2(a[2], a[3]), pack2(b[0], b[1]), pack2(b[2], b[3])};
    return __builtin_bit_cast(bf16x8, p);
}
#define FU(x) __float_as_uint(x)
#define UF(x) __uint_as_float(x)
DI float x32_sum(float v) { auto r = __builtin_amdgcn_permlane32_swap(FU(v), FU(v), false, false); return UF(r[0]) + UF(r[1]); }
DI float x16_sum(float v) { auto r = __builtin_amdgcn_permlane16_swap(FU(v), FU(v), false, false); return UF(r[0]) + UF(r[1]); }
DI float xg_sum(float v) { return x16_sum(x32_sum(v)); }
template <int CTRL> DI float dpp_add(float v) { return v + UF(__builtin_amdgcn_update_dpp(0, (int)FU(v), CTRL, 0xf, 0xf, false)); }
DI float row16_sum(float v) { return dpp_add<0x121>(dpp_add<0x122>(dpp_add<0x124>(dpp_add<0x128>(v)))); }
template <int CTRL> DI unsigned dpp_umax(unsigned v) { return max(v, (unsigned)__builtin_amdgcn_update_dpp(0, (int)v, CTRL, 0xf, 0xf, false)); }
DI unsigned wave_umax(unsigned v) {
    auto r = __builtin_amdgcn_permlane32_swap(v, v, false, false);
    v = max(r[0], r[1]);
    auto q = __builtin_amdgcn_permlane16_swap(v, v, false, false);
    v = max(q[0], q[1]);
    return dpp_umax<0x121>(dpp_umax<0x122>(dpp_umax<0x124>(dpp_umax<0x128>(v))));
}
DI unsigned add4(u32x4 v, unsigned s) { return (v[0] + v[1] + v[2]) + (v[3] + s); }
template <int CTRL> DI unsigned dpp_uadd(unsigned v) { return v + (unsigned)__builtin_amdgcn_update_dpp(0, (int)v, CTRL, 0xf, 0xf, false); }
DI unsigned row16_usum(unsigned v) { return dpp_uadd<0x121>(dpp_uadd<0x122>(dpp_uadd<0x124>(dpp_uadd<0x128>(v)))); }
DI rsrc_t mk_rsrc(const void* p, unsigned bytes) { return __builtin_amdgcn_make_buffer_rsrc((void*)p, (short)0, (int)bytes, 0x00020000); }
DI u32x4 ld128c(rsrc_t rs, unsigned vo, unsigned so) { return __builtin_amdgcn_raw_buffer_load_b128(rs, vo, so, AUX_SC1); }
DI u32x2 ld64c(rsrc_t rs, unsigned vo, unsigned so) { return __builtin_amdgcn_raw_buffer_load_b64(rs, vo, so, AUX_SC1); }
DI unsigned ld32c(rsrc_t rs, unsigned vo, unsigned so) { return __builtin_amdgcn_raw_buffer_load_b32(rs, vo, so, AUX_SC1); }
DI void st128c(u32x4 v, rsrc_t rs, unsigned vo, unsigned so) {
    __builtin_amdgcn_raw_buffer_store_b128(v, rs, vo, so, AUX_SC1);
    asm volatile("s_nop 1" :: "v"(v) : "memory");
}
DI void st64c(u32x2 v, rsrc_t rs, unsigned vo, unsigned so) { __builtin_amdgcn_raw_buffer_store_b64(v, rs, vo, so, AUX_SC1); }
DI void st32c(unsigned v, rsrc_t rs, unsigned vo, unsigned so) { __builtin_amdgcn_raw_buffer_store_b32(v, rs, vo, so, AUX_SC1); }

DI void dma16(rsrc_t rs, unsigned voff, unsigned soff, unsigned lds_addr) {
    asm volatile("s_mov_b32 m0, %0\n\ts_nop 0\n\tbuffer_load_dwordx4 %1, %2, %3 offen lds"
                 :: "s"(lds_addr), "v"(voff), "s"(rs), "s"(soff) : "memory");
}
template <int PC> DI void dma4(rsrc_t rs, const unsigned* v, unsigned soff, unsigned lds_addr) {
#define D4(p) asm volatile("s_mov_b32 m0, %0\n\ts_nop 0\n\tbuffer_load_dwordx4 %1, %5, %6 offen lds\n\t" p "buffer_load_dwordx4 %2, %5, %6 offen offset:1024 lds\n\t" p \
                 "buffer_load_dwordx4 %3, %5, %6 offen offset:2048 lds\n\t" p "buffer_load_dwordx4 %4, %5, %6 offen offset:3072 lds" \
                 :: "s"(lds_addr), "v"(v[0]), "v"(v[1]), "v"(v[2]), "v"(v[3]), "s"(rs), "s"(soff), "i"(PC ? PC - 1 : 0) : "memory")
    if (PC) D4("s_nop %7\n\t"); else D4("");
}
template <int N> DI void wait_vm() { asm volatile("s_waitcnt vmcnt(%0)" :: "i"(N) : "memory"); }
DI void wait_lgkm0() { asm volatile("" ::: "memory"); __builtin_amdgcn_s_waitcnt(0xC07F); asm volatile("" ::: "memory"); }
DI void bar() { asm volatile("" ::: "memory"); __builtin_amdgcn_s_barrier(); asm volatile("" ::: "memory"); }
DI void fence_c() { asm volatile("" ::: "memory"); }
DI int fresh_lane() {
    int v;
    asm volatile("v_mbcnt_lo_u32_b32 %0, -1, 0\n\tv_mbcnt_hi_u32_b32 %0, -1, %0" : "=v"(v));
    return v;
}
#define LANE_VARS const int l = fresh_lane(), c = l & 15, g = l >> 4, i4 = (l & 15) >> 2, i3 = l & 3; (void)c; (void)g; (void)i4; (void)i3
#define PSW ((unsigned)(4 * g + i4) * 128u + (unsigned)((i4 ^ (g & 1)) << 5))
#define WPO ((unsigned)(4 * g + i4) * 64u + (unsigned)((g & 1) << 5) + (unsigned)(8 * i3))
#define WSW ((unsigned)(8 * g + i4) * 128u + (unsigned)((i4 ^ (g & 1)) << 5) + (unsigned)(8 * i3))

#define WO_VOFF ((unsigned)(l >> 3) * 2048u + (unsigned)(((((((l & 7) >> 1) ^ ((l >> 3) & 3) ^ (l >> 5)) << 1) | (l & 1))) << 4))

DI void lds_wait(unsigned addr, unsigned target) {
#pragma unroll 1
    for (int it = 0; it < (1 << 22); ++it) {
        unsigned v = __hip_atomic_load((AS3 unsigned*)(size_t)addr, RLX_WG);
        if (__builtin_amdgcn_readfirstlane(v) >= target) break;
        __builtin_amdgcn_s_sleep(1);
    }
    fence_c();
}
DI unsigned now32() { unsigned long long t; asm volatile("s_memrealtime %0\n\ts_waitcnt lgkmcnt(0)" : "=s"(t) :: "memory"); return (unsigned)t; }
DI void wait_until(unsigned t) {
#pragma unroll 1
    for (int it = 0; it < 256; ++it) {
        const int rem = (int)(t - now32());
        if (rem <= 4) break;
        if (rem > 12) __builtin_amdgcn_s_sleep(2);
    }
    fence_c();
}
DI unsigned wave_usum(unsigned m) {
    auto p = __builtin_amdgcn_permlane32_swap(m, m, false, false);
    m = p[0] + p[1];
    auto q = __builtin_amdgcn_permlane16_swap(m, m, false, false);
    return row16_usum(q[0] + q[1]);
}
DI void post(unsigned addr, unsigned v) {
    fence_c();
    *(AS3 volatile unsigned*)(size_t)addr = v;
    fence_c();
}
DI void wait4(unsigned addr, unsigned target) {
#pragma unroll 1
    for (int it = 0; it < (1 << 22); ++it) {
        const u32x4 v = *(AS3 volatile const u32x4*)(size_t)addr;
        if (__builtin_amdgcn_readfirstlane(min(min(v[0], v[1]), min(v[2], v[3]))) >= target) break;
        __builtin_amdgcn_s_sleep(1);
    }
    fence_c();
}
constexpr int POLL_LIMIT = 1 << 16, TRY_LIMIT = 1 << 12;

#define KNOB(n, v) static constexpr int n = v
#define KNOBS
struct Dflt {
    KNOB(KT0, 0x76534210); KNOB(KT1, 0x76453201); KNOB(KT2, 0x75643120); KNOB(KT3, 0x67542310);
    KNOB(BGT, 0x76227622); KNOB(BUT, 0x77327732); KNOB(BRT, 0x77777777); KNOB(BST, 0x33333333); KNOB(PBT, (int)0xaaaaaaaa);
    KNOB(SLG, 0); KNOB(SLS, 20); KNOB(X0, 130); KNOB(X1, 2); KNOB(X2, 2); KNOB(TOPV, 1);
    KNOB(PRED_K, 16); KNOB(PRED_C, 90);
    KNOB(D1, 40); KNOB(D2, 120); KNOB(D3, 140); KNOB(FG_AT, -130); KNOB(Y_AT, 260); KNOB(QA, 30); KNOB(QB, 40);
    KNOB(RETRY_S, 8); KNOB(PT_S, 12); KNOB(KF_S, 10); KNOB(WO_D, 24); KNOB(FD, 12);
    KNOB(PT_B, 10); KNOB(XE, 20);
    KNOB(RE1, 1); KNOB(L2_S, 12);
    KNOB(FORCE_REDO, 0);
    KNOB(ND, 6); KNOB(XAH, 0); KNOB(QG, 2); KNOB(HP, 2); KNOB(Q5E, 1); KNOB(PADS, 1); KNOB(WOSK, 0); KNOB(WOE, 0); KNOB(TNT, 0); KNOB(TWB, 13); KNOB(TMW, 0); KNOB(DWP, 1); KNOB(WOG, 0); KNOB(PK13, 3); KNOB(DS1, 2); KNOB(DS2, 9); KNOB(DS3, 16); KNOB(PACE, 0);
};
struct K : Dflt { KNOBS };

constexpr unsigned WS_EPOCH = 0;
constexpr unsigned WS_RG = 4096;
constexpr unsigned WS_PT = 6144;
constexpr unsigned WS_G = 8192;
constexpr unsigned WS_YF = 24576;
constexpr unsigned WS_FG = 65536;
constexpr unsigned WS_Y = 1u << 19;
constexpr unsigned WS_BYTES = 1u << 20;
constexpr unsigned WB_KVP = 0;
constexpr unsigned WB_SM = 1u << 20;
constexpr unsigned WB_PRB = 3u << 19;
constexpr unsigned WB_OG = 7u << 18;
constexpr unsigned WB_OF = 8u << 20;
constexpr unsigned WB_BYTES = 13u << 20;
DI char* bulk_base(char* ws) { return (char*)(((size_t)ws + (3u << 20)) & ~(size_t)((2u << 20) - 1)); }

constexpr unsigned SLOT = 40960;
constexpr unsigned L_PHI = 98304;
constexpr unsigned L_VF = 131072;
constexpr unsigned L_MISC = 147456;
constexpr unsigned L_WP = L_MISC, L_KVF = L_MISC + 4096, L_SM = L_MISC + 12288;
constexpr unsigned C_ST = L_SM + 0  , C_R = L_SM + 8  , C_P = L_SM + 16  ;
constexpr unsigned C_WO = L_SM + 56, C_YRDY = L_SM + 68, C_IDX = L_SM + 76, C_TM = L_SM + 80, C_XE = L_SM + 84, C_Y1 = L_SM + 88;
constexpr unsigned P_E14 = 1, P_E15 = 2, P_PHA = 3, P_PHB = 4, P_KVD = 5, P_PUB = 6, P_Q1 = 7  , P_QRD = 11, P_O = 12;
constexpr unsigned C_Q = L_SM + 92, C_HOLD = L_SM + 96;
constexpr unsigned XS0[3] = {16384, 98304, L_VF}, XS1[3] = {57344, 122880, L_VF + 8192};
constexpr unsigned QX[3] = {40960, 81920, 0}, QW[6] = {L_KVF, 65536, 73728, 24576, 32768, L_PHI};
constexpr unsigned R_L1I = 1, R_L1 = 2, R_L2I = 3, R_KVF = 4;
constexpr unsigned L_XE = L_VF;
constexpr unsigned L_KEY = L_SM + 256, L_TAG = L_SM + 288, L_IDX = L_SM + 296, L_TM = L_SM + 304  , L_PLC = L_SM + 320  , L_PRB = L_SM + 336  , L_ZF = L_SM + 512, L_Y = L_SM + 1536;

constexpr unsigned long long L2E2 = 0x3fb8aa3b3fb8aa3bull;

#define PHASE_AB_ASM \
    "s_branch .Lpad_ab0\n\t" \
    ".Lab_s1:\n\t" \
    "s_mov_b32 s89, 0x400000\n\t" \
    ".Lab_s1a:\n\t" \
    "s_waitcnt lgkmcnt(0)\n\t" \
    "ds_read_b32 v237, v249\n\t" \
    "ds_read_b32 v238, v249 offset:4\n\t" \
    "s_waitcnt lgkmcnt(0)\n\t" \
    "v_min_u32 v237, v237, v238\n\t" \
    "s_nop 1\n\t" \
    "v_readfirstlane_b32 s85, v237\n\t" \
    "s_sub_u32 s89, s89, 1\n\t" \
    "s_cbranch_scc1 .Lab_s1b\n\t" \
    "s_cmp_lt_u32 s85, 1\n\t" \
    "s_cbranch_scc1 .Lab_s1a\n\t" \
    ".Lab_s1b:\n\t" \
    "s_nop 7\n\t" \
    "s_waitcnt lgkmcnt(0)\n\t" \
    "s_branch .Lab_s1r\n\t" \
    ".Lab_s2:\n\t" \
    "s_mov_b32 s89, 0x400000\n\t" \
    ".Lab_s2a:\n\t" \
    "s_waitcnt lgkmcnt(0)\n\t" \
    "ds_read_b32 v237, v249 offset:16\n\t" \
    "ds_read_b32 v238, v249 offset:20\n\t" \
    "ds_read_b32 v255, v249 offset:24\n\t" \
    "s_waitcnt lgkmcnt(0)\n\t" \
    "v_min3_u32 v237, v237, v238, v255\n\t" \
    "ds_read_b32 v238, v249 offset:28\n\t" \
    "s_waitcnt lgkmcnt(0)\n\t" \
    "v_min_u32 v237, v237, v238\n\t" \
    "s_nop 1\n\t" \
    "v_readfirstlane_b32 s85, v237\n\t" \
    "s_sub_u32 s89, s89, 1\n\t" \
    "s_cbranch_scc1 .Lab_s2b\n\t" \
    "s_cmp_lt_u32 s85, 2\n\t" \
    "s_cbranch_scc1 .Lab_s2a\n\t" \
    ".Lab_s2b:\n\t" \
    "s_nop 7\n\t" \
    "s_waitcnt lgkmcnt(0)\n\t" \
    "s_branch .Lab_s2r\n\t" \
    ".Lab_s3:\n\t" \
    "s_mov_b32 s89, 0x400000\n\t" \
    ".Lab_s3a:\n\t" \
    "s_waitcnt lgkmcnt(0)\n\t" \
    "ds_read_b32 v237, v250\n\t" \
    "s_waitcnt lgkmcnt(0)\n\t" \
    "s_nop 1\n\t" \
    "v_readfirstlane_b32 s85, v237\n\t" \
    "s_sub_u32 s89, s89, 1\n\t" \
    "s_cbranch_scc1 .Lab_s3b\n\t" \
    "s_cmp_lt_u32 s85, 3\n\t" \
    "s_cbranch_scc1 .Lab_s3a\n\t" \
    ".Lab_s3b:\n\t" \
    "s_nop 7\n\t" \
    "ds_read_b64_tr_b16 v[104:105], v245\n\t" \
    "ds_read_b64_tr_b16 v[106:107], v245 offset:2048\n\t" \
    "ds_read_b64_tr_b16 v[108:109], v255\n\t" \
    "ds_read_b64_tr_b16 v[110:111], v255 offset:2048\n\t" \
    "ds_read_b128 v[80:83], v242\n\t" \
    "ds_read_b128 v[84:87], v242 offset:1024\n\t" \
    "ds_read_b128 v[88:91], v242 offset:4096\n\t" \
    "ds_read_b128 v[92:95], v242 offset:5120\n\t" \
    "s_waitcnt lgkmcnt(0)\n\t" \
    "s_branch .Lab_s3r\n\t" \
    ".Lab_s4:\n\t" \
    "s_mov_b32 s89, 0x400000\n\t" \
    ".Lab_s4a:\n\t" \
    "s_waitcnt lgkmcnt(0)\n\t" \
    "ds_read_b32 v237, v250\n\t" \
    "s_waitcnt lgkmcnt(0)\n\t" \
    "s_nop 1\n\t" \
    "v_readfirstlane_b32 s85, v237\n\t" \
    "s_sub_u32 s89, s89, 1\n\t" \
    "s_cbranch_scc1 .Lab_s4b\n\t" \
    "s_cmp_lt_u32 s85, 4\n\t" \
    "s_cbranch_scc1 .Lab_s4a\n\t" \
    ".Lab_s4b:\n\t" \
    "s_nop 7\n\t" \
    "ds_read_b64_tr_b16 v[8:9], v245 offset:4096\n\t" \
    "ds_read_b64_tr_b16 v[10:11], v245 offset:6144\n\t" \
    "ds_read_b64_tr_b16 v[12:13], v255 offset:4096\n\t" \
    "ds_read_b64_tr_b16 v[14:15], v255 offset:6144\n\t" \
    "v_mov_b32 v237, 5\n\t" \
    "s_waitcnt lgkmcnt(0)\n\t" \
    "s_branch .Lab_s4r\n\t" \
    ".Lab_s5:\n\t" \
    "s_mov_b32 s89, 0x400000\n\t" \
    ".Lab_s5a:\n\t" \
    "s_waitcnt lgkmcnt(0)\n\t" \
    "ds_read_b32 v237, v249 offset:92\n\t" \
    "s_waitcnt lgkmcnt(0)\n\t" \
    "s_nop 1\n\t" \
    "v_readfirstlane_b32 s85, v237\n\t" \
    "s_sub_u32 s89, s89, 1\n\t" \
    "s_cbranch_scc1 .Lab_s5b\n\t" \
    "s_cmp_lt_u32 s85, 1\n\t" \
    "s_cbranch_scc1 .Lab_s5a\n\t" \
    ".Lab_s5b:\n\t" \
    "s_nop 7\n\t" \
    "ds_read_b64_tr_b16 v[80:81], v253\n\t" \
    "ds_read_b64_tr_b16 v[82:83], v253 offset:512\n\t" \
    "ds_read_b64_tr_b16 v[84:85], v251\n\t" \
    "ds_read_b64_tr_b16 v[86:87], v251 offset:512\n\t" \
    "ds_read_b64_tr_b16 v[88:89], v253 offset:4096\n\t" \
    "ds_read_b64_tr_b16 v[90:91], v253 offset:4608\n\t" \
    "ds_read_b64_tr_b16 v[92:93], v251 offset:4096\n\t" \
    "ds_read_b64_tr_b16 v[94:95], v251 offset:4608\n\t" \
    "s_waitcnt lgkmcnt(0)\n\t" \
    "s_branch .Lab_s5r\n\t" \
    ".Lab_s6:\n\t" \
    "s_mov_b32 s89, 0x400000\n\t" \
    ".Lab_s6a:\n\t" \
    "s_waitcnt lgkmcnt(0)\n\t" \
    "ds_read_b32 v237, v249 offset:92\n\t" \
    "s_waitcnt lgkmcnt(0)\n\t" \
    "s_nop 1\n\t" \
    "v_readfirstlane_b32 s85, v237\n\t" \
    "s_sub_u32 s89, s89, 1\n\t" \
    "s_cbranch_scc1 .Lab_s6b\n\t" \
    "s_cmp_lt_u32 s85, 2\n\t" \
    "s_cbranch_scc1 .Lab_s6a\n\t" \
    ".Lab_s6b:\n\t" \
    "s_nop 7\n\t" \
    "v_mov_b32 v237, 10\n\t" \
    "s_waitcnt lgkmcnt(0)\n\t" \
    "s_branch .Lab_s6r\n\t" \
    ".Lab_s7:\n\t" \
    "s_mov_b32 s89, 0x400000\n\t" \
    ".Lab_s7a:\n\t" \
    "s_sleep 1\n\t" \
    "ds_read_b32 v237, v249 offset:8\n\t" \
    "s_waitcnt lgkmcnt(0)\n\t" \
    "v_readfirstlane_b32 s85, v237\n\t" \
    "s_sub_u32 s89, s89, 1\n\t" \
    "s_cbranch_scc1 .Lab_s7r\n\t" \
    "s_cmp_lt_u32 s85, 3\n\t" \
    "s_cbranch_scc1 .Lab_s7a\n\t" \
    "s_sleep 5\n\t" \
    "s_branch .Lab_s7r\n\t" \
    ".Lab_sa:\n\t" \
    "ds_read_b32 v36, v249 offset:8\n\t" \
    "s_waitcnt lgkmcnt(0)\n\t" \
    "v_readfirstlane_b32 s80, v36\n\t" \
    "s_sub_u32 s89, s89, 1\n\t" \
    "s_cbranch_scc1 .Lab_sar\n\t" \
    "s_cmp_lt_u32 s80, s82\n\t" \
    "s_cbranch_scc1 .Lab_sa\n\t" \
    "s_cmp_eq_u32 s80, s82\n\t" \
    "s_cbranch_scc1 .Lab_sar\n\t" \
    "s_waitcnt vmcnt(0)\n\t" \
    "s_branch .Lab_k\n\t" \
    ".Lab_s8:\n\t" \
    "s_mov_b32 s89, 0x10000\n\t" \
    "v_lshlrev_b32 v35, 3, v34\n\t" \
    ".Lab_s8a:\n\t" \
    "s_sleep 8\n\t" \
    "buffer_load_dwordx2 v[232:233], v35, %[rsw], %[sg] offen sc1\n\t" \
    "buffer_load_dwordx2 v[234:235], v35, %[rsw], %[sg] offen offset:128 sc1\n\t" \
    "s_sub_u32 s89, s89, 1\n\t" \
    "s_waitcnt vmcnt(0)\n\t" \
    "s_cbranch_scc1 .Lab_s8r\n\t" \
    "v_cmp_ne_u32 vcc, v236, v233\n\t" \
    "v_cmp_ne_u32_e64 s[92:93], v236, v235\n\t" \
    "s_nop 0\n\t" \
    "s_or_b64 vcc, vcc, s[92:93]\n\t" \
    "s_cbranch_scc1 .Lab_s8a\n\t" \
    "s_branch .Lab_s8r\n\t" \
    ".Lab_s9:\n\t" \
    "s_mov_b32 s89, 0x400000\n\t" \
    ".Lab_s9a:\n\t" \
    "s_waitcnt lgkmcnt(0)\n\t" \
    "ds_read_b32 v237, v249 offset:56\n\t" \
    "s_waitcnt lgkmcnt(0)\n\t" \
    "s_nop 1\n\t" \
    "v_readfirstlane_b32 s85, v237\n\t" \
    "s_sub_u32 s89, s89, 1\n\t" \
    "s_cbranch_scc1 .Lab_s9b\n\t" \
    "s_cmp_lt_u32 s85, 2\n\t" \
    "s_cbranch_scc1 .Lab_s9a\n\t" \
    ".Lab_s9b:\n\t" \
    "s_nop 7\n\t" \
    "s_waitcnt lgkmcnt(0)\n\t" \
    "s_branch .Lab_s9r\n\t" \
    ".p2align 12\n\t" \
    ".Lpad_ab0:\n\t" \
    "s_cbranch_execz .Lpad_top\n\t" \
    "v_mov_b32 v32, 0\n\t" \
    "v_mov_b32 v33, 0\n\t" \
    "v_mov_b32 v34, 0\n\t" \
    "v_mov_b32 v35, 0\n\t" \
    "v_mov_b32 v36, 0\n\t" \
    "v_mov_b32 v37, 0\n\t" \
    "v_mov_b32 v38, 0\n\t" \
    "v_mov_b32 v39, 0\n\t" \
    "v_mov_b32 v40, 0\n\t" \
    "v_mov_b32 v41, 0\n\t" \
    "v_mov_b32 v42, 0\n\t" \
    "v_mov_b32 v43, 0\n\t" \
    "v_mov_b32 v44, 0\n\t" \
    "v_mov_b32 v45, 0\n\t" \
    "v_mov_b32 v46, 0\n\t" \
    "v_mov_b32 v47, 0\n\t" \
    "v_mov_b32 v48, 0\n\t" \
    "v_mov_b32 v49, 0\n\t" \
    "v_mov_b32 v50, 0\n\t" \
    "v_mov_b32 v51, 0\n\t" \
    "v_mov_b32 v52, 0\n\t" \
    "v_mov_b32 v53, 0\n\t" \
    "v_mov_b32 v54, 0\n\t" \
    "v_mov_b32 v55, 0\n\t" \
    "v_mov_b32 v56, 0\n\t" \
    "v_mov_b32 v57, 0\n\t" \
    "v_mov_b32 v58, 0\n\t" \
    "v_mov_b32 v59, 0\n\t" \
    "v_mov_b32 v60, 0\n\t" \
    "v_mov_b32 v61, 0\n\t" \
    "v_mov_b32 v62, 0\n\t" \
    "v_mov_b32 v63, 0\n\t" \
    "v_mov_b32 v80, 0\n\t" \
    "v_mov_b32 v81, 0\n\t" \
    "v_mov_b32 v82, 0\n\t" \
    "v_mov_b32 v83, 0\n\t" \
    "v_mov_b32 v84, 0\n\t" \
    "v_mov_b32 v85, 0\n\t" \
    "v_mov_b32 v86, 0\n\t" \
    "v_mov_b32 v87, 0\n\t" \
    "s_waitcnt lgkmcnt(0)\n\t" \
    "s_memtime s[82:83]\n\t" \
    "s_load_dword s80, %[prb], 0\n\t" \
    "v_mov_b32 v88, 0\n\t" \
    "v_mov_b32 v89, 0\n\t" \
    "v_mov_b32 v90, 0\n\t" \
    "v_mov_b32 v91, 0\n\t" \
    "v_mov_b32 v92, 0\n\t" \
    "v_mov_b32 v93, 0\n\t" \
    "v_mov_b32 v94, 0\n\t" \
    "v_mov_b32 v95, 0\n\t" \
    "v_mov_b32 v96, 0\n\t" \
    "v_mov_b32 v97, 0\n\t" \
    "v_mov_b32 v98, 0\n\t" \
    "v_mov_b32 v99, 0\n\t" \
    "v_mov_b32 v100, 0\n\t" \
    "v_mov_b32 v101, 0\n\t" \
    "v_mov_b32 v102, 0\n\t" \
    "v_mov_b32 v103, 0\n\t" \
    "v_mov_b32 v104, 0\n\t" \
    "v_mov_b32 v105, 0\n\t" \
    "v_mov_b32 v106, 0\n\t" \
    "v_mov_b32 v107, 0\n\t" \
    "v_mov_b32 v108, 0\n\t" \
    "v_mov_b32 v109, 0\n\t" \
    "v_mov_b32 v110, 0\n\t" \
    "v_mov_b32 v111, 0\n\t" \
    "v_mov_b32 v192, 0\n\t" \
    "v_mov_b32 v193, 0\n\t" \
    "v_mov_b32 v194, 0\n\t" \
    "v_mov_b32 v195, 0\n\t" \
    "v_mov_b32 v196, 0\n\t" \
    "v_mov_b32 v197, 0\n\t" \
    "v_mov_b32 v198, 0\n\t" \
    "v_mov_b32 v199, 0\n\t" \
    "v_mov_b32 v200, 0\n\t" \
    "v_mov_b32 v201, 0\n\t" \
    "v_mov_b32 v202, 0\n\t" \
    "v_mov_b32 v203, 0\n\t" \
    "v_mov_b32 v204, 0\n\t" \
    "v_mov_b32 v205, 0\n\t" \
    "v_mov_b32 v206, 0\n\t" \
    "v_mov_b32 v207, 0\n\t" \
    "v_mov_b32 v208, 0\n\t" \
    "v_mov_b32 v209, 0\n\t" \
    "v_mov_b32 v210, 0\n\t" \
    "v_mov_b32 v211, 0\n\t" \
    "v_mov_b32 v212, 0\n\t" \
    "v_mov_b32 v213, 0\n\t" \
    "v_mov_b32 v214, 0\n\t" \
    "v_mov_b32 v215, 0\n\t" \
    "v_mov_b32 v216, 0\n\t" \
    "v_mov_b32 v217, 0\n\t" \
    "v_mov_b32 v218, 0\n\t" \
    "v_mov_b32 v219, 0\n\t" \
    "v_mov_b32 v220, 0\n\t" \
    "v_mov_b32 v221, 0\n\t" \
    "v_mov_b32 v222, 0\n\t" \
    "v_mov_b32 v223, 0\n\t" \
    "s_waitcnt lgkmcnt(0)\n\t" \
    "s_memtime s[84:85]\n\t" \
    "s_waitcnt lgkmcnt(0)\n\t" \
    "s_sub_u32 s82, s84, s82\n\t" \
    "v_mov_b32 v237, s82\n\t" \
    "ds_write_b32 v248, v237 offset:320\n\t" \
    "v_mov_b32 v232, 0x3f803f80\n\t" \
    "v_mov_b32 v233, 0x3f803f80\n\t" \
    "v_mov_b32 v234, 0x3f803f80\n\t" \
    "v_mov_b32 v235, 0x3f803f80\n\t" \
    "v_mov_b32 v251, v240\n\t" \
    "v_xor_b32 v252, 64, v240\n\t" \
    "v_mov_b32 v253, v241\n\t" \
    "v_xor_b32 v254, 32, v241\n\t" \
    "s_mov_b32 s80, 0\n\t" \
    "s_mov_b32 s81, 10\n\t" \
    "s_mov_b32 s90, 0x3fb8aa3b\n\t" \
    "s_mov_b32 s91, 0x3fb8aa3b\n\t" \
    "v_cmp_eq_u32_e64 s[92:93], 2, v250\n\t" \
    "v_cmp_gt_u32_e64 s[94:95], 2, v250\n\t" \
    "v_cmp_eq_u32 vcc, 0, v250\n\t" \
    "v_xor_b32 v250, 4, v248\n\t" \
    "s_mov_b32 s84, 0x140a0014\n\t" \
    "s_lshl_b32 s83, %[xs0], 4\n\t" \
    "s_lshl_b32 s85, %[xs1], 12\n\t" \
    "s_or_b32 s83, s83, s85\n\t" \
    "s_or_b32 s83, s83, 0x14\n\t" \
    "s_waitcnt lgkmcnt(0)\n\t" \
    "s_barrier\n\t" \
    "ds_read_b128 v[128:131], v251\n\t" \
    "ds_read_b128 v[132:135], v251 offset:2048\n\t" \
    "ds_read_b128 v[136:139], v251 offset:4096\n\t" \
    "ds_read_b128 v[140:143], v251 offset:6144\n\t" \
    "ds_read_b64_tr_b16 v[0:1], v253 offset:8192\n\t" \
    "ds_read_b64_tr_b16 v[2:3], v253 offset:8704\n\t" \
    "ds_read_b64_tr_b16 v[4:5], v254 offset:8192\n\t" \
    "ds_read_b64_tr_b16 v[6:7], v254 offset:8704\n\t" \
    "ds_read_b64_tr_b16 v[8:9], v253 offset:16384\n\t" \
    "ds_read_b64_tr_b16 v[10:11], v253 offset:16896\n\t" \
    "ds_read_b64_tr_b16 v[12:13], v254 offset:16384\n\t" \
    "ds_read_b64_tr_b16 v[14:15], v254 offset:16896\n\t" \
    "ds_read_b64_tr_b16 v[16:17], v253\n\t" \
    "ds_read_b64_tr_b16 v[18:19], v253 offset:512\n\t" \
    "ds_read_b64_tr_b16 v[20:21], v254\n\t" \
    "ds_read_b64_tr_b16 v[22:23], v254 offset:512\n\t" \
    ".Lab_l1:\n\t" \
    "s_waitcnt lgkmcnt(0)\n\t" \
    "v_mfma_f32_16x16x32_bf16 v[32:35], v[0:3], v[128:131], v[32:35]\n\t" \
    "ds_read_b128 v[144:147], v252\n\t" \
    "v_mfma_f32_16x16x32_bf16 v[40:43], v[0:3], v[132:135], v[40:43]\n\t" \
    "ds_read_b128 v[148:151], v252 offset:2048\n\t" \
    "v_mfma_f32_16x16x32_bf16 v[48:51], v[0:3], v[136:139], v[48:51]\n\t" \
    "ds_read_b128 v[152:155], v252 offset:4096\n\t" \
    "v_mfma_f32_16x16x32_bf16 v[56:59], v[0:3], v[140:143], v[56:59]\n\t" \
    "ds_read_b128 v[156:159], v252 offset:6144\n\t" \
    "v_mfma_f32_16x16x32_bf16 v[36:39], v[4:7], v[128:131], v[36:39]\n\t" \
    "ds_read_b64_tr_b16 v[112:113], v253 offset:12288\n\t" \
    "v_mfma_f32_16x16x32_bf16 v[44:47], v[4:7], v[132:135], v[44:47]\n\t" \
    "ds_read_b64_tr_b16 v[114:115], v253 offset:12800\n\t" \
    "v_mfma_f32_16x16x32_bf16 v[52:55], v[4:7], v[136:139], v[52:55]\n\t" \
    "ds_read_b64_tr_b16 v[116:117], v254 offset:12288\n\t" \
    "v_mfma_f32_16x16x32_bf16 v[60:63], v[4:7], v[140:143], v[60:63]\n\t" \
    "ds_read_b64_tr_b16 v[118:119], v254 offset:12800\n\t" \
    "v_mfma_f32_16x16x32_bf16 v[80:83], v[128:131], v[8:11], v[80:83]\n\t" \
    "ds_read_b64_tr_b16 v[120:121], v253 offset:20480\n\t" \
    "v_mfma_f32_16x16x32_bf16 v[88:91], v[132:135], v[8:11], v[88:91]\n\t" \
    "ds_read_b64_tr_b16 v[122:123], v253 offset:20992\n\t" \
    "v_mfma_f32_16x16x32_bf16 v[96:99], v[136:139], v[8:11], v[96:99]\n\t" \
    "ds_read_b64_tr_b16 v[124:125], v254 offset:20480\n\t" \
    "v_mfma_f32_16x16x32_bf16 v[104:107], v[140:143], v[8:11], v[104:107]\n\t" \
    "ds_read_b64_tr_b16 v[126:127], v254 offset:20992\n\t" \
    "v_mfma_f32_16x16x32_bf16 v[84:87], v[128:131], v[12:15], v[84:87]\n\t" \
    "ds_read_b64_tr_b16 v[24:25], v253 offset:4096\n\t" \
    "v_mfma_f32_16x16x32_bf16 v[92:95], v[132:135], v[12:15], v[92:95]\n\t" \
    "ds_read_b64_tr_b16 v[26:27], v253 offset:4608\n\t" \
    "v_mfma_f32_16x16x32_bf16 v[100:103], v[136:139], v[12:15], v[100:103]\n\t" \
    "ds_read_b64_tr_b16 v[28:29], v254 offset:4096\n\t" \
    "v_mfma_f32_16x16x32_bf16 v[108:111], v[140:143], v[12:15], v[108:111]\n\t" \
    "ds_read_b64_tr_b16 v[30:31], v254 offset:4608\n\t" \
    "v_mfma_f32_16x16x32_bf16 v[192:195], v[16:19], v[128:131], v[192:195]\n\t" \
    "s_add_u32 s85, s80, 40960\n\t" \
    "s_cmp_lg_u32 s80, 81920\n\t" \
    "v_mfma_f32_16x16x32_bf16 v[200:203], v[16:19], v[132:135], v[200:203]\n\t" \
    "s_cselect_b32 s80, s85, 0\n\t" \
    "v_mfma_f32_16x16x32_bf16 v[208:211], v[16:19], v[136:139], v[208:211]\n\t" \
    "v_add_u32 v251, s80, v240\n\t" \
    "v_mfma_f32_16x16x32_bf16 v[216:219], v[16:19], v[140:143], v[216:219]\n\t" \
    "v_add_u32 v253, s80, v241\n\t" \
    "v_mfma_f32_16x16x32_bf16 v[196:199], v[20:23], v[128:131], v[196:199]\n\t" \
    "v_xor_b32 v254, 32, v253\n\t" \
    "v_mfma_f32_16x16x32_bf16 v[204:207], v[20:23], v[132:135], v[204:207]\n\t" \
    "v_mfma_f32_16x16x32_bf16 v[212:215], v[20:23], v[136:139], v[212:215]\n\t" \
    "v_mfma_f32_16x16x32_bf16 v[220:223], v[20:23], v[140:143], v[220:223]\n\t" \
    "s_waitcnt lgkmcnt(0)\n\t" \
    "s_barrier\n\t" \
    "v_mfma_f32_16x16x32_bf16 v[32:35], v[112:115], v[144:147], v[32:35]\n\t" \
    "ds_read_b128 v[128:131], v251\n\t" \
    "v_mfma_f32_16x16x32_bf16 v[40:43], v[112:115], v[148:151], v[40:43]\n\t" \
    "ds_read_b128 v[132:135], v251 offset:2048\n\t" \
    "v_mfma_f32_16x16x32_bf16 v[48:51], v[112:115], v[152:155], v[48:51]\n\t" \
    "ds_read_b128 v[136:139], v251 offset:4096\n\t" \
    "v_mfma_f32_16x16x32_bf16 v[56:59], v[112:115], v[156:159], v[56:59]\n\t" \
    "ds_read_b128 v[140:143], v251 offset:6144\n\t" \
    "v_mfma_f32_16x16x32_bf16 v[36:39], v[116:119], v[144:147], v[36:39]\n\t" \
    "ds_read_b64_tr_b16 v[0:1], v253 offset:8192\n\t" \
    "v_mfma_f32_16x16x32_bf16 v[44:47], v[116:119], v[148:151], v[44:47]\n\t" \
    "ds_read_b64_tr_b16 v[2:3], v253 offset:8704\n\t" \
    "v_mfma_f32_16x16x32_bf16 v[52:55], v[116:119], v[152:155], v[52:55]\n\t" \
    "ds_read_b64_tr_b16 v[4:5], v254 offset:8192\n\t" \
    "v_mfma_f32_16x16x32_bf16 v[60:63], v[116:119], v[156:159], v[60:63]\n\t" \
    "ds_read_b64_tr_b16 v[6:7], v254 offset:8704\n\t" \
    "v_mfma_f32_16x16x32_bf16 v[80:83], v[144:147], v[120:123], v[80:83]\n\t" \
    "ds_read_b64_tr_b16 v[8:9], v253 offset:16384\n\t" \
    "v_mfma_f32_16x16x32_bf16 v[88:91], v[148:151], v[120:123], v[88:91]\n\t" \
    "ds_read_b64_tr_b16 v[10:11], v253 offset:16896\n\t" \
    "v_mfma_f32_16x16x32_bf16 v[96:99], v[152:155], v[120:123], v[96:99]\n\t" \
    "ds_read_b64_tr_b16 v[12:13], v254 offset:16384\n\t" \
    "v_mfma_f32_16x16x32_bf16 v[104:107], v[156:159], v[120:123], v[104:107]\n\t" \
    "ds_read_b64_tr_b16 v[14:15], v254 offset:16896\n\t" \
    "v_mfma_f32_16x16x32_bf16 v[84:87], v[144:147], v[124:127], v[84:87]\n\t" \
    "ds_read_b64_tr_b16 v[16:17], v253\n\t" \
    "v_mfma_f32_16x16x32_bf16 v[92:95], v[148:151], v[124:127], v[92:95]\n\t" \
    "ds_read_b64_tr_b16 v[18:19], v253 offset:512\n\t" \
    "v_mfma_f32_16x16x32_bf16 v[100:103], v[152:155], v[124:127], v[100:103]\n\t" \
    "ds_read_b64_tr_b16 v[20:21], v254\n\t" \
    "v_mfma_f32_16x16x32_bf16 v[108:111], v[156:159], v[124:127], v[108:111]\n\t" \
    "ds_read_b64_tr_b16 v[22:23], v254 offset:512\n\t" \
    "v_mfma_f32_16x16x32_bf16 v[192:195], v[24:27], v[144:147], v[192:195]\n\t" \
    "v_xor_b32 v252, 64, v251\n\t" \
    "v_mfma_f32_16x16x32_bf16 v[200:203], v[24:27], v[148:151], v[200:203]\n\t" \
    "v_mfma_f32_16x16x32_bf16 v[208:211], v[24:27], v[152:155], v[208:211]\n\t" \
    "s_sub_u32 s81, s81, 1\n\t" \
    "v_mfma_f32_16x16x32_bf16 v[216:219], v[24:27], v[156:159], v[216:219]\n\t" \
    "s_cmp_lg_u32 s81, 0\n\t" \
    "v_mfma_f32_16x16x32_bf16 v[196:199], v[28:31], v[144:147], v[196:199]\n\t" \
    "v_mfma_f32_16x16x32_bf16 v[204:207], v[28:31], v[148:151], v[204:207]\n\t" \
    "v_mfma_f32_16x16x32_bf16 v[212:215], v[28:31], v[152:155], v[212:215]\n\t" \
    "v_mfma_f32_16x16x32_bf16 v[220:223], v[28:31], v[156:159], v[220:223]\n\t" \
    "s_cbranch_scc1 .Lab_l1\n\t" \
    ".Lab_l2:\n\t" \
    "s_waitcnt lgkmcnt(0)\n\t" \
    "v_mfma_f32_16x16x32_bf16 v[32:35], v[0:3], v[128:131], v[32:35]\n\t" \
    "ds_read_b128 v[144:147], v252\n\t" \
    "ds_read_b64_tr_b16 v[112:113], v253 offset:12288\n\t" \
    "v_mfma_f32_16x16x32_bf16 v[40:43], v[0:3], v[132:135], v[40:43]\n\t" \
    "ds_read_b128 v[148:151], v252 offset:2048\n\t" \
    "ds_read_b64_tr_b16 v[114:115], v253 offset:12800\n\t" \
    "v_mfma_f32_16x16x32_bf16 v[48:51], v[0:3], v[136:139], v[48:51]\n\t" \
    "ds_read_b128 v[152:155], v252 offset:4096\n\t" \
    "s_and_b32 s80, s84, 255\n\t" \
    "v_mfma_f32_16x16x32_bf16 v[56:59], v[0:3], v[140:143], v[56:59]\n\t" \
    "ds_read_b128 v[156:159], v252 offset:6144\n\t" \
    "s_and_b32 s82, s83, 255\n\t" \
    "v_mfma_f32_16x16x32_bf16 v[36:39], v[4:7], v[128:131], v[36:39]\n\t" \
    "ds_read_b64_tr_b16 v[116:117], v254 offset:12288\n\t" \
    "s_lshl_b32 s80, s80, 12\n\t" \
    "v_mfma_f32_16x16x32_bf16 v[44:47], v[4:7], v[132:135], v[44:47]\n\t" \
    "ds_read_b64_tr_b16 v[118:119], v254 offset:12800\n\t" \
    "s_lshl_b32 s82, s82, 12\n\t" \
    "v_mfma_f32_16x16x32_bf16 v[52:55], v[4:7], v[136:139], v[52:55]\n\t" \
    "ds_read_b64_tr_b16 v[120:121], v253 offset:20480\n\t" \
    "v_mfma_f32_16x16x32_bf16 v[60:63], v[4:7], v[140:143], v[60:63]\n\t" \
    "ds_read_b64_tr_b16 v[122:123], v253 offset:20992\n\t" \
    "v_mfma_f32_16x16x32_bf16 v[80:83], v[128:131], v[8:11], v[80:83]\n\t" \
    "ds_read_b64_tr_b16 v[124:125], v254 offset:20480\n\t" \
    "v_mfma_f32_16x16x32_bf16 v[88:91], v[132:135], v[8:11], v[88:91]\n\t" \
    "ds_read_b64_tr_b16 v[126:127], v254 offset:20992\n\t" \
    "v_mfma_f32_16x16x32_bf16 v[96:99], v[136:139], v[8:11], v[96:99]\n\t" \
    "v_add_u32 v251, s82, v240\n\t" \
    "v_mfma_f32_16x16x32_bf16 v[104:107], v[140:143], v[8:11], v[104:107]\n\t" \
    "v_add_u32 v253, s80, v241\n\t" \
    "v_mfma_f32_16x16x32_bf16 v[84:87], v[128:131], v[12:15], v[84:87]\n\t" \
    "v_xor_b32 v254, 32, v253\n\t" \
    "v_mfma_f32_16x16x32_bf16 v[92:95], v[132:135], v[12:15], v[92:95]\n\t" \
    "v_mfma_f32_16x16x32_bf16 v[100:103], v[136:139], v[12:15], v[100:103]\n\t" \
    "v_mfma_f32_16x16x32_bf16 v[108:111], v[140:143], v[12:15], v[108:111]\n\t" \
    "s_waitcnt lgkmcnt(0)\n\t" \
    "s_barrier\n\t" \
    "v_mfma_f32_16x16x32_bf16 v[32:35], v[112:115], v[144:147], v[32:35]\n\t" \
    "ds_read_b128 v[128:131], v251\n\t" \
    "ds_read_b64_tr_b16 v[0:1], v253 offset:8192\n\t" \
    "v_mfma_f32_16x16x32_bf16 v[40:43], v[112:115], v[148:151], v[40:43]\n\t" \
    "ds_read_b128 v[132:135], v251 offset:2048\n\t" \
    "ds_read_b64_tr_b16 v[2:3], v253 offset:8704\n\t" \
    "v_mfma_f32_16x16x32_bf16 v[48:51], v[112:115], v[152:155], v[48:51]\n\t" \
    "ds_read_b128 v[136:139], v251 offset:4096\n\t" \
    "v_mfma_f32_16x16x32_bf16 v[56:59], v[112:115], v[156:159], v[56:59]\n\t" \
    "ds_read_b128 v[140:143], v251 offset:6144\n\t" \
    "v_mfma_f32_16x16x32_bf16 v[36:39], v[116:119], v[144:147], v[36:39]\n\t" \
    "ds_read_b64_tr_b16 v[4:5], v254 offset:8192\n\t" \
    "v_mfma_f32_16x16x32_bf16 v[44:47], v[116:119], v[148:151], v[44:47]\n\t" \
    "ds_read_b64_tr_b16 v[6:7], v254 offset:8704\n\t" \
    "v_mfma_f32_16x16x32_bf16 v[52:55], v[116:119], v[152:155], v[52:55]\n\t" \
    "ds_read_b64_tr_b16 v[8:9], v253 offset:16384\n\t" \
    "v_mfma_f32_16x16x32_bf16 v[60:63], v[116:119], v[156:159], v[60:63]\n\t" \
    "ds_read_b64_tr_b16 v[10:11], v253 offset:16896\n\t" \
    "v_mfma_f32_16x16x32_bf16 v[80:83], v[144:147], v[120:123], v[80:83]\n\t" \
    "ds_read_b64_tr_b16 v[12:13], v254 offset:16384\n\t" \
    "v_mfma_f32_16x16x32_bf16 v[88:91], v[148:151], v[120:123], v[88:91]\n\t" \
    "ds_read_b64_tr_b16 v[14:15], v254 offset:16896\n\t" \
    "v_mfma_f32_16x16x32_bf16 v[96:99], v[152:155], v[120:123], v[96:99]\n\t" \
    "v_xor_b32 v252, 64, v251\n\t" \
    "v_mfma_f32_16x16x32_bf16 v[104:107], v[156:159], v[120:123], v[104:107]\n\t" \
    "s_lshr_b32 s83, s83, 8\n\t" \
    "v_mfma_f32_16x16x32_bf16 v[84:87], v[144:147], v[124:127], v[84:87]\n\t" \
    "s_lshr_b32 s84, s84, 8\n\t" \
    "v_mfma_f32_16x16x32_bf16 v[92:95], v[148:151], v[124:127], v[92:95]\n\t" \
    "v_mfma_f32_16x16x32_bf16 v[100:103], v[152:155], v[124:127], v[100:103]\n\t" \
    "v_mfma_f32_16x16x32_bf16 v[108:111], v[156:159], v[124:127], v[108:111]\n\t" \
    "s_cbranch_scc1 .Lab_l2\n\t" \
    "s_waitcnt lgkmcnt(0)\n\t" \
    "v_mfma_f32_16x16x32_bf16 v[32:35], v[0:3], v[128:131], v[32:35]\n\t" \
    "ds_read_b128 v[144:147], v252\n\t" \
    "ds_read_b64_tr_b16 v[112:113], v253 offset:12288\n\t" \
    "v_mfma_f32_16x16x32_bf16 v[40:43], v[0:3], v[132:135], v[40:43]\n\t" \
    "ds_read_b128 v[148:151], v252 offset:2048\n\t" \
    "ds_read_b64_tr_b16 v[114:115], v253 offset:12800\n\t" \
    "v_mfma_f32_16x16x32_bf16 v[48:51], v[0:3], v[136:139], v[48:51]\n\t" \
    "ds_read_b128 v[152:155], v252 offset:4096\n\t" \
    "ds_read_b64_tr_b16 v[116:117], v254 offset:12288\n\t" \
    "v_mfma_f32_16x16x32_bf16 v[56:59], v[0:3], v[140:143], v[56:59]\n\t" \
    "ds_read_b128 v[156:159], v252 offset:6144\n\t" \
    "ds_read_b64_tr_b16 v[118:119], v254 offset:12800\n\t" \
    "v_mfma_f32_16x16x32_bf16 v[36:39], v[4:7], v[128:131], v[36:39]\n\t" \
    "ds_read_b64_tr_b16 v[120:121], v253 offset:20480\n\t" \
    "ds_read_b64_tr_b16 v[122:123], v253 offset:20992\n\t" \
    "v_mfma_f32_16x16x32_bf16 v[44:47], v[4:7], v[132:135], v[44:47]\n\t" \
    "ds_read_b64_tr_b16 v[124:125], v254 offset:20480\n\t" \
    "ds_read_b64_tr_b16 v[126:127], v254 offset:20992\n\t" \
    "v_mfma_f32_16x16x32_bf16 v[52:55], v[4:7], v[136:139], v[52:55]\n\t" \
    "ds_read_b64_tr_b16 v[224:225], v245\n\t" \
    "ds_read_b64_tr_b16 v[226:227], v245 offset:1024\n\t" \
    "v_mfma_f32_16x16x32_bf16 v[60:63], v[4:7], v[140:143], v[60:63]\n\t" \
    "v_xor_b32 v245, 32, v245\n\t" \
    "ds_read_b32 v236, v249 offset:288\n\t" \
    "v_mfma_f32_16x16x32_bf16 v[80:83], v[128:131], v[8:11], v[80:83]\n\t" \
    "ds_read_b64_tr_b16 v[228:229], v245\n\t" \
    "ds_read_b64_tr_b16 v[230:231], v245 offset:1024\n\t" \
    "v_mfma_f32_16x16x32_bf16 v[88:91], v[132:135], v[8:11], v[88:91]\n\t" \
    "ds_read_b128 v[68:71], v249 offset:320\n\t" \
    "ds_read_b64 v[64:65], v249 offset:0\n\t" \
    "v_mfma_f32_16x16x32_bf16 v[96:99], v[136:139], v[8:11], v[96:99]\n\t" \
    "v_add_u32 v251, 131072, v240\n\t" \
    "v_mfma_f32_16x16x32_bf16 v[104:107], v[140:143], v[8:11], v[104:107]\n\t" \
    "v_xor_b32 v254, 32, v241\n\t" \
    "v_mfma_f32_16x16x32_bf16 v[84:87], v[128:131], v[12:15], v[84:87]\n\t" \
    "v_mov_b32 v253, v241\n\t" \
    "v_mfma_f32_16x16x32_bf16 v[92:95], v[132:135], v[12:15], v[92:95]\n\t" \
    "v_xor_b32 v252, 64, v251\n\t" \
    "v_mfma_f32_16x16x32_bf16 v[100:103], v[136:139], v[12:15], v[100:103]\n\t" \
    "v_mfma_f32_16x16x32_bf16 v[108:111], v[140:143], v[12:15], v[108:111]\n\t" \
    "s_waitcnt lgkmcnt(0)\n\t" \
    "v_mfma_f32_16x16x32_bf16 v[32:35], v[112:115], v[144:147], v[32:35]\n\t" \
    "v_min_u32 v64, v64, v65\n\t" \
    "v_mfma_f32_16x16x32_bf16 v[40:43], v[112:115], v[148:151], v[40:43]\n\t" \
    "v_readfirstlane_b32 s85, v64\n\t" \
    "s_cmp_lt_u32 s85, 1\n\t" \
    "s_cbranch_scc1 .Lab_s1\n\t" \
    ".Lab_s1r:\n\t" \
    "v_mfma_f32_16x16x32_bf16 v[48:51], v[112:115], v[152:155], v[48:51]\n\t" \
    "ds_read_b128 v[160:163], v251\n\t" \
    "ds_read_b64_tr_b16 v[0:1], v253 offset:8192\n\t" \
    "v_mfma_f32_16x16x32_bf16 v[56:59], v[112:115], v[156:159], v[56:59]\n\t" \
    "ds_read_b128 v[164:167], v251 offset:2048\n\t" \
    "ds_read_b64_tr_b16 v[2:3], v253 offset:8704\n\t" \
    "v_mfma_f32_16x16x32_bf16 v[36:39], v[116:119], v[144:147], v[36:39]\n\t" \
    "ds_read_b128 v[168:171], v251 offset:4096\n\t" \
    "ds_read_b64_tr_b16 v[4:5], v254 offset:8192\n\t" \
    "v_mfma_f32_16x16x32_bf16 v[44:47], v[116:119], v[148:151], v[44:47]\n\t" \
    "ds_read_b128 v[172:175], v251 offset:6144\n\t" \
    "ds_read_b64_tr_b16 v[6:7], v254 offset:8704\n\t" \
    "v_mfma_f32_16x16x32_bf16 v[52:55], v[116:119], v[152:155], v[52:55]\n\t" \
    "ds_read_b64_tr_b16 v[8:9], v253 offset:16384\n\t" \
    "ds_read_b64_tr_b16 v[10:11], v253 offset:16896\n\t" \
    "v_mfma_f32_16x16x32_bf16 v[60:63], v[116:119], v[156:159], v[60:63]\n\t" \
    "ds_read_b64_tr_b16 v[12:13], v254 offset:16384\n\t" \
    "ds_read_b64_tr_b16 v[14:15], v254 offset:16896\n\t" \
    "v_mfma_f32_16x16x32_bf16 v[80:83], v[144:147], v[120:123], v[80:83]\n\t" \
    "v_readfirstlane_b32 s87, v68\n\t" \
    "v_mfma_f32_16x16x32_bf16 v[88:91], v[148:151], v[120:123], v[88:91]\n\t" \
    "v_readfirstlane_b32 s88, v69\n\t" \
    "v_mfma_f32_16x16x32_bf16 v[96:99], v[152:155], v[120:123], v[96:99]\n\t" \
    "v_readfirstlane_b32 %[sof], v70\n\t" \
    "v_mfma_f32_16x16x32_bf16 v[104:107], v[156:159], v[120:123], v[104:107]\n\t" \
    "s_xor_b32 s87, s87, %[kpx]\n\t" \
    "s_add_u32 s88, s88, %[kma]\n\t" \
    "v_mfma_f32_16x16x32_bf16 v[84:87], v[144:147], v[124:127], v[84:87]\n\t" \
    "s_add_u32 s87, s87, %[kpa]\n\t" \
    "v_mfma_f32_16x16x32_bf16 v[92:95], v[148:151], v[124:127], v[92:95]\n\t" \
    "s_xor_b32 s86, s87, 0x8000\n\t" \
    "v_mfma_f32_16x16x32_bf16 v[100:103], v[152:155], v[124:127], v[100:103]\n\t" \
    "v_mfma_f32_16x16x32_bf16 v[108:111], v[156:159], v[124:127], v[108:111]\n\t" \
    "s_waitcnt lgkmcnt(0)\n\t" \
    "v_mfma_f32_16x16x32_bf16 v[32:35], v[0:3], v[160:163], v[32:35]\n\t" \
    "ds_read_b128 v[176:179], v252\n\t" \
    "ds_read_b64_tr_b16 v[112:113], v253 offset:12288\n\t" \
    "v_mfma_f32_16x16x32_bf16 v[40:43], v[0:3], v[164:167], v[40:43]\n\t" \
    "ds_read_b128 v[180:183], v252 offset:2048\n\t" \
    "ds_read_b64_tr_b16 v[114:115], v253 offset:12800\n\t" \
    "v_mfma_f32_16x16x32_bf16 v[48:51], v[0:3], v[168:171], v[48:51]\n\t" \
    "ds_read_b128 v[184:187], v252 offset:4096\n\t" \
    "v_mov_b32 v237, 2\n\t" \
    "v_mfma_f32_16x16x32_bf16 v[56:59], v[0:3], v[172:175], v[56:59]\n\t" \
    "ds_read_b128 v[188:191], v252 offset:6144\n\t" \
    "v_mfma_f32_16x16x32_bf16 v[36:39], v[4:7], v[160:163], v[36:39]\n\t" \
    "ds_read_b64_tr_b16 v[116:117], v254 offset:12288\n\t" \
    "v_mfma_f32_16x16x32_bf16 v[44:47], v[4:7], v[164:167], v[44:47]\n\t" \
    "ds_read_b64_tr_b16 v[118:119], v254 offset:12800\n\t" \
    "v_mfma_f32_16x16x32_bf16 v[52:55], v[4:7], v[168:171], v[52:55]\n\t" \
    "ds_read_b64_tr_b16 v[120:121], v253 offset:20480\n\t" \
    "v_mfma_f32_16x16x32_bf16 v[60:63], v[4:7], v[172:175], v[60:63]\n\t" \
    "ds_read_b64_tr_b16 v[122:123], v253 offset:20992\n\t" \
    "v_mfma_f32_16x16x32_bf16 v[80:83], v[160:163], v[8:11], v[80:83]\n\t" \
    "ds_read_b64_tr_b16 v[124:125], v254 offset:20480\n\t" \
    "v_mfma_f32_16x16x32_bf16 v[88:91], v[164:167], v[8:11], v[88:91]\n\t" \
    "ds_read_b64_tr_b16 v[126:127], v254 offset:20992\n\t" \
    "v_mfma_f32_16x16x32_bf16 v[96:99], v[168:171], v[8:11], v[96:99]\n\t" \
    "ds_write_b32 v248, v237\n\t" \
    "v_mfma_f32_16x16x32_bf16 v[104:107], v[172:175], v[8:11], v[104:107]\n\t" \
    "v_xor_b32 v251, 32, v243\n\t" \
    "v_mfma_f32_16x16x32_bf16 v[84:87], v[160:163], v[12:15], v[84:87]\n\t" \
    "v_xor_b32 v252, 64, v243\n\t" \
    "v_mfma_f32_16x16x32_bf16 v[92:95], v[164:167], v[12:15], v[92:95]\n\t" \
    "v_xor_b32 v253, 0x60, v243\n\t" \
    "v_mfma_f32_16x16x32_bf16 v[100:103], v[168:171], v[12:15], v[100:103]\n\t" \
    "s_setprio 3\n\t" \
    "v_mfma_f32_16x16x32_bf16 v[108:111], v[172:175], v[12:15], v[108:111]\n\t" \
    "s_waitcnt lgkmcnt(0)\n\t" \
    "v_mfma_f32_16x16x32_bf16 v[32:35], v[112:115], v[176:179], v[32:35]\n\t" \
    "v_mfma_f32_16x16x32_bf16 v[36:39], v[116:119], v[176:179], v[36:39]\n\t" \
    "v_mfma_f32_16x16x32_bf16 v[40:43], v[112:115], v[180:183], v[40:43]\n\t" \
    "v_mfma_f32_16x16x32_bf16 v[44:47], v[116:119], v[180:183], v[44:47]\n\t" \
    "ds_read_b128 v[28:31], v249 offset:16\n\t" \
    "v_mfma_f32_16x16x32_bf16 v[48:51], v[112:115], v[184:187], v[48:51]\n\t" \
    "v_mfma_f32_16x16x32_bf16 v[52:55], v[116:119], v[184:187], v[52:55]\n\t" \
    "v_mfma_f32_16x16x32_bf16 v[56:59], v[112:115], v[188:191], v[56:59]\n\t" \
    "v_mfma_f32_16x16x32_bf16 v[60:63], v[116:119], v[188:191], v[60:63]\n\t" \
    "v_cvt_pk_bf16_f32 v64, v32, v33\n\t" \
    "v_cvt_pk_bf16_f32 v65, v34, v35\n\t" \
    "v_mfma_f32_16x16x32_bf16 v[80:83], v[176:179], v[120:123], v[80:83]\n\t" \
    "v_cvt_pk_bf16_f32 v66, v36, v37\n\t" \
    "v_cvt_pk_bf16_f32 v67, v38, v39\n\t" \
    "v_mfma_f32_16x16x32_bf16 v[88:91], v[180:183], v[120:123], v[88:91]\n\t" \
    "v_cvt_pk_bf16_f32 v68, v40, v41\n\t" \
    "v_cvt_pk_bf16_f32 v69, v42, v43\n\t" \
    "v_mfma_f32_16x16x32_bf16 v[84:87], v[176:179], v[124:127], v[84:87]\n\t" \
    "v_cvt_pk_bf16_f32 v70, v44, v45\n\t" \
    "v_cvt_pk_bf16_f32 v71, v46, v47\n\t" \
    "v_mfma_f32_16x16x32_bf16 v[92:95], v[180:183], v[124:127], v[92:95]\n\t" \
    "v_cvt_pk_bf16_f32 v72, v48, v49\n\t" \
    "v_cvt_pk_bf16_f32 v73, v50, v51\n\t" \
    "v_mfma_f32_16x16x32_bf16 v[96:99], v[184:187], v[120:123], v[96:99]\n\t" \
    "v_cvt_pk_bf16_f32 v74, v52, v53\n\t" \
    "v_cvt_pk_bf16_f32 v75, v54, v55\n\t" \
    "v_mfma_f32_16x16x32_bf16 v[104:107], v[188:191], v[120:123], v[104:107]\n\t" \
    "v_cvt_pk_bf16_f32 v76, v56, v57\n\t" \
    "v_cvt_pk_bf16_f32 v77, v58, v59\n\t" \
    "v_mfma_f32_16x16x32_bf16 v[100:103], v[184:187], v[124:127], v[100:103]\n\t" \
    "v_cvt_pk_bf16_f32 v78, v60, v61\n\t" \
    "v_cvt_pk_bf16_f32 v79, v62, v63\n\t" \
    "v_mfma_f32_16x16x32_bf16 v[108:111], v[188:191], v[124:127], v[108:111]\n\t" \
    "s_waitcnt lgkmcnt(0)\n\t" \
    "v_min_u32 v238, v28, v29\n\t" \
    "v_min3_u32 v238, v238, v30, v31\n\t" \
    "v_mfma_f32_16x16x32_bf16 v[0:3], v[224:227], v[64:67], 0\n\t" \
    "v_cvt_pk_bf16_f32 v112, v80, v81\n\t" \
    "v_cvt_pk_bf16_f32 v113, v82, v83\n\t" \
    "v_mfma_f32_16x16x32_bf16 v[4:7], v[228:231], v[64:67], 0\n\t" \
    "v_cvt_pk_bf16_f32 v114, v88, v89\n\t" \
    "v_cvt_pk_bf16_f32 v115, v90, v91\n\t" \
    "v_mfma_f32_16x16x32_bf16 v[8:11], v[224:227], v[68:71], 0\n\t" \
    "v_cvt_pk_bf16_f32 v116, v84, v85\n\t" \
    "v_cvt_pk_bf16_f32 v117, v86, v87\n\t" \
    "v_mfma_f32_16x16x32_bf16 v[12:15], v[228:231], v[68:71], 0\n\t" \
    "v_cvt_pk_bf16_f32 v118, v92, v93\n\t" \
    "v_cvt_pk_bf16_f32 v119, v94, v95\n\t" \
    "v_mfma_f32_16x16x32_bf16 v[16:19], v[224:227], v[72:75], 0\n\t" \
    "v_cvt_pk_bf16_f32 v120, v96, v97\n\t" \
    "v_cvt_pk_bf16_f32 v121, v98, v99\n\t" \
    "v_mfma_f32_16x16x32_bf16 v[20:23], v[228:231], v[72:75], 0\n\t" \
    "v_cvt_pk_bf16_f32 v122, v104, v105\n\t" \
    "v_cvt_pk_bf16_f32 v123, v106, v107\n\t" \
    "v_mfma_f32_16x16x32_bf16 v[24:27], v[224:227], v[76:79], 0\n\t" \
    "v_cvt_pk_bf16_f32 v124, v100, v101\n\t" \
    "v_cvt_pk_bf16_f32 v125, v102, v103\n\t" \
    "v_mfma_f32_16x16x32_bf16 v[28:31], v[228:231], v[76:79], 0\n\t" \
    "v_cvt_pk_bf16_f32 v126, v108, v109\n\t" \
    "v_cvt_pk_bf16_f32 v127, v110, v111\n\t" \
    "v_readfirstlane_b32 s85, v238\n\t" \
    "s_cmp_lt_u32 s85, 2\n\t" \
    "s_cbranch_scc1 .Lab_s2\n\t" \
    ".Lab_s2r:\n\t" \
    "ds_write_b128 v242, v[112:115]\n\t" \
    "v_max3_f32 v64, v0, v1, v2\n\t" \
    "v_max3_f32 v66, -v0, -v1, -v2\n\t" \
    "v_max3_f32 v68, v8, v9, v10\n\t" \
    "v_max3_f32 v64, v64, v3, v4\n\t" \
    "v_max3_f32 v66, v66, -v3, -v4\n\t" \
    "v_max3_f32 v68, v68, v11, v12\n\t" \
    "v_max3_f32 v64, v64, v5, v6\n\t" \
    "v_max3_f32 v66, v66, -v5, -v6\n\t" \
    "ds_write_b128 v242, v[116:119] offset:1024\n\t" \
    "v_max3_f32 v68, v68, v13, v14\n\t" \
    "v_max_f32_e64 v64, v64, v7\n\t" \
    "v_max_f32_e64 v66, v66, -v7\n\t" \
    "v_max_f32_e64 v68, v68, v15\n\t" \
    "v_max3_f32 v70, -v8, -v9, -v10\n\t" \
    "v_permlane32_swap_b32 v64, v66\n\t" \
    "v_max3_f32 v72, v16, v17, v18\n\t" \
    "v_max3_f32 v70, v70, -v11, -v12\n\t" \
    "ds_write_b128 v242, v[120:123] offset:4096\n\t" \
    "v_max_f32 v64, v64, v66\n\t" \
    "v_max3_f32 v72, v72, v19, v20\n\t" \
    "v_max3_f32 v70, v70, -v13, -v14\n\t" \
    "v_max3_f32 v74, -v16, -v17, -v18\n\t" \
    "v_max3_f32 v72, v72, v21, v22\n\t" \
    "v_max_f32_e64 v70, v70, -v15\n\t" \
    "v_max3_f32 v74, v74, -v19, -v20\n\t" \
    "v_max_f32_e64 v72, v72, v23\n\t" \
    "ds_write_b128 v242, v[124:127] offset:5120\n\t" \
    "v_permlane32_swap_b32 v68, v70\n\t" \
    "v_max3_f32 v74, v74, -v21, -v22\n\t" \
    "v_max3_f32 v76, v24, v25, v26\n\t" \
    "v_max_f32 v68, v68, v70\n\t" \
    "v_max_f32_e64 v74, v74, -v23\n\t" \
    "v_max3_f32 v76, v76, v27, v28\n\t" \
    "v_permlane16_swap_b32 v64, v68\n\t" \
    "v_permlane32_swap_b32 v72, v74\n\t" \
    "v_max3_f32 v76, v76, v29, v30\n\t" \
    "v_max_f32 v66, v64, v68\n\t" \
    "v_max_f32 v72, v72, v74\n\t" \
    "v_max_f32_e64 v76, v76, v31\n\t" \
    "v_xor_b32 v254, 32, v244\n\t" \
    "v_mul_f32 v64, 0xbfb8aa3b, v66\n\t" \
    "v_mul_f32 v68, 0xbfb8aa3b, v66\n\t" \
    "v_xor_b32 v245, 0x2000, v244\n\t" \
    "v_max3_f32 v78, -v24, -v25, -v26\n\t" \
    "v_max3_f32 v78, v78, -v27, -v28\n\t" \
    "v_xor_b32 v255, 0x2020, v244\n\t" \
    "v_permlane16_swap_b32 v64, v68\n\t" \
    "v_mov_b32 v66, v64\n\t" \
    "v_xor_b32 v242, 0x2000, v242\n\t" \
    "v_max3_f32 v78, v78, -v29, -v30\n\t" \
    "v_mov_b32 v70, v68\n\t" \
    "v_permlane32_swap_b32 v64, v66\n\t" \
    "v_max_f32_e64 v78, v78, -v31\n\t" \
    "v_permlane32_swap_b32 v68, v70\n\t" \
    "v_pk_fma_f32 v[32:33], v[0:1], s[90:91], v[64:65] op_sel_hi:[1,1,0]\n\t" \
    "v_permlane32_swap_b32 v76, v78\n\t" \
    "v_pk_fma_f32 v[0:1], v[0:1], s[90:91], v[66:67] op_sel_hi:[1,1,0] neg_lo:[1,0,0] neg_hi:[1,0,0]\n\t" \
    "v_pk_fma_f32 v[34:35], v[2:3], s[90:91], v[64:65] op_sel_hi:[1,1,0]\n\t" \
    "v_max_f32 v76, v76, v78\n\t" \
    "v_pk_fma_f32 v[2:3], v[2:3], s[90:91], v[66:67] op_sel_hi:[1,1,0] neg_lo:[1,0,0] neg_hi:[1,0,0]\n\t" \
    "v_pk_fma_f32 v[36:37], v[4:5], s[90:91], v[64:65] op_sel_hi:[1,1,0]\n\t" \
    "v_permlane16_swap_b32 v72, v76\n\t" \
    "v_pk_fma_f32 v[4:5], v[4:5], s[90:91], v[66:67] op_sel_hi:[1,1,0] neg_lo:[1,0,0] neg_hi:[1,0,0]\n\t" \
    "v_pk_fma_f32 v[38:39], v[6:7], s[90:91], v[64:65] op_sel_hi:[1,1,0]\n\t" \
    "v_max_f32 v74, v72, v76\n\t" \
    "v_pk_fma_f32 v[6:7], v[6:7], s[90:91], v[66:67] op_sel_hi:[1,1,0] neg_lo:[1,0,0] neg_hi:[1,0,0]\n\t" \
    "v_exp_f32 v32, v32\n\t" \
    "v_mul_f32 v72, 0xbfb8aa3b, v74\n\t" \
    "v_mul_f32 v76, 0xbfb8aa3b, v74\n\t" \
    "v_exp_f32 v0, v0\n\t" \
    "v_exp_f32 v33, v33\n\t" \
    "v_permlane16_swap_b32 v72, v76\n\t" \
    "v_exp_f32 v1, v1\n\t" \
    "v_exp_f32 v34, v34\n\t" \
    "v_mov_b32 v74, v72\n\t" \
    "v_mov_b32 v78, v76\n\t" \
    "v_exp_f32 v2, v2\n\t" \
    "v_permlane32_swap_b32 v72, v74\n\t" \
    "v_permlane32_swap_b32 v76, v78\n\t" \
    "v_exp_f32 v35, v35\n\t" \
    "v_exp_f32 v3, v3\n\t" \
    "v_exp_f32 v36, v36\n\t" \
    "v_pk_add_f32 v[64:65], v[32:33], v[34:35]\n\t" \
    "v_pk_add_f32 v[66:67], v[0:1], v[2:3]\n\t" \
    "v_exp_f32 v4, v4\n\t" \
    "v_exp_f32 v37, v37\n\t" \
    "v_exp_f32 v5, v5\n\t" \
    "v_exp_f32 v38, v38\n\t" \
    "v_pk_add_f32 v[64:65], v[64:65], v[36:37]\n\t" \
    "v_pk_add_f32 v[66:67], v[66:67], v[4:5]\n\t" \
    "v_exp_f32 v6, v6\n\t" \
    "v_exp_f32 v39, v39\n\t" \
    "v_exp_f32 v7, v7\n\t" \
    "v_pk_fma_f32 v[40:41], v[8:9], s[90:91], v[68:69] op_sel_hi:[1,1,0]\n\t" \
    "v_pk_add_f32 v[64:65], v[64:65], v[38:39]\n\t" \
    "v_pk_add_f32 v[66:67], v[66:67], v[6:7]\n\t" \
    "v_pk_fma_f32 v[8:9], v[8:9], s[90:91], v[70:71] op_sel_hi:[1,1,0] neg_lo:[1,0,0] neg_hi:[1,0,0]\n\t" \
    "v_add_f32 v64, v64, v65\n\t" \
    "v_add_f32 v66, v66, v67\n\t" \
    "v_pk_fma_f32 v[42:43], v[10:11], s[90:91], v[68:69] op_sel_hi:[1,1,0]\n\t" \
    "v_pk_fma_f32 v[10:11], v[10:11], s[90:91], v[70:71] op_sel_hi:[1,1,0] neg_lo:[1,0,0] neg_hi:[1,0,0]\n\t" \
    "v_permlane32_swap_b32 v64, v66\n\t" \
    "v_pk_fma_f32 v[44:45], v[12:13], s[90:91], v[68:69] op_sel_hi:[1,1,0]\n\t" \
    "v_pk_fma_f32 v[12:13], v[12:13], s[90:91], v[70:71] op_sel_hi:[1,1,0] neg_lo:[1,0,0] neg_hi:[1,0,0]\n\t" \
    "v_add_f32 v64, v64, v66\n\t" \
    "v_pk_fma_f32 v[46:47], v[14:15], s[90:91], v[68:69] op_sel_hi:[1,1,0]\n\t" \
    "v_pk_fma_f32 v[14:15], v[14:15], s[90:91], v[70:71] op_sel_hi:[1,1,0] neg_lo:[1,0,0] neg_hi:[1,0,0]\n\t" \
    "v_exp_f32 v40, v40\n\t" \
    "v_exp_f32 v8, v8\n\t" \
    "v_exp_f32 v41, v41\n\t" \
    "v_exp_f32 v9, v9\n\t" \
    "v_exp_f32 v42, v42\n\t" \
    "v_exp_f32 v10, v10\n\t" \
    "v_exp_f32 v43, v43\n\t" \
    "v_exp_f32 v11, v11\n\t" \
    "v_exp_f32 v44, v44\n\t" \
    "v_pk_add_f32 v[68:69], v[40:41], v[42:43]\n\t" \
    "v_pk_add_f32 v[70:71], v[8:9], v[10:11]\n\t" \
    "v_exp_f32 v12, v12\n\t" \
    "v_exp_f32 v45, v45\n\t" \
    "v_exp_f32 v13, v13\n\t" \
    "v_exp_f32 v46, v46\n\t" \
    "v_pk_add_f32 v[68:69], v[68:69], v[44:45]\n\t" \
    "v_pk_add_f32 v[70:71], v[70:71], v[12:13]\n\t" \
    "v_exp_f32 v14, v14\n\t" \
    "v_exp_f32 v47, v47\n\t" \
    "v_exp_f32 v15, v15\n\t" \
    "v_pk_fma_f32 v[48:49], v[16:17], s[90:91], v[72:73] op_sel_hi:[1,1,0]\n\t" \
    "v_pk_add_f32 v[68:69], v[68:69], v[46:47]\n\t" \
    "v_pk_add_f32 v[70:71], v[70:71], v[14:15]\n\t" \
    "v_pk_fma_f32 v[16:17], v[16:17], s[90:91], v[74:75] op_sel_hi:[1,1,0] neg_lo:[1,0,0] neg_hi:[1,0,0]\n\t" \
    "v_add_f32 v68, v68, v69\n\t" \
    "v_add_f32 v70, v70, v71\n\t" \
    "v_pk_fma_f32 v[50:51], v[18:19], s[90:91], v[72:73] op_sel_hi:[1,1,0]\n\t" \
    "v_pk_fma_f32 v[18:19], v[18:19], s[90:91], v[74:75] op_sel_hi:[1,1,0] neg_lo:[1,0,0] neg_hi:[1,0,0]\n\t" \
    "v_permlane32_swap_b32 v68, v70\n\t" \
    "v_pk_fma_f32 v[52:53], v[20:21], s[90:91], v[72:73] op_sel_hi:[1,1,0]\n\t" \
    ".Lpad_ab1:\n\t" \
    "s_cbranch_execz .Lpad_ret\n\t" \
    "v_pk_fma_f32 v[20:21], v[20:21], s[90:91], v[74:75] op_sel_hi:[1,1,0] neg_lo:[1,0,0] neg_hi:[1,0,0]\n\t" \
    "v_add_f32 v68, v68, v70\n\t" \
    "v_pk_fma_f32 v[54:55], v[22:23], s[90:91], v[72:73] op_sel_hi:[1,1,0]\n\t" \
    "v_pk_fma_f32 v[22:23], v[22:23], s[90:91], v[74:75] op_sel_hi:[1,1,0] neg_lo:[1,0,0] neg_hi:[1,0,0]\n\t" \
    "v_permlane16_swap_b32 v64, v68\n\t" \
    "v_exp_f32 v48, v48\n\t" \
    "v_exp_f32 v16, v16\n\t" \
    "v_add_f32 v66, v64, v68\n\t" \
    "v_exp_f32 v49, v49\n\t" \
    "v_exp_f32 v17, v17\n\t" \
    "v_rcp_f32 v64, v66\n\t" \
    "v_exp_f32 v50, v50\n\t" \
    "v_exp_f32 v18, v18\n\t" \
    "v_mov_b32 v68, v64\n\t" \
    "v_exp_f32 v51, v51\n\t" \
    "v_exp_f32 v19, v19\n\t" \
    "v_permlane16_swap_b32 v64, v68\n\t" \
    "v_pk_add_f32 v[72:73], v[48:49], v[50:51]\n\t" \
    "v_pk_add_f32 v[74:75], v[16:17], v[18:19]\n\t" \
    "v_mov_b32 v66, v64\n\t" \
    "v_mov_b32 v70, v68\n\t" \
    "v_exp_f32 v52, v52\n\t" \
    "v_permlane32_swap_b32 v64, v66\n\t" \
    "v_permlane32_swap_b32 v68, v70\n\t" \
    "v_exp_f32 v20, v20\n\t" \
    "v_pk_mul_f32 v[0:1], v[0:1], v[66:67] op_sel_hi:[1,0]\n\t" \
    "v_pk_mul_f32 v[32:33], v[32:33], v[64:65] op_sel_hi:[1,0]\n\t" \
    "v_pk_mul_f32 v[2:3], v[2:3], v[66:67] op_sel_hi:[1,0]\n\t" \
    "v_pk_mul_f32 v[34:35], v[34:35], v[64:65] op_sel_hi:[1,0]\n\t" \
    "v_pk_mul_f32 v[4:5], v[4:5], v[66:67] op_sel_hi:[1,0]\n\t" \
    "v_pk_mul_f32 v[36:37], v[36:37], v[64:65] op_sel_hi:[1,0]\n\t" \
    "v_pk_mul_f32 v[6:7], v[6:7], v[66:67] op_sel_hi:[1,0]\n\t" \
    "v_pk_mul_f32 v[38:39], v[38:39], v[64:65] op_sel_hi:[1,0]\n\t" \
    "v_exp_f32 v53, v53\n\t" \
    "v_cvt_pk_bf16_f32 v7, v6, v7\n\t" \
    "v_cvt_pk_bf16_f32 v6, v4, v5\n\t" \
    "v_cvt_pk_bf16_f32 v5, v2, v3\n\t" \
    "v_cvt_pk_bf16_f32 v4, v0, v1\n\t" \
    "ds_write_b64 v253, v[6:7] offset:0\n\t" \
    "v_cvt_pk_bf16_f32 v0, v32, v33\n\t" \
    "v_cvt_pk_bf16_f32 v1, v34, v35\n\t" \
    "v_cvt_pk_bf16_f32 v2, v36, v37\n\t" \
    "v_cvt_pk_bf16_f32 v3, v38, v39\n\t" \
    "v_pk_add_f32 v[72:73], v[72:73], v[52:53]\n\t" \
    "ds_write_b64 v243, v[0:1] offset:0\n\t" \
    "v_exp_f32 v21, v21\n\t" \
    "v_exp_f32 v54, v54\n\t" \
    "v_exp_f32 v22, v22\n\t" \
    "v_pk_add_f32 v[74:75], v[74:75], v[20:21]\n\t" \
    "v_exp_f32 v55, v55\n\t" \
    "ds_write_b64 v251, v[2:3] offset:0\n\t" \
    "v_exp_f32 v23, v23\n\t" \
    "v_pk_add_f32 v[72:73], v[72:73], v[54:55]\n\t" \
    "v_pk_fma_f32 v[56:57], v[24:25], s[90:91], v[76:77] op_sel_hi:[1,1,0]\n\t" \
    "v_pk_add_f32 v[74:75], v[74:75], v[22:23]\n\t" \
    "v_add_f32 v72, v72, v73\n\t" \
    "ds_write_b64 v252, v[4:5] offset:0\n\t" \
    "v_add_f32 v74, v74, v75\n\t" \
    "v_pk_fma_f32 v[24:25], v[24:25], s[90:91], v[78:79] op_sel_hi:[1,1,0] neg_lo:[1,0,0] neg_hi:[1,0,0]\n\t" \
    "v_pk_fma_f32 v[58:59], v[26:27], s[90:91], v[76:77] op_sel_hi:[1,1,0]\n\t" \
    "v_permlane32_swap_b32 v72, v74\n\t" \
    "v_pk_fma_f32 v[26:27], v[26:27], s[90:91], v[78:79] op_sel_hi:[1,1,0] neg_lo:[1,0,0] neg_hi:[1,0,0]\n\t" \
    "v_pk_fma_f32 v[60:61], v[28:29], s[90:91], v[76:77] op_sel_hi:[1,1,0]\n\t" \
    "v_add_f32 v72, v72, v74\n\t" \
    "v_pk_fma_f32 v[28:29], v[28:29], s[90:91], v[78:79] op_sel_hi:[1,1,0] neg_lo:[1,0,0] neg_hi:[1,0,0]\n\t" \
    "v_pk_fma_f32 v[62:63], v[30:31], s[90:91], v[76:77] op_sel_hi:[1,1,0]\n\t" \
    "v_pk_fma_f32 v[30:31], v[30:31], s[90:91], v[78:79] op_sel_hi:[1,1,0] neg_lo:[1,0,0] neg_hi:[1,0,0]\n\t" \
    "v_exp_f32 v56, v56\n\t" \
    "v_exp_f32 v24, v24\n\t" \
    "v_exp_f32 v57, v57\n\t" \
    "v_exp_f32 v25, v25\n\t" \
    "v_exp_f32 v58, v58\n\t" \
    "v_exp_f32 v26, v26\n\t" \
    "v_exp_f32 v59, v59\n\t" \
    "v_exp_f32 v27, v27\n\t" \
    "v_exp_f32 v60, v60\n\t" \
    "v_pk_add_f32 v[76:77], v[56:57], v[58:59]\n\t" \
    "v_pk_add_f32 v[78:79], v[24:25], v[26:27]\n\t" \
    "v_exp_f32 v28, v28\n\t" \
    "v_exp_f32 v61, v61\n\t" \
    "v_exp_f32 v29, v29\n\t" \
    "v_exp_f32 v62, v62\n\t" \
    "v_pk_add_f32 v[76:77], v[76:77], v[60:61]\n\t" \
    "v_pk_add_f32 v[78:79], v[78:79], v[28:29]\n\t" \
    "v_exp_f32 v30, v30\n\t" \
    "v_exp_f32 v63, v63\n\t" \
    "v_exp_f32 v31, v31\n\t" \
    "v_pk_mul_f32 v[8:9], v[8:9], v[70:71] op_sel_hi:[1,0]\n\t" \
    "v_pk_add_f32 v[76:77], v[76:77], v[62:63]\n\t" \
    "v_pk_add_f32 v[78:79], v[78:79], v[30:31]\n\t" \
    "v_pk_mul_f32 v[40:41], v[40:41], v[68:69] op_sel_hi:[1,0]\n\t" \
    "v_add_f32 v76, v76, v77\n\t" \
    "v_add_f32 v78, v78, v79\n\t" \
    "v_pk_mul_f32 v[10:11], v[10:11], v[70:71] op_sel_hi:[1,0]\n\t" \
    "v_pk_mul_f32 v[42:43], v[42:43], v[68:69] op_sel_hi:[1,0]\n\t" \
    "v_permlane32_swap_b32 v76, v78\n\t" \
    "v_pk_mul_f32 v[12:13], v[12:13], v[70:71] op_sel_hi:[1,0]\n\t" \
    "v_pk_mul_f32 v[44:45], v[44:45], v[68:69] op_sel_hi:[1,0]\n\t" \
    "v_add_f32 v76, v76, v78\n\t" \
    "v_pk_mul_f32 v[14:15], v[14:15], v[70:71] op_sel_hi:[1,0]\n\t" \
    "v_pk_mul_f32 v[46:47], v[46:47], v[68:69] op_sel_hi:[1,0]\n\t" \
    "v_permlane16_swap_b32 v72, v76\n\t" \
    "v_cvt_pk_bf16_f32 v15, v14, v15\n\t" \
    "v_cvt_pk_bf16_f32 v14, v12, v13\n\t" \
    "v_add_f32 v74, v72, v76\n\t" \
    "v_cvt_pk_bf16_f32 v13, v10, v11\n\t" \
    "ds_write_b64 v253, v[14:15] offset:2048\n\t" \
    "v_rcp_f32 v72, v74\n\t" \
    "v_cvt_pk_bf16_f32 v12, v8, v9\n\t" \
    "v_cvt_pk_bf16_f32 v8, v40, v41\n\t" \
    "v_mov_b32 v76, v72\n\t" \
    "v_cvt_pk_bf16_f32 v9, v42, v43\n\t" \
    "ds_write_b64 v252, v[12:13] offset:2048\n\t" \
    "v_permlane16_swap_b32 v72, v76\n\t" \
    "v_cvt_pk_bf16_f32 v10, v44, v45\n\t" \
    "v_cvt_pk_bf16_f32 v11, v46, v47\n\t" \
    "v_mov_b32 v74, v72\n\t" \
    "v_mov_b32 v78, v76\n\t" \
    "ds_write_b64 v243, v[8:9] offset:2048\n\t" \
    "v_permlane32_swap_b32 v72, v74\n\t" \
    "v_permlane32_swap_b32 v76, v78\n\t" \
    "ds_write_b64 v251, v[10:11] offset:2048\n\t" \
    "v_mov_b32 v237, 3\n\t" \
    "v_pk_mul_f32 v[16:17], v[16:17], v[74:75] op_sel_hi:[1,0]\n\t" \
    "ds_write_b32 v248, v237\n\t" \
    "v_pk_mul_f32 v[48:49], v[48:49], v[72:73] op_sel_hi:[1,0]\n\t" \
    "ds_read_b64_tr_b16 v[96:97], v244\n\t" \
    "v_pk_mul_f32 v[18:19], v[18:19], v[74:75] op_sel_hi:[1,0]\n\t" \
    "ds_read_b64_tr_b16 v[98:99], v244 offset:2048\n\t" \
    "v_pk_mul_f32 v[50:51], v[50:51], v[72:73] op_sel_hi:[1,0]\n\t" \
    "ds_read_b64_tr_b16 v[100:101], v254\n\t" \
    "v_pk_mul_f32 v[20:21], v[20:21], v[74:75] op_sel_hi:[1,0]\n\t" \
    "ds_read_b64_tr_b16 v[102:103], v254 offset:2048\n\t" \
    "v_pk_mul_f32 v[52:53], v[52:53], v[72:73] op_sel_hi:[1,0]\n\t" \
    "v_pk_mul_f32 v[22:23], v[22:23], v[74:75] op_sel_hi:[1,0]\n\t" \
    "v_pk_mul_f32 v[54:55], v[54:55], v[72:73] op_sel_hi:[1,0]\n\t" \
    "v_pk_mul_f32 v[24:25], v[24:25], v[78:79] op_sel_hi:[1,0]\n\t" \
    "v_cvt_pk_bf16_f32 v23, v22, v23\n\t" \
    "v_cvt_pk_bf16_f32 v22, v20, v21\n\t" \
    "v_cvt_pk_bf16_f32 v21, v18, v19\n\t" \
    "v_cvt_pk_bf16_f32 v20, v16, v17\n\t" \
    "ds_write_b64 v253, v[22:23] offset:4096\n\t" \
    "v_cvt_pk_bf16_f32 v16, v48, v49\n\t" \
    "v_cvt_pk_bf16_f32 v17, v50, v51\n\t" \
    "v_cvt_pk_bf16_f32 v18, v52, v53\n\t" \
    "v_cvt_pk_bf16_f32 v19, v54, v55\n\t" \
    "v_pk_mul_f32 v[56:57], v[56:57], v[76:77] op_sel_hi:[1,0]\n\t" \
    "ds_write_b64 v243, v[16:17] offset:4096\n\t" \
    "v_pk_mul_f32 v[26:27], v[26:27], v[78:79] op_sel_hi:[1,0]\n\t" \
    "v_pk_mul_f32 v[58:59], v[58:59], v[76:77] op_sel_hi:[1,0]\n\t" \
    "v_pk_mul_f32 v[28:29], v[28:29], v[78:79] op_sel_hi:[1,0]\n\t" \
    "v_pk_mul_f32 v[60:61], v[60:61], v[76:77] op_sel_hi:[1,0]\n\t" \
    "v_pk_mul_f32 v[30:31], v[30:31], v[78:79] op_sel_hi:[1,0]\n\t" \
    "ds_write_b64 v251, v[18:19] offset:4096\n\t" \
    "v_pk_mul_f32 v[62:63], v[62:63], v[76:77] op_sel_hi:[1,0]\n\t" \
    "v_cvt_pk_bf16_f32 v31, v30, v31\n\t" \
    "v_cvt_pk_bf16_f32 v30, v28, v29\n\t" \
    "v_cvt_pk_bf16_f32 v29, v26, v27\n\t" \
    "v_cvt_pk_bf16_f32 v28, v24, v25\n\t" \
    "ds_write_b64 v252, v[20:21] offset:4096\n\t" \
    "ds_read_b32 v238, v250\n\t" \
    "v_cvt_pk_bf16_f32 v24, v56, v57\n\t" \
    "ds_read_b64_tr_b16 v[104:105], v245\n\t" \
    "v_cvt_pk_bf16_f32 v25, v58, v59\n\t" \
    "ds_read_b64_tr_b16 v[106:107], v245 offset:2048\n\t" \
    "v_cvt_pk_bf16_f32 v26, v60, v61\n\t" \
    "ds_read_b64_tr_b16 v[108:109], v255\n\t" \
    "v_cvt_pk_bf16_f32 v27, v62, v63\n\t" \
    "ds_read_b64_tr_b16 v[110:111], v255 offset:2048\n\t" \
    "ds_write_b64 v243, v[24:25] offset:6144\n\t" \
    "ds_read_b128 v[80:83], v242\n\t" \
    "ds_write_b64 v251, v[26:27] offset:6144\n\t" \
    "ds_read_b128 v[84:87], v242 offset:1024\n\t" \
    "ds_write_b64 v252, v[28:29] offset:6144\n\t" \
    "ds_read_b128 v[88:91], v242 offset:4096\n\t" \
    "ds_write_b64 v253, v[30:31] offset:6144\n\t" \
    "ds_read_b128 v[92:95], v242 offset:5120\n\t" \
    "v_mov_b32 v237, 4\n\t" \
    "ds_write_b32 v248, v237\n\t" \
    "s_waitcnt lgkmcnt(14)\n\t" \
    "v_mfma_f32_16x16x32_bf16 v[64:67], v[96:99], v[232:235], 0\n\t" \
    "ds_read_b64_tr_b16 v[0:1], v244 offset:4096\n\t" \
    "ds_read_b64_tr_b16 v[2:3], v244 offset:6144\n\t" \
    "v_mfma_f32_16x16x32_bf16 v[68:71], v[100:103], v[232:235], 0\n\t" \
    "ds_read_b64_tr_b16 v[4:5], v254 offset:4096\n\t" \
    "ds_read_b64_tr_b16 v[6:7], v254 offset:6144\n\t" \
    "v_mfma_f32_16x16x32_bf16 v[32:35], v[96:99], v[112:115], 0\n\t" \
    "v_mfma_f32_16x16x32_bf16 v[40:43], v[100:103], v[112:115], 0\n\t" \
    "s_waitcnt lgkmcnt(14)\n\t" \
    "v_readfirstlane_b32 s85, v238\n\t" \
    "s_cmp_lt_u32 s85, 3\n\t" \
    "s_cbranch_scc1 .Lab_s3\n\t" \
    ".Lab_s3r:\n\t" \
    "v_mfma_f32_16x16x32_bf16 v[36:39], v[96:99], v[116:119], 0\n\t" \
    "v_mfma_f32_16x16x32_bf16 v[44:47], v[100:103], v[116:119], 0\n\t" \
    "ds_read_b32 v238, v250\n\t" \
    "ds_read_b64_tr_b16 v[8:9], v245 offset:4096\n\t" \
    "v_mfma_f32_16x16x32_bf16 v[64:67], v[104:107], v[232:235], v[64:67]\n\t" \
    "ds_read_b64_tr_b16 v[10:11], v245 offset:6144\n\t" \
    "ds_read_b64_tr_b16 v[12:13], v255 offset:4096\n\t" \
    "s_waitcnt lgkmcnt(14)\n\t" \
    "v_mfma_f32_16x16x32_bf16 v[68:71], v[108:111], v[232:235], v[68:71]\n\t" \
    "ds_read_b64_tr_b16 v[14:15], v255 offset:6144\n\t" \
    "v_mfma_f32_16x16x32_bf16 v[32:35], v[104:107], v[80:83], v[32:35]\n\t" \
    "v_mfma_f32_16x16x32_bf16 v[40:43], v[108:111], v[80:83], v[40:43]\n\t" \
    "s_waitcnt lgkmcnt(14)\n\t" \
    "v_mfma_f32_16x16x32_bf16 v[36:39], v[104:107], v[84:87], v[36:39]\n\t" \
    "v_add_u32 v253, 135168, v241\n\t" \
    "v_mfma_f32_16x16x32_bf16 v[44:47], v[108:111], v[84:87], v[44:47]\n\t" \
    "v_xor_b32 v251, 32, v253\n\t" \
    "s_waitcnt lgkmcnt(7)\n\t" \
    "v_mfma_f32_16x16x32_bf16 v[64:67], v[0:3], v[232:235], v[64:67]\n\t" \
    "v_mov_b32 v237, 5\n\t" \
    "s_waitcnt lgkmcnt(5)\n\t" \
    "v_mfma_f32_16x16x32_bf16 v[68:71], v[4:7], v[232:235], v[68:71]\n\t" \
    "v_mfma_f32_16x16x32_bf16 v[32:35], v[0:3], v[120:123], v[32:35]\n\t" \
    "v_mfma_f32_16x16x32_bf16 v[40:43], v[4:7], v[120:123], v[40:43]\n\t" \
    "v_mfma_f32_16x16x32_bf16 v[36:39], v[0:3], v[124:127], v[36:39]\n\t" \
    "s_waitcnt lgkmcnt(4)\n\t" \
    "v_readfirstlane_b32 s85, v238\n\t" \
    "s_cmp_lt_u32 s85, 4\n\t" \
    "s_cbranch_scc1 .Lab_s4\n\t" \
    ".Lab_s4r:\n\t" \
    "v_mfma_f32_16x16x32_bf16 v[44:47], v[4:7], v[124:127], v[44:47]\n\t" \
    "ds_write_b32 v248, v237\n\t" \
    "s_waitcnt lgkmcnt(3)\n\t" \
    "v_mfma_f32_16x16x32_bf16 v[64:67], v[8:11], v[232:235], v[64:67]\n\t" \
    "ds_read_b32 v238, v249 offset:92\n\t" \
    "s_waitcnt lgkmcnt(2)\n\t" \
    "v_mfma_f32_16x16x32_bf16 v[68:71], v[12:15], v[232:235], v[68:71]\n\t" \
    "v_mfma_f32_16x16x32_bf16 v[32:35], v[8:11], v[88:91], v[32:35]\n\t" \
    "v_mfma_f32_16x16x32_bf16 v[40:43], v[12:15], v[88:91], v[40:43]\n\t" \
    "v_mfma_f32_16x16x32_bf16 v[36:39], v[8:11], v[92:95], v[36:39]\n\t" \
    "v_mfma_f32_16x16x32_bf16 v[44:47], v[12:15], v[92:95], v[44:47]\n\t" \
    "ds_read_b64_tr_b16 v[80:81], v253\n\t" \
    "ds_read_b64_tr_b16 v[82:83], v253 offset:512\n\t" \
    "ds_read_b64_tr_b16 v[84:85], v251\n\t" \
    "s_nop 0\n\t" \
    "v_cndmask_b32 v24, v68, v64, vcc\n\t" \
    "v_cndmask_b32 v25, v69, v65, vcc\n\t" \
    "v_cndmask_b32 v26, v70, v66, vcc\n\t" \
    "v_cndmask_b32 v27, v71, v67, vcc\n\t" \
    "v_add3_u32 v30, v24, v25, v26\n\t" \
    "v_add_u32 v30, v30, v27\n\t" \
    "v_cvt_pk_bf16_f32 v16, v32, v33\n\t" \
    "v_cvt_pk_bf16_f32 v17, v34, v35\n\t" \
    "v_cvt_pk_bf16_f32 v18, v40, v41\n\t" \
    "v_cvt_pk_bf16_f32 v19, v42, v43\n\t" \
    "v_cvt_pk_bf16_f32 v20, v36, v37\n\t" \
    "v_cvt_pk_bf16_f32 v21, v38, v39\n\t" \
    "v_cvt_pk_bf16_f32 v22, v44, v45\n\t" \
    "v_cvt_pk_bf16_f32 v23, v46, v47\n\t" \
    "v_add3_u32 v28, v16, v17, v18\n\t" \
    "v_add3_u32 v29, v20, v21, v22\n\t" \
    "v_add_u32 v28, v28, v19\n\t" \
    "v_add_u32 v29, v29, v23\n\t" \
    "v_cndmask_b32_e64 v30, 0, v30, s[94:95]\n\t" \
    "buffer_store_dwordx4 v[16:19], v246, %[rsb], s87 offen sc1\n\t" \
    "v_permlane16_swap_b32 v28, v29\n\t" \
    "v_add3_u32 v28, v28, v29, v30\n\t" \
    "buffer_store_dwordx4 v[20:23], v246, %[rsb], s86 offen sc1\n\t" \
    "v_cndmask_b32_e64 v25, v25, v236, s[92:93]\n\t" \
    "v_add_u32_dpp v28, v28, v28 row_ror:8 row_mask:0xf bank_mask:0xf bound_ctrl:1\n\t" \
    "v_mov_b32 v237, 6\n\t" \
    "ds_read_b64_tr_b16 v[86:87], v251 offset:512\n\t" \
    "v_add_u32_dpp v28, v28, v28 row_ror:4 row_mask:0xf bank_mask:0xf bound_ctrl:1\n\t" \
    "ds_read_b64_tr_b16 v[88:89], v253 offset:4096\n\t" \
    "ds_read_b64_tr_b16 v[90:91], v253 offset:4608\n\t" \
    "v_add_u32_dpp v28, v28, v28 row_ror:2 row_mask:0xf bank_mask:0xf bound_ctrl:1\n\t" \
    "ds_read_b64_tr_b16 v[92:93], v251 offset:4096\n\t" \
    "ds_read_b64_tr_b16 v[94:95], v251 offset:4608\n\t" \
    "v_add_u32_dpp v28, v28, v28 row_ror:1 row_mask:0xf bank_mask:0xf bound_ctrl:1\n\t" \
    "v_cndmask_b32_e64 v24, v24, v28, s[92:93]\n\t" \
    "buffer_store_dwordx4 v[24:27], v247, %[rsb], s88 offen sc1\n\t" \
    "ds_write_b32 v248, v237\n\t" \
    "s_setprio 1\n\t" \
    "s_waitcnt lgkmcnt(9)\n\t" \
    "v_readfirstlane_b32 s85, v238\n\t" \
    "s_cmp_lt_u32 s85, 1\n\t" \
    "s_cbranch_scc1 .Lab_s5\n\t" \
    ".Lab_s5r:\n\t" \
    "s_waitcnt lgkmcnt(7)\n\t" \
    "v_mfma_f32_16x16x32_bf16 v[192:195], v[80:83], v[128:131], v[192:195]\n\t" \
    "v_add_u32 v244, 49152, v241\n\t" \
    "v_xor_b32 v245, 32, v244\n\t" \
    "v_mfma_f32_16x16x32_bf16 v[200:203], v[80:83], v[132:135], v[200:203]\n\t" \
    "ds_read_b64_tr_b16 v[96:97], v244\n\t" \
    "ds_read_b64_tr_b16 v[98:99], v244 offset:512\n\t" \
    "v_mfma_f32_16x16x32_bf16 v[208:211], v[80:83], v[136:139], v[208:211]\n\t" \
    "ds_read_b64_tr_b16 v[100:101], v245\n\t" \
    "ds_read_b64_tr_b16 v[102:103], v245 offset:512\n\t" \
    "v_mfma_f32_16x16x32_bf16 v[216:219], v[80:83], v[140:143], v[216:219]\n\t" \
    "ds_read_b64_tr_b16 v[104:105], v244 offset:4096\n\t" \
    "ds_read_b64_tr_b16 v[106:107], v244 offset:4608\n\t" \
    "s_waitcnt lgkmcnt(11)\n\t" \
    "v_mfma_f32_16x16x32_bf16 v[196:199], v[84:87], v[128:131], v[196:199]\n\t" \
    "ds_read_b64_tr_b16 v[108:109], v245 offset:4096\n\t" \
    "ds_read_b64_tr_b16 v[110:111], v245 offset:4608\n\t" \
    "v_mfma_f32_16x16x32_bf16 v[204:207], v[84:87], v[132:135], v[204:207]\n\t" \
    "v_xor_b32 v246, 64, v240\n\t" \
    "v_subrev_u32 v242, 16384, v241\n\t" \
    "v_mfma_f32_16x16x32_bf16 v[212:215], v[84:87], v[136:139], v[212:215]\n\t" \
    "v_xor_b32 v243, 32, v242\n\t" \
    "v_add_u32 v247, 65536, v240\n\t" \
    "v_mfma_f32_16x16x32_bf16 v[220:223], v[84:87], v[140:143], v[220:223]\n\t" \
    "v_mov_b32 v237, 7\n\t" \
    "ds_write_b32 v248, v237\n\t" \
    "s_waitcnt lgkmcnt(12)\n\t" \
    "v_mfma_f32_16x16x32_bf16 v[192:195], v[88:91], v[144:147], v[192:195]\n\t" \
    "ds_read_b64_tr_b16 v[80:81], v244 offset:8192\n\t" \
    "v_mfma_f32_16x16x32_bf16 v[200:203], v[88:91], v[148:151], v[200:203]\n\t" \
    "ds_read_b128 v[128:131], v240 offset:40960\n\t" \
    "v_mfma_f32_16x16x32_bf16 v[208:211], v[88:91], v[152:155], v[208:211]\n\t" \
    "ds_read_b64_tr_b16 v[82:83], v244 offset:8704\n\t" \
    "v_add_u32 v237, 1, v237\n\t" \
    "v_mfma_f32_16x16x32_bf16 v[216:219], v[88:91], v[156:159], v[216:219]\n\t" \
    "ds_read_b128 v[132:135], v240 offset:43008\n\t" \
    "s_waitcnt lgkmcnt(14)\n\t" \
    "v_mfma_f32_16x16x32_bf16 v[196:199], v[92:95], v[144:147], v[196:199]\n\t" \
    "ds_read_b64_tr_b16 v[84:85], v245 offset:8192\n\t" \
    "v_xor_b32 v255, 64, v247\n\t" \
    "v_mfma_f32_16x16x32_bf16 v[204:207], v[92:95], v[148:151], v[204:207]\n\t" \
    "ds_read_b128 v[136:139], v240 offset:45056\n\t" \
    "v_mfma_f32_16x16x32_bf16 v[212:215], v[92:95], v[152:155], v[212:215]\n\t" \
    "ds_read_b64_tr_b16 v[86:87], v245 offset:8704\n\t" \
    "v_mfma_f32_16x16x32_bf16 v[220:223], v[92:95], v[156:159], v[220:223]\n\t" \
    "ds_read_b128 v[140:143], v240 offset:47104\n\t" \
    "s_waitcnt lgkmcnt(14)\n\t" \
    "v_mfma_f32_16x16x32_bf16 v[192:195], v[96:99], v[160:163], v[192:195]\n\t" \
    "ds_read_b64_tr_b16 v[88:89], v244 offset:12288\n\t" \
    "v_mfma_f32_16x16x32_bf16 v[200:203], v[96:99], v[164:167], v[200:203]\n\t" \
    "ds_read_b128 v[144:147], v246 offset:40960\n\t" \
    "v_mfma_f32_16x16x32_bf16 v[208:211], v[96:99], v[168:171], v[208:211]\n\t" \
    "ds_read_b64_tr_b16 v[90:91], v244 offset:12800\n\t" \
    "v_mfma_f32_16x16x32_bf16 v[216:219], v[96:99], v[172:175], v[216:219]\n\t" \
    "ds_read_b128 v[148:151], v246 offset:43008\n\t" \
    "s_waitcnt lgkmcnt(14)\n\t" \
    "v_mfma_f32_16x16x32_bf16 v[196:199], v[100:103], v[160:163], v[196:199]\n\t" \
    "ds_read_b64_tr_b16 v[92:93], v245 offset:12288\n\t" \
    "v_mfma_f32_16x16x32_bf16 v[204:207], v[100:103], v[164:167], v[204:207]\n\t" \
    "ds_read_b128 v[152:155], v246 offset:45056\n\t" \
    "v_mfma_f32_16x16x32_bf16 v[212:215], v[100:103], v[168:171], v[212:215]\n\t" \
    "ds_read_b64_tr_b16 v[94:95], v245 offset:12800\n\t" \
    "v_mfma_f32_16x16x32_bf16 v[220:223], v[100:103], v[172:175], v[220:223]\n\t" \
    "ds_read_b128 v[156:159], v246 offset:47104\n\t" \
    "ds_write_b32 v248, v237\n\t" \
    "v_mfma_f32_16x16x32_bf16 v[192:195], v[104:107], v[176:179], v[192:195]\n\t" \
    "ds_read_b64_tr_b16 v[96:97], v242 offset:24576\n\t" \
    "v_mfma_f32_16x16x32_bf16 v[200:203], v[104:107], v[180:183], v[200:203]\n\t" \
    "ds_read_b128 v[160:163], v247 offset:16384\n\t" \
    "v_mfma_f32_16x16x32_bf16 v[208:211], v[104:107], v[184:187], v[208:211]\n\t" \
    "ds_read_b64_tr_b16 v[98:99], v242 offset:25088\n\t" \
    "v_add_u32 v237, 1, v237\n\t" \
    "v_mfma_f32_16x16x32_bf16 v[216:219], v[104:107], v[188:191], v[216:219]\n\t" \
    "ds_read_b128 v[164:167], v247 offset:18432\n\t" \
    "s_waitcnt lgkmcnt(14)\n\t" \
    "v_mfma_f32_16x16x32_bf16 v[196:199], v[108:111], v[176:179], v[196:199]\n\t" \
    "ds_read_b64_tr_b16 v[100:101], v243 offset:24576\n\t" \
    "v_mfma_f32_16x16x32_bf16 v[204:207], v[108:111], v[180:183], v[204:207]\n\t" \
    "ds_read_b128 v[168:171], v247 offset:20480\n\t" \
    "v_mfma_f32_16x16x32_bf16 v[212:215], v[108:111], v[184:187], v[212:215]\n\t" \
    "ds_read_b64_tr_b16 v[102:103], v243 offset:25088\n\t" \
    "v_mfma_f32_16x16x32_bf16 v[220:223], v[108:111], v[188:191], v[220:223]\n\t" \
    "ds_read_b128 v[172:175], v247 offset:22528\n\t" \
    "v_mfma_f32_16x16x32_bf16 v[192:195], v[80:83], v[128:131], v[192:195]\n\t" \
    "ds_read_b64_tr_b16 v[104:105], v242 offset:28672\n\t" \
    "v_mfma_f32_16x16x32_bf16 v[200:203], v[80:83], v[132:135], v[200:203]\n\t" \
    "ds_read_b128 v[176:179], v255 offset:16384\n\t" \
    "v_mfma_f32_16x16x32_bf16 v[208:211], v[80:83], v[136:139], v[208:211]\n\t" \
    "ds_read_b64_tr_b16 v[106:107], v242 offset:29184\n\t" \
    "s_waitcnt lgkmcnt(14)\n\t" \
    "v_mfma_f32_16x16x32_bf16 v[216:219], v[80:83], v[140:143], v[216:219]\n\t" \
    "ds_read_b128 v[180:183], v255 offset:18432\n\t" \
    "v_mfma_f32_16x16x32_bf16 v[196:199], v[84:87], v[128:131], v[196:199]\n\t" \
    "ds_read_b64_tr_b16 v[108:109], v243 offset:28672\n\t" \
    "v_mfma_f32_16x16x32_bf16 v[204:207], v[84:87], v[132:135], v[204:207]\n\t" \
    "ds_read_b128 v[184:187], v255 offset:20480\n\t" \
    "v_mfma_f32_16x16x32_bf16 v[212:215], v[84:87], v[136:139], v[212:215]\n\t" \
    "ds_read_b64_tr_b16 v[110:111], v243 offset:29184\n\t" \
    "v_mfma_f32_16x16x32_bf16 v[220:223], v[84:87], v[140:143], v[220:223]\n\t" \
    "ds_read_b128 v[188:191], v255 offset:22528\n\t" \
    "ds_write_b32 v248, v237\n\t" \
    "v_mfma_f32_16x16x32_bf16 v[192:195], v[88:91], v[144:147], v[192:195]\n\t" \
    "ds_read_b64_tr_b16 v[80:81], v242 offset:32768\n\t" \
    "v_mfma_f32_16x16x32_bf16 v[200:203], v[88:91], v[148:151], v[200:203]\n\t" \
    "ds_read_b128 v[128:131], v240\n\t" \
    "v_mfma_f32_16x16x32_bf16 v[208:211], v[88:91], v[152:155], v[208:211]\n\t" \
    "ds_read_b64_tr_b16 v[82:83], v242 offset:33280\n\t" \
    "v_add_u32 v237, 1, v237\n\t" \
    "s_waitcnt lgkmcnt(14)\n\t" \
    "v_mfma_f32_16x16x32_bf16 v[216:219], v[88:91], v[156:159], v[216:219]\n\t" \
    "ds_read_b128 v[132:135], v240 offset:2048\n\t" \
    "v_mfma_f32_16x16x32_bf16 v[196:199], v[92:95], v[144:147], v[196:199]\n\t" \
    "ds_read_b64_tr_b16 v[84:85], v243 offset:32768\n\t" \
    "v_mfma_f32_16x16x32_bf16 v[204:207], v[92:95], v[148:151], v[204:207]\n\t" \
    "ds_read_b128 v[136:139], v240 offset:4096\n\t" \
    "v_mfma_f32_16x16x32_bf16 v[212:215], v[92:95], v[152:155], v[212:215]\n\t" \
    "ds_read_b64_tr_b16 v[86:87], v243 offset:33280\n\t" \
    "ds_read_b32 v238, v249 offset:92\n\t" \
    "v_mfma_f32_16x16x32_bf16 v[220:223], v[92:95], v[156:159], v[220:223]\n\t" \
    "ds_read_b128 v[140:143], v240 offset:6144\n\t" \
    "v_mfma_f32_16x16x32_bf16 v[192:195], v[96:99], v[160:163], v[192:195]\n\t" \
    "ds_read_b64_tr_b16 v[88:89], v242 offset:36864\n\t" \
    "v_add_u32 v247, %[xs0], v240\n\t" \
    "v_mfma_f32_16x16x32_bf16 v[200:203], v[96:99], v[164:167], v[200:203]\n\t" \
    "ds_read_b128 v[144:147], v246\n\t" \
    "v_mfma_f32_16x16x32_bf16 v[208:211], v[96:99], v[168:171], v[208:211]\n\t" \
    "ds_read_b64_tr_b16 v[90:91], v242 offset:37376\n\t" \
    "v_xor_b32 v255, 64, v247\n\t" \
    "s_waitcnt lgkmcnt(14)\n\t" \
    "v_mfma_f32_16x16x32_bf16 v[216:219], v[96:99], v[172:175], v[216:219]\n\t" \
    "ds_read_b128 v[148:151], v246 offset:2048\n\t" \
    "v_mfma_f32_16x16x32_bf16 v[196:199], v[100:103], v[160:163], v[196:199]\n\t" \
    "ds_read_b64_tr_b16 v[92:93], v243 offset:36864\n\t" \
    "v_mfma_f32_16x16x32_bf16 v[204:207], v[100:103], v[164:167], v[204:207]\n\t" \
    "ds_read_b128 v[152:155], v246 offset:4096\n\t" \
    "v_mfma_f32_16x16x32_bf16 v[212:215], v[100:103], v[168:171], v[212:215]\n\t" \
    "ds_read_b64_tr_b16 v[94:95], v243 offset:37376\n\t" \
    "v_mfma_f32_16x16x32_bf16 v[220:223], v[100:103], v[172:175], v[220:223]\n\t" \
    "ds_read_b128 v[156:159], v246 offset:6144\n\t" \
    "ds_write_b32 v248, v237\n\t" \
    "v_mfma_f32_16x16x32_bf16 v[192:195], v[104:107], v[176:179], v[192:195]\n\t" \
    "s_waitcnt lgkmcnt(10)\n\t" \
    "v_readfirstlane_b32 s85, v238\n\t" \
    "s_cmp_lt_u32 s85, 2\n\t" \
    "s_cbranch_scc1 .Lab_s6\n\t" \
    ".Lab_s6r:\n\t" \
    "ds_read_b64_tr_b16 v[96:97], v244 offset:32768\n\t" \
    "v_mfma_f32_16x16x32_bf16 v[200:203], v[104:107], v[180:183], v[200:203]\n\t" \
    "ds_read_b128 v[160:163], v247\n\t" \
    "v_mfma_f32_16x16x32_bf16 v[208:211], v[104:107], v[184:187], v[208:211]\n\t" \
    "ds_read_b64_tr_b16 v[98:99], v244 offset:33280\n\t" \
    "v_add_u32 v237, 1, v237\n\t" \
    "v_mfma_f32_16x16x32_bf16 v[216:219], v[104:107], v[188:191], v[216:219]\n\t" \
    "ds_read_b128 v[164:167], v247 offset:2048\n\t" \
    "v_mfma_f32_16x16x32_bf16 v[196:199], v[108:111], v[176:179], v[196:199]\n\t" \
    "ds_read_b64_tr_b16 v[100:101], v245 offset:32768\n\t" \
    "v_mfma_f32_16x16x32_bf16 v[204:207], v[108:111], v[180:183], v[204:207]\n\t" \
    "ds_read_b128 v[168:171], v247 offset:4096\n\t" \
    "v_mfma_f32_16x16x32_bf16 v[212:215], v[108:111], v[184:187], v[212:215]\n\t" \
    "ds_read_b64_tr_b16 v[102:103], v245 offset:33280\n\t" \
    "v_mfma_f32_16x16x32_bf16 v[220:223], v[108:111], v[188:191], v[220:223]\n\t" \
    "ds_read_b128 v[172:175], v247 offset:6144\n\t" \
    "v_mfma_f32_16x16x32_bf16 v[192:195], v[80:83], v[128:131], v[192:195]\n\t" \
    "ds_read_b64_tr_b16 v[104:105], v244 offset:36864\n\t" \
    "v_mfma_f32_16x16x32_bf16 v[200:203], v[80:83], v[132:135], v[200:203]\n\t" \
    "ds_read_b128 v[176:179], v255\n\t" \
    "v_mfma_f32_16x16x32_bf16 v[208:211], v[80:83], v[136:139], v[208:211]\n\t" \
    "ds_read_b64_tr_b16 v[106:107], v244 offset:37376\n\t" \
    "s_waitcnt lgkmcnt(14)\n\t" \
    "v_mfma_f32_16x16x32_bf16 v[216:219], v[80:83], v[140:143], v[216:219]\n\t" \
    "ds_read_b128 v[180:183], v255 offset:2048\n\t" \
    "v_mfma_f32_16x16x32_bf16 v[196:199], v[84:87], v[128:131], v[196:199]\n\t" \
    "ds_read_b64_tr_b16 v[108:109], v245 offset:36864\n\t" \
    "v_mfma_f32_16x16x32_bf16 v[204:207], v[84:87], v[132:135], v[204:207]\n\t" \
    "ds_read_b128 v[184:187], v255 offset:4096\n\t" \
    "v_mfma_f32_16x16x32_bf16 v[212:215], v[84:87], v[136:139], v[212:215]\n\t" \
    "ds_read_b64_tr_b16 v[110:111], v245 offset:37376\n\t" \
    "v_mfma_f32_16x16x32_bf16 v[220:223], v[84:87], v[140:143], v[220:223]\n\t" \
    "ds_read_b128 v[188:191], v255 offset:6144\n\t" \
    "ds_write_b32 v248, v237\n\t" \
    "v_mfma_f32_16x16x32_bf16 v[192:195], v[88:91], v[144:147], v[192:195]\n\t" \
    "v_mbcnt_lo_u32_b32 v250, -1, 0\n\t" \
    "v_mbcnt_hi_u32_b32 v250, -1, v250\n\t" \
    "v_mfma_f32_16x16x32_bf16 v[200:203], v[88:91], v[148:151], v[200:203]\n\t" \
    "v_and_b32 v238, 15, v250\n\t" \
    "v_lshlrev_b32 v238, 3, v238\n\t" \
    "v_mfma_f32_16x16x32_bf16 v[208:211], v[88:91], v[152:155], v[208:211]\n\t" \
    "v_add_u32 v237, 1, v237\n\t" \
    "s_waitcnt lgkmcnt(14)\n\t" \
    "v_mfma_f32_16x16x32_bf16 v[216:219], v[88:91], v[156:159], v[216:219]\n\t" \
    "buffer_load_dwordx2 v[232:233], v238, %[rsw], %[sg] offen sc1\n\t" \
    "v_mfma_f32_16x16x32_bf16 v[196:199], v[92:95], v[144:147], v[196:199]\n\t" \
    "v_lshlrev_b32 v251, 4, v250\n\t" \
    "s_add_u32 s85, %[sof], %[ofc]\n\t" \
    "v_mfma_f32_16x16x32_bf16 v[204:207], v[92:95], v[148:151], v[204:207]\n\t" \
    "v_add_u32 v240, s85, v251\n\t" \
    "s_add_u32 s87, %[sof], %[ofp]\n\t" \
    "v_mfma_f32_16x16x32_bf16 v[212:215], v[92:95], v[152:155], v[212:215]\n\t" \
    "buffer_load_dwordx2 v[234:235], v238, %[rsw], %[sg] offen offset:128 sc1\n\t" \
    "v_mfma_f32_16x16x32_bf16 v[220:223], v[92:95], v[156:159], v[220:223]\n\t" \
    "v_add_u32 v241, 0x1000, v240\n\t" \
    "s_add_u32 s88, %[sof], %[ogp]\n\t" \
    "v_mfma_f32_16x16x32_bf16 v[192:195], v[96:99], v[160:163], v[192:195]\n\t" \
    "s_mov_b32 s86, 0x10000\n\t" \
    "v_add_u32 v242, 0x2000, v240\n\t" \
    "v_add_u32 v243, 0x3000, v240\n\t" \
    "s_waitcnt lgkmcnt(10)\n\t" \
    "v_mfma_f32_16x16x32_bf16 v[196:199], v[100:103], v[160:163], v[196:199]\n\t" \
    "v_bfe_u32 v244, v251, 1, 8\n\t" \
    "v_add_u32 v253, %[kac], v251\n\t" \
    "s_waitcnt lgkmcnt(6)\n\t" \
    "v_mfma_f32_16x16x32_bf16 v[192:195], v[104:107], v[176:179], v[192:195]\n\t" \
    "v_lshrrev_b32 v112, 2, v250\n\t" \
    "v_bfe_u32 v113, v250, 4, 1\n\t" \
    "s_waitcnt lgkmcnt(2)\n\t" \
    "v_mfma_f32_16x16x32_bf16 v[196:199], v[108:111], v[176:179], v[196:199]\n\t" \
    "v_lshlrev_b32 v245, 7, v112\n\t" \
    "v_bitop3_b32 v112, v112, v113, 3 bitop3:0x28\n\t" \
    "v_mfma_f32_16x16x32_bf16 v[200:203], v[96:99], v[164:167], v[200:203]\n\t" \
    "v_lshl_or_b32 v245, v112, 5, v245\n\t" \
    "v_and_b32 v112, 24, v238\n\t" \
    "v_mfma_f32_16x16x32_bf16 v[204:207], v[100:103], v[164:167], v[204:207]\n\t" \
    "v_or3_b32 v245, v245, v112, s86\n\t" \
    "v_lshlrev_b32 v247, 8, v238\n\t" \
    "v_mfma_f32_16x16x32_bf16 v[200:203], v[104:107], v[180:183], v[200:203]\n\t" \
    "v_lshl_or_b32 v247, v113, 5, v247\n\t" \
    "v_lshrrev_b32 v112, 5, v250\n\t" \
    "v_mfma_f32_16x16x32_bf16 v[204:207], v[108:111], v[180:183], v[204:207]\n\t" \
    "v_lshl_or_b32 v247, v112, 4, v247\n\t" \
    "v_and_b32 v252, 48, v247\n\t" \
    "v_mfma_f32_16x16x32_bf16 v[208:211], v[96:99], v[168:171], v[208:211]\n\t" \
    "v_lshl_add_u32 v252, v252, 2, %[zac]\n\t" \
    "v_lshlrev_b32 v246, 4, v238\n\t" \
    "v_mfma_f32_16x16x32_bf16 v[212:215], v[100:103], v[168:171], v[212:215]\n\t" \
    "v_cvt_pk_bf16_f32 v64, v192, v193\n\t" \
    "v_cvt_pk_bf16_f32 v65, v194, v195\n\t" \
    "v_mfma_f32_16x16x32_bf16 v[208:211], v[104:107], v[184:187], v[208:211]\n\t" \
    "v_cvt_pk_bf16_f32 v66, v196, v197\n\t" \
    "v_cvt_pk_bf16_f32 v67, v198, v199\n\t" \
    "v_mfma_f32_16x16x32_bf16 v[212:215], v[108:111], v[184:187], v[212:215]\n\t" \
    "v_lshl_or_b32 v246, v113, 3, v246\n\t" \
    "v_lshrrev_b32 v113, 4, v238\n\t" \
    "v_mfma_f32_16x16x32_bf16 v[216:219], v[96:99], v[172:175], v[216:219]\n\t" \
    "v_cvt_pk_bf16_f32 v68, v200, v201\n\t" \
    "v_cvt_pk_bf16_f32 v69, v202, v203\n\t" \
    "v_mfma_f32_16x16x32_bf16 v[220:223], v[100:103], v[172:175], v[220:223]\n\t" \
    "v_cvt_pk_bf16_f32 v70, v204, v205\n\t" \
    "v_cvt_pk_bf16_f32 v71, v206, v207\n\t" \
    "s_waitcnt lgkmcnt(1)\n\t" \
    "v_mfma_f32_16x16x32_bf16 v[216:219], v[104:107], v[188:191], v[216:219]\n\t" \
    "v_xor_b32 v112, v112, v113\n\t" \
    "v_lshl_or_b32 v246, v112, 4, v246\n\t" \
    "v_mfma_f32_16x16x32_bf16 v[220:223], v[108:111], v[188:191], v[220:223]\n\t" \
    "v_add_u32 v246, %[xec], v246\n\t" \
    "v_mfma_f32_16x16x32_bf16 v[0:3], v[224:227], v[64:67], 0\n\t" \
    "v_cvt_pk_bf16_f32 v72, v208, v209\n\t" \
    "v_cvt_pk_bf16_f32 v73, v210, v211\n\t" \
    "v_mfma_f32_16x16x32_bf16 v[4:7], v[228:231], v[64:67], 0\n\t" \
    "v_cvt_pk_bf16_f32 v74, v212, v213\n\t" \
    "v_cvt_pk_bf16_f32 v75, v214, v215\n\t" \
    "v_mfma_f32_16x16x32_bf16 v[8:11], v[224:227], v[68:71], 0\n\t" \
    "v_mov_b32 v120, 0x358637bd\n\t" \
    "v_mov_b32 v121, 0x358637bd\n\t" \
    "v_mfma_f32_16x16x32_bf16 v[12:15], v[228:231], v[68:71], 0\n\t" \
    "v_mov_b32 v122, 0x358637bd\n\t" \
    "v_mov_b32 v123, 0x358637bd\n\t" \
    "v_mfma_f32_16x16x32_bf16 v[16:19], v[224:227], v[72:75], 0\n\t" \
    "v_cvt_pk_bf16_f32 v76, v216, v217\n\t" \
    "v_cvt_pk_bf16_f32 v77, v218, v219\n\t" \
    "v_mfma_f32_16x16x32_bf16 v[20:23], v[228:231], v[72:75], 0\n\t" \
    "v_cvt_pk_bf16_f32 v78, v220, v221\n\t" \
    "v_cvt_pk_bf16_f32 v79, v222, v223\n\t" \
    "s_nop 1\n\t" \
    "v_mfma_f32_16x16x32_bf16 v[24:27], v[224:227], v[76:79], 0\n\t" \
    "v_mfma_f32_16x16x32_bf16 v[28:31], v[228:231], v[76:79], 0\n\t" \
    "v_max3_f32 v64, v0, v1, v2\n\t" \
    "v_max3_f32 v66, -v0, -v1, -v2\n\t" \
    "v_max3_f32 v68, v8, v9, v10\n\t" \
    "v_max3_f32 v64, v64, v3, v4\n\t" \
    "v_max3_f32 v66, v66, -v3, -v4\n\t" \
    "v_max3_f32 v68, v68, v11, v12\n\t" \
    "v_max3_f32 v64, v64, v5, v6\n\t" \
    "v_max3_f32 v66, v66, -v5, -v6\n\t" \
    "v_max3_f32 v68, v68, v13, v14\n\t" \
    "v_max_f32_e64 v64, v64, v7\n\t" \
    "v_max_f32_e64 v66, v66, -v7\n\t" \
    "v_max_f32_e64 v68, v68, v15\n\t" \
    "v_max3_f32 v70, -v8, -v9, -v10\n\t" \
    "v_permlane32_swap_b32 v64, v66\n\t" \
    "v_max3_f32 v72, v16, v17, v18\n\t" \
    "v_max3_f32 v70, v70, -v11, -v12\n\t" \
    "v_max_f32 v64, v64, v66\n\t" \
    "v_max3_f32 v72, v72, v19, v20\n\t" \
    "v_max3_f32 v70, v70, -v13, -v14\n\t" \
    "v_max3_f32 v74, -v16, -v17, -v18\n\t" \
    "v_max3_f32 v72, v72, v21, v22\n\t" \
    "v_max_f32_e64 v70, v70, -v15\n\t" \
    "v_max3_f32 v74, v74, -v19, -v20\n\t" \
    "v_max_f32_e64 v72, v72, v23\n\t" \
    "v_permlane32_swap_b32 v68, v70\n\t" \
    "v_max3_f32 v74, v74, -v21, -v22\n\t" \
    "v_max3_f32 v76, v24, v25, v26\n\t" \
    "v_max_f32 v68, v68, v70\n\t" \
    "v_max_f32_e64 v74, v74, -v23\n\t" \
    "v_max3_f32 v76, v76, v27, v28\n\t" \
    "v_permlane16_swap_b32 v64, v68\n\t" \
    "v_permlane32_swap_b32 v72, v74\n\t" \
    "v_max3_f32 v76, v76, v29, v30\n\t" \
    "v_max_f32 v66, v64, v68\n\t" \
    "v_max_f32 v72, v72, v74\n\t" \
    "v_max_f32_e64 v76, v76, v31\n\t" \
    ".Lpad_ab2:\n\t" \
    "s_cbranch_execz .Lpad_ret\n\t" \
    "v_mul_f32 v64, 0xbfb8aa3b, v66\n\t" \
    "v_mul_f32 v68, 0xbfb8aa3b, v66\n\t" \
    "v_max3_f32 v78, -v24, -v25, -v26\n\t" \
    "v_max3_f32 v78, v78, -v27, -v28\n\t" \
    "v_permlane16_swap_b32 v64, v68\n\t" \
    "v_mov_b32 v66, v64\n\t" \
    "v_max3_f32 v78, v78, -v29, -v30\n\t" \
    "v_mov_b32 v70, v68\n\t" \
    "v_permlane32_swap_b32 v64, v66\n\t" \
    "v_max_f32_e64 v78, v78, -v31\n\t" \
    "v_permlane32_swap_b32 v68, v70\n\t" \
    "v_pk_fma_f32 v[32:33], v[0:1], s[90:91], v[64:65] op_sel_hi:[1,1,0]\n\t" \
    "v_permlane32_swap_b32 v76, v78\n\t" \
    "v_pk_fma_f32 v[0:1], v[0:1], s[90:91], v[66:67] op_sel_hi:[1,1,0] neg_lo:[1,0,0] neg_hi:[1,0,0]\n\t" \
    "v_pk_fma_f32 v[34:35], v[2:3], s[90:91], v[64:65] op_sel_hi:[1,1,0]\n\t" \
    "v_max_f32 v76, v76, v78\n\t" \
    "v_pk_fma_f32 v[2:3], v[2:3], s[90:91], v[66:67] op_sel_hi:[1,1,0] neg_lo:[1,0,0] neg_hi:[1,0,0]\n\t" \
    "v_pk_fma_f32 v[36:37], v[4:5], s[90:91], v[64:65] op_sel_hi:[1,1,0]\n\t" \
    "v_permlane16_swap_b32 v72, v76\n\t" \
    "v_pk_fma_f32 v[4:5], v[4:5], s[90:91], v[66:67] op_sel_hi:[1,1,0] neg_lo:[1,0,0] neg_hi:[1,0,0]\n\t" \
    "v_pk_fma_f32 v[38:39], v[6:7], s[90:91], v[64:65] op_sel_hi:[1,1,0]\n\t" \
    "v_max_f32 v74, v72, v76\n\t" \
    "v_pk_fma_f32 v[6:7], v[6:7], s[90:91], v[66:67] op_sel_hi:[1,1,0] neg_lo:[1,0,0] neg_hi:[1,0,0]\n\t" \
    "v_exp_f32 v32, v32\n\t" \
    "v_mul_f32 v72, 0xbfb8aa3b, v74\n\t" \
    "v_mul_f32 v76, 0xbfb8aa3b, v74\n\t" \
    "v_exp_f32 v0, v0\n\t" \
    "v_exp_f32 v33, v33\n\t" \
    "v_permlane16_swap_b32 v72, v76\n\t" \
    "v_exp_f32 v1, v1\n\t" \
    "v_exp_f32 v34, v34\n\t" \
    "v_mov_b32 v74, v72\n\t" \
    "v_mov_b32 v78, v76\n\t" \
    "v_exp_f32 v2, v2\n\t" \
    "v_permlane32_swap_b32 v72, v74\n\t" \
    "v_permlane32_swap_b32 v76, v78\n\t" \
    "v_exp_f32 v35, v35\n\t" \
    "v_exp_f32 v3, v3\n\t" \
    "v_exp_f32 v36, v36\n\t" \
    "v_pk_add_f32 v[64:65], v[32:33], v[34:35]\n\t" \
    "v_pk_add_f32 v[66:67], v[0:1], v[2:3]\n\t" \
    "v_exp_f32 v4, v4\n\t" \
    "v_exp_f32 v37, v37\n\t" \
    "v_exp_f32 v5, v5\n\t" \
    "v_exp_f32 v38, v38\n\t" \
    "v_pk_add_f32 v[64:65], v[64:65], v[36:37]\n\t" \
    "v_pk_add_f32 v[66:67], v[66:67], v[4:5]\n\t" \
    "v_exp_f32 v6, v6\n\t" \
    "v_exp_f32 v39, v39\n\t" \
    "v_exp_f32 v7, v7\n\t" \
    "v_pk_fma_f32 v[40:41], v[8:9], s[90:91], v[68:69] op_sel_hi:[1,1,0]\n\t" \
    "v_pk_add_f32 v[64:65], v[64:65], v[38:39]\n\t" \
    "v_pk_add_f32 v[66:67], v[66:67], v[6:7]\n\t" \
    "v_pk_fma_f32 v[8:9], v[8:9], s[90:91], v[70:71] op_sel_hi:[1,1,0] neg_lo:[1,0,0] neg_hi:[1,0,0]\n\t" \
    "v_add_f32 v64, v64, v65\n\t" \
    "v_add_f32 v66, v66, v67\n\t" \
    "v_pk_fma_f32 v[42:43], v[10:11], s[90:91], v[68:69] op_sel_hi:[1,1,0]\n\t" \
    "v_pk_fma_f32 v[10:11], v[10:11], s[90:91], v[70:71] op_sel_hi:[1,1,0] neg_lo:[1,0,0] neg_hi:[1,0,0]\n\t" \
    "v_permlane32_swap_b32 v64, v66\n\t" \
    "v_pk_fma_f32 v[44:45], v[12:13], s[90:91], v[68:69] op_sel_hi:[1,1,0]\n\t" \
    "v_pk_fma_f32 v[12:13], v[12:13], s[90:91], v[70:71] op_sel_hi:[1,1,0] neg_lo:[1,0,0] neg_hi:[1,0,0]\n\t" \
    "v_add_f32 v64, v64, v66\n\t" \
    "v_pk_fma_f32 v[46:47], v[14:15], s[90:91], v[68:69] op_sel_hi:[1,1,0]\n\t" \
    "v_pk_fma_f32 v[14:15], v[14:15], s[90:91], v[70:71] op_sel_hi:[1,1,0] neg_lo:[1,0,0] neg_hi:[1,0,0]\n\t" \
    "v_exp_f32 v40, v40\n\t" \
    "v_exp_f32 v8, v8\n\t" \
    "v_exp_f32 v41, v41\n\t" \
    "v_exp_f32 v9, v9\n\t" \
    "v_exp_f32 v42, v42\n\t" \
    "v_exp_f32 v10, v10\n\t" \
    "v_exp_f32 v43, v43\n\t" \
    "v_exp_f32 v11, v11\n\t" \
    "v_exp_f32 v44, v44\n\t" \
    "v_pk_add_f32 v[68:69], v[40:41], v[42:43]\n\t" \
    "v_pk_add_f32 v[70:71], v[8:9], v[10:11]\n\t" \
    "v_exp_f32 v12, v12\n\t" \
    "v_exp_f32 v45, v45\n\t" \
    "v_exp_f32 v13, v13\n\t" \
    "v_exp_f32 v46, v46\n\t" \
    "v_pk_add_f32 v[68:69], v[68:69], v[44:45]\n\t" \
    "v_pk_add_f32 v[70:71], v[70:71], v[12:13]\n\t" \
    "v_exp_f32 v14, v14\n\t" \
    "v_exp_f32 v47, v47\n\t" \
    "v_exp_f32 v15, v15\n\t" \
    "v_pk_fma_f32 v[48:49], v[16:17], s[90:91], v[72:73] op_sel_hi:[1,1,0]\n\t" \
    "v_pk_add_f32 v[68:69], v[68:69], v[46:47]\n\t" \
    "v_pk_add_f32 v[70:71], v[70:71], v[14:15]\n\t" \
    "v_pk_fma_f32 v[16:17], v[16:17], s[90:91], v[74:75] op_sel_hi:[1,1,0] neg_lo:[1,0,0] neg_hi:[1,0,0]\n\t" \
    "v_add_f32 v68, v68, v69\n\t" \
    "v_add_f32 v70, v70, v71\n\t" \
    "v_pk_fma_f32 v[50:51], v[18:19], s[90:91], v[72:73] op_sel_hi:[1,1,0]\n\t" \
    "v_pk_fma_f32 v[18:19], v[18:19], s[90:91], v[74:75] op_sel_hi:[1,1,0] neg_lo:[1,0,0] neg_hi:[1,0,0]\n\t" \
    "v_permlane32_swap_b32 v68, v70\n\t" \
    "v_pk_fma_f32 v[52:53], v[20:21], s[90:91], v[72:73] op_sel_hi:[1,1,0]\n\t" \
    "v_pk_fma_f32 v[20:21], v[20:21], s[90:91], v[74:75] op_sel_hi:[1,1,0] neg_lo:[1,0,0] neg_hi:[1,0,0]\n\t" \
    "v_add_f32 v68, v68, v70\n\t" \
    "v_pk_fma_f32 v[54:55], v[22:23], s[90:91], v[72:73] op_sel_hi:[1,1,0]\n\t" \
    "v_pk_fma_f32 v[22:23], v[22:23], s[90:91], v[74:75] op_sel_hi:[1,1,0] neg_lo:[1,0,0] neg_hi:[1,0,0]\n\t" \
    "v_permlane16_swap_b32 v64, v68\n\t" \
    "v_exp_f32 v48, v48\n\t" \
    "v_exp_f32 v16, v16\n\t" \
    "v_add_f32 v66, v64, v68\n\t" \
    "v_exp_f32 v49, v49\n\t" \
    "v_exp_f32 v17, v17\n\t" \
    "v_rcp_f32 v64, v66\n\t" \
    "v_exp_f32 v50, v50\n\t" \
    "v_exp_f32 v18, v18\n\t" \
    "v_mov_b32 v68, v64\n\t" \
    "v_exp_f32 v51, v51\n\t" \
    "v_exp_f32 v19, v19\n\t" \
    "v_permlane16_swap_b32 v64, v68\n\t" \
    "v_pk_add_f32 v[72:73], v[48:49], v[50:51]\n\t" \
    "v_pk_add_f32 v[74:75], v[16:17], v[18:19]\n\t" \
    "v_mov_b32 v66, v64\n\t" \
    "v_mov_b32 v70, v68\n\t" \
    "v_exp_f32 v52, v52\n\t" \
    "v_permlane32_swap_b32 v64, v66\n\t" \
    "v_permlane32_swap_b32 v68, v70\n\t" \
    "v_exp_f32 v20, v20\n\t" \
    "v_exp_f32 v53, v53\n\t" \
    "v_exp_f32 v21, v21\n\t" \
    "v_exp_f32 v54, v54\n\t" \
    "v_pk_add_f32 v[72:73], v[72:73], v[52:53]\n\t" \
    "v_pk_add_f32 v[74:75], v[74:75], v[20:21]\n\t" \
    "v_exp_f32 v22, v22\n\t" \
    "v_exp_f32 v55, v55\n\t" \
    "v_exp_f32 v23, v23\n\t" \
    "v_pk_fma_f32 v[56:57], v[24:25], s[90:91], v[76:77] op_sel_hi:[1,1,0]\n\t" \
    "v_pk_add_f32 v[72:73], v[72:73], v[54:55]\n\t" \
    "v_pk_add_f32 v[74:75], v[74:75], v[22:23]\n\t" \
    "v_pk_fma_f32 v[24:25], v[24:25], s[90:91], v[78:79] op_sel_hi:[1,1,0] neg_lo:[1,0,0] neg_hi:[1,0,0]\n\t" \
    "v_add_f32 v72, v72, v73\n\t" \
    "v_add_f32 v74, v74, v75\n\t" \
    "v_pk_fma_f32 v[58:59], v[26:27], s[90:91], v[76:77] op_sel_hi:[1,1,0]\n\t" \
    "v_pk_fma_f32 v[26:27], v[26:27], s[90:91], v[78:79] op_sel_hi:[1,1,0] neg_lo:[1,0,0] neg_hi:[1,0,0]\n\t" \
    "v_permlane32_swap_b32 v72, v74\n\t" \
    "v_pk_fma_f32 v[60:61], v[28:29], s[90:91], v[76:77] op_sel_hi:[1,1,0]\n\t" \
    "v_pk_fma_f32 v[28:29], v[28:29], s[90:91], v[78:79] op_sel_hi:[1,1,0] neg_lo:[1,0,0] neg_hi:[1,0,0]\n\t" \
    "v_add_f32 v72, v72, v74\n\t" \
    "v_pk_fma_f32 v[62:63], v[30:31], s[90:91], v[76:77] op_sel_hi:[1,1,0]\n\t" \
    "v_pk_fma_f32 v[30:31], v[30:31], s[90:91], v[78:79] op_sel_hi:[1,1,0] neg_lo:[1,0,0] neg_hi:[1,0,0]\n\t" \
    "v_exp_f32 v56, v56\n\t" \
    "v_exp_f32 v24, v24\n\t" \
    "v_exp_f32 v57, v57\n\t" \
    "v_exp_f32 v25, v25\n\t" \
    "v_exp_f32 v58, v58\n\t" \
    "v_exp_f32 v26, v26\n\t" \
    "v_exp_f32 v59, v59\n\t" \
    "v_exp_f32 v27, v27\n\t" \
    "v_exp_f32 v60, v60\n\t" \
    "v_pk_add_f32 v[76:77], v[56:57], v[58:59]\n\t" \
    "v_pk_add_f32 v[78:79], v[24:25], v[26:27]\n\t" \
    "v_exp_f32 v28, v28\n\t" \
    "v_exp_f32 v61, v61\n\t" \
    "v_exp_f32 v29, v29\n\t" \
    "v_exp_f32 v62, v62\n\t" \
    "v_pk_add_f32 v[76:77], v[76:77], v[60:61]\n\t" \
    "v_pk_add_f32 v[78:79], v[78:79], v[28:29]\n\t" \
    "v_exp_f32 v30, v30\n\t" \
    "v_exp_f32 v63, v63\n\t" \
    "v_exp_f32 v31, v31\n\t" \
    "v_pk_mul_f32 v[0:1], v[0:1], v[66:67] op_sel_hi:[1,0]\n\t" \
    "v_pk_add_f32 v[76:77], v[76:77], v[62:63]\n\t" \
    "v_pk_add_f32 v[78:79], v[78:79], v[30:31]\n\t" \
    "v_pk_mul_f32 v[32:33], v[32:33], v[64:65] op_sel_hi:[1,0]\n\t" \
    "v_add_f32 v76, v76, v77\n\t" \
    "v_add_f32 v78, v78, v79\n\t" \
    "v_pk_mul_f32 v[2:3], v[2:3], v[66:67] op_sel_hi:[1,0]\n\t" \
    "v_pk_mul_f32 v[34:35], v[34:35], v[64:65] op_sel_hi:[1,0]\n\t" \
    "v_permlane32_swap_b32 v76, v78\n\t" \
    "v_pk_mul_f32 v[4:5], v[4:5], v[66:67] op_sel_hi:[1,0]\n\t" \
    "v_pk_mul_f32 v[36:37], v[36:37], v[64:65] op_sel_hi:[1,0]\n\t" \
    "v_add_f32 v76, v76, v78\n\t" \
    "v_pk_mul_f32 v[6:7], v[6:7], v[66:67] op_sel_hi:[1,0]\n\t" \
    "v_pk_mul_f32 v[38:39], v[38:39], v[64:65] op_sel_hi:[1,0]\n\t" \
    "v_permlane16_swap_b32 v72, v76\n\t" \
    "v_cvt_pk_bf16_f32 v7, v6, v7\n\t" \
    "v_cvt_pk_bf16_f32 v6, v4, v5\n\t" \
    "v_add_f32 v74, v72, v76\n\t" \
    "v_cvt_pk_bf16_f32 v5, v2, v3\n\t" \
    "v_cvt_pk_bf16_f32 v4, v0, v1\n\t" \
    "v_rcp_f32 v72, v74\n\t" \
    "v_cvt_pk_bf16_f32 v0, v32, v33\n\t" \
    "v_cvt_pk_bf16_f32 v1, v34, v35\n\t" \
    "v_mov_b32 v76, v72\n\t" \
    "v_cvt_pk_bf16_f32 v2, v36, v37\n\t" \
    "v_cvt_pk_bf16_f32 v3, v38, v39\n\t" \
    "v_permlane16_swap_b32 v72, v76\n\t" \
    "v_pk_mul_f32 v[8:9], v[8:9], v[70:71] op_sel_hi:[1,0]\n\t" \
    "v_pk_mul_f32 v[40:41], v[40:41], v[68:69] op_sel_hi:[1,0]\n\t" \
    "v_mov_b32 v74, v72\n\t" \
    "v_mov_b32 v78, v76\n\t" \
    "v_pk_mul_f32 v[10:11], v[10:11], v[70:71] op_sel_hi:[1,0]\n\t" \
    "v_permlane32_swap_b32 v72, v74\n\t" \
    "v_permlane32_swap_b32 v76, v78\n\t" \
    "v_pk_mul_f32 v[42:43], v[42:43], v[68:69] op_sel_hi:[1,0]\n\t" \
    "v_pk_mul_f32 v[12:13], v[12:13], v[70:71] op_sel_hi:[1,0]\n\t" \
    "v_pk_mul_f32 v[44:45], v[44:45], v[68:69] op_sel_hi:[1,0]\n\t" \
    "v_pk_mul_f32 v[14:15], v[14:15], v[70:71] op_sel_hi:[1,0]\n\t" \
    "v_pk_mul_f32 v[46:47], v[46:47], v[68:69] op_sel_hi:[1,0]\n\t" \
    "v_pk_mul_f32 v[16:17], v[16:17], v[74:75] op_sel_hi:[1,0]\n\t" \
    "v_cvt_pk_bf16_f32 v15, v14, v15\n\t" \
    "v_cvt_pk_bf16_f32 v14, v12, v13\n\t" \
    "v_cvt_pk_bf16_f32 v13, v10, v11\n\t" \
    "v_cvt_pk_bf16_f32 v12, v8, v9\n\t" \
    "v_cvt_pk_bf16_f32 v8, v40, v41\n\t" \
    "v_cvt_pk_bf16_f32 v9, v42, v43\n\t" \
    "v_cvt_pk_bf16_f32 v10, v44, v45\n\t" \
    "v_cvt_pk_bf16_f32 v11, v46, v47\n\t" \
    "v_pk_mul_f32 v[48:49], v[48:49], v[72:73] op_sel_hi:[1,0]\n\t" \
    "v_pk_mul_f32 v[18:19], v[18:19], v[74:75] op_sel_hi:[1,0]\n\t" \
    "v_pk_mul_f32 v[50:51], v[50:51], v[72:73] op_sel_hi:[1,0]\n\t" \
    "v_pk_mul_f32 v[20:21], v[20:21], v[74:75] op_sel_hi:[1,0]\n\t" \
    "v_pk_mul_f32 v[52:53], v[52:53], v[72:73] op_sel_hi:[1,0]\n\t" \
    "v_pk_mul_f32 v[22:23], v[22:23], v[74:75] op_sel_hi:[1,0]\n\t" \
    "v_pk_mul_f32 v[54:55], v[54:55], v[72:73] op_sel_hi:[1,0]\n\t" \
    "ds_read_b32 v237, v249 offset:8\n\t" \
    "v_pk_mul_f32 v[24:25], v[24:25], v[78:79] op_sel_hi:[1,0]\n\t" \
    "v_cvt_pk_bf16_f32 v23, v22, v23\n\t" \
    "v_cvt_pk_bf16_f32 v22, v20, v21\n\t" \
    "v_cvt_pk_bf16_f32 v21, v18, v19\n\t" \
    "v_cvt_pk_bf16_f32 v20, v16, v17\n\t" \
    "v_cvt_pk_bf16_f32 v16, v48, v49\n\t" \
    "v_cvt_pk_bf16_f32 v17, v50, v51\n\t" \
    "v_cvt_pk_bf16_f32 v18, v52, v53\n\t" \
    "v_cvt_pk_bf16_f32 v19, v54, v55\n\t" \
    "v_pk_mul_f32 v[56:57], v[56:57], v[76:77] op_sel_hi:[1,0]\n\t" \
    "v_pk_mul_f32 v[26:27], v[26:27], v[78:79] op_sel_hi:[1,0]\n\t" \
    "v_pk_mul_f32 v[58:59], v[58:59], v[76:77] op_sel_hi:[1,0]\n\t" \
    "v_pk_mul_f32 v[28:29], v[28:29], v[78:79] op_sel_hi:[1,0]\n\t" \
    "v_pk_mul_f32 v[60:61], v[60:61], v[76:77] op_sel_hi:[1,0]\n\t" \
    "v_pk_mul_f32 v[30:31], v[30:31], v[78:79] op_sel_hi:[1,0]\n\t" \
    "v_pk_mul_f32 v[62:63], v[62:63], v[76:77] op_sel_hi:[1,0]\n\t" \
    "v_cvt_pk_bf16_f32 v31, v30, v31\n\t" \
    "v_cvt_pk_bf16_f32 v30, v28, v29\n\t" \
    "v_cvt_pk_bf16_f32 v29, v26, v27\n\t" \
    "v_cvt_pk_bf16_f32 v28, v24, v25\n\t" \
    "v_cvt_pk_bf16_f32 v24, v56, v57\n\t" \
    "v_cvt_pk_bf16_f32 v25, v58, v59\n\t" \
    "v_cvt_pk_bf16_f32 v26, v60, v61\n\t" \
    "v_cvt_pk_bf16_f32 v27, v62, v63\n\t" \
    "s_setprio 2\n\t" \
    "s_waitcnt lgkmcnt(0)\n\t" \
    "v_readfirstlane_b32 s85, v237\n\t" \
    "s_add_u32 s86, s87, 0x10000\n\t" \
    "s_cmp_lt_u32 s85, 3\n\t" \
    "s_cbranch_scc1 .Lab_s7\n\t" \
    ".Lab_s7r:\n\t" \
    "s_mov_b32 s89, 0x400000\n\t" \
    "s_mov_b32 s82, 4\n\t" \
    ".Lab_k:\n\t" \
    "ds_read_b32 v237, v249 offset:8\n\t" \
    "ds_read_b128 v[32:35], v252\n\t" \
    "ds_read_b128 v[36:39], v252 offset:256\n\t" \
    "ds_read_b128 v[40:43], v253\n\t" \
    "ds_read_b128 v[44:47], v253 offset:1024\n\t" \
    "ds_read_b128 v[48:51], v253 offset:2048\n\t" \
    "ds_read_b128 v[52:55], v253 offset:3072\n\t" \
    "s_sub_u32 s89, s89, 1\n\t" \
    "s_cbranch_scc1 .Lab_kr\n\t" \
    "s_waitcnt lgkmcnt(6)\n\t" \
    "v_readfirstlane_b32 s85, v237\n\t" \
    "s_cmp_lt_u32 s85, s82\n\t" \
    "s_cbranch_scc1 .Lab_k\n\t" \
    ".Lab_kr:\n\t" \
    "s_waitcnt lgkmcnt(5)\n\t" \
    "v_mfma_f32_16x16x32_bf16 v[56:59], v[32:35], v[0:3], v[120:123]\n\t" \
    "ds_read_b32 v126, v249 offset:296\n\t" \
    "v_mfma_f32_16x16x32_bf16 v[60:63], v[32:35], v[8:11], v[120:123]\n\t" \
    "ds_read_b32 v127, v249 offset:308\n\t" \
    "v_mfma_f32_16x16x32_bf16 v[64:67], v[32:35], v[16:19], v[120:123]\n\t" \
    "v_cmp_eq_u32 vcc, 0, v244\n\t" \
    "v_mfma_f32_16x16x32_bf16 v[68:71], v[32:35], v[24:27], v[120:123]\n\t" \
    "v_lshrrev_b32 v237, 1, v251\n\t" \
    "v_bfrev_b32 v238, 1\n\t" \
    "s_waitcnt lgkmcnt(6)\n\t" \
    "v_mfma_f32_16x16x32_bf16 v[56:59], v[36:39], v[4:7], v[56:59]\n\t" \
    "v_mfma_f32_16x16x32_bf16 v[60:63], v[36:39], v[12:15], v[60:63]\n\t" \
    "v_cndmask_b32 v254, v238, v237, vcc\n\t" \
    "v_mfma_f32_16x16x32_bf16 v[64:67], v[36:39], v[20:23], v[64:67]\n\t" \
    "s_or_b32 s82, s85, 1\n\t" \
    "v_mfma_f32_16x16x32_bf16 v[68:71], v[36:39], v[28:31], v[68:71]\n\t" \
    "s_waitcnt lgkmcnt(5)\n\t" \
    "v_mfma_f32_16x16x32_bf16 v[72:75], v[40:43], v[0:3], 0\n\t" \
    "s_waitcnt lgkmcnt(4)\n\t" \
    "v_mfma_f32_16x16x32_bf16 v[76:79], v[44:47], v[0:3], 0\n\t" \
    "s_waitcnt lgkmcnt(3)\n\t" \
    "v_mfma_f32_16x16x32_bf16 v[72:75], v[48:51], v[4:7], v[72:75]\n\t" \
    "s_waitcnt lgkmcnt(2)\n\t" \
    "v_mfma_f32_16x16x32_bf16 v[76:79], v[52:55], v[4:7], v[76:79]\n\t" \
    "v_mfma_f32_16x16x32_bf16 v[80:83], v[40:43], v[8:11], 0\n\t" \
    "v_rcp_f32 v56, v56\n\t" \
    "v_mfma_f32_16x16x32_bf16 v[84:87], v[44:47], v[8:11], 0\n\t" \
    "v_rcp_f32 v60, v60\n\t" \
    "v_mfma_f32_16x16x32_bf16 v[80:83], v[48:51], v[12:15], v[80:83]\n\t" \
    "v_rcp_f32 v64, v64\n\t" \
    "v_mfma_f32_16x16x32_bf16 v[84:87], v[52:55], v[12:15], v[84:87]\n\t" \
    "v_rcp_f32 v68, v68\n\t" \
    "v_mfma_f32_16x16x32_bf16 v[88:91], v[40:43], v[16:19], 0\n\t" \
    "v_mfma_f32_16x16x32_bf16 v[92:95], v[44:47], v[16:19], 0\n\t" \
    "v_mul_f32 v72, v72, v56\n\t" \
    "v_mul_f32 v73, v73, v56\n\t" \
    "v_mfma_f32_16x16x32_bf16 v[88:91], v[48:51], v[20:23], v[88:91]\n\t" \
    "v_mul_f32 v74, v74, v56\n\t" \
    "v_mul_f32 v75, v75, v56\n\t" \
    "v_mfma_f32_16x16x32_bf16 v[92:95], v[52:55], v[20:23], v[92:95]\n\t" \
    "v_mul_f32 v76, v76, v56\n\t" \
    "v_mul_f32 v77, v77, v56\n\t" \
    "v_mfma_f32_16x16x32_bf16 v[96:99], v[40:43], v[24:27], 0\n\t" \
    "v_mul_f32 v78, v78, v56\n\t" \
    "v_mul_f32 v79, v79, v56\n\t" \
    "v_mfma_f32_16x16x32_bf16 v[100:103], v[44:47], v[24:27], 0\n\t" \
    "v_cvt_pk_bf16_f32 v104, v72, v73\n\t" \
    "v_cvt_pk_bf16_f32 v105, v74, v75\n\t" \
    "v_mfma_f32_16x16x32_bf16 v[96:99], v[48:51], v[28:31], v[96:99]\n\t" \
    "v_cvt_pk_bf16_f32 v106, v76, v77\n\t" \
    "v_cvt_pk_bf16_f32 v107, v78, v79\n\t" \
    "v_mfma_f32_16x16x32_bf16 v[100:103], v[52:55], v[28:31], v[100:103]\n\t" \
    "buffer_store_dwordx4 v[104:107], v251, %[rsb], s87 offen sc1\n\t" \
    "v_pk_mul_f32 v[80:81], v[80:81], v[60:61] op_sel_hi:[1,0]\n\t" \
    "v_pk_mul_f32 v[82:83], v[82:83], v[60:61] op_sel_hi:[1,0]\n\t" \
    "v_pk_mul_f32 v[84:85], v[84:85], v[60:61] op_sel_hi:[1,0]\n\t" \
    "v_pk_mul_f32 v[86:87], v[86:87], v[60:61] op_sel_hi:[1,0]\n\t" \
    "v_cvt_pk_bf16_f32 v108, v80, v81\n\t" \
    "v_cvt_pk_bf16_f32 v109, v82, v83\n\t" \
    "v_cvt_pk_bf16_f32 v110, v84, v85\n\t" \
    "v_cvt_pk_bf16_f32 v111, v86, v87\n\t" \
    "buffer_store_dwordx4 v[108:111], v251, %[rsb], s87 offen offset:1024 sc1\n\t" \
    "v_pk_mul_f32 v[88:89], v[88:89], v[64:65] op_sel_hi:[1,0]\n\t" \
    "v_pk_mul_f32 v[90:91], v[90:91], v[64:65] op_sel_hi:[1,0]\n\t" \
    "v_pk_mul_f32 v[92:93], v[92:93], v[64:65] op_sel_hi:[1,0]\n\t" \
    "v_pk_mul_f32 v[94:95], v[94:95], v[64:65] op_sel_hi:[1,0]\n\t" \
    "v_cvt_pk_bf16_f32 v112, v88, v89\n\t" \
    "v_cvt_pk_bf16_f32 v113, v90, v91\n\t" \
    "v_cvt_pk_bf16_f32 v114, v92, v93\n\t" \
    "v_cvt_pk_bf16_f32 v115, v94, v95\n\t" \
    "buffer_store_dwordx4 v[112:115], v251, %[rsb], s86 offen sc1\n\t" \
    "v_pk_mul_f32 v[96:97], v[96:97], v[68:69] op_sel_hi:[1,0]\n\t" \
    "v_pk_mul_f32 v[98:99], v[98:99], v[68:69] op_sel_hi:[1,0]\n\t" \
    "v_pk_mul_f32 v[100:101], v[100:101], v[68:69] op_sel_hi:[1,0]\n\t" \
    "v_pk_mul_f32 v[102:103], v[102:103], v[68:69] op_sel_hi:[1,0]\n\t" \
    "v_cvt_pk_bf16_f32 v116, v96, v97\n\t" \
    "v_cvt_pk_bf16_f32 v117, v98, v99\n\t" \
    "v_cvt_pk_bf16_f32 v118, v100, v101\n\t" \
    "v_cvt_pk_bf16_f32 v119, v102, v103\n\t" \
    "v_add3_u32 v32, v104, v105, 0\n\t" \
    "v_add3_u32 v32, v106, v107, v32\n\t" \
    "v_add3_u32 v33, v112, v113, 0\n\t" \
    "v_add3_u32 v33, v114, v115, v33\n\t" \
    "v_add3_u32 v32, v108, v109, v32\n\t" \
    "v_add3_u32 v32, v110, v111, v32\n\t" \
    "v_add3_u32 v33, v116, v117, v33\n\t" \
    "v_add3_u32 v33, v118, v119, v33\n\t" \
    "buffer_store_dwordx4 v[116:119], v251, %[rsb], s86 offen offset:1024 sc1\n\t" \
    "ds_read_b32 v36, v249 offset:8\n\t" \
    "v_permlane32_swap_b32 v32, v33\n\t" \
    "v_add_u32 v32, v32, v33\n\t" \
    "v_mov_b32 v33, v32\n\t" \
    "s_waitcnt lgkmcnt(2)\n\t" \
    "v_readfirstlane_b32 %[rid], v126\n\t" \
    "s_waitcnt lgkmcnt(1)\n\t" \
    "v_readfirstlane_b32 %[t3], v127\n\t" \
    "v_permlane16_swap_b32 v32, v33\n\t" \
    "v_add_u32 v32, v32, v33\n\t" \
    "v_mov_b32 v33, v236\n\t" \
    "ds_read_b32 v238, v249 offset:56\n\t" \
    "v_add_u32_dpp v32, v32, v32 row_ror:8 row_mask:0xf bank_mask:0xf bound_ctrl:1\n\t" \
    "v_mov_b32 v237, 12\n\t" \
    "v_and_b32 v34, 15, v250\n\t" \
    "v_add_u32_dpp v32, v32, v32 row_ror:4 row_mask:0xf bank_mask:0xf bound_ctrl:1\n\t" \
    "s_waitcnt lgkmcnt(1)\n\t" \
    "v_readfirstlane_b32 s80, v36\n\t" \
    "s_and_b32 s85, %[rid], 15\n\t" \
    "v_add_u32_dpp v32, v32, v32 row_ror:2 row_mask:0xf bank_mask:0xf bound_ctrl:1\n\t" \
    "v_cmp_eq_u32_e64 %[rm], s85, v34\n\t" \
    "s_cmp_lg_u32 s80, s82\n\t" \
    "v_add_u32_dpp v32, v32, v32 row_ror:1 row_mask:0xf bank_mask:0xf bound_ctrl:1\n\t" \
    "s_cbranch_scc1 .Lab_sa\n\t" \
    ".Lab_sar:\n\t" \
    "buffer_store_dwordx2 v[32:33], v254, %[rsb], s88 offen sc1\n\t" \
    "ds_write_b32 v248, v237\n\t" \
    "v_and_b32 v252, 48, v250\n\t" \
    "s_lshr_b32 s85, %[rid], 5\n\t" \
    "v_add_u32 v252, 161536, v252\n\t" \
    "s_cmp_eq_u32 s85, %[own]\n\t" \
    "s_cselect_b64 %[rm], %[rm], 0\n\t" \
    "s_waitcnt vmcnt(5)\n\t" \
    "v_cmp_ne_u32 vcc, v236, v233\n\t" \
    "v_cmp_ne_u32_e64 s[92:93], v236, v235\n\t" \
    "s_nop 0\n\t" \
    "s_or_b64 vcc, vcc, s[92:93]\n\t" \
    "s_cbranch_scc1 .Lab_s8\n\t" \
    ".Lab_s8r:\n\t" \
    "v_sub_f32 v254, 1.0, v232\n\t" \
    "v_sub_f32 v255, 1.0, v234\n\t" \
    "v_cvt_pk_bf16_f32 v255, v254, v255\n\t" \
    "v_lshlrev_b32 v254, 16, v255\n\t" \
    "v_and_b32 v255, 0xffff0000, v255\n\t" \
    "s_waitcnt lgkmcnt(1)\n\t" \
    "v_readfirstlane_b32 s85, v238\n\t" \
    "s_cmp_lt_u32 s85, 2\n\t" \
    "s_cbranch_scc1 .Lab_s9\n\t" \
    ".Lab_s9r:\n\t" \
    "s_waitcnt lgkmcnt(0)\n\t"
#define PHASE_C_ASM \
    "s_branch .Lpad_c0\n\t" \
    ".Lc_x:\n\t" \
    "s_waitcnt vmcnt(0) lgkmcnt(0)\n\t" \
    "s_branch .Lc_end\n\t" \
    ".Lc_w0:\n\t" \
    "s_mov_b32 s86, 0x400000\n\t" \
    ".Lc_a0:\n\t" \
    "ds_read_b32 v160, v249 offset:56\n\t" \
    "s_waitcnt lgkmcnt(0)\n\t" \
    "v_readfirstlane_b32 s83, v160\n\t" \
    "s_sub_u32 s86, s86, 1\n\t" \
    "s_cbranch_scc1 .Lc_r0\n\t" \
    "s_cmp_lt_u32 s83, s82\n\t" \
    "s_cbranch_scc1 .Lc_a0\n\t" \
    "s_branch .Lc_r0\n\t" \
    ".Lc_w1:\n\t" \
    "s_mov_b32 s86, 0x400000\n\t" \
    ".Lc_a1:\n\t" \
    "ds_read_b32 v160, v249 offset:56\n\t" \
    "s_waitcnt lgkmcnt(0)\n\t" \
    "v_readfirstlane_b32 s83, v160\n\t" \
    "s_sub_u32 s86, s86, 1\n\t" \
    "s_cbranch_scc1 .Lc_r1\n\t" \
    "s_cmp_lt_u32 s83, s82\n\t" \
    "s_cbranch_scc1 .Lc_a1\n\t" \
    "s_branch .Lc_r1\n\t" \
    ".Lc_y:\n\t" \
    "s_mov_b32 s86, 0x400000\n\t" \
    ".Lc_ya:\n\t" \
    "s_waitcnt lgkmcnt(0)\n\t" \
    "v_readfirstlane_b32 s83, v231\n\t" \
    "s_sub_u32 s86, s86, 1\n\t" \
    "s_cbranch_scc1 .Lc_yb\n\t" \
    "s_cmp_lg_u32 s83, 0\n\t" \
    "s_cbranch_scc1 .Lc_yb\n\t" \
    "ds_read_b32 v231, v249 offset:68\n\t" \
    "ds_read_b128 v[96:99], v252\n\t" \
    "ds_read_b128 v[100:103], v252 offset:64\n\t" \
    "ds_read_b128 v[104:107], v252 offset:128\n\t" \
    "ds_read_b128 v[108:111], v252 offset:192\n\t" \
    "s_branch .Lc_ya\n\t" \
    ".Lc_yb:\n\t" \
    "s_mov_b64 exec, %[rm]\n\t" \
    "s_bitcmp1_b32 %[rid], 4\n\t" \
    "s_cbranch_scc1 .Lc_yc\n\t" \
    "v_pk_mul_f32 v[194:195], v[96:97], v[254:255] op_sel_hi:[1,0]\n\t" \
    "v_pk_mul_f32 v[196:197], v[98:99], v[254:255] op_sel_hi:[1,0]\n\t" \
    "v_pk_mul_f32 v[198:199], v[100:101], v[254:255] op_sel_hi:[1,0]\n\t" \
    "v_pk_mul_f32 v[200:201], v[102:103], v[254:255] op_sel_hi:[1,0]\n\t" \
    "v_pk_mul_f32 v[202:203], v[104:105], v[254:255] op_sel_hi:[1,0]\n\t" \
    "v_pk_mul_f32 v[204:205], v[106:107], v[254:255] op_sel_hi:[1,0]\n\t" \
    "v_pk_mul_f32 v[206:207], v[108:109], v[254:255] op_sel_hi:[1,0]\n\t" \
    "v_pk_mul_f32 v[208:209], v[110:111], v[254:255] op_sel_hi:[1,0]\n\t" \
    "s_mov_b64 exec, -1\n\t" \
    "s_branch .Lc_yr\n\t" \
    ".Lc_yc:\n\t" \
    "v_pk_mul_f32 v[210:211], v[96:97], v[254:255] op_sel:[0,1] op_sel_hi:[1,1]\n\t" \
    "v_pk_mul_f32 v[212:213], v[98:99], v[254:255] op_sel:[0,1] op_sel_hi:[1,1]\n\t" \
    "v_pk_mul_f32 v[214:215], v[100:101], v[254:255] op_sel:[0,1] op_sel_hi:[1,1]\n\t" \
    "v_pk_mul_f32 v[216:217], v[102:103], v[254:255] op_sel:[0,1] op_sel_hi:[1,1]\n\t" \
    "v_pk_mul_f32 v[218:219], v[104:105], v[254:255] op_sel:[0,1] op_sel_hi:[1,1]\n\t" \
    "v_pk_mul_f32 v[220:221], v[106:107], v[254:255] op_sel:[0,1] op_sel_hi:[1,1]\n\t" \
    "v_pk_mul_f32 v[222:223], v[108:109], v[254:255] op_sel:[0,1] op_sel_hi:[1,1]\n\t" \
    "v_pk_mul_f32 v[224:225], v[110:111], v[254:255] op_sel:[0,1] op_sel_hi:[1,1]\n\t" \
    "s_mov_b64 exec, -1\n\t" \
    "s_branch .Lc_yr\n\t" \
    ".p2align 12\n\t" \
    ".Lpad_c0:\n\t" \
    "s_cbranch_execz .Lpad_ret\n\t" \
    "s_setprio 2\n\t" \
    "v_xor_b32 v226, %[flip], v245\n\t" \
    "v_xor_b32 v227, 32, v226\n\t" \
    "v_xor_b32 v228, 64, v226\n\t" \
    "v_xor_b32 v229, 96, v226\n\t" \
    "v_and_b32 v231, 0x7f, v244\n\t" \
    "s_lshl_b32 s80, %[grp], 14\n\t" \
    "s_lshl_b32 s83, %[grp], 3\n\t" \
    "s_lshl_b32 s84, 0xffff, s83\n\t" \
    "s_lshl_b32 s85, %[grp], 6\n\t" \
    "s_add_u32 s85, s85, %[sog]\n\t" \
    "s_mov_b32 s81, 256\n\t" \
    "v_xor_b32 v1, 32, v246\n\t" \
    "v_xor_b32 v2, 64, v246\n\t" \
    "v_xor_b32 v3, 96, v246\n\t" \
    ".Lc_t:\n\t" \
    "s_memrealtime s[86:87]\n\t" \
    "s_sub_u32 s81, s81, 1\n\t" \
    "s_cbranch_scc1 .Lc_go\n\t" \
    "s_waitcnt lgkmcnt(0)\n\t" \
    "s_sub_u32 s86, %[t3], s86\n\t" \
    "s_cmp_lt_i32 s86, 5\n\t" \
    "s_cbranch_scc1 .Lc_go\n\t" \
    "s_cmp_lt_i32 s86, 13\n\t" \
    "s_cbranch_scc1 .Lc_t\n\t" \
    "s_sleep 2\n\t" \
    "s_branch .Lc_t\n\t" \
    ".Lc_go:\n\t" \
    "buffer_load_dwordx2 v[160:161], v231, %[rsb], s85 offen sc1\n\t" \
    "buffer_load_dwordx4 v[32:35], v240, %[rsb], s80 offen offset:2048 sc1\n\t" \
    "buffer_load_dwordx4 v[36:39], v240, %[rsb], s80 offen offset:3072 sc1\n\t" \
    "buffer_load_dwordx4 v[40:43], v241, %[rsb], s80 offen sc1\n\t" \
    "buffer_load_dwordx4 v[44:47], v241, %[rsb], s80 offen offset:1024 sc1\n\t" \
    "buffer_load_dwordx4 v[48:51], v241, %[rsb], s80 offen offset:2048 sc1\n\t" \
    "buffer_load_dwordx4 v[52:55], v241, %[rsb], s80 offen offset:3072 sc1\n\t" \
    "buffer_load_dwordx4 v[56:59], v242, %[rsb], s80 offen sc1\n\t" \
    "buffer_load_dwordx4 v[60:63], v242, %[rsb], s80 offen offset:1024 sc1\n\t" \
    "buffer_load_dwordx4 v[64:67], v242, %[rsb], s80 offen offset:2048 sc1\n\t" \
    "buffer_load_dwordx4 v[68:71], v242, %[rsb], s80 offen offset:3072 sc1\n\t" \
    "buffer_load_dwordx4 v[72:75], v243, %[rsb], s80 offen sc1\n\t" \
    "buffer_load_dwordx4 v[76:79], v243, %[rsb], s80 offen offset:1024 sc1\n\t" \
    "buffer_load_dwordx4 v[80:83], v243, %[rsb], s80 offen offset:2048 sc1\n\t" \
    "buffer_load_dwordx4 v[84:87], v243, %[rsb], s80 offen offset:3072 sc1\n\t" \
    "s_add_u32 s80, s80, 0x4000\n\t" \
    "s_and_b32 s80, s80, 0xc000\n\t" \
    "ds_read_b64 v[196:197], v246\n\t" \
    "ds_read_b64 v[200:201], v1\n\t" \
    "ds_read_b64 v[204:205], v2\n\t" \
    "ds_read_b64 v[208:209], v3\n\t" \
    "ds_read_b64 v[212:213], v246 offset:2048\n\t" \
    "ds_read_b64 v[216:217], v1 offset:2048\n\t" \
    "ds_read_b64 v[220:221], v2 offset:2048\n\t" \
    "ds_read_b64 v[224:225], v3 offset:2048\n\t" \
    "ds_read_b64_tr_b16 v[96:97], v226\n\t" \
    "ds_read_b64_tr_b16 v[98:99], v226 offset:2048\n\t" \
    "ds_read_b64_tr_b16 v[100:101], v227\n\t" \
    "ds_read_b64_tr_b16 v[102:103], v227 offset:2048\n\t" \
    "ds_read_b64_tr_b16 v[104:105], v228\n\t" \
    "ds_read_b64_tr_b16 v[106:107], v228 offset:2048\n\t" \
    "ds_read_b64_tr_b16 v[108:109], v229\n\t" \
    "ds_read_b64_tr_b16 v[110:111], v229 offset:2048\n\t" \
    "ds_read_b64_tr_b16 v[112:113], v226 offset:4096\n\t" \
    "ds_read_b64_tr_b16 v[114:115], v226 offset:6144\n\t" \
    "ds_read_b64_tr_b16 v[116:117], v227 offset:4096\n\t" \
    "ds_read_b64_tr_b16 v[118:119], v227 offset:6144\n\t" \
    "ds_read_b64_tr_b16 v[120:121], v228 offset:4096\n\t" \
    "ds_read_b64_tr_b16 v[122:123], v228 offset:6144\n\t" \
    "ds_read_b64_tr_b16 v[124:125], v229 offset:4096\n\t" \
    "ds_read_b64_tr_b16 v[126:127], v229 offset:6144\n\t" \
    "v_mov_b32 v0, 0\n\t" \
    "v_mov_b32 v1, 0\n\t" \
    "v_mov_b32 v2, 0\n\t" \
    "v_mov_b32 v3, 0\n\t" \
    "v_mov_b32 v4, 0\n\t" \
    "v_mov_b32 v5, 0\n\t" \
    "v_mov_b32 v6, 0\n\t" \
    "v_mov_b32 v7, 0\n\t" \
    "v_mov_b32 v8, 0\n\t" \
    "v_mov_b32 v9, 0\n\t" \
    "v_mov_b32 v10, 0\n\t" \
    "v_mov_b32 v11, 0\n\t" \
    "v_mov_b32 v12, 0\n\t" \
    "v_mov_b32 v13, 0\n\t" \
    "v_mov_b32 v14, 0\n\t" \
    "v_mov_b32 v15, 0\n\t" \
    "v_mov_b32 v16, 0\n\t" \
    "v_mov_b32 v17, 0\n\t" \
    "v_mov_b32 v18, 0\n\t" \
    "v_mov_b32 v19, 0\n\t" \
    "v_mov_b32 v20, 0\n\t" \
    "v_mov_b32 v21, 0\n\t" \
    "v_mov_b32 v22, 0\n\t" \
    "v_mov_b32 v23, 0\n\t" \
    "v_mov_b32 v24, 0\n\t" \
    "v_mov_b32 v25, 0\n\t" \
    "v_mov_b32 v26, 0\n\t" \
    "v_mov_b32 v27, 0\n\t" \
    "v_mov_b32 v28, 0\n\t" \
    "v_mov_b32 v29, 0\n\t" \
    "v_mov_b32 v30, 0\n\t" \
    "v_mov_b32 v31, 0\n\t" \
    "s_waitcnt lgkmcnt(0)\n\t" \
    "v_lshlrev_b32 v194, 16, v196\n\t" \
    "v_and_b32 v195, 0xffff0000, v196\n\t" \
    "v_lshlrev_b32 v196, 16, v197\n\t" \
    "v_and_b32 v197, 0xffff0000, v197\n\t" \
    "v_lshlrev_b32 v198, 16, v200\n\t" \
    "v_and_b32 v199, 0xffff0000, v200\n\t" \
    "v_lshlrev_b32 v200, 16, v201\n\t" \
    "v_and_b32 v201, 0xffff0000, v201\n\t" \
    "v_lshlrev_b32 v202, 16, v204\n\t" \
    "v_and_b32 v203, 0xffff0000, v204\n\t" \
    "v_lshlrev_b32 v204, 16, v205\n\t" \
    "v_and_b32 v205, 0xffff0000, v205\n\t" \
    "v_lshlrev_b32 v206, 16, v208\n\t" \
    "v_and_b32 v207, 0xffff0000, v208\n\t" \
    "v_lshlrev_b32 v208, 16, v209\n\t" \
    "v_and_b32 v209, 0xffff0000, v209\n\t" \
    "v_lshlrev_b32 v210, 16, v212\n\t" \
    "v_and_b32 v211, 0xffff0000, v212\n\t" \
    "v_lshlrev_b32 v212, 16, v213\n\t" \
    "v_and_b32 v213, 0xffff0000, v213\n\t" \
    "v_lshlrev_b32 v214, 16, v216\n\t" \
    "v_and_b32 v215, 0xffff0000, v216\n\t" \
    "v_lshlrev_b32 v216, 16, v217\n\t" \
    "v_and_b32 v217, 0xffff0000, v217\n\t" \
    "v_lshlrev_b32 v218, 16, v220\n\t" \
    "v_and_b32 v219, 0xffff0000, v220\n\t" \
    "v_lshlrev_b32 v220, 16, v221\n\t" \
    "v_and_b32 v221, 0xffff0000, v221\n\t" \
    "v_lshlrev_b32 v222, 16, v224\n\t" \
    "v_and_b32 v223, 0xffff0000, v224\n\t" \
    "v_lshlrev_b32 v224, 16, v225\n\t" \
    "v_and_b32 v225, 0xffff0000, v225\n\t" \
    "v_pk_mul_f32 v[194:195], v[194:195], v[254:255] op_sel_hi:[1,0]\n\t" \
    "v_pk_mul_f32 v[196:197], v[196:197], v[254:255] op_sel_hi:[1,0]\n\t" \
    "v_pk_mul_f32 v[198:199], v[198:199], v[254:255] op_sel_hi:[1,0]\n\t" \
    "v_pk_mul_f32 v[200:201], v[200:201], v[254:255] op_sel_hi:[1,0]\n\t" \
    "v_pk_mul_f32 v[202:203], v[202:203], v[254:255] op_sel_hi:[1,0]\n\t" \
    "v_pk_mul_f32 v[204:205], v[204:205], v[254:255] op_sel_hi:[1,0]\n\t" \
    "v_pk_mul_f32 v[206:207], v[206:207], v[254:255] op_sel_hi:[1,0]\n\t" \
    "v_pk_mul_f32 v[208:209], v[208:209], v[254:255] op_sel_hi:[1,0]\n\t" \
    "v_pk_mul_f32 v[210:211], v[210:211], v[254:255] op_sel:[0,1] op_sel_hi:[1,1]\n\t" \
    "v_pk_mul_f32 v[212:213], v[212:213], v[254:255] op_sel:[0,1] op_sel_hi:[1,1]\n\t" \
    "v_pk_mul_f32 v[214:215], v[214:215], v[254:255] op_sel:[0,1] op_sel_hi:[1,1]\n\t" \
    "v_pk_mul_f32 v[216:217], v[216:217], v[254:255] op_sel:[0,1] op_sel_hi:[1,1]\n\t" \
    "v_pk_mul_f32 v[218:219], v[218:219], v[254:255] op_sel:[0,1] op_sel_hi:[1,1]\n\t" \
    "v_pk_mul_f32 v[220:221], v[220:221], v[254:255] op_sel:[0,1] op_sel_hi:[1,1]\n\t" \
    "v_pk_mul_f32 v[222:223], v[222:223], v[254:255] op_sel:[0,1] op_sel_hi:[1,1]\n\t" \
    "v_pk_mul_f32 v[224:225], v[224:225], v[254:255] op_sel:[0,1] op_sel_hi:[1,1]\n\t" \
    "v_mov_b32 v230, 0\n\t" \
    "s_mov_b32 s81, 2\n\t" \
    "s_mov_b32 s82, 1\n\t" \
    "s_mov_b32 %[st], 1\n\t" \
    "s_waitcnt vmcnt(14)\n\t" \
    "v_cmp_ne_u32 vcc, v236, v161\n\t" \
    "s_nop 1\n\t" \
    "s_and_b32 vcc_lo, vcc_lo, s84\n\t" \
    "s_cbranch_scc1 .Lc_x\n\t" \
    ".Lc_l:\n\t" \
    "s_waitcnt vmcnt(12) lgkmcnt(8)\n\t" \
    "v_mfma_f32_16x16x32_bf16 v[0:3], v[96:99], v[32:35], v[0:3]\n\t" \
    "ds_read_b64_tr_b16 v[128:129], v226 offset:8192\n\t" \
    "buffer_load_dwordx4 v[88:91], v240, %[rsb], s80 offen sc1\n\t" \
    "v_mfma_f32_16x16x32_bf16 v[16:19], v[96:99], v[36:39], v[16:19]\n\t" \
    "ds_read_b64_tr_b16 v[130:131], v226 offset:10240\n\t" \
    "v_add3_u32 v230, v32, v33, v230\n\t" \
    "v_mfma_f32_16x16x32_bf16 v[4:7], v[100:103], v[32:35], v[4:7]\n\t" \
    "ds_read_b64_tr_b16 v[132:133], v227 offset:8192\n\t" \
    "buffer_load_dwordx4 v[92:95], v240, %[rsb], s80 offen offset:1024 sc1\n\t" \
    "v_mfma_f32_16x16x32_bf16 v[20:23], v[100:103], v[36:39], v[20:23]\n\t" \
    "ds_read_b64_tr_b16 v[134:135], v227 offset:10240\n\t" \
    "v_add3_u32 v230, v34, v35, v230\n\t" \
    "v_mfma_f32_16x16x32_bf16 v[8:11], v[104:107], v[32:35], v[8:11]\n\t" \
    "ds_read_b64_tr_b16 v[136:137], v228 offset:8192\n\t" \
    "v_add3_u32 v230, v36, v37, v230\n\t" \
    "v_mfma_f32_16x16x32_bf16 v[24:27], v[104:107], v[36:39], v[24:27]\n\t" \
    "ds_read_b64_tr_b16 v[138:139], v228 offset:10240\n\t" \
    "v_add3_u32 v230, v38, v39, v230\n\t" \
    "v_mfma_f32_16x16x32_bf16 v[12:15], v[108:111], v[32:35], v[12:15]\n\t" \
    "ds_read_b64_tr_b16 v[140:141], v229 offset:8192\n\t" \
    "v_mfma_f32_16x16x32_bf16 v[28:31], v[108:111], v[36:39], v[28:31]\n\t" \
    "ds_read_b64_tr_b16 v[142:143], v229 offset:10240\n\t" \
    "s_waitcnt vmcnt(12) lgkmcnt(8)\n\t" \
    "v_mfma_f32_16x16x32_bf16 v[0:3], v[112:115], v[40:43], v[0:3]\n\t" \
    "ds_read_b64_tr_b16 v[144:145], v226 offset:12288\n\t" \
    "buffer_load_dwordx4 v[32:35], v240, %[rsb], s80 offen offset:2048 sc1\n\t" \
    "v_mfma_f32_16x16x32_bf16 v[16:19], v[112:115], v[44:47], v[16:19]\n\t" \
    "ds_read_b64_tr_b16 v[146:147], v226 offset:14336\n\t" \
    "v_add3_u32 v230, v40, v41, v230\n\t" \
    "v_mfma_f32_16x16x32_bf16 v[4:7], v[116:119], v[40:43], v[4:7]\n\t" \
    "ds_read_b64_tr_b16 v[148:149], v227 offset:12288\n\t" \
    "buffer_load_dwordx4 v[36:39], v240, %[rsb], s80 offen offset:3072 sc1\n\t" \
    "v_mfma_f32_16x16x32_bf16 v[20:23], v[116:119], v[44:47], v[20:23]\n\t" \
    "ds_read_b64_tr_b16 v[150:151], v227 offset:14336\n\t" \
    "v_add3_u32 v230, v42, v43, v230\n\t" \
    "v_mfma_f32_16x16x32_bf16 v[8:11], v[120:123], v[40:43], v[8:11]\n\t" \
    "ds_read_b64_tr_b16 v[152:153], v228 offset:12288\n\t" \
    "v_add3_u32 v230, v44, v45, v230\n\t" \
    "v_mfma_f32_16x16x32_bf16 v[24:27], v[120:123], v[44:47], v[24:27]\n\t" \
    "ds_read_b64_tr_b16 v[154:155], v228 offset:14336\n\t" \
    "v_add3_u32 v230, v46, v47, v230\n\t" \
    "v_mfma_f32_16x16x32_bf16 v[12:15], v[124:127], v[40:43], v[12:15]\n\t" \
    "ds_read_b64_tr_b16 v[156:157], v229 offset:12288\n\t" \
    "v_mfma_f32_16x16x32_bf16 v[28:31], v[124:127], v[44:47], v[28:31]\n\t" \
    "ds_read_b64_tr_b16 v[158:159], v229 offset:14336\n\t" \
    "s_waitcnt vmcnt(12) lgkmcnt(8)\n\t" \
    "v_mfma_f32_16x16x32_bf16 v[0:3], v[128:131], v[48:51], v[0:3]\n\t" \
    "ds_read_b64_tr_b16 v[96:97], v226 offset:16384\n\t" \
    "buffer_load_dwordx4 v[40:43], v241, %[rsb], s80 offen sc1\n\t" \
    "v_mfma_f32_16x16x32_bf16 v[16:19], v[128:131], v[52:55], v[16:19]\n\t" \
    "ds_read_b64_tr_b16 v[98:99], v226 offset:18432\n\t" \
    "v_add3_u32 v230, v48, v49, v230\n\t" \
    "v_mfma_f32_16x16x32_bf16 v[4:7], v[132:135], v[48:51], v[4:7]\n\t" \
    "ds_read_b64_tr_b16 v[100:101], v227 offset:16384\n\t" \
    "buffer_load_dwordx4 v[44:47], v241, %[rsb], s80 offen offset:1024 sc1\n\t" \
    "v_mfma_f32_16x16x32_bf16 v[20:23], v[132:135], v[52:55], v[20:23]\n\t" \
    "ds_read_b64_tr_b16 v[102:103], v227 offset:18432\n\t" \
    "v_add3_u32 v230, v50, v51, v230\n\t" \
    "v_mfma_f32_16x16x32_bf16 v[8:11], v[136:139], v[48:51], v[8:11]\n\t" \
    "ds_read_b64_tr_b16 v[104:105], v228 offset:16384\n\t" \
    "v_add3_u32 v230, v52, v53, v230\n\t" \
    "v_mfma_f32_16x16x32_bf16 v[24:27], v[136:139], v[52:55], v[24:27]\n\t" \
    "ds_read_b64_tr_b16 v[106:107], v228 offset:18432\n\t" \
    "v_add3_u32 v230, v54, v55, v230\n\t" \
    "v_mfma_f32_16x16x32_bf16 v[12:15], v[140:143], v[48:51], v[12:15]\n\t" \
    "ds_read_b64_tr_b16 v[108:109], v229 offset:16384\n\t" \
    "v_mfma_f32_16x16x32_bf16 v[28:31], v[140:143], v[52:55], v[28:31]\n\t" \
    "ds_read_b64_tr_b16 v[110:111], v229 offset:18432\n\t" \
    "s_waitcnt vmcnt(12) lgkmcnt(8)\n\t" \
    "v_mfma_f32_16x16x32_bf16 v[0:3], v[144:147], v[56:59], v[0:3]\n\t" \
    "ds_read_b64_tr_b16 v[112:113], v226 offset:20480\n\t" \
    "buffer_load_dwordx4 v[48:51], v241, %[rsb], s80 offen offset:2048 sc1\n\t" \
    "v_mfma_f32_16x16x32_bf16 v[16:19], v[144:147], v[60:63], v[16:19]\n\t" \
    "ds_read_b64_tr_b16 v[114:115], v226 offset:22528\n\t" \
    "v_add3_u32 v230, v56, v57, v230\n\t" \
    "v_mfma_f32_16x16x32_bf16 v[4:7], v[148:151], v[56:59], v[4:7]\n\t" \
    "ds_read_b64_tr_b16 v[116:117], v227 offset:20480\n\t" \
    "buffer_load_dwordx4 v[52:55], v241, %[rsb], s80 offen offset:3072 sc1\n\t" \
    "v_mfma_f32_16x16x32_bf16 v[20:23], v[148:151], v[60:63], v[20:23]\n\t" \
    "ds_read_b64_tr_b16 v[118:119], v227 offset:22528\n\t" \
    "v_add3_u32 v230, v58, v59, v230\n\t" \
    "v_mfma_f32_16x16x32_bf16 v[8:11], v[152:155], v[56:59], v[8:11]\n\t" \
    "ds_read_b64_tr_b16 v[120:121], v228 offset:20480\n\t" \
    "v_add3_u32 v230, v60, v61, v230\n\t" \
    "v_mfma_f32_16x16x32_bf16 v[24:27], v[152:155], v[60:63], v[24:27]\n\t" \
    "ds_read_b64_tr_b16 v[122:123], v228 offset:22528\n\t" \
    "v_add3_u32 v230, v62, v63, v230\n\t" \
    "v_mfma_f32_16x16x32_bf16 v[12:15], v[156:159], v[56:59], v[12:15]\n\t" \
    "ds_read_b64_tr_b16 v[124:125], v229 offset:20480\n\t" \
    "v_mfma_f32_16x16x32_bf16 v[28:31], v[156:159], v[60:63], v[28:31]\n\t" \
    "ds_read_b64_tr_b16 v[126:127], v229 offset:22528\n\t" \
    "s_waitcnt vmcnt(12) lgkmcnt(8)\n\t" \
    "ds_read_b32 v160, v249 offset:56\n\t" \
    "v_mfma_f32_16x16x32_bf16 v[0:3], v[96:99], v[64:67], v[0:3]\n\t" \
    "ds_read_b64_tr_b16 v[128:129], v226 offset:24576\n\t" \
    "buffer_load_dwordx4 v[56:59], v242, %[rsb], s80 offen sc1\n\t" \
    "v_mfma_f32_16x16x32_bf16 v[16:19], v[96:99], v[68:71], v[16:19]\n\t" \
    "ds_read_b64_tr_b16 v[130:131], v226 offset:26624\n\t" \
    "v_add3_u32 v230, v64, v65, v230\n\t" \
    "v_mfma_f32_16x16x32_bf16 v[4:7], v[100:103], v[64:67], v[4:7]\n\t" \
    "ds_read_b64_tr_b16 v[132:133], v227 offset:24576\n\t" \
    "buffer_load_dwordx4 v[60:63], v242, %[rsb], s80 offen offset:1024 sc1\n\t" \
    "v_mfma_f32_16x16x32_bf16 v[20:23], v[100:103], v[68:71], v[20:23]\n\t" \
    "ds_read_b64_tr_b16 v[134:135], v227 offset:26624\n\t" \
    "v_add3_u32 v230, v66, v67, v230\n\t" \
    "v_mfma_f32_16x16x32_bf16 v[8:11], v[104:107], v[64:67], v[8:11]\n\t" \
    "ds_read_b64_tr_b16 v[136:137], v228 offset:24576\n\t" \
    "v_add3_u32 v230, v68, v69, v230\n\t" \
    "v_mfma_f32_16x16x32_bf16 v[24:27], v[104:107], v[68:71], v[24:27]\n\t" \
    "ds_read_b64_tr_b16 v[138:139], v228 offset:26624\n\t" \
    "v_add3_u32 v230, v70, v71, v230\n\t" \
    "v_mfma_f32_16x16x32_bf16 v[12:15], v[108:111], v[64:67], v[12:15]\n\t" \
    "ds_read_b64_tr_b16 v[140:141], v229 offset:24576\n\t" \
    "v_mfma_f32_16x16x32_bf16 v[28:31], v[108:111], v[68:71], v[28:31]\n\t" \
    "ds_read_b64_tr_b16 v[142:143], v229 offset:26624\n\t" \
    "s_waitcnt vmcnt(12) lgkmcnt(8)\n\t" \
    "v_mfma_f32_16x16x32_bf16 v[0:3], v[112:115], v[72:75], v[0:3]\n\t" \
    "ds_read_b64_tr_b16 v[144:145], v226 offset:28672\n\t" \
    "buffer_load_dwordx4 v[64:67], v242, %[rsb], s80 offen offset:2048 sc1\n\t" \
    "v_mfma_f32_16x16x32_bf16 v[16:19], v[112:115], v[76:79], v[16:19]\n\t" \
    "ds_read_b64_tr_b16 v[146:147], v226 offset:30720\n\t" \
    "v_add3_u32 v230, v72, v73, v230\n\t" \
    "v_mfma_f32_16x16x32_bf16 v[4:7], v[116:119], v[72:75], v[4:7]\n\t" \
    "ds_read_b64_tr_b16 v[148:149], v227 offset:28672\n\t" \
    "buffer_load_dwordx4 v[68:71], v242, %[rsb], s80 offen offset:3072 sc1\n\t" \
    "v_add_u32 v226, 0x8000, v226\n\t" \
    "v_and_b32 v226, 0x1ffff, v226\n\t" \
    "v_mfma_f32_16x16x32_bf16 v[20:23], v[116:119], v[76:79], v[20:23]\n\t" \
    "ds_read_b64_tr_b16 v[150:151], v227 offset:30720\n\t" \
    "v_add3_u32 v230, v74, v75, v230\n\t" \
    "v_mfma_f32_16x16x32_bf16 v[8:11], v[120:123], v[72:75], v[8:11]\n\t" \
    "ds_read_b64_tr_b16 v[152:153], v228 offset:28672\n\t" \
    "v_add3_u32 v230, v76, v77, v230\n\t" \
    "v_add_u32 v227, 0x8000, v227\n\t" \
    "v_and_b32 v227, 0x1ffff, v227\n\t" \
    "v_mfma_f32_16x16x32_bf16 v[24:27], v[120:123], v[76:79], v[24:27]\n\t" \
    "ds_read_b64_tr_b16 v[154:155], v228 offset:30720\n\t" \
    "v_add3_u32 v230, v78, v79, v230\n\t" \
    "v_mfma_f32_16x16x32_bf16 v[12:15], v[124:127], v[72:75], v[12:15]\n\t" \
    "v_readfirstlane_b32 s83, v160\n\t" \
    "s_add_u32 s82, s82, 1\n\t" \
    "ds_read_b64_tr_b16 v[156:157], v229 offset:28672\n\t" \
    "v_add_u32 v228, 0x8000, v228\n\t" \
    "v_and_b32 v228, 0x1ffff, v228\n\t" \
    "v_mfma_f32_16x16x32_bf16 v[28:31], v[124:127], v[76:79], v[28:31]\n\t" \
    "s_cmp_lt_u32 s83, s82\n\t" \
    "s_cbranch_scc1 .Lc_w0\n\t" \
    ".Lc_r0:\n\t" \
    "ds_read_b64_tr_b16 v[158:159], v229 offset:30720\n\t" \
    "v_add_u32 v229, 0x8000, v229\n\t" \
    "v_and_b32 v229, 0x1ffff, v229\n\t" \
    "s_waitcnt vmcnt(12) lgkmcnt(8)\n\t" \
    "v_mfma_f32_16x16x32_bf16 v[0:3], v[128:131], v[80:83], v[0:3]\n\t" \
    "ds_read_b64_tr_b16 v[96:97], v226\n\t" \
    "buffer_load_dwordx4 v[72:75], v243, %[rsb], s80 offen sc1\n\t" \
    "v_mfma_f32_16x16x32_bf16 v[16:19], v[128:131], v[84:87], v[16:19]\n\t" \
    "ds_read_b64_tr_b16 v[98:99], v226 offset:2048\n\t" \
    "v_add3_u32 v230, v80, v81, v230\n\t" \
    "v_mfma_f32_16x16x32_bf16 v[4:7], v[132:135], v[80:83], v[4:7]\n\t" \
    "ds_read_b64_tr_b16 v[100:101], v227\n\t" \
    "buffer_load_dwordx4 v[76:79], v243, %[rsb], s80 offen offset:1024 sc1\n\t" \
    "v_mfma_f32_16x16x32_bf16 v[20:23], v[132:135], v[84:87], v[20:23]\n\t" \
    "ds_read_b64_tr_b16 v[102:103], v227 offset:2048\n\t" \
    "v_add3_u32 v230, v82, v83, v230\n\t" \
    "v_mfma_f32_16x16x32_bf16 v[8:11], v[136:139], v[80:83], v[8:11]\n\t" \
    "ds_read_b64_tr_b16 v[104:105], v228\n\t" \
    "v_add3_u32 v230, v84, v85, v230\n\t" \
    "v_mfma_f32_16x16x32_bf16 v[24:27], v[136:139], v[84:87], v[24:27]\n\t" \
    "ds_read_b64_tr_b16 v[106:107], v228 offset:2048\n\t" \
    "v_add3_u32 v230, v86, v87, v230\n\t" \
    "v_mfma_f32_16x16x32_bf16 v[12:15], v[140:143], v[80:83], v[12:15]\n\t" \
    "ds_read_b64_tr_b16 v[108:109], v229\n\t" \
    "v_mfma_f32_16x16x32_bf16 v[28:31], v[140:143], v[84:87], v[28:31]\n\t" \
    "ds_read_b64_tr_b16 v[110:111], v229 offset:2048\n\t" \
    "s_waitcnt vmcnt(12) lgkmcnt(8)\n\t" \
    "v_mfma_f32_16x16x32_bf16 v[0:3], v[144:147], v[88:91], v[0:3]\n\t" \
    "ds_read_b64_tr_b16 v[112:113], v226 offset:4096\n\t" \
    "buffer_load_dwordx4 v[80:83], v243, %[rsb], s80 offen offset:2048 sc1\n\t" \
    "v_mfma_f32_16x16x32_bf16 v[16:19], v[144:147], v[92:95], v[16:19]\n\t" \
    "ds_read_b64_tr_b16 v[114:115], v226 offset:6144\n\t" \
    "v_add3_u32 v230, v88, v89, v230\n\t" \
    "v_mfma_f32_16x16x32_bf16 v[4:7], v[148:151], v[88:91], v[4:7]\n\t" \
    "ds_read_b64_tr_b16 v[116:117], v227 offset:4096\n\t" \
    "buffer_load_dwordx4 v[84:87], v243, %[rsb], s80 offen offset:3072 sc1\n\t" \
    "v_mfma_f32_16x16x32_bf16 v[20:23], v[148:151], v[92:95], v[20:23]\n\t" \
    "ds_read_b64_tr_b16 v[118:119], v227 offset:6144\n\t" \
    "v_add3_u32 v230, v90, v91, v230\n\t" \
    "v_mfma_f32_16x16x32_bf16 v[8:11], v[152:155], v[88:91], v[8:11]\n\t" \
    "ds_read_b64_tr_b16 v[120:121], v228 offset:4096\n\t" \
    "v_add3_u32 v230, v92, v93, v230\n\t" \
    "v_mfma_f32_16x16x32_bf16 v[24:27], v[152:155], v[92:95], v[24:27]\n\t" \
    "ds_read_b64_tr_b16 v[122:123], v228 offset:6144\n\t" \
    "v_add3_u32 v230, v94, v95, v230\n\t" \
    "v_mfma_f32_16x16x32_bf16 v[12:15], v[156:159], v[88:91], v[12:15]\n\t" \
    "ds_read_b64_tr_b16 v[124:125], v229 offset:4096\n\t" \
    "s_add_u32 s80, s80, 0x4000\n\t" \
    "s_and_b32 s80, s80, 0xc000\n\t" \
    "v_mfma_f32_16x16x32_bf16 v[28:31], v[156:159], v[92:95], v[28:31]\n\t" \
    "ds_read_b64_tr_b16 v[126:127], v229 offset:6144\n\t" \
    "s_sub_u32 s81, s81, 1\n\t" \
    "s_cmp_lg_u32 s81, 0\n\t" \
    "s_cbranch_scc1 .Lc_l\n\t" \
    "s_waitcnt vmcnt(12) lgkmcnt(8)\n\t" \
    "v_mfma_f32_16x16x32_bf16 v[162:165], v[96:99], v[32:35], 0\n\t" \
    "ds_read_b64_tr_b16 v[128:129], v226 offset:8192\n\t" \
    "buffer_load_dwordx4 v[88:91], v240, %[rsb], s80 offen sc1\n\t" \
    "v_mfma_f32_16x16x32_bf16 v[178:181], v[96:99], v[36:39], 0\n\t" \
    "ds_read_b64_tr_b16 v[130:131], v226 offset:10240\n\t" \
    "v_add3_u32 v230, v32, v33, v230\n\t" \
    "v_mfma_f32_16x16x32_bf16 v[166:169], v[100:103], v[32:35], 0\n\t" \
    "ds_read_b64_tr_b16 v[132:133], v227 offset:8192\n\t" \
    "buffer_load_dwordx4 v[92:95], v240, %[rsb], s80 offen offset:1024 sc1\n\t" \
    "v_mfma_f32_16x16x32_bf16 v[182:185], v[100:103], v[36:39], 0\n\t" \
    "ds_read_b64_tr_b16 v[134:135], v227 offset:10240\n\t" \
    "v_add3_u32 v230, v34, v35, v230\n\t" \
    "v_mfma_f32_16x16x32_bf16 v[170:173], v[104:107], v[32:35], 0\n\t" \
    "ds_read_b64_tr_b16 v[136:137], v228 offset:8192\n\t" \
    "v_add3_u32 v230, v36, v37, v230\n\t" \
    "v_mfma_f32_16x16x32_bf16 v[186:189], v[104:107], v[36:39], 0\n\t" \
    "ds_read_b64_tr_b16 v[138:139], v228 offset:10240\n\t" \
    "v_add3_u32 v230, v38, v39, v230\n\t" \
    "v_mfma_f32_16x16x32_bf16 v[174:177], v[108:111], v[32:35], 0\n\t" \
    "ds_read_b64_tr_b16 v[140:141], v229 offset:8192\n\t" \
    "v_mfma_f32_16x16x32_bf16 v[190:193], v[108:111], v[36:39], 0\n\t" \
    "ds_read_b64_tr_b16 v[142:143], v229 offset:10240\n\t" \
    "s_waitcnt vmcnt(12) lgkmcnt(8)\n\t" \
    "v_mfma_f32_16x16x32_bf16 v[162:165], v[112:115], v[40:43], v[162:165]\n\t" \
    "ds_read_b64_tr_b16 v[144:145], v226 offset:12288\n\t" \
    "buffer_load_dwordx4 v[32:35], v240, %[rsb], s80 offen offset:2048 sc1\n\t" \
    "v_mfma_f32_16x16x32_bf16 v[178:181], v[112:115], v[44:47], v[178:181]\n\t" \
    "ds_read_b64_tr_b16 v[146:147], v226 offset:14336\n\t" \
    "v_add3_u32 v230, v40, v41, v230\n\t" \
    "v_mfma_f32_16x16x32_bf16 v[166:169], v[116:119], v[40:43], v[166:169]\n\t" \
    "ds_read_b64_tr_b16 v[148:149], v227 offset:12288\n\t" \
    "buffer_load_dwordx4 v[36:39], v240, %[rsb], s80 offen offset:3072 sc1\n\t" \
    "v_mfma_f32_16x16x32_bf16 v[182:185], v[116:119], v[44:47], v[182:185]\n\t" \
    "ds_read_b64_tr_b16 v[150:151], v227 offset:14336\n\t" \
    "v_add3_u32 v230, v42, v43, v230\n\t" \
    "v_mfma_f32_16x16x32_bf16 v[170:173], v[120:123], v[40:43], v[170:173]\n\t" \
    "ds_read_b64_tr_b16 v[152:153], v228 offset:12288\n\t" \
    "v_add3_u32 v230, v44, v45, v230\n\t" \
    "v_mfma_f32_16x16x32_bf16 v[186:189], v[120:123], v[44:47], v[186:189]\n\t" \
    "ds_read_b64_tr_b16 v[154:155], v228 offset:14336\n\t" \
    "v_add3_u32 v230, v46, v47, v230\n\t" \
    "v_mfma_f32_16x16x32_bf16 v[174:177], v[124:127], v[40:43], v[174:177]\n\t" \
    "ds_read_b64_tr_b16 v[156:157], v229 offset:12288\n\t" \
    "v_mfma_f32_16x16x32_bf16 v[190:193], v[124:127], v[44:47], v[190:193]\n\t" \
    "ds_read_b64_tr_b16 v[158:159], v229 offset:14336\n\t" \
    "s_waitcnt vmcnt(12) lgkmcnt(8)\n\t" \
    "v_mfma_f32_16x16x32_bf16 v[162:165], v[128:131], v[48:51], v[162:165]\n\t" \
    "ds_read_b64_tr_b16 v[96:97], v226 offset:16384\n\t" \
    "buffer_load_dwordx4 v[40:43], v241, %[rsb], s80 offen sc1\n\t" \
    "v_mfma_f32_16x16x32_bf16 v[178:181], v[128:131], v[52:55], v[178:181]\n\t" \
    "ds_read_b64_tr_b16 v[98:99], v226 offset:18432\n\t" \
    "v_add3_u32 v230, v48, v49, v230\n\t" \
    "v_mfma_f32_16x16x32_bf16 v[166:169], v[132:135], v[48:51], v[166:169]\n\t" \
    "ds_read_b64_tr_b16 v[100:101], v227 offset:16384\n\t" \
    "buffer_load_dwordx4 v[44:47], v241, %[rsb], s80 offen offset:1024 sc1\n\t" \
    "v_mfma_f32_16x16x32_bf16 v[182:185], v[132:135], v[52:55], v[182:185]\n\t" \
    "ds_read_b64_tr_b16 v[102:103], v227 offset:18432\n\t" \
    "v_add3_u32 v230, v50, v51, v230\n\t" \
    "v_mfma_f32_16x16x32_bf16 v[170:173], v[136:139], v[48:51], v[170:173]\n\t" \
    "ds_read_b64_tr_b16 v[104:105], v228 offset:16384\n\t" \
    "v_add3_u32 v230, v52, v53, v230\n\t" \
    "v_mfma_f32_16x16x32_bf16 v[186:189], v[136:139], v[52:55], v[186:189]\n\t" \
    "ds_read_b64_tr_b16 v[106:107], v228 offset:18432\n\t" \
    "v_add3_u32 v230, v54, v55, v230\n\t" \
    "v_mfma_f32_16x16x32_bf16 v[174:177], v[140:143], v[48:51], v[174:177]\n\t" \
    "ds_read_b64_tr_b16 v[108:109], v229 offset:16384\n\t" \
    "v_mfma_f32_16x16x32_bf16 v[190:193], v[140:143], v[52:55], v[190:193]\n\t" \
    "ds_read_b64_tr_b16 v[110:111], v229 offset:18432\n\t" \
    "s_waitcnt vmcnt(12) lgkmcnt(8)\n\t" \
    "v_mfma_f32_16x16x32_bf16 v[162:165], v[144:147], v[56:59], v[162:165]\n\t" \
    "ds_read_b64_tr_b16 v[112:113], v226 offset:20480\n\t" \
    "buffer_load_dwordx4 v[48:51], v241, %[rsb], s80 offen offset:2048 sc1\n\t" \
    "v_mfma_f32_16x16x32_bf16 v[178:181], v[144:147], v[60:63], v[178:181]\n\t" \
    "ds_read_b64_tr_b16 v[114:115], v226 offset:22528\n\t" \
    "v_add3_u32 v230, v56, v57, v230\n\t" \
    "v_mfma_f32_16x16x32_bf16 v[166:169], v[148:151], v[56:59], v[166:169]\n\t" \
    "ds_read_b64_tr_b16 v[116:117], v227 offset:20480\n\t" \
    "buffer_load_dwordx4 v[52:55], v241, %[rsb], s80 offen offset:3072 sc1\n\t" \
    "v_mfma_f32_16x16x32_bf16 v[182:185], v[148:151], v[60:63], v[182:185]\n\t" \
    "ds_read_b64_tr_b16 v[118:119], v227 offset:22528\n\t" \
    "v_add3_u32 v230, v58, v59, v230\n\t" \
    "v_mfma_f32_16x16x32_bf16 v[170:173], v[152:155], v[56:59], v[170:173]\n\t" \
    "ds_read_b64_tr_b16 v[120:121], v228 offset:20480\n\t" \
    "v_add3_u32 v230, v60, v61, v230\n\t" \
    "v_mfma_f32_16x16x32_bf16 v[186:189], v[152:155], v[60:63], v[186:189]\n\t" \
    "ds_read_b64_tr_b16 v[122:123], v228 offset:22528\n\t" \
    "v_add3_u32 v230, v62, v63, v230\n\t" \
    "v_mfma_f32_16x16x32_bf16 v[174:177], v[156:159], v[56:59], v[174:177]\n\t" \
    "ds_read_b64_tr_b16 v[124:125], v229 offset:20480\n\t" \
    "v_mfma_f32_16x16x32_bf16 v[190:193], v[156:159], v[60:63], v[190:193]\n\t" \
    "ds_read_b64_tr_b16 v[126:127], v229 offset:22528\n\t" \
    "s_waitcnt vmcnt(12) lgkmcnt(8)\n\t" \
    "ds_read_b32 v160, v249 offset:56\n\t" \
    "v_mfma_f32_16x16x32_bf16 v[162:165], v[96:99], v[64:67], v[162:165]\n\t" \
    "ds_read_b64_tr_b16 v[128:129], v226 offset:24576\n\t" \
    "buffer_load_dwordx4 v[56:59], v242, %[rsb], s80 offen sc1\n\t" \
    "v_mfma_f32_16x16x32_bf16 v[178:181], v[96:99], v[68:71], v[178:181]\n\t" \
    "ds_read_b64_tr_b16 v[130:131], v226 offset:26624\n\t" \
    "v_add3_u32 v230, v64, v65, v230\n\t" \
    "v_mfma_f32_16x16x32_bf16 v[166:169], v[100:103], v[64:67], v[166:169]\n\t" \
    "ds_read_b64_tr_b16 v[132:133], v227 offset:24576\n\t" \
    "buffer_load_dwordx4 v[60:63], v242, %[rsb], s80 offen offset:1024 sc1\n\t" \
    "v_mfma_f32_16x16x32_bf16 v[182:185], v[100:103], v[68:71], v[182:185]\n\t" \
    "ds_read_b64_tr_b16 v[134:135], v227 offset:26624\n\t" \
    "v_add3_u32 v230, v66, v67, v230\n\t" \
    "v_mfma_f32_16x16x32_bf16 v[170:173], v[104:107], v[64:67], v[170:173]\n\t" \
    "ds_read_b64_tr_b16 v[136:137], v228 offset:24576\n\t" \
    "v_add3_u32 v230, v68, v69, v230\n\t" \
    "v_mfma_f32_16x16x32_bf16 v[186:189], v[104:107], v[68:71], v[186:189]\n\t" \
    "ds_read_b64_tr_b16 v[138:139], v228 offset:26624\n\t" \
    "v_add3_u32 v230, v70, v71, v230\n\t" \
    "v_mfma_f32_16x16x32_bf16 v[174:177], v[108:111], v[64:67], v[174:177]\n\t" \
    "ds_read_b64_tr_b16 v[140:141], v229 offset:24576\n\t" \
    "v_mfma_f32_16x16x32_bf16 v[190:193], v[108:111], v[68:71], v[190:193]\n\t" \
    "ds_read_b64_tr_b16 v[142:143], v229 offset:26624\n\t" \
    "s_waitcnt vmcnt(12) lgkmcnt(8)\n\t" \
    "v_mfma_f32_16x16x32_bf16 v[162:165], v[112:115], v[72:75], v[162:165]\n\t" \
    "ds_read_b64_tr_b16 v[144:145], v226 offset:28672\n\t" \
    "buffer_load_dwordx4 v[64:67], v242, %[rsb], s80 offen offset:2048 sc1\n\t" \
    "v_mfma_f32_16x16x32_bf16 v[178:181], v[112:115], v[76:79], v[178:181]\n\t" \
    "ds_read_b64_tr_b16 v[146:147], v226 offset:30720\n\t" \
    "v_add3_u32 v230, v72, v73, v230\n\t" \
    "v_mfma_f32_16x16x32_bf16 v[166:169], v[116:119], v[72:75], v[166:169]\n\t" \
    "ds_read_b64_tr_b16 v[148:149], v227 offset:28672\n\t" \
    "buffer_load_dwordx4 v[68:71], v242, %[rsb], s80 offen offset:3072 sc1\n\t" \
    "v_add_u32 v226, 0x8000, v226\n\t" \
    "v_and_b32 v226, 0x1ffff, v226\n\t" \
    "v_mfma_f32_16x16x32_bf16 v[182:185], v[116:119], v[76:79], v[182:185]\n\t" \
    "ds_read_b64_tr_b16 v[150:151], v227 offset:30720\n\t" \
    "v_add3_u32 v230, v74, v75, v230\n\t" \
    "v_mfma_f32_16x16x32_bf16 v[170:173], v[120:123], v[72:75], v[170:173]\n\t" \
    "ds_read_b64_tr_b16 v[152:153], v228 offset:28672\n\t" \
    "v_add3_u32 v230, v76, v77, v230\n\t" \
    "v_add_u32 v227, 0x8000, v227\n\t" \
    "v_and_b32 v227, 0x1ffff, v227\n\t" \
    "v_mfma_f32_16x16x32_bf16 v[186:189], v[120:123], v[76:79], v[186:189]\n\t" \
    "ds_read_b64_tr_b16 v[154:155], v228 offset:30720\n\t" \
    "v_add3_u32 v230, v78, v79, v230\n\t" \
    "v_mfma_f32_16x16x32_bf16 v[174:177], v[124:127], v[72:75], v[174:177]\n\t" \
    "v_readfirstlane_b32 s83, v160\n\t" \
    "s_add_u32 s82, s82, 1\n\t" \
    "ds_read_b64_tr_b16 v[156:157], v229 offset:28672\n\t" \
    "v_add_u32 v228, 0x8000, v228\n\t" \
    "v_and_b32 v228, 0x1ffff, v228\n\t" \
    "v_mfma_f32_16x16x32_bf16 v[190:193], v[124:127], v[76:79], v[190:193]\n\t" \
    "s_cmp_lt_u32 s83, s82\n\t" \
    "s_cbranch_scc1 .Lc_w1\n\t" \
    ".Lc_r1:\n\t" \
    "ds_read_b64_tr_b16 v[158:159], v229 offset:30720\n\t" \
    "v_add_u32 v229, 0x8000, v229\n\t" \
    "v_and_b32 v229, 0x1ffff, v229\n\t" \
    "s_waitcnt vmcnt(12) lgkmcnt(8)\n\t" \
    "v_mfma_f32_16x16x32_bf16 v[162:165], v[128:131], v[80:83], v[162:165]\n\t" \
    "ds_read_b64_tr_b16 v[96:97], v226\n\t" \
    "buffer_load_dwordx4 v[72:75], v243, %[rsb], s80 offen sc1\n\t" \
    "v_mfma_f32_16x16x32_bf16 v[178:181], v[128:131], v[84:87], v[178:181]\n\t" \
    "ds_read_b64_tr_b16 v[98:99], v226 offset:2048\n\t" \
    "v_add3_u32 v230, v80, v81, v230\n\t" \
    "v_mfma_f32_16x16x32_bf16 v[166:169], v[132:135], v[80:83], v[166:169]\n\t" \
    "ds_read_b64_tr_b16 v[100:101], v227\n\t" \
    "buffer_load_dwordx4 v[76:79], v243, %[rsb], s80 offen offset:1024 sc1\n\t" \
    "v_mfma_f32_16x16x32_bf16 v[182:185], v[132:135], v[84:87], v[182:185]\n\t" \
    "ds_read_b64_tr_b16 v[102:103], v227 offset:2048\n\t" \
    "v_add3_u32 v230, v82, v83, v230\n\t" \
    "v_mfma_f32_16x16x32_bf16 v[170:173], v[136:139], v[80:83], v[170:173]\n\t" \
    "ds_read_b64_tr_b16 v[104:105], v228\n\t" \
    "v_add3_u32 v230, v84, v85, v230\n\t" \
    "v_mfma_f32_16x16x32_bf16 v[186:189], v[136:139], v[84:87], v[186:189]\n\t" \
    "ds_read_b64_tr_b16 v[106:107], v228 offset:2048\n\t" \
    "v_add3_u32 v230, v86, v87, v230\n\t" \
    "v_mfma_f32_16x16x32_bf16 v[174:177], v[140:143], v[80:83], v[174:177]\n\t" \
    "ds_read_b64_tr_b16 v[108:109], v229\n\t" \
    "v_mfma_f32_16x16x32_bf16 v[190:193], v[140:143], v[84:87], v[190:193]\n\t" \
    "ds_read_b64_tr_b16 v[110:111], v229 offset:2048\n\t" \
    "s_waitcnt vmcnt(12) lgkmcnt(8)\n\t" \
    "v_mfma_f32_16x16x32_bf16 v[162:165], v[144:147], v[88:91], v[162:165]\n\t" \
    "ds_read_b64_tr_b16 v[112:113], v226 offset:4096\n\t" \
    "buffer_load_dwordx4 v[80:83], v243, %[rsb], s80 offen offset:2048 sc1\n\t" \
    "v_mfma_f32_16x16x32_bf16 v[178:181], v[144:147], v[92:95], v[178:181]\n\t" \
    "ds_read_b64_tr_b16 v[114:115], v226 offset:6144\n\t" \
    "v_add3_u32 v230, v88, v89, v230\n\t" \
    "v_mfma_f32_16x16x32_bf16 v[166:169], v[148:151], v[88:91], v[166:169]\n\t" \
    "ds_read_b64_tr_b16 v[116:117], v227 offset:4096\n\t" \
    "buffer_load_dwordx4 v[84:87], v243, %[rsb], s80 offen offset:3072 sc1\n\t" \
    "v_mfma_f32_16x16x32_bf16 v[182:185], v[148:151], v[92:95], v[182:185]\n\t" \
    "ds_read_b64_tr_b16 v[118:119], v227 offset:6144\n\t" \
    "v_add3_u32 v230, v90, v91, v230\n\t" \
    "v_mfma_f32_16x16x32_bf16 v[170:173], v[152:155], v[88:91], v[170:173]\n\t" \
    "ds_read_b64_tr_b16 v[120:121], v228 offset:4096\n\t" \
    "v_add3_u32 v230, v92, v93, v230\n\t" \
    "v_mfma_f32_16x16x32_bf16 v[186:189], v[152:155], v[92:95], v[186:189]\n\t" \
    "ds_read_b64_tr_b16 v[122:123], v228 offset:6144\n\t" \
    "v_add3_u32 v230, v94, v95, v230\n\t" \
    "v_mfma_f32_16x16x32_bf16 v[174:177], v[156:159], v[88:91], v[174:177]\n\t" \
    "ds_read_b64_tr_b16 v[124:125], v229 offset:4096\n\t" \
    "s_add_u32 s80, s80, 0x4000\n\t" \
    "s_and_b32 s80, s80, 0xc000\n\t" \
    "v_mfma_f32_16x16x32_bf16 v[190:193], v[156:159], v[92:95], v[190:193]\n\t" \
    "ds_read_b64_tr_b16 v[126:127], v229 offset:6144\n\t" \
    "s_waitcnt vmcnt(12) lgkmcnt(8)\n\t" \
    "v_mfma_f32_16x16x32_bf16 v[162:165], v[96:99], v[32:35], v[162:165]\n\t" \
    "ds_read_b64_tr_b16 v[128:129], v226 offset:8192\n\t" \
    "buffer_load_dwordx4 v[88:91], v240, %[rsb], s80 offen sc1\n\t" \
    "v_mfma_f32_16x16x32_bf16 v[178:181], v[96:99], v[36:39], v[178:181]\n\t" \
    "ds_read_b64_tr_b16 v[130:131], v226 offset:10240\n\t" \
    "v_add3_u32 v230, v32, v33, v230\n\t" \
    ".Lpad_c1:\n\t" \
    "s_cbranch_execz .Lpad_ret\n\t" \
    "v_mfma_f32_16x16x32_bf16 v[166:169], v[100:103], v[32:35], v[166:169]\n\t" \
    "ds_read_b64_tr_b16 v[132:133], v227 offset:8192\n\t" \
    "buffer_load_dwordx4 v[92:95], v240, %[rsb], s80 offen offset:1024 sc1\n\t" \
    "v_mfma_f32_16x16x32_bf16 v[182:185], v[100:103], v[36:39], v[182:185]\n\t" \
    "ds_read_b64_tr_b16 v[134:135], v227 offset:10240\n\t" \
    "v_add3_u32 v230, v34, v35, v230\n\t" \
    "v_mfma_f32_16x16x32_bf16 v[170:173], v[104:107], v[32:35], v[170:173]\n\t" \
    "ds_read_b64_tr_b16 v[136:137], v228 offset:8192\n\t" \
    "v_add3_u32 v230, v36, v37, v230\n\t" \
    "v_mfma_f32_16x16x32_bf16 v[186:189], v[104:107], v[36:39], v[186:189]\n\t" \
    "ds_read_b64_tr_b16 v[138:139], v228 offset:10240\n\t" \
    "v_add3_u32 v230, v38, v39, v230\n\t" \
    "v_mfma_f32_16x16x32_bf16 v[174:177], v[108:111], v[32:35], v[174:177]\n\t" \
    "ds_read_b64_tr_b16 v[140:141], v229 offset:8192\n\t" \
    "buffer_load_dwordx2 v[160:161], v244, %[rsb], %[sog] offen sc1\n\t" \
    "v_mfma_f32_16x16x32_bf16 v[190:193], v[108:111], v[36:39], v[190:193]\n\t" \
    "ds_read_b64_tr_b16 v[142:143], v229 offset:10240\n\t" \
    "s_waitcnt vmcnt(13) lgkmcnt(8)\n\t" \
    "v_mfma_f32_16x16x32_bf16 v[162:165], v[112:115], v[40:43], v[162:165]\n\t" \
    "ds_read_b64_tr_b16 v[144:145], v226 offset:12288\n\t" \
    "v_mfma_f32_16x16x32_bf16 v[178:181], v[112:115], v[44:47], v[178:181]\n\t" \
    "ds_read_b64_tr_b16 v[146:147], v226 offset:14336\n\t" \
    "v_add3_u32 v230, v40, v41, v230\n\t" \
    "v_mfma_f32_16x16x32_bf16 v[166:169], v[116:119], v[40:43], v[166:169]\n\t" \
    "ds_read_b64_tr_b16 v[148:149], v227 offset:12288\n\t" \
    "v_mfma_f32_16x16x32_bf16 v[182:185], v[116:119], v[44:47], v[182:185]\n\t" \
    "ds_read_b64_tr_b16 v[150:151], v227 offset:14336\n\t" \
    "v_add3_u32 v230, v42, v43, v230\n\t" \
    "v_mfma_f32_16x16x32_bf16 v[170:173], v[120:123], v[40:43], v[170:173]\n\t" \
    "ds_read_b64_tr_b16 v[152:153], v228 offset:12288\n\t" \
    "v_add3_u32 v230, v44, v45, v230\n\t" \
    "v_mfma_f32_16x16x32_bf16 v[186:189], v[120:123], v[44:47], v[186:189]\n\t" \
    "ds_read_b64_tr_b16 v[154:155], v228 offset:14336\n\t" \
    "v_add3_u32 v230, v46, v47, v230\n\t" \
    "v_mfma_f32_16x16x32_bf16 v[174:177], v[124:127], v[40:43], v[174:177]\n\t" \
    "ds_read_b64_tr_b16 v[156:157], v229 offset:12288\n\t" \
    "v_mfma_f32_16x16x32_bf16 v[190:193], v[124:127], v[44:47], v[190:193]\n\t" \
    "ds_read_b64_tr_b16 v[158:159], v229 offset:14336\n\t" \
    "s_waitcnt vmcnt(11) lgkmcnt(8)\n\t" \
    "v_mfma_f32_16x16x32_bf16 v[162:165], v[128:131], v[48:51], v[162:165]\n\t" \
    "ds_read_b64_tr_b16 v[96:97], v226 offset:16384\n\t" \
    "v_mfma_f32_16x16x32_bf16 v[178:181], v[128:131], v[52:55], v[178:181]\n\t" \
    "ds_read_b64_tr_b16 v[98:99], v226 offset:18432\n\t" \
    "v_add3_u32 v230, v48, v49, v230\n\t" \
    "v_mfma_f32_16x16x32_bf16 v[166:169], v[132:135], v[48:51], v[166:169]\n\t" \
    "ds_read_b64_tr_b16 v[100:101], v227 offset:16384\n\t" \
    "v_mfma_f32_16x16x32_bf16 v[182:185], v[132:135], v[52:55], v[182:185]\n\t" \
    "ds_read_b64_tr_b16 v[102:103], v227 offset:18432\n\t" \
    "v_add3_u32 v230, v50, v51, v230\n\t" \
    "v_mfma_f32_16x16x32_bf16 v[170:173], v[136:139], v[48:51], v[170:173]\n\t" \
    "ds_read_b64_tr_b16 v[104:105], v228 offset:16384\n\t" \
    "v_add3_u32 v230, v52, v53, v230\n\t" \
    "v_mfma_f32_16x16x32_bf16 v[186:189], v[136:139], v[52:55], v[186:189]\n\t" \
    "ds_read_b64_tr_b16 v[106:107], v228 offset:18432\n\t" \
    "v_add3_u32 v230, v54, v55, v230\n\t" \
    "v_mfma_f32_16x16x32_bf16 v[174:177], v[140:143], v[48:51], v[174:177]\n\t" \
    "ds_read_b64_tr_b16 v[108:109], v229 offset:16384\n\t" \
    "v_mfma_f32_16x16x32_bf16 v[190:193], v[140:143], v[52:55], v[190:193]\n\t" \
    "ds_read_b64_tr_b16 v[110:111], v229 offset:18432\n\t" \
    "s_waitcnt vmcnt(9) lgkmcnt(8)\n\t" \
    "v_mfma_f32_16x16x32_bf16 v[162:165], v[144:147], v[56:59], v[162:165]\n\t" \
    "ds_read_b64_tr_b16 v[112:113], v226 offset:20480\n\t" \
    "v_mfma_f32_16x16x32_bf16 v[178:181], v[144:147], v[60:63], v[178:181]\n\t" \
    "ds_read_b64_tr_b16 v[114:115], v226 offset:22528\n\t" \
    "v_add3_u32 v230, v56, v57, v230\n\t" \
    "v_mfma_f32_16x16x32_bf16 v[166:169], v[148:151], v[56:59], v[166:169]\n\t" \
    "ds_read_b64_tr_b16 v[116:117], v227 offset:20480\n\t" \
    "v_mfma_f32_16x16x32_bf16 v[182:185], v[148:151], v[60:63], v[182:185]\n\t" \
    "ds_read_b64_tr_b16 v[118:119], v227 offset:22528\n\t" \
    "v_add3_u32 v230, v58, v59, v230\n\t" \
    "v_mfma_f32_16x16x32_bf16 v[170:173], v[152:155], v[56:59], v[170:173]\n\t" \
    "ds_read_b64_tr_b16 v[120:121], v228 offset:20480\n\t" \
    "v_add3_u32 v230, v60, v61, v230\n\t" \
    "v_mfma_f32_16x16x32_bf16 v[186:189], v[152:155], v[60:63], v[186:189]\n\t" \
    "ds_read_b64_tr_b16 v[122:123], v228 offset:22528\n\t" \
    "v_add3_u32 v230, v62, v63, v230\n\t" \
    "v_mfma_f32_16x16x32_bf16 v[174:177], v[156:159], v[56:59], v[174:177]\n\t" \
    "ds_read_b64_tr_b16 v[124:125], v229 offset:20480\n\t" \
    "v_mfma_f32_16x16x32_bf16 v[190:193], v[156:159], v[60:63], v[190:193]\n\t" \
    "ds_read_b64_tr_b16 v[126:127], v229 offset:22528\n\t" \
    "s_waitcnt vmcnt(7) lgkmcnt(8)\n\t" \
    "v_mfma_f32_16x16x32_bf16 v[162:165], v[96:99], v[64:67], v[162:165]\n\t" \
    "ds_read_b64_tr_b16 v[128:129], v226 offset:24576\n\t" \
    "v_mfma_f32_16x16x32_bf16 v[178:181], v[96:99], v[68:71], v[178:181]\n\t" \
    "ds_read_b64_tr_b16 v[130:131], v226 offset:26624\n\t" \
    "v_add3_u32 v230, v64, v65, v230\n\t" \
    "v_mfma_f32_16x16x32_bf16 v[166:169], v[100:103], v[64:67], v[166:169]\n\t" \
    "ds_read_b64_tr_b16 v[132:133], v227 offset:24576\n\t" \
    "v_mfma_f32_16x16x32_bf16 v[182:185], v[100:103], v[68:71], v[182:185]\n\t" \
    "ds_read_b64_tr_b16 v[134:135], v227 offset:26624\n\t" \
    "v_add3_u32 v230, v66, v67, v230\n\t" \
    "v_mfma_f32_16x16x32_bf16 v[170:173], v[104:107], v[64:67], v[170:173]\n\t" \
    "ds_read_b64_tr_b16 v[136:137], v228 offset:24576\n\t" \
    "v_add3_u32 v230, v68, v69, v230\n\t" \
    "v_mfma_f32_16x16x32_bf16 v[186:189], v[104:107], v[68:71], v[186:189]\n\t" \
    "ds_read_b64_tr_b16 v[138:139], v228 offset:26624\n\t" \
    "v_add3_u32 v230, v70, v71, v230\n\t" \
    "v_mfma_f32_16x16x32_bf16 v[174:177], v[108:111], v[64:67], v[174:177]\n\t" \
    "ds_read_b64_tr_b16 v[140:141], v229 offset:24576\n\t" \
    "v_mfma_f32_16x16x32_bf16 v[190:193], v[108:111], v[68:71], v[190:193]\n\t" \
    "ds_read_b64_tr_b16 v[142:143], v229 offset:26624\n\t" \
    "s_waitcnt vmcnt(5) lgkmcnt(8)\n\t" \
    "v_mfma_f32_16x16x32_bf16 v[162:165], v[112:115], v[72:75], v[162:165]\n\t" \
    "ds_read_b64_tr_b16 v[144:145], v226 offset:28672\n\t" \
    "v_mfma_f32_16x16x32_bf16 v[178:181], v[112:115], v[76:79], v[178:181]\n\t" \
    "ds_read_b64_tr_b16 v[146:147], v226 offset:30720\n\t" \
    "v_add3_u32 v230, v72, v73, v230\n\t" \
    "v_mfma_f32_16x16x32_bf16 v[166:169], v[116:119], v[72:75], v[166:169]\n\t" \
    "ds_read_b64_tr_b16 v[148:149], v227 offset:28672\n\t" \
    "v_mfma_f32_16x16x32_bf16 v[182:185], v[116:119], v[76:79], v[182:185]\n\t" \
    "ds_read_b64_tr_b16 v[150:151], v227 offset:30720\n\t" \
    "v_add3_u32 v230, v74, v75, v230\n\t" \
    "v_mfma_f32_16x16x32_bf16 v[170:173], v[120:123], v[72:75], v[170:173]\n\t" \
    "ds_read_b64_tr_b16 v[152:153], v228 offset:28672\n\t" \
    "v_add3_u32 v230, v76, v77, v230\n\t" \
    "v_mfma_f32_16x16x32_bf16 v[186:189], v[120:123], v[76:79], v[186:189]\n\t" \
    "ds_read_b64_tr_b16 v[154:155], v228 offset:30720\n\t" \
    "v_add3_u32 v230, v78, v79, v230\n\t" \
    "v_mfma_f32_16x16x32_bf16 v[174:177], v[124:127], v[72:75], v[174:177]\n\t" \
    "ds_read_b64_tr_b16 v[156:157], v229 offset:28672\n\t" \
    "v_mfma_f32_16x16x32_bf16 v[190:193], v[124:127], v[76:79], v[190:193]\n\t" \
    "ds_read_b64_tr_b16 v[158:159], v229 offset:30720\n\t" \
    "s_waitcnt vmcnt(3) lgkmcnt(8)\n\t" \
    "v_mfma_f32_16x16x32_bf16 v[162:165], v[128:131], v[80:83], v[162:165]\n\t" \
    "ds_read_b32 v231, v249 offset:68\n\t" \
    "v_mfma_f32_16x16x32_bf16 v[178:181], v[128:131], v[84:87], v[178:181]\n\t" \
    "v_add3_u32 v230, v80, v81, v230\n\t" \
    "v_mfma_f32_16x16x32_bf16 v[166:169], v[132:135], v[80:83], v[166:169]\n\t" \
    "ds_read_b128 v[96:99], v252\n\t" \
    "v_mfma_f32_16x16x32_bf16 v[182:185], v[132:135], v[84:87], v[182:185]\n\t" \
    "ds_read_b128 v[100:103], v252 offset:64\n\t" \
    "v_add3_u32 v230, v82, v83, v230\n\t" \
    "v_mfma_f32_16x16x32_bf16 v[170:173], v[136:139], v[80:83], v[170:173]\n\t" \
    "ds_read_b128 v[104:107], v252 offset:128\n\t" \
    "v_add3_u32 v230, v84, v85, v230\n\t" \
    "v_mfma_f32_16x16x32_bf16 v[186:189], v[136:139], v[84:87], v[186:189]\n\t" \
    "ds_read_b128 v[108:111], v252 offset:192\n\t" \
    "v_add3_u32 v230, v86, v87, v230\n\t" \
    "v_mfma_f32_16x16x32_bf16 v[174:177], v[140:143], v[80:83], v[174:177]\n\t" \
    "v_mfma_f32_16x16x32_bf16 v[190:193], v[140:143], v[84:87], v[190:193]\n\t" \
    "s_waitcnt vmcnt(1) lgkmcnt(5)\n\t" \
    "v_mfma_f32_16x16x32_bf16 v[162:165], v[144:147], v[88:91], v[162:165]\n\t" \
    "v_mfma_f32_16x16x32_bf16 v[166:169], v[148:151], v[88:91], v[166:169]\n\t" \
    "v_add3_u32 v230, v88, v89, v230\n\t" \
    "v_mfma_f32_16x16x32_bf16 v[170:173], v[152:155], v[88:91], v[170:173]\n\t" \
    "v_mfma_f32_16x16x32_bf16 v[174:177], v[156:159], v[88:91], v[174:177]\n\t" \
    "v_add3_u32 v230, v90, v91, v230\n\t" \
    "v_mfma_f32_16x16x32_bf16 v[178:181], v[144:147], v[92:95], v[178:181]\n\t" \
    "v_add3_u32 v230, v92, v93, v230\n\t" \
    "v_mfma_f32_16x16x32_bf16 v[182:185], v[148:151], v[92:95], v[182:185]\n\t" \
    "v_add3_u32 v230, v94, v95, v230\n\t" \
    "v_mfma_f32_16x16x32_bf16 v[186:189], v[152:155], v[92:95], v[186:189]\n\t" \
    "s_cmp_lg_u64 %[rm], 0\n\t" \
    "v_mfma_f32_16x16x32_bf16 v[190:193], v[156:159], v[92:95], v[190:193]\n\t" \
    "s_cbranch_scc1 .Lc_y\n\t" \
    ".Lc_yr:\n\t" \
    "s_add_u32 s85, %[sout], 0x8000\n\t" \
    "v_pk_add_f32 v[0:1], v[0:1], v[162:163]\n\t" \
    "v_pk_add_f32 v[2:3], v[2:3], v[164:165]\n\t" \
    "v_pk_add_f32 v[4:5], v[4:5], v[166:167]\n\t" \
    "v_pk_add_f32 v[6:7], v[6:7], v[168:169]\n\t" \
    "v_pk_fma_f32 v[0:1], v[232:233], v[0:1], v[194:195] op_sel_hi:[0,1,1]\n\t" \
    "v_pk_fma_f32 v[2:3], v[232:233], v[2:3], v[196:197] op_sel_hi:[0,1,1]\n\t" \
    "v_pk_fma_f32 v[4:5], v[232:233], v[4:5], v[198:199] op_sel_hi:[0,1,1]\n\t" \
    "v_pk_fma_f32 v[6:7], v[232:233], v[6:7], v[200:201] op_sel_hi:[0,1,1]\n\t" \
    "v_cvt_pk_bf16_f32 v32, v0, v1\n\t" \
    "v_cvt_pk_bf16_f32 v34, v4, v5\n\t" \
    "v_cvt_pk_bf16_f32 v33, v2, v3\n\t" \
    "v_cvt_pk_bf16_f32 v35, v6, v7\n\t" \
    "s_nop 0\n\t" \
    "v_permlane16_swap_b32 v32, v34\n\t" \
    "v_permlane16_swap_b32 v33, v35\n\t" \
    "buffer_store_dwordx4 v[32:35], v247, %[rso], %[sout] offen sc1\n\t" \
    "v_pk_add_f32 v[8:9], v[8:9], v[170:171]\n\t" \
    "v_pk_add_f32 v[10:11], v[10:11], v[172:173]\n\t" \
    "v_pk_add_f32 v[12:13], v[12:13], v[174:175]\n\t" \
    "v_pk_add_f32 v[14:15], v[14:15], v[176:177]\n\t" \
    "v_pk_fma_f32 v[8:9], v[232:233], v[8:9], v[202:203] op_sel_hi:[0,1,1]\n\t" \
    "v_pk_fma_f32 v[10:11], v[232:233], v[10:11], v[204:205] op_sel_hi:[0,1,1]\n\t" \
    "v_pk_fma_f32 v[12:13], v[232:233], v[12:13], v[206:207] op_sel_hi:[0,1,1]\n\t" \
    "v_pk_fma_f32 v[14:15], v[232:233], v[14:15], v[208:209] op_sel_hi:[0,1,1]\n\t" \
    "v_cvt_pk_bf16_f32 v36, v8, v9\n\t" \
    "v_cvt_pk_bf16_f32 v38, v12, v13\n\t" \
    "v_cvt_pk_bf16_f32 v37, v10, v11\n\t" \
    "v_cvt_pk_bf16_f32 v39, v14, v15\n\t" \
    "v_cmp_gt_u32 vcc, 32, v250\n\t" \
    "v_permlane16_swap_b32 v36, v38\n\t" \
    "v_permlane16_swap_b32 v37, v39\n\t" \
    "buffer_store_dwordx4 v[36:39], v247, %[rso], %[sout] offen offset:64 sc1\n\t" \
    "v_pk_add_f32 v[16:17], v[16:17], v[178:179]\n\t" \
    "v_pk_add_f32 v[18:19], v[18:19], v[180:181]\n\t" \
    "v_pk_add_f32 v[20:21], v[20:21], v[182:183]\n\t" \
    "v_pk_add_f32 v[22:23], v[22:23], v[184:185]\n\t" \
    "v_pk_fma_f32 v[16:17], v[234:235], v[16:17], v[210:211] op_sel_hi:[0,1,1]\n\t" \
    "v_pk_fma_f32 v[18:19], v[234:235], v[18:19], v[212:213] op_sel_hi:[0,1,1]\n\t" \
    "v_pk_fma_f32 v[20:21], v[234:235], v[20:21], v[214:215] op_sel_hi:[0,1,1]\n\t" \
    "v_pk_fma_f32 v[22:23], v[234:235], v[22:23], v[216:217] op_sel_hi:[0,1,1]\n\t" \
    "v_cvt_pk_bf16_f32 v40, v16, v17\n\t" \
    "v_cvt_pk_bf16_f32 v42, v20, v21\n\t" \
    "v_cvt_pk_bf16_f32 v41, v18, v19\n\t" \
    "v_cvt_pk_bf16_f32 v43, v22, v23\n\t" \
    "s_waitcnt vmcnt(2)\n\t" \
    "v_permlane16_swap_b32 v40, v42\n\t" \
    "v_permlane16_swap_b32 v41, v43\n\t" \
    "buffer_store_dwordx4 v[40:43], v247, %[rso], s85 offen sc1\n\t" \
    "v_pk_add_f32 v[24:25], v[24:25], v[186:187]\n\t" \
    "v_pk_add_f32 v[26:27], v[26:27], v[188:189]\n\t" \
    "v_pk_add_f32 v[28:29], v[28:29], v[190:191]\n\t" \
    "v_pk_add_f32 v[30:31], v[30:31], v[192:193]\n\t" \
    "v_pk_fma_f32 v[24:25], v[234:235], v[24:25], v[218:219] op_sel_hi:[0,1,1]\n\t" \
    "v_pk_fma_f32 v[26:27], v[234:235], v[26:27], v[220:221] op_sel_hi:[0,1,1]\n\t" \
    "v_pk_fma_f32 v[28:29], v[234:235], v[28:29], v[222:223] op_sel_hi:[0,1,1]\n\t" \
    "v_pk_fma_f32 v[30:31], v[234:235], v[30:31], v[224:225] op_sel_hi:[0,1,1]\n\t" \
    "v_cvt_pk_bf16_f32 v44, v24, v25\n\t" \
    "v_cvt_pk_bf16_f32 v46, v28, v29\n\t" \
    "v_cvt_pk_bf16_f32 v45, v26, v27\n\t" \
    "v_cvt_pk_bf16_f32 v47, v30, v31\n\t" \
    "s_waitcnt lgkmcnt(4)\n\t" \
    "v_cndmask_b32 v231, 0, v160, vcc\n\t" \
    "v_permlane16_swap_b32 v44, v46\n\t" \
    "v_permlane16_swap_b32 v45, v47\n\t" \
    "buffer_store_dwordx4 v[44:47], v247, %[rso], s85 offen offset:64 sc1\n\t" \
    "v_sub_u32 v231, v230, v231\n\t" \
    "v_cmp_ne_u32 vcc, v236, v161\n\t" \
    "v_mov_b32 v0, v231\n\t" \
    "s_nop 1\n\t" \
    "v_permlane32_swap_b32 v231, v0\n\t" \
    "v_add_u32 v231, v231, v0\n\t" \
    "v_mov_b32 v0, v231\n\t" \
    "s_nop 1\n\t" \
    "v_permlane16_swap_b32 v231, v0\n\t" \
    "v_add_u32 v231, v231, v0\n\t" \
    "s_nop 1\n\t" \
    "v_add_u32_dpp v231, v231, v231 row_ror:8 row_mask:0xf bank_mask:0xf bound_ctrl:1\n\t" \
    "s_nop 1\n\t" \
    "v_add_u32_dpp v231, v231, v231 row_ror:4 row_mask:0xf bank_mask:0xf bound_ctrl:1\n\t" \
    "s_nop 1\n\t" \
    "v_add_u32_dpp v231, v231, v231 row_ror:2 row_mask:0xf bank_mask:0xf bound_ctrl:1\n\t" \
    "s_nop 1\n\t" \
    "v_add_u32_dpp v231, v231, v231 row_ror:1 row_mask:0xf bank_mask:0xf bound_ctrl:1\n\t" \
    "s_nop 0\n\t" \
    "v_readfirstlane_b32 s83, v231\n\t" \
    "s_or_b32 s83, s83, vcc_lo\n\t" \
    "s_cselect_b32 %[st], 2, 0\n\t" \
    "s_waitcnt lgkmcnt(0)\n\t" \
    ".Lc_end:\n\t"
#define RED1_ASM \
    "s_mov_b32 s80, 256\n\t" \
    "s_add_u32 s81, %[s0], 0x1000\n\t" \
    "v_mov_b32 v41, 1\n\t" \
    ".Lr_t:\n\t" \
    "s_memrealtime s[82:83]\n\t" \
    "s_sub_u32 s80, s80, 1\n\t" \
    "s_cbranch_scc1 .Lr_go\n\t" \
    "s_waitcnt lgkmcnt(0)\n\t" \
    "s_sub_u32 s82, %[t1], s82\n\t" \
    "s_cmp_lt_i32 s82, 5\n\t" \
    "s_cbranch_scc1 .Lr_go\n\t" \
    "s_cmp_lt_i32 s82, 13\n\t" \
    "s_cbranch_scc1 .Lr_t\n\t" \
    "s_sleep 2\n\t" \
    "s_branch .Lr_t\n\t" \
    ".Lr_go:\n\t" \
    "buffer_load_dwordx4 v[0:3], %[v16], %[rsb], %[sm] offen sc1\n\t" \
    "buffer_load_dwordx4 v[4:7], %[v16], %[rsb], %[s0] offen sc1\n\t" \
    "buffer_load_dwordx4 v[8:11], %[v16], %[rsb], %[s0] offen offset:1024 sc1\n\t" \
    "buffer_load_dwordx4 v[12:15], %[v16], %[rsb], %[s0] offen offset:2048 sc1\n\t" \
    "buffer_load_dwordx4 v[16:19], %[v16], %[rsb], %[s0] offen offset:3072 sc1\n\t" \
    "buffer_load_dwordx4 v[20:23], %[v16], %[rsb], s81 offen sc1\n\t" \
    "buffer_load_dwordx4 v[24:27], %[v16], %[rsb], s81 offen offset:1024 sc1\n\t" \
    "buffer_load_dwordx4 v[28:31], %[v16], %[rsb], s81 offen offset:2048 sc1\n\t" \
    "buffer_load_dwordx4 v[32:35], %[v16], %[rsb], s81 offen offset:3072 sc1\n\t" \
    "ds_write_b32 %[ra], v41\n\t" \
    "s_waitcnt vmcnt(8)\n\t" \
    "v_add3_u32 v40, v0, v1, v2\n\t" \
    "v_xor_b32 v41, %[tag], v1\n\t" \
    "v_and_b32 v42, %[mk2], v0\n\t" \
    "v_add_u32 v40, v40, v3\n\t" \
    "v_add_f32_dpp v0, v0, v0 row_ror:8 row_mask:0xf bank_mask:0xf bound_ctrl:1\n\t" \
    "v_add_f32_dpp v1, v1, v1 row_ror:8 row_mask:0xf bank_mask:0xf bound_ctrl:1\n\t" \
    "v_add_f32_dpp v2, v2, v2 row_ror:8 row_mask:0xf bank_mask:0xf bound_ctrl:1\n\t" \
    "v_add_f32_dpp v3, v3, v3 row_ror:8 row_mask:0xf bank_mask:0xf bound_ctrl:1\n\t" \
    "v_and_b32 v40, %[mk01], v40\n\t" \
    "v_sub_u32 v40, v40, v42\n\t" \
    "s_waitcnt vmcnt(7)\n\t" \
    "v_mfma_f32_16x16x32_bf16 v[36:39], %[sel], v[4:7], 0\n\t" \
    "v_add3_u32 v40, v4, v5, v40\n\t" \
    "v_add3_u32 v40, v6, v7, v40\n\t" \
    "v_add_f32_dpp v0, v0, v0 row_ror:4 row_mask:0xf bank_mask:0xf bound_ctrl:1\n\t" \
    "v_add_f32_dpp v1, v1, v1 row_ror:4 row_mask:0xf bank_mask:0xf bound_ctrl:1\n\t" \
    "s_waitcnt vmcnt(6)\n\t" \
    "v_mfma_f32_16x16x32_bf16 v[36:39], %[sel], v[8:11], v[36:39]\n\t" \
    "v_add3_u32 v40, v8, v9, v40\n\t" \
    "v_add3_u32 v40, v10, v11, v40\n\t" \
    "v_add_f32_dpp v2, v2, v2 row_ror:4 row_mask:0xf bank_mask:0xf bound_ctrl:1\n\t" \
    "v_add_f32_dpp v3, v3, v3 row_ror:4 row_mask:0xf bank_mask:0xf bound_ctrl:1\n\t" \
    "s_waitcnt vmcnt(5)\n\t" \
    "v_mfma_f32_16x16x32_bf16 v[36:39], %[sel], v[12:15], v[36:39]\n\t" \
    "v_add3_u32 v40, v12, v13, v40\n\t" \
    "v_add3_u32 v40, v14, v15, v40\n\t" \
    "v_and_b32 v41, %[mk2], v41\n\t" \
    "v_cmp_ne_u32 vcc, 0, v41\n\t" \
    "s_waitcnt vmcnt(4)\n\t" \
    "v_mfma_f32_16x16x32_bf16 v[36:39], %[sel], v[16:19], v[36:39]\n\t" \
    "v_add3_u32 v40, v16, v17, v40\n\t" \
    "v_add3_u32 v40, v18, v19, v40\n\t" \
    "v_permlane32_swap_b32 v0, v2\n\t" \
    "v_permlane32_swap_b32 v1, v3\n\t" \
    "s_waitcnt vmcnt(3)\n\t" \
    "v_mfma_f32_16x16x32_bf16 v[36:39], %[sel], v[20:23], v[36:39]\n\t" \
    "v_add3_u32 v40, v20, v21, v40\n\t" \
    "v_add3_u32 v40, v22, v23, v40\n\t" \
    "v_add_f32 v0, v0, v2\n\t" \
    "v_add_f32 v1, v1, v3\n\t" \
    "s_waitcnt vmcnt(2)\n\t" \
    "v_mfma_f32_16x16x32_bf16 v[36:39], %[sel], v[24:27], v[36:39]\n\t" \
    "v_add3_u32 v40, v24, v25, v40\n\t" \
    "v_add3_u32 v40, v26, v27, v40\n\t" \
    "v_mov_b32 v2, v0\n\t" \
    "v_mov_b32 v3, v1\n\t" \
    "s_waitcnt vmcnt(1)\n\t" \
    "v_mfma_f32_16x16x32_bf16 v[36:39], %[sel], v[28:31], v[36:39]\n\t" \
    "v_add3_u32 v40, v28, v29, v40\n\t" \
    "v_add3_u32 v40, v30, v31, v40\n\t" \
    "s_or_b32 s80, vcc_lo, vcc_hi\n\t" \
    "s_waitcnt vmcnt(0)\n\t" \
    "v_mfma_f32_16x16x32_bf16 v[36:39], %[sel], v[32:35], v[36:39]\n\t" \
    "v_add3_u32 v40, v32, v33, v40\n\t" \
    "v_add3_u32 v40, v34, v35, v40\n\t" \
    "v_permlane16_swap_b32 v0, v2\n\t" \
    "v_permlane16_swap_b32 v1, v3\n\t" \
    "v_add_f32 v0, v0, v2\n\t" \
    "v_add_f32 v1, v1, v3\n\t" \
    "v_cvt_pk_bf16_f32 v44, v0, v1\n\t" \
    "v_mov_b32 v45, v44\n\t" \
    "v_cvt_pk_bf16_f32 v48, v36, v37\n\t" \
    "v_cvt_pk_bf16_f32 v49, v38, v39\n\t" \
    "v_and_b32 v46, %[mkl], v44\n\t" \
    "v_add3_u32 v46, v46, v48, v49\n\t" \
    "v_permlane32_swap_b32 v44, v45\n\t" \
    "buffer_store_dwordx2 v[48:49], %[vkf], %[rsb], %[sf] offen sc1\n\t" \
    "v_permlane32_swap_b32 v40, v46\n\t" \
    "v_add_u32 v40, v40, v46\n\t" \
    "v_mov_b32 v46, v40\n\t" \
    "v_cndmask_b32_e64 v45, v45, %[tag], %[hi]\n\t" \
    "s_nop 0\n\t" \
    "v_permlane16_swap_b32 v40, v46\n\t" \
    "v_add_u32 v40, v40, v46\n\t" \
    "s_nop 1\n\t" \
    "v_add_u32_dpp v40, v40, v40 row_ror:8 row_mask:0xf bank_mask:0xf bound_ctrl:1\n\t" \
    "s_nop 1\n\t" \
    "v_add_u32_dpp v40, v40, v40 row_ror:4 row_mask:0xf bank_mask:0xf bound_ctrl:1\n\t" \
    "s_nop 1\n\t" \
    "v_add_u32_dpp v40, v40, v40 row_ror:2 row_mask:0xf bank_mask:0xf bound_ctrl:1\n\t" \
    "s_nop 1\n\t" \
    "v_add_u32_dpp v40, v40, v40 row_ror:1 row_mask:0xf bank_mask:0xf bound_ctrl:1\n\t" \
    "s_nop 0\n\t" \
    "v_readfirstlane_b32 s81, v40\n\t" \
    "v_cndmask_b32_e64 v44, v44, v40, %[hi]\n\t" \
    "s_or_b32 %[st], s80, s81\n\t" \
    "s_cselect_b32 s82, 0x70000000, %[sg]\n\t" \
    "buffer_store_dwordx2 v[44:45], %[vfm], %[rsb], s82 offen sc1\n\t"
#define RED2_ASM \
    "s_mov_b32 s80, 256\n\t" \
    "s_add_u32 s81, %[s0], 0x1000\n\t" \
    "s_add_u32 s84, %[s0], 0x4000\n\t" \
    "s_mov_b32 m0, 160256\n\t" \
    ".Lr_u:\n\t" \
    "s_memrealtime s[82:83]\n\t" \
    "s_sub_u32 s80, s80, 1\n\t" \
    "s_cbranch_scc1 .Lr_g2\n\t" \
    "s_waitcnt lgkmcnt(0)\n\t" \
    "s_sub_u32 s82, %[t2], s82\n\t" \
    "s_cmp_lt_i32 s82, 5\n\t" \
    "s_cbranch_scc1 .Lr_g2\n\t" \
    "s_cmp_lt_i32 s82, 13\n\t" \
    "s_cbranch_scc1 .Lr_u\n\t" \
    "s_sleep 2\n\t" \
    "s_branch .Lr_u\n\t" \
    ".Lr_g2:\n\t" \
    "buffer_load_dwordx4 %[v16], %[rsb], s84 offen sc1 lds\n\t" \
    "s_mov_b32 m0, 151552\n\t" \
    "s_nop 0\n\t" \
    "buffer_load_dwordx4 %[v16], %[rsb], %[s0] offen sc1 lds\n\t" \
    "buffer_load_dwordx4 %[v16], %[rsb], %[s0] offen offset:1024 sc1 lds\n\t" \
    "buffer_load_dwordx4 %[v16], %[rsb], %[s0] offen offset:2048 sc1 lds\n\t" \
    "buffer_load_dwordx4 %[v16], %[rsb], %[s0] offen offset:3072 sc1 lds\n\t" \
    "s_mov_b32 m0, 155648\n\t" \
    "s_nop 0\n\t" \
    "buffer_load_dwordx4 %[v16], %[rsb], s81 offen sc1 lds\n\t" \
    "buffer_load_dwordx4 %[v16], %[rsb], s81 offen offset:1024 sc1 lds\n\t" \
    "buffer_load_dwordx4 %[v16], %[rsb], s81 offen offset:2048 sc1 lds\n\t" \
    "buffer_load_dwordx4 %[v16], %[rsb], s81 offen offset:3072 sc1 lds\n\t" \
    "ds_write_b32 %[ra], %[pv]\n\t" \
    "v_or_b32 %[pv], 1, %[pv]\n\t" \
    "v_add_u32 %[pv], 1, %[pv]\n\t" \
    "s_waitcnt vmcnt(0)\n\t" \
    "ds_write_b32 %[ra], %[pv]\n\t" \
    "ds_read_b128 v[0:3], %[kwa] offset:8704\n\t" \
    "ds_read_b128 v[4:7], %[kwa]\n\t" \
    "ds_read_b128 v[8:11], %[kwa] offset:1024\n\t" \
    "ds_read_b128 v[12:15], %[kwa] offset:2048\n\t" \
    "ds_read_b128 v[16:19], %[kwa] offset:3072\n\t" \
    "ds_read_b128 v[20:23], %[kwa] offset:4096\n\t" \
    "ds_read_b128 v[24:27], %[kwa] offset:5120\n\t" \
    "ds_read_b128 v[28:31], %[kwa] offset:6144\n\t" \
    "ds_read_b128 v[32:35], %[kwa] offset:7168\n\t" \
    "s_waitcnt lgkmcnt(8)\n\t" \
    "v_add3_u32 v40, v0, v1, v2\n\t" \
    "v_xor_b32 v41, %[tag], v1\n\t" \
    "v_and_b32 v42, %[mk1], v0\n\t" \
    "v_add_u32 v40, v40, v3\n\t" \
    "v_and_b32 v41, %[mk1], v41\n\t" \
    "v_and_b32 v40, %[mk0], v40\n\t" \
    "v_cmp_ne_u32 vcc, 0, v41\n\t" \
    "v_sub_u32 v40, v40, v42\n\t" \
    "v_mov_b32 v42, %[frc]\n\t" \
    "s_waitcnt lgkmcnt(4)\n\t" \
    "v_add3_u32 v40, v4, v5, v40\n\t" \
    "v_add3_u32 v42, v6, v7, v42\n\t" \
    "v_add3_u32 v40, v8, v9, v40\n\t" \
    "v_add3_u32 v42, v10, v11, v42\n\t" \
    "v_add3_u32 v40, v12, v13, v40\n\t" \
    "v_add3_u32 v42, v14, v15, v42\n\t" \
    "v_add3_u32 v40, v16, v17, v40\n\t" \
    "v_add3_u32 v42, v18, v19, v42\n\t" \
    "s_waitcnt lgkmcnt(0)\n\t" \
    "v_add3_u32 v40, v20, v21, v40\n\t" \
    "v_add3_u32 v42, v22, v23, v42\n\t" \
    "v_add3_u32 v40, v24, v25, v40\n\t" \
    "v_add3_u32 v42, v26, v27, v42\n\t" \
    "v_add3_u32 v40, v28, v29, v40\n\t" \
    "v_add3_u32 v42, v30, v31, v42\n\t" \
    "v_add3_u32 v40, v32, v33, v40\n\t" \
    "v_add3_u32 v42, v34, v35, v42\n\t" \
    "s_or_b32 s80, vcc_lo, vcc_hi\n\t" \
    "s_nop 0\n\t" \
    "v_permlane32_swap_b32 v40, v42\n\t" \
    "v_add_u32 v40, v40, v42\n\t" \
    "s_nop 1\n\t" \
    "v_add_u32_dpp v40, v40, v40 row_ror:8 row_mask:0xf bank_mask:0xf bound_ctrl:1\n\t" \
    "s_nop 1\n\t" \
    "v_add_u32_dpp v40, v40, v40 row_ror:4 row_mask:0xf bank_mask:0xf bound_ctrl:1\n\t" \
    "s_nop 1\n\t" \
    "v_add_u32_dpp v40, v40, v40 row_ror:2 row_mask:0xf bank_mask:0xf bound_ctrl:1\n\t" \
    "s_nop 1\n\t" \
    "v_add_u32_dpp v40, v40, v40 row_ror:1 row_mask:0xf bank_mask:0xf bound_ctrl:1\n\t" \
    "s_nop 0\n\t" \
    "v_readlane_b32 s81, v40, 0\n\t" \
    "v_readlane_b32 s82, v40, 16\n\t" \
    "v_readlane_b32 s83, v40, 32\n\t" \
    "v_readlane_b32 s84, v40, 48\n\t" \
    "s_add_u32 s81, s81, s82\n\t" \
    "s_add_u32 s83, s83, s84\n\t" \
    "s_add_u32 s81, s81, s83\n\t" \
    "s_or_b32 %[st], s80, s81\n\t" \
    "s_cselect_b32 s81, 0, 1\n\t" \
    "v_add_u32 v41, s81, %[pv]\n\t" \
    "ds_write_b32 %[ra], v41\n\t"
#define PHASE_AB_CLOB "v0", "v1", "v2", "v3", "v4", "v5", "v6", "v7", "v8", "v9", "v10", "v11", "v12", "v13", "v14", "v15", "v16", "v17", "v18", "v19", "v20", "v21", "v22", "v23", "v24", "v25", "v26", "v27", "v28", "v29", "v30", "v31", "v32", "v33", "v34", "v35", "v36", "v37", "v38", "v39", "v40", "v41", "v42", "v43", "v44", "v45", "v46", "v47", "v48", "v49", "v50", "v51", "v52", "v53", "v54", "v55", "v56", "v57", "v58", "v59", "v60", "v61", "v62", "v63", "v64", "v65", "v66", "v67", "v68", "v69", "v70", "v71", "v72", "v73", "v74", "v75", "v76", "v77", "v78", "v79", "v80", "v81", "v82", "v83", "v84", "v85", "v86", "v87", "v88", "v89", "v90", "v91", "v92", "v93", "v94", "v95", "v96", "v97", "v98", "v99", "v100", "v101", "v102", "v103", "v104", "v105", "v106", "v107", "v108", "v109", "v110", "v111", "v112", "v113", "v114", "v115", "v116", "v117", "v118", "v119", "v120", "v121", "v122", "v123", "v124", "v125", "v126", "v127", "v128", "v129", "v130", "v131", "v132", "v133", "v134", "v135", "v136", "v137", "v138", "v139", "v140", "v141", "v142", "v143", "v144", "v145", "v146", "v147", "v148", "v149", "v150", "v151", "v152", "v153", "v154", "v155", "v156", "v157", "v158", "v159", "v160", "v161", "v162", "v163", "v164", "v165", "v166", "v167", "v168", "v169", "v170", "v171", "v172", "v173", "v174", "v175", "v176", "v177", "v178", "v179", "v180", "v181", "v182", "v183", "v184", "v185", "v186", "v187", "v188", "v189", "v190", "v191", "v192", "v193", "v194", "v195", "v196", "v197", "v198", "v199", "v200", "v201", "v202", "v203", "v204", "v205", "v206", "v207", "v208", "v209", "v210", "v211", "v212", "v213", "v214", "v215", "v216", "v217", "v218", "v219", "v220", "v221", "v222", "v223", "v224", "v225", "v226", "v227", "v228", "v229", "v230", "v231", "v237", "v238", "s81", "s82", "s83", "s84", "s85", "s86", "s87", "s88", "s89", "s90", "s91", "s92", "s93", "s94", "s95"
#define PHASE_C_CLOB "v0", "v1", "v2", "v3", "v4", "v5", "v6", "v7", "v8", "v9", "v10", "v11", "v12", "v13", "v14", "v15", "v16", "v17", "v18", "v19", "v20", "v21", "v22", "v23", "v24", "v25", "v26", "v27", "v28", "v29", "v30", "v31", "v32", "v33", "v34", "v35", "v36", "v37", "v38", "v39", "v40", "v41", "v42", "v43", "v44", "v45", "v46", "v47", "v48", "v49", "v50", "v51", "v52", "v53", "v54", "v55", "v56", "v57", "v58", "v59", "v60", "v61", "v62", "v63", "v64", "v65", "v66", "v67", "v68", "v69", "v70", "v71", "v72", "v73", "v74", "v75", "v76", "v77", "v78", "v79", "v80", "v81", "v82", "v83", "v84", "v85", "v86", "v87", "v88", "v89", "v90", "v91", "v92", "v93", "v94", "v95", "v96", "v97", "v98", "v99", "v100", "v101", "v102", "v103", "v104", "v105", "v106", "v107", "v108", "v109", "v110", "v111", "v112", "v113", "v114", "v115", "v116", "v117", "v118", "v119", "v120", "v121", "v122", "v123", "v124", "v125", "v126", "v127", "v128", "v129", "v130", "v131", "v132", "v133", "v134", "v135", "v136", "v137", "v138", "v139", "v140", "v141", "v142", "v143", "v144", "v145", "v146", "v147", "v148", "v149", "v150", "v151", "v152", "v153", "v154", "v155", "v156", "v157", "v158", "v159", "v160", "v161", "v162", "v163", "v164", "v165", "v166", "v167", "v168", "v169", "v170", "v171", "v172", "v173", "v174", "v175", "v176", "v177", "v178", "v179", "v180", "v181", "v182", "v183", "v184", "v185", "v186", "v187", "v188", "v189", "v190", "v191", "v192", "v193", "v194", "v195", "v196", "v197", "v198", "v199", "v200", "v201", "v202", "v203", "v204", "v205", "v206", "v207", "v208", "v209", "v210", "v211", "v212", "v213", "v214", "v215", "v216", "v217", "v218", "v219", "v220", "v221", "v222", "v223", "v224", "v225", "v226", "v227", "v228", "v229", "v231", "s80", "s81", "s82", "s83", "s84", "s85", "s86", "s87"
#define RED1_CLOB "v0", "v1", "v2", "v3", "v4", "v5", "v6", "v7", "v8", "v9", "v10", "v11", "v12", "v13", "v14", "v15", "v16", "v17", "v18", "v19", "v20", "v21", "v22", "v23", "v24", "v25", "v26", "v27", "v28", "v29", "v30", "v31", "v32", "v33", "v34", "v35", "v36", "v37", "v38", "v39", "v40", "v41", "v42", "v43", "v44", "v45", "v46", "v47", "v48", "v49", "s80", "s81", "s82", "s83", "s84"

#define KNAME "fused_fwd"
#define CODE_FETCH(dst, kb) do { unsigned long long pc_; unsigned len_, off_; \
    asm volatile("s_getpc_b64 %0\n1:\n\ts_mov_b32 %1, 1b-" KNAME "\n\ts_mov_b32 %2, .Lff_end0-" KNAME "\n\ts_max_u32 %2, %2, .Lff_end1-" KNAME \
                 "\n\ts_max_u32 %2, %2, .Lff_end2-" KNAME : "=s"(pc_), "=s"(off_), "=s"(len_) :: "scc"); \
    const rsrc_t rsC_ = mk_rsrc((const void*)(pc_ - off_), len_); \
    asm volatile("s_nop 4\n\tbuffer_load_dwordx4 %0, %1, %2, %3 offen" : "=v"(dst) : "v"(fresh_lane() * 16), "s"(rsC_), "s"((unsigned)(kb) * 1024u) : "memory"); } while (0)

extern "C" __global__ __launch_bounds__(512, 2) void fused_fwd(const u16* __restrict__ x, const u16* __restrict__ wqkv,
                                                    const u16* __restrict__ wphi, const u16* __restrict__ wo,
                                                    const u16* __restrict__ wr, const u16* __restrict__ wgp,
                                                    const u16* __restrict__ wup, const u16* __restrict__ wdp,
                                                    const u16* __restrict__ gw, char* ws, u16* out) {
    __shared__ __attribute__((aligned(16))) char smem[163840];
    const int w = __builtin_amdgcn_readfirstlane(threadIdx.x >> 6);
    if (K::TOPV) {
        asm volatile("s_mov_b64 exec, 0\n\ts_cmp_eq_u32 %0, 6\n\ts_cbranch_scc1 .Lpad_x\n\t"
                     "s_cmp_eq_u32 %0, 0\n\ts_cbranch_scc1 .Lpad_d\n\ts_cmp_eq_u32 %0, 1\n\ts_cbranch_scc1 .Lpad_ab0\n"
                     ".Lpad_top:\n\ts_mov_b64 exec, -1" :: "s"(w) : "scc", "memory");
    } else {
        asm volatile(".Lpad_top:");
    }
    asm volatile("" :: "s"(x), "s"(wqkv));
    const int bid = blockIdx.x, xcd = bid & 7, slot = bid >> 3;
    const int mtA = (xcd & 1) * 8 + (slot & 7), hpA = (xcd >> 1) * 4 + (slot >> 3);
    const int mtB = (xcd & 1) * 8 + ((xcd >> 1) & 1) * 4 + (slot & 3), hpB = (xcd >> 2) * 8 + (slot >> 2);
    const int rotA = 8 * (xcd & 1);
    const bool has_ffn = slot < 24 || (xcd & 2);
    const int fb = slot < 24 ? bid : 192 + (slot - 24) * 4 + (xcd >> 2) * 2 + (xcd & 1);
    const unsigned lds0 = (unsigned)(size_t)(AS3 char*)smem;
    const rsrc_t rsWS = mk_rsrc(ws, WS_BYTES);
    const rsrc_t rsX = mk_rsrc(x, 2048u * 2048u);

    auto touch3 = [&](unsigned& dst, int half, bool on = true) {
        const rsrc_t rsT = mk_rsrc(wqkv, (on && slot < 24 && !(xcd & 2)) ? 1024u * 6144u : 0u);
        const unsigned so = (unsigned)(((slot & 7) + 8 * half + rotA) & 15) * (64u * 6144u) + (unsigned)(slot >> 3) * 2048u + (unsigned)((xcd >> 1) * 4 + 3) * 128u;
        asm volatile("s_nop 4\n\tbuffer_load_dword %0, %1, %2, %3 offen" : "=v"(dst) : "v"(fresh_lane() * 6144), "s"(rsT), "s"(so) : "memory");
    };
    unsigned gate_t = 0;
    int gate_s = 0;
    auto hush = [&]() {
        if (gate_s == 0) {
            gate_t = __builtin_amdgcn_readfirstlane(*(AS3 volatile const unsigned*)(size_t)(lds0 + L_TM + 8)) - K::QA;
            gate_s = 1;
        }
        if (gate_s == 3 || (int)(now32() - gate_t) < 0) return;
        if (gate_s == 1) {
            wait4(lds0 + C_P, P_PUB);
            lds_wait(lds0 + C_TM, 1);
            gate_t = __builtin_amdgcn_readfirstlane(*(AS3 volatile const unsigned*)(size_t)(lds0 + L_TM)) - K::QB;
            gate_s = 2;
            if ((int)(now32() - gate_t) < 0) return;
        }
        lds_wait(lds0 + C_R, R_L1I);
        gate_s = 3;
    };

    unsigned ep, pf0;
    u32x4 xv[4][2], rv[2], gv[2], pf;
    auto ldi = [&]() {
            const int l = fresh_lane(), a = w & 1, row0 = mtB * 128 + hpB * 8 + 4 * a;
            const rsrc_t rsR = mk_rsrc(wr, 2048u), rsGW = mk_rsrc(gw, 2048u);
            const unsigned v16 = (unsigned)l * 16u;
            asm volatile("s_nop 4\n\tbuffer_load_dword %0, off, %1, 0 sc1" : "=v"(ep) : "s"(rsWS) : "memory");
#pragma unroll
            for (int r = 0; r < 4; ++r) {
                asm volatile("buffer_load_dwordx4 %0, %1, %2, %3 offen" : "=v"(xv[r][0]) : "v"(v16), "s"(rsX), "s"((unsigned)(row0 + r) * 2048u) : "memory");
                asm volatile("buffer_load_dwordx4 %0, %1, %2, %3 offen offset:1024" : "=v"(xv[r][1]) : "v"(v16), "s"(rsX), "s"((unsigned)(row0 + r) * 2048u) : "memory");
            }
            asm volatile("buffer_load_dwordx4 %0, %1, %2, 0 offen" : "=v"(rv[0]) : "v"(v16), "s"(rsR) : "memory");
            asm volatile("buffer_load_dwordx4 %0, %1, %2, 0 offen offset:1024" : "=v"(rv[1]) : "v"(v16), "s"(rsR) : "memory");
            asm volatile("buffer_load_dwordx4 %0, %1, %2, 0 offen" : "=v"(gv[0]) : "v"(v16), "s"(rsGW) : "memory");
            asm volatile("buffer_load_dwordx4 %0, %1, %2, 0 offen offset:1024" : "=v"(gv[1]) : "v"(v16), "s"(rsGW) : "memory");
            {
                const rsrc_t rsP = mk_rsrc(wphi, 32u * 2048u);
                const unsigned vp = (unsigned)(l ^ (((l >> 4) & 1) << 1)) * 16u;
#pragma unroll
                for (int i = 0; i < 2; ++i) dma16(rsP, vp, (unsigned)hpA * 4096u + a * 2048u + i * 1024u, lds0 + L_WP + a * 2048 + i * 1024);
            }
            CODE_FETCH(pf, slot * 2 + a);
            touch3(pf0, 0, a == 0);
    };
    if (__builtin_expect((w >> 1) == 2, 1)) {
        const int d = w - 4;
        LANE_VARS;
        const int srow = l >> 3, pos = l & 7;
        const rsrc_t rsW = mk_rsrc(wqkv, 1024u * 6144u);
        const unsigned vx0 = srow * 2048 + ((pos ^ ((srow >> 1) & 7)) << 4);
        const unsigned vw0 = srow * 6144 + (((((pos >> 1) ^ (srow & 3)) << 1) | (pos & 1)) << 4);
        unsigned vxs[4], vws[4];
#pragma unroll
        for (int i = 0; i < 4; ++i) {
            vxs[i] = (vx0 ^ ((i & 1) * 64u)) + i * (16384u - 1024u);
            vws[i] = (vw0 ^ ((i & 1) * 32u)) + i * (8u * 6144u - 1024u);
        }
        const unsigned sx0 = (unsigned)(mtA * 128) * 2048u, sw0 = (unsigned)hpA * 128u;
        const int lq = ((slot >> 3) - slot) & 3;
        const unsigned ktab = lq == 0 ? K::KT0 : lq == 1 ? K::KT1 : lq == 2 ? K::KT2 : K::KT3;
        auto chunk = [&](int kt) { return (((ktab >> (4 * (kt >> 1))) & 7u) * 2u + (unsigned)((kt & 1) + rotA)) & 15u; };
        auto dx = [&](unsigned kk, int i, unsigned la) { dma4<K::PACE>(rsX, vxs, sx0 + kk * 128u + i * 65536u, lds0 + la + i * 4096); };
        auto dw = [&](unsigned kk, int p, int i, unsigned la) { dma4<K::PACE>(rsW, vws, sw0 + p * 2048u + (kk * 64u + i * 32u) * 6144u, lds0 + la + i * 4096); };
        asm volatile("s_nop 4" ::: "memory");
        unsigned lb = 0;
#pragma unroll 1
        for (int kt = 0; kt < 13; ++kt) {
            if (kt >= 2) {
                if (kt <= 10) wait_vm<20>(); else wait_vm<16>();
                bar();
            }
            const unsigned kk = chunk(kt);
            dx(kk, 2 * d, lb);
            dw(kk, 1 + d, 0, lb + 24576 + d * 8192);
            dx(kk, 2 * d + 1, lb);
            dw(kk, 1 + d, 1, lb + 24576 + d * 8192);
            if (kt < 10) dw(kk, 0, d, lb + 16384);
            lb = lb == 2 * SLOT ? 0u : lb + SLOT;
        }
        auto dxs = [&](int i) {
            const unsigned kk = chunk(13 + i), lx = d ? XS1[i] - 8192u : XS0[i];
            dx(kk, 2 * d, lx);
            dx(kk, 2 * d + 1, lx);
        };
        auto dws = [&](int i) {
            const unsigned kk = chunk(13 + i), lw = ((1 + i) % 3) * SLOT + 24576 + d * 8192;
            dw(kk, 1 + d, 0, lw);
            dw(kk, 1 + d, 1, lw);
        };
        auto dq = [&](int i) {
            const unsigned kk = chunk(i < 2 ? 14 + i : 8 + i);
            dw(kk, 0, 0, QW[i]);
            dw(kk, 0, 1, QW[i]);
        };
        if (K::XAH) dxs(0);
        wait_vm<K::XAH ? 24 : 16>();
        bar();
        dws(0);
        dxs(K::XAH ? 1 : 0);
        wait_vm<K::XAH ? 24 : 16>();
        bar();
        dws(1);
        dxs(K::XAH ? 2 : 1);
        wait_vm<K::XAH ? 24 : 16>();
        bar();
        dws(2);
        if (!K::XAH) dxs(2);
        if (d == 0) {
            dq(0);
            wait_vm<24>();
        } else {
            wait_vm<16>();
        }
        bar();
        if (d == 0) wait_vm<8>(); else wait_vm<0>();
        post(lds0 + C_ST + d * 4, 1);
        __builtin_amdgcn_s_setprio(3);
        if (d == 0) {
            const rsrc_t rsO = mk_rsrc(wo, 1024u * 2048u);
            unsigned vos[4];
#pragma unroll
            for (int i = 0; i < 4; ++i) vos[i] = WO_VOFF + i * (16384u - 1024u);
            unsigned sb[2] = {0u, 0u};
            auto wo_run = [&](int b0, int b1) {
#pragma unroll
                for (int b = b0; b < b1; ++b) {
                    unsigned so;
                    asm volatile("s_mov_b32 m0, %7\n\ts_add_u32 %0, %6, %8\n\tbuffer_load_dwordx4 %1, %5, %0 offen lds\n\tbuffer_load_dwordx4 %2, %5, %0 offen offset:1024 lds\n\t"
                                 "buffer_load_dwordx4 %3, %5, %0 offen offset:2048 lds\n\tbuffer_load_dwordx4 %4, %5, %0 offen offset:3072 lds\n\ts_waitcnt vmcnt(%9)"
                                 : "=&s"(so) : "v"(vos[0]), "v"(vos[1]), "v"(vos[2]), "v"(vos[3]), "s"(rsO), "s"(sb[b >> 4]), "i"(b * 4096), "i"((b & 15) * 65536), "i"(K::WO_D)
                                 : "memory", "scc");
                }
            };
            dq(1);
            dq(2);
            wait4(lds0 + C_P, P_E15);
            dq(3);
            dq(4);
            wait_vm<0>();
            post(lds0 + C_Q, 1);
            wait4(lds0 + C_P, P_KVD);
            dq(5);
            wait_vm<0>();
            post(lds0 + C_Q, 2);
            wait4(lds0 + C_P, P_PUB);
            {
                const unsigned s0 = (unsigned)(mtB * 128) * 2048u + (unsigned)hpB * 128u;
#pragma unroll
                for (int i = 0; i < 4; ++i) dma4<0>(rsX, vxs, s0 + i * 65536u, lds0 + L_XE + i * 4096);
            }
            lds_wait(lds0 + C_TM, 1);
            sb[1] = (__builtin_amdgcn_readfirstlane(*(AS3 volatile const unsigned*)(size_t)(lds0 + L_TM + 4)) & 1u) * (16u * 65536u) + (unsigned)hpB * 128u;
            sb[0] = sb[1] ^ (16u * 65536u);
            asm volatile("s_nop 4" : "+s"(sb[0]), "+s"(sb[1]));
            wo_run(26, 32);
            post(lds0 + C_XE, 1);
            wait4(lds0 + C_P, P_Q1 + 2);
            wo_run(16, 24);
            wait4(lds0 + C_P, P_QRD);
            wo_run(24, 26);
            wo_run(0, 6);
            post(lds0 + C_WO, 2);
            wo_run(6, 14);
            post(lds0 + C_WO, 3);
            wo_run(14, 16);
            wait_vm<0>();
            post(lds0 + C_WO, 4);
        } else {
            const unsigned v16 = (unsigned)l * 16u;
            const unsigned tag = *(AS3 const unsigned*)(size_t)(lds0 + L_TAG);
            const rsrc_t rsB = mk_rsrc(bulk_base(ws), WB_BYTES);
            lds_wait(lds0 + C_IDX, 1);
            const u32x2 pl = *(AS3 const u32x2*)(size_t)(lds0 + L_PLC);
            const unsigned okp = __builtin_amdgcn_readfirstlane(pl[0]), osm = __builtin_amdgcn_readfirstlane(pl[1]);
            unsigned t1 = 0;
            const unsigned base = now32() - 4096u;
            u32x4 p0, p1;
            CODE_FETCH(p0, slot * 2);
            CODE_FETCH(p1, slot * 2 + 1);
            const int ch = l & 3;
            unsigned mk01 = ch < 2 ? ~0u : 0u, mk0 = ch == 0 ? ~0u : 0u, mk1 = ch == 1 ? ~0u : 0u, mk2 = ch == 2 ? ~0u : 0u, mkl = (l & 31) < 2 ? ~0u : 0u;
            unsigned vkf = (unsigned)((g >> 1) * 16 + c) * 16u + (unsigned)(g & 1) * 8u, vfm = l < 2 ? (unsigned)l * 8u : l == 32 ? 16u : 0x80000000u;
            unsigned kwa = lds0 + L_KVF + l * 16, ra = lds0 + C_R;
            asm volatile("" : "+v"(mk01), "+v"(mk0), "+v"(mk1), "+v"(mk2), "+v"(mkl), "+v"(vkf), "+v"(vfm), "+v"(kwa), "+v"(ra));
            bf16x8 sel;
#pragma unroll
            for (int j = 0; j < 8; ++j) sel[j] = ((c & 7) == j && (c >> 3) == (g & 1)) ? (short)0x3f80 : (short)0;
            asm volatile("" : "+v"(sel));
            {
                unsigned ta, tb;
                auto latest = [&](unsigned off, bool& all) {
                    const u32x4 pa = ld128c(rsWS, v16, off), pb = ld128c(rsWS, v16 + 1024u, off);
                    const unsigned ma = max(pa[1] == tag ? min(pa[0] - base, 8192u) : 0u, pa[3] == tag ? min(pa[2] - base, 8192u) : 0u);
                    const unsigned mb = max(pb[1] == tag ? min(pb[0] - base, 8192u) : 0u, pb[3] == tag ? min(pb[2] - base, 8192u) : 0u);
                    all = __all(pa[1] == tag && pa[3] == tag && pb[1] == tag && pb[3] == tag);
                    ta = __builtin_amdgcn_readfirstlane(wave_umax(ma));
                    tb = __builtin_amdgcn_readfirstlane(wave_umax(mb));
                    return base + max(ta, tb);
                };
                bool all;
                wait4(lds0 + C_P, has_ffn ? K::TMW : P_PUB);
#pragma unroll 1
                for (int it = 0; it < 4; ++it) {
                    t1 = latest(WS_PT, all) + K::D1;
                    if (all) break;
                    wait4(lds0 + C_P, P_PUB);
                    __builtin_amdgcn_s_sleep(K::PT_S);
                }
                asm volatile("s_waitcnt vmcnt(0)" : "+v"(p0), "+v"(p1) :: "memory");
                post(lds0 + L_TM, t1);
                const unsigned t3 = t1 + (K::D2 + K::D3);
                post(lds0 + L_TM + 4, (t3 & ~1u) | (tb < ta ? 1u : 0u));
                post(lds0 + C_TM, 1);
                asm volatile("s_branch .Lpad_r\n\t.p2align 12\n.Lpad_r:\n\ts_cbranch_execz .Lpad_ret" ::: "memory");
                wait4(lds0 + C_P, P_PUB);
            }
            bool late = false;
#pragma unroll 1
            for (int it = 0; it < TRY_LIMIT; ++it) {
                unsigned bad;
                asm volatile(RED1_ASM : [st] "=&s"(bad)
                             : [rsb] "s"(rsB), [t1] "s"(t1), [s0] "s"(okp ^ ((unsigned)mtA * 16384u)), [sm] "s"(osm + (unsigned)((mtA >> 3) * 16384 + (mtA & 7) * 1024)),
                               [sf] "s"(osm + 32768u + (unsigned)mtA * 512u), [sg] "s"(osm + 49152u + (unsigned)mtA * 64u), [hi] "s"(0xffffffff00000000ull),
                               [v16] "v"(v16), [ra] "v"(ra), [tag] "v"(tag), [mk01] "v"(mk01), [mk2] "v"(mk2), [mkl] "v"(mkl), [sel] "v"(sel), [vkf] "v"(vkf), [vfm] "v"(vfm)
                             : "memory", "vcc", "scc", RED1_CLOB);
                if (__builtin_expect(!bad, 1)) break;
                late = true;
                if (it < K::RE1) continue;
                int i2 = 0;
#pragma unroll 1
                for (; i2 < POLL_LIMIT; ++i2) {
                    __builtin_amdgcn_s_sleep(K::RETRY_S);
                    fence_c();
                    if (__all(ld32c(rsB, (unsigned)c * 64u + 36u, osm + (unsigned)((mtA >> 3) * 16384 + (mtA & 7) * 1024)) == tag)) break;
                }
                if (i2 == POLL_LIMIT) break;
            }
            post(lds0 + C_R, R_L1);
            wait4(lds0 + C_P, P_Q1);
            if (late) __builtin_amdgcn_s_sleep(K::L2_S);
            unsigned pv = R_L2I;
#pragma unroll 1
            for (int it = 0; it < TRY_LIMIT; ++it) {
                unsigned bad;
                asm volatile(RED2_ASM : [st] "=&s"(bad), [pv] "+v"(pv)
                             : [rsb] "s"(rsB), [t2] "s"(t1 + K::D2), [s0] "s"(osm + 32768u), [frc] "s"(K::FORCE_REDO && it == 0 ? 1 : 0),
                               [v16] "v"(v16), [ra] "v"(ra), [kwa] "v"(kwa), [tag] "v"(tag), [mk0] "v"(mk0), [mk1] "v"(mk1)
                             : "memory", "vcc", "scc", RED1_CLOB);
                if (__builtin_expect(!bad, 1)) break;
                if (it < K::RE1) continue;
                int i2 = 0;
#pragma unroll 1
                for (; i2 < POLL_LIMIT; ++i2) {
                    __builtin_amdgcn_s_sleep(K::RETRY_S);
                    fence_c();
                    if (__all(ld32c(rsB, (unsigned)c * 64u + 20u, osm + 49152u) == tag)) break;
                }
                if (i2 == POLL_LIMIT) break;
            }
            post(ra, pv + 1u);
            asm volatile(".Lff_end1:");
        }
        return;
    }

    asm volatile(".Lpad_d:\n\ts_cbranch_execz .Lpad_top" ::: "memory");
    __builtin_amdgcn_sched_barrier(0);
    if (w >= 6) {
        const int a = w - 6;
        const unsigned tdbg = now32();
        unsigned vdbg = 0;
        const int cb = fb >> 2, kq = fb & 3;
        u32x4 wg_[16], wu_[16], wd_[16];
        unsigned tag;
        int idx;
        u32x4 xs[2];
        {
        LANE_VARS;
        const int srow = l >> 3, pos = l & 7;
        const unsigned nb = has_ffn ? 1024u * 7168u : 0u;
        const rsrc_t rsG = mk_rsrc(wgp, nb), rsU = mk_rsrc(wup, nb), rsD = mk_rsrc(wdp, nb);
        const unsigned vf = (unsigned)(16 * srow) * 7168u + (unsigned)pos * 16u;
        const int k8 = fb & 7, cg = (fb >> 4) * 4 + ((fb >> 3) & 1) * 2 + a, cu = cg ^ 2;
        const unsigned sg = (unsigned)(k8 * 128) * 7168u + (unsigned)cg * 128u, su = sg ^ 256u;
        auto ldg = [&]() {
#pragma unroll
            for (int i = 0; i < 16; ++i) {
                asm volatile("buffer_load_dwordx4 %0, %1, %2, %3 offen nt" : "=v"(wg_[i]) : "v"(vf), "s"(rsG), "s"(sg + i * 7168u) : "memory");
            }
        };
        auto ldu = [&]() {
#pragma unroll
            for (int i = 0; i < 16; ++i) {
                asm volatile("buffer_load_dwordx4 %0, %1, %2, %3 offen nt" : "=v"(wu_[i]) : "v"(vf), "s"(rsU), "s"(su + i * 7168u) : "memory");
            }
        };
        auto ldd = [&](int i0, int i1) {
            const unsigned vd = (unsigned)g * (16u * 2048u) + (unsigned)c * 16u;
            const unsigned sd = (unsigned)(cb * 64) * 2048u + (unsigned)(kq * 512 + a * 256);
#pragma unroll
            for (int i = i0; i < i1; ++i) {
                if ((i & 3) == 0) { wait_vm<K::FD>(); hush(); }
                asm volatile("buffer_load_dwordx4 %0, %1, %2, %3 offen nt" : "=v"(wd_[i]) : "v"(vd), "s"(rsD), "s"(sd + i * 2048u) : "memory");
            }
        };
        const int row0 = mtB * 128 + hpB * 8 + 4 * a;
        if (a == 0) {
            *(AS3 unsigned*)(size_t)(lds0 + L_SM + l * 4) = 0u;
            wait_lgkm0();
        }
        int b = 0;
        unsigned tl = 0, pmin = 255;
        auto tick = [&]() {
            bar();
            if (a == 1) {
                const unsigned t = now32();
                if (b > 2 && b <= 16 - K::ND) pmin = min(pmin, t - tl);
                tl = t;
            }
            ++b;
        };
        auto upto = [&](int e) {
#pragma unroll 1
            while (b <= e) tick();
        };
        auto pads = [&]() {
#pragma unroll 1
            for (int i = a; i < 6; i += 2) {
                unsigned long long ex;
                asm volatile("s_mov_b64 %0, exec\n\ts_mov_b64 exec, 0\n\t"
                             "s_cmp_eq_u32 %1, 0\n\ts_cbranch_scc1 .Lpad_ab1\n\ts_cmp_eq_u32 %1, 1\n\ts_cbranch_scc1 .Lpad_ab2\n\t"
                             "s_cmp_eq_u32 %1, 2\n\ts_cbranch_scc1 .Lpad_c0\n\ts_cmp_eq_u32 %1, 3\n\ts_cbranch_scc1 .Lpad_c1\n\t"
                             "s_cmp_eq_u32 %1, 4\n\ts_cbranch_scc1 .Lpad_r\n"
                             ".Lpad_ret:\n\ts_mov_b64 exec, %0" : "=&s"(ex) : "s"(i) : "scc", "memory");
                if (i < 4 && b <= 9) tick();
            }
        };
        auto nap = [&](int n) {
#pragma unroll 1
            for (int i = 0; i < n; ++i) __builtin_amdgcn_s_sleep(1);
        };
        const int lq4 = ((((slot >> 3) - slot) & 3) + (slot & 4)) * 4;
        const int bg = ((unsigned)K::BGT >> lq4) & 15, bu = ((unsigned)K::BUT >> lq4) & 15;
        unsigned pf3 = 0;
        ldi();
        u32x2 gr;
        unsigned vgr;
        auto router = [&](auto n_) {
            tick();
            asm volatile("s_waitcnt vmcnt(%15)"
                         : "+v"(ep), "+v"(xv[0][0]), "+v"(xv[0][1]), "+v"(xv[1][0]), "+v"(xv[1][1]), "+v"(xv[2][0]), "+v"(xv[2][1]), "+v"(xv[3][0]),
                           "+v"(xv[3][1]), "+v"(rv[0]), "+v"(rv[1]), "+v"(gv[0]), "+v"(gv[1]), "+v"(pf), "+v"(pf0)
                         : "i"(decltype(n_)::value) : "memory");
            tag = __builtin_amdgcn_readfirstlane(ep) * 2654435761u + 0x9e3779b9u;
            float ra[4], ga[4];
#pragma unroll
            for (int r = 0; r < 4; ++r) {
                float racc = 0.f, gacc = 0.f;
#pragma unroll
                for (int i = 0; i < 2; ++i)
#pragma unroll
                    for (int j = 0; j < 4; ++j) {
                        racc = dot2(xv[r][i][j], rv[i][j], racc);
                        gacc = dot2(xv[r][i][j], gv[i][j], gacc);
                    }
                ra[r] = racc;
                ga[r] = gacc;
                asm volatile("" : "+v"(ra[r]), "+v"(ga[r]));
            }
            tick();
            float r2[2], g2[2];
#pragma unroll
            for (int i = 0; i < 2; ++i) {
                auto p = __builtin_amdgcn_permlane32_swap(FU(ra[2 * i]), FU(ra[2 * i + 1]), false, false);
                r2[i] = UF(p[0]) + UF(p[1]);
                auto q = __builtin_amdgcn_permlane32_swap(FU(ga[2 * i]), FU(ga[2 * i + 1]), false, false);
                g2[i] = UF(q[0]) + UF(q[1]);
            }
            auto p = __builtin_amdgcn_permlane16_swap(FU(r2[0]), FU(r2[1]), false, false);
            auto q = __builtin_amdgcn_permlane16_swap(FU(g2[0]), FU(g2[1]), false, false);
            float rs = row16_sum(UF(p[0]) + UF(p[1])), gs = row16_sum(UF(q[0]) + UF(q[1]));
            asm volatile("" : "+v"(rs), "+v"(gs));
            const int row = row0 + (g >> 1) + 2 * (g & 1);
            const unsigned rbits = f2bf(rs);
            const unsigned okey = (rbits & 0x8000u) ? (~rbits & 0xffffu) : (rbits | 0x8000u);
            if (c == 0) *(AS3 unsigned*)(size_t)(lds0 + L_KEY + (4 * a + g) * 4) = (okey << 16) | (unsigned)(0xffff - row);
            if (l == 0 && a == 0) *(AS3 unsigned*)(size_t)(lds0 + L_TAG) = tag;
            gr = u32x2{FU(rbf(1.f / (1.f + __expf(-rbf(gs))))), tag};
            vgr = c == 0 ? WS_G + (unsigned)row * 8u : 0x80000000u;
            asm volatile("" : "+v"(gr), "+v"(vgr));
            wait_lgkm0();
            tick();
            if (a == 0) {
                unsigned k8 = *(AS3 const unsigned*)(size_t)(lds0 + L_KEY + (l & 7) * 4);
                k8 = max(k8, (unsigned)__shfl_xor((int)k8, 1));
                k8 = max(k8, (unsigned)__shfl_xor((int)k8, 2));
                k8 = max(k8, (unsigned)__shfl_xor((int)k8, 4));
                const u32x4 pv = *(AS3 const u32x4*)(size_t)(lds0 + L_PRB);
                st64c(u32x2{k8 ^ (((((pv[1] < pv[0] ? 1u : 0u) ^ slot) & 1u) + (((pv[3] < pv[2] ? 1u : 0u) ^ slot) & 1u)) << 11), tag}, rsWS, l ? 0x80000000u : WS_RG + (unsigned)bid * 8u, 0);
            }
        };
        u32x4 ga, gb;
        const unsigned vr = WS_RG + (unsigned)l * 16u;
        auto ldrg = [&]() {
            asm volatile("buffer_load_dwordx4 %0, %1, %2, 0 offen sc1" : "=v"(ga) : "v"(vr), "s"(rsWS) : "memory");
            asm volatile("buffer_load_dwordx4 %0, %1, %2, 0 offen offset:1024 sc1" : "=v"(gb) : "v"(vr), "s"(rsWS) : "memory");
        };
        router(std::integral_constant<int, 0>{});
        __builtin_amdgcn_s_setprio(2);
        upto(bg);
        nap(K::SLG);
        if (a == 0) {
            touch3(pf3, 1);
        } else {
            const rsrc_t rsT = mk_rsrc(wqkv, 1024u * 6144u);
            asm volatile("s_nop 4\n\tbuffer_load_dword %0, %1, %2, %3 offen" : "=v"(pf3) : "v"((unsigned)(l & 31) * 6144u), "s"(rsT),
                         "s"((unsigned)(((12 + rotA) & 15) * 64 + (slot & 7) * 32) * 6144u + (unsigned)hpA * 128u) : "memory");
        }
        ldg();
        upto(bu);
        if (bu != bg) nap(K::SLG);
        ldu();
        upto(((unsigned)K::BRT >> lq4) & 15);
        ldrg();
        pads();
        upto(9);
        asm volatile("s_waitcnt vmcnt(0)"
                     : "+v"(wg_[0]), "+v"(wg_[1]), "+v"(wg_[2]), "+v"(wg_[3]), "+v"(wg_[4]), "+v"(wg_[5]), "+v"(wg_[6]), "+v"(wg_[7]),
                       "+v"(wg_[8]), "+v"(wg_[9]), "+v"(wg_[10]), "+v"(wg_[11]), "+v"(wg_[12]), "+v"(wg_[13]), "+v"(wg_[14]), "+v"(wg_[15]),
                       "+v"(ga), "+v"(gb), "+v"(pf3)
                     :: "memory");
        asm volatile(""
                     : "+v"(wu_[0]), "+v"(wu_[1]), "+v"(wu_[2]), "+v"(wu_[3]), "+v"(wu_[4]), "+v"(wu_[5]), "+v"(wu_[6]), "+v"(wu_[7]),
                       "+v"(wu_[8]), "+v"(wu_[9]), "+v"(wu_[10]), "+v"(wu_[11]), "+v"(wu_[12]), "+v"(wu_[13]), "+v"(wu_[14]), "+v"(wu_[15])
                     :: "memory");
#pragma unroll 1
        for (int it = 0; it < POLL_LIMIT; ++it) {
            if (__all((ga[1] == tag) && (ga[3] == tag) && (gb[1] == tag) && (gb[3] == tag))) break;
            __builtin_amdgcn_s_sleep(4 * K::RETRY_S);
            fence_c();
            ga = ld128c(rsWS, vr, 0);
            gb = ld128c(rsWS, vr + 1024u, 0);
        }
        const unsigned key = __builtin_amdgcn_readfirstlane(wave_umax(max(max(ga[0] | 0x3800u, ga[2] | 0x3800u), max(gb[0] | 0x3800u, gb[2] | 0x3800u))));
        idx = (0xffff - (int)(key & 0xffffu)) & 2047;
        unsigned tw[2];
        auto twf = [&]() {
            const rsrc_t rsO = mk_rsrc(wo, 1024u * 2048u);
            const unsigned vt = (unsigned)srow * 2048u + (unsigned)pos * 128u, st = (unsigned)(slot * 32 + a * 16) * 2048u + (unsigned)(xcd >> 2) * 1024u;
            nap(K::X1);
            if (K::TNT) {
            asm volatile("s_nop 4\n\tbuffer_load_dword %0, %1, %2, %3 offen nt" : "=v"(tw[0]) : "v"(vt), "s"(rsO), "s"(st) : "memory");
            asm volatile("buffer_load_dword %0, %1, %2, %3 offen nt" : "=v"(tw[1]) : "v"(vt), "s"(rsO), "s"(st + 16384u) : "memory");
            } else {
            asm volatile("s_nop 4\n\tbuffer_load_dword %0, %1, %2, %3 offen" : "=v"(tw[0]) : "v"(vt), "s"(rsO), "s"(st) : "memory");
            asm volatile("buffer_load_dword %0, %1, %2, %3 offen" : "=v"(tw[1]) : "v"(vt), "s"(rsO), "s"(st + 16384u) : "memory");
            }
        };
        tick();
        if (K::TWB == 10) twf();
        {
            const unsigned vs = (unsigned)srow * 32u, ss = (unsigned)idx * 2048u + (unsigned)(k8 * 256);
            asm volatile("s_nop 4\n\tbuffer_load_dwordx4 %0, %1, %2, %3 offen" : "=v"(xs[0]) : "v"(vs), "s"(rsX), "s"(ss) : "memory");
            asm volatile("buffer_load_dwordx4 %0, %1, %2, %3 offen offset:16" : "=v"(xs[1]) : "v"(vs), "s"(rsX), "s"(ss) : "memory");
        }
        if (a == 1) {
            const u32x2 pt = {tl + (pmin * (K::PRED_K + 12) >> 2) + K::PRED_C + K::XE, tag};
            st64c(pt, rsWS, l ? 0x80000000u : WS_PT + (unsigned)(hpA * 16 + mtA) * 8u, 0);
        } else {
            const unsigned kb = key >> 16;
            const unsigned rb_ = (kb & 0x8000u) ? (kb & 0x7fffu) : (~kb & 0xffffu);
            const float rsel = rbf(1.f / (1.f + __expf(-UF(rb_ << 16))));
            const u32x2 iv = {(unsigned)idx, FU(rsel)};
            *(AS3 u32x2*)(size_t)(lds0 + L_IDX) = iv;
            const unsigned nv = (~ga[0] & 0x3800u) + (~ga[2] & 0x3800u) + (~gb[0] & 0x3800u) + (~gb[2] & 0x3800u);
            const unsigned votes = wave_usum((l & 3) == (xcd >> 1) ? nv >> 11 : 0u);
            vdbg = __builtin_amdgcn_readfirstlane(votes);
            const unsigned o0 = (unsigned)(xcd >> 1) * (2u << 20) + (K::X2 < 2 ? K::X2 * 8192u : votes > 64u ? 8192u : 0u);
            const u32x4 pl = {o0 + WB_KVP + (((unsigned)(hpA & 3) * 8u) ^ (tag & 31u)) * 32768u, o0 + WB_SM + (((unsigned)hpA & 3u) ^ ((tag >> 5) & 7u)) * 65536u,
                              ((tag >> 8) & 63u) * 2048u, 0u};
            *(AS3 u32x4*)(size_t)(lds0 + L_PLC) = pl;
            post(lds0 + C_IDX, 1);
            wait_lgkm0();
        }
        unsigned mk[4];
#pragma unroll
        for (int k = 0; k < 4; ++k) mk[k] = (pos >> 1) == k ? ((pos & 1) ? 0xffff0000u : 0x0000ffffu) : 0u;
        f32x4 sg4 = {0.f, 0.f, 0.f, 0.f}, su4 = sg4;
        auto dot = [&](int i0, int i1) {
#pragma unroll
            for (int i = i0; i < i1; ++i) {
                const unsigned xw = xs[i >> 3][(i >> 1) & 3];
                const unsigned xd = __builtin_amdgcn_perm(xw, xw, (i & 1) ? 0x03020302u : 0x01000100u);
                const u32x4 am = {xd & mk[0], xd & mk[1], xd & mk[2], xd & mk[3]};
                sg4 = mfma16(__builtin_bit_cast(bf16x8, am), __builtin_bit_cast(bf16x8, wg_[i]), sg4);
                su4 = mfma16(__builtin_bit_cast(bf16x8, am), __builtin_bit_cast(bf16x8, wu_[i]), su4);
            }
            asm volatile("" : "+v"(sg4), "+v"(su4));
        };
        tick();
        asm volatile("s_waitcnt vmcnt(0)" : "+v"(xs[0]), "+v"(xs[1]) :: "memory");
        if (K::TWB == 11) twf();
        dot(0, K::DS1);
        tick();
        dot(K::DS1, K::DS2);
        if (K::TWB == 12) twf();
        tick();
        if (a == 1) {
            post(lds0 + L_TM + 8, tl + (pmin * (K::PRED_K + K::PK13) >> 2) + K::PRED_C);
            wait_lgkm0();
        }
        dot(K::DS2, K::DS3);
        if (K::TWB == 13) twf();
        __builtin_amdgcn_s_setprio(3);
        tick();
        ldd(0, 8);
        dot(K::DS3, 16);
        {
            unsigned fo[4];
#pragma unroll
            for (int r = 0; r < 4; ++r) {
                auto p = __builtin_amdgcn_permlane32_swap(FU(sg4[r]), FU(su4[r]), false, false);
                fo[r] = FU(UF(p[0]) + UF(__builtin_amdgcn_update_dpp(0, (int)p[1], 0x128, 0xf, 0xf, false)));
            }
            const unsigned vf0 = ((l & 8) || !has_ffn) ? 0x80000000u : WS_FG + (unsigned)(((l >> 5) ? cu : cg) * 8 + k8) * 1024u + (unsigned)((l >> 5) * 512 + (pos * 8 + (g & 1) * 4) * 8);
            const u32x4 s0 = {fo[0], tag, fo[1], tag}, s1 = {fo[2], tag, fo[3], tag};
            st128c(s0, rsWS, vf0, 0);
            st128c(s1, rsWS, vf0 + 16u, 0);
            st64c(gr, rsWS, vgr, 0);
        }
        if (bid == 0 && a == 0 && l == 0) st32c(tag, rsWS, WS_EPOCH, 0);
        ldd(8, 16);
        lds_wait(lds0 + C_TM, 1);
        const unsigned tm0 = *(AS3 const unsigned*)(size_t)(lds0 + L_TM);
        const unsigned vq = (unsigned)((l & 8) * 64 + (g * 8 + (l & 7)) * 16);
        u32x4 fq[8];
        if (has_ffn) {
            wait_until(__builtin_amdgcn_readfirstlane(tm0) + K::FG_AT);
#pragma unroll 1
            for (int it = 0; it < POLL_LIMIT; ++it) {
                unsigned bad = 0;
#pragma unroll
                for (int i = 0; i < 8; ++i) fq[i] = ld128c(rsWS, vq + (i & 3) * 1024u, WS_FG + (unsigned)cb * 8192u + (i >> 2) * 4096u);
#pragma unroll
                for (int i = 0; i < 8; ++i) bad |= (fq[i][1] ^ tag) | (fq[i][3] ^ tag);
                if (__all(bad == 0u)) break;
                __builtin_amdgcn_s_sleep(2 * K::RETRY_S);
                fence_c();
            }
        }
        asm volatile("s_waitcnt vmcnt(0)"
                     : "+v"(wd_[0]), "+v"(wd_[1]), "+v"(wd_[2]), "+v"(wd_[3]), "+v"(wd_[4]), "+v"(wd_[5]), "+v"(wd_[6]), "+v"(wd_[7]),
                       "+v"(wd_[8]), "+v"(wd_[9]), "+v"(wd_[10]), "+v"(wd_[11]), "+v"(wd_[12]), "+v"(wd_[13]), "+v"(wd_[14]), "+v"(wd_[15]),
                       "+v"(tw[0]), "+v"(tw[1])
                     :: "memory");
        if (has_ffn) {
            if (K::DWP) lds_wait(lds0 + C_P + (2 + a) * 4, P_PUB);
            __builtin_amdgcn_s_setprio(K::HP);
            unsigned dp[8][8];
#pragma unroll
            for (int m = 0; m < 8; ++m)
#pragma unroll
                for (int j = 0; j < 4; ++j) {
                    dp[m][2 * j] = __builtin_amdgcn_perm(wd_[2 * m + 1][j], wd_[2 * m][j], 0x05040100u);
                    dp[m][2 * j + 1] = __builtin_amdgcn_perm(wd_[2 * m + 1][j], wd_[2 * m][j], 0x07060302u);
                }
            float hv[2];
#pragma unroll
            for (int j = 0; j < 2; ++j) {
                const float gs = rbf(((UF(fq[0][2 * j]) + UF(fq[1][2 * j])) + (UF(fq[2][2 * j]) + UF(fq[3][2 * j]))) +
                                     ((UF(fq[4][2 * j]) + UF(fq[5][2 * j])) + (UF(fq[6][2 * j]) + UF(fq[7][2 * j]))));
                const float us = UF(__builtin_amdgcn_update_dpp(0, (int)FU(gs), 0x128, 0xf, 0xf, false));
                hv[j] = rbf(gs / (1.f + __expf(-gs))) * us;
            }
            const unsigned hp = pack2(hv[0], hv[1]);
            float ya[8];
#pragma unroll
            for (int j = 0; j < 8; ++j) ya[j] = 0.f;
            auto dn = [&](auto mc) {
                constexpr int m = decltype(mc)::value;
                const unsigned hb = (unsigned)__builtin_amdgcn_update_dpp(0, (int)hp, 0x150 + m, 0xf, 0xf, false);
#pragma unroll
                for (int j = 0; j < 8; ++j) ya[j] = dot2(dp[m][j], hb, ya[j]);
            };
            dn(std::integral_constant<int, 0>{}); dn(std::integral_constant<int, 1>{}); dn(std::integral_constant<int, 2>{}); dn(std::integral_constant<int, 3>{});
            dn(std::integral_constant<int, 4>{}); dn(std::integral_constant<int, 5>{}); dn(std::integral_constant<int, 6>{}); dn(std::integral_constant<int, 7>{});
            float y4[4], y2[2];
#pragma unroll
            for (int j = 0; j < 4; ++j) {
                auto p = __builtin_amdgcn_permlane32_swap(FU(ya[j]), FU(ya[4 + j]), false, false);
                y4[j] = UF(p[0]) + UF(p[1]);
            }
#pragma unroll
            for (int j = 0; j < 2; ++j) {
                auto p = __builtin_amdgcn_permlane16_swap(FU(y4[j]), FU(y4[2 + j]), false, false);
                y2[j] = UF(p[0]) + UF(p[1]);
            }
            const u32x4 st = {FU(y2[0]), tag, FU(y2[1]), tag};
            st128c(st, rsWS, (unsigned)(c * 64 + g * 16), WS_Y + (unsigned)cb * 8192u + (unsigned)(kq * 2048 + a * 1024));
        }
        {
            const unsigned col8 = (unsigned)(xcd * 128 + slot * 4 + a * 2) * 8u;
            const unsigned vy = l < 56 ? (unsigned)l * 8192u : 0x80000000u;
            u32x4 t;
            __builtin_amdgcn_s_setprio(3);
            wait_until(__builtin_amdgcn_readfirstlane(tm0) + K::Y_AT);
#pragma unroll 1
            for (int it = 0; it < POLL_LIMIT; ++it) {
                t = ld128c(rsWS, vy, WS_Y + col8);
                if (__all(l >= 56 || (t[1] == tag && t[3] == tag))) break;
                __builtin_amdgcn_s_sleep(4 * K::RETRY_S);
                fence_c();
            }
            auto p = __builtin_amdgcn_permlane32_swap(t[0], t[2], false, false);
            const float s = row16_sum(x16_sum(UF(p[0]) + UF(p[1])));
            st64c(u32x2{FU(s), tag}, rsWS, (l & 31) ? 0x80000000u : (unsigned)(l >> 2), WS_YF + col8);
        }
        if ((idx >> 7) == mtB && a == 0) {
            u32x4 t;
            wait_until(__builtin_amdgcn_readfirstlane(tm0) + K::Y_AT + K::X0);
#pragma unroll 1
            for (int it = 0; it < POLL_LIMIT; ++it) {
                t = ld128c(rsWS, (unsigned)(l & 31) * 16u, WS_YF + (unsigned)hpB * 512u);
                if (__all(t[1] == tag && t[3] == tag)) break;
                __builtin_amdgcn_s_sleep(2 * K::RETRY_S);
                fence_c();
            }
            lds_wait(lds0 + C_XE, 1);
            const int rr = idx & 127;
            const unsigned xx = *(AS3 const unsigned*)(size_t)(lds0 + L_XE + rr * 128 + ((((l & 31) >> 2) ^ ((rr >> 1) & 7)) << 4) + (l & 3) * 4);
            const float rsel = *(AS3 const float*)(size_t)(lds0 + L_IDX + 4);
            *(AS3 f32x2*)(size_t)(lds0 + L_Y + 256 + (l & 31) * 8) = f32x2{rbf(lo16(xx) + rbf(rbf(UF(t[0])) * rsel)), rbf(hi16(xx) + rbf(rbf(UF(t[2])) * rsel))};
            post(lds0 + C_YRDY, 1);
        }
        }
        if (bid == DBG_BID && a == 0) { for (int i = 0; i < 3; ++i) wait_until(tdbg + 2800 + DBG_K * vdbg); }
        asm volatile(".Lff_end0:");
        return;
    }

    const int v = w & 1, hd = w >> 1;
    const rsrc_t rsB = mk_rsrc(bulk_base(ws), WB_BYTES);
    unsigned tag, sof, t3, rid;
    unsigned long long rm;
    u32x16 ad;
    u32x4 g4;
    {
        const unsigned pi = (unsigned)((slot * 2 + (xcd & 1)) * 2 + hd);
        const char* pa = bulk_base(ws) + ((unsigned)(xcd >> 1) * (2u << 20) + WB_PRB + (pi >> 5) * 16384u + (pi & 31u) * 256u + ((v ^ slot) & 1) * 8192u);
        unsigned j0;
        asm volatile("s_load_dword %0, %1, 64" : "={s80}"(j0) : "s"(pa) : "memory");
        LANE_VARS;
        ad[0] = lds0 + (unsigned)(64 * v + c) * 128u + (unsigned)((g ^ ((c >> 1) & 7)) << 4);
        ad[1] = lds0 + 16384u + (WSW ^ (unsigned)(hd << 6));
        ad[2] = lds0 + L_VF + (unsigned)(hd * 2048 + v * 8192 + l * 16);
        ad[3] = lds0 + L_PHI + hd * 16384 + (unsigned)(64 * v + c) * 128u + (unsigned)((((c & 3) ^ ((c >> 2) & 1)) << 5) + ((g ^ (c >> 2)) << 3));
        ad[4] = lds0 + L_PHI + hd * 16384 + v * 8192 + ((PSW + (unsigned)((i3 ^ g) << 3)) ^ (unsigned)(v << 6));
        ad[5] = lds0 + L_WP + hd * 2048 + WPO;
        ad[6] = (unsigned)(l >> 5) * 16384u + (unsigned)(l & 31) * 16u;
        ad[7] = c < 3 ? (unsigned)(((g & 1) * 2 + (g >> 1)) * 1024 + c * 16) : 0x80000000u;
        ad[8] = lds0 + C_P + w * 4;
        ad[9] = lds0 + L_SM;
        ad[10] = c;
#pragma unroll
        for (int i = 11; i < 16; ++i) ad[i] = 0u;
        asm volatile(PHASE_AB_ASM
                     : "+{v[240:255]}"(ad), "={v[232:235]}"(g4), "={v236}"(tag), [sof] "=&s"(sof), [t3] "=&s"(t3), [rid] "=&s"(rid), [rm] "=&s"(rm), "+{s80}"(j0)
                     : [rsb] "s"(rsB), [rsw] "s"(rsWS), [xs0] "s"(v ? XS1[0] - 8192u : XS0[0]), [xs1] "s"(v ? XS1[1] - 8192u : XS0[1]), [kpx] "s"(w * 65536),
                       [kpa] "s"(mtA * 512), [kma] "s"(hd * 16384 + v * 4096 + mtA * 64), [sg] "s"(WS_G + (unsigned)(mtB * 128 + 32 * w) * 8u),
                       [ofc] "s"(WB_OF + (unsigned)(mtB * 4 + w) * 65536u), [ofp] "s"(WB_OF + (unsigned)((mtA * 4 + 2 * v) * 32 + ((2 * hpA + hd + 1) & 31)) * 2048u),
                       [ogp] "s"(WB_OG + (unsigned)((mtA * 4 + 2 * v) * 32 + 2 * hpA + hd) * 8u), [kac] "s"(L_KVF + hd * 4096), [zac] "s"(L_ZF + hd * 512),
                       [xec] "s"(L_XE + w * 4096), [own] "s"(mtB * 4 + w), [prb] "s"(pa)
                     : "memory", "vcc", "scc", PHASE_AB_CLOB);
    }
    {
        const rsrc_t rsOut = mk_rsrc(out, 2048u * 2048u);
        const unsigned sog = WB_OG + sof + (unsigned)(mtB * 4 + w) * 256u;
        unsigned grp = (t3 & 1u) * 2u, flip = 0;
#pragma unroll 1
        for (int att = 0; att < TRY_LIMIT; ++att) {
            unsigned cs, st;
            asm volatile(PHASE_C_ASM
                         : "={v230}"(cs), [st] "=&s"(st)
                         : "{v[240:255]}"(ad), "{v[232:235]}"(g4), "{v236}"(tag), [rsb] "s"(rsB), [rso] "s"(rsOut), [sog] "s"(sog), [grp] "s"(grp), [flip] "s"(flip),
                           [t3] "s"(t3), [rid] "s"(rid), [rm] "s"(rm), [sout] "s"((unsigned)(mtB * 128 + 32 * w) * 2048u + (unsigned)hpB * 128u)
                         : "memory", "vcc", "scc", PHASE_C_CLOB);
            if (K::FORCE_REDO && att < 2) st = 2 - att, cs += 1u;
            if (__builtin_expect(st == 0u, 1)) break;
            u32x2 og = ld64c(rsB, ad[4], sog);
            const unsigned miss = (unsigned)__builtin_amdgcn_ballot_w64(og[1] != tag);
            if (st == 1u && ((miss && !(miss & ~(0xffffu << (8u * grp)))) || K::FORCE_REDO)) {
                grp ^= 2u;
                flip ^= 65536u;
                lds_wait(lds0 + C_WO, 4);
                continue;
            }
            int it = 0;
#pragma unroll 1
            for (; it < POLL_LIMIT && !__all(og[1] == tag); ++it) {
                __builtin_amdgcn_s_sleep(K::RETRY_S);
                fence_c();
                og = ld64c(rsB, ad[4], sog);
            }
            if (it == POLL_LIMIT) break;
            if (st == 2u) {
                if (__all(wave_usum(cs - (ad[10] < 32u ? og[0] : 0u)) == 0u)) break;
            }
        }
    }
    asm volatile(".Lff_end2:\n.Lpad_x:\n\ts_cbranch_execz .Lpad_top");
}

extern "C" void kernel_launch(void* const* d_in, const int* in_sizes, int n_in, void* d_out, int out_size,
                              void* d_ws, size_t ws_size, hipStream_t stream) {
    const u16* x = (const u16*)d_in[0];
    const u16* wqkv = (const u16*)d_in[1];
    const u16* wphi = (const u16*)d_in[2];
    const u16* wo = (const u16*)d_in[3];
    const u16* wr = (const u16*)d_in[5];
    const u16* wg = (const u16*)d_in[6];
    const u16* wu = (const u16*)d_in[7];
    const u16* wd = (const u16*)d_in[8];
    const u16* gw = (const u16*)d_in[9];
    fused_fwd<<<256, 512, 0, stream>>>(x, wqkv, wphi, wo, wr, wg, wu, wd, gw, (char*)d_ws, (u16*)d_out);
}
```

```cpp
#include <hip/hip_runtime.h>
#include <stdint.h>
#include <type_traits>

typedef __attribute__((ext_vector_type(8))) short bf16x8;
typedef __attribute__((ext_vector_type(4))) short s16x4;
typedef __attribute__((ext_vector_type(4))) float f32x4;
typedef __attribute__((ext_vector_type(2))) float f32x2;
typedef __attribute__((ext_vector_type(32))) float f32x32;
typedef __attribute__((ext_vector_type(2))) unsigned u32x2;
typedef __attribute__((ext_vector_type(4))) unsigned u32x4;
typedef __attribute__((ext_vector_type(8))) unsigned u32x8;
typedef __attribute__((ext_vector_type(16))) unsigned u32x16;
typedef __attribute__((ext_vector_type(32))) unsigned u32x32;
typedef __attribute__((ext_vector_type(2))) __bf16 bf16x2_t;
typedef unsigned short u16;
typedef __amdgpu_buffer_rsrc_t rsrc_t;

#define DI __device__ __forceinline__
#define AS3 __attribute__((address_space(3)))
#define RLX_WG __ATOMIC_RELAXED, __HIP_MEMORY_SCOPE_WORKGROUP

constexpr int AUX_SC1 = 16;

DI float bf2f(u16 v) { return __uint_as_float(((unsigned)v) << 16); }
DI u16 f2bf(float f) { __bf16 b = (__bf16)f; return __builtin_bit_cast(u16, b); }
DI float rbf(float f) { return bf2f(f2bf(f)); }
DI unsigned pack2(float lo, float hi) {
    f32x2 v = {lo, hi};
    bf16x2_t b = __builtin_convertvector(v, bf16x2_t);
    return __builtin_bit_cast(unsigned, b);
}
DI float lo16(unsigned u) { return __uint_as_float(u << 16); }
DI float hi16(unsigned u) { return __uint_as_float(u & 0xffff0000u); }
DI float dot2(unsigned a, unsigned b, float c) {
    return __builtin_amdgcn_fdot2_f32_bf16(__builtin_bit_cast(bf16x2_t, a), __builtin_bit_cast(bf16x2_t, b), c, false);
}
DI s16x4 tr_read(unsigned a) { return __builtin_amdgcn_ds_read_tr16_b64_v4i16((AS3 s16x4*)(size_t)a); }
DI bf16x8 cat8(s16x4 a, s16x4 b) { return __builtin_shufflevector(a, b, 0, 1, 2, 3, 4, 5, 6, 7); }
DI bf16x8 lds_rd128(unsigned a) { return *(AS3 const bf16x8*)(size_t)a; }
DI void lds_wr128(unsigned a, bf16x8 v) { *(AS3 bf16x8*)(size_t)a = v; }
DI f32x4 mfma16(bf16x8 a, bf16x8 b, f32x4 c) { return __builtin_amdgcn_mfma_f32_16x16x32_bf16(a, b, c, 0, 0, 0); }
DI bf16x8 pack44(f32x4 a, f32x4 b) {
    u32x4 p = {pack2(a[0], a[1]), pack2(a[2], a[3]), pack2(b[0], b[1]), pack2(b[2], b[3])};
    return __builtin_bit_cast(bf16x8, p);
}
#define FU(x) __float_as_uint(x)
#define UF(x) __uint_as_float(x)
DI float x32_sum(float v) { auto r = __builtin_amdgcn_permlane32_swap(FU(v), FU(v), false, false); return UF(r[0]) + UF(r[1]); }
DI float x16_sum(float v) { auto r = __builtin_amdgcn_permlane16_swap(FU(v), FU(v), false, false); return UF(r[0]) + UF(r[1]); }
DI float xg_sum(float v) { return x16_sum(x32_sum(v)); }
template <int CTRL> DI float dpp_add(float v) { return v + UF(__builtin_amdgcn_update_dpp(0, (int)FU(v), CTRL, 0xf, 0xf, false)); }
DI float row16_sum(float v) { return dpp_add<0x121>(dpp_add<0x122>(dpp_add<0x124>(dpp_add<0x128>(v)))); }
template <int CTRL> DI unsigned dpp_umax(unsigned v) { return max(v, (unsigned)__builtin_amdgcn_update_dpp(0, (int)v, CTRL, 0xf, 0xf, false)); }
DI unsigned wave_umax(unsigned v) {
    auto r = __builtin_amdgcn_permlane32_swap(v, v, false, false);
    v = max(r[0], r[1]);
    auto q = __builtin_amdgcn_permlane16_swap(v, v, false, false);
    v = max(q[0], q[1]);
    return dpp_umax<0x121>(dpp_umax<0x122>(dpp_umax<0x124>(dpp_umax<0x128>(v))));
}
DI unsigned add4(u32x4 v, unsigned s) { return (v[0] + v[1] + v[2]) + (v[3] + s); }
template <int CTRL> DI unsigned dpp_uadd(unsigned v) { return v + (unsigned)__builtin_amdgcn_update_dpp(0, (int)v, CTRL, 0xf, 0xf, false); }
DI unsigned row16_usum(unsigned v) { return dpp_uadd<0x121>(dpp_uadd<0x122>(dpp_uadd<0x124>(dpp_uadd<0x128>(v)))); }
DI rsrc_t mk_rsrc(const void* p, unsigned bytes) { return __builtin_amdgcn_make_buffer_rsrc((void*)p, (short)0, (int)bytes, 0x00020000); }
DI u32x4 ld128c(rsrc_t rs, unsigned vo, unsigned so) { return __builtin_amdgcn_raw_buffer_load_b128(rs, vo, so, AUX_SC1); }
DI u32x2 ld64c(rsrc_t rs, unsigned vo, unsigned so) { return __builtin_amdgcn_raw_buffer_load_b64(rs, vo, so, AUX_SC1); }
DI unsigned ld32c(rsrc_t rs, unsigned vo, unsigned so) { return __builtin_amdgcn_raw_buffer_load_b32(rs, vo, so, AUX_SC1); }
DI void st128c(u32x4 v, rsrc_t rs, unsigned vo, unsigned so) {
    __builtin_amdgcn_raw_buffer_store_b128(v, rs, vo, so, AUX_SC1);
    asm volatile("s_nop 1" :: "v"(v) : "memory");
}
DI void st64c(u32x2 v, rsrc_t rs, unsigned vo, unsigned so) { __builtin_amdgcn_raw_buffer_store_b64(v, rs, vo, so, AUX_SC1); }
DI void st32c(unsigned v, rsrc_t rs, unsigned vo, unsigned so) { __builtin_amdgcn_raw_buffer_store_b32(v, rs, vo, so, AUX_SC1); }

DI void dma16(rsrc_t rs, unsigned voff, unsigned soff, unsigned lds_addr) {
    asm volatile("s_mov_b32 m0, %0\n\ts_nop 0\n\tbuffer_load_dwordx4 %1, %2, %3 offen lds"
                 :: "s"(lds_addr), "v"(voff), "s"(rs), "s"(soff) : "memory");
}
template <int PC> DI void dma4(rsrc_t rs, const unsigned* v, unsigned soff, unsigned lds_addr) {
#define D4(p) asm volatile("s_mov_b32 m0, %0\n\ts_nop 0\n\tbuffer_load_dwordx4 %1, %5, %6 offen lds\n\t" p "buffer_load_dwordx4 %2, %5, %6 offen offset:1024 lds\n\t" p \
                 "buffer_load_dwordx4 %3, %5, %6 offen offset:2048 lds\n\t" p "buffer_load_dwordx4 %4, %5, %6 offen offset:3072 lds" \
                 :: "s"(lds_addr), "v"(v[0]), "v"(v[1]), "v"(v[2]), "v"(v[3]), "s"(rs), "s"(soff), "i"(PC ? PC - 1 : 0) : "memory")
    if (PC) D4("s_nop %7\n\t"); else D4("");
}
template <int N> DI void wait_vm() { asm volatile("s_waitcnt vmcnt(%0)" :: "i"(N) : "memory"); }
DI void wait_lgkm0() { asm volatile("" ::: "memory"); __builtin_amdgcn_s_waitcnt(0xC07F); asm volatile("" ::: "memory"); }
DI void bar() { asm volatile("" ::: "memory"); __builtin_amdgcn_s_barrier(); asm volatile("" ::: "memory"); }
DI void fence_c() { asm volatile("" ::: "memory"); }
DI int fresh_lane() {
    int v;
    asm volatile("v_mbcnt_lo_u32_b32 %0, -1, 0\n\tv_mbcnt_hi_u32_b32 %0, -1, %0" : "=v"(v));
    return v;
}
#define LANE_VARS const int l = fresh_lane(), c = l & 15, g = l >> 4, i4 = (l & 15) >> 2, i3 = l & 3; (void)c; (void)g; (void)i4; (void)i3
#define PSW ((unsigned)(4 * g + i4) * 128u + (unsigned)((i4 ^ (g & 1)) << 5))
#define WPO ((unsigned)(4 * g + i4) * 64u + (unsigned)((g & 1) << 5) + (unsigned)(8 * i3))
#define WSW ((unsigned)(8 * g + i4) * 128u + (unsigned)((i4 ^ (g & 1)) << 5) + (unsigned)(8 * i3))

#define WO_VOFF ((unsigned)(l >> 3) * 2048u + (unsigned)(((((((l & 7) >> 1) ^ ((l >> 3) & 3) ^ (l >> 5)) << 1) | (l & 1))) << 4))

DI void lds_wait(unsigned addr, unsigned target) {
#pragma unroll 1
    for (int it = 0; it < (1 << 22); ++it) {
        unsigned v = __hip_atomic_load((AS3 unsigned*)(size_t)addr, RLX_WG);
        if (__builtin_amdgcn_readfirstlane(v) >= target) break;
        __builtin_amdgcn_s_sleep(1);
    }
    fence_c();
}
DI unsigned now32() { unsigned long long t; asm volatile("s_memrealtime %0\n\ts_waitcnt lgkmcnt(0)" : "=s"(t) :: "memory"); return (unsigned)t; }
DI void wait_until(unsigned t) {
#pragma unroll 1
    for (int it = 0; it < 256; ++it) {
        const int rem = (int)(t - now32());
        if (rem <= 4) break;
        if (rem > 12) __builtin_amdgcn_s_sleep(2);
    }
    fence_c();
}
DI unsigned wave_usum(unsigned m) {
    auto p = __builtin_amdgcn_permlane32_swap(m, m, false, false);
    m = p[0] + p[1];
    auto q = __builtin_amdgcn_permlane16_swap(m, m, false, false);
    return row16_usum(q[0] + q[1]);
}
DI void post(unsigned addr, unsigned v) {
    fence_c();
    *(AS3 volatile unsigned*)(size_t)addr = v;
    fence_c();
}
DI void wait4(unsigned addr, unsigned target) {
#pragma unroll 1
    for (int it = 0; it < (1 << 22); ++it) {
        const u32x4 v = *(AS3 volatile const u32x4*)(size_t)addr;
        if (__builtin_amdgcn_readfirstlane(min(min(v[0], v[1]), min(v[2], v[3]))) >= target) break;
        __builtin_amdgcn_s_sleep(1);
    }
    fence_c();
}
constexpr int POLL_LIMIT = 1 << 16, TRY_LIMIT = 1 << 12;

#define KNOB(n, v) static constexpr int n = v
#define KNOBS
struct Dflt {
    KNOB(KT0, 0x76534210); KNOB(KT1, 0x76453201); KNOB(KT2, 0x75643120); KNOB(KT3, 0x67542310);
    KNOB(BGT, 0x76227622); KNOB(BUT, 0x77327732); KNOB(BRT, 0x77777777); KNOB(BST, 0x33333333); KNOB(PBT, (int)0xaaaaaaaa);
    KNOB(SLG, 0); KNOB(SLS, 130); KNOB(X0, 130); KNOB(X1, 2); KNOB(X2, 2); KNOB(TOPV, 1);
    KNOB(PRED_K, 16); KNOB(PRED_C, 90);
    KNOB(D1, 40); KNOB(D2, 120); KNOB(D3, 140); KNOB(FG_AT, -130); KNOB(Y_AT, 260); KNOB(QA, 30); KNOB(QB, 40);
    KNOB(RETRY_S, 8); KNOB(PT_S, 12); KNOB(KF_S, 10); KNOB(WO_D, 24); KNOB(FD, 12);
    KNOB(PT_B, 10); KNOB(XE, 20);
    KNOB(RE1, 1); KNOB(L2_S, 12);
    KNOB(FORCE_REDO, 0);
    KNOB(ND, 6); KNOB(XAH, 0); KNOB(QG, 2); KNOB(HP, 2); KNOB(Q5E, 1); KNOB(PADS, 1); KNOB(WOSK, 0); KNOB(WOE, 0); KNOB(TNT, 0); KNOB(TWB, 13); KNOB(TMW, 0); KNOB(DWP, 1); KNOB(WOG, 0); KNOB(PK13, 3); KNOB(DS1, 2); KNOB(DS2, 9); KNOB(DS3, 16); KNOB(PACE, 0);
};
struct K : Dflt { KNOBS };

constexpr unsigned WS_EPOCH = 0;
constexpr unsigned WS_RG = 4096;
constexpr unsigned WS_PT = 6144;
constexpr unsigned WS_G = 8192;
constexpr unsigned WS_YF = 24576;
constexpr unsigned WS_FG = 65536;
constexpr unsigned WS_Y = 1u << 19;
constexpr unsigned WS_BYTES = 1u << 20;
constexpr unsigned WB_KVP = 0;
constexpr unsigned WB_SM = 1u << 20;
constexpr unsigned WB_PRB = 3u << 19;
constexpr unsigned WB_OG = 7u << 18;
constexpr unsigned WB_OF = 8u << 20;
constexpr unsigned WB_BYTES = 13u << 20;
DI char* bulk_base(char* ws) { return (char*)(((size_t)ws + (3u << 20)) & ~(size_t)((2u << 20) - 1)); }

constexpr unsigned SLOT = 40960;
constexpr unsigned L_PHI = 98304;
constexpr unsigned L_VF = 131072;
constexpr unsigned L_MISC = 147456;
constexpr unsigned L_WP = L_MISC, L_KVF = L_MISC + 4096, L_SM = L_MISC + 12288;
constexpr unsigned C_ST = L_SM + 0  , C_R = L_SM + 8  , C_P = L_SM + 16  ;
constexpr unsigned C_WO = L_SM + 56, C_YRDY = L_SM + 68, C_IDX = L_SM + 76, C_TM = L_SM + 80, C_XE = L_SM + 84, C_Y1 = L_SM + 88;
constexpr unsigned P_E14 = 1, P_E15 = 2, P_PHA = 3, P_PHB = 4, P_KVD = 5, P_PUB = 6, P_Q1 = 7  , P_QRD = 11, P_O = 12;
constexpr unsigned C_Q = L_SM + 92, C_HOLD = L_SM + 96;
constexpr unsigned XS0[3] = {16384, 98304, L_VF}, XS1[3] = {57344, 122880, L_VF + 8192};
constexpr unsigned QX[3] = {40960, 81920, 0}, QW[6] = {L_KVF, 65536, 73728, 24576, 32768, L_PHI};
constexpr unsigned R_L1I = 1, R_L1 = 2, R_L2I = 3, R_KVF = 4;
constexpr unsigned L_XE = L_VF;
constexpr unsigned L_KEY = L_SM + 256, L_TAG = L_SM + 288, L_IDX = L_SM + 296, L_TM = L_SM + 304  , L_PLC = L_SM + 320  , L_PRB = L_SM + 336  , L_ZF = L_SM + 512, L_Y = L_SM + 1536;

constexpr unsigned long long L2E2 = 0x3fb8aa3b3fb8aa3bull;

#define PHASE_AB_ASM \
    "s_branch .Lpad_ab0\n\t" \
    ".Lab_s1:\n\t" \
    "s_mov_b32 s89, 0x400000\n\t" \
    ".Lab_s1a:\n\t" \
    "s_waitcnt lgkmcnt(0)\n\t" \
    "ds_read_b32 v237, v249\n\t" \
    "ds_read_b32 v238, v249 offset:4\n\t" \
    "s_waitcnt lgkmcnt(0)\n\t" \
    "v_min_u32 v237, v237, v238\n\t" \
    "s_nop 1\n\t" \
    "v_readfirstlane_b32 s85, v237\n\t" \
    "s_sub_u32 s89, s89, 1\n\t" \
    "s_cbranch_scc1 .Lab_s1b\n\t" \
    "s_cmp_lt_u32 s85, 1\n\t" \
    "s_cbranch_scc1 .Lab_s1a\n\t" \
    ".Lab_s1b:\n\t" \
    "s_nop 7\n\t" \
    "s_waitcnt lgkmcnt(0)\n\t" \
    "s_branch .Lab_s1r\n\t" \
    ".Lab_s2:\n\t" \
    "s_mov_b32 s89, 0x400000\n\t" \
    ".Lab_s2a:\n\t" \
    "s_waitcnt lgkmcnt(0)\n\t" \
    "ds_read_b32 v237, v249 offset:16\n\t" \
    "ds_read_b32 v238, v249 offset:20\n\t" \
    "ds_read_b32 v255, v249 offset:24\n\t" \
    "s_waitcnt lgkmcnt(0)\n\t" \
    "v_min3_u32 v237, v237, v238, v255\n\t" \
    "ds_read_b32 v238, v249 offset:28\n\t" \
    "s_waitcnt lgkmcnt(0)\n\t" \
    "v_min_u32 v237, v237, v238\n\t" \
    "s_nop 1\n\t" \
    "v_readfirstlane_b32 s85, v237\n\t" \
    "s_sub_u32 s89, s89, 1\n\t" \
    "s_cbranch_scc1 .Lab_s2b\n\t" \
    "s_cmp_lt_u32 s85, 2\n\t" \
    "s_cbranch_scc1 .Lab_s2a\n\t" \
    ".Lab_s2b:\n\t" \
    "s_nop 7\n\t" \
    "s_waitcnt lgkmcnt(0)\n\t" \
    "s_branch .Lab_s2r\n\t" \
    ".Lab_s3:\n\t" \
    "s_mov_b32 s89, 0x400000\n\t" \
    ".Lab_s3a:\n\t" \
    "s_waitcnt lgkmcnt(0)\n\t" \
    "ds_read_b32 v237, v250\n\t" \
    "s_waitcnt lgkmcnt(0)\n\t" \
    "s_nop 1\n\t" \
    "v_readfirstlane_b32 s85, v237\n\t" \
    "s_sub_u32 s89, s89, 1\n\t" \
    "s_cbranch_scc1 .Lab_s3b\n\t" \
    "s_cmp_lt_u32 s85, 3\n\t" \
    "s_cbranch_scc1 .Lab_s3a\n\t" \
    ".Lab_s3b:\n\t" \
    "s_nop 7\n\t" \
    "ds_read_b64_tr_b16 v[104:105], v245\n\t" \
    "ds_read_b64_tr_b16 v[106:107], v245 offset:2048\n\t" \
    "ds_read_b64_tr_b16 v[108:109], v255\n\t" \
    "ds_read_b64_tr_b16 v[110:111], v255 offset:2048\n\t" \
    "ds_read_b128 v[80:83], v242\n\t" \
    "ds_read_b128 v[84:87], v242 offset:1024\n\t" \
    "ds_read_b128 v[88:91], v242 offset:4096\n\t" \
    "ds_read_b128 v[92:95], v242 offset:5120\n\t" \
    "s_waitcnt lgkmcnt(0)\n\t" \
    "s_branch .Lab_s3r\n\t" \
    ".Lab_s4:\n\t" \
    "s_mov_b32 s89, 0x400000\n\t" \
    ".Lab_s4a:\n\t" \
    "s_waitcnt lgkmcnt(0)\n\t" \
    "ds_read_b32 v237, v250\n\t" \
    "s_waitcnt lgkmcnt(0)\n\t" \
    "s_nop 1\n\t" \
    "v_readfirstlane_b32 s85, v237\n\t" \
    "s_sub_u32 s89, s89, 1\n\t" \
    "s_cbranch_scc1 .Lab_s4b\n\t" \
    "s_cmp_lt_u32 s85, 4\n\t" \
    "s_cbranch_scc1 .Lab_s4a\n\t" \
    ".Lab_s4b:\n\t" \
    "s_nop 7\n\t" \
    "ds_read_b64_tr_b16 v[8:9], v245 offset:4096\n\t" \
    "ds_read_b64_tr_b16 v[10:11], v245 offset:6144\n\t" \
    "ds_read_b64_tr_b16 v[12:13], v255 offset:4096\n\t" \
    "ds_read_b64_tr_b16 v[14:15], v255 offset:6144\n\t" \
    "v_mov_b32 v237, 5\n\t" \
    "s_waitcnt lgkmcnt(0)\n\t" \
    "s_branch .Lab_s4r\n\t" \
    ".Lab_s5:\n\t" \
    "s_mov_b32 s89, 0x400000\n\t" \
    ".Lab_s5a:\n\t" \
    "s_waitcnt lgkmcnt(0)\n\t" \
    "ds_read_b32 v237, v249 offset:92\n\t" \
    "s_waitcnt lgkmcnt(0)\n\t" \
    "s_nop 1\n\t" \
    "v_readfirstlane_b32 s85, v237\n\t" \
    "s_sub_u32 s89, s89, 1\n\t" \
    "s_cbranch_scc1 .Lab_s5b\n\t" \
    "s_cmp_lt_u32 s85, 1\n\t" \
    "s_cbranch_scc1 .Lab_s5a\n\t" \
    ".Lab_s5b:\n\t" \
    "s_nop 7\n\t" \
    "ds_read_b64_tr_b16 v[80:81], v253\n\t" \
    "ds_read_b64_tr_b16 v[82:83], v253 offset:512\n\t" \
    "ds_read_b64_tr_b16 v[84:85], v251\n\t" \
    "ds_read_b64_tr_b16 v[86:87], v251 offset:512\n\t" \
    "ds_read_b64_tr_b16 v[88:89], v253 offset:4096\n\t" \
    "ds_read_b64_tr_b16 v[90:91], v253 offset:4608\n\t" \
    "ds_read_b64_tr_b16 v[92:93], v251 offset:4096\n\t" \
    "ds_read_b64_tr_b16 v[94:95], v251 offset:4608\n\t" \
    "s_waitcnt lgkmcnt(0)\n\t" \
    "s_branch .Lab_s5r\n\t" \
    ".Lab_s6:\n\t" \
    "s_mov_b32 s89, 0x400000\n\t" \
    ".Lab_s6a:\n\t" \
    "s_waitcnt lgkmcnt(0)\n\t" \
    "ds_read_b32 v237, v249 offset:92\n\t" \
    "s_waitcnt lgkmcnt(0)\n\t" \
    "s_nop 1\n\t" \
    "v_readfirstlane_b32 s85, v237\n\t" \
    "s_sub_u32 s89, s89, 1\n\t" \
    "s_cbranch_scc1 .Lab_s6b\n\t" \
    "s_cmp_lt_u32 s85, 2\n\t" \
    "s_cbranch_scc1 .Lab_s6a\n\t" \
    ".Lab_s6b:\n\t" \
    "s_nop 7\n\t" \
    "v_mov_b32 v237, 10\n\t" \
    "s_waitcnt lgkmcnt(0)\n\t" \
    "s_branch .Lab_s6r\n\t" \
    ".Lab_s7:\n\t" \
    "s_mov_b32 s89, 0x400000\n\t" \
    ".Lab_s7a:\n\t" \
    "s_sleep 1\n\t" \
    "ds_read_b32 v237, v249 offset:8\n\t" \
    "s_waitcnt lgkmcnt(0)\n\t" \
    "v_readfirstlane_b32 s85, v237\n\t" \
    "s_sub_u32 s89, s89, 1\n\t" \
    "s_cbranch_scc1 .Lab_s7r\n\t" \
    "s_cmp_lt_u32 s85, 3\n\t" \
    "s_cbranch_scc1 .Lab_s7a\n\t" \
    "s_sleep 5\n\t" \
    "s_branch .Lab_s7r\n\t" \
    ".Lab_sa:\n\t" \
    "ds_read_b32 v36, v249 offset:8\n\t" \
    "s_waitcnt lgkmcnt(0)\n\t" \
    "v_readfirstlane_b32 s80, v36\n\t" \
    "s_sub_u32 s89, s89, 1\n\t" \
    "s_cbranch_scc1 .Lab_sar\n\t" \
    "s_cmp_lt_u32 s80, s82\n\t" \
    "s_cbranch_scc1 .Lab_sa\n\t" \
    "s_cmp_eq_u32 s80, s82\n\t" \
    "s_cbranch_scc1 .Lab_sar\n\t" \
    "s_waitcnt vmcnt(0)\n\t" \
    "s_branch .Lab_k\n\t" \
    ".Lab_s8:\n\t" \
    "s_mov_b32 s89, 0x10000\n\t" \
    "v_lshlrev_b32 v35, 3, v34\n\t" \
    ".Lab_s8a:\n\t" \
    "s_sleep 8\n\t" \
    "buffer_load_dwordx2 v[232:233], v35, %[rsw], %[sg] offen sc1\n\t" \
    "buffer_load_dwordx2 v[234:235], v35, %[rsw], %[sg] offen offset:128 sc1\n\t" \
    "s_sub_u32 s89, s89, 1\n\t" \
    "s_waitcnt vmcnt(0)\n\t" \
    "s_cbranch_scc1 .Lab_s8r\n\t" \
    "v_cmp_ne_u32 vcc, v236, v233\n\t" \
    "v_cmp_ne_u32_e64 s[92:93], v236, v235\n\t" \
    "s_nop 0\n\t" \
    "s_or_b64 vcc, vcc, s[92:93]\n\t" \
    "s_cbranch_scc1 .Lab_s8a\n\t" \
    "s_branch .Lab_s8r\n\t" \
    ".Lab_s9:\n\t" \
    "s_mov_b32 s89, 0x400000\n\t" \
    ".Lab_s9a:\n\t" \
    "s_waitcnt lgkmcnt(0)\n\t" \
    "ds_read_b32 v237, v249 offset:56\n\t" \
    "s_waitcnt lgkmcnt(0)\n\t" \
    "s_nop 1\n\t" \
    "v_readfirstlane_b32 s85, v237\n\t" \
    "s_sub_u32 s89, s89, 1\n\t" \
    "s_cbranch_scc1 .Lab_s9b\n\t" \
    "s_cmp_lt_u32 s85, 2\n\t" \
    "s_cbranch_scc1 .Lab_s9a\n\t" \
    ".Lab_s9b:\n\t" \
    "s_nop 7\n\t" \
    "s_waitcnt lgkmcnt(0)\n\t" \
    "s_branch .Lab_s9r\n\t" \
    ".p2align 12\n\t" \
    ".Lpad_ab0:\n\t" \
    "s_cbranch_execz .Lpad_top\n\t" \
    "v_mov_b32 v32, 0\n\t" \
    "v_mov_b32 v33, 0\n\t" \
    "v_mov_b32 v34, 0\n\t" \
    "v_mov_b32 v35, 0\n\t" \
    "v_mov_b32 v36, 0\n\t" \
    "v_mov_b32 v37, 0\n\t" \
    "v_mov_b32 v38, 0\n\t" \
    "v_mov_b32 v39, 0\n\t" \
    "v_mov_b32 v40, 0\n\t" \
    "v_mov_b32 v41, 0\n\t" \
    "v_mov_b32 v42, 0\n\t" \
    "v_mov_b32 v43, 0\n\t" \
    "v_mov_b32 v44, 0\n\t" \
    "v_mov_b32 v45, 0\n\t" \
    "v_mov_b32 v46, 0\n\t" \
    "v_mov_b32 v47, 0\n\t" \
    "v_mov_b32 v48, 0\n\t" \
    "v_mov_b32 v49, 0\n\t" \
    "v_mov_b32 v50, 0\n\t" \
    "v_mov_b32 v51, 0\n\t" \
    "v_mov_b32 v52, 0\n\t" \
    "v_mov_b32 v53, 0\n\t" \
    "v_mov_b32 v54, 0\n\t" \
    "v_mov_b32 v55, 0\n\t" \
    "v_mov_b32 v56, 0\n\t" \
    "v_mov_b32 v57, 0\n\t" \
    "v_mov_b32 v58, 0\n\t" \
    "v_mov_b32 v59, 0\n\t" \
    "v_mov_b32 v60, 0\n\t" \
    "v_mov_b32 v61, 0\n\t" \
    "v_mov_b32 v62, 0\n\t" \
    "v_mov_b32 v63, 0\n\t" \
    "v_mov_b32 v80, 0\n\t" \
    "v_mov_b32 v81, 0\n\t" \
    "v_mov_b32 v82, 0\n\t" \
    "v_mov_b32 v83, 0\n\t" \
    "v_mov_b32 v84, 0\n\t" \
    "v_mov_b32 v85, 0\n\t" \
    "v_mov_b32 v86, 0\n\t" \
    "v_mov_b32 v87, 0\n\t" \
    "v_mov_b32 v88, 0\n\t" \
    "v_mov_b32 v89, 0\n\t" \
    "v_mov_b32 v90, 0\n\t" \
    "v_mov_b32 v91, 0\n\t" \
    "v_mov_b32 v92, 0\n\t" \
    "v_mov_b32 v93, 0\n\t" \
    "v_mov_b32 v94, 0\n\t" \
    "v_mov_b32 v95, 0\n\t" \
    "v_mov_b32 v96, 0\n\t" \
    "v_mov_b32 v97, 0\n\t" \
    "v_mov_b32 v98, 0\n\t" \
    "v_mov_b32 v99, 0\n\t" \
    "v_mov_b32 v100, 0\n\t" \
    "v_mov_b32 v101, 0\n\t" \
    "v_mov_b32 v102, 0\n\t" \
    "v_mov_b32 v103, 0\n\t" \
    "v_mov_b32 v104, 0\n\t" \
    "v_mov_b32 v105, 0\n\t" \
    "v_mov_b32 v106, 0\n\t" \
    "v_mov_b32 v107, 0\n\t" \
    "v_mov_b32 v108, 0\n\t" \
    "v_mov_b32 v109, 0\n\t" \
    "v_mov_b32 v110, 0\n\t" \
    "v_mov_b32 v111, 0\n\t" \
    "v_mov_b32 v192, 0\n\t" \
    "v_mov_b32 v193, 0\n\t" \
    "v_mov_b32 v194, 0\n\t" \
    "v_mov_b32 v195, 0\n\t" \
    "v_mov_b32 v196, 0\n\t" \
    "v_mov_b32 v197, 0\n\t" \
    "v_mov_b32 v198, 0\n\t" \
    "v_mov_b32 v199, 0\n\t" \
    "v_mov_b32 v200, 0\n\t" \
    "v_mov_b32 v201, 0\n\t" \
    "v_mov_b32 v202, 0\n\t" \
    "v_mov_b32 v203, 0\n\t" \
    "v_mov_b32 v204, 0\n\t" \
    "v_mov_b32 v205, 0\n\t" \
    "v_mov_b32 v206, 0\n\t" \
    "v_mov_b32 v207, 0\n\t" \
    "v_mov_b32 v208, 0\n\t" \
    "v_mov_b32 v209, 0\n\t" \
    "v_mov_b32 v210, 0\n\t" \
    "v_mov_b32 v211, 0\n\t" \
    "v_mov_b32 v212, 0\n\t" \
    "v_mov_b32 v213, 0\n\t" \
    "v_mov_b32 v214, 0\n\t" \
    "v_mov_b32 v215, 0\n\t" \
    "v_mov_b32 v216, 0\n\t" \
    "v_mov_b32 v217, 0\n\t" \
    "v_mov_b32 v218, 0\n\t" \
    "v_mov_b32 v219, 0\n\t" \
    "v_mov_b32 v220, 0\n\t" \
    "v_mov_b32 v221, 0\n\t" \
    "v_mov_b32 v222, 0\n\t" \
    "v_mov_b32 v223, 0\n\t" \
    "v_mov_b32 v232, 0x3f803f80\n\t" \
    "v_mov_b32 v233, 0x3f803f80\n\t" \
    "v_mov_b32 v234, 0x3f803f80\n\t" \
    "v_mov_b32 v235, 0x3f803f80\n\t" \
    "v_mov_b32 v251, v240\n\t" \
    "v_xor_b32 v252, 64, v240\n\t" \
    "v_mov_b32 v253, v241\n\t" \
    "v_xor_b32 v254, 32, v241\n\t" \
    "s_mov_b32 s90, 0x3fb8aa3b\n\t" \
    "s_mov_b32 s91, 0x3fb8aa3b\n\t" \
    "v_cmp_eq_u32_e64 s[92:93], 2, v250\n\t" \
    "v_cmp_gt_u32_e64 s[94:95], 2, v250\n\t" \
    "v_cmp_eq_u32 vcc, 0, v250\n\t" \
    "v_xor_b32 v250, 4, v248\n\t" \
    "s_mov_b32 s84, 0x140a0014\n\t" \
    "s_lshl_b32 s83, %[xs0], 4\n\t" \
    "s_lshl_b32 s85, %[xs1], 12\n\t" \
    "s_or_b32 s83, s83, s85\n\t" \
    "s_or_b32 s83, s83, 0x14\n\t" \
    "s_waitcnt lgkmcnt(0)\n\t" \
    "s_mov_b32 s80, 64\n\t" \
    "s_add_u32 s81, %[pro], 64\n\t" \
    ".Lab_p:\n\t" \
    "s_memtime s[86:87]\n\t" \
    "s_load_dword s85, %[prb], s80\n\t" \
    "s_waitcnt lgkmcnt(0)\n\t" \
    "s_memtime s[88:89]\n\t" \
    "s_load_dword s85, %[prb], s81\n\t" \
    "s_waitcnt lgkmcnt(0)\n\t" \
    "s_memtime s[96:97]\n\t" \
    "s_mov_b32 s81, %[pro]\n\t" \
    "s_sub_u32 s80, s80, 64\n\t" \
    "s_waitcnt lgkmcnt(0)\n\t" \
    "s_cbranch_scc0 .Lab_p\n\t" \
    "s_sub_u32 s86, s88, s86\n\t" \
    "s_sub_u32 s88, s96, s88\n\t" \
    "s_mov_b32 s80, 0\n\t" \
    "s_mov_b32 s81, 10\n\t" \
    "s_pack_ll_b32_b16 s86, s86, s88\n\t" \
    "v_mov_b32 v237, s86\n\t" \
    "ds_write_b32 v248, v237 offset:320\n\t" \
    "s_waitcnt lgkmcnt(0)\n\t" \
    "s_barrier\n\t" \
    "ds_read_b128 v[128:131], v251\n\t" \
    "ds_read_b128 v[132:135], v251 offset:2048\n\t" \
    "ds_read_b128 v[136:139], v251 offset:4096\n\t" \
    "ds_read_b128 v[140:143], v251 offset:6144\n\t" \
    "ds_read_b64_tr_b16 v[0:1], v253 offset:8192\n\t" \
    "ds_read_b64_tr_b16 v[2:3], v253 offset:8704\n\t" \
    "ds_read_b64_tr_b16 v[4:5], v254 offset:8192\n\t" \
    "ds_read_b64_tr_b16 v[6:7], v254 offset:8704\n\t" \
    "ds_read_b64_tr_b16 v[8:9], v253 offset:16384\n\t" \
    "ds_read_b64_tr_b16 v[10:11], v253 offset:16896\n\t" \
    "ds_read_b64_tr_b16 v[12:13], v254 offset:16384\n\t" \
    "ds_read_b64_tr_b16 v[14:15], v254 offset:16896\n\t" \
    "ds_read_b64_tr_b16 v[16:17], v253\n\t" \
    "ds_read_b64_tr_b16 v[18:19], v253 offset:512\n\t" \
    "ds_read_b64_tr_b16 v[20:21], v254\n\t" \
    "ds_read_b64_tr_b16 v[22:23], v254 offset:512\n\t" \
    ".Lab_l1:\n\t" \
    "s_waitcnt lgkmcnt(0)\n\t" \
    "v_mfma_f32_16x16x32_bf16 v[32:35], v[0:3], v[128:131], v[32:35]\n\t" \
    "ds_read_b128 v[144:147], v252\n\t" \
    "v_mfma_f32_16x16x32_bf16 v[40:43], v[0:3], v[132:135], v[40:43]\n\t" \
    "ds_read_b128 v[148:151], v252 offset:2048\n\t" \
    "v_mfma_f32_16x16x32_bf16 v[48:51], v[0:3], v[136:139], v[48:51]\n\t" \
    "ds_read_b128 v[152:155], v252 offset:4096\n\t" \
    "v_mfma_f32_16x16x32_bf16 v[56:59], v[0:3], v[140:143], v[56:59]\n\t" \
    "ds_read_b128 v[156:159], v252 offset:6144\n\t" \
    "v_mfma_f32_16x16x32_bf16 v[36:39], v[4:7], v[128:131], v[36:39]\n\t" \
    "ds_read_b64_tr_b16 v[112:113], v253 offset:12288\n\t" \
    "v_mfma_f32_16x16x32_bf16 v[44:47], v[4:7], v[132:135], v[44:47]\n\t" \
    "ds_read_b64_tr_b16 v[114:115], v253 offset:12800\n\t" \
    "v_mfma_f32_16x16x32_bf16 v[52:55], v[4:7], v[136:139], v[52:55]\n\t" \
    "ds_read_b64_tr_b16 v[116:117], v254 offset:12288\n\t" \
    "v_mfma_f32_16x16x32_bf16 v[60:63], v[4:7], v[140:143], v[60:63]\n\t" \
    "ds_read_b64_tr_b16 v[118:119], v254 offset:12800\n\t" \
    "v_mfma_f32_16x16x32_bf16 v[80:83], v[128:131], v[8:11], v[80:83]\n\t" \
    "ds_read_b64_tr_b16 v[120:121], v253 offset:20480\n\t" \
    "v_mfma_f32_16x16x32_bf16 v[88:91], v[132:135], v[8:11], v[88:91]\n\t" \
    "ds_read_b64_tr_b16 v[122:123], v253 offset:20992\n\t" \
    "v_mfma_f32_16x16x32_bf16 v[96:99], v[136:139], v[8:11], v[96:99]\n\t" \
    "ds_read_b64_tr_b16 v[124:125], v254 offset:20480\n\t" \
    "v_mfma_f32_16x16x32_bf16 v[104:107], v[140:143], v[8:11], v[104:107]\n\t" \
    "ds_read_b64_tr_b16 v[126:127], v254 offset:20992\n\t" \
    "v_mfma_f32_16x16x32_bf16 v[84:87], v[128:131], v[12:15], v[84:87]\n\t" \
    "ds_read_b64_tr_b16 v[24:25], v253 offset:4096\n\t" \
    "v_mfma_f32_16x16x32_bf16 v[92:95], v[132:135], v[12:15], v[92:95]\n\t" \
    "ds_read_b64_tr_b16 v[26:27], v253 offset:4608\n\t" \
    "v_mfma_f32_16x16x32_bf16 v[100:103], v[136:139], v[12:15], v[100:103]\n\t" \
    "ds_read_b64_tr_b16 v[28:29], v254 offset:4096\n\t" \
    "v_mfma_f32_16x16x32_bf16 v[108:111], v[140:143], v[12:15], v[108:111]\n\t" \
    "ds_read_b64_tr_b16 v[30:31], v254 offset:4608\n\t" \
    "v_mfma_f32_16x16x32_bf16 v[192:195], v[16:19], v[128:131], v[192:195]\n\t" \
    "s_add_u32 s85, s80, 40960\n\t" \
    "s_cmp_lg_u32 s80, 81920\n\t" \
    "v_mfma_f32_16x16x32_bf16 v[200:203], v[16:19], v[132:135], v[200:203]\n\t" \
    "s_cselect_b32 s80, s85, 0\n\t" \
    "v_mfma_f32_16x16x32_bf16 v[208:211], v[16:19], v[136:139], v[208:211]\n\t" \
    "v_add_u32 v251, s80, v240\n\t" \
    "v_mfma_f32_16x16x32_bf16 v[216:219], v[16:19], v[140:143], v[216:219]\n\t" \
    "v_add_u32 v253, s80, v241\n\t" \
    "v_mfma_f32_16x16x32_bf16 v[196:199], v[20:23], v[128:131], v[196:199]\n\t" \
    "v_xor_b32 v254, 32, v253\n\t" \
    "v_mfma_f32_16x16x32_bf16 v[204:207], v[20:23], v[132:135], v[204:207]\n\t" \
    "v_mfma_f32_16x16x32_bf16 v[212:215], v[20:23], v[136:139], v[212:215]\n\t" \
    "v_mfma_f32_16x16x32_bf16 v[220:223], v[20:23], v[140:143], v[220:223]\n\t" \
    "s_waitcnt lgkmcnt(0)\n\t" \
    "s_barrier\n\t" \
    "v_mfma_f32_16x16x32_bf16 v[32:35], v[112:115], v[144:147], v[32:35]\n\t" \
    "ds_read_b128 v[128:131], v251\n\t" \
    "v_mfma_f32_16x16x32_bf16 v[40:43], v[112:115], v[148:151], v[40:43]\n\t" \
    "ds_read_b128 v[132:135], v251 offset:2048\n\t" \
    "v_mfma_f32_16x16x32_bf16 v[48:51], v[112:115], v[152:155], v[48:51]\n\t" \
    "ds_read_b128 v[136:139], v251 offset:4096\n\t" \
    "v_mfma_f32_16x16x32_bf16 v[56:59], v[112:115], v[156:159], v[56:59]\n\t" \
    "ds_read_b128 v[140:143], v251 offset:6144\n\t" \
    "v_mfma_f32_16x16x32_bf16 v[36:39], v[116:119], v[144:147], v[36:39]\n\t" \
    "ds_read_b64_tr_b16 v[0:1], v253 offset:8192\n\t" \
    "v_mfma_f32_16x16x32_bf16 v[44:47], v[116:119], v[148:151], v[44:47]\n\t" \
    "ds_read_b64_tr_b16 v[2:3], v253 offset:8704\n\t" \
    "v_mfma_f32_16x16x32_bf16 v[52:55], v[116:119], v[152:155], v[52:55]\n\t" \
    "ds_read_b64_tr_b16 v[4:5], v254 offset:8192\n\t" \
    "v_mfma_f32_16x16x32_bf16 v[60:63], v[116:119], v[156:159], v[60:63]\n\t" \
    "ds_read_b64_tr_b16 v[6:7], v254 offset:8704\n\t" \
    "v_mfma_f32_16x16x32_bf16 v[80:83], v[144:147], v[120:123], v[80:83]\n\t" \
    "ds_read_b64_tr_b16 v[8:9], v253 offset:16384\n\t" \
    "v_mfma_f32_16x16x32_bf16 v[88:91], v[148:151], v[120:123], v[88:91]\n\t" \
    "ds_read_b64_tr_b16 v[10:11], v253 offset:16896\n\t" \
    "v_mfma_f32_16x16x32_bf16 v[96:99], v[152:155], v[120:123], v[96:99]\n\t" \
    "ds_read_b64_tr_b16 v[12:13], v254 offset:16384\n\t" \
    "v_mfma_f32_16x16x32_bf16 v[104:107], v[156:159], v[120:123], v[104:107]\n\t" \
    "ds_read_b64_tr_b16 v[14:15], v254 offset:16896\n\t" \
    "v_mfma_f32_16x16x32_bf16 v[84:87], v[144:147], v[124:127], v[84:87]\n\t" \
    "ds_read_b64_tr_b16 v[16:17], v253\n\t" \
    "v_mfma_f32_16x16x32_bf16 v[92:95], v[148:151], v[124:127], v[92:95]\n\t" \
    "ds_read_b64_tr_b16 v[18:19], v253 offset:512\n\t" \
    "v_mfma_f32_16x16x32_bf16 v[100:103], v[152:155], v[124:127], v[100:103]\n\t" \
    "ds_read_b64_tr_b16 v[20:21], v254\n\t" \
    "v_mfma_f32_16x16x32_bf16 v[108:111], v[156:159], v[124:127], v[108:111]\n\t" \
    "ds_read_b64_tr_b16 v[22:23], v254 offset:512\n\t" \
    "v_mfma_f32_16x16x32_bf16 v[192:195], v[24:27], v[144:147], v[192:195]\n\t" \
    "v_xor_b32 v252, 64, v251\n\t" \
    "v_mfma_f32_16x16x32_bf16 v[200:203], v[24:27], v[148:151], v[200:203]\n\t" \
    "v_mfma_f32_16x16x32_bf16 v[208:211], v[24:27], v[152:155], v[208:211]\n\t" \
    "s_sub_u32 s81, s81, 1\n\t" \
    "v_mfma_f32_16x16x32_bf16 v[216:219], v[24:27], v[156:159], v[216:219]\n\t" \
    "s_cmp_lg_u32 s81, 0\n\t" \
    "v_mfma_f32_16x16x32_bf16 v[196:199], v[28:31], v[144:147], v[196:199]\n\t" \
    "v_mfma_f32_16x16x32_bf16 v[204:207], v[28:31], v[148:151], v[204:207]\n\t" \
    "v_mfma_f32_16x16x32_bf16 v[212:215], v[28:31], v[152:155], v[212:215]\n\t" \
    "v_mfma_f32_16x16x32_bf16 v[220:223], v[28:31], v[156:159], v[220:223]\n\t" \
    "s_cbranch_scc1 .Lab_l1\n\t" \
    ".Lab_l2:\n\t" \
    "s_waitcnt lgkmcnt(0)\n\t" \
    "v_mfma_f32_16x16x32_bf16 v[32:35], v[0:3], v[128:131], v[32:35]\n\t" \
    "ds_read_b128 v[144:147], v252\n\t" \
    "ds_read_b64_tr_b16 v[112:113], v253 offset:12288\n\t" \
    "v_mfma_f32_16x16x32_bf16 v[40:43], v[0:3], v[132:135], v[40:43]\n\t" \
    "ds_read_b128 v[148:151], v252 offset:2048\n\t" \
    "ds_read_b64_tr_b16 v[114:115], v253 offset:12800\n\t" \
    "v_mfma_f32_16x16x32_bf16 v[48:51], v[0:3], v[136:139], v[48:51]\n\t" \
    "ds_read_b128 v[152:155], v252 offset:4096\n\t" \
    "s_and_b32 s80, s84, 255\n\t" \
    "v_mfma_f32_16x16x32_bf16 v[56:59], v[0:3], v[140:143], v[56:59]\n\t" \
    "ds_read_b128 v[156:159], v252 offset:6144\n\t" \
    "s_and_b32 s82, s83, 255\n\t" \
    "v_mfma_f32_16x16x32_bf16 v[36:39], v[4:7], v[128:131], v[36:39]\n\t" \
    "ds_read_b64_tr_b16 v[116:117], v254 offset:12288\n\t" \
    "s_lshl_b32 s80, s80, 12\n\t" \
    "v_mfma_f32_16x16x32_bf16 v[44:47], v[4:7], v[132:135], v[44:47]\n\t" \
    "ds_read_b64_tr_b16 v[118:119], v254 offset:12800\n\t" \
    "s_lshl_b32 s82, s82, 12\n\t" \
    "v_mfma_f32_16x16x32_bf16 v[52:55], v[4:7], v[136:139], v[52:55]\n\t" \
    "ds_read_b64_tr_b16 v[120:121], v253 offset:20480\n\t" \
    "v_mfma_f32_16x16x32_bf16 v[60:63], v[4:7], v[140:143], v[60:63]\n\t" \
    "ds_read_b64_tr_b16 v[122:123], v253 offset:20992\n\t" \
    "v_mfma_f32_16x16x32_bf16 v[80:83], v[128:131], v[8:11], v[80:83]\n\t" \
    "ds_read_b64_tr_b16 v[124:125], v254 offset:20480\n\t" \
    "v_mfma_f32_16x16x32_bf16 v[88:91], v[132:135], v[8:11], v[88:91]\n\t" \
    "ds_read_b64_tr_b16 v[126:127], v254 offset:20992\n\t" \
    "v_mfma_f32_16x16x32_bf16 v[96:99], v[136:139], v[8:11], v[96:99]\n\t" \
    "v_add_u32 v251, s82, v240\n\t" \
    "v_mfma_f32_16x16x32_bf16 v[104:107], v[140:143], v[8:11], v[104:107]\n\t" \
    "v_add_u32 v253, s80, v241\n\t" \
    "v_mfma_f32_16x16x32_bf16 v[84:87], v[128:131], v[12:15], v[84:87]\n\t" \
    "v_xor_b32 v254, 32, v253\n\t" \
    "v_mfma_f32_16x16x32_bf16 v[92:95], v[132:135], v[12:15], v[92:95]\n\t" \
    "v_mfma_f32_16x16x32_bf16 v[100:103], v[136:139], v[12:15], v[100:103]\n\t" \
    "v_mfma_f32_16x16x32_bf16 v[108:111], v[140:143], v[12:15], v[108:111]\n\t" \
    "s_waitcnt lgkmcnt(0)\n\t" \
    "s_barrier\n\t" \
    "v_mfma_f32_16x16x32_bf16 v[32:35], v[112:115], v[144:147], v[32:35]\n\t" \
    "ds_read_b128 v[128:131], v251\n\t" \
    "ds_read_b64_tr_b16 v[0:1], v253 offset:8192\n\t" \
    "v_mfma_f32_16x16x32_bf16 v[40:43], v[112:115], v[148:151], v[40:43]\n\t" \
    "ds_read_b128 v[132:135], v251 offset:2048\n\t" \
    "ds_read_b64_tr_b16 v[2:3], v253 offset:8704\n\t" \
    "v_mfma_f32_16x16x32_bf16 v[48:51], v[112:115], v[152:155], v[48:51]\n\t" \
    "ds_read_b128 v[136:139], v251 offset:4096\n\t" \
    "v_mfma_f32_16x16x32_bf16 v[56:59], v[112:115], v[156:159], v[56:59]\n\t" \
    "ds_read_b128 v[140:143], v251 offset:6144\n\t" \
    "v_mfma_f32_16x16x32_bf16 v[36:39], v[116:119], v[144:147], v[36:39]\n\t" \
    "ds_read_b64_tr_b16 v[4:5], v254 offset:8192\n\t" \
    "v_mfma_f32_16x16x32_bf16 v[44:47], v[116:119], v[148:151], v[44:47]\n\t" \
    "ds_read_b64_tr_b16 v[6:7], v254 offset:8704\n\t" \
    "v_mfma_f32_16x16x32_bf16 v[52:55], v[116:119], v[152:155], v[52:55]\n\t" \
    "ds_read_b64_tr_b16 v[8:9], v253 offset:16384\n\t" \
    "v_mfma_f32_16x16x32_bf16 v[60:63], v[116:119], v[156:159], v[60:63]\n\t" \
    "ds_read_b64_tr_b16 v[10:11], v253 offset:16896\n\t" \
    "v_mfma_f32_16x16x32_bf16 v[80:83], v[144:147], v[120:123], v[80:83]\n\t" \
    "ds_read_b64_tr_b16 v[12:13], v254 offset:16384\n\t" \
    "v_mfma_f32_16x16x32_bf16 v[88:91], v[148:151], v[120:123], v[88:91]\n\t" \
    "ds_read_b64_tr_b16 v[14:15], v254 offset:16896\n\t" \
    "v_mfma_f32_16x16x32_bf16 v[96:99], v[152:155], v[120:123], v[96:99]\n\t" \
    "v_xor_b32 v252, 64, v251\n\t" \
    "v_mfma_f32_16x16x32_bf16 v[104:107], v[156:159], v[120:123], v[104:107]\n\t" \
    "s_lshr_b32 s83, s83, 8\n\t" \
    "v_mfma_f32_16x16x32_bf16 v[84:87], v[144:147], v[124:127], v[84:87]\n\t" \
    "s_lshr_b32 s84, s84, 8\n\t" \
    "v_mfma_f32_16x16x32_bf16 v[92:95], v[148:151], v[124:127], v[92:95]\n\t" \
    "v_mfma_f32_16x16x32_bf16 v[100:103], v[152:155], v[124:127], v[100:103]\n\t" \
    "v_mfma_f32_16x16x32_bf16 v[108:111], v[156:159], v[124:127], v[108:111]\n\t" \
    "s_cbranch_scc1 .Lab_l2\n\t" \
    "s_waitcnt lgkmcnt(0)\n\t" \
    "v_mfma_f32_16x16x32_bf16 v[32:35], v[0:3], v[128:131], v[32:35]\n\t" \
    "ds_read_b128 v[144:147], v252\n\t" \
    "ds_read_b64_tr_b16 v[112:113], v253 offset:12288\n\t" \
    "v_mfma_f32_16x16x32_bf16 v[40:43], v[0:3], v[132:135], v[40:43]\n\t" \
    "ds_read_b128 v[148:151], v252 offset:2048\n\t" \
    "ds_read_b64_tr_b16 v[114:115], v253 offset:12800\n\t" \
    "v_mfma_f32_16x16x32_bf16 v[48:51], v[0:3], v[136:139], v[48:51]\n\t" \
    "ds_read_b128 v[152:155], v252 offset:4096\n\t" \
    "ds_read_b64_tr_b16 v[116:117], v254 offset:12288\n\t" \
    "v_mfma_f32_16x16x32_bf16 v[56:59], v[0:3], v[140:143], v[56:59]\n\t" \
    "ds_read_b128 v[156:159], v252 offset:6144\n\t" \
    "ds_read_b64_tr_b16 v[118:119], v254 offset:12800\n\t" \
    "v_mfma_f32_16x16x32_bf16 v[36:39], v[4:7], v[128:131], v[36:39]\n\t" \
    "ds_read_b64_tr_b16 v[120:121], v253 offset:20480\n\t" \
    "ds_read_b64_tr_b16 v[122:123], v253 offset:20992\n\t" \
    "v_mfma_f32_16x16x32_bf16 v[44:47], v[4:7], v[132:135], v[44:47]\n\t" \
    "ds_read_b64_tr_b16 v[124:125], v254 offset:20480\n\t" \
    "ds_read_b64_tr_b16 v[126:127], v254 offset:20992\n\t" \
    "v_mfma_f32_16x16x32_bf16 v[52:55], v[4:7], v[136:139], v[52:55]\n\t" \
    "ds_read_b64_tr_b16 v[224:225], v245\n\t" \
    "ds_read_b64_tr_b16 v[226:227], v245 offset:1024\n\t" \
    "v_mfma_f32_16x16x32_bf16 v[60:63], v[4:7], v[140:143], v[60:63]\n\t" \
    "v_xor_b32 v245, 32, v245\n\t" \
    "ds_read_b32 v236, v249 offset:288\n\t" \
    "v_mfma_f32_16x16x32_bf16 v[80:83], v[128:131], v[8:11], v[80:83]\n\t" \
    "ds_read_b64_tr_b16 v[228:229], v245\n\t" \
    "ds_read_b64_tr_b16 v[230:231], v245 offset:1024\n\t" \
    "v_mfma_f32_16x16x32_bf16 v[88:91], v[132:135], v[8:11], v[88:91]\n\t" \
    "ds_read_b128 v[68:71], v249 offset:320\n\t" \
    "ds_read_b64 v[64:65], v249 offset:0\n\t" \
    "v_mfma_f32_16x16x32_bf16 v[96:99], v[136:139], v[8:11], v[96:99]\n\t" \
    "v_add_u32 v251, 131072, v240\n\t" \
    "v_mfma_f32_16x16x32_bf16 v[104:107], v[140:143], v[8:11], v[104:107]\n\t" \
    "v_xor_b32 v254, 32, v241\n\t" \
    "v_mfma_f32_16x16x32_bf16 v[84:87], v[128:131], v[12:15], v[84:87]\n\t" \
    "v_mov_b32 v253, v241\n\t" \
    "v_mfma_f32_16x16x32_bf16 v[92:95], v[132:135], v[12:15], v[92:95]\n\t" \
    "v_xor_b32 v252, 64, v251\n\t" \
    "v_mfma_f32_16x16x32_bf16 v[100:103], v[136:139], v[12:15], v[100:103]\n\t" \
    "v_mfma_f32_16x16x32_bf16 v[108:111], v[140:143], v[12:15], v[108:111]\n\t" \
    "s_waitcnt lgkmcnt(0)\n\t" \
    "v_mfma_f32_16x16x32_bf16 v[32:35], v[112:115], v[144:147], v[32:35]\n\t" \
    "v_min_u32 v64, v64, v65\n\t" \
    "v_mfma_f32_16x16x32_bf16 v[40:43], v[112:115], v[148:151], v[40:43]\n\t" \
    "v_readfirstlane_b32 s85, v64\n\t" \
    "s_cmp_lt_u32 s85, 1\n\t" \
    "s_cbranch_scc1 .Lab_s1\n\t" \
    ".Lab_s1r:\n\t" \
    "v_mfma_f32_16x16x32_bf16 v[48:51], v[112:115], v[152:155], v[48:51]\n\t" \
    "ds_read_b128 v[160:163], v251\n\t" \
    "ds_read_b64_tr_b16 v[0:1], v253 offset:8192\n\t" \
    "v_mfma_f32_16x16x32_bf16 v[56:59], v[112:115], v[156:159], v[56:59]\n\t" \
    "ds_read_b128 v[164:167], v251 offset:2048\n\t" \
    "ds_read_b64_tr_b16 v[2:3], v253 offset:8704\n\t" \
    "v_mfma_f32_16x16x32_bf16 v[36:39], v[116:119], v[144:147], v[36:39]\n\t" \
    "ds_read_b128 v[168:171], v251 offset:4096\n\t" \
    "ds_read_b64_tr_b16 v[4:5], v254 offset:8192\n\t" \
    "v_mfma_f32_16x16x32_bf16 v[44:47], v[116:119], v[148:151], v[44:47]\n\t" \
    "ds_read_b128 v[172:175], v251 offset:6144\n\t" \
    "ds_read_b64_tr_b16 v[6:7], v254 offset:8704\n\t" \
    "v_mfma_f32_16x16x32_bf16 v[52:55], v[116:119], v[152:155], v[52:55]\n\t" \
    "ds_read_b64_tr_b16 v[8:9], v253 offset:16384\n\t" \
    "ds_read_b64_tr_b16 v[10:11], v253 offset:16896\n\t" \
    "v_mfma_f32_16x16x32_bf16 v[60:63], v[116:119], v[156:159], v[60:63]\n\t" \
    "ds_read_b64_tr_b16 v[12:13], v254 offset:16384\n\t" \
    "ds_read_b64_tr_b16 v[14:15], v254 offset:16896\n\t" \
    "v_mfma_f32_16x16x32_bf16 v[80:83], v[144:147], v[120:123], v[80:83]\n\t" \
    "v_readfirstlane_b32 s87, v68\n\t" \
    "v_mfma_f32_16x16x32_bf16 v[88:91], v[148:151], v[120:123], v[88:91]\n\t" \
    "v_readfirstlane_b32 s88, v69\n\t" \
    "v_mfma_f32_16x16x32_bf16 v[96:99], v[152:155], v[120:123], v[96:99]\n\t" \
    "v_readfirstlane_b32 %[sof], v70\n\t" \
    "v_mfma_f32_16x16x32_bf16 v[104:107], v[156:159], v[120:123], v[104:107]\n\t" \
    "s_xor_b32 s87, s87, %[kpx]\n\t" \
    "s_add_u32 s88, s88, %[kma]\n\t" \
    "v_mfma_f32_16x16x32_bf16 v[84:87], v[144:147], v[124:127], v[84:87]\n\t" \
    "s_add_u32 s87, s87, %[kpa]\n\t" \
    "v_mfma_f32_16x16x32_bf16 v[92:95], v[148:151], v[124:127], v[92:95]\n\t" \
    "s_xor_b32 s86, s87, 0x8000\n\t" \
    "v_mfma_f32_16x16x32_bf16 v[100:103], v[152:155], v[124:127], v[100:103]\n\t" \
    "v_mfma_f32_16x16x32_bf16 v[108:111], v[156:159], v[124:127], v[108:111]\n\t" \
    "s_waitcnt lgkmcnt(0)\n\t" \
    "v_mfma_f32_16x16x32_bf16 v[32:35], v[0:3], v[160:163], v[32:35]\n\t" \
    "ds_read_b128 v[176:179], v252\n\t" \
    "ds_read_b64_tr_b16 v[112:113], v253 offset:12288\n\t" \
    "v_mfma_f32_16x16x32_bf16 v[40:43], v[0:3], v[164:167], v[40:43]\n\t" \
    "ds_read_b128 v[180:183], v252 offset:2048\n\t" \
    "ds_read_b64_tr_b16 v[114:115], v253 offset:12800\n\t" \
    "v_mfma_f32_16x16x32_bf16 v[48:51], v[0:3], v[168:171], v[48:51]\n\t" \
    "ds_read_b128 v[184:187], v252 offset:4096\n\t" \
    "v_mov_b32 v237, 2\n\t" \
    "v_mfma_f32_16x16x32_bf16 v[56:59], v[0:3], v[172:175], v[56:59]\n\t" \
    "ds_read_b128 v[188:191], v252 offset:6144\n\t" \
    "v_mfma_f32_16x16x32_bf16 v[36:39], v[4:7], v[160:163], v[36:39]\n\t" \
    "ds_read_b64_tr_b16 v[116:117], v254 offset:12288\n\t" \
    "v_mfma_f32_16x16x32_bf16 v[44:47], v[4:7], v[164:167], v[44:47]\n\t" \
    "ds_read_b64_tr_b16 v[118:119], v254 offset:12800\n\t" \
    "v_mfma_f32_16x16x32_bf16 v[52:55], v[4:7], v[168:171], v[52:55]\n\t" \
    "ds_read_b64_tr_b16 v[120:121], v253 offset:20480\n\t" \
    "v_mfma_f32_16x16x32_bf16 v[60:63], v[4:7], v[172:175], v[60:63]\n\t" \
    "ds_read_b64_tr_b16 v[122:123], v253 offset:20992\n\t" \
    "v_mfma_f32_16x16x32_bf16 v[80:83], v[160:163], v[8:11], v[80:83]\n\t" \
    "ds_read_b64_tr_b16 v[124:125], v254 offset:20480\n\t" \
    "v_mfma_f32_16x16x32_bf16 v[88:91], v[164:167], v[8:11], v[88:91]\n\t" \
    "ds_read_b64_tr_b16 v[126:127], v254 offset:20992\n\t" \
    "v_mfma_f32_16x16x32_bf16 v[96:99], v[168:171], v[8:11], v[96:99]\n\t" \
    "ds_write_b32 v248, v237\n\t" \
    "v_mfma_f32_16x16x32_bf16 v[104:107], v[172:175], v[8:11], v[104:107]\n\t" \
    "v_xor_b32 v251, 32, v243\n\t" \
    "v_mfma_f32_16x16x32_bf16 v[84:87], v[160:163], v[12:15], v[84:87]\n\t" \
    "v_xor_b32 v252, 64, v243\n\t" \
    "v_mfma_f32_16x16x32_bf16 v[92:95], v[164:167], v[12:15], v[92:95]\n\t" \
    "v_xor_b32 v253, 0x60, v243\n\t" \
    "v_mfma_f32_16x16x32_bf16 v[100:103], v[168:171], v[12:15], v[100:103]\n\t" \
    "s_setprio 3\n\t" \
    "v_mfma_f32_16x16x32_bf16 v[108:111], v[172:175], v[12:15], v[108:111]\n\t" \
    "s_waitcnt lgkmcnt(0)\n\t" \
    "v_mfma_f32_16x16x32_bf16 v[32:35], v[112:115], v[176:179], v[32:35]\n\t" \
    "v_mfma_f32_16x16x32_bf16 v[36:39], v[116:119], v[176:179], v[36:39]\n\t" \
    "v_mfma_f32_16x16x32_bf16 v[40:43], v[112:115], v[180:183], v[40:43]\n\t" \
    "v_mfma_f32_16x16x32_bf16 v[44:47], v[116:119], v[180:183], v[44:47]\n\t" \
    "ds_read_b128 v[28:31], v249 offset:16\n\t" \
    "v_mfma_f32_16x16x32_bf16 v[48:51], v[112:115], v[184:187], v[48:51]\n\t" \
    "v_mfma_f32_16x16x32_bf16 v[52:55], v[116:119], v[184:187], v[52:55]\n\t" \
    "v_mfma_f32_16x16x32_bf16 v[56:59], v[112:115], v[188:191], v[56:59]\n\t" \
    "v_mfma_f32_16x16x32_bf16 v[60:63], v[116:119], v[188:191], v[60:63]\n\t" \
    "v_cvt_pk_bf16_f32 v64, v32, v33\n\t" \
    "v_cvt_pk_bf16_f32 v65, v34, v35\n\t" \
    "v_mfma_f32_16x16x32_bf16 v[80:83], v[176:179], v[120:123], v[80:83]\n\t" \
    "v_cvt_pk_bf16_f32 v66, v36, v37\n\t" \
    "v_cvt_pk_bf16_f32 v67, v38, v39\n\t" \
    "v_mfma_f32_16x16x32_bf16 v[88:91], v[180:183], v[120:123], v[88:91]\n\t" \
    "v_cvt_pk_bf16_f32 v68, v40, v41\n\t" \
    "v_cvt_pk_bf16_f32 v69, v42, v43\n\t" \
    "v_mfma_f32_16x16x32_bf16 v[84:87], v[176:179], v[124:127], v[84:87]\n\t" \
    "v_cvt_pk_bf16_f32 v70, v44, v45\n\t" \
    "v_cvt_pk_bf16_f32 v71, v46, v47\n\t" \
    "v_mfma_f32_16x16x32_bf16 v[92:95], v[180:183], v[124:127], v[92:95]\n\t" \
    "v_cvt_pk_bf16_f32 v72, v48, v49\n\t" \
    "v_cvt_pk_bf16_f32 v73, v50, v51\n\t" \
    "v_mfma_f32_16x16x32_bf16 v[96:99], v[184:187], v[120:123], v[96:99]\n\t" \
    "v_cvt_pk_bf16_f32 v74, v52, v53\n\t" \
    "v_cvt_pk_bf16_f32 v75, v54, v55\n\t" \
    "v_mfma_f32_16x16x32_bf16 v[104:107], v[188:191], v[120:123], v[104:107]\n\t" \
    "v_cvt_pk_bf16_f32 v76, v56, v57\n\t" \
    "v_cvt_pk_bf16_f32 v77, v58, v59\n\t" \
    "v_mfma_f32_16x16x32_bf16 v[100:103], v[184:187], v[124:127], v[100:103]\n\t" \
    "v_cvt_pk_bf16_f32 v78, v60, v61\n\t" \
    "v_cvt_pk_bf16_f32 v79, v62, v63\n\t" \
    "v_mfma_f32_16x16x32_bf16 v[108:111], v[188:191], v[124:127], v[108:111]\n\t" \
    "s_waitcnt lgkmcnt(0)\n\t" \
    "v_min_u32 v238, v28, v29\n\t" \
    "v_min3_u32 v238, v238, v30, v31\n\t" \
    "v_mfma_f32_16x16x32_bf16 v[0:3], v[224:227], v[64:67], 0\n\t" \
    "v_cvt_pk_bf16_f32 v112, v80, v81\n\t" \
    "v_cvt_pk_bf16_f32 v113, v82, v83\n\t" \
    "v_mfma_f32_16x16x32_bf16 v[4:7], v[228:231], v[64:67], 0\n\t" \
    "v_cvt_pk_bf16_f32 v114, v88, v89\n\t" \
    "v_cvt_pk_bf16_f32 v115, v90, v91\n\t" \
    "v_mfma_f32_16x16x32_bf16 v[8:11], v[224:227], v[68:71], 0\n\t" \
    "v_cvt_pk_bf16_f32 v116, v84, v85\n\t" \
    "v_cvt_pk_bf16_f32 v117, v86, v87\n\t" \
    "v_mfma_f32_16x16x32_bf16 v[12:15], v[228:231], v[68:71], 0\n\t" \
    "v_cvt_pk_bf16_f32 v118, v92, v93\n\t" \
    "v_cvt_pk_bf16_f32 v119, v94, v95\n\t" \
    "v_mfma_f32_16x16x32_bf16 v[16:19], v[224:227], v[72:75], 0\n\t" \
    "v_cvt_pk_bf16_f32 v120, v96, v97\n\t" \
    "v_cvt_pk_bf16_f32 v121, v98, v99\n\t" \
    "v_mfma_f32_16x16x32_bf16 v[20:23], v[228:231], v[72:75], 0\n\t" \
    "v_cvt_pk_bf16_f32 v122, v104, v105\n\t" \
    "v_cvt_pk_bf16_f32 v123, v106, v107\n\t" \
    "v_mfma_f32_16x16x32_bf16 v[24:27], v[224:227], v[76:79], 0\n\t" \
    "v_cvt_pk_bf16_f32 v124, v100, v101\n\t" \
    "v_cvt_pk_bf16_f32 v125, v102, v103\n\t" \
    "v_mfma_f32_16x16x32_bf16 v[28:31], v[228:231], v[76:79], 0\n\t" \
    "v_cvt_pk_bf16_f32 v126, v108, v109\n\t" \
    "v_cvt_pk_bf16_f32 v127, v110, v111\n\t" \
    "v_readfirstlane_b32 s85, v238\n\t" \
    "s_cmp_lt_u32 s85, 2\n\t" \
    "s_cbranch_scc1 .Lab_s2\n\t" \
    ".Lab_s2r:\n\t" \
    "ds_write_b128 v242, v[112:115]\n\t" \
    "v_max3_f32 v64, v0, v1, v2\n\t" \
    "v_max3_f32 v66, -v0, -v1, -v2\n\t" \
    "v_max3_f32 v68, v8, v9, v10\n\t" \
    "v_max3_f32 v64, v64, v3, v4\n\t" \
    "v_max3_f32 v66, v66, -v3, -v4\n\t" \
    "v_max3_f32 v68, v68, v11, v12\n\t" \
    "v_max3_f32 v64, v64, v5, v6\n\t" \
    "v_max3_f32 v66, v66, -v5, -v6\n\t" \
    "ds_write_b128 v242, v[116:119] offset:1024\n\t" \
    "v_max3_f32 v68, v68, v13, v14\n\t" \
    "v_max_f32_e64 v64, v64, v7\n\t" \
    "v_max_f32_e64 v66, v66, -v7\n\t" \
    "v_max_f32_e64 v68, v68, v15\n\t" \
    "v_max3_f32 v70, -v8, -v9, -v10\n\t" \
    "v_permlane32_swap_b32 v64, v66\n\t" \
    "v_max3_f32 v72, v16, v17, v18\n\t" \
    "v_max3_f32 v70, v70, -v11, -v12\n\t" \
    "ds_write_b128 v242, v[120:123] offset:4096\n\t" \
    "v_max_f32 v64, v64, v66\n\t" \
    "v_max3_f32 v72, v72, v19, v20\n\t" \
    "v_max3_f32 v70, v70, -v13, -v14\n\t" \
    "v_max3_f32 v74, -v16, -v17, -v18\n\t" \
    "v_max3_f32 v72, v72, v21, v22\n\t" \
    "v_max_f32_e64 v70, v70, -v15\n\t" \
    "v_max3_f32 v74, v74, -v19, -v20\n\t" \
    "v_max_f32_e64 v72, v72, v23\n\t" \
    "ds_write_b128 v242, v[124:127] offset:5120\n\t" \
    "v_permlane32_swap_b32 v68, v70\n\t" \
    "v_max3_f32 v74, v74, -v21, -v22\n\t" \
    "v_max3_f32 v76, v24, v25, v26\n\t" \
    "v_max_f32 v68, v68, v70\n\t" \
    "v_max_f32_e64 v74, v74, -v23\n\t" \
    "v_max3_f32 v76, v76, v27, v28\n\t" \
    "v_permlane16_swap_b32 v64, v68\n\t" \
    "v_permlane32_swap_b32 v72, v74\n\t" \
    "v_max3_f32 v76, v76, v29, v30\n\t" \
    "v_max_f32 v66, v64, v68\n\t" \
    "v_max_f32 v72, v72, v74\n\t" \
    "v_max_f32_e64 v76, v76, v31\n\t" \
    "v_xor_b32 v254, 32, v244\n\t" \
    "v_mul_f32 v64, 0xbfb8aa3b, v66\n\t" \
    "v_mul_f32 v68, 0xbfb8aa3b, v66\n\t" \
    "v_xor_b32 v245, 0x2000, v244\n\t" \
    "v_max3_f32 v78, -v24, -v25, -v26\n\t" \
    "v_max3_f32 v78, v78, -v27, -v28\n\t" \
    "v_xor_b32 v255, 0x2020, v244\n\t" \
    "v_permlane16_swap_b32 v64, v68\n\t" \
    "v_mov_b32 v66, v64\n\t" \
    "v_xor_b32 v242, 0x2000, v242\n\t" \
    "v_max3_f32 v78, v78, -v29, -v30\n\t" \
    "v_mov_b32 v70, v68\n\t" \
    "v_permlane32_swap_b32 v64, v66\n\t" \
    "v_max_f32_e64 v78, v78, -v31\n\t" \
    "v_permlane32_swap_b32 v68, v70\n\t" \
    "v_pk_fma_f32 v[32:33], v[0:1], s[90:91], v[64:65] op_sel_hi:[1,1,0]\n\t" \
    "v_permlane32_swap_b32 v76, v78\n\t" \
    "v_pk_fma_f32 v[0:1], v[0:1], s[90:91], v[66:67] op_sel_hi:[1,1,0] neg_lo:[1,0,0] neg_hi:[1,0,0]\n\t" \
    "v_pk_fma_f32 v[34:35], v[2:3], s[90:91], v[64:65] op_sel_hi:[1,1,0]\n\t" \
    "v_max_f32 v76, v76, v78\n\t" \
    "v_pk_fma_f32 v[2:3], v[2:3], s[90:91], v[66:67] op_sel_hi:[1,1,0] neg_lo:[1,0,0] neg_hi:[1,0,0]\n\t" \
    "v_pk_fma_f32 v[36:37], v[4:5], s[90:91], v[64:65] op_sel_hi:[1,1,0]\n\t" \
    "v_permlane16_swap_b32 v72, v76\n\t" \
    "v_pk_fma_f32 v[4:5], v[4:5], s[90:91], v[66:67] op_sel_hi:[1,1,0] neg_lo:[1,0,0] neg_hi:[1,0,0]\n\t" \
    "v_pk_fma_f32 v[38:39], v[6:7], s[90:91], v[64:65] op_sel_hi:[1,1,0]\n\t" \
    "v_max_f32 v74, v72, v76\n\t" \
    "v_pk_fma_f32 v[6:7], v[6:7], s[90:91], v[66:67] op_sel_hi:[1,1,0] neg_lo:[1,0,0] neg_hi:[1,0,0]\n\t" \
    "v_exp_f32 v32, v32\n\t" \
    "v_mul_f32 v72, 0xbfb8aa3b, v74\n\t" \
    "v_mul_f32 v76, 0xbfb8aa3b, v74\n\t" \
    "v_exp_f32 v0, v0\n\t" \
    "v_exp_f32 v33, v33\n\t" \
    "v_permlane16_swap_b32 v72, v76\n\t" \
    "v_exp_f32 v1, v1\n\t" \
    "v_exp_f32 v34, v34\n\t" \
    "v_mov_b32 v74, v72\n\t" \
    "v_mov_b32 v78, v76\n\t" \
    "v_exp_f32 v2, v2\n\t" \
    "v_permlane32_swap_b32 v72, v74\n\t" \
    "v_permlane32_swap_b32 v76, v78\n\t" \
    "v_exp_f32 v35, v35\n\t" \
    "v_exp_f32 v3, v3\n\t" \
    "v_exp_f32 v36, v36\n\t" \
    "v_pk_add_f32 v[64:65], v[32:33], v[34:35]\n\t" \
    "v_pk_add_f32 v[66:67], v[0:1], v[2:3]\n\t" \
    "v_exp_f32 v4, v4\n\t" \
    "v_exp_f32 v37, v37\n\t" \
    "v_exp_f32 v5, v5\n\t" \
    "v_exp_f32 v38, v38\n\t" \
    "v_pk_add_f32 v[64:65], v[64:65], v[36:37]\n\t" \
    "v_pk_add_f32 v[66:67], v[66:67], v[4:5]\n\t" \
    "v_exp_f32 v6, v6\n\t" \
    "v_exp_f32 v39, v39\n\t" \
    "v_exp_f32 v7, v7\n\t" \
    "v_pk_fma_f32 v[40:41], v[8:9], s[90:91], v[68:69] op_sel_hi:[1,1,0]\n\t" \
    "v_pk_add_f32 v[64:65], v[64:65], v[38:39]\n\t" \
    "v_pk_add_f32 v[66:67], v[66:67], v[6:7]\n\t" \
    "v_pk_fma_f32 v[8:9], v[8:9], s[90:91], v[70:71] op_sel_hi:[1,1,0] neg_lo:[1,0,0] neg_hi:[1,0,0]\n\t" \
    "v_add_f32 v64, v64, v65\n\t" \
    "v_add_f32 v66, v66, v67\n\t" \
    "v_pk_fma_f32 v[42:43], v[10:11], s[90:91], v[68:69] op_sel_hi:[1,1,0]\n\t" \
    "v_pk_fma_f32 v[10:11], v[10:11], s[90:91], v[70:71] op_sel_hi:[1,1,0] neg_lo:[1,0,0] neg_hi:[1,0,0]\n\t" \
    "v_permlane32_swap_b32 v64, v66\n\t" \
    "v_pk_fma_f32 v[44:45], v[12:13], s[90:91], v[68:69] op_sel_hi:[1,1,0]\n\t" \
    "v_pk_fma_f32 v[12:13], v[12:13], s[90:91], v[70:71] op_sel_hi:[1,1,0] neg_lo:[1,0,0] neg_hi:[1,0,0]\n\t" \
    "v_add_f32 v64, v64, v66\n\t" \
    "v_pk_fma_f32 v[46:47], v[14:15], s[90:91], v[68:69] op_sel_hi:[1,1,0]\n\t" \
    "v_pk_fma_f32 v[14:15], v[14:15], s[90:91], v[70:71] op_sel_hi:[1,1,0] neg_lo:[1,0,0] neg_hi:[1,0,0]\n\t" \
    "v_exp_f32 v40, v40\n\t" \
    "v_exp_f32 v8, v8\n\t" \
    "v_exp_f32 v41, v41\n\t" \
    "v_exp_f32 v9, v9\n\t" \
    "v_exp_f32 v42, v42\n\t" \
    "v_exp_f32 v10, v10\n\t" \
    "v_exp_f32 v43, v43\n\t" \
    "v_exp_f32 v11, v11\n\t" \
    "v_exp_f32 v44, v44\n\t" \
    "v_pk_add_f32 v[68:69], v[40:41], v[42:43]\n\t" \
    "v_pk_add_f32 v[70:71], v[8:9], v[10:11]\n\t" \
    "v_exp_f32 v12, v12\n\t" \
    "v_exp_f32 v45, v45\n\t" \
    "v_exp_f32 v13, v13\n\t" \
    "v_exp_f32 v46, v46\n\t" \
    "v_pk_add_f32 v[68:69], v[68:69], v[44:45]\n\t" \
    "v_pk_add_f32 v[70:71], v[70:71], v[12:13]\n\t" \
    "v_exp_f32 v14, v14\n\t" \
    "v_exp_f32 v47, v47\n\t" \
    "v_exp_f32 v15, v15\n\t" \
    "v_pk_fma_f32 v[48:49], v[16:17], s[90:91], v[72:73] op_sel_hi:[1,1,0]\n\t" \
    "v_pk_add_f32 v[68:69], v[68:69], v[46:47]\n\t" \
    "v_pk_add_f32 v[70:71], v[70:71], v[14:15]\n\t" \
    ".Lpad_ab1:\n\t" \
    "s_cbranch_execz .Lpad_ret\n\t" \
    "v_pk_fma_f32 v[16:17], v[16:17], s[90:91], v[74:75] op_sel_hi:[1,1,0] neg_lo:[1,0,0] neg_hi:[1,0,0]\n\t" \
    "v_add_f32 v68, v68, v69\n\t" \
    "v_add_f32 v70, v70, v71\n\t" \
    "v_pk_fma_f32 v[50:51], v[18:19], s[90:91], v[72:73] op_sel_hi:[1,1,0]\n\t" \
    "v_pk_fma_f32 v[18:19], v[18:19], s[90:91], v[74:75] op_sel_hi:[1,1,0] neg_lo:[1,0,0] neg_hi:[1,0,0]\n\t" \
    "v_permlane32_swap_b32 v68, v70\n\t" \
    "v_pk_fma_f32 v[52:53], v[20:21], s[90:91], v[72:73] op_sel_hi:[1,1,0]\n\t" \
    "v_pk_fma_f32 v[20:21], v[20:21], s[90:91], v[74:75] op_sel_hi:[1,1,0] neg_lo:[1,0,0] neg_hi:[1,0,0]\n\t" \
    "v_add_f32 v68, v68, v70\n\t" \
    "v_pk_fma_f32 v[54:55], v[22:23], s[90:91], v[72:73] op_sel_hi:[1,1,0]\n\t" \
    "v_pk_fma_f32 v[22:23], v[22:23], s[90:91], v[74:75] op_sel_hi:[1,1,0] neg_lo:[1,0,0] neg_hi:[1,0,0]\n\t" \
    "v_permlane16_swap_b32 v64, v68\n\t" \
    "v_exp_f32 v48, v48\n\t" \
    "v_exp_f32 v16, v16\n\t" \
    "v_add_f32 v66, v64, v68\n\t" \
    "v_exp_f32 v49, v49\n\t" \
    "v_exp_f32 v17, v17\n\t" \
    "v_rcp_f32 v64, v66\n\t" \
    "v_exp_f32 v50, v50\n\t" \
    "v_exp_f32 v18, v18\n\t" \
    "v_mov_b32 v68, v64\n\t" \
    "v_exp_f32 v51, v51\n\t" \
    "v_exp_f32 v19, v19\n\t" \
    "v_permlane16_swap_b32 v64, v68\n\t" \
    "v_pk_add_f32 v[72:73], v[48:49], v[50:51]\n\t" \
    "v_pk_add_f32 v[74:75], v[16:17], v[18:19]\n\t" \
    "v_mov_b32 v66, v64\n\t" \
    "v_mov_b32 v70, v68\n\t" \
    "v_exp_f32 v52, v52\n\t" \
    "v_permlane32_swap_b32 v64, v66\n\t" \
    "v_permlane32_swap_b32 v68, v70\n\t" \
    "v_exp_f32 v20, v20\n\t" \
    "v_pk_mul_f32 v[0:1], v[0:1], v[66:67] op_sel_hi:[1,0]\n\t" \
    "v_pk_mul_f32 v[32:33], v[32:33], v[64:65] op_sel_hi:[1,0]\n\t" \
    "v_pk_mul_f32 v[2:3], v[2:3], v[66:67] op_sel_hi:[1,0]\n\t" \
    "v_pk_mul_f32 v[34:35], v[34:35], v[64:65] op_sel_hi:[1,0]\n\t" \
    "v_pk_mul_f32 v[4:5], v[4:5], v[66:67] op_sel_hi:[1,0]\n\t" \
    "v_pk_mul_f32 v[36:37], v[36:37], v[64:65] op_sel_hi:[1,0]\n\t" \
    "v_pk_mul_f32 v[6:7], v[6:7], v[66:67] op_sel_hi:[1,0]\n\t" \
    "v_pk_mul_f32 v[38:39], v[38:39], v[64:65] op_sel_hi:[1,0]\n\t" \
    "v_exp_f32 v53, v53\n\t" \
    "v_cvt_pk_bf16_f32 v7, v6, v7\n\t" \
    "v_cvt_pk_bf16_f32 v6, v4, v5\n\t" \
    "v_cvt_pk_bf16_f32 v5, v2, v3\n\t" \
    "v_cvt_pk_bf16_f32 v4, v0, v1\n\t" \
    "ds_write_b64 v253, v[6:7] offset:0\n\t" \
    "v_cvt_pk_bf16_f32 v0, v32, v33\n\t" \
    "v_cvt_pk_bf16_f32 v1, v34, v35\n\t" \
    "v_cvt_pk_bf16_f32 v2, v36, v37\n\t" \
    "v_cvt_pk_bf16_f32 v3, v38, v39\n\t" \
    "v_pk_add_f32 v[72:73], v[72:73], v[52:53]\n\t" \
    "ds_write_b64 v243, v[0:1] offset:0\n\t" \
    "v_exp_f32 v21, v21\n\t" \
    "v_exp_f32 v54, v54\n\t" \
    "v_exp_f32 v22, v22\n\t" \
    "v_pk_add_f32 v[74:75], v[74:75], v[20:21]\n\t" \
    "v_exp_f32 v55, v55\n\t" \
    "ds_write_b64 v251, v[2:3] offset:0\n\t" \
    "v_exp_f32 v23, v23\n\t" \
    "v_pk_add_f32 v[72:73], v[72:73], v[54:55]\n\t" \
    "v_pk_fma_f32 v[56:57], v[24:25], s[90:91], v[76:77] op_sel_hi:[1,1,0]\n\t" \
    "v_pk_add_f32 v[74:75], v[74:75], v[22:23]\n\t" \
    "v_add_f32 v72, v72, v73\n\t" \
    "ds_write_b64 v252, v[4:5] offset:0\n\t" \
    "v_add_f32 v74, v74, v75\n\t" \
    "v_pk_fma_f32 v[24:25], v[24:25], s[90:91], v[78:79] op_sel_hi:[1,1,0] neg_lo:[1,0,0] neg_hi:[1,0,0]\n\t" \
    "v_pk_fma_f32 v[58:59], v[26:27], s[90:91], v[76:77] op_sel_hi:[1,1,0]\n\t" \
    "v_permlane32_swap_b32 v72, v74\n\t" \
    "v_pk_fma_f32 v[26:27], v[26:27], s[90:91], v[78:79] op_sel_hi:[1,1,0] neg_lo:[1,0,0] neg_hi:[1,0,0]\n\t" \
    "v_pk_fma_f32 v[60:61], v[28:29], s[90:91], v[76:77] op_sel_hi:[1,1,0]\n\t" \
    "v_add_f32 v72, v72, v74\n\t" \
    "v_pk_fma_f32 v[28:29], v[28:29], s[90:91], v[78:79] op_sel_hi:[1,1,0] neg_lo:[1,0,0] neg_hi:[1,0,0]\n\t" \
    "v_pk_fma_f32 v[62:63], v[30:31], s[90:91], v[76:77] op_sel_hi:[1,1,0]\n\t" \
    "v_pk_fma_f32 v[30:31], v[30:31], s[90:91], v[78:79] op_sel_hi:[1,1,0] neg_lo:[1,0,0] neg_hi:[1,0,0]\n\t" \
    "v_exp_f32 v56, v56\n\t" \
    "v_exp_f32 v24, v24\n\t" \
    "v_exp_f32 v57, v57\n\t" \
    "v_exp_f32 v25, v25\n\t" \
    "v_exp_f32 v58, v58\n\t" \
    "v_exp_f32 v26, v26\n\t" \
    "v_exp_f32 v59, v59\n\t" \
    "v_exp_f32 v27, v27\n\t" \
    "v_exp_f32 v60, v60\n\t" \
    "v_pk_add_f32 v[76:77], v[56:57], v[58:59]\n\t" \
    "v_pk_add_f32 v[78:79], v[24:25], v[26:27]\n\t" \
    "v_exp_f32 v28, v28\n\t" \
    "v_exp_f32 v61, v61\n\t" \
    "v_exp_f32 v29, v29\n\t" \
    "v_exp_f32 v62, v62\n\t" \
    "v_pk_add_f32 v[76:77], v[76:77], v[60:61]\n\t" \
    "v_pk_add_f32 v[78:79], v[78:79], v[28:29]\n\t" \
    "v_exp_f32 v30, v30\n\t" \
    "v_exp_f32 v63, v63\n\t" \
    "v_exp_f32 v31, v31\n\t" \
    "v_pk_mul_f32 v[8:9], v[8:9], v[70:71] op_sel_hi:[1,0]\n\t" \
    "v_pk_add_f32 v[76:77], v[76:77], v[62:63]\n\t" \
    "v_pk_add_f32 v[78:79], v[78:79], v[30:31]\n\t" \
    "v_pk_mul_f32 v[40:41], v[40:41], v[68:69] op_sel_hi:[1,0]\n\t" \
    "v_add_f32 v76, v76, v77\n\t" \
    "v_add_f32 v78, v78, v79\n\t" \
    "v_pk_mul_f32 v[10:11], v[10:11], v[70:71] op_sel_hi:[1,0]\n\t" \
    "v_pk_mul_f32 v[42:43], v[42:43], v[68:69] op_sel_hi:[1,0]\n\t" \
    "v_permlane32_swap_b32 v76, v78\n\t" \
    "v_pk_mul_f32 v[12:13], v[12:13], v[70:71] op_sel_hi:[1,0]\n\t" \
    "v_pk_mul_f32 v[44:45], v[44:45], v[68:69] op_sel_hi:[1,0]\n\t" \
    "v_add_f32 v76, v76, v78\n\t" \
    "v_pk_mul_f32 v[14:15], v[14:15], v[70:71] op_sel_hi:[1,0]\n\t" \
    "v_pk_mul_f32 v[46:47], v[46:47], v[68:69] op_sel_hi:[1,0]\n\t" \
    "v_permlane16_swap_b32 v72, v76\n\t" \
    "v_cvt_pk_bf16_f32 v15, v14, v15\n\t" \
    "v_cvt_pk_bf16_f32 v14, v12, v13\n\t" \
    "v_add_f32 v74, v72, v76\n\t" \
    "v_cvt_pk_bf16_f32 v13, v10, v11\n\t" \
    "ds_write_b64 v253, v[14:15] offset:2048\n\t" \
    "v_rcp_f32 v72, v74\n\t" \
    "v_cvt_pk_bf16_f32 v12, v8, v9\n\t" \
    "v_cvt_pk_bf16_f32 v8, v40, v41\n\t" \
    "v_mov_b32 v76, v72\n\t" \
    "v_cvt_pk_bf16_f32 v9, v42, v43\n\t" \
    "ds_write_b64 v252, v[12:13] offset:2048\n\t" \
    "v_permlane16_swap_b32 v72, v76\n\t" \
    "v_cvt_pk_bf16_f32 v10, v44, v45\n\t" \
    "v_cvt_pk_bf16_f32 v11, v46, v47\n\t" \
    "v_mov_b32 v74, v72\n\t" \
    "v_mov_b32 v78, v76\n\t" \
    "ds_write_b64 v243, v[8:9] offset:2048\n\t" \
    "v_permlane32_swap_b32 v72, v74\n\t" \
    "v_permlane32_swap_b32 v76, v78\n\t" \
    "ds_write_b64 v251, v[10:11] offset:2048\n\t" \
    "v_mov_b32 v237, 3\n\t" \
    "v_pk_mul_f32 v[16:17], v[16:17], v[74:75] op_sel_hi:[1,0]\n\t" \
    "ds_write_b32 v248, v237\n\t" \
    "v_pk_mul_f32 v[48:49], v[48:49], v[72:73] op_sel_hi:[1,0]\n\t" \
    "ds_read_b64_tr_b16 v[96:97], v244\n\t" \
    "v_pk_mul_f32 v[18:19], v[18:19], v[74:75] op_sel_hi:[1,0]\n\t" \
    "ds_read_b64_tr_b16 v[98:99], v244 offset:2048\n\t" \
    "v_pk_mul_f32 v[50:51], v[50:51], v[72:73] op_sel_hi:[1,0]\n\t" \
    "ds_read_b64_tr_b16 v[100:101], v254\n\t" \
    "v_pk_mul_f32 v[20:21], v[20:21], v[74:75] op_sel_hi:[1,0]\n\t" \
    "ds_read_b64_tr_b16 v[102:103], v254 offset:2048\n\t" \
    "v_pk_mul_f32 v[52:53], v[52:53], v[72:73] op_sel_hi:[1,0]\n\t" \
    "v_pk_mul_f32 v[22:23], v[22:23], v[74:75] op_sel_hi:[1,0]\n\t" \
    "v_pk_mul_f32 v[54:55], v[54:55], v[72:73] op_sel_hi:[1,0]\n\t" \
    "v_pk_mul_f32 v[24:25], v[24:25], v[78:79] op_sel_hi:[1,0]\n\t" \
    "v_cvt_pk_bf16_f32 v23, v22, v23\n\t" \
    "v_cvt_pk_bf16_f32 v22, v20, v21\n\t" \
    "v_cvt_pk_bf16_f32 v21, v18, v19\n\t" \
    "v_cvt_pk_bf16_f32 v20, v16, v17\n\t" \
    "ds_write_b64 v253, v[22:23] offset:4096\n\t" \
    "v_cvt_pk_bf16_f32 v16, v48, v49\n\t" \
    "v_cvt_pk_bf16_f32 v17, v50, v51\n\t" \
    "v_cvt_pk_bf16_f32 v18, v52, v53\n\t" \
    "v_cvt_pk_bf16_f32 v19, v54, v55\n\t" \
    "v_pk_mul_f32 v[56:57], v[56:57], v[76:77] op_sel_hi:[1,0]\n\t" \
    "ds_write_b64 v243, v[16:17] offset:4096\n\t" \
    "v_pk_mul_f32 v[26:27], v[26:27], v[78:79] op_sel_hi:[1,0]\n\t" \
    "v_pk_mul_f32 v[58:59], v[58:59], v[76:77] op_sel_hi:[1,0]\n\t" \
    "v_pk_mul_f32 v[28:29], v[28:29], v[78:79] op_sel_hi:[1,0]\n\t" \
    "v_pk_mul_f32 v[60:61], v[60:61], v[76:77] op_sel_hi:[1,0]\n\t" \
    "v_pk_mul_f32 v[30:31], v[30:31], v[78:79] op_sel_hi:[1,0]\n\t" \
    "ds_write_b64 v251, v[18:19] offset:4096\n\t" \
    "v_pk_mul_f32 v[62:63], v[62:63], v[76:77] op_sel_hi:[1,0]\n\t" \
    "v_cvt_pk_bf16_f32 v31, v30, v31\n\t" \
    "v_cvt_pk_bf16_f32 v30, v28, v29\n\t" \
    "v_cvt_pk_bf16_f32 v29, v26, v27\n\t" \
    "v_cvt_pk_bf16_f32 v28, v24, v25\n\t" \
    "ds_write_b64 v252, v[20:21] offset:4096\n\t" \
    "ds_read_b32 v238, v250\n\t" \
    "v_cvt_pk_bf16_f32 v24, v56, v57\n\t" \
    "ds_read_b64_tr_b16 v[104:105], v245\n\t" \
    "v_cvt_pk_bf16_f32 v25, v58, v59\n\t" \
    "ds_read_b64_tr_b16 v[106:107], v245 offset:2048\n\t" \
    "v_cvt_pk_bf16_f32 v26, v60, v61\n\t" \
    "ds_read_b64_tr_b16 v[108:109], v255\n\t" \
    "v_cvt_pk_bf16_f32 v27, v62, v63\n\t" \
    "ds_read_b64_tr_b16 v[110:111], v255 offset:2048\n\t" \
    "ds_write_b64 v243, v[24:25] offset:6144\n\t" \
    "ds_read_b128 v[80:83], v242\n\t" \
    "ds_write_b64 v251, v[26:27] offset:6144\n\t" \
    "ds_read_b128 v[84:87], v242 offset:1024\n\t" \
    "ds_write_b64 v252, v[28:29] offset:6144\n\t" \
    "ds_read_b128 v[88:91], v242 offset:4096\n\t" \
    "ds_write_b64 v253, v[30:31] offset:6144\n\t" \
    "ds_read_b128 v[92:95], v242 offset:5120\n\t" \
    "v_mov_b32 v237, 4\n\t" \
    "ds_write_b32 v248, v237\n\t" \
    "s_waitcnt lgkmcnt(14)\n\t" \
    "v_mfma_f32_16x16x32_bf16 v[64:67], v[96:99], v[232:235], 0\n\t" \
    "ds_read_b64_tr_b16 v[0:1], v244 offset:4096\n\t" \
    "ds_read_b64_tr_b16 v[2:3], v244 offset:6144\n\t" \
    "v_mfma_f32_16x16x32_bf16 v[68:71], v[100:103], v[232:235], 0\n\t" \
    "ds_read_b64_tr_b16 v[4:5], v254 offset:4096\n\t" \
    "ds_read_b64_tr_b16 v[6:7], v254 offset:6144\n\t" \
    "v_mfma_f32_16x16x32_bf16 v[32:35], v[96:99], v[112:115], 0\n\t" \
    "v_mfma_f32_16x16x32_bf16 v[40:43], v[100:103], v[112:115], 0\n\t" \
    "s_waitcnt lgkmcnt(14)\n\t" \
    "v_readfirstlane_b32 s85, v238\n\t" \
    "s_cmp_lt_u32 s85, 3\n\t" \
    "s_cbranch_scc1 .Lab_s3\n\t" \
    ".Lab_s3r:\n\t" \
    "v_mfma_f32_16x16x32_bf16 v[36:39], v[96:99], v[116:119], 0\n\t" \
    "v_mfma_f32_16x16x32_bf16 v[44:47], v[100:103], v[116:119], 0\n\t" \
    "ds_read_b32 v238, v250\n\t" \
    "ds_read_b64_tr_b16 v[8:9], v245 offset:4096\n\t" \
    "v_mfma_f32_16x16x32_bf16 v[64:67], v[104:107], v[232:235], v[64:67]\n\t" \
    "ds_read_b64_tr_b16 v[10:11], v245 offset:6144\n\t" \
    "ds_read_b64_tr_b16 v[12:13], v255 offset:4096\n\t" \
    "s_waitcnt lgkmcnt(14)\n\t" \
    "v_mfma_f32_16x16x32_bf16 v[68:71], v[108:111], v[232:235], v[68:71]\n\t" \
    "ds_read_b64_tr_b16 v[14:15], v255 offset:6144\n\t" \
    "v_mfma_f32_16x16x32_bf16 v[32:35], v[104:107], v[80:83], v[32:35]\n\t" \
    "v_mfma_f32_16x16x32_bf16 v[40:43], v[108:111], v[80:83], v[40:43]\n\t" \
    "s_waitcnt lgkmcnt(14)\n\t" \
    "v_mfma_f32_16x16x32_bf16 v[36:39], v[104:107], v[84:87], v[36:39]\n\t" \
    "v_add_u32 v253, 135168, v241\n\t" \
    "v_mfma_f32_16x16x32_bf16 v[44:47], v[108:111], v[84:87], v[44:47]\n\t" \
    "v_xor_b32 v251, 32, v253\n\t" \
    "s_waitcnt lgkmcnt(7)\n\t" \
    "v_mfma_f32_16x16x32_bf16 v[64:67], v[0:3], v[232:235], v[64:67]\n\t" \
    "v_mov_b32 v237, 5\n\t" \
    "s_waitcnt lgkmcnt(5)\n\t" \
    "v_mfma_f32_16x16x32_bf16 v[68:71], v[4:7], v[232:235], v[68:71]\n\t" \
    "v_mfma_f32_16x16x32_bf16 v[32:35], v[0:3], v[120:123], v[32:35]\n\t" \
    "v_mfma_f32_16x16x32_bf16 v[40:43], v[4:7], v[120:123], v[40:43]\n\t" \
    "v_mfma_f32_16x16x32_bf16 v[36:39], v[0:3], v[124:127], v[36:39]\n\t" \
    "s_waitcnt lgkmcnt(4)\n\t" \
    "v_readfirstlane_b32 s85, v238\n\t" \
    "s_cmp_lt_u32 s85, 4\n\t" \
    "s_cbranch_scc1 .Lab_s4\n\t" \
    ".Lab_s4r:\n\t" \
    "v_mfma_f32_16x16x32_bf16 v[44:47], v[4:7], v[124:127], v[44:47]\n\t" \
    "ds_write_b32 v248, v237\n\t" \
    "s_waitcnt lgkmcnt(3)\n\t" \
    "v_mfma_f32_16x16x32_bf16 v[64:67], v[8:11], v[232:235], v[64:67]\n\t" \
    "ds_read_b32 v238, v249 offset:92\n\t" \
    "s_waitcnt lgkmcnt(2)\n\t" \
    "v_mfma_f32_16x16x32_bf16 v[68:71], v[12:15], v[232:235], v[68:71]\n\t" \
    "v_mfma_f32_16x16x32_bf16 v[32:35], v[8:11], v[88:91], v[32:35]\n\t" \
    "v_mfma_f32_16x16x32_bf16 v[40:43], v[12:15], v[88:91], v[40:43]\n\t" \
    "v_mfma_f32_16x16x32_bf16 v[36:39], v[8:11], v[92:95], v[36:39]\n\t" \
    "v_mfma_f32_16x16x32_bf16 v[44:47], v[12:15], v[92:95], v[44:47]\n\t" \
    "ds_read_b64_tr_b16 v[80:81], v253\n\t" \
    "ds_read_b64_tr_b16 v[82:83], v253 offset:512\n\t" \
    "ds_read_b64_tr_b16 v[84:85], v251\n\t" \
    "s_nop 0\n\t" \
    "v_cndmask_b32 v24, v68, v64, vcc\n\t" \
    "v_cndmask_b32 v25, v69, v65, vcc\n\t" \
    "v_cndmask_b32 v26, v70, v66, vcc\n\t" \
    "v_cndmask_b32 v27, v71, v67, vcc\n\t" \
    "v_add3_u32 v30, v24, v25, v26\n\t" \
    "v_add_u32 v30, v30, v27\n\t" \
    "v_cvt_pk_bf16_f32 v16, v32, v33\n\t" \
    "v_cvt_pk_bf16_f32 v17, v34, v35\n\t" \
    "v_cvt_pk_bf16_f32 v18, v40, v41\n\t" \
    "v_cvt_pk_bf16_f32 v19, v42, v43\n\t" \
    "v_cvt_pk_bf16_f32 v20, v36, v37\n\t" \
    "v_cvt_pk_bf16_f32 v21, v38, v39\n\t" \
    "v_cvt_pk_bf16_f32 v22, v44, v45\n\t" \
    "v_cvt_pk_bf16_f32 v23, v46, v47\n\t" \
    "v_add3_u32 v28, v16, v17, v18\n\t" \
    "v_add3_u32 v29, v20, v21, v22\n\t" \
    "v_add_u32 v28, v28, v19\n\t" \
    "v_add_u32 v29, v29, v23\n\t" \
    "v_cndmask_b32_e64 v30, 0, v30, s[94:95]\n\t" \
    "buffer_store_dwordx4 v[16:19], v246, %[rsb], s87 offen sc1\n\t" \
    "v_permlane16_swap_b32 v28, v29\n\t" \
    "v_add3_u32 v28, v28, v29, v30\n\t" \
    "buffer_store_dwordx4 v[20:23], v246, %[rsb], s86 offen sc1\n\t" \
    "v_cndmask_b32_e64 v25, v25, v236, s[92:93]\n\t" \
    "v_add_u32_dpp v28, v28, v28 row_ror:8 row_mask:0xf bank_mask:0xf bound_ctrl:1\n\t" \
    "v_mov_b32 v237, 6\n\t" \
    "ds_read_b64_tr_b16 v[86:87], v251 offset:512\n\t" \
    "v_add_u32_dpp v28, v28, v28 row_ror:4 row_mask:0xf bank_mask:0xf bound_ctrl:1\n\t" \
    "ds_read_b64_tr_b16 v[88:89], v253 offset:4096\n\t" \
    "ds_read_b64_tr_b16 v[90:91], v253 offset:4608\n\t" \
    "v_add_u32_dpp v28, v28, v28 row_ror:2 row_mask:0xf bank_mask:0xf bound_ctrl:1\n\t" \
    "ds_read_b64_tr_b16 v[92:93], v251 offset:4096\n\t" \
    "ds_read_b64_tr_b16 v[94:95], v251 offset:4608\n\t" \
    "v_add_u32_dpp v28, v28, v28 row_ror:1 row_mask:0xf bank_mask:0xf bound_ctrl:1\n\t" \
    "v_cndmask_b32_e64 v24, v24, v28, s[92:93]\n\t" \
    "buffer_store_dwordx4 v[24:27], v247, %[rsb], s88 offen sc1\n\t" \
    "ds_write_b32 v248, v237\n\t" \
    "s_setprio 1\n\t" \
    "s_waitcnt lgkmcnt(9)\n\t" \
    "v_readfirstlane_b32 s85, v238\n\t" \
    "s_cmp_lt_u32 s85, 1\n\t" \
    "s_cbranch_scc1 .Lab_s5\n\t" \
    ".Lab_s5r:\n\t" \
    "s_waitcnt lgkmcnt(7)\n\t" \
    "v_mfma_f32_16x16x32_bf16 v[192:195], v[80:83], v[128:131], v[192:195]\n\t" \
    "v_add_u32 v244, 49152, v241\n\t" \
    "v_xor_b32 v245, 32, v244\n\t" \
    "v_mfma_f32_16x16x32_bf16 v[200:203], v[80:83], v[132:135], v[200:203]\n\t" \
    "ds_read_b64_tr_b16 v[96:97], v244\n\t" \
    "ds_read_b64_tr_b16 v[98:99], v244 offset:512\n\t" \
    "v_mfma_f32_16x16x32_bf16 v[208:211], v[80:83], v[136:139], v[208:211]\n\t" \
    "ds_read_b64_tr_b16 v[100:101], v245\n\t" \
    "ds_read_b64_tr_b16 v[102:103], v245 offset:512\n\t" \
    "v_mfma_f32_16x16x32_bf16 v[216:219], v[80:83], v[140:143], v[216:219]\n\t" \
    "ds_read_b64_tr_b16 v[104:105], v244 offset:4096\n\t" \
    "ds_read_b64_tr_b16 v[106:107], v244 offset:4608\n\t" \
    "s_waitcnt lgkmcnt(11)\n\t" \
    "v_mfma_f32_16x16x32_bf16 v[196:199], v[84:87], v[128:131], v[196:199]\n\t" \
    "ds_read_b64_tr_b16 v[108:109], v245 offset:4096\n\t" \
    "ds_read_b64_tr_b16 v[110:111], v245 offset:4608\n\t" \
    "v_mfma_f32_16x16x32_bf16 v[204:207], v[84:87], v[132:135], v[204:207]\n\t" \
    "v_xor_b32 v246, 64, v240\n\t" \
    "v_subrev_u32 v242, 16384, v241\n\t" \
    "v_mfma_f32_16x16x32_bf16 v[212:215], v[84:87], v[136:139], v[212:215]\n\t" \
    "v_xor_b32 v243, 32, v242\n\t" \
    "v_add_u32 v247, 65536, v240\n\t" \
    "v_mfma_f32_16x16x32_bf16 v[220:223], v[84:87], v[140:143], v[220:223]\n\t" \
    "v_mov_b32 v237, 7\n\t" \
    "ds_write_b32 v248, v237\n\t" \
    "s_waitcnt lgkmcnt(12)\n\t" \
    "v_mfma_f32_16x16x32_bf16 v[192:195], v[88:91], v[144:147], v[192:195]\n\t" \
    "ds_read_b64_tr_b16 v[80:81], v244 offset:8192\n\t" \
    "v_mfma_f32_16x16x32_bf16 v[200:203], v[88:91], v[148:151], v[200:203]\n\t" \
    "ds_read_b128 v[128:131], v240 offset:40960\n\t" \
    "v_mfma_f32_16x16x32_bf16 v[208:211], v[88:91], v[152:155], v[208:211]\n\t" \
    "ds_read_b64_tr_b16 v[82:83], v244 offset:8704\n\t" \
    "v_add_u32 v237, 1, v237\n\t" \
    "v_mfma_f32_16x16x32_bf16 v[216:219], v[88:91], v[156:159], v[216:219]\n\t" \
    "ds_read_b128 v[132:135], v240 offset:43008\n\t" \
    "s_waitcnt lgkmcnt(14)\n\t" \
    "v_mfma_f32_16x16x32_bf16 v[196:199], v[92:95], v[144:147], v[196:199]\n\t" \
    "ds_read_b64_tr_b16 v[84:85], v245 offset:8192\n\t" \
    "v_xor_b32 v255, 64, v247\n\t" \
    "v_mfma_f32_16x16x32_bf16 v[204:207], v[92:95], v[148:151], v[204:207]\n\t" \
    "ds_read_b128 v[136:139], v240 offset:45056\n\t" \
    "v_mfma_f32_16x16x32_bf16 v[212:215], v[92:95], v[152:155], v[212:215]\n\t" \
    "ds_read_b64_tr_b16 v[86:87], v245 offset:8704\n\t" \
    "v_mfma_f32_16x16x32_bf16 v[220:223], v[92:95], v[156:159], v[220:223]\n\t" \
    "ds_read_b128 v[140:143], v240 offset:47104\n\t" \
    "s_waitcnt lgkmcnt(14)\n\t" \
    "v_mfma_f32_16x16x32_bf16 v[192:195], v[96:99], v[160:163], v[192:195]\n\t" \
    "ds_read_b64_tr_b16 v[88:89], v244 offset:12288\n\t" \
    "v_mfma_f32_16x16x32_bf16 v[200:203], v[96:99], v[164:167], v[200:203]\n\t" \
    "ds_read_b128 v[144:147], v246 offset:40960\n\t" \
    "v_mfma_f32_16x16x32_bf16 v[208:211], v[96:99], v[168:171], v[208:211]\n\t" \
    "ds_read_b64_tr_b16 v[90:91], v244 offset:12800\n\t" \
    "v_mfma_f32_16x16x32_bf16 v[216:219], v[96:99], v[172:175], v[216:219]\n\t" \
    "ds_read_b128 v[148:151], v246 offset:43008\n\t" \
    "s_waitcnt lgkmcnt(14)\n\t" \
    "v_mfma_f32_16x16x32_bf16 v[196:199], v[100:103], v[160:163], v[196:199]\n\t" \
    "ds_read_b64_tr_b16 v[92:93], v245 offset:12288\n\t" \
    "v_mfma_f32_16x16x32_bf16 v[204:207], v[100:103], v[164:167], v[204:207]\n\t" \
    "ds_read_b128 v[152:155], v246 offset:45056\n\t" \
    "v_mfma_f32_16x16x32_bf16 v[212:215], v[100:103], v[168:171], v[212:215]\n\t" \
    "ds_read_b64_tr_b16 v[94:95], v245 offset:12800\n\t" \
    "v_mfma_f32_16x16x32_bf16 v[220:223], v[100:103], v[172:175], v[220:223]\n\t" \
    "ds_read_b128 v[156:159], v246 offset:47104\n\t" \
    "ds_write_b32 v248, v237\n\t" \
    "v_mfma_f32_16x16x32_bf16 v[192:195], v[104:107], v[176:179], v[192:195]\n\t" \
    "ds_read_b64_tr_b16 v[96:97], v242 offset:24576\n\t" \
    "v_mfma_f32_16x16x32_bf16 v[200:203], v[104:107], v[180:183], v[200:203]\n\t" \
    "ds_read_b128 v[160:163], v247 offset:16384\n\t" \
    "v_mfma_f32_16x16x32_bf16 v[208:211], v[104:107], v[184:187], v[208:211]\n\t" \
    "ds_read_b64_tr_b16 v[98:99], v242 offset:25088\n\t" \
    "v_add_u32 v237, 1, v237\n\t" \
    "v_mfma_f32_16x16x32_bf16 v[216:219], v[104:107], v[188:191], v[216:219]\n\t" \
    "ds_read_b128 v[164:167], v247 offset:18432\n\t" \
    "s_waitcnt lgkmcnt(14)\n\t" \
    "v_mfma_f32_16x16x32_bf16 v[196:199], v[108:111], v[176:179], v[196:199]\n\t" \
    "ds_read_b64_tr_b16 v[100:101], v243 offset:24576\n\t" \
    "v_mfma_f32_16x16x32_bf16 v[204:207], v[108:111], v[180:183], v[204:207]\n\t" \
    "ds_read_b128 v[168:171], v247 offset:20480\n\t" \
    "v_mfma_f32_16x16x32_bf16 v[212:215], v[108:111], v[184:187], v[212:215]\n\t" \
    "ds_read_b64_tr_b16 v[102:103], v243 offset:25088\n\t" \
    "v_mfma_f32_16x16x32_bf16 v[220:223], v[108:111], v[188:191], v[220:223]\n\t" \
    "ds_read_b128 v[172:175], v247 offset:22528\n\t" \
    "v_mfma_f32_16x16x32_bf16 v[192:195], v[80:83], v[128:131], v[192:195]\n\t" \
    "ds_read_b64_tr_b16 v[104:105], v242 offset:28672\n\t" \
    "v_mfma_f32_16x16x32_bf16 v[200:203], v[80:83], v[132:135], v[200:203]\n\t" \
    "ds_read_b128 v[176:179], v255 offset:16384\n\t" \
    "v_mfma_f32_16x16x32_bf16 v[208:211], v[80:83], v[136:139], v[208:211]\n\t" \
    "ds_read_b64_tr_b16 v[106:107], v242 offset:29184\n\t" \
    "s_waitcnt lgkmcnt(14)\n\t" \
    "v_mfma_f32_16x16x32_bf16 v[216:219], v[80:83], v[140:143], v[216:219]\n\t" \
    "ds_read_b128 v[180:183], v255 offset:18432\n\t" \
    "v_mfma_f32_16x16x32_bf16 v[196:199], v[84:87], v[128:131], v[196:199]\n\t" \
    "ds_read_b64_tr_b16 v[108:109], v243 offset:28672\n\t" \
    "v_mfma_f32_16x16x32_bf16 v[204:207], v[84:87], v[132:135], v[204:207]\n\t" \
    "ds_read_b128 v[184:187], v255 offset:20480\n\t" \
    "v_mfma_f32_16x16x32_bf16 v[212:215], v[84:87], v[136:139], v[212:215]\n\t" \
    "ds_read_b64_tr_b16 v[110:111], v243 offset:29184\n\t" \
    "v_mfma_f32_16x16x32_bf16 v[220:223], v[84:87], v[140:143], v[220:223]\n\t" \
    "ds_read_b128 v[188:191], v255 offset:22528\n\t" \
    "ds_write_b32 v248, v237\n\t" \
    "v_mfma_f32_16x16x32_bf16 v[192:195], v[88:91], v[144:147], v[192:195]\n\t" \
    "ds_read_b64_tr_b16 v[80:81], v242 offset:32768\n\t" \
    "v_mfma_f32_16x16x32_bf16 v[200:203], v[88:91], v[148:151], v[200:203]\n\t" \
    "ds_read_b128 v[128:131], v240\n\t" \
    "v_mfma_f32_16x16x32_bf16 v[208:211], v[88:91], v[152:155], v[208:211]\n\t" \
    "ds_read_b64_tr_b16 v[82:83], v242 offset:33280\n\t" \
    "v_add_u32 v237, 1, v237\n\t" \
    "s_waitcnt lgkmcnt(14)\n\t" \
    "v_mfma_f32_16x16x32_bf16 v[216:219], v[88:91], v[156:159], v[216:219]\n\t" \
    "ds_read_b128 v[132:135], v240 offset:2048\n\t" \
    "v_mfma_f32_16x16x32_bf16 v[196:199], v[92:95], v[144:147], v[196:199]\n\t" \
    "ds_read_b64_tr_b16 v[84:85], v243 offset:32768\n\t" \
    "v_mfma_f32_16x16x32_bf16 v[204:207], v[92:95], v[148:151], v[204:207]\n\t" \
    "ds_read_b128 v[136:139], v240 offset:4096\n\t" \
    "v_mfma_f32_16x16x32_bf16 v[212:215], v[92:95], v[152:155], v[212:215]\n\t" \
    "ds_read_b64_tr_b16 v[86:87], v243 offset:33280\n\t" \
    "ds_read_b32 v238, v249 offset:92\n\t" \
    "v_mfma_f32_16x16x32_bf16 v[220:223], v[92:95], v[156:159], v[220:223]\n\t" \
    "ds_read_b128 v[140:143], v240 offset:6144\n\t" \
    "v_mfma_f32_16x16x32_bf16 v[192:195], v[96:99], v[160:163], v[192:195]\n\t" \
    "ds_read_b64_tr_b16 v[88:89], v242 offset:36864\n\t" \
    "v_add_u32 v247, %[xs0], v240\n\t" \
    "v_mfma_f32_16x16x32_bf16 v[200:203], v[96:99], v[164:167], v[200:203]\n\t" \
    "ds_read_b128 v[144:147], v246\n\t" \
    "v_mfma_f32_16x16x32_bf16 v[208:211], v[96:99], v[168:171], v[208:211]\n\t" \
    "ds_read_b64_tr_b16 v[90:91], v242 offset:37376\n\t" \
    "v_xor_b32 v255, 64, v247\n\t" \
    "s_waitcnt lgkmcnt(14)\n\t" \
    "v_mfma_f32_16x16x32_bf16 v[216:219], v[96:99], v[172:175], v[216:219]\n\t" \
    "ds_read_b128 v[148:151], v246 offset:2048\n\t" \
    "v_mfma_f32_16x16x32_bf16 v[196:199], v[100:103], v[160:163], v[196:199]\n\t" \
    "ds_read_b64_tr_b16 v[92:93], v243 offset:36864\n\t" \
    "v_mfma_f32_16x16x32_bf16 v[204:207], v[100:103], v[164:167], v[204:207]\n\t" \
    "ds_read_b128 v[152:155], v246 offset:4096\n\t" \
    "v_mfma_f32_16x16x32_bf16 v[212:215], v[100:103], v[168:171], v[212:215]\n\t" \
    "ds_read_b64_tr_b16 v[94:95], v243 offset:37376\n\t" \
    "v_mfma_f32_16x16x32_bf16 v[220:223], v[100:103], v[172:175], v[220:223]\n\t" \
    "ds_read_b128 v[156:159], v246 offset:6144\n\t" \
    "ds_write_b32 v248, v237\n\t" \
    "v_mfma_f32_16x16x32_bf16 v[192:195], v[104:107], v[176:179], v[192:195]\n\t" \
    "s_waitcnt lgkmcnt(10)\n\t" \
    "v_readfirstlane_b32 s85, v238\n\t" \
    "s_cmp_lt_u32 s85, 2\n\t" \
    "s_cbranch_scc1 .Lab_s6\n\t" \
    ".Lab_s6r:\n\t" \
    "ds_read_b64_tr_b16 v[96:97], v244 offset:32768\n\t" \
    "v_mfma_f32_16x16x32_bf16 v[200:203], v[104:107], v[180:183], v[200:203]\n\t" \
    "ds_read_b128 v[160:163], v247\n\t" \
    "v_mfma_f32_16x16x32_bf16 v[208:211], v[104:107], v[184:187], v[208:211]\n\t" \
    "ds_read_b64_tr_b16 v[98:99], v244 offset:33280\n\t" \
    "v_add_u32 v237, 1, v237\n\t" \
    "v_mfma_f32_16x16x32_bf16 v[216:219], v[104:107], v[188:191], v[216:219]\n\t" \
    "ds_read_b128 v[164:167], v247 offset:2048\n\t" \
    "v_mfma_f32_16x16x32_bf16 v[196:199], v[108:111], v[176:179], v[196:199]\n\t" \
    "ds_read_b64_tr_b16 v[100:101], v245 offset:32768\n\t" \
    "v_mfma_f32_16x16x32_bf16 v[204:207], v[108:111], v[180:183], v[204:207]\n\t" \
    "ds_read_b128 v[168:171], v247 offset:4096\n\t" \
    "v_mfma_f32_16x16x32_bf16 v[212:215], v[108:111], v[184:187], v[212:215]\n\t" \
    "ds_read_b64_tr_b16 v[102:103], v245 offset:33280\n\t" \
    "v_mfma_f32_16x16x32_bf16 v[220:223], v[108:111], v[188:191], v[220:223]\n\t" \
    "ds_read_b128 v[172:175], v247 offset:6144\n\t" \
    "v_mfma_f32_16x16x32_bf16 v[192:195], v[80:83], v[128:131], v[192:195]\n\t" \
    "ds_read_b64_tr_b16 v[104:105], v244 offset:36864\n\t" \
    "v_mfma_f32_16x16x32_bf16 v[200:203], v[80:83], v[132:135], v[200:203]\n\t" \
    "ds_read_b128 v[176:179], v255\n\t" \
    "v_mfma_f32_16x16x32_bf16 v[208:211], v[80:83], v[136:139], v[208:211]\n\t" \
    "ds_read_b64_tr_b16 v[106:107], v244 offset:37376\n\t" \
    "s_waitcnt lgkmcnt(14)\n\t" \
    "v_mfma_f32_16x16x32_bf16 v[216:219], v[80:83], v[140:143], v[216:219]\n\t" \
    "ds_read_b128 v[180:183], v255 offset:2048\n\t" \
    "v_mfma_f32_16x16x32_bf16 v[196:199], v[84:87], v[128:131], v[196:199]\n\t" \
    "ds_read_b64_tr_b16 v[108:109], v245 offset:36864\n\t" \
    "v_mfma_f32_16x16x32_bf16 v[204:207], v[84:87], v[132:135], v[204:207]\n\t" \
    "ds_read_b128 v[184:187], v255 offset:4096\n\t" \
    "v_mfma_f32_16x16x32_bf16 v[212:215], v[84:87], v[136:139], v[212:215]\n\t" \
    "ds_read_b64_tr_b16 v[110:111], v245 offset:37376\n\t" \
    "v_mfma_f32_16x16x32_bf16 v[220:223], v[84:87], v[140:143], v[220:223]\n\t" \
    "ds_read_b128 v[188:191], v255 offset:6144\n\t" \
    "ds_write_b32 v248, v237\n\t" \
    "v_mfma_f32_16x16x32_bf16 v[192:195], v[88:91], v[144:147], v[192:195]\n\t" \
    "v_mbcnt_lo_u32_b32 v250, -1, 0\n\t" \
    "v_mbcnt_hi_u32_b32 v250, -1, v250\n\t" \
    "v_mfma_f32_16x16x32_bf16 v[200:203], v[88:91], v[148:151], v[200:203]\n\t" \
    "v_and_b32 v238, 15, v250\n\t" \
    "v_lshlrev_b32 v238, 3, v238\n\t" \
    "v_mfma_f32_16x16x32_bf16 v[208:211], v[88:91], v[152:155], v[208:211]\n\t" \
    "v_add_u32 v237, 1, v237\n\t" \
    "s_waitcnt lgkmcnt(14)\n\t" \
    "v_mfma_f32_16x16x32_bf16 v[216:219], v[88:91], v[156:159], v[216:219]\n\t" \
    "buffer_load_dwordx2 v[232:233], v238, %[rsw], %[sg] offen sc1\n\t" \
    "v_mfma_f32_16x16x32_bf16 v[196:199], v[92:95], v[144:147], v[196:199]\n\t" \
    "v_lshlrev_b32 v251, 4, v250\n\t" \
    "s_add_u32 s85, %[sof], %[ofc]\n\t" \
    "v_mfma_f32_16x16x32_bf16 v[204:207], v[92:95], v[148:151], v[204:207]\n\t" \
    "v_add_u32 v240, s85, v251\n\t" \
    "s_add_u32 s87, %[sof], %[ofp]\n\t" \
    "v_mfma_f32_16x16x32_bf16 v[212:215], v[92:95], v[152:155], v[212:215]\n\t" \
    "buffer_load_dwordx2 v[234:235], v238, %[rsw], %[sg] offen offset:128 sc1\n\t" \
    "v_mfma_f32_16x16x32_bf16 v[220:223], v[92:95], v[156:159], v[220:223]\n\t" \
    "v_add_u32 v241, 0x1000, v240\n\t" \
    "s_add_u32 s88, %[sof], %[ogp]\n\t" \
    "v_mfma_f32_16x16x32_bf16 v[192:195], v[96:99], v[160:163], v[192:195]\n\t" \
    "s_mov_b32 s86, 0x10000\n\t" \
    "v_add_u32 v242, 0x2000, v240\n\t" \
    "v_add_u32 v243, 0x3000, v240\n\t" \
    "s_waitcnt lgkmcnt(10)\n\t" \
    "v_mfma_f32_16x16x32_bf16 v[196:199], v[100:103], v[160:163], v[196:199]\n\t" \
    "v_bfe_u32 v244, v251, 1, 8\n\t" \
    "v_add_u32 v253, %[kac], v251\n\t" \
    "s_waitcnt lgkmcnt(6)\n\t" \
    "v_mfma_f32_16x16x32_bf16 v[192:195], v[104:107], v[176:179], v[192:195]\n\t" \
    "v_lshrrev_b32 v112, 2, v250\n\t" \
    "v_bfe_u32 v113, v250, 4, 1\n\t" \
    "s_waitcnt lgkmcnt(2)\n\t" \
    "v_mfma_f32_16x16x32_bf16 v[196:199], v[108:111], v[176:179], v[196:199]\n\t" \
    "v_lshlrev_b32 v245, 7, v112\n\t" \
    "v_bitop3_b32 v112, v112, v113, 3 bitop3:0x28\n\t" \
    "v_mfma_f32_16x16x32_bf16 v[200:203], v[96:99], v[164:167], v[200:203]\n\t" \
    "v_lshl_or_b32 v245, v112, 5, v245\n\t" \
    "v_and_b32 v112, 24, v238\n\t" \
    "v_mfma_f32_16x16x32_bf16 v[204:207], v[100:103], v[164:167], v[204:207]\n\t" \
    "v_or3_b32 v245, v245, v112, s86\n\t" \
    "v_lshlrev_b32 v247, 8, v238\n\t" \
    "v_mfma_f32_16x16x32_bf16 v[200:203], v[104:107], v[180:183], v[200:203]\n\t" \
    "v_lshl_or_b32 v247, v113, 5, v247\n\t" \
    "v_lshrrev_b32 v112, 5, v250\n\t" \
    "v_mfma_f32_16x16x32_bf16 v[204:207], v[108:111], v[180:183], v[204:207]\n\t" \
    "v_lshl_or_b32 v247, v112, 4, v247\n\t" \
    "v_and_b32 v252, 48, v247\n\t" \
    "v_mfma_f32_16x16x32_bf16 v[208:211], v[96:99], v[168:171], v[208:211]\n\t" \
    "v_lshl_add_u32 v252, v252, 2, %[zac]\n\t" \
    "v_lshlrev_b32 v246, 4, v238\n\t" \
    "v_mfma_f32_16x16x32_bf16 v[212:215], v[100:103], v[168:171], v[212:215]\n\t" \
    "v_cvt_pk_bf16_f32 v64, v192, v193\n\t" \
    "v_cvt_pk_bf16_f32 v65, v194, v195\n\t" \
    "v_mfma_f32_16x16x32_bf16 v[208:211], v[104:107], v[184:187], v[208:211]\n\t" \
    "v_cvt_pk_bf16_f32 v66, v196, v197\n\t" \
    "v_cvt_pk_bf16_f32 v67, v198, v199\n\t" \
    "v_mfma_f32_16x16x32_bf16 v[212:215], v[108:111], v[184:187], v[212:215]\n\t" \
    "v_lshl_or_b32 v246, v113, 3, v246\n\t" \
    "v_lshrrev_b32 v113, 4, v238\n\t" \
    "v_mfma_f32_16x16x32_bf16 v[216:219], v[96:99], v[172:175], v[216:219]\n\t" \
    "v_cvt_pk_bf16_f32 v68, v200, v201\n\t" \
    "v_cvt_pk_bf16_f32 v69, v202, v203\n\t" \
    "v_mfma_f32_16x16x32_bf16 v[220:223], v[100:103], v[172:175], v[220:223]\n\t" \
    "v_cvt_pk_bf16_f32 v70, v204, v205\n\t" \
    "v_cvt_pk_bf16_f32 v71, v206, v207\n\t" \
    "s_waitcnt lgkmcnt(1)\n\t" \
    "v_mfma_f32_16x16x32_bf16 v[216:219], v[104:107], v[188:191], v[216:219]\n\t" \
    "v_xor_b32 v112, v112, v113\n\t" \
    "v_lshl_or_b32 v246, v112, 4, v246\n\t" \
    "v_mfma_f32_16x16x32_bf16 v[220:223], v[108:111], v[188:191], v[220:223]\n\t" \
    "v_add_u32 v246, %[xec], v246\n\t" \
    "v_mfma_f32_16x16x32_bf16 v[0:3], v[224:227], v[64:67], 0\n\t" \
    "v_cvt_pk_bf16_f32 v72, v208, v209\n\t" \
    "v_cvt_pk_bf16_f32 v73, v210, v211\n\t" \
    "v_mfma_f32_16x16x32_bf16 v[4:7], v[228:231], v[64:67], 0\n\t" \
    "v_cvt_pk_bf16_f32 v74, v212, v213\n\t" \
    "v_cvt_pk_bf16_f32 v75, v214, v215\n\t" \
    "v_mfma_f32_16x16x32_bf16 v[8:11], v[224:227], v[68:71], 0\n\t" \
    "v_mov_b32 v120, 0x358637bd\n\t" \
    "v_mov_b32 v121, 0x358637bd\n\t" \
    "v_mfma_f32_16x16x32_bf16 v[12:15], v[228:231], v[68:71], 0\n\t" \
    "v_mov_b32 v122, 0x358637bd\n\t" \
    "v_mov_b32 v123, 0x358637bd\n\t" \
    "v_mfma_f32_16x16x32_bf16 v[16:19], v[224:227], v[72:75], 0\n\t" \
    "v_cvt_pk_bf16_f32 v76, v216, v217\n\t" \
    "v_cvt_pk_bf16_f32 v77, v218, v219\n\t" \
    "v_mfma_f32_16x16x32_bf16 v[20:23], v[228:231], v[72:75], 0\n\t" \
    "v_cvt_pk_bf16_f32 v78, v220, v221\n\t" \
    "v_cvt_pk_bf16_f32 v79, v222, v223\n\t" \
    "s_nop 1\n\t" \
    "v_mfma_f32_16x16x32_bf16 v[24:27], v[224:227], v[76:79], 0\n\t" \
    "v_mfma_f32_16x16x32_bf16 v[28:31], v[228:231], v[76:79], 0\n\t" \
    "v_max3_f32 v64, v0, v1, v2\n\t" \
    "v_max3_f32 v66, -v0, -v1, -v2\n\t" \
    "v_max3_f32 v68, v8, v9, v10\n\t" \
    "v_max3_f32 v64, v64, v3, v4\n\t" \
    "v_max3_f32 v66, v66, -v3, -v4\n\t" \
    "v_max3_f32 v68, v68, v11, v12\n\t" \
    "v_max3_f32 v64, v64, v5, v6\n\t" \
    "v_max3_f32 v66, v66, -v5, -v6\n\t" \
    "v_max3_f32 v68, v68, v13, v14\n\t" \
    "v_max_f32_e64 v64, v64, v7\n\t" \
    "v_max_f32_e64 v66, v66, -v7\n\t" \
    "v_max_f32_e64 v68, v68, v15\n\t" \
    "v_max3_f32 v70, -v8, -v9, -v10\n\t" \
    "v_permlane32_swap_b32 v64, v66\n\t" \
    "v_max3_f32 v72, v16, v17, v18\n\t" \
    "v_max3_f32 v70, v70, -v11, -v12\n\t" \
    "v_max_f32 v64, v64, v66\n\t" \
    "v_max3_f32 v72, v72, v19, v20\n\t" \
    "v_max3_f32 v70, v70, -v13, -v14\n\t" \
    "v_max3_f32 v74, -v16, -v17, -v18\n\t" \
    "v_max3_f32 v72, v72, v21, v22\n\t" \
    "v_max_f32_e64 v70, v70, -v15\n\t" \
    "v_max3_f32 v74, v74, -v19, -v20\n\t" \
    "v_max_f32_e64 v72, v72, v23\n\t" \
    "v_permlane32_swap_b32 v68, v70\n\t" \
    "v_max3_f32 v74, v74, -v21, -v22\n\t" \
    "v_max3_f32 v76, v24, v25, v26\n\t" \
    ".Lpad_ab2:\n\t" \
    "s_cbranch_execz .Lpad_ret\n\t" \
    "v_max_f32 v68, v68, v70\n\t" \
    "v_max_f32_e64 v74, v74, -v23\n\t" \
    "v_max3_f32 v76, v76, v27, v28\n\t" \
    "v_permlane16_swap_b32 v64, v68\n\t" \
    "v_permlane32_swap_b32 v72, v74\n\t" \
    "v_max3_f32 v76, v76, v29, v30\n\t" \
    "v_max_f32 v66, v64, v68\n\t" \
    "v_max_f32 v72, v72, v74\n\t" \
    "v_max_f32_e64 v76, v76, v31\n\t" \
    "v_mul_f32 v64, 0xbfb8aa3b, v66\n\t" \
    "v_mul_f32 v68, 0xbfb8aa3b, v66\n\t" \
    "v_max3_f32 v78, -v24, -v25, -v26\n\t" \
    "v_max3_f32 v78, v78, -v27, -v28\n\t" \
    "v_permlane16_swap_b32 v64, v68\n\t" \
    "v_mov_b32 v66, v64\n\t" \
    "v_max3_f32 v78, v78, -v29, -v30\n\t" \
    "v_mov_b32 v70, v68\n\t" \
    "v_permlane32_swap_b32 v64, v66\n\t" \
    "v_max_f32_e64 v78, v78, -v31\n\t" \
    "v_permlane32_swap_b32 v68, v70\n\t" \
    "v_pk_fma_f32 v[32:33], v[0:1], s[90:91], v[64:65] op_sel_hi:[1,1,0]\n\t" \
    "v_permlane32_swap_b32 v76, v78\n\t" \
    "v_pk_fma_f32 v[0:1], v[0:1], s[90:91], v[66:67] op_sel_hi:[1,1,0] neg_lo:[1,0,0] neg_hi:[1,0,0]\n\t" \
    "v_pk_fma_f32 v[34:35], v[2:3], s[90:91], v[64:65] op_sel_hi:[1,1,0]\n\t" \
    "v_max_f32 v76, v76, v78\n\t" \
    "v_pk_fma_f32 v[2:3], v[2:3], s[90:91], v[66:67] op_sel_hi:[1,1,0] neg_lo:[1,0,0] neg_hi:[1,0,0]\n\t" \
    "v_pk_fma_f32 v[36:37], v[4:5], s[90:91], v[64:65] op_sel_hi:[1,1,0]\n\t" \
    "v_permlane16_swap_b32 v72, v76\n\t" \
    "v_pk_fma_f32 v[4:5], v[4:5], s[90:91], v[66:67] op_sel_hi:[1,1,0] neg_lo:[1,0,0] neg_hi:[1,0,0]\n\t" \
    "v_pk_fma_f32 v[38:39], v[6:7], s[90:91], v[64:65] op_sel_hi:[1,1,0]\n\t" \
    "v_max_f32 v74, v72, v76\n\t" \
    "v_pk_fma_f32 v[6:7], v[6:7], s[90:91], v[66:67] op_sel_hi:[1,1,0] neg_lo:[1,0,0] neg_hi:[1,0,0]\n\t" \
    "v_exp_f32 v32, v32\n\t" \
    "v_mul_f32 v72, 0xbfb8aa3b, v74\n\t" \
    "v_mul_f32 v76, 0xbfb8aa3b, v74\n\t" \
    "v_exp_f32 v0, v0\n\t" \
    "v_exp_f32 v33, v33\n\t" \
    "v_permlane16_swap_b32 v72, v76\n\t" \
    "v_exp_f32 v1, v1\n\t" \
    "v_exp_f32 v34, v34\n\t" \
    "v_mov_b32 v74, v72\n\t" \
    "v_mov_b32 v78, v76\n\t" \
    "v_exp_f32 v2, v2\n\t" \
    "v_permlane32_swap_b32 v72, v74\n\t" \
    "v_permlane32_swap_b32 v76, v78\n\t" \
    "v_exp_f32 v35, v35\n\t" \
    "v_exp_f32 v3, v3\n\t" \
    "v_exp_f32 v36, v36\n\t" \
    "v_pk_add_f32 v[64:65], v[32:33], v[34:35]\n\t" \
    "v_pk_add_f32 v[66:67], v[0:1], v[2:3]\n\t" \
    "v_exp_f32 v4, v4\n\t" \
    "v_exp_f32 v37, v37\n\t" \
    "v_exp_f32 v5, v5\n\t" \
    "v_exp_f32 v38, v38\n\t" \
    "v_pk_add_f32 v[64:65], v[64:65], v[36:37]\n\t" \
    "v_pk_add_f32 v[66:67], v[66:67], v[4:5]\n\t" \
    "v_exp_f32 v6, v6\n\t" \
    "v_exp_f32 v39, v39\n\t" \
    "v_exp_f32 v7, v7\n\t" \
    "v_pk_fma_f32 v[40:41], v[8:9], s[90:91], v[68:69] op_sel_hi:[1,1,0]\n\t" \
    "v_pk_add_f32 v[64:65], v[64:65], v[38:39]\n\t" \
    "v_pk_add_f32 v[66:67], v[66:67], v[6:7]\n\t" \
    "v_pk_fma_f32 v[8:9], v[8:9], s[90:91], v[70:71] op_sel_hi:[1,1,0] neg_lo:[1,0,0] neg_hi:[1,0,0]\n\t" \
    "v_add_f32 v64, v64, v65\n\t" \
    "v_add_f32 v66, v66, v67\n\t" \
    "v_pk_fma_f32 v[42:43], v[10:11], s[90:91], v[68:69] op_sel_hi:[1,1,0]\n\t" \
    "v_pk_fma_f32 v[10:11], v[10:11], s[90:91], v[70:71] op_sel_hi:[1,1,0] neg_lo:[1,0,0] neg_hi:[1,0,0]\n\t" \
    "v_permlane32_swap_b32 v64, v66\n\t" \
    "v_pk_fma_f32 v[44:45], v[12:13], s[90:91], v[68:69] op_sel_hi:[1,1,0]\n\t" \
    "v_pk_fma_f32 v[12:13], v[12:13], s[90:91], v[70:71] op_sel_hi:[1,1,0] neg_lo:[1,0,0] neg_hi:[1,0,0]\n\t" \
    "v_add_f32 v64, v64, v66\n\t" \
    "v_pk_fma_f32 v[46:47], v[14:15], s[90:91], v[68:69] op_sel_hi:[1,1,0]\n\t" \
    "v_pk_fma_f32 v[14:15], v[14:15], s[90:91], v[70:71] op_sel_hi:[1,1,0] neg_lo:[1,0,0] neg_hi:[1,0,0]\n\t" \
    "v_exp_f32 v40, v40\n\t" \
    "v_exp_f32 v8, v8\n\t" \
    "v_exp_f32 v41, v41\n\t" \
    "v_exp_f32 v9, v9\n\t" \
    "v_exp_f32 v42, v42\n\t" \
    "v_exp_f32 v10, v10\n\t" \
    "v_exp_f32 v43, v43\n\t" \
    "v_exp_f32 v11, v11\n\t" \
    "v_exp_f32 v44, v44\n\t" \
    "v_pk_add_f32 v[68:69], v[40:41], v[42:43]\n\t" \
    "v_pk_add_f32 v[70:71], v[8:9], v[10:11]\n\t" \
    "v_exp_f32 v12, v12\n\t" \
    "v_exp_f32 v45, v45\n\t" \
    "v_exp_f32 v13, v13\n\t" \
    "v_exp_f32 v46, v46\n\t" \
    "v_pk_add_f32 v[68:69], v[68:69], v[44:45]\n\t" \
    "v_pk_add_f32 v[70:71], v[70:71], v[12:13]\n\t" \
    "v_exp_f32 v14, v14\n\t" \
    "v_exp_f32 v47, v47\n\t" \
    "v_exp_f32 v15, v15\n\t" \
    "v_pk_fma_f32 v[48:49], v[16:17], s[90:91], v[72:73] op_sel_hi:[1,1,0]\n\t" \
    "v_pk_add_f32 v[68:69], v[68:69], v[46:47]\n\t" \
    "v_pk_add_f32 v[70:71], v[70:71], v[14:15]\n\t" \
    "v_pk_fma_f32 v[16:17], v[16:17], s[90:91], v[74:75] op_sel_hi:[1,1,0] neg_lo:[1,0,0] neg_hi:[1,0,0]\n\t" \
    "v_add_f32 v68, v68, v69\n\t" \
    "v_add_f32 v70, v70, v71\n\t" \
    "v_pk_fma_f32 v[50:51], v[18:19], s[90:91], v[72:73] op_sel_hi:[1,1,0]\n\t" \
    "v_pk_fma_f32 v[18:19], v[18:19], s[90:91], v[74:75] op_sel_hi:[1,1,0] neg_lo:[1,0,0] neg_hi:[1,0,0]\n\t" \
    "v_permlane32_swap_b32 v68, v70\n\t" \
    "v_pk_fma_f32 v[52:53], v[20:21], s[90:91], v[72:73] op_sel_hi:[1,1,0]\n\t" \
    "v_pk_fma_f32 v[20:21], v[20:21], s[90:91], v[74:75] op_sel_hi:[1,1,0] neg_lo:[1,0,0] neg_hi:[1,0,0]\n\t" \
    "v_add_f32 v68, v68, v70\n\t" \
    "v_pk_fma_f32 v[54:55], v[22:23], s[90:91], v[72:73] op_sel_hi:[1,1,0]\n\t" \
    "v_pk_fma_f32 v[22:23], v[22:23], s[90:91], v[74:75] op_sel_hi:[1,1,0] neg_lo:[1,0,0] neg_hi:[1,0,0]\n\t" \
    "v_permlane16_swap_b32 v64, v68\n\t" \
    "v_exp_f32 v48, v48\n\t" \
    "v_exp_f32 v16, v16\n\t" \
    "v_add_f32 v66, v64, v68\n\t" \
    "v_exp_f32 v49, v49\n\t" \
    "v_exp_f32 v17, v17\n\t" \
    "v_rcp_f32 v64, v66\n\t" \
    "v_exp_f32 v50, v50\n\t" \
    "v_exp_f32 v18, v18\n\t" \
    "v_mov_b32 v68, v64\n\t" \
    "v_exp_f32 v51, v51\n\t" \
    "v_exp_f32 v19, v19\n\t" \
    "v_permlane16_swap_b32 v64, v68\n\t" \
    "v_pk_add_f32 v[72:73], v[48:49], v[50:51]\n\t" \
    "v_pk_add_f32 v[74:75], v[16:17], v[18:19]\n\t" \
    "v_mov_b32 v66, v64\n\t" \
    "v_mov_b32 v70, v68\n\t" \
    "v_exp_f32 v52, v52\n\t" \
    "v_permlane32_swap_b32 v64, v66\n\t" \
    "v_permlane32_swap_b32 v68, v70\n\t" \
    "v_exp_f32 v20, v20\n\t" \
    "v_exp_f32 v53, v53\n\t" \
    "v_exp_f32 v21, v21\n\t" \
    "v_exp_f32 v54, v54\n\t" \
    "v_pk_add_f32 v[72:73], v[72:73], v[52:53]\n\t" \
    "v_pk_add_f32 v[74:75], v[74:75], v[20:21]\n\t" \
    "v_exp_f32 v22, v22\n\t" \
    "v_exp_f32 v55, v55\n\t" \
    "v_exp_f32 v23, v23\n\t" \
    "v_pk_fma_f32 v[56:57], v[24:25], s[90:91], v[76:77] op_sel_hi:[1,1,0]\n\t" \
    "v_pk_add_f32 v[72:73], v[72:73], v[54:55]\n\t" \
    "v_pk_add_f32 v[74:75], v[74:75], v[22:23]\n\t" \
    "v_pk_fma_f32 v[24:25], v[24:25], s[90:91], v[78:79] op_sel_hi:[1,1,0] neg_lo:[1,0,0] neg_hi:[1,0,0]\n\t" \
    "v_add_f32 v72, v72, v73\n\t" \
    "v_add_f32 v74, v74, v75\n\t" \
    "v_pk_fma_f32 v[58:59], v[26:27], s[90:91], v[76:77] op_sel_hi:[1,1,0]\n\t" \
    "v_pk_fma_f32 v[26:27], v[26:27], s[90:91], v[78:79] op_sel_hi:[1,1,0] neg_lo:[1,0,0] neg_hi:[1,0,0]\n\t" \
    "v_permlane32_swap_b32 v72, v74\n\t" \
    "v_pk_fma_f32 v[60:61], v[28:29], s[90:91], v[76:77] op_sel_hi:[1,1,0]\n\t" \
    "v_pk_fma_f32 v[28:29], v[28:29], s[90:91], v[78:79] op_sel_hi:[1,1,0] neg_lo:[1,0,0] neg_hi:[1,0,0]\n\t" \
    "v_add_f32 v72, v72, v74\n\t" \
    "v_pk_fma_f32 v[62:63], v[30:31], s[90:91], v[76:77] op_sel_hi:[1,1,0]\n\t" \
    "v_pk_fma_f32 v[30:31], v[30:31], s[90:91], v[78:79] op_sel_hi:[1,1,0] neg_lo:[1,0,0] neg_hi:[1,0,0]\n\t" \
    "v_exp_f32 v56, v56\n\t" \
    "v_exp_f32 v24, v24\n\t" \
    "v_exp_f32 v57, v57\n\t" \
    "v_exp_f32 v25, v25\n\t" \
    "v_exp_f32 v58, v58\n\t" \
    "v_exp_f32 v26, v26\n\t" \
    "v_exp_f32 v59, v59\n\t" \
    "v_exp_f32 v27, v27\n\t" \
    "v_exp_f32 v60, v60\n\t" \
    "v_pk_add_f32 v[76:77], v[56:57], v[58:59]\n\t" \
    "v_pk_add_f32 v[78:79], v[24:25], v[26:27]\n\t" \
    "v_exp_f32 v28, v28\n\t" \
    "v_exp_f32 v61, v61\n\t" \
    "v_exp_f32 v29, v29\n\t" \
    "v_exp_f32 v62, v62\n\t" \
    "v_pk_add_f32 v[76:77], v[76:77], v[60:61]\n\t" \
    "v_pk_add_f32 v[78:79], v[78:79], v[28:29]\n\t" \
    "v_exp_f32 v30, v30\n\t" \
    "v_exp_f32 v63, v63\n\t" \
    "v_exp_f32 v31, v31\n\t" \
    "v_pk_mul_f32 v[0:1], v[0:1], v[66:67] op_sel_hi:[1,0]\n\t" \
    "v_pk_add_f32 v[76:77], v[76:77], v[62:63]\n\t" \
    "v_pk_add_f32 v[78:79], v[78:79], v[30:31]\n\t" \
    "v_pk_mul_f32 v[32:33], v[32:33], v[64:65] op_sel_hi:[1,0]\n\t" \
    "v_add_f32 v76, v76, v77\n\t" \
    "v_add_f32 v78, v78, v79\n\t" \
    "v_pk_mul_f32 v[2:3], v[2:3], v[66:67] op_sel_hi:[1,0]\n\t" \
    "v_pk_mul_f32 v[34:35], v[34:35], v[64:65] op_sel_hi:[1,0]\n\t" \
    "v_permlane32_swap_b32 v76, v78\n\t" \
    "v_pk_mul_f32 v[4:5], v[4:5], v[66:67] op_sel_hi:[1,0]\n\t" \
    "v_pk_mul_f32 v[36:37], v[36:37], v[64:65] op_sel_hi:[1,0]\n\t" \
    "v_add_f32 v76, v76, v78\n\t" \
    "v_pk_mul_f32 v[6:7], v[6:7], v[66:67] op_sel_hi:[1,0]\n\t" \
    "v_pk_mul_f32 v[38:39], v[38:39], v[64:65] op_sel_hi:[1,0]\n\t" \
    "v_permlane16_swap_b32 v72, v76\n\t" \
    "v_cvt_pk_bf16_f32 v7, v6, v7\n\t" \
    "v_cvt_pk_bf16_f32 v6, v4, v5\n\t" \
    "v_add_f32 v74, v72, v76\n\t" \
    "v_cvt_pk_bf16_f32 v5, v2, v3\n\t" \
    "v_cvt_pk_bf16_f32 v4, v0, v1\n\t" \
    "v_rcp_f32 v72, v74\n\t" \
    "v_cvt_pk_bf16_f32 v0, v32, v33\n\t" \
    "v_cvt_pk_bf16_f32 v1, v34, v35\n\t" \
    "v_mov_b32 v76, v72\n\t" \
    "v_cvt_pk_bf16_f32 v2, v36, v37\n\t" \
    "v_cvt_pk_bf16_f32 v3, v38, v39\n\t" \
    "v_permlane16_swap_b32 v72, v76\n\t" \
    "v_pk_mul_f32 v[8:9], v[8:9], v[70:71] op_sel_hi:[1,0]\n\t" \
    "v_pk_mul_f32 v[40:41], v[40:41], v[68:69] op_sel_hi:[1,0]\n\t" \
    "v_mov_b32 v74, v72\n\t" \
    "v_mov_b32 v78, v76\n\t" \
    "v_pk_mul_f32 v[10:11], v[10:11], v[70:71] op_sel_hi:[1,0]\n\t" \
    "v_permlane32_swap_b32 v72, v74\n\t" \
    "v_permlane32_swap_b32 v76, v78\n\t" \
    "v_pk_mul_f32 v[42:43], v[42:43], v[68:69] op_sel_hi:[1,0]\n\t" \
    "v_pk_mul_f32 v[12:13], v[12:13], v[70:71] op_sel_hi:[1,0]\n\t" \
    "v_pk_mul_f32 v[44:45], v[44:45], v[68:69] op_sel_hi:[1,0]\n\t" \
    "v_pk_mul_f32 v[14:15], v[14:15], v[70:71] op_sel_hi:[1,0]\n\t" \
    "v_pk_mul_f32 v[46:47], v[46:47], v[68:69] op_sel_hi:[1,0]\n\t" \
    "v_pk_mul_f32 v[16:17], v[16:17], v[74:75] op_sel_hi:[1,0]\n\t" \
    "v_cvt_pk_bf16_f32 v15, v14, v15\n\t" \
    "v_cvt_pk_bf16_f32 v14, v12, v13\n\t" \
    "v_cvt_pk_bf16_f32 v13, v10, v11\n\t" \
    "v_cvt_pk_bf16_f32 v12, v8, v9\n\t" \
    "v_cvt_pk_bf16_f32 v8, v40, v41\n\t" \
    "v_cvt_pk_bf16_f32 v9, v42, v43\n\t" \
    "v_cvt_pk_bf16_f32 v10, v44, v45\n\t" \
    "v_cvt_pk_bf16_f32 v11, v46, v47\n\t" \
    "v_pk_mul_f32 v[48:49], v[48:49], v[72:73] op_sel_hi:[1,0]\n\t" \
    "v_pk_mul_f32 v[18:19], v[18:19], v[74:75] op_sel_hi:[1,0]\n\t" \
    "v_pk_mul_f32 v[50:51], v[50:51], v[72:73] op_sel_hi:[1,0]\n\t" \
    "v_pk_mul_f32 v[20:21], v[20:21], v[74:75] op_sel_hi:[1,0]\n\t" \
    "v_pk_mul_f32 v[52:53], v[52:53], v[72:73] op_sel_hi:[1,0]\n\t" \
    "v_pk_mul_f32 v[22:23], v[22:23], v[74:75] op_sel_hi:[1,0]\n\t" \
    "v_pk_mul_f32 v[54:55], v[54:55], v[72:73] op_sel_hi:[1,0]\n\t" \
    "ds_read_b32 v237, v249 offset:8\n\t" \
    "v_pk_mul_f32 v[24:25], v[24:25], v[78:79] op_sel_hi:[1,0]\n\t" \
    "v_cvt_pk_bf16_f32 v23, v22, v23\n\t" \
    "v_cvt_pk_bf16_f32 v22, v20, v21\n\t" \
    "v_cvt_pk_bf16_f32 v21, v18, v19\n\t" \
    "v_cvt_pk_bf16_f32 v20, v16, v17\n\t" \
    "v_cvt_pk_bf16_f32 v16, v48, v49\n\t" \
    "v_cvt_pk_bf16_f32 v17, v50, v51\n\t" \
    "v_cvt_pk_bf16_f32 v18, v52, v53\n\t" \
    "v_cvt_pk_bf16_f32 v19, v54, v55\n\t" \
    "v_pk_mul_f32 v[56:57], v[56:57], v[76:77] op_sel_hi:[1,0]\n\t" \
    "v_pk_mul_f32 v[26:27], v[26:27], v[78:79] op_sel_hi:[1,0]\n\t" \
    "v_pk_mul_f32 v[58:59], v[58:59], v[76:77] op_sel_hi:[1,0]\n\t" \
    "v_pk_mul_f32 v[28:29], v[28:29], v[78:79] op_sel_hi:[1,0]\n\t" \
    "v_pk_mul_f32 v[60:61], v[60:61], v[76:77] op_sel_hi:[1,0]\n\t" \
    "v_pk_mul_f32 v[30:31], v[30:31], v[78:79] op_sel_hi:[1,0]\n\t" \
    "v_pk_mul_f32 v[62:63], v[62:63], v[76:77] op_sel_hi:[1,0]\n\t" \
    "v_cvt_pk_bf16_f32 v31, v30, v31\n\t" \
    "v_cvt_pk_bf16_f32 v30, v28, v29\n\t" \
    "v_cvt_pk_bf16_f32 v29, v26, v27\n\t" \
    "v_cvt_pk_bf16_f32 v28, v24, v25\n\t" \
    "v_cvt_pk_bf16_f32 v24, v56, v57\n\t" \
    "v_cvt_pk_bf16_f32 v25, v58, v59\n\t" \
    "v_cvt_pk_bf16_f32 v26, v60, v61\n\t" \
    "v_cvt_pk_bf16_f32 v27, v62, v63\n\t" \
    "s_setprio 2\n\t" \
    "s_waitcnt lgkmcnt(0)\n\t" \
    "v_readfirstlane_b32 s85, v237\n\t" \
    "s_add_u32 s86, s87, 0x10000\n\t" \
    "s_cmp_lt_u32 s85, 3\n\t" \
    "s_cbranch_scc1 .Lab_s7\n\t" \
    ".Lab_s7r:\n\t" \
    "s_mov_b32 s89, 0x400000\n\t" \
    "s_mov_b32 s82, 4\n\t" \
    ".Lab_k:\n\t" \
    "ds_read_b32 v237, v249 offset:8\n\t" \
    "ds_read_b128 v[32:35], v252\n\t" \
    "ds_read_b128 v[36:39], v252 offset:256\n\t" \
    "ds_read_b128 v[40:43], v253\n\t" \
    "ds_read_b128 v[44:47], v253 offset:1024\n\t" \
    "ds_read_b128 v[48:51], v253 offset:2048\n\t" \
    "ds_read_b128 v[52:55], v253 offset:3072\n\t" \
    "s_sub_u32 s89, s89, 1\n\t" \
    "s_cbranch_scc1 .Lab_kr\n\t" \
    "s_waitcnt lgkmcnt(6)\n\t" \
    "v_readfirstlane_b32 s85, v237\n\t" \
    "s_cmp_lt_u32 s85, s82\n\t" \
    "s_cbranch_scc1 .Lab_k\n\t" \
    ".Lab_kr:\n\t" \
    "s_waitcnt lgkmcnt(5)\n\t" \
    "v_mfma_f32_16x16x32_bf16 v[56:59], v[32:35], v[0:3], v[120:123]\n\t" \
    "ds_read_b32 v126, v249 offset:296\n\t" \
    "v_mfma_f32_16x16x32_bf16 v[60:63], v[32:35], v[8:11], v[120:123]\n\t" \
    "ds_read_b32 v127, v249 offset:308\n\t" \
    "v_mfma_f32_16x16x32_bf16 v[64:67], v[32:35], v[16:19], v[120:123]\n\t" \
    "v_cmp_eq_u32 vcc, 0, v244\n\t" \
    "v_mfma_f32_16x16x32_bf16 v[68:71], v[32:35], v[24:27], v[120:123]\n\t" \
    "v_lshrrev_b32 v237, 1, v251\n\t" \
    "v_bfrev_b32 v238, 1\n\t" \
    "s_waitcnt lgkmcnt(6)\n\t" \
    "v_mfma_f32_16x16x32_bf16 v[56:59], v[36:39], v[4:7], v[56:59]\n\t" \
    "v_mfma_f32_16x16x32_bf16 v[60:63], v[36:39], v[12:15], v[60:63]\n\t" \
    "v_cndmask_b32 v254, v238, v237, vcc\n\t" \
    "v_mfma_f32_16x16x32_bf16 v[64:67], v[36:39], v[20:23], v[64:67]\n\t" \
    "s_or_b32 s82, s85, 1\n\t" \
    "v_mfma_f32_16x16x32_bf16 v[68:71], v[36:39], v[28:31], v[68:71]\n\t" \
    "s_waitcnt lgkmcnt(5)\n\t" \
    "v_mfma_f32_16x16x32_bf16 v[72:75], v[40:43], v[0:3], 0\n\t" \
    "s_waitcnt lgkmcnt(4)\n\t" \
    "v_mfma_f32_16x16x32_bf16 v[76:79], v[44:47], v[0:3], 0\n\t" \
    "s_waitcnt lgkmcnt(3)\n\t" \
    "v_mfma_f32_16x16x32_bf16 v[72:75], v[48:51], v[4:7], v[72:75]\n\t" \
    "s_waitcnt lgkmcnt(2)\n\t" \
    "v_mfma_f32_16x16x32_bf16 v[76:79], v[52:55], v[4:7], v[76:79]\n\t" \
    "v_mfma_f32_16x16x32_bf16 v[80:83], v[40:43], v[8:11], 0\n\t" \
    "v_rcp_f32 v56, v56\n\t" \
    "v_mfma_f32_16x16x32_bf16 v[84:87], v[44:47], v[8:11], 0\n\t" \
    "v_rcp_f32 v60, v60\n\t" \
    "v_mfma_f32_16x16x32_bf16 v[80:83], v[48:51], v[12:15], v[80:83]\n\t" \
    "v_rcp_f32 v64, v64\n\t" \
    "v_mfma_f32_16x16x32_bf16 v[84:87], v[52:55], v[12:15], v[84:87]\n\t" \
    "v_rcp_f32 v68, v68\n\t" \
    "v_mfma_f32_16x16x32_bf16 v[88:91], v[40:43], v[16:19], 0\n\t" \
    "v_mfma_f32_16x16x32_bf16 v[92:95], v[44:47], v[16:19], 0\n\t" \
    "v_mul_f32 v72, v72, v56\n\t" \
    "v_mul_f32 v73, v73, v56\n\t" \
    "v_mfma_f32_16x16x32_bf16 v[88:91], v[48:51], v[20:23], v[88:91]\n\t" \
    "v_mul_f32 v74, v74, v56\n\t" \
    "v_mul_f32 v75, v75, v56\n\t" \
    "v_mfma_f32_16x16x32_bf16 v[92:95], v[52:55], v[20:23], v[92:95]\n\t" \
    "v_mul_f32 v76, v76, v56\n\t" \
    "v_mul_f32 v77, v77, v56\n\t" \
    "v_mfma_f32_16x16x32_bf16 v[96:99], v[40:43], v[24:27], 0\n\t" \
    "v_mul_f32 v78, v78, v56\n\t" \
    "v_mul_f32 v79, v79, v56\n\t" \
    "v_mfma_f32_16x16x32_bf16 v[100:103], v[44:47], v[24:27], 0\n\t" \
    "v_cvt_pk_bf16_f32 v104, v72, v73\n\t" \
    "v_cvt_pk_bf16_f32 v105, v74, v75\n\t" \
    "v_mfma_f32_16x16x32_bf16 v[96:99], v[48:51], v[28:31], v[96:99]\n\t" \
    "v_cvt_pk_bf16_f32 v106, v76, v77\n\t" \
    "v_cvt_pk_bf16_f32 v107, v78, v79\n\t" \
    "v_mfma_f32_16x16x32_bf16 v[100:103], v[52:55], v[28:31], v[100:103]\n\t" \
    "buffer_store_dwordx4 v[104:107], v251, %[rsb], s87 offen sc1\n\t" \
    "v_pk_mul_f32 v[80:81], v[80:81], v[60:61] op_sel_hi:[1,0]\n\t" \
    "v_pk_mul_f32 v[82:83], v[82:83], v[60:61] op_sel_hi:[1,0]\n\t" \
    "v_pk_mul_f32 v[84:85], v[84:85], v[60:61] op_sel_hi:[1,0]\n\t" \
    "v_pk_mul_f32 v[86:87], v[86:87], v[60:61] op_sel_hi:[1,0]\n\t" \
    "v_cvt_pk_bf16_f32 v108, v80, v81\n\t" \
    "v_cvt_pk_bf16_f32 v109, v82, v83\n\t" \
    "v_cvt_pk_bf16_f32 v110, v84, v85\n\t" \
    "v_cvt_pk_bf16_f32 v111, v86, v87\n\t" \
    "buffer_store_dwordx4 v[108:111], v251, %[rsb], s87 offen offset:1024 sc1\n\t" \
    "v_pk_mul_f32 v[88:89], v[88:89], v[64:65] op_sel_hi:[1,0]\n\t" \
    "v_pk_mul_f32 v[90:91], v[90:91], v[64:65] op_sel_hi:[1,0]\n\t" \
    "v_pk_mul_f32 v[92:93], v[92:93], v[64:65] op_sel_hi:[1,0]\n\t" \
    "v_pk_mul_f32 v[94:95], v[94:95], v[64:65] op_sel_hi:[1,0]\n\t" \
    "v_cvt_pk_bf16_f32 v112, v88, v89\n\t" \
    "v_cvt_pk_bf16_f32 v113, v90, v91\n\t" \
    "v_cvt_pk_bf16_f32 v114, v92, v93\n\t" \
    "v_cvt_pk_bf16_f32 v115, v94, v95\n\t" \
    "buffer_store_dwordx4 v[112:115], v251, %[rsb], s86 offen sc1\n\t" \
    "v_pk_mul_f32 v[96:97], v[96:97], v[68:69] op_sel_hi:[1,0]\n\t" \
    "v_pk_mul_f32 v[98:99], v[98:99], v[68:69] op_sel_hi:[1,0]\n\t" \
    "v_pk_mul_f32 v[100:101], v[100:101], v[68:69] op_sel_hi:[1,0]\n\t" \
    "v_pk_mul_f32 v[102:103], v[102:103], v[68:69] op_sel_hi:[1,0]\n\t" \
    "v_cvt_pk_bf16_f32 v116, v96, v97\n\t" \
    "v_cvt_pk_bf16_f32 v117, v98, v99\n\t" \
    "v_cvt_pk_bf16_f32 v118, v100, v101\n\t" \
    "v_cvt_pk_bf16_f32 v119, v102, v103\n\t" \
    "v_add3_u32 v32, v104, v105, 0\n\t" \
    "v_add3_u32 v32, v106, v107, v32\n\t" \
    "v_add3_u32 v33, v112, v113, 0\n\t" \
    "v_add3_u32 v33, v114, v115, v33\n\t" \
    "v_add3_u32 v32, v108, v109, v32\n\t" \
    "v_add3_u32 v32, v110, v111, v32\n\t" \
    "v_add3_u32 v33, v116, v117, v33\n\t" \
    "v_add3_u32 v33, v118, v119, v33\n\t" \
    "buffer_store_dwordx4 v[116:119], v251, %[rsb], s86 offen offset:1024 sc1\n\t" \
    "ds_read_b32 v36, v249 offset:8\n\t" \
    "v_permlane32_swap_b32 v32, v33\n\t" \
    "v_add_u32 v32, v32, v33\n\t" \
    "v_mov_b32 v33, v32\n\t" \
    "s_waitcnt lgkmcnt(2)\n\t" \
    "v_readfirstlane_b32 %[rid], v126\n\t" \
    "s_waitcnt lgkmcnt(1)\n\t" \
    "v_readfirstlane_b32 %[t3], v127\n\t" \
    "v_permlane16_swap_b32 v32, v33\n\t" \
    "v_add_u32 v32, v32, v33\n\t" \
    "v_mov_b32 v33, v236\n\t" \
    "ds_read_b32 v238, v249 offset:56\n\t" \
    "v_add_u32_dpp v32, v32, v32 row_ror:8 row_mask:0xf bank_mask:0xf bound_ctrl:1\n\t" \
    "v_mov_b32 v237, 12\n\t" \
    "v_and_b32 v34, 15, v250\n\t" \
    "v_add_u32_dpp v32, v32, v32 row_ror:4 row_mask:0xf bank_mask:0xf bound_ctrl:1\n\t" \
    "s_waitcnt lgkmcnt(1)\n\t" \
    "v_readfirstlane_b32 s80, v36\n\t" \
    "s_and_b32 s85, %[rid], 15\n\t" \
    "v_add_u32_dpp v32, v32, v32 row_ror:2 row_mask:0xf bank_mask:0xf bound_ctrl:1\n\t" \
    "v_cmp_eq_u32_e64 %[rm], s85, v34\n\t" \
    "s_cmp_lg_u32 s80, s82\n\t" \
    "v_add_u32_dpp v32, v32, v32 row_ror:1 row_mask:0xf bank_mask:0xf bound_ctrl:1\n\t" \
    "s_cbranch_scc1 .Lab_sa\n\t" \
    ".Lab_sar:\n\t" \
    "buffer_store_dwordx2 v[32:33], v254, %[rsb], s88 offen sc1\n\t" \
    "ds_write_b32 v248, v237\n\t" \
    "v_and_b32 v252, 48, v250\n\t" \
    "s_lshr_b32 s85, %[rid], 5\n\t" \
    "v_add_u32 v252, 161536, v252\n\t" \
    "s_cmp_eq_u32 s85, %[own]\n\t" \
    "s_cselect_b64 %[rm], %[rm], 0\n\t" \
    "s_waitcnt vmcnt(5)\n\t" \
    "v_cmp_ne_u32 vcc, v236, v233\n\t" \
    "v_cmp_ne_u32_e64 s[92:93], v236, v235\n\t" \
    "s_nop 0\n\t" \
    "s_or_b64 vcc, vcc, s[92:93]\n\t" \
    "s_cbranch_scc1 .Lab_s8\n\t" \
    ".Lab_s8r:\n\t" \
    "v_sub_f32 v254, 1.0, v232\n\t" \
    "v_sub_f32 v255, 1.0, v234\n\t" \
    "v_cvt_pk_bf16_f32 v255, v254, v255\n\t" \
    "v_lshlrev_b32 v254, 16, v255\n\t" \
    "v_and_b32 v255, 0xffff0000, v255\n\t" \
    "s_waitcnt lgkmcnt(1)\n\t" \
    "v_readfirstlane_b32 s85, v238\n\t" \
    "s_cmp_lt_u32 s85, 2\n\t" \
    "s_cbranch_scc1 .Lab_s9\n\t" \
    ".Lab_s9r:\n\t" \
    "s_waitcnt lgkmcnt(0)\n\t"
#define PHASE_C_ASM \
    "s_branch .Lpad_c0\n\t" \
    ".Lc_x:\n\t" \
    "s_mov_b32 s86, 2\n\t" \
    ".Lc_xa:\n\t" \
    "s_lshl_b32 s80, %[grp], 14\n\t" \
    "s_sleep 2\n\t" \
    "buffer_load_dwordx2 v[160:161], v231, %[rsb], s85 offen sc1\n\t" \
    "buffer_load_dwordx4 v[32:35], v240, %[rsb], s80 offen offset:2048 sc1\n\t" \
    "buffer_load_dwordx4 v[36:39], v240, %[rsb], s80 offen offset:3072 sc1\n\t" \
    "buffer_load_dwordx4 v[40:43], v241, %[rsb], s80 offen sc1\n\t" \
    "buffer_load_dwordx4 v[44:47], v241, %[rsb], s80 offen offset:1024 sc1\n\t" \
    "buffer_load_dwordx4 v[48:51], v241, %[rsb], s80 offen offset:2048 sc1\n\t" \
    "buffer_load_dwordx4 v[52:55], v241, %[rsb], s80 offen offset:3072 sc1\n\t" \
    "buffer_load_dwordx4 v[56:59], v242, %[rsb], s80 offen sc1\n\t" \
    "buffer_load_dwordx4 v[60:63], v242, %[rsb], s80 offen offset:1024 sc1\n\t" \
    "buffer_load_dwordx4 v[64:67], v242, %[rsb], s80 offen offset:2048 sc1\n\t" \
    "buffer_load_dwordx4 v[68:71], v242, %[rsb], s80 offen offset:3072 sc1\n\t" \
    "buffer_load_dwordx4 v[72:75], v243, %[rsb], s80 offen sc1\n\t" \
    "buffer_load_dwordx4 v[76:79], v243, %[rsb], s80 offen offset:1024 sc1\n\t" \
    "buffer_load_dwordx4 v[80:83], v243, %[rsb], s80 offen offset:2048 sc1\n\t" \
    "buffer_load_dwordx4 v[84:87], v243, %[rsb], s80 offen offset:3072 sc1\n\t" \
    "s_add_u32 s80, s80, 0x4000\n\t" \
    "s_and_b32 s80, s80, 0xc000\n\t" \
    "s_waitcnt vmcnt(14)\n\t" \
    "v_cmp_ne_u32 vcc, v236, v161\n\t" \
    "s_nop 1\n\t" \
    "s_and_b32 vcc_lo, vcc_lo, s84\n\t" \
    "s_cbranch_scc0 .Lc_l\n\t" \
    "s_sub_u32 s86, s86, 1\n\t" \
    "s_cmp_lg_u32 s86, 0\n\t" \
    "s_cbranch_scc1 .Lc_xa\n\t" \
    "s_waitcnt vmcnt(0) lgkmcnt(0)\n\t" \
    "s_branch .Lc_end\n\t" \
    ".Lc_w0:\n\t" \
    "s_mov_b32 s86, 0x400000\n\t" \
    ".Lc_a0:\n\t" \
    "ds_read_b32 v160, v249 offset:56\n\t" \
    "s_waitcnt lgkmcnt(0)\n\t" \
    "v_readfirstlane_b32 s83, v160\n\t" \
    "s_sub_u32 s86, s86, 1\n\t" \
    "s_cbranch_scc1 .Lc_r0\n\t" \
    "s_cmp_lt_u32 s83, s82\n\t" \
    "s_cbranch_scc1 .Lc_a0\n\t" \
    "s_branch .Lc_r0\n\t" \
    ".Lc_w1:\n\t" \
    "s_mov_b32 s86, 0x400000\n\t" \
    ".Lc_a1:\n\t" \
    "ds_read_b32 v160, v249 offset:56\n\t" \
    "s_waitcnt lgkmcnt(0)\n\t" \
    "v_readfirstlane_b32 s83, v160\n\t" \
    "s_sub_u32 s86, s86, 1\n\t" \
    "s_cbranch_scc1 .Lc_r1\n\t" \
    "s_cmp_lt_u32 s83, s82\n\t" \
    "s_cbranch_scc1 .Lc_a1\n\t" \
    "s_branch .Lc_r1\n\t" \
    ".Lc_y:\n\t" \
    "s_mov_b32 s86, 0x400000\n\t" \
    ".Lc_ya:\n\t" \
    "s_waitcnt lgkmcnt(0)\n\t" \
    "v_readfirstlane_b32 s83, v231\n\t" \
    "s_sub_u32 s86, s86, 1\n\t" \
    "s_cbranch_scc1 .Lc_yb\n\t" \
    "s_cmp_lg_u32 s83, 0\n\t" \
    "s_cbranch_scc1 .Lc_yb\n\t" \
    "ds_read_b32 v231, v249 offset:68\n\t" \
    "ds_read_b128 v[96:99], v252\n\t" \
    "ds_read_b128 v[100:103], v252 offset:64\n\t" \
    "ds_read_b128 v[104:107], v252 offset:128\n\t" \
    "ds_read_b128 v[108:111], v252 offset:192\n\t" \
    "s_branch .Lc_ya\n\t" \
    ".Lc_yb:\n\t" \
    "s_mov_b64 exec, %[rm]\n\t" \
    "s_bitcmp1_b32 %[rid], 4\n\t" \
    "s_cbranch_scc1 .Lc_yc\n\t" \
    "v_pk_mul_f32 v[194:195], v[96:97], v[254:255] op_sel_hi:[1,0]\n\t" \
    "v_pk_mul_f32 v[196:197], v[98:99], v[254:255] op_sel_hi:[1,0]\n\t" \
    "v_pk_mul_f32 v[198:199], v[100:101], v[254:255] op_sel_hi:[1,0]\n\t" \
    "v_pk_mul_f32 v[200:201], v[102:103], v[254:255] op_sel_hi:[1,0]\n\t" \
    "v_pk_mul_f32 v[202:203], v[104:105], v[254:255] op_sel_hi:[1,0]\n\t" \
    "v_pk_mul_f32 v[204:205], v[106:107], v[254:255] op_sel_hi:[1,0]\n\t" \
    "v_pk_mul_f32 v[206:207], v[108:109], v[254:255] op_sel_hi:[1,0]\n\t" \
    "v_pk_mul_f32 v[208:209], v[110:111], v[254:255] op_sel_hi:[1,0]\n\t" \
    "s_mov_b64 exec, -1\n\t" \
    "s_branch .Lc_yr\n\t" \
    ".Lc_yc:\n\t" \
    "v_pk_mul_f32 v[210:211], v[96:97], v[254:255] op_sel:[0,1] op_sel_hi:[1,1]\n\t" \
    "v_pk_mul_f32 v[212:213], v[98:99], v[254:255] op_sel:[0,1] op_sel_hi:[1,1]\n\t" \
    "v_pk_mul_f32 v[214:215], v[100:101], v[254:255] op_sel:[0,1] op_sel_hi:[1,1]\n\t" \
    "v_pk_mul_f32 v[216:217], v[102:103], v[254:255] op_sel:[0,1] op_sel_hi:[1,1]\n\t" \
    "v_pk_mul_f32 v[218:219], v[104:105], v[254:255] op_sel:[0,1] op_sel_hi:[1,1]\n\t" \
    "v_pk_mul_f32 v[220:221], v[106:107], v[254:255] op_sel:[0,1] op_sel_hi:[1,1]\n\t" \
    "v_pk_mul_f32 v[222:223], v[108:109], v[254:255] op_sel:[0,1] op_sel_hi:[1,1]\n\t" \
    "v_pk_mul_f32 v[224:225], v[110:111], v[254:255] op_sel:[0,1] op_sel_hi:[1,1]\n\t" \
    "s_mov_b64 exec, -1\n\t" \
    "s_branch .Lc_yr\n\t" \
    ".p2align 12\n\t" \
    ".Lpad_c0:\n\t" \
    "s_cbranch_execz .Lpad_ret\n\t" \
    "s_setprio 2\n\t" \
    "v_xor_b32 v226, %[flip], v245\n\t" \
    "v_xor_b32 v227, 32, v226\n\t" \
    "v_xor_b32 v228, 64, v226\n\t" \
    "v_xor_b32 v229, 96, v226\n\t" \
    "v_and_b32 v231, 0x7f, v244\n\t" \
    "s_lshl_b32 s80, %[grp], 14\n\t" \
    "s_lshl_b32 s83, %[grp], 3\n\t" \
    "s_lshl_b32 s84, 0xffff, s83\n\t" \
    "s_lshl_b32 s85, %[grp], 6\n\t" \
    "s_add_u32 s85, s85, %[sog]\n\t" \
    "s_mov_b32 s81, 256\n\t" \
    "v_xor_b32 v1, 32, v246\n\t" \
    "v_xor_b32 v2, 64, v246\n\t" \
    "v_xor_b32 v3, 96, v246\n\t" \
    ".Lc_t:\n\t" \
    "s_memrealtime s[86:87]\n\t" \
    "s_sub_u32 s81, s81, 1\n\t" \
    "s_cbranch_scc1 .Lc_go\n\t" \
    "s_waitcnt lgkmcnt(0)\n\t" \
    "s_sub_u32 s86, %[t3], s86\n\t" \
    "s_cmp_lt_i32 s86, 5\n\t" \
    "s_cbranch_scc1 .Lc_go\n\t" \
    "s_cmp_lt_i32 s86, 13\n\t" \
    "s_cbranch_scc1 .Lc_t\n\t" \
    "s_sleep 2\n\t" \
    "s_branch .Lc_t\n\t" \
    ".Lc_go:\n\t" \
    "buffer_load_dwordx2 v[160:161], v231, %[rsb], s85 offen sc1\n\t" \
    "buffer_load_dwordx4 v[32:35], v240, %[rsb], s80 offen offset:2048 sc1\n\t" \
    "buffer_load_dwordx4 v[36:39], v240, %[rsb], s80 offen offset:3072 sc1\n\t" \
    "buffer_load_dwordx4 v[40:43], v241, %[rsb], s80 offen sc1\n\t" \
    "buffer_load_dwordx4 v[44:47], v241, %[rsb], s80 offen offset:1024 sc1\n\t" \
    "buffer_load_dwordx4 v[48:51], v241, %[rsb], s80 offen offset:2048 sc1\n\t" \
    "buffer_load_dwordx4 v[52:55], v241, %[rsb], s80 offen offset:3072 sc1\n\t" \
    "buffer_load_dwordx4 v[56:59], v242, %[rsb], s80 offen sc1\n\t" \
    "buffer_load_dwordx4 v[60:63], v242, %[rsb], s80 offen offset:1024 sc1\n\t" \
    "buffer_load_dwordx4 v[64:67], v242, %[rsb], s80 offen offset:2048 sc1\n\t" \
    "buffer_load_dwordx4 v[68:71], v242, %[rsb], s80 offen offset:3072 sc1\n\t" \
    "buffer_load_dwordx4 v[72:75], v243, %[rsb], s80 offen sc1\n\t" \
    "buffer_load_dwordx4 v[76:79], v243, %[rsb], s80 offen offset:1024 sc1\n\t" \
    "buffer_load_dwordx4 v[80:83], v243, %[rsb], s80 offen offset:2048 sc1\n\t" \
    "buffer_load_dwordx4 v[84:87], v243, %[rsb], s80 offen offset:3072 sc1\n\t" \
    "s_add_u32 s80, s80, 0x4000\n\t" \
    "s_and_b32 s80, s80, 0xc000\n\t" \
    "ds_read_b64 v[196:197], v246\n\t" \
    "ds_read_b64 v[200:201], v1\n\t" \
    "ds_read_b64 v[204:205], v2\n\t" \
    "ds_read_b64 v[208:209], v3\n\t" \
    "ds_read_b64 v[212:213], v246 offset:2048\n\t" \
    "ds_read_b64 v[216:217], v1 offset:2048\n\t" \
    "ds_read_b64 v[220:221], v2 offset:2048\n\t" \
    "ds_read_b64 v[224:225], v3 offset:2048\n\t" \
    "ds_read_b64_tr_b16 v[96:97], v226\n\t" \
    "ds_read_b64_tr_b16 v[98:99], v226 offset:2048\n\t" \
    "ds_read_b64_tr_b16 v[100:101], v227\n\t" \
    "ds_read_b64_tr_b16 v[102:103], v227 offset:2048\n\t" \
    "ds_read_b64_tr_b16 v[104:105], v228\n\t" \
    "ds_read_b64_tr_b16 v[106:107], v228 offset:2048\n\t" \
    "ds_read_b64_tr_b16 v[108:109], v229\n\t" \
    "ds_read_b64_tr_b16 v[110:111], v229 offset:2048\n\t" \
    "ds_read_b64_tr_b16 v[112:113], v226 offset:4096\n\t" \
    "ds_read_b64_tr_b16 v[114:115], v226 offset:6144\n\t" \
    "ds_read_b64_tr_b16 v[116:117], v227 offset:4096\n\t" \
    "ds_read_b64_tr_b16 v[118:119], v227 offset:6144\n\t" \
    "ds_read_b64_tr_b16 v[120:121], v228 offset:4096\n\t" \
    "ds_read_b64_tr_b16 v[122:123], v228 offset:6144\n\t" \
    "ds_read_b64_tr_b16 v[124:125], v229 offset:4096\n\t" \
    "ds_read_b64_tr_b16 v[126:127], v229 offset:6144\n\t" \
    "v_mov_b32 v0, 0\n\t" \
    "v_mov_b32 v1, 0\n\t" \
    "v_mov_b32 v2, 0\n\t" \
    "v_mov_b32 v3, 0\n\t" \
    "v_mov_b32 v4, 0\n\t" \
    "v_mov_b32 v5, 0\n\t" \
    "v_mov_b32 v6, 0\n\t" \
    "v_mov_b32 v7, 0\n\t" \
    "v_mov_b32 v8, 0\n\t" \
    "v_mov_b32 v9, 0\n\t" \
    "v_mov_b32 v10, 0\n\t" \
    "v_mov_b32 v11, 0\n\t" \
    "v_mov_b32 v12, 0\n\t" \
    "v_mov_b32 v13, 0\n\t" \
    "v_mov_b32 v14, 0\n\t" \
    "v_mov_b32 v15, 0\n\t" \
    "v_mov_b32 v16, 0\n\t" \
    "v_mov_b32 v17, 0\n\t" \
    "v_mov_b32 v18, 0\n\t" \
    "v_mov_b32 v19, 0\n\t" \
    "v_mov_b32 v20, 0\n\t" \
    "v_mov_b32 v21, 0\n\t" \
    "v_mov_b32 v22, 0\n\t" \
    "v_mov_b32 v23, 0\n\t" \
    "v_mov_b32 v24, 0\n\t" \
    "v_mov_b32 v25, 0\n\t" \
    "v_mov_b32 v26, 0\n\t" \
    "v_mov_b32 v27, 0\n\t" \
    "v_mov_b32 v28, 0\n\t" \
    "v_mov_b32 v29, 0\n\t" \
    "v_mov_b32 v30, 0\n\t" \
    "v_mov_b32 v31, 0\n\t" \
    "s_waitcnt lgkmcnt(0)\n\t" \
    "v_lshlrev_b32 v194, 16, v196\n\t" \
    "v_and_b32 v195, 0xffff0000, v196\n\t" \
    "v_lshlrev_b32 v196, 16, v197\n\t" \
    "v_and_b32 v197, 0xffff0000, v197\n\t" \
    "v_lshlrev_b32 v198, 16, v200\n\t" \
    "v_and_b32 v199, 0xffff0000, v200\n\t" \
    "v_lshlrev_b32 v200, 16, v201\n\t" \
    "v_and_b32 v201, 0xffff0000, v201\n\t" \
    "v_lshlrev_b32 v202, 16, v204\n\t" \
    "v_and_b32 v203, 0xffff0000, v204\n\t" \
    "v_lshlrev_b32 v204, 16, v205\n\t" \
    "v_and_b32 v205, 0xffff0000, v205\n\t" \
    "v_lshlrev_b32 v206, 16, v208\n\t" \
    "v_and_b32 v207, 0xffff0000, v208\n\t" \
    "v_lshlrev_b32 v208, 16, v209\n\t" \
    "v_and_b32 v209, 0xffff0000, v209\n\t" \
    "v_lshlrev_b32 v210, 16, v212\n\t" \
    "v_and_b32 v211, 0xffff0000, v212\n\t" \
    "v_lshlrev_b32 v212, 16, v213\n\t" \
    "v_and_b32 v213, 0xffff0000, v213\n\t" \
    "v_lshlrev_b32 v214, 16, v216\n\t" \
    "v_and_b32 v215, 0xffff0000, v216\n\t" \
    "v_lshlrev_b32 v216, 16, v217\n\t" \
    "v_and_b32 v217, 0xffff0000, v217\n\t" \
    "v_lshlrev_b32 v218, 16, v220\n\t" \
    "v_and_b32 v219, 0xffff0000, v220\n\t" \
    "v_lshlrev_b32 v220, 16, v221\n\t" \
    "v_and_b32 v221, 0xffff0000, v221\n\t" \
    "v_lshlrev_b32 v222, 16, v224\n\t" \
    "v_and_b32 v223, 0xffff0000, v224\n\t" \
    "v_lshlrev_b32 v224, 16, v225\n\t" \
    "v_and_b32 v225, 0xffff0000, v225\n\t" \
    "v_pk_mul_f32 v[194:195], v[194:195], v[254:255] op_sel_hi:[1,0]\n\t" \
    "v_pk_mul_f32 v[196:197], v[196:197], v[254:255] op_sel_hi:[1,0]\n\t" \
    "v_pk_mul_f32 v[198:199], v[198:199], v[254:255] op_sel_hi:[1,0]\n\t" \
    "v_pk_mul_f32 v[200:201], v[200:201], v[254:255] op_sel_hi:[1,0]\n\t" \
    "v_pk_mul_f32 v[202:203], v[202:203], v[254:255] op_sel_hi:[1,0]\n\t" \
    "v_pk_mul_f32 v[204:205], v[204:205], v[254:255] op_sel_hi:[1,0]\n\t" \
    "v_pk_mul_f32 v[206:207], v[206:207], v[254:255] op_sel_hi:[1,0]\n\t" \
    "v_pk_mul_f32 v[208:209], v[208:209], v[254:255] op_sel_hi:[1,0]\n\t" \
    "v_pk_mul_f32 v[210:211], v[210:211], v[254:255] op_sel:[0,1] op_sel_hi:[1,1]\n\t" \
    "v_pk_mul_f32 v[212:213], v[212:213], v[254:255] op_sel:[0,1] op_sel_hi:[1,1]\n\t" \
    "v_pk_mul_f32 v[214:215], v[214:215], v[254:255] op_sel:[0,1] op_sel_hi:[1,1]\n\t" \
    "v_pk_mul_f32 v[216:217], v[216:217], v[254:255] op_sel:[0,1] op_sel_hi:[1,1]\n\t" \
    "v_pk_mul_f32 v[218:219], v[218:219], v[254:255] op_sel:[0,1] op_sel_hi:[1,1]\n\t" \
    "v_pk_mul_f32 v[220:221], v[220:221], v[254:255] op_sel:[0,1] op_sel_hi:[1,1]\n\t" \
    "v_pk_mul_f32 v[222:223], v[222:223], v[254:255] op_sel:[0,1] op_sel_hi:[1,1]\n\t" \
    "v_pk_mul_f32 v[224:225], v[224:225], v[254:255] op_sel:[0,1] op_sel_hi:[1,1]\n\t" \
    "v_mov_b32 v230, 0\n\t" \
    "s_mov_b32 s81, 2\n\t" \
    "s_mov_b32 s82, 1\n\t" \
    "s_mov_b32 %[st], 1\n\t" \
    "s_waitcnt vmcnt(14)\n\t" \
    "v_cmp_ne_u32 vcc, v236, v161\n\t" \
    "s_nop 1\n\t" \
    "s_and_b32 vcc_lo, vcc_lo, s84\n\t" \
    "s_cbranch_scc1 .Lc_x\n\t" \
    ".Lc_l:\n\t" \
    "s_waitcnt vmcnt(12) lgkmcnt(8)\n\t" \
    "v_mfma_f32_16x16x32_bf16 v[0:3], v[96:99], v[32:35], v[0:3]\n\t" \
    "ds_read_b64_tr_b16 v[128:129], v226 offset:8192\n\t" \
    "buffer_load_dwordx4 v[88:91], v240, %[rsb], s80 offen sc1\n\t" \
    "v_mfma_f32_16x16x32_bf16 v[16:19], v[96:99], v[36:39], v[16:19]\n\t" \
    "ds_read_b64_tr_b16 v[130:131], v226 offset:10240\n\t" \
    "v_add3_u32 v230, v32, v33, v230\n\t" \
    "v_mfma_f32_16x16x32_bf16 v[4:7], v[100:103], v[32:35], v[4:7]\n\t" \
    "ds_read_b64_tr_b16 v[132:133], v227 offset:8192\n\t" \
    "buffer_load_dwordx4 v[92:95], v240, %[rsb], s80 offen offset:1024 sc1\n\t" \
    "v_mfma_f32_16x16x32_bf16 v[20:23], v[100:103], v[36:39], v[20:23]\n\t" \
    "ds_read_b64_tr_b16 v[134:135], v227 offset:10240\n\t" \
    "v_add3_u32 v230, v34, v35, v230\n\t" \
    "v_mfma_f32_16x16x32_bf16 v[8:11], v[104:107], v[32:35], v[8:11]\n\t" \
    "ds_read_b64_tr_b16 v[136:137], v228 offset:8192\n\t" \
    "v_add3_u32 v230, v36, v37, v230\n\t" \
    "v_mfma_f32_16x16x32_bf16 v[24:27], v[104:107], v[36:39], v[24:27]\n\t" \
    "ds_read_b64_tr_b16 v[138:139], v228 offset:10240\n\t" \
    "v_add3_u32 v230, v38, v39, v230\n\t" \
    "v_mfma_f32_16x16x32_bf16 v[12:15], v[108:111], v[32:35], v[12:15]\n\t" \
    "ds_read_b64_tr_b16 v[140:141], v229 offset:8192\n\t" \
    "v_mfma_f32_16x16x32_bf16 v[28:31], v[108:111], v[36:39], v[28:31]\n\t" \
    "ds_read_b64_tr_b16 v[142:143], v229 offset:10240\n\t" \
    "s_waitcnt vmcnt(12) lgkmcnt(8)\n\t" \
    "v_mfma_f32_16x16x32_bf16 v[0:3], v[112:115], v[40:43], v[0:3]\n\t" \
    "ds_read_b64_tr_b16 v[144:145], v226 offset:12288\n\t" \
    "buffer_load_dwordx4 v[32:35], v240, %[rsb], s80 offen offset:2048 sc1\n\t" \
    "v_mfma_f32_16x16x32_bf16 v[16:19], v[112:115], v[44:47], v[16:19]\n\t" \
    "ds_read_b64_tr_b16 v[146:147], v226 offset:14336\n\t" \
    "v_add3_u32 v230, v40, v41, v230\n\t" \
    "v_mfma_f32_16x16x32_bf16 v[4:7], v[116:119], v[40:43], v[4:7]\n\t" \
    "ds_read_b64_tr_b16 v[148:149], v227 offset:12288\n\t" \
    "buffer_load_dwordx4 v[36:39], v240, %[rsb], s80 offen offset:3072 sc1\n\t" \
    "v_mfma_f32_16x16x32_bf16 v[20:23], v[116:119], v[44:47], v[20:23]\n\t" \
    "ds_read_b64_tr_b16 v[150:151], v227 offset:14336\n\t" \
    "v_add3_u32 v230, v42, v43, v230\n\t" \
    "v_mfma_f32_16x16x32_bf16 v[8:11], v[120:123], v[40:43], v[8:11]\n\t" \
    "ds_read_b64_tr_b16 v[152:153], v228 offset:12288\n\t" \
    "v_add3_u32 v230, v44, v45, v230\n\t" \
    "v_mfma_f32_16x16x32_bf16 v[24:27], v[120:123], v[44:47], v[24:27]\n\t" \
    "ds_read_b64_tr_b16 v[154:155], v228 offset:14336\n\t" \
    "v_add3_u32 v230, v46, v47, v230\n\t" \
    "v_mfma_f32_16x16x32_bf16 v[12:15], v[124:127], v[40:43], v[12:15]\n\t" \
    "ds_read_b64_tr_b16 v[156:157], v229 offset:12288\n\t" \
    "v_mfma_f32_16x16x32_bf16 v[28:31], v[124:127], v[44:47], v[28:31]\n\t" \
    "ds_read_b64_tr_b16 v[158:159], v229 offset:14336\n\t" \
    "s_waitcnt vmcnt(12) lgkmcnt(8)\n\t" \
    "v_mfma_f32_16x16x32_bf16 v[0:3], v[128:131], v[48:51], v[0:3]\n\t" \
    "ds_read_b64_tr_b16 v[96:97], v226 offset:16384\n\t" \
    "buffer_load_dwordx4 v[40:43], v241, %[rsb], s80 offen sc1\n\t" \
    "v_mfma_f32_16x16x32_bf16 v[16:19], v[128:131], v[52:55], v[16:19]\n\t" \
    "ds_read_b64_tr_b16 v[98:99], v226 offset:18432\n\t" \
    "v_add3_u32 v230, v48, v49, v230\n\t" \
    "v_mfma_f32_16x16x32_bf16 v[4:7], v[132:135], v[48:51], v[4:7]\n\t" \
    "ds_read_b64_tr_b16 v[100:101], v227 offset:16384\n\t" \
    "buffer_load_dwordx4 v[44:47], v241, %[rsb], s80 offen offset:1024 sc1\n\t" \
    "v_mfma_f32_16x16x32_bf16 v[20:23], v[132:135], v[52:55], v[20:23]\n\t" \
    "ds_read_b64_tr_b16 v[102:103], v227 offset:18432\n\t" \
    "v_add3_u32 v230, v50, v51, v230\n\t" \
    "v_mfma_f32_16x16x32_bf16 v[8:11], v[136:139], v[48:51], v[8:11]\n\t" \
    "ds_read_b64_tr_b16 v[104:105], v228 offset:16384\n\t" \
    "v_add3_u32 v230, v52, v53, v230\n\t" \
    "v_mfma_f32_16x16x32_bf16 v[24:27], v[136:139], v[52:55], v[24:27]\n\t" \
    "ds_read_b64_tr_b16 v[106:107], v228 offset:18432\n\t" \
    "v_add3_u32 v230, v54, v55, v230\n\t" \
    "v_mfma_f32_16x16x32_bf16 v[12:15], v[140:143], v[48:51], v[12:15]\n\t" \
    "ds_read_b64_tr_b16 v[108:109], v229 offset:16384\n\t" \
    "v_mfma_f32_16x16x32_bf16 v[28:31], v[140:143], v[52:55], v[28:31]\n\t" \
    "ds_read_b64_tr_b16 v[110:111], v229 offset:18432\n\t" \
    "s_waitcnt vmcnt(12) lgkmcnt(8)\n\t" \
    "v_mfma_f32_16x16x32_bf16 v[0:3], v[144:147], v[56:59], v[0:3]\n\t" \
    "ds_read_b64_tr_b16 v[112:113], v226 offset:20480\n\t" \
    "buffer_load_dwordx4 v[48:51], v241, %[rsb], s80 offen offset:2048 sc1\n\t" \
    "v_mfma_f32_16x16x32_bf16 v[16:19], v[144:147], v[60:63], v[16:19]\n\t" \
    "ds_read_b64_tr_b16 v[114:115], v226 offset:22528\n\t" \
    "v_add3_u32 v230, v56, v57, v230\n\t" \
    "v_mfma_f32_16x16x32_bf16 v[4:7], v[148:151], v[56:59], v[4:7]\n\t" \
    "ds_read_b64_tr_b16 v[116:117], v227 offset:20480\n\t" \
    "buffer_load_dwordx4 v[52:55], v241, %[rsb], s80 offen offset:3072 sc1\n\t" \
    "v_mfma_f32_16x16x32_bf16 v[20:23], v[148:151], v[60:63], v[20:23]\n\t" \
    "ds_read_b64_tr_b16 v[118:119], v227 offset:22528\n\t" \
    "v_add3_u32 v230, v58, v59, v230\n\t" \
    "v_mfma_f32_16x16x32_bf16 v[8:11], v[152:155], v[56:59], v[8:11]\n\t" \
    "ds_read_b64_tr_b16 v[120:121], v228 offset:20480\n\t" \
    "v_add3_u32 v230, v60, v61, v230\n\t" \
    "v_mfma_f32_16x16x32_bf16 v[24:27], v[152:155], v[60:63], v[24:27]\n\t" \
    "ds_read_b64_tr_b16 v[122:123], v228 offset:22528\n\t" \
    "v_add3_u32 v230, v62, v63, v230\n\t" \
    "v_mfma_f32_16x16x32_bf16 v[12:15], v[156:159], v[56:59], v[12:15]\n\t" \
    "ds_read_b64_tr_b16 v[124:125], v229 offset:20480\n\t" \
    "v_mfma_f32_16x16x32_bf16 v[28:31], v[156:159], v[60:63], v[28:31]\n\t" \
    "ds_read_b64_tr_b16 v[126:127], v229 offset:22528\n\t" \
    "s_waitcnt vmcnt(12) lgkmcnt(8)\n\t" \
    "ds_read_b32 v160, v249 offset:56\n\t" \
    "v_mfma_f32_16x16x32_bf16 v[0:3], v[96:99], v[64:67], v[0:3]\n\t" \
    "ds_read_b64_tr_b16 v[128:129], v226 offset:24576\n\t" \
    "buffer_load_dwordx4 v[56:59], v242, %[rsb], s80 offen sc1\n\t" \
    "v_mfma_f32_16x16x32_bf16 v[16:19], v[96:99], v[68:71], v[16:19]\n\t" \
    "ds_read_b64_tr_b16 v[130:131], v226 offset:26624\n\t" \
    "v_add3_u32 v230, v64, v65, v230\n\t" \
    "v_mfma_f32_16x16x32_bf16 v[4:7], v[100:103], v[64:67], v[4:7]\n\t" \
    "ds_read_b64_tr_b16 v[132:133], v227 offset:24576\n\t" \
    "buffer_load_dwordx4 v[60:63], v242, %[rsb], s80 offen offset:1024 sc1\n\t" \
    "v_mfma_f32_16x16x32_bf16 v[20:23], v[100:103], v[68:71], v[20:23]\n\t" \
    "ds_read_b64_tr_b16 v[134:135], v227 offset:26624\n\t" \
    "v_add3_u32 v230, v66, v67, v230\n\t" \
    "v_mfma_f32_16x16x32_bf16 v[8:11], v[104:107], v[64:67], v[8:11]\n\t" \
    "ds_read_b64_tr_b16 v[136:137], v228 offset:24576\n\t" \
    "v_add3_u32 v230, v68, v69, v230\n\t" \
    "v_mfma_f32_16x16x32_bf16 v[24:27], v[104:107], v[68:71], v[24:27]\n\t" \
    "ds_read_b64_tr_b16 v[138:139], v228 offset:26624\n\t" \
    "v_add3_u32 v230, v70, v71, v230\n\t" \
    "v_mfma_f32_16x16x32_bf16 v[12:15], v[108:111], v[64:67], v[12:15]\n\t" \
    "ds_read_b64_tr_b16 v[140:141], v229 offset:24576\n\t" \
    "v_mfma_f32_16x16x32_bf16 v[28:31], v[108:111], v[68:71], v[28:31]\n\t" \
    "ds_read_b64_tr_b16 v[142:143], v229 offset:26624\n\t" \
    "s_waitcnt vmcnt(12) lgkmcnt(8)\n\t" \
    "v_mfma_f32_16x16x32_bf16 v[0:3], v[112:115], v[72:75], v[0:3]\n\t" \
    "ds_read_b64_tr_b16 v[144:145], v226 offset:28672\n\t" \
    "buffer_load_dwordx4 v[64:67], v242, %[rsb], s80 offen offset:2048 sc1\n\t" \
    "v_mfma_f32_16x16x32_bf16 v[16:19], v[112:115], v[76:79], v[16:19]\n\t" \
    "ds_read_b64_tr_b16 v[146:147], v226 offset:30720\n\t" \
    "v_add3_u32 v230, v72, v73, v230\n\t" \
    "v_mfma_f32_16x16x32_bf16 v[4:7], v[116:119], v[72:75], v[4:7]\n\t" \
    "ds_read_b64_tr_b16 v[148:149], v227 offset:28672\n\t" \
    "buffer_load_dwordx4 v[68:71], v242, %[rsb], s80 offen offset:3072 sc1\n\t" \
    "v_add_u32 v226, 0x8000, v226\n\t" \
    "v_and_b32 v226, 0x1ffff, v226\n\t" \
    "v_mfma_f32_16x16x32_bf16 v[20:23], v[116:119], v[76:79], v[20:23]\n\t" \
    "ds_read_b64_tr_b16 v[150:151], v227 offset:30720\n\t" \
    "v_add3_u32 v230, v74, v75, v230\n\t" \
    "v_mfma_f32_16x16x32_bf16 v[8:11], v[120:123], v[72:75], v[8:11]\n\t" \
    "ds_read_b64_tr_b16 v[152:153], v228 offset:28672\n\t" \
    "v_add3_u32 v230, v76, v77, v230\n\t" \
    "v_add_u32 v227, 0x8000, v227\n\t" \
    "v_and_b32 v227, 0x1ffff, v227\n\t" \
    "v_mfma_f32_16x16x32_bf16 v[24:27], v[120:123], v[76:79], v[24:27]\n\t" \
    "ds_read_b64_tr_b16 v[154:155], v228 offset:30720\n\t" \
    "v_add3_u32 v230, v78, v79, v230\n\t" \
    "v_mfma_f32_16x16x32_bf16 v[12:15], v[124:127], v[72:75], v[12:15]\n\t" \
    "v_readfirstlane_b32 s83, v160\n\t" \
    "s_add_u32 s82, s82, 1\n\t" \
    "ds_read_b64_tr_b16 v[156:157], v229 offset:28672\n\t" \
    "v_add_u32 v228, 0x8000, v228\n\t" \
    "v_and_b32 v228, 0x1ffff, v228\n\t" \
    "v_mfma_f32_16x16x32_bf16 v[28:31], v[124:127], v[76:79], v[28:31]\n\t" \
    "s_cmp_lt_u32 s83, s82\n\t" \
    "s_cbranch_scc1 .Lc_w0\n\t" \
    ".Lc_r0:\n\t" \
    "ds_read_b64_tr_b16 v[158:159], v229 offset:30720\n\t" \
    "v_add_u32 v229, 0x8000, v229\n\t" \
    "v_and_b32 v229, 0x1ffff, v229\n\t" \
    "s_waitcnt vmcnt(12) lgkmcnt(8)\n\t" \
    "v_mfma_f32_16x16x32_bf16 v[0:3], v[128:131], v[80:83], v[0:3]\n\t" \
    "ds_read_b64_tr_b16 v[96:97], v226\n\t" \
    "buffer_load_dwordx4 v[72:75], v243, %[rsb], s80 offen sc1\n\t" \
    "v_mfma_f32_16x16x32_bf16 v[16:19], v[128:131], v[84:87], v[16:19]\n\t" \
    "ds_read_b64_tr_b16 v[98:99], v226 offset:2048\n\t" \
    "v_add3_u32 v230, v80, v81, v230\n\t" \
    "v_mfma_f32_16x16x32_bf16 v[4:7], v[132:135], v[80:83], v[4:7]\n\t" \
    "ds_read_b64_tr_b16 v[100:101], v227\n\t" \
    "buffer_load_dwordx4 v[76:79], v243, %[rsb], s80 offen offset:1024 sc1\n\t" \
    "v_mfma_f32_16x16x32_bf16 v[20:23], v[132:135], v[84:87], v[20:23]\n\t" \
    "ds_read_b64_tr_b16 v[102:103], v227 offset:2048\n\t" \
    "v_add3_u32 v230, v82, v83, v230\n\t" \
    "v_mfma_f32_16x16x32_bf16 v[8:11], v[136:139], v[80:83], v[8:11]\n\t" \
    "ds_read_b64_tr_b16 v[104:105], v228\n\t" \
    "v_add3_u32 v230, v84, v85, v230\n\t" \
    "v_mfma_f32_16x16x32_bf16 v[24:27], v[136:139], v[84:87], v[24:27]\n\t" \
    "ds_read_b64_tr_b16 v[106:107], v228 offset:2048\n\t" \
    "v_add3_u32 v230, v86, v87, v230\n\t" \
    "v_mfma_f32_16x16x32_bf16 v[12:15], v[140:143], v[80:83], v[12:15]\n\t" \
    "ds_read_b64_tr_b16 v[108:109], v229\n\t" \
    "v_mfma_f32_16x16x32_bf16 v[28:31], v[140:143], v[84:87], v[28:31]\n\t" \
    "ds_read_b64_tr_b16 v[110:111], v229 offset:2048\n\t" \
    "s_waitcnt vmcnt(12) lgkmcnt(8)\n\t" \
    "v_mfma_f32_16x16x32_bf16 v[0:3], v[144:147], v[88:91], v[0:3]\n\t" \
    "ds_read_b64_tr_b16 v[112:113], v226 offset:4096\n\t" \
    "buffer_load_dwordx4 v[80:83], v243, %[rsb], s80 offen offset:2048 sc1\n\t" \
    "v_mfma_f32_16x16x32_bf16 v[16:19], v[144:147], v[92:95], v[16:19]\n\t" \
    "ds_read_b64_tr_b16 v[114:115], v226 offset:6144\n\t" \
    "v_add3_u32 v230, v88, v89, v230\n\t" \
    "v_mfma_f32_16x16x32_bf16 v[4:7], v[148:151], v[88:91], v[4:7]\n\t" \
    "ds_read_b64_tr_b16 v[116:117], v227 offset:4096\n\t" \
    "buffer_load_dwordx4 v[84:87], v243, %[rsb], s80 offen offset:3072 sc1\n\t" \
    "v_mfma_f32_16x16x32_bf16 v[20:23], v[148:151], v[92:95], v[20:23]\n\t" \
    "ds_read_b64_tr_b16 v[118:119], v227 offset:6144\n\t" \
    "v_add3_u32 v230, v90, v91, v230\n\t" \
    "v_mfma_f32_16x16x32_bf16 v[8:11], v[152:155], v[88:91], v[8:11]\n\t" \
    "ds_read_b64_tr_b16 v[120:121], v228 offset:4096\n\t" \
    "v_add3_u32 v230, v92, v93, v230\n\t" \
    "v_mfma_f32_16x16x32_bf16 v[24:27], v[152:155], v[92:95], v[24:27]\n\t" \
    "ds_read_b64_tr_b16 v[122:123], v228 offset:6144\n\t" \
    "v_add3_u32 v230, v94, v95, v230\n\t" \
    "v_mfma_f32_16x16x32_bf16 v[12:15], v[156:159], v[88:91], v[12:15]\n\t" \
    "ds_read_b64_tr_b16 v[124:125], v229 offset:4096\n\t" \
    "s_add_u32 s80, s80, 0x4000\n\t" \
    "s_and_b32 s80, s80, 0xc000\n\t" \
    "v_mfma_f32_16x16x32_bf16 v[28:31], v[156:159], v[92:95], v[28:31]\n\t" \
    "ds_read_b64_tr_b16 v[126:127], v229 offset:6144\n\t" \
    "s_sub_u32 s81, s81, 1\n\t" \
    "s_cmp_lg_u32 s81, 0\n\t" \
    "s_cbranch_scc1 .Lc_l\n\t" \
    "s_waitcnt vmcnt(12) lgkmcnt(8)\n\t" \
    "v_mfma_f32_16x16x32_bf16 v[162:165], v[96:99], v[32:35], 0\n\t" \
    "ds_read_b64_tr_b16 v[128:129], v226 offset:8192\n\t" \
    "buffer_load_dwordx4 v[88:91], v240, %[rsb], s80 offen sc1\n\t" \
    "v_mfma_f32_16x16x32_bf16 v[178:181], v[96:99], v[36:39], 0\n\t" \
    "ds_read_b64_tr_b16 v[130:131], v226 offset:10240\n\t" \
    "v_add3_u32 v230, v32, v33, v230\n\t" \
    "v_mfma_f32_16x16x32_bf16 v[166:169], v[100:103], v[32:35], 0\n\t" \
    "ds_read_b64_tr_b16 v[132:133], v227 offset:8192\n\t" \
    "buffer_load_dwordx4 v[92:95], v240, %[rsb], s80 offen offset:1024 sc1\n\t" \
    "v_mfma_f32_16x16x32_bf16 v[182:185], v[100:103], v[36:39], 0\n\t" \
    "ds_read_b64_tr_b16 v[134:135], v227 offset:10240\n\t" \
    "v_add3_u32 v230, v34, v35, v230\n\t" \
    "v_mfma_f32_16x16x32_bf16 v[170:173], v[104:107], v[32:35], 0\n\t" \
    "ds_read_b64_tr_b16 v[136:137], v228 offset:8192\n\t" \
    "v_add3_u32 v230, v36, v37, v230\n\t" \
    "v_mfma_f32_16x16x32_bf16 v[186:189], v[104:107], v[36:39], 0\n\t" \
    "ds_read_b64_tr_b16 v[138:139], v228 offset:10240\n\t" \
    "v_add3_u32 v230, v38, v39, v230\n\t" \
    "v_mfma_f32_16x16x32_bf16 v[174:177], v[108:111], v[32:35], 0\n\t" \
    "ds_read_b64_tr_b16 v[140:141], v229 offset:8192\n\t" \
    "v_mfma_f32_16x16x32_bf16 v[190:193], v[108:111], v[36:39], 0\n\t" \
    "ds_read_b64_tr_b16 v[142:143], v229 offset:10240\n\t" \
    "s_waitcnt vmcnt(12) lgkmcnt(8)\n\t" \
    "v_mfma_f32_16x16x32_bf16 v[162:165], v[112:115], v[40:43], v[162:165]\n\t" \
    "ds_read_b64_tr_b16 v[144:145], v226 offset:12288\n\t" \
    "buffer_load_dwordx4 v[32:35], v240, %[rsb], s80 offen offset:2048 sc1\n\t" \
    "v_mfma_f32_16x16x32_bf16 v[178:181], v[112:115], v[44:47], v[178:181]\n\t" \
    "ds_read_b64_tr_b16 v[146:147], v226 offset:14336\n\t" \
    "v_add3_u32 v230, v40, v41, v230\n\t" \
    "v_mfma_f32_16x16x32_bf16 v[166:169], v[116:119], v[40:43], v[166:169]\n\t" \
    "ds_read_b64_tr_b16 v[148:149], v227 offset:12288\n\t" \
    "buffer_load_dwordx4 v[36:39], v240, %[rsb], s80 offen offset:3072 sc1\n\t" \
    "v_mfma_f32_16x16x32_bf16 v[182:185], v[116:119], v[44:47], v[182:185]\n\t" \
    "ds_read_b64_tr_b16 v[150:151], v227 offset:14336\n\t" \
    "v_add3_u32 v230, v42, v43, v230\n\t" \
    "v_mfma_f32_16x16x32_bf16 v[170:173], v[120:123], v[40:43], v[170:173]\n\t" \
    "ds_read_b64_tr_b16 v[152:153], v228 offset:12288\n\t" \
    "v_add3_u32 v230, v44, v45, v230\n\t" \
    "v_mfma_f32_16x16x32_bf16 v[186:189], v[120:123], v[44:47], v[186:189]\n\t" \
    "ds_read_b64_tr_b16 v[154:155], v228 offset:14336\n\t" \
    "v_add3_u32 v230, v46, v47, v230\n\t" \
    "v_mfma_f32_16x16x32_bf16 v[174:177], v[124:127], v[40:43], v[174:177]\n\t" \
    "ds_read_b64_tr_b16 v[156:157], v229 offset:12288\n\t" \
    "v_mfma_f32_16x16x32_bf16 v[190:193], v[124:127], v[44:47], v[190:193]\n\t" \
    "ds_read_b64_tr_b16 v[158:159], v229 offset:14336\n\t" \
    "s_waitcnt vmcnt(12) lgkmcnt(8)\n\t" \
    "v_mfma_f32_16x16x32_bf16 v[162:165], v[128:131], v[48:51], v[162:165]\n\t" \
    "ds_read_b64_tr_b16 v[96:97], v226 offset:16384\n\t" \
    "buffer_load_dwordx4 v[40:43], v241, %[rsb], s80 offen sc1\n\t" \
    "v_mfma_f32_16x16x32_bf16 v[178:181], v[128:131], v[52:55], v[178:181]\n\t" \
    "ds_read_b64_tr_b16 v[98:99], v226 offset:18432\n\t" \
    "v_add3_u32 v230, v48, v49, v230\n\t" \
    "v_mfma_f32_16x16x32_bf16 v[166:169], v[132:135], v[48:51], v[166:169]\n\t" \
    "ds_read_b64_tr_b16 v[100:101], v227 offset:16384\n\t" \
    "buffer_load_dwordx4 v[44:47], v241, %[rsb], s80 offen offset:1024 sc1\n\t" \
    "v_mfma_f32_16x16x32_bf16 v[182:185], v[132:135], v[52:55], v[182:185]\n\t" \
    "ds_read_b64_tr_b16 v[102:103], v227 offset:18432\n\t" \
    "v_add3_u32 v230, v50, v51, v230\n\t" \
    "v_mfma_f32_16x16x32_bf16 v[170:173], v[136:139], v[48:51], v[170:173]\n\t" \
    "ds_read_b64_tr_b16 v[104:105], v228 offset:16384\n\t" \
    "v_add3_u32 v230, v52, v53, v230\n\t" \
    "v_mfma_f32_16x16x32_bf16 v[186:189], v[136:139], v[52:55], v[186:189]\n\t" \
    "ds_read_b64_tr_b16 v[106:107], v228 offset:18432\n\t" \
    "v_add3_u32 v230, v54, v55, v230\n\t" \
    "v_mfma_f32_16x16x32_bf16 v[174:177], v[140:143], v[48:51], v[174:177]\n\t" \
    "ds_read_b64_tr_b16 v[108:109], v229 offset:16384\n\t" \
    "v_mfma_f32_16x16x32_bf16 v[190:193], v[140:143], v[52:55], v[190:193]\n\t" \
    "ds_read_b64_tr_b16 v[110:111], v229 offset:18432\n\t" \
    "s_waitcnt vmcnt(12) lgkmcnt(8)\n\t" \
    "v_mfma_f32_16x16x32_bf16 v[162:165], v[144:147], v[56:59], v[162:165]\n\t" \
    "ds_read_b64_tr_b16 v[112:113], v226 offset:20480\n\t" \
    "buffer_load_dwordx4 v[48:51], v241, %[rsb], s80 offen offset:2048 sc1\n\t" \
    "v_mfma_f32_16x16x32_bf16 v[178:181], v[144:147], v[60:63], v[178:181]\n\t" \
    "ds_read_b64_tr_b16 v[114:115], v226 offset:22528\n\t" \
    "v_add3_u32 v230, v56, v57, v230\n\t" \
    "v_mfma_f32_16x16x32_bf16 v[166:169], v[148:151], v[56:59], v[166:169]\n\t" \
    "ds_read_b64_tr_b16 v[116:117], v227 offset:20480\n\t" \
    "buffer_load_dwordx4 v[52:55], v241, %[rsb], s80 offen offset:3072 sc1\n\t" \
    "v_mfma_f32_16x16x32_bf16 v[182:185], v[148:151], v[60:63], v[182:185]\n\t" \
    "ds_read_b64_tr_b16 v[118:119], v227 offset:22528\n\t" \
    "v_add3_u32 v230, v58, v59, v230\n\t" \
    "v_mfma_f32_16x16x32_bf16 v[170:173], v[152:155], v[56:59], v[170:173]\n\t" \
    "ds_read_b64_tr_b16 v[120:121], v228 offset:20480\n\t" \
    "v_add3_u32 v230, v60, v61, v230\n\t" \
    "v_mfma_f32_16x16x32_bf16 v[186:189], v[152:155], v[60:63], v[186:189]\n\t" \
    "ds_read_b64_tr_b16 v[122:123], v228 offset:22528\n\t" \
    "v_add3_u32 v230, v62, v63, v230\n\t" \
    "v_mfma_f32_16x16x32_bf16 v[174:177], v[156:159], v[56:59], v[174:177]\n\t" \
    "ds_read_b64_tr_b16 v[124:125], v229 offset:20480\n\t" \
    "v_mfma_f32_16x16x32_bf16 v[190:193], v[156:159], v[60:63], v[190:193]\n\t" \
    "ds_read_b64_tr_b16 v[126:127], v229 offset:22528\n\t" \
    "s_waitcnt vmcnt(12) lgkmcnt(8)\n\t" \
    "ds_read_b32 v160, v249 offset:56\n\t" \
    "v_mfma_f32_16x16x32_bf16 v[162:165], v[96:99], v[64:67], v[162:165]\n\t" \
    "ds_read_b64_tr_b16 v[128:129], v226 offset:24576\n\t" \
    "buffer_load_dwordx4 v[56:59], v242, %[rsb], s80 offen sc1\n\t" \
    "v_mfma_f32_16x16x32_bf16 v[178:181], v[96:99], v[68:71], v[178:181]\n\t" \
    "ds_read_b64_tr_b16 v[130:131], v226 offset:26624\n\t" \
    "v_add3_u32 v230, v64, v65, v230\n\t" \
    "v_mfma_f32_16x16x32_bf16 v[166:169], v[100:103], v[64:67], v[166:169]\n\t" \
    "ds_read_b64_tr_b16 v[132:133], v227 offset:24576\n\t" \
    "buffer_load_dwordx4 v[60:63], v242, %[rsb], s80 offen offset:1024 sc1\n\t" \
    "v_mfma_f32_16x16x32_bf16 v[182:185], v[100:103], v[68:71], v[182:185]\n\t" \
    "ds_read_b64_tr_b16 v[134:135], v227 offset:26624\n\t" \
    "v_add3_u32 v230, v66, v67, v230\n\t" \
    "v_mfma_f32_16x16x32_bf16 v[170:173], v[104:107], v[64:67], v[170:173]\n\t" \
    "ds_read_b64_tr_b16 v[136:137], v228 offset:24576\n\t" \
    "v_add3_u32 v230, v68, v69, v230\n\t" \
    "v_mfma_f32_16x16x32_bf16 v[186:189], v[104:107], v[68:71], v[186:189]\n\t" \
    "ds_read_b64_tr_b16 v[138:139], v228 offset:26624\n\t" \
    "v_add3_u32 v230, v70, v71, v230\n\t" \
    "v_mfma_f32_16x16x32_bf16 v[174:177], v[108:111], v[64:67], v[174:177]\n\t" \
    "ds_read_b64_tr_b16 v[140:141], v229 offset:24576\n\t" \
    "v_mfma_f32_16x16x32_bf16 v[190:193], v[108:111], v[68:71], v[190:193]\n\t" \
    "ds_read_b64_tr_b16 v[142:143], v229 offset:26624\n\t" \
    "s_waitcnt vmcnt(12) lgkmcnt(8)\n\t" \
    "v_mfma_f32_16x16x32_bf16 v[162:165], v[112:115], v[72:75], v[162:165]\n\t" \
    "ds_read_b64_tr_b16 v[144:145], v226 offset:28672\n\t" \
    "buffer_load_dwordx4 v[64:67], v242, %[rsb], s80 offen offset:2048 sc1\n\t" \
    "v_mfma_f32_16x16x32_bf16 v[178:181], v[112:115], v[76:79], v[178:181]\n\t" \
    "ds_read_b64_tr_b16 v[146:147], v226 offset:30720\n\t" \
    "v_add3_u32 v230, v72, v73, v230\n\t" \
    "v_mfma_f32_16x16x32_bf16 v[166:169], v[116:119], v[72:75], v[166:169]\n\t" \
    "ds_read_b64_tr_b16 v[148:149], v227 offset:28672\n\t" \
    "buffer_load_dwordx4 v[68:71], v242, %[rsb], s80 offen offset:3072 sc1\n\t" \
    "v_add_u32 v226, 0x8000, v226\n\t" \
    "v_and_b32 v226, 0x1ffff, v226\n\t" \
    "v_mfma_f32_16x16x32_bf16 v[182:185], v[116:119], v[76:79], v[182:185]\n\t" \
    "ds_read_b64_tr_b16 v[150:151], v227 offset:30720\n\t" \
    "v_add3_u32 v230, v74, v75, v230\n\t" \
    "v_mfma_f32_16x16x32_bf16 v[170:173], v[120:123], v[72:75], v[170:173]\n\t" \
    "ds_read_b64_tr_b16 v[152:153], v228 offset:28672\n\t" \
    "v_add3_u32 v230, v76, v77, v230\n\t" \
    "v_add_u32 v227, 0x8000, v227\n\t" \
    "v_and_b32 v227, 0x1ffff, v227\n\t" \
    "v_mfma_f32_16x16x32_bf16 v[186:189], v[120:123], v[76:79], v[186:189]\n\t" \
    "ds_read_b64_tr_b16 v[154:155], v228 offset:30720\n\t" \
    "v_add3_u32 v230, v78, v79, v230\n\t" \
    "v_mfma_f32_16x16x32_bf16 v[174:177], v[124:127], v[72:75], v[174:177]\n\t" \
    "v_readfirstlane_b32 s83, v160\n\t" \
    "s_add_u32 s82, s82, 1\n\t" \
    "ds_read_b64_tr_b16 v[156:157], v229 offset:28672\n\t" \
    "v_add_u32 v228, 0x8000, v228\n\t" \
    "v_and_b32 v228, 0x1ffff, v228\n\t" \
    "v_mfma_f32_16x16x32_bf16 v[190:193], v[124:127], v[76:79], v[190:193]\n\t" \
    "s_cmp_lt_u32 s83, s82\n\t" \
    "s_cbranch_scc1 .Lc_w1\n\t" \
    ".Lc_r1:\n\t" \
    "ds_read_b64_tr_b16 v[158:159], v229 offset:30720\n\t" \
    "v_add_u32 v229, 0x8000, v229\n\t" \
    "v_and_b32 v229, 0x1ffff, v229\n\t" \
    "s_waitcnt vmcnt(12) lgkmcnt(8)\n\t" \
    "v_mfma_f32_16x16x32_bf16 v[162:165], v[128:131], v[80:83], v[162:165]\n\t" \
    "ds_read_b64_tr_b16 v[96:97], v226\n\t" \
    "buffer_load_dwordx4 v[72:75], v243, %[rsb], s80 offen sc1\n\t" \
    "v_mfma_f32_16x16x32_bf16 v[178:181], v[128:131], v[84:87], v[178:181]\n\t" \
    "ds_read_b64_tr_b16 v[98:99], v226 offset:2048\n\t" \
    "v_add3_u32 v230, v80, v81, v230\n\t" \
    "v_mfma_f32_16x16x32_bf16 v[166:169], v[132:135], v[80:83], v[166:169]\n\t" \
    "ds_read_b64_tr_b16 v[100:101], v227\n\t" \
    "buffer_load_dwordx4 v[76:79], v243, %[rsb], s80 offen offset:1024 sc1\n\t" \
    "v_mfma_f32_16x16x32_bf16 v[182:185], v[132:135], v[84:87], v[182:185]\n\t" \
    "ds_read_b64_tr_b16 v[102:103], v227 offset:2048\n\t" \
    "v_add3_u32 v230, v82, v83, v230\n\t" \
    "v_mfma_f32_16x16x32_bf16 v[170:173], v[136:139], v[80:83], v[170:173]\n\t" \
    "ds_read_b64_tr_b16 v[104:105], v228\n\t" \
    "v_add3_u32 v230, v84, v85, v230\n\t" \
    "v_mfma_f32_16x16x32_bf16 v[186:189], v[136:139], v[84:87], v[186:189]\n\t" \
    "ds_read_b64_tr_b16 v[106:107], v228 offset:2048\n\t" \
    "v_add3_u32 v230, v86, v87, v230\n\t" \
    "v_mfma_f32_16x16x32_bf16 v[174:177], v[140:143], v[80:83], v[174:177]\n\t" \
    "ds_read_b64_tr_b16 v[108:109], v229\n\t" \
    "v_mfma_f32_16x16x32_bf16 v[190:193], v[140:143], v[84:87], v[190:193]\n\t" \
    "ds_read_b64_tr_b16 v[110:111], v229 offset:2048\n\t" \
    "s_waitcnt vmcnt(12) lgkmcnt(8)\n\t" \
    "v_mfma_f32_16x16x32_bf16 v[162:165], v[144:147], v[88:91], v[162:165]\n\t" \
    "ds_read_b64_tr_b16 v[112:113], v226 offset:4096\n\t" \
    "buffer_load_dwordx4 v[80:83], v243, %[rsb], s80 offen offset:2048 sc1\n\t" \
    "v_mfma_f32_16x16x32_bf16 v[178:181], v[144:147], v[92:95], v[178:181]\n\t" \
    "ds_read_b64_tr_b16 v[114:115], v226 offset:6144\n\t" \
    "v_add3_u32 v230, v88, v89, v230\n\t" \
    "v_mfma_f32_16x16x32_bf16 v[166:169], v[148:151], v[88:91], v[166:169]\n\t" \
    "ds_read_b64_tr_b16 v[116:117], v227 offset:4096\n\t" \
    "buffer_load_dwordx4 v[84:87], v243, %[rsb], s80 offen offset:3072 sc1\n\t" \
    "v_mfma_f32_16x16x32_bf16 v[182:185], v[148:151], v[92:95], v[182:185]\n\t" \
    "ds_read_b64_tr_b16 v[118:119], v227 offset:6144\n\t" \
    "v_add3_u32 v230, v90, v91, v230\n\t" \
    "v_mfma_f32_16x16x32_bf16 v[170:173], v[152:155], v[88:91], v[170:173]\n\t" \
    "ds_read_b64_tr_b16 v[120:121], v228 offset:4096\n\t" \
    "v_add3_u32 v230, v92, v93, v230\n\t" \
    "v_mfma_f32_16x16x32_bf16 v[186:189], v[152:155], v[92:95], v[186:189]\n\t" \
    "ds_read_b64_tr_b16 v[122:123], v228 offset:6144\n\t" \
    "v_add3_u32 v230, v94, v95, v230\n\t" \
    "v_mfma_f32_16x16x32_bf16 v[174:177], v[156:159], v[88:91], v[174:177]\n\t" \
    "ds_read_b64_tr_b16 v[124:125], v229 offset:4096\n\t" \
    "s_add_u32 s80, s80, 0x4000\n\t" \
    "s_and_b32 s80, s80, 0xc000\n\t" \
    "v_mfma_f32_16x16x32_bf16 v[190:193], v[156:159], v[92:95], v[190:193]\n\t" \
    "ds_read_b64_tr_b16 v[126:127], v229 offset:6144\n\t" \
    "s_waitcnt vmcnt(12) lgkmcnt(8)\n\t" \
    "v_mfma_f32_16x16x32_bf16 v[162:165], v[96:99], v[32:35], v[162:165]\n\t" \
    "ds_read_b64_tr_b16 v[128:129], v226 offset:8192\n\t" \
    "buffer_load_dwordx4 v[88:91], v240, %[rsb], s80 offen sc1\n\t" \
    "v_mfma_f32_16x16x32_bf16 v[178:181], v[96:99], v[36:39], v[178:181]\n\t" \
    "ds_read_b64_tr_b16 v[130:131], v226 offset:10240\n\t" \
    "v_add3_u32 v230, v32, v33, v230\n\t" \
    ".Lpad_c1:\n\t" \
    "s_cbranch_execz .Lpad_ret\n\t" \
    "v_mfma_f32_16x16x32_bf16 v[166:169], v[100:103], v[32:35], v[166:169]\n\t" \
    "ds_read_b64_tr_b16 v[132:133], v227 offset:8192\n\t" \
    "buffer_load_dwordx4 v[92:95], v240, %[rsb], s80 offen offset:1024 sc1\n\t" \
    "v_mfma_f32_16x16x32_bf16 v[182:185], v[100:103], v[36:39], v[182:185]\n\t" \
    "ds_read_b64_tr_b16 v[134:135], v227 offset:10240\n\t" \
    "v_add3_u32 v230, v34, v35, v230\n\t" \
    "v_mfma_f32_16x16x32_bf16 v[170:173], v[104:107], v[32:35], v[170:173]\n\t" \
    "ds_read_b64_tr_b16 v[136:137], v228 offset:8192\n\t" \
    "v_add3_u32 v230, v36, v37, v230\n\t" \
    "v_mfma_f32_16x16x32_bf16 v[186:189], v[104:107], v[36:39], v[186:189]\n\t" \
    "ds_read_b64_tr_b16 v[138:139], v228 offset:10240\n\t" \
    "v_add3_u32 v230, v38, v39, v230\n\t" \
    "v_mfma_f32_16x16x32_bf16 v[174:177], v[108:111], v[32:35], v[174:177]\n\t" \
    "ds_read_b64_tr_b16 v[140:141], v229 offset:8192\n\t" \
    "buffer_load_dwordx2 v[160:161], v244, %[rsb], %[sog] offen sc1\n\t" \
    "v_mfma_f32_16x16x32_bf16 v[190:193], v[108:111], v[36:39], v[190:193]\n\t" \
    "ds_read_b64_tr_b16 v[142:143], v229 offset:10240\n\t" \
    "s_waitcnt vmcnt(13) lgkmcnt(8)\n\t" \
    "v_mfma_f32_16x16x32_bf16 v[162:165], v[112:115], v[40:43], v[162:165]\n\t" \
    "ds_read_b64_tr_b16 v[144:145], v226 offset:12288\n\t" \
    "v_mfma_f32_16x16x32_bf16 v[178:181], v[112:115], v[44:47], v[178:181]\n\t" \
    "ds_read_b64_tr_b16 v[146:147], v226 offset:14336\n\t" \
    "v_add3_u32 v230, v40, v41, v230\n\t" \
    "v_mfma_f32_16x16x32_bf16 v[166:169], v[116:119], v[40:43], v[166:169]\n\t" \
    "ds_read_b64_tr_b16 v[148:149], v227 offset:12288\n\t" \
    "v_mfma_f32_16x16x32_bf16 v[182:185], v[116:119], v[44:47], v[182:185]\n\t" \
    "ds_read_b64_tr_b16 v[150:151], v227 offset:14336\n\t" \
    "v_add3_u32 v230, v42, v43, v230\n\t" \
    "v_mfma_f32_16x16x32_bf16 v[170:173], v[120:123], v[40:43], v[170:173]\n\t" \
    "ds_read_b64_tr_b16 v[152:153], v228 offset:12288\n\t" \
    "v_add3_u32 v230, v44, v45, v230\n\t" \
    "v_mfma_f32_16x16x32_bf16 v[186:189], v[120:123], v[44:47], v[186:189]\n\t" \
    "ds_read_b64_tr_b16 v[154:155], v228 offset:14336\n\t" \
    "v_add3_u32 v230, v46, v47, v230\n\t" \
    "v_mfma_f32_16x16x32_bf16 v[174:177], v[124:127], v[40:43], v[174:177]\n\t" \
    "ds_read_b64_tr_b16 v[156:157], v229 offset:12288\n\t" \
    "v_mfma_f32_16x16x32_bf16 v[190:193], v[124:127], v[44:47], v[190:193]\n\t" \
    "ds_read_b64_tr_b16 v[158:159], v229 offset:14336\n\t" \
    "s_waitcnt vmcnt(11) lgkmcnt(8)\n\t" \
    "v_mfma_f32_16x16x32_bf16 v[162:165], v[128:131], v[48:51], v[162:165]\n\t" \
    "ds_read_b64_tr_b16 v[96:97], v226 offset:16384\n\t" \
    "v_mfma_f32_16x16x32_bf16 v[178:181], v[128:131], v[52:55], v[178:181]\n\t" \
    "ds_read_b64_tr_b16 v[98:99], v226 offset:18432\n\t" \
    "v_add3_u32 v230, v48, v49, v230\n\t" \
    "v_mfma_f32_16x16x32_bf16 v[166:169], v[132:135], v[48:51], v[166:169]\n\t" \
    "ds_read_b64_tr_b16 v[100:101], v227 offset:16384\n\t" \
    "v_mfma_f32_16x16x32_bf16 v[182:185], v[132:135], v[52:55], v[182:185]\n\t" \
    "ds_read_b64_tr_b16 v[102:103], v227 offset:18432\n\t" \
    "v_add3_u32 v230, v50, v51, v230\n\t" \
    "v_mfma_f32_16x16x32_bf16 v[170:173], v[136:139], v[48:51], v[170:173]\n\t" \
    "ds_read_b64_tr_b16 v[104:105], v228 offset:16384\n\t" \
    "v_add3_u32 v230, v52, v53, v230\n\t" \
    "v_mfma_f32_16x16x32_bf16 v[186:189], v[136:139], v[52:55], v[186:189]\n\t" \
    "ds_read_b64_tr_b16 v[106:107], v228 offset:18432\n\t" \
    "v_add3_u32 v230, v54, v55, v230\n\t" \
    "v_mfma_f32_16x16x32_bf16 v[174:177], v[140:143], v[48:51], v[174:177]\n\t" \
    "ds_read_b64_tr_b16 v[108:109], v229 offset:16384\n\t" \
    "v_mfma_f32_16x16x32_bf16 v[190:193], v[140:143], v[52:55], v[190:193]\n\t" \
    "ds_read_b64_tr_b16 v[110:111], v229 offset:18432\n\t" \
    "s_waitcnt vmcnt(9) lgkmcnt(8)\n\t" \
    "v_mfma_f32_16x16x32_bf16 v[162:165], v[144:147], v[56:59], v[162:165]\n\t" \
    "ds_read_b64_tr_b16 v[112:113], v226 offset:20480\n\t" \
    "v_mfma_f32_16x16x32_bf16 v[178:181], v[144:147], v[60:63], v[178:181]\n\t" \
    "ds_read_b64_tr_b16 v[114:115], v226 offset:22528\n\t" \
    "v_add3_u32 v230, v56, v57, v230\n\t" \
    "v_mfma_f32_16x16x32_bf16 v[166:169], v[148:151], v[56:59], v[166:169]\n\t" \
    "ds_read_b64_tr_b16 v[116:117], v227 offset:20480\n\t" \
    "v_mfma_f32_16x16x32_bf16 v[182:185], v[148:151], v[60:63], v[182:185]\n\t" \
    "ds_read_b64_tr_b16 v[118:119], v227 offset:22528\n\t" \
    "v_add3_u32 v230, v58, v59, v230\n\t" \
    "v_mfma_f32_16x16x32_bf16 v[170:173], v[152:155], v[56:59], v[170:173]\n\t" \
    "ds_read_b64_tr_b16 v[120:121], v228 offset:20480\n\t" \
    "v_add3_u32 v230, v60, v61, v230\n\t" \
    "v_mfma_f32_16x16x32_bf16 v[186:189], v[152:155], v[60:63], v[186:189]\n\t" \
    "ds_read_b64_tr_b16 v[122:123], v228 offset:22528\n\t" \
    "v_add3_u32 v230, v62, v63, v230\n\t" \
    "v_mfma_f32_16x16x32_bf16 v[174:177], v[156:159], v[56:59], v[174:177]\n\t" \
    "ds_read_b64_tr_b16 v[124:125], v229 offset:20480\n\t" \
    "v_mfma_f32_16x16x32_bf16 v[190:193], v[156:159], v[60:63], v[190:193]\n\t" \
    "ds_read_b64_tr_b16 v[126:127], v229 offset:22528\n\t" \
    "s_waitcnt vmcnt(7) lgkmcnt(8)\n\t" \
    "v_mfma_f32_16x16x32_bf16 v[162:165], v[96:99], v[64:67], v[162:165]\n\t" \
    "ds_read_b64_tr_b16 v[128:129], v226 offset:24576\n\t" \
    "v_mfma_f32_16x16x32_bf16 v[178:181], v[96:99], v[68:71], v[178:181]\n\t" \
    "ds_read_b64_tr_b16 v[130:131], v226 offset:26624\n\t" \
    "v_add3_u32 v230, v64, v65, v230\n\t" \
    "v_mfma_f32_16x16x32_bf16 v[166:169], v[100:103], v[64:67], v[166:169]\n\t" \
    "ds_read_b64_tr_b16 v[132:133], v227 offset:24576\n\t" \
    "v_mfma_f32_16x16x32_bf16 v[182:185], v[100:103], v[68:71], v[182:185]\n\t" \
    "ds_read_b64_tr_b16 v[134:135], v227 offset:26624\n\t" \
    "v_add3_u32 v230, v66, v67, v230\n\t" \
    "v_mfma_f32_16x16x32_bf16 v[170:173], v[104:107], v[64:67], v[170:173]\n\t" \
    "ds_read_b64_tr_b16 v[136:137], v228 offset:24576\n\t" \
    "v_add3_u32 v230, v68, v69, v230\n\t" \
    "v_mfma_f32_16x16x32_bf16 v[186:189], v[104:107], v[68:71], v[186:189]\n\t" \
    "ds_read_b64_tr_b16 v[138:139], v228 offset:26624\n\t" \
    "v_add3_u32 v230, v70, v71, v230\n\t" \
    "v_mfma_f32_16x16x32_bf16 v[174:177], v[108:111], v[64:67], v[174:177]\n\t" \
    "ds_read_b64_tr_b16 v[140:141], v229 offset:24576\n\t" \
    "v_mfma_f32_16x16x32_bf16 v[190:193], v[108:111], v[68:71], v[190:193]\n\t" \
    "ds_read_b64_tr_b16 v[142:143], v229 offset:26624\n\t" \
    "s_waitcnt vmcnt(5) lgkmcnt(8)\n\t" \
    "v_mfma_f32_16x16x32_bf16 v[162:165], v[112:115], v[72:75], v[162:165]\n\t" \
    "ds_read_b64_tr_b16 v[144:145], v226 offset:28672\n\t" \
    "v_mfma_f32_16x16x32_bf16 v[178:181], v[112:115], v[76:79], v[178:181]\n\t" \
    "ds_read_b64_tr_b16 v[146:147], v226 offset:30720\n\t" \
    "v_add3_u32 v230, v72, v73, v230\n\t" \
    "v_mfma_f32_16x16x32_bf16 v[166:169], v[116:119], v[72:75], v[166:169]\n\t" \
    "ds_read_b64_tr_b16 v[148:149], v227 offset:28672\n\t" \
    "v_mfma_f32_16x16x32_bf16 v[182:185], v[116:119], v[76:79], v[182:185]\n\t" \
    "ds_read_b64_tr_b16 v[150:151], v227 offset:30720\n\t" \
    "v_add3_u32 v230, v74, v75, v230\n\t" \
    "v_mfma_f32_16x16x32_bf16 v[170:173], v[120:123], v[72:75], v[170:173]\n\t" \
    "ds_read_b64_tr_b16 v[152:153], v228 offset:28672\n\t" \
    "v_add3_u32 v230, v76, v77, v230\n\t" \
    "v_mfma_f32_16x16x32_bf16 v[186:189], v[120:123], v[76:79], v[186:189]\n\t" \
    "ds_read_b64_tr_b16 v[154:155], v228 offset:30720\n\t" \
    "v_add3_u32 v230, v78, v79, v230\n\t" \
    "v_mfma_f32_16x16x32_bf16 v[174:177], v[124:127], v[72:75], v[174:177]\n\t" \
    "ds_read_b64_tr_b16 v[156:157], v229 offset:28672\n\t" \
    "v_mfma_f32_16x16x32_bf16 v[190:193], v[124:127], v[76:79], v[190:193]\n\t" \
    "ds_read_b64_tr_b16 v[158:159], v229 offset:30720\n\t" \
    "s_waitcnt vmcnt(3) lgkmcnt(8)\n\t" \
    "v_mfma_f32_16x16x32_bf16 v[162:165], v[128:131], v[80:83], v[162:165]\n\t" \
    "ds_read_b32 v231, v249 offset:68\n\t" \
    "v_mfma_f32_16x16x32_bf16 v[178:181], v[128:131], v[84:87], v[178:181]\n\t" \
    "v_add3_u32 v230, v80, v81, v230\n\t" \
    "v_mfma_f32_16x16x32_bf16 v[166:169], v[132:135], v[80:83], v[166:169]\n\t" \
    "ds_read_b128 v[96:99], v252\n\t" \
    "v_mfma_f32_16x16x32_bf16 v[182:185], v[132:135], v[84:87], v[182:185]\n\t" \
    "ds_read_b128 v[100:103], v252 offset:64\n\t" \
    "v_add3_u32 v230, v82, v83, v230\n\t" \
    "v_mfma_f32_16x16x32_bf16 v[170:173], v[136:139], v[80:83], v[170:173]\n\t" \
    "ds_read_b128 v[104:107], v252 offset:128\n\t" \
    "v_add3_u32 v230, v84, v85, v230\n\t" \
    "v_mfma_f32_16x16x32_bf16 v[186:189], v[136:139], v[84:87], v[186:189]\n\t" \
    "ds_read_b128 v[108:111], v252 offset:192\n\t" \
    "v_add3_u32 v230, v86, v87, v230\n\t" \
    "v_mfma_f32_16x16x32_bf16 v[174:177], v[140:143], v[80:83], v[174:177]\n\t" \
    "v_mfma_f32_16x16x32_bf16 v[190:193], v[140:143], v[84:87], v[190:193]\n\t" \
    "s_waitcnt vmcnt(1) lgkmcnt(5)\n\t" \
    "v_mfma_f32_16x16x32_bf16 v[162:165], v[144:147], v[88:91], v[162:165]\n\t" \
    "v_mfma_f32_16x16x32_bf16 v[166:169], v[148:151], v[88:91], v[166:169]\n\t" \
    "v_add3_u32 v230, v88, v89, v230\n\t" \
    "v_mfma_f32_16x16x32_bf16 v[170:173], v[152:155], v[88:91], v[170:173]\n\t" \
    "v_mfma_f32_16x16x32_bf16 v[174:177], v[156:159], v[88:91], v[174:177]\n\t" \
    "v_add3_u32 v230, v90, v91, v230\n\t" \
    "v_mfma_f32_16x16x32_bf16 v[178:181], v[144:147], v[92:95], v[178:181]\n\t" \
    "v_add3_u32 v230, v92, v93, v230\n\t" \
    "v_mfma_f32_16x16x32_bf16 v[182:185], v[148:151], v[92:95], v[182:185]\n\t" \
    "v_add3_u32 v230, v94, v95, v230\n\t" \
    "v_mfma_f32_16x16x32_bf16 v[186:189], v[152:155], v[92:95], v[186:189]\n\t" \
    "s_cmp_lg_u64 %[rm], 0\n\t" \
    "v_mfma_f32_16x16x32_bf16 v[190:193], v[156:159], v[92:95], v[190:193]\n\t" \
    "s_cbranch_scc1 .Lc_y\n\t" \
    ".Lc_yr:\n\t" \
    "s_add_u32 s85, %[sout], 0x8000\n\t" \
    "v_pk_add_f32 v[0:1], v[0:1], v[162:163]\n\t" \
    "v_pk_add_f32 v[2:3], v[2:3], v[164:165]\n\t" \
    "v_pk_add_f32 v[4:5], v[4:5], v[166:167]\n\t" \
    "v_pk_add_f32 v[6:7], v[6:7], v[168:169]\n\t" \
    "v_pk_fma_f32 v[0:1], v[232:233], v[0:1], v[194:195] op_sel_hi:[0,1,1]\n\t" \
    "v_pk_fma_f32 v[2:3], v[232:233], v[2:3], v[196:197] op_sel_hi:[0,1,1]\n\t" \
    "v_pk_fma_f32 v[4:5], v[232:233], v[4:5], v[198:199] op_sel_hi:[0,1,1]\n\t" \
    "v_pk_fma_f32 v[6:7], v[232:233], v[6:7], v[200:201] op_sel_hi:[0,1,1]\n\t" \
    "v_cvt_pk_bf16_f32 v32, v0, v1\n\t" \
    "v_cvt_pk_bf16_f32 v34, v4, v5\n\t" \
    "v_cvt_pk_bf16_f32 v33, v2, v3\n\t" \
    "v_cvt_pk_bf16_f32 v35, v6, v7\n\t" \
    "s_nop 0\n\t" \
    "v_permlane16_swap_b32 v32, v34\n\t" \
    "v_permlane16_swap_b32 v33, v35\n\t" \
    "buffer_store_dwordx4 v[32:35], v247, %[rso], %[sout] offen sc1\n\t" \
    "v_pk_add_f32 v[8:9], v[8:9], v[170:171]\n\t" \
    "v_pk_add_f32 v[10:11], v[10:11], v[172:173]\n\t" \
    "v_pk_add_f32 v[12:13], v[12:13], v[174:175]\n\t" \
    "v_pk_add_f32 v[14:15], v[14:15], v[176:177]\n\t" \
    "v_pk_fma_f32 v[8:9], v[232:233], v[8:9], v[202:203] op_sel_hi:[0,1,1]\n\t" \
    "v_pk_fma_f32 v[10:11], v[232:233], v[10:11], v[204:205] op_sel_hi:[0,1,1]\n\t" \
    "v_pk_fma_f32 v[12:13], v[232:233], v[12:13], v[206:207] op_sel_hi:[0,1,1]\n\t" \
    "v_pk_fma_f32 v[14:15], v[232:233], v[14:15], v[208:209] op_sel_hi:[0,1,1]\n\t" \
    "v_cvt_pk_bf16_f32 v36, v8, v9\n\t" \
    "v_cvt_pk_bf16_f32 v38, v12, v13\n\t" \
    "v_cvt_pk_bf16_f32 v37, v10, v11\n\t" \
    "v_cvt_pk_bf16_f32 v39, v14, v15\n\t" \
    "v_cmp_gt_u32 vcc, 32, v250\n\t" \
    "v_permlane16_swap_b32 v36, v38\n\t" \
    "v_permlane16_swap_b32 v37, v39\n\t" \
    "buffer_store_dwordx4 v[36:39], v247, %[rso], %[sout] offen offset:64 sc1\n\t" \
    "v_pk_add_f32 v[16:17], v[16:17], v[178:179]\n\t" \
    "v_pk_add_f32 v[18:19], v[18:19], v[180:181]\n\t" \
    "v_pk_add_f32 v[20:21], v[20:21], v[182:183]\n\t" \
    "v_pk_add_f32 v[22:23], v[22:23], v[184:185]\n\t" \
    "v_pk_fma_f32 v[16:17], v[234:235], v[16:17], v[210:211] op_sel_hi:[0,1,1]\n\t" \
    "v_pk_fma_f32 v[18:19], v[234:235], v[18:19], v[212:213] op_sel_hi:[0,1,1]\n\t" \
    "v_pk_fma_f32 v[20:21], v[234:235], v[20:21], v[214:215] op_sel_hi:[0,1,1]\n\t" \
    "v_pk_fma_f32 v[22:23], v[234:235], v[22:23], v[216:217] op_sel_hi:[0,1,1]\n\t" \
    "v_cvt_pk_bf16_f32 v40, v16, v17\n\t" \
    "v_cvt_pk_bf16_f32 v42, v20, v21\n\t" \
    "v_cvt_pk_bf16_f32 v41, v18, v19\n\t" \
    "v_cvt_pk_bf16_f32 v43, v22, v23\n\t" \
    "s_waitcnt vmcnt(2)\n\t" \
    "v_permlane16_swap_b32 v40, v42\n\t" \
    "v_permlane16_swap_b32 v41, v43\n\t" \
    "buffer_store_dwordx4 v[40:43], v247, %[rso], s85 offen sc1\n\t" \
    "v_pk_add_f32 v[24:25], v[24:25], v[186:187]\n\t" \
    "v_pk_add_f32 v[26:27], v[26:27], v[188:189]\n\t" \
    "v_pk_add_f32 v[28:29], v[28:29], v[190:191]\n\t" \
    "v_pk_add_f32 v[30:31], v[30:31], v[192:193]\n\t" \
    "v_pk_fma_f32 v[24:25], v[234:235], v[24:25], v[218:219] op_sel_hi:[0,1,1]\n\t" \
    "v_pk_fma_f32 v[26:27], v[234:235], v[26:27], v[220:221] op_sel_hi:[0,1,1]\n\t" \
    "v_pk_fma_f32 v[28:29], v[234:235], v[28:29], v[222:223] op_sel_hi:[0,1,1]\n\t" \
    "v_pk_fma_f32 v[30:31], v[234:235], v[30:31], v[224:225] op_sel_hi:[0,1,1]\n\t" \
    "v_cvt_pk_bf16_f32 v44, v24, v25\n\t" \
    "v_cvt_pk_bf16_f32 v46, v28, v29\n\t" \
    "v_cvt_pk_bf16_f32 v45, v26, v27\n\t" \
    "v_cvt_pk_bf16_f32 v47, v30, v31\n\t" \
    "s_waitcnt lgkmcnt(4)\n\t" \
    "v_cndmask_b32 v231, 0, v160, vcc\n\t" \
    "v_permlane16_swap_b32 v44, v46\n\t" \
    "v_permlane16_swap_b32 v45, v47\n\t" \
    "buffer_store_dwordx4 v[44:47], v247, %[rso], s85 offen offset:64 sc1\n\t" \
    "v_sub_u32 v231, v230, v231\n\t" \
    "v_cmp_ne_u32 vcc, v236, v161\n\t" \
    "v_mov_b32 v0, v231\n\t" \
    "s_nop 1\n\t" \
    "v_permlane32_swap_b32 v231, v0\n\t" \
    "v_add_u32 v231, v231, v0\n\t" \
    "v_mov_b32 v0, v231\n\t" \
    "s_nop 1\n\t" \
    "v_permlane16_swap_b32 v231, v0\n\t" \
    "v_add_u32 v231, v231, v0\n\t" \
    "s_nop 1\n\t" \
    "v_add_u32_dpp v231, v231, v231 row_ror:8 row_mask:0xf bank_mask:0xf bound_ctrl:1\n\t" \
    "s_nop 1\n\t" \
    "v_add_u32_dpp v231, v231, v231 row_ror:4 row_mask:0xf bank_mask:0xf bound_ctrl:1\n\t" \
    "s_nop 1\n\t" \
    "v_add_u32_dpp v231, v231, v231 row_ror:2 row_mask:0xf bank_mask:0xf bound_ctrl:1\n\t" \
    "s_nop 1\n\t" \
    "v_add_u32_dpp v231, v231, v231 row_ror:1 row_mask:0xf bank_mask:0xf bound_ctrl:1\n\t" \
    "s_nop 0\n\t" \
    "v_readfirstlane_b32 s83, v231\n\t" \
    "s_or_b32 s83, s83, vcc_lo\n\t" \
    "s_cselect_b32 %[st], 2, 0\n\t" \
    "s_waitcnt lgkmcnt(0)\n\t" \
    ".Lc_end:\n\t"
#define RED1_ASM \
    "s_mov_b32 s80, 256\n\t" \
    "s_add_u32 s81, %[s0], 0x1000\n\t" \
    "v_mov_b32 v41, 1\n\t" \
    ".Lr_t:\n\t" \
    "s_memrealtime s[82:83]\n\t" \
    "s_sub_u32 s80, s80, 1\n\t" \
    "s_cbranch_scc1 .Lr_go\n\t" \
    "s_waitcnt lgkmcnt(0)\n\t" \
    "s_sub_u32 s82, %[t1], s82\n\t" \
    "s_cmp_lt_i32 s82, 5\n\t" \
    "s_cbranch_scc1 .Lr_go\n\t" \
    "s_cmp_lt_i32 s82, 13\n\t" \
    "s_cbranch_scc1 .Lr_t\n\t" \
    "s_sleep 2\n\t" \
    "s_branch .Lr_t\n\t" \
    ".Lr_go:\n\t" \
    "buffer_load_dwordx4 v[0:3], %[v16], %[rsb], %[sm] offen sc1\n\t" \
    "buffer_load_dwordx4 v[4:7], %[v16], %[rsb], %[s0] offen sc1\n\t" \
    "buffer_load_dwordx4 v[8:11], %[v16], %[rsb], %[s0] offen offset:1024 sc1\n\t" \
    "buffer_load_dwordx4 v[12:15], %[v16], %[rsb], %[s0] offen offset:2048 sc1\n\t" \
    "buffer_load_dwordx4 v[16:19], %[v16], %[rsb], %[s0] offen offset:3072 sc1\n\t" \
    "buffer_load_dwordx4 v[20:23], %[v16], %[rsb], s81 offen sc1\n\t" \
    "buffer_load_dwordx4 v[24:27], %[v16], %[rsb], s81 offen offset:1024 sc1\n\t" \
    "buffer_load_dwordx4 v[28:31], %[v16], %[rsb], s81 offen offset:2048 sc1\n\t" \
    "buffer_load_dwordx4 v[32:35], %[v16], %[rsb], s81 offen offset:3072 sc1\n\t" \
    "ds_write_b32 %[ra], v41\n\t" \
    "s_waitcnt vmcnt(8)\n\t" \
    "v_add3_u32 v40, v0, v1, v2\n\t" \
    "v_xor_b32 v41, %[tag], v1\n\t" \
    "v_and_b32 v42, %[mk2], v0\n\t" \
    "v_add_u32 v40, v40, v3\n\t" \
    "v_add_f32_dpp v0, v0, v0 row_ror:8 row_mask:0xf bank_mask:0xf bound_ctrl:1\n\t" \
    "v_add_f32_dpp v1, v1, v1 row_ror:8 row_mask:0xf bank_mask:0xf bound_ctrl:1\n\t" \
    "v_add_f32_dpp v2, v2, v2 row_ror:8 row_mask:0xf bank_mask:0xf bound_ctrl:1\n\t" \
    "v_add_f32_dpp v3, v3, v3 row_ror:8 row_mask:0xf bank_mask:0xf bound_ctrl:1\n\t" \
    "v_and_b32 v40, %[mk01], v40\n\t" \
    "v_sub_u32 v40, v40, v42\n\t" \
    "s_waitcnt vmcnt(7)\n\t" \
    "v_mfma_f32_16x16x32_bf16 v[36:39], %[sel], v[4:7], 0\n\t" \
    "v_add3_u32 v40, v4, v5, v40\n\t" \
    "v_add3_u32 v40, v6, v7, v40\n\t" \
    "v_add_f32_dpp v0, v0, v0 row_ror:4 row_mask:0xf bank_mask:0xf bound_ctrl:1\n\t" \
    "v_add_f32_dpp v1, v1, v1 row_ror:4 row_mask:0xf bank_mask:0xf bound_ctrl:1\n\t" \
    "s_waitcnt vmcnt(6)\n\t" \
    "v_mfma_f32_16x16x32_bf16 v[36:39], %[sel], v[8:11], v[36:39]\n\t" \
    "v_add3_u32 v40, v8, v9, v40\n\t" \
    "v_add3_u32 v40, v10, v11, v40\n\t" \
    "v_add_f32_dpp v2, v2, v2 row_ror:4 row_mask:0xf bank_mask:0xf bound_ctrl:1\n\t" \
    "v_add_f32_dpp v3, v3, v3 row_ror:4 row_mask:0xf bank_mask:0xf bound_ctrl:1\n\t" \
    "s_waitcnt vmcnt(5)\n\t" \
    "v_mfma_f32_16x16x32_bf16 v[36:39], %[sel], v[12:15], v[36:39]\n\t" \
    "v_add3_u32 v40, v12, v13, v40\n\t" \
    "v_add3_u32 v40, v14, v15, v40\n\t" \
    "v_and_b32 v41, %[mk2], v41\n\t" \
    "v_cmp_ne_u32 vcc, 0, v41\n\t" \
    "s_waitcnt vmcnt(4)\n\t" \
    "v_mfma_f32_16x16x32_bf16 v[36:39], %[sel], v[16:19], v[36:39]\n\t" \
    "v_add3_u32 v40, v16, v17, v40\n\t" \
    "v_add3_u32 v40, v18, v19, v40\n\t" \
    "v_permlane32_swap_b32 v0, v2\n\t" \
    "v_permlane32_swap_b32 v1, v3\n\t" \
    "s_waitcnt vmcnt(3)\n\t" \
    "v_mfma_f32_16x16x32_bf16 v[36:39], %[sel], v[20:23], v[36:39]\n\t" \
    "v_add3_u32 v40, v20, v21, v40\n\t" \
    "v_add3_u32 v40, v22, v23, v40\n\t" \
    "v_add_f32 v0, v0, v2\n\t" \
    "v_add_f32 v1, v1, v3\n\t" \
    "s_waitcnt vmcnt(2)\n\t" \
    "v_mfma_f32_16x16x32_bf16 v[36:39], %[sel], v[24:27], v[36:39]\n\t" \
    "v_add3_u32 v40, v24, v25, v40\n\t" \
    "v_add3_u32 v40, v26, v27, v40\n\t" \
    "v_mov_b32 v2, v0\n\t" \
    "v_mov_b32 v3, v1\n\t" \
    "s_waitcnt vmcnt(1)\n\t" \
    "v_mfma_f32_16x16x32_bf16 v[36:39], %[sel], v[28:31], v[36:39]\n\t" \
    "v_add3_u32 v40, v28, v29, v40\n\t" \
    "v_add3_u32 v40, v30, v31, v40\n\t" \
    "s_or_b32 s80, vcc_lo, vcc_hi\n\t" \
    "s_waitcnt vmcnt(0)\n\t" \
    "v_mfma_f32_16x16x32_bf16 v[36:39], %[sel], v[32:35], v[36:39]\n\t" \
    "v_add3_u32 v40, v32, v33, v40\n\t" \
    "v_add3_u32 v40, v34, v35, v40\n\t" \
    "v_permlane16_swap_b32 v0, v2\n\t" \
    "v_permlane16_swap_b32 v1, v3\n\t" \
    "v_add_f32 v0, v0, v2\n\t" \
    "v_add_f32 v1, v1, v3\n\t" \
    "v_cvt_pk_bf16_f32 v44, v0, v1\n\t" \
    "v_mov_b32 v45, v44\n\t" \
    "v_cvt_pk_bf16_f32 v48, v36, v37\n\t" \
    "v_cvt_pk_bf16_f32 v49, v38, v39\n\t" \
    "v_and_b32 v46, %[mkl], v44\n\t" \
    "v_add3_u32 v46, v46, v48, v49\n\t" \
    "v_permlane32_swap_b32 v44, v45\n\t" \
    "buffer_store_dwordx2 v[48:49], %[vkf], %[rsb], %[sf] offen sc1\n\t" \
    "v_permlane32_swap_b32 v40, v46\n\t" \
    "v_add_u32 v40, v40, v46\n\t" \
    "v_mov_b32 v46, v40\n\t" \
    "v_cndmask_b32_e64 v45, v45, %[tag], %[hi]\n\t" \
    "s_nop 0\n\t" \
    "v_permlane16_swap_b32 v40, v46\n\t" \
    "v_add_u32 v40, v40, v46\n\t" \
    "s_nop 1\n\t" \
    "v_add_u32_dpp v40, v40, v40 row_ror:8 row_mask:0xf bank_mask:0xf bound_ctrl:1\n\t" \
    "s_nop 1\n\t" \
    "v_add_u32_dpp v40, v40, v40 row_ror:4 row_mask:0xf bank_mask:0xf bound_ctrl:1\n\t" \
    "s_nop 1\n\t" \
    "v_add_u32_dpp v40, v40, v40 row_ror:2 row_mask:0xf bank_mask:0xf bound_ctrl:1\n\t" \
    "s_nop 1\n\t" \
    "v_add_u32_dpp v40, v40, v40 row_ror:1 row_mask:0xf bank_mask:0xf bound_ctrl:1\n\t" \
    "s_nop 0\n\t" \
    "v_readfirstlane_b32 s81, v40\n\t" \
    "v_cndmask_b32_e64 v44, v44, v40, %[hi]\n\t" \
    "s_or_b32 %[st], s80, s81\n\t" \
    "s_cselect_b32 s82, 0x70000000, %[sg]\n\t" \
    "buffer_store_dwordx2 v[44:45], %[vfm], %[rsb], s82 offen sc1\n\t"
#define RED2_ASM \
    "s_mov_b32 s80, 256\n\t" \
    "s_add_u32 s81, %[s0], 0x1000\n\t" \
    "s_add_u32 s84, %[s0], 0x4000\n\t" \
    "s_mov_b32 m0, 160256\n\t" \
    ".Lr_u:\n\t" \
    "s_memrealtime s[82:83]\n\t" \
    "s_sub_u32 s80, s80, 1\n\t" \
    "s_cbranch_scc1 .Lr_g2\n\t" \
    "s_waitcnt lgkmcnt(0)\n\t" \
    "s_sub_u32 s82, %[t2], s82\n\t" \
    "s_cmp_lt_i32 s82, 5\n\t" \
    "s_cbranch_scc1 .Lr_g2\n\t" \
    "s_cmp_lt_i32 s82, 13\n\t" \
    "s_cbranch_scc1 .Lr_u\n\t" \
    "s_sleep 2\n\t" \
    "s_branch .Lr_u\n\t" \
    ".Lr_g2:\n\t" \
    "buffer_load_dwordx4 %[v16], %[rsb], s84 offen sc1 lds\n\t" \
    "s_mov_b32 m0, 151552\n\t" \
    "s_nop 0\n\t" \
    "buffer_load_dwordx4 %[v16], %[rsb], %[s0] offen sc1 lds\n\t" \
    "buffer_load_dwordx4 %[v16], %[rsb], %[s0] offen offset:1024 sc1 lds\n\t" \
    "buffer_load_dwordx4 %[v16], %[rsb], %[s0] offen offset:2048 sc1 lds\n\t" \
    "buffer_load_dwordx4 %[v16], %[rsb], %[s0] offen offset:3072 sc1 lds\n\t" \
    "s_mov_b32 m0, 155648\n\t" \
    "s_nop 0\n\t" \
    "buffer_load_dwordx4 %[v16], %[rsb], s81 offen sc1 lds\n\t" \
    "buffer_load_dwordx4 %[v16], %[rsb], s81 offen offset:1024 sc1 lds\n\t" \
    "buffer_load_dwordx4 %[v16], %[rsb], s81 offen offset:2048 sc1 lds\n\t" \
    "buffer_load_dwordx4 %[v16], %[rsb], s81 offen offset:3072 sc1 lds\n\t" \
    "ds_write_b32 %[ra], %[pv]\n\t" \
    "v_or_b32 %[pv], 1, %[pv]\n\t" \
    "v_add_u32 %[pv], 1, %[pv]\n\t" \
    "s_waitcnt vmcnt(0)\n\t" \
    "ds_write_b32 %[ra], %[pv]\n\t" \
    "ds_read_b128 v[0:3], %[kwa] offset:8704\n\t" \
    "ds_read_b128 v[4:7], %[kwa]\n\t" \
    "ds_read_b128 v[8:11], %[kwa] offset:1024\n\t" \
    "ds_read_b128 v[12:15], %[kwa] offset:2048\n\t" \
    "ds_read_b128 v[16:19], %[kwa] offset:3072\n\t" \
    "ds_read_b128 v[20:23], %[kwa] offset:4096\n\t" \
    "ds_read_b128 v[24:27], %[kwa] offset:5120\n\t" \
    "ds_read_b128 v[28:31], %[kwa] offset:6144\n\t" \
    "ds_read_b128 v[32:35], %[kwa] offset:7168\n\t" \
    "s_waitcnt lgkmcnt(8)\n\t" \
    "v_add3_u32 v40, v0, v1, v2\n\t" \
    "v_xor_b32 v41, %[tag], v1\n\t" \
    "v_and_b32 v42, %[mk1], v0\n\t" \
    "v_add_u32 v40, v40, v3\n\t" \
    "v_and_b32 v41, %[mk1], v41\n\t" \
    "v_and_b32 v40, %[mk0], v40\n\t" \
    "v_cmp_ne_u32 vcc, 0, v41\n\t" \
    "v_sub_u32 v40, v40, v42\n\t" \
    "v_mov_b32 v42, %[frc]\n\t" \
    "s_waitcnt lgkmcnt(4)\n\t" \
    "v_add3_u32 v40, v4, v5, v40\n\t" \
    "v_add3_u32 v42, v6, v7, v42\n\t" \
    "v_add3_u32 v40, v8, v9, v40\n\t" \
    "v_add3_u32 v42, v10, v11, v42\n\t" \
    "v_add3_u32 v40, v12, v13, v40\n\t" \
    "v_add3_u32 v42, v14, v15, v42\n\t" \
    "v_add3_u32 v40, v16, v17, v40\n\t" \
    "v_add3_u32 v42, v18, v19, v42\n\t" \
    "s_waitcnt lgkmcnt(0)\n\t" \
    "v_add3_u32 v40, v20, v21, v40\n\t" \
    "v_add3_u32 v42, v22, v23, v42\n\t" \
    "v_add3_u32 v40, v24, v25, v40\n\t" \
    "v_add3_u32 v42, v26, v27, v42\n\t" \
    "v_add3_u32 v40, v28, v29, v40\n\t" \
    "v_add3_u32 v42, v30, v31, v42\n\t" \
    "v_add3_u32 v40, v32, v33, v40\n\t" \
    "v_add3_u32 v42, v34, v35, v42\n\t" \
    "s_or_b32 s80, vcc_lo, vcc_hi\n\t" \
    "s_nop 0\n\t" \
    "v_permlane32_swap_b32 v40, v42\n\t" \
    "v_add_u32 v40, v40, v42\n\t" \
    "s_nop 1\n\t" \
    "v_add_u32_dpp v40, v40, v40 row_ror:8 row_mask:0xf bank_mask:0xf bound_ctrl:1\n\t" \
    "s_nop 1\n\t" \
    "v_add_u32_dpp v40, v40, v40 row_ror:4 row_mask:0xf bank_mask:0xf bound_ctrl:1\n\t" \
    "s_nop 1\n\t" \
    "v_add_u32_dpp v40, v40, v40 row_ror:2 row_mask:0xf bank_mask:0xf bound_ctrl:1\n\t" \
    "s_nop 1\n\t" \
    "v_add_u32_dpp v40, v40, v40 row_ror:1 row_mask:0xf bank_mask:0xf bound_ctrl:1\n\t" \
    "s_nop 0\n\t" \
    "v_readlane_b32 s81, v40, 0\n\t" \
    "v_readlane_b32 s82, v40, 16\n\t" \
    "v_readlane_b32 s83, v40, 32\n\t" \
    "v_readlane_b32 s84, v40, 48\n\t" \
    "s_add_u32 s81, s81, s82\n\t" \
    "s_add_u32 s83, s83, s84\n\t" \
    "s_add_u32 s81, s81, s83\n\t" \
    "s_or_b32 %[st], s80, s81\n\t" \
    "s_cselect_b32 s81, 0, 1\n\t" \
    "v_add_u32 v41, s81, %[pv]\n\t" \
    "ds_write_b32 %[ra], v41\n\t"
#define PHASE_AB_CLOB "v0", "v1", "v2", "v3", "v4", "v5", "v6", "v7", "v8", "v9", "v10", "v11", "v12", "v13", "v14", "v15", "v16", "v17", "v18", "v19", "v20", "v21", "v22", "v23", "v24", "v25", "v26", "v27", "v28", "v29", "v30", "v31", "v32", "v33", "v34", "v35", "v36", "v37", "v38", "v39", "v40", "v41", "v42", "v43", "v44", "v45", "v46", "v47", "v48", "v49", "v50", "v51", "v52", "v53", "v54", "v55", "v56", "v57", "v58", "v59", "v60", "v61", "v62", "v63", "v64", "v65", "v66", "v67", "v68", "v69", "v70", "v71", "v72", "v73", "v74", "v75", "v76", "v77", "v78", "v79", "v80", "v81", "v82", "v83", "v84", "v85", "v86", "v87", "v88", "v89", "v90", "v91", "v92", "v93", "v94", "v95", "v96", "v97", "v98", "v99", "v100", "v101", "v102", "v103", "v104", "v105", "v106", "v107", "v108", "v109", "v110", "v111", "v112", "v113", "v114", "v115", "v116", "v117", "v118", "v119", "v120", "v121", "v122", "v123", "v124", "v125", "v126", "v127", "v128", "v129", "v130", "v131", "v132", "v133", "v134", "v135", "v136", "v137", "v138", "v139", "v140", "v141", "v142", "v143", "v144", "v145", "v146", "v147", "v148", "v149", "v150", "v151", "v152", "v153", "v154", "v155", "v156", "v157", "v158", "v159", "v160", "v161", "v162", "v163", "v164", "v165", "v166", "v167", "v168", "v169", "v170", "v171", "v172", "v173", "v174", "v175", "v176", "v177", "v178", "v179", "v180", "v181", "v182", "v183", "v184", "v185", "v186", "v187", "v188", "v189", "v190", "v191", "v192", "v193", "v194", "v195", "v196", "v197", "v198", "v199", "v200", "v201", "v202", "v203", "v204", "v205", "v206", "v207", "v208", "v209", "v210", "v211", "v212", "v213", "v214", "v215", "v216", "v217", "v218", "v219", "v220", "v221", "v222", "v223", "v224", "v225", "v226", "v227", "v228", "v229", "v230", "v231", "v237", "v238", "s82", "s83", "s84", "s85", "s86", "s87", "s88", "s89", "s90", "s91", "s92", "s93", "s94", "s95", "s96", "s97"
#define PHASE_C_CLOB "v0", "v1", "v2", "v3", "v4", "v5", "v6", "v7", "v8", "v9", "v10", "v11", "v12", "v13", "v14", "v15", "v16", "v17", "v18", "v19", "v20", "v21", "v22", "v23", "v24", "v25", "v26", "v27", "v28", "v29", "v30", "v31", "v32", "v33", "v34", "v35", "v36", "v37", "v38", "v39", "v40", "v41", "v42", "v43", "v44", "v45", "v46", "v47", "v48", "v49", "v50", "v51", "v52", "v53", "v54", "v55", "v56", "v57", "v58", "v59", "v60", "v61", "v62", "v63", "v64", "v65", "v66", "v67", "v68", "v69", "v70", "v71", "v72", "v73", "v74", "v75", "v76", "v77", "v78", "v79", "v80", "v81", "v82", "v83", "v84", "v85", "v86", "v87", "v88", "v89", "v90", "v91", "v92", "v93", "v94", "v95", "v96", "v97", "v98", "v99", "v100", "v101", "v102", "v103", "v104", "v105", "v106", "v107", "v108", "v109", "v110", "v111", "v112", "v113", "v114", "v115", "v116", "v117", "v118", "v119", "v120", "v121", "v122", "v123", "v124", "v125", "v126", "v127", "v128", "v129", "v130", "v131", "v132", "v133", "v134", "v135", "v136", "v137", "v138", "v139", "v140", "v141", "v142", "v143", "v144", "v145", "v146", "v147", "v148", "v149", "v150", "v151", "v152", "v153", "v154", "v155", "v156", "v157", "v158", "v159", "v160", "v161", "v162", "v163", "v164", "v165", "v166", "v167", "v168", "v169", "v170", "v171", "v172", "v173", "v174", "v175", "v176", "v177", "v178", "v179", "v180", "v181", "v182", "v183", "v184", "v185", "v186", "v187", "v188", "v189", "v190", "v191", "v192", "v193", "v194", "v195", "v196", "v197", "v198", "v199", "v200", "v201", "v202", "v203", "v204", "v205", "v206", "v207", "v208", "v209", "v210", "v211", "v212", "v213", "v214", "v215", "v216", "v217", "v218", "v219", "v220", "v221", "v222", "v223", "v224", "v225", "v226", "v227", "v228", "v229", "v231", "s80", "s81", "s82", "s83", "s84", "s85", "s86", "s87"
#define RED1_CLOB "v0", "v1", "v2", "v3", "v4", "v5", "v6", "v7", "v8", "v9", "v10", "v11", "v12", "v13", "v14", "v15", "v16", "v17", "v18", "v19", "v20", "v21", "v22", "v23", "v24", "v25", "v26", "v27", "v28", "v29", "v30", "v31", "v32", "v33", "v34", "v35", "v36", "v37", "v38", "v39", "v40", "v41", "v42", "v43", "v44", "v45", "v46", "v47", "v48", "v49", "s80", "s81", "s82", "s83", "s84"

#define KNAME "fused_fwd"
#define CODE_FETCH(dst, kb) do { unsigned long long pc_; unsigned len_, off_; \
    asm volatile("s_getpc_b64 %0\n1:\n\ts_mov_b32 %1, 1b-" KNAME "\n\ts_mov_b32 %2, .Lff_end0-" KNAME "\n\ts_max_u32 %2, %2, .Lff_end1-" KNAME \
                 "\n\ts_max_u32 %2, %2, .Lff_end2-" KNAME : "=s"(pc_), "=s"(off_), "=s"(len_) :: "scc"); \
    const rsrc_t rsC_ = mk_rsrc((const void*)(pc_ - off_), len_); \
    asm volatile("s_nop 4\n\tbuffer_load_dwordx4 %0, %1, %2, %3 offen" : "=v"(dst) : "v"(fresh_lane() * 16), "s"(rsC_), "s"((unsigned)(kb) * 1024u) : "memory"); } while (0)

extern "C" __global__ __launch_bounds__(512, 2) void fused_fwd(const u16* __restrict__ x, const u16* __restrict__ wqkv,
                                                    const u16* __restrict__ wphi, const u16* __restrict__ wo,
                                                    const u16* __restrict__ wr, const u16* __restrict__ wgp,
                                                    const u16* __restrict__ wup, const u16* __restrict__ wdp,
                                                    const u16* __restrict__ gw, char* ws, u16* out) {
    __shared__ __attribute__((aligned(16))) char smem[163840];
    const int w = __builtin_amdgcn_readfirstlane(threadIdx.x >> 6);
    if (K::TOPV) {
        asm volatile("s_mov_b64 exec, 0\n\ts_cmp_eq_u32 %0, 6\n\ts_cbranch_scc1 .Lpad_x\n\t"
                     "s_cmp_eq_u32 %0, 0\n\ts_cbranch_scc1 .Lpad_d\n\ts_cmp_eq_u32 %0, 1\n\ts_cbranch_scc1 .Lpad_ab0\n"
                     ".Lpad_top:\n\ts_mov_b64 exec, -1" :: "s"(w) : "scc", "memory");
    } else {
        asm volatile(".Lpad_top:");
    }
    asm volatile("" :: "s"(x), "s"(wqkv));
    const int bid = blockIdx.x, xcd = bid & 7, slot = bid >> 3;
    const int mtA = (xcd & 1) * 8 + (slot & 7), hpA = (xcd >> 1) * 4 + (slot >> 3);
    const int mtB = (xcd & 1) * 8 + ((xcd >> 1) & 1) * 4 + (slot & 3), hpB = (xcd >> 2) * 8 + (slot >> 2);
    const int rotA = 8 * (xcd & 1);
    const bool has_ffn = slot < 24 || (xcd & 2);
    const int fb = slot < 24 ? bid : 192 + (slot - 24) * 4 + (xcd >> 2) * 2 + (xcd & 1);
    const unsigned lds0 = (unsigned)(size_t)(AS3 char*)smem;
    const rsrc_t rsWS = mk_rsrc(ws, WS_BYTES);
    const rsrc_t rsX = mk_rsrc(x, 2048u * 2048u);

    auto touch3 = [&](unsigned& dst, int half, bool on = true) {
        const rsrc_t rsT = mk_rsrc(wqkv, (on && slot < 24 && !(xcd & 2)) ? 1024u * 6144u : 0u);
        const unsigned so = (unsigned)(((slot & 7) + 8 * half + rotA) & 15) * (64u * 6144u) + (unsigned)(slot >> 3) * 2048u + (unsigned)((xcd >> 1) * 4 + 3) * 128u;
        asm volatile("s_nop 4\n\tbuffer_load_dword %0, %1, %2, %3 offen" : "=v"(dst) : "v"(fresh_lane() * 6144), "s"(rsT), "s"(so) : "memory");
    };
    unsigned gate_t = 0;
    int gate_s = 0;
    auto hush = [&]() {
        if (gate_s == 0) {
            gate_t = __builtin_amdgcn_readfirstlane(*(AS3 volatile const unsigned*)(size_t)(lds0 + L_TM + 8)) - K::QA;
            gate_s = 1;
        }
        if (gate_s == 3 || (int)(now32() - gate_t) < 0) return;
        if (gate_s == 1) {
            wait4(lds0 + C_P, P_PUB);
            lds_wait(lds0 + C_TM, 1);
            gate_t = __builtin_amdgcn_readfirstlane(*(AS3 volatile const unsigned*)(size_t)(lds0 + L_TM)) - K::QB;
            gate_s = 2;
            if ((int)(now32() - gate_t) < 0) return;
        }
        lds_wait(lds0 + C_R, R_L1I);
        gate_s = 3;
    };

    unsigned ep, pf0;
    u32x4 xv[4][2], rv[2], gv[2], pf;
    auto ldi = [&]() {
            const int l = fresh_lane(), a = w & 1, row0 = mtB * 128 + hpB * 8 + 4 * a;
            const rsrc_t rsR = mk_rsrc(wr, 2048u), rsGW = mk_rsrc(gw, 2048u);
            const unsigned v16 = (unsigned)l * 16u;
            asm volatile("s_nop 4\n\tbuffer_load_dword %0, off, %1, 0 sc1" : "=v"(ep) : "s"(rsWS) : "memory");
#pragma unroll
            for (int r = 0; r < 4; ++r) {
                asm volatile("buffer_load_dwordx4 %0, %1, %2, %3 offen" : "=v"(xv[r][0]) : "v"(v16), "s"(rsX), "s"((unsigned)(row0 + r) * 2048u) : "memory");
                asm volatile("buffer_load_dwordx4 %0, %1, %2, %3 offen offset:1024" : "=v"(xv[r][1]) : "v"(v16), "s"(rsX), "s"((unsigned)(row0 + r) * 2048u) : "memory");
            }
            asm volatile("buffer_load_dwordx4 %0, %1, %2, 0 offen" : "=v"(rv[0]) : "v"(v16), "s"(rsR) : "memory");
            asm volatile("buffer_load_dwordx4 %0, %1, %2, 0 offen offset:1024" : "=v"(rv[1]) : "v"(v16), "s"(rsR) : "memory");
            asm volatile("buffer_load_dwordx4 %0, %1, %2, 0 offen" : "=v"(gv[0]) : "v"(v16), "s"(rsGW) : "memory");
            asm volatile("buffer_load_dwordx4 %0, %1, %2, 0 offen offset:1024" : "=v"(gv[1]) : "v"(v16), "s"(rsGW) : "memory");
            {
                const rsrc_t rsP = mk_rsrc(wphi, 32u * 2048u);
                const unsigned vp = (unsigned)(l ^ (((l >> 4) & 1) << 1)) * 16u;
#pragma unroll
                for (int i = 0; i < 2; ++i) dma16(rsP, vp, (unsigned)hpA * 4096u + a * 2048u + i * 1024u, lds0 + L_WP + a * 2048 + i * 1024);
            }
            CODE_FETCH(pf, slot * 2 + a);
            touch3(pf0, 0, a == 0);
    };
    if (__builtin_expect((w >> 1) == 2, 1)) {
        const int d = w - 4;
        LANE_VARS;
        const int srow = l >> 3, pos = l & 7;
        const rsrc_t rsW = mk_rsrc(wqkv, 1024u * 6144u);
        const unsigned vx0 = srow * 2048 + ((pos ^ ((srow >> 1) & 7)) << 4);
        const unsigned vw0 = srow * 6144 + (((((pos >> 1) ^ (srow & 3)) << 1) | (pos & 1)) << 4);
        unsigned vxs[4], vws[4];
#pragma unroll
        for (int i = 0; i < 4; ++i) {
            vxs[i] = (vx0 ^ ((i & 1) * 64u)) + i * (16384u - 1024u);
            vws[i] = (vw0 ^ ((i & 1) * 32u)) + i * (8u * 6144u - 1024u);
        }
        const unsigned sx0 = (unsigned)(mtA * 128) * 2048u, sw0 = (unsigned)hpA * 128u;
        const int lq = ((slot >> 3) - slot) & 3;
        const unsigned ktab = lq == 0 ? K::KT0 : lq == 1 ? K::KT1 : lq == 2 ? K::KT2 : K::KT3;
        auto chunk = [&](int kt) { return (((ktab >> (4 * (kt >> 1))) & 7u) * 2u + (unsigned)((kt & 1) + rotA)) & 15u; };
        auto dx = [&](unsigned kk, int i, unsigned la) { dma4<K::PACE>(rsX, vxs, sx0 + kk * 128u + i * 65536u, lds0 + la + i * 4096); };
        auto dw = [&](unsigned kk, int p, int i, unsigned la) { dma4<K::PACE>(rsW, vws, sw0 + p * 2048u + (kk * 64u + i * 32u) * 6144u, lds0 + la + i * 4096); };
        asm volatile("s_nop 4" ::: "memory");
        unsigned lb = 0;
#pragma unroll 1
        for (int kt = 0; kt < 13; ++kt) {
            if (kt >= 2) {
                if (kt <= 10) wait_vm<20>(); else wait_vm<16>();
                bar();
            }
            const unsigned kk = chunk(kt);
            dx(kk, 2 * d, lb);
            dw(kk, 1 + d, 0, lb + 24576 + d * 8192);
            dx(kk, 2 * d + 1, lb);
            dw(kk, 1 + d, 1, lb + 24576 + d * 8192);
            if (kt < 10) dw(kk, 0, d, lb + 16384);
            lb = lb == 2 * SLOT ? 0u : lb + SLOT;
        }
        auto dxs = [&](int i) {
            const unsigned kk = chunk(13 + i), lx = d ? XS1[i] - 8192u : XS0[i];
            dx(kk, 2 * d, lx);
            dx(kk, 2 * d + 1, lx);
        };
        auto dws = [&](int i) {
            const unsigned kk = chunk(13 + i), lw = ((1 + i) % 3) * SLOT + 24576 + d * 8192;
            dw(kk, 1 + d, 0, lw);
            dw(kk, 1 + d, 1, lw);
        };
        auto dq = [&](int i) {
            const unsigned kk = chunk(i < 2 ? 14 + i : 8 + i);
            dw(kk, 0, 0, QW[i]);
            dw(kk, 0, 1, QW[i]);
        };
        if (K::XAH) dxs(0);
        wait_vm<K::XAH ? 24 : 16>();
        bar();
        dws(0);
        dxs(K::XAH ? 1 : 0);
        wait_vm<K::XAH ? 24 : 16>();
        bar();
        dws(1);
        dxs(K::XAH ? 2 : 1);
        wait_vm<K::XAH ? 24 : 16>();
        bar();
        dws(2);
        if (!K::XAH) dxs(2);
        if (d == 0) {
            dq(0);
            wait_vm<24>();
        } else {
            wait_vm<16>();
        }
        bar();
        if (d == 0) wait_vm<8>(); else wait_vm<0>();
        post(lds0 + C_ST + d * 4, 1);
        __builtin_amdgcn_s_setprio(3);
        if (d == 0) {
            const rsrc_t rsO = mk_rsrc(wo, 1024u * 2048u);
            unsigned vos[4];
#pragma unroll
            for (int i = 0; i < 4; ++i) vos[i] = WO_VOFF + i * (16384u - 1024u);
            unsigned sb[2] = {0u, 0u};
            auto wo_run = [&](int b0, int b1) {
#pragma unroll
                for (int b = b0; b < b1; ++b) {
                    unsigned so;
                    asm volatile("s_mov_b32 m0, %7\n\ts_add_u32 %0, %6, %8\n\tbuffer_load_dwordx4 %1, %5, %0 offen lds\n\tbuffer_load_dwordx4 %2, %5, %0 offen offset:1024 lds\n\t"
                                 "buffer_load_dwordx4 %3, %5, %0 offen offset:2048 lds\n\tbuffer_load_dwordx4 %4, %5, %0 offen offset:3072 lds\n\ts_waitcnt vmcnt(%9)"
                                 : "=&s"(so) : "v"(vos[0]), "v"(vos[1]), "v"(vos[2]), "v"(vos[3]), "s"(rsO), "s"(sb[b >> 4]), "i"(b * 4096), "i"((b & 15) * 65536), "i"(K::WO_D)
                                 : "memory", "scc");
                }
            };
            dq(1);
            dq(2);
            wait4(lds0 + C_P, P_E15);
            dq(3);
            dq(4);
            wait_vm<0>();
            post(lds0 + C_Q, 1);
            wait4(lds0 + C_P, P_KVD);
            dq(5);
            wait_vm<0>();
            post(lds0 + C_Q, 2);
            wait4(lds0 + C_P, P_PUB);
            {
                const unsigned s0 = (unsigned)(mtB * 128) * 2048u + (unsigned)hpB * 128u;
#pragma unroll
                for (int i = 0; i < 4; ++i) dma4<0>(rsX, vxs, s0 + i * 65536u, lds0 + L_XE + i * 4096);
            }
            lds_wait(lds0 + C_TM, 1);
            sb[1] = (__builtin_amdgcn_readfirstlane(*(AS3 volatile const unsigned*)(size_t)(lds0 + L_TM + 4)) & 1u) * (16u * 65536u) + (unsigned)hpB * 128u;
            sb[0] = sb[1] ^ (16u * 65536u);
            asm volatile("s_nop 4" : "+s"(sb[0]), "+s"(sb[1]));
            wo_run(26, 32);
            post(lds0 + C_XE, 1);
            wait4(lds0 + C_P, P_Q1 + 2);
            wo_run(16, 24);
            wait4(lds0 + C_P, P_QRD);
            wo_run(24, 26);
            wo_run(0, 6);
            post(lds0 + C_WO, 2);
            wo_run(6, 14);
            post(lds0 + C_WO, 3);
            wo_run(14, 16);
            wait_vm<0>();
            post(lds0 + C_WO, 4);
        } else {
            const unsigned v16 = (unsigned)l * 16u;
            const unsigned tag = *(AS3 const unsigned*)(size_t)(lds0 + L_TAG);
            const rsrc_t rsB = mk_rsrc(bulk_base(ws), WB_BYTES);
            lds_wait(lds0 + C_IDX, 1);
            const u32x2 pl = *(AS3 const u32x2*)(size_t)(lds0 + L_PLC);
            const unsigned okp = __builtin_amdgcn_readfirstlane(pl[0]), osm = __builtin_amdgcn_readfirstlane(pl[1]);
            unsigned t1 = 0;
            const unsigned base = now32() - 4096u;
            u32x4 p0, p1;
            CODE_FETCH(p0, slot * 2);
            CODE_FETCH(p1, slot * 2 + 1);
            const int ch = l & 3;
            unsigned mk01 = ch < 2 ? ~0u : 0u, mk0 = ch == 0 ? ~0u : 0u, mk1 = ch == 1 ? ~0u : 0u, mk2 = ch == 2 ? ~0u : 0u, mkl = (l & 31) < 2 ? ~0u : 0u;
            unsigned vkf = (unsigned)((g >> 1) * 16 + c) * 16u + (unsigned)(g & 1) * 8u, vfm = l < 2 ? (unsigned)l * 8u : l == 32 ? 16u : 0x80000000u;
            unsigned kwa = lds0 + L_KVF + l * 16, ra = lds0 + C_R;
            asm volatile("" : "+v"(mk01), "+v"(mk0), "+v"(mk1), "+v"(mk2), "+v"(mkl), "+v"(vkf), "+v"(vfm), "+v"(kwa), "+v"(ra));
            bf16x8 sel;
#pragma unroll
            for (int j = 0; j < 8; ++j) sel[j] = ((c & 7) == j && (c >> 3) == (g & 1)) ? (short)0x3f80 : (short)0;
            asm volatile("" : "+v"(sel));
            {
                unsigned ta, tb;
                auto latest = [&](unsigned off, bool& all) {
                    const u32x4 pa = ld128c(rsWS, v16, off), pb = ld128c(rsWS, v16 + 1024u, off);
                    const unsigned ma = max(pa[1] == tag ? min(pa[0] - base, 8192u) : 0u, pa[3] == tag ? min(pa[2] - base, 8192u) : 0u);
                    const unsigned mb = max(pb[1] == tag ? min(pb[0] - base, 8192u) : 0u, pb[3] == tag ? min(pb[2] - base, 8192u) : 0u);
                    all = __all(pa[1] == tag && pa[3] == tag && pb[1] == tag && pb[3] == tag);
                    ta = __builtin_amdgcn_readfirstlane(wave_umax(ma));
                    tb = __builtin_amdgcn_readfirstlane(wave_umax(mb));
                    const unsigned av = __builtin_amdgcn_readfirstlane(wave_usum(min(pa[0] - base, 8192u) + min(pa[2] - base, 8192u) + min(pb[0] - base, 8192u) + min(pb[2] - base, 8192u))) >> 8;
                    return base + max(max(ta, tb), all ? av + K::SLS : 0u);
                };
                bool all;
                wait4(lds0 + C_P, has_ffn ? K::TMW : P_PUB);
#pragma unroll 1
                for (int it = 0; it < 4; ++it) {
                    t1 = latest(WS_PT, all) + K::D1;
                    if (all) break;
                    wait4(lds0 + C_P, P_PUB);
                    __builtin_amdgcn_s_sleep(K::PT_S);
                }
                asm volatile("s_waitcnt vmcnt(0)" : "+v"(p0), "+v"(p1) :: "memory");
                post(lds0 + L_TM, t1);
                const unsigned t3 = t1 + (K::D2 + K::D3);
                post(lds0 + L_TM + 4, (t3 & ~1u) | (tb < ta ? 1u : 0u));
                post(lds0 + C_TM, 1);
                asm volatile("s_branch .Lpad_r\n\t.p2align 12\n.Lpad_r:\n\ts_cbranch_execz .Lpad_ret" ::: "memory");
                wait4(lds0 + C_P, P_PUB);
            }
            bool late = false;
#pragma unroll 1
            for (int it = 0; it < TRY_LIMIT; ++it) {
                unsigned bad;
                asm volatile(RED1_ASM : [st] "=&s"(bad)
                             : [rsb] "s"(rsB), [t1] "s"(t1), [s0] "s"(okp ^ ((unsigned)mtA * 16384u)), [sm] "s"(osm + (unsigned)((mtA >> 3) * 16384 + (mtA & 7) * 1024)),
                               [sf] "s"(osm + 32768u + (unsigned)mtA * 512u), [sg] "s"(osm + 49152u + (unsigned)mtA * 64u), [hi] "s"(0xffffffff00000000ull),
                               [v16] "v"(v16), [ra] "v"(ra), [tag] "v"(tag), [mk01] "v"(mk01), [mk2] "v"(mk2), [mkl] "v"(mkl), [sel] "v"(sel), [vkf] "v"(vkf), [vfm] "v"(vfm)
                             : "memory", "vcc", "scc", RED1_CLOB);
                if (__builtin_expect(!bad, 1)) break;
                late = true;
                if (it < K::RE1) continue;
                int i2 = 0;
#pragma unroll 1
                for (; i2 < POLL_LIMIT; ++i2) {
                    __builtin_amdgcn_s_sleep(K::RETRY_S);
                    fence_c();
                    if (__all(ld32c(rsB, (unsigned)c * 64u + 36u, osm + (unsigned)((mtA >> 3) * 16384 + (mtA & 7) * 1024)) == tag)) break;
                }
                if (i2 == POLL_LIMIT) break;
            }
            post(lds0 + C_R, R_L1);
            wait4(lds0 + C_P, P_Q1);
            if (late) __builtin_amdgcn_s_sleep(K::L2_S);
            unsigned pv = R_L2I;
#pragma unroll 1
            for (int it = 0; it < TRY_LIMIT; ++it) {
                unsigned bad;
                asm volatile(RED2_ASM : [st] "=&s"(bad), [pv] "+v"(pv)
                             : [rsb] "s"(rsB), [t2] "s"(t1 + K::D2), [s0] "s"(osm + 32768u), [frc] "s"(K::FORCE_REDO && it == 0 ? 1 : 0),
                               [v16] "v"(v16), [ra] "v"(ra), [kwa] "v"(kwa), [tag] "v"(tag), [mk0] "v"(mk0), [mk1] "v"(mk1)
                             : "memory", "vcc", "scc", RED1_CLOB);
                if (__builtin_expect(!bad, 1)) break;
                if (it < K::RE1) continue;
                int i2 = 0;
#pragma unroll 1
                for (; i2 < POLL_LIMIT; ++i2) {
                    __builtin_amdgcn_s_sleep(K::RETRY_S);
                    fence_c();
                    if (__all(ld32c(rsB, (unsigned)c * 64u + 20u, osm + 49152u) == tag)) break;
                }
                if (i2 == POLL_LIMIT) break;
            }
            post(ra, pv + 1u);
            asm volatile(".Lff_end1:");
        }
        return;
    }

    asm volatile(".Lpad_d:\n\ts_cbranch_execz .Lpad_top" ::: "memory");
    __builtin_amdgcn_sched_barrier(0);
    if (w >= 6) {
        const int a = w - 6;
        const int cb = fb >> 2, kq = fb & 3;
        u32x4 wg_[16], wu_[16], wd_[16];
        unsigned tag;
        int idx;
        u32x4 xs[2];
        {
        LANE_VARS;
        const int srow = l >> 3, pos = l & 7;
        const unsigned nb = has_ffn ? 1024u * 7168u : 0u;
        const rsrc_t rsG = mk_rsrc(wgp, nb), rsU = mk_rsrc(wup, nb), rsD = mk_rsrc(wdp, nb);
        const unsigned vf = (unsigned)(16 * srow) * 7168u + (unsigned)pos * 16u;
        const int k8 = fb & 7, cg = (fb >> 4) * 4 + ((fb >> 3) & 1) * 2 + a, cu = cg ^ 2;
        const unsigned sg = (unsigned)(k8 * 128) * 7168u + (unsigned)cg * 128u, su = sg ^ 256u;
        auto ldg = [&]() {
#pragma unroll
            for (int i = 0; i < 16; ++i) {
                asm volatile("buffer_load_dwordx4 %0, %1, %2, %3 offen nt" : "=v"(wg_[i]) : "v"(vf), "s"(rsG), "s"(sg + i * 7168u) : "memory");
            }
        };
        auto ldu = [&]() {
#pragma unroll
            for (int i = 0; i < 16; ++i) {
                asm volatile("buffer_load_dwordx4 %0, %1, %2, %3 offen nt" : "=v"(wu_[i]) : "v"(vf), "s"(rsU), "s"(su + i * 7168u) : "memory");
            }
        };
        auto ldd = [&](int i0, int i1) {
            const unsigned vd = (unsigned)g * (16u * 2048u) + (unsigned)c * 16u;
            const unsigned sd = (unsigned)(cb * 64) * 2048u + (unsigned)(kq * 512 + a * 256);
#pragma unroll
            for (int i = i0; i < i1; ++i) {
                if ((i & 3) == 0) { wait_vm<K::FD>(); hush(); }
                asm volatile("buffer_load_dwordx4 %0, %1, %2, %3 offen nt" : "=v"(wd_[i]) : "v"(vd), "s"(rsD), "s"(sd + i * 2048u) : "memory");
            }
        };
        const int row0 = mtB * 128 + hpB * 8 + 4 * a;
        if (a == 0) {
            *(AS3 unsigned*)(size_t)(lds0 + L_SM + l * 4) = 0u;
            wait_lgkm0();
        }
        int b = 0;
        unsigned tl = 0, pmin = 255;
        auto tick = [&]() {
            bar();
            if (a == 1) {
                const unsigned t = now32();
                if (b > 2 && b <= 16 - K::ND) pmin = min(pmin, t - tl);
                tl = t;
            }
            ++b;
        };
        auto upto = [&](int e) {
#pragma unroll 1
            while (b <= e) tick();
        };
        auto pads = [&]() {
#pragma unroll 1
            for (int i = a; i < 6; i += 2) {
                unsigned long long ex;
                asm volatile("s_mov_b64 %0, exec\n\ts_mov_b64 exec, 0\n\t"
                             "s_cmp_eq_u32 %1, 0\n\ts_cbranch_scc1 .Lpad_ab1\n\ts_cmp_eq_u32 %1, 1\n\ts_cbranch_scc1 .Lpad_ab2\n\t"
                             "s_cmp_eq_u32 %1, 2\n\ts_cbranch_scc1 .Lpad_c0\n\ts_cmp_eq_u32 %1, 3\n\ts_cbranch_scc1 .Lpad_c1\n\t"
                             "s_cmp_eq_u32 %1, 4\n\ts_cbranch_scc1 .Lpad_r\n"
                             ".Lpad_ret:\n\ts_mov_b64 exec, %0" : "=&s"(ex) : "s"(i) : "scc", "memory");
                if (i < 4 && b <= 9) tick();
            }
        };
        auto nap = [&](int n) {
#pragma unroll 1
            for (int i = 0; i < n; ++i) __builtin_amdgcn_s_sleep(1);
        };
        const int lq4 = ((((slot >> 3) - slot) & 3) + (slot & 4)) * 4;
        const int bg = ((unsigned)K::BGT >> lq4) & 15, bu = ((unsigned)K::BUT >> lq4) & 15;
        unsigned pf3 = 0;
        ldi();
        u32x2 gr;
        unsigned vgr;
        auto router = [&](auto n_) {
            tick();
            asm volatile("s_waitcnt vmcnt(%15)"
                         : "+v"(ep), "+v"(xv[0][0]), "+v"(xv[0][1]), "+v"(xv[1][0]), "+v"(xv[1][1]), "+v"(xv[2][0]), "+v"(xv[2][1]), "+v"(xv[3][0]),
                           "+v"(xv[3][1]), "+v"(rv[0]), "+v"(rv[1]), "+v"(gv[0]), "+v"(gv[1]), "+v"(pf), "+v"(pf0)
                         : "i"(decltype(n_)::value) : "memory");
            tag = __builtin_amdgcn_readfirstlane(ep) * 2654435761u + 0x9e3779b9u;
            float ra[4], ga[4];
#pragma unroll
            for (int r = 0; r < 4; ++r) {
                float racc = 0.f, gacc = 0.f;
#pragma unroll
                for (int i = 0; i < 2; ++i)
#pragma unroll
                    for (int j = 0; j < 4; ++j) {
                        racc = dot2(xv[r][i][j], rv[i][j], racc);
                        gacc = dot2(xv[r][i][j], gv[i][j], gacc);
                    }
                ra[r] = racc;
                ga[r] = gacc;
                asm volatile("" : "+v"(ra[r]), "+v"(ga[r]));
            }
            tick();
            float r2[2], g2[2];
#pragma unroll
            for (int i = 0; i < 2; ++i) {
                auto p = __builtin_amdgcn_permlane32_swap(FU(ra[2 * i]), FU(ra[2 * i + 1]), false, false);
                r2[i] = UF(p[0]) + UF(p[1]);
                auto q = __builtin_amdgcn_permlane32_swap(FU(ga[2 * i]), FU(ga[2 * i + 1]), false, false);
                g2[i] = UF(q[0]) + UF(q[1]);
            }
            auto p = __builtin_amdgcn_permlane16_swap(FU(r2[0]), FU(r2[1]), false, false);
            auto q = __builtin_amdgcn_permlane16_swap(FU(g2[0]), FU(g2[1]), false, false);
            float rs = row16_sum(UF(p[0]) + UF(p[1])), gs = row16_sum(UF(q[0]) + UF(q[1]));
            asm volatile("" : "+v"(rs), "+v"(gs));
            const int row = row0 + (g >> 1) + 2 * (g & 1);
            const unsigned rbits = f2bf(rs);
            const unsigned okey = (rbits & 0x8000u) ? (~rbits & 0xffffu) : (rbits | 0x8000u);
            if (c == 0) *(AS3 unsigned*)(size_t)(lds0 + L_KEY + (4 * a + g) * 4) = (okey << 16) | (unsigned)(0xffff - row);
            if (l == 0 && a == 0) *(AS3 unsigned*)(size_t)(lds0 + L_TAG) = tag;
            gr = u32x2{FU(rbf(1.f / (1.f + __expf(-rbf(gs))))), tag};
            vgr = c == 0 ? WS_G + (unsigned)row * 8u : 0x80000000u;
            asm volatile("" : "+v"(gr), "+v"(vgr));
            wait_lgkm0();
            tick();
            if (a == 0) {
                unsigned k8 = *(AS3 const unsigned*)(size_t)(lds0 + L_KEY + (l & 7) * 4);
                k8 = max(k8, (unsigned)__shfl_xor((int)k8, 1));
                k8 = max(k8, (unsigned)__shfl_xor((int)k8, 2));
                k8 = max(k8, (unsigned)__shfl_xor((int)k8, 4));
                const u32x4 pv = *(AS3 const u32x4*)(size_t)(lds0 + L_PRB);
                const unsigned lo = min(min(pv[0] & 0xffffu, pv[2] & 0xffffu), min(pv[1] >> 16, pv[3] >> 16));
                const unsigned up = min(min(pv[0] >> 16, pv[2] >> 16), min(pv[1] & 0xffffu, pv[3] & 0xffffu));
                st64c(u32x2{k8 ^ (up < lo ? 0x2000u : 0u), tag}, rsWS, l ? 0x80000000u : WS_RG + (unsigned)bid * 8u, 0);
            }
        };
        u32x4 ga, gb;
        const unsigned vr = WS_RG + (unsigned)l * 16u;
        auto ldrg = [&]() {
            asm volatile("buffer_load_dwordx4 %0, %1, %2, 0 offen sc1" : "=v"(ga) : "v"(vr), "s"(rsWS) : "memory");
            asm volatile("buffer_load_dwordx4 %0, %1, %2, 0 offen offset:1024 sc1" : "=v"(gb) : "v"(vr), "s"(rsWS) : "memory");
        };
        router(std::integral_constant<int, 0>{});
        __builtin_amdgcn_s_setprio(2);
        upto(bg);
        nap(K::SLG);
        if (a == 0) {
            touch3(pf3, 1);
        } else {
            const rsrc_t rsT = mk_rsrc(wqkv, 1024u * 6144u);
            asm volatile("s_nop 4\n\tbuffer_load_dword %0, %1, %2, %3 offen" : "=v"(pf3) : "v"((unsigned)(l & 31) * 6144u), "s"(rsT),
                         "s"((unsigned)(((12 + rotA) & 15) * 64 + (slot & 7) * 32) * 6144u + (unsigned)hpA * 128u) : "memory");
        }
        ldg();
        upto(bu);
        if (bu != bg) nap(K::SLG);
        ldu();
        upto(((unsigned)K::BRT >> lq4) & 15);
        ldrg();
        pads();
        upto(9);
        asm volatile("s_waitcnt vmcnt(0)"
                     : "+v"(wg_[0]), "+v"(wg_[1]), "+v"(wg_[2]), "+v"(wg_[3]), "+v"(wg_[4]), "+v"(wg_[5]), "+v"(wg_[6]), "+v"(wg_[7]),
                       "+v"(wg_[8]), "+v"(wg_[9]), "+v"(wg_[10]), "+v"(wg_[11]), "+v"(wg_[12]), "+v"(wg_[13]), "+v"(wg_[14]), "+v"(wg_[15]),
                       "+v"(ga), "+v"(gb), "+v"(pf3)
                     :: "memory");
        asm volatile(""
                     : "+v"(wu_[0]), "+v"(wu_[1]), "+v"(wu_[2]), "+v"(wu_[3]), "+v"(wu_[4]), "+v"(wu_[5]), "+v"(wu_[6]), "+v"(wu_[7]),
                       "+v"(wu_[8]), "+v"(wu_[9]), "+v"(wu_[10]), "+v"(wu_[11]), "+v"(wu_[12]), "+v"(wu_[13]), "+v"(wu_[14]), "+v"(wu_[15])
                     :: "memory");
#pragma unroll 1
        for (int it = 0; it < POLL_LIMIT; ++it) {
            if (__all((ga[1] == tag) && (ga[3] == tag) && (gb[1] == tag) && (gb[3] == tag))) break;
            __builtin_amdgcn_s_sleep(4 * K::RETRY_S);
            fence_c();
            ga = ld128c(rsWS, vr, 0);
            gb = ld128c(rsWS, vr + 1024u, 0);
        }
        const unsigned key = __builtin_amdgcn_readfirstlane(wave_umax(max(max(ga[0] | 0x3800u, ga[2] | 0x3800u), max(gb[0] | 0x3800u, gb[2] | 0x3800u))));
        idx = (0xffff - (int)(key & 0xffffu)) & 2047;
        unsigned tw[2];
        auto twf = [&]() {
            const rsrc_t rsO = mk_rsrc(wo, 1024u * 2048u);
            const unsigned vt = (unsigned)srow * 2048u + (unsigned)pos * 128u, st = (unsigned)(slot * 32 + a * 16) * 2048u + (unsigned)(xcd >> 2) * 1024u;
            nap(K::X1);
            if (K::TNT) {
            asm volatile("s_nop 4\n\tbuffer_load_dword %0, %1, %2, %3 offen nt" : "=v"(tw[0]) : "v"(vt), "s"(rsO), "s"(st) : "memory");
            asm volatile("buffer_load_dword %0, %1, %2, %3 offen nt" : "=v"(tw[1]) : "v"(vt), "s"(rsO), "s"(st + 16384u) : "memory");
            } else {
            asm volatile("s_nop 4\n\tbuffer_load_dword %0, %1, %2, %3 offen" : "=v"(tw[0]) : "v"(vt), "s"(rsO), "s"(st) : "memory");
            asm volatile("buffer_load_dword %0, %1, %2, %3 offen" : "=v"(tw[1]) : "v"(vt), "s"(rsO), "s"(st + 16384u) : "memory");
            }
        };
        tick();
        if (K::TWB == 10) twf();
        {
            const unsigned vs = (unsigned)srow * 32u, ss = (unsigned)idx * 2048u + (unsigned)(k8 * 256);
            asm volatile("s_nop 4\n\tbuffer_load_dwordx4 %0, %1, %2, %3 offen" : "=v"(xs[0]) : "v"(vs), "s"(rsX), "s"(ss) : "memory");
            asm volatile("buffer_load_dwordx4 %0, %1, %2, %3 offen offset:16" : "=v"(xs[1]) : "v"(vs), "s"(rsX), "s"(ss) : "memory");
        }
        if (a == 1) {
            const u32x2 pt = {tl + (pmin * (K::PRED_K + 12) >> 2) + K::PRED_C + K::XE, tag};
            st64c(pt, rsWS, l ? 0x80000000u : WS_PT + (unsigned)(hpA * 16 + mtA) * 8u, 0);
        } else {
            const unsigned kb = key >> 16;
            const unsigned rb_ = (kb & 0x8000u) ? (kb & 0x7fffu) : (~kb & 0xffffu);
            const float rsel = rbf(1.f / (1.f + __expf(-UF(rb_ << 16))));
            const u32x2 iv = {(unsigned)idx, FU(rsel)};
            *(AS3 u32x2*)(size_t)(lds0 + L_IDX) = iv;
            const unsigned nv = (~ga[0] & 0x3800u) + (~ga[2] & 0x3800u) + (~gb[0] & 0x3800u) + (~gb[2] & 0x3800u);
            const unsigned votes = wave_usum((l & 3) == (xcd >> 1) ? nv >> 11 : 0u);
            const unsigned o0 = (unsigned)(xcd >> 1) * (2u << 20) + (K::X2 < 2 ? K::X2 * 8192u : votes > 128u ? 8192u : 0u);
            const u32x4 pl = {o0 + WB_KVP + (((unsigned)(hpA & 3) * 8u) ^ (tag & 31u)) * 32768u, o0 + WB_SM + (((unsigned)hpA & 3u) ^ ((tag >> 5) & 7u)) * 65536u,
                              ((tag >> 8) & 63u) * 2048u, 0u};
            *(AS3 u32x4*)(size_t)(lds0 + L_PLC) = pl;
            post(lds0 + C_IDX, 1);
            wait_lgkm0();
        }
        unsigned mk[4];
#pragma unroll
        for (int k = 0; k < 4; ++k) mk[k] = (pos >> 1) == k ? ((pos & 1) ? 0xffff0000u : 0x0000ffffu) : 0u;
        f32x4 sg4 = {0.f, 0.f, 0.f, 0.f}, su4 = sg4;
        auto dot = [&](int i0, int i1) {
#pragma unroll
            for (int i = i0; i < i1; ++i) {
                const unsigned xw = xs[i >> 3][(i >> 1) & 3];
                const unsigned xd = __builtin_amdgcn_perm(xw, xw, (i & 1) ? 0x03020302u : 0x01000100u);
                const u32x4 am = {xd & mk[0], xd & mk[1], xd & mk[2], xd & mk[3]};
                sg4 = mfma16(__builtin_bit_cast(bf16x8, am), __builtin_bit_cast(bf16x8, wg_[i]), sg4);
                su4 = mfma16(__builtin_bit_cast(bf16x8, am), __builtin_bit_cast(bf16x8, wu_[i]), su4);
            }
            asm volatile("" : "+v"(sg4), "+v"(su4));
        };
        tick();
        asm volatile("s_waitcnt vmcnt(0)" : "+v"(xs[0]), "+v"(xs[1]) :: "memory");
        if (K::TWB == 11) twf();
        dot(0, K::DS1);
        tick();
        dot(K::DS1, K::DS2);
        if (K::TWB == 12) twf();
        tick();
        if (a == 1) {
            post(lds0 + L_TM + 8, tl + (pmin * (K::PRED_K + K::PK13) >> 2) + K::PRED_C);
            wait_lgkm0();
        }
        dot(K::DS2, K::DS3);
        if (K::TWB == 13) twf();
        __builtin_amdgcn_s_setprio(3);
        tick();
        ldd(0, 8);
        dot(K::DS3, 16);
        {
            unsigned fo[4];
#pragma unroll
            for (int r = 0; r < 4; ++r) {
                auto p = __builtin_amdgcn_permlane32_swap(FU(sg4[r]), FU(su4[r]), false, false);
                fo[r] = FU(UF(p[0]) + UF(__builtin_amdgcn_update_dpp(0, (int)p[1], 0x128, 0xf, 0xf, false)));
            }
            const unsigned vf0 = ((l & 8) || !has_ffn) ? 0x80000000u : WS_FG + (unsigned)(((l >> 5) ? cu : cg) * 8 + k8) * 1024u + (unsigned)((l >> 5) * 512 + (pos * 8 + (g & 1) * 4) * 8);
            const u32x4 s0 = {fo[0], tag, fo[1], tag}, s1 = {fo[2], tag, fo[3], tag};
            st128c(s0, rsWS, vf0, 0);
            st128c(s1, rsWS, vf0 + 16u, 0);
            st64c(gr, rsWS, vgr, 0);
        }
        if (bid == 0 && a == 0 && l == 0) st32c(tag, rsWS, WS_EPOCH, 0);
        ldd(8, 16);
        lds_wait(lds0 + C_TM, 1);
        const unsigned tm0 = *(AS3 const unsigned*)(size_t)(lds0 + L_TM);
        const unsigned vq = (unsigned)((l & 8) * 64 + (g * 8 + (l & 7)) * 16);
        u32x4 fq[8];
        unsigned ds[4];
#pragma unroll
        for (int k = 0; k < 4; ++k) ds[k] = (((l & 8) ? 0x03020302u : 0x01000100u) & mk[k]) | (0x0c0c0c0cu & ~mk[k]);
        asm volatile("" : "+v"(ds[0]), "+v"(ds[1]), "+v"(ds[2]), "+v"(ds[3]));
        if (has_ffn) {
            wait_until(__builtin_amdgcn_readfirstlane(tm0) + K::FG_AT);
#pragma unroll 1
            for (int it = 0; it < POLL_LIMIT; ++it) {
                unsigned bad = 0;
#pragma unroll
                for (int i = 0; i < 8; ++i) fq[i] = ld128c(rsWS, vq + (i & 3) * 1024u, WS_FG + (unsigned)cb * 8192u + (i >> 2) * 4096u);
#pragma unroll
                for (int i = 0; i < 8; ++i) bad |= (fq[i][1] ^ tag) | (fq[i][3] ^ tag);
                if (__all(bad == 0u)) break;
                __builtin_amdgcn_s_sleep(2 * K::RETRY_S);
                fence_c();
            }
        }
        asm volatile("s_waitcnt vmcnt(0)"
                     : "+v"(wd_[0]), "+v"(wd_[1]), "+v"(wd_[2]), "+v"(wd_[3]), "+v"(wd_[4]), "+v"(wd_[5]), "+v"(wd_[6]), "+v"(wd_[7]),
                       "+v"(wd_[8]), "+v"(wd_[9]), "+v"(wd_[10]), "+v"(wd_[11]), "+v"(wd_[12]), "+v"(wd_[13]), "+v"(wd_[14]), "+v"(wd_[15]),
                       "+v"(tw[0]), "+v"(tw[1])
                     :: "memory");
        if (has_ffn) {
            if (K::DWP) lds_wait(lds0 + C_P + (2 + a) * 4, P_PUB);
            __builtin_amdgcn_s_setprio(K::HP);
            float hv[2];
#pragma unroll
            for (int j = 0; j < 2; ++j) {
                const float gs = rbf(((UF(fq[0][2 * j]) + UF(fq[1][2 * j])) + (UF(fq[2][2 * j]) + UF(fq[3][2 * j]))) +
                                     ((UF(fq[4][2 * j]) + UF(fq[5][2 * j])) + (UF(fq[6][2 * j]) + UF(fq[7][2 * j]))));
                const float us = UF(__builtin_amdgcn_update_dpp(0, (int)FU(gs), 0x128, 0xf, 0xf, false));
                hv[j] = rbf(gs / (1.f + __expf(-gs))) * us;
            }
            const unsigned hp = pack2(hv[0], hv[1]);
            f32x4 ye = {0.f, 0.f, 0.f, 0.f}, yo = ye;
            auto dn = [&](auto mc) {
                constexpr int m = decltype(mc)::value;
                const unsigned hb = (unsigned)__builtin_amdgcn_update_dpp(0, (int)hp, 0x150 + m, 0xf, 0xf, false);
                const u32x4 am = {__builtin_amdgcn_perm(hb, hb, ds[0]), __builtin_amdgcn_perm(hb, hb, ds[1]), __builtin_amdgcn_perm(hb, hb, ds[2]), __builtin_amdgcn_perm(hb, hb, ds[3])};
                ye = mfma16(__builtin_bit_cast(bf16x8, am), __builtin_bit_cast(bf16x8, wd_[2 * m]), ye);
                yo = mfma16(__builtin_bit_cast(bf16x8, am), __builtin_bit_cast(bf16x8, wd_[2 * m + 1]), yo);
            };
            dn(std::integral_constant<int, 0>{}); dn(std::integral_constant<int, 1>{}); dn(std::integral_constant<int, 2>{}); dn(std::integral_constant<int, 3>{});
            dn(std::integral_constant<int, 4>{}); dn(std::integral_constant<int, 5>{}); dn(std::integral_constant<int, 6>{}); dn(std::integral_constant<int, 7>{});
            float y2[2];
#pragma unroll
            for (int j = 0; j < 2; ++j) {
                auto p = __builtin_amdgcn_permlane32_swap(FU(ye[j]), FU(ye[2 + j]), false, false);
                auto q = __builtin_amdgcn_permlane32_swap(FU(yo[j]), FU(yo[2 + j]), false, false);
                y2[j] = UF(p[0]) + UF(q[1]);
            }
            const u32x4 st = {FU(y2[0]), tag, FU(y2[1]), tag};
            st128c(st, rsWS, (unsigned)(c * 64 + (g & 1) * 32 + (g >> 1) * 16), WS_Y + (unsigned)cb * 8192u + (unsigned)(kq * 2048 + a * 1024));
        }
        {
            const unsigned col8 = (unsigned)(xcd * 128 + slot * 4 + a * 2) * 8u;
            const unsigned vy = l < 56 ? (unsigned)l * 8192u : 0x80000000u;
            u32x4 t;
            __builtin_amdgcn_s_setprio(3);
            wait_until(__builtin_amdgcn_readfirstlane(tm0) + K::Y_AT);
#pragma unroll 1
            for (int it = 0; it < POLL_LIMIT; ++it) {
                t = ld128c(rsWS, vy, WS_Y + col8);
                if (__all(l >= 56 || (t[1] == tag && t[3] == tag))) break;
                __builtin_amdgcn_s_sleep(4 * K::RETRY_S);
                fence_c();
            }
            auto p = __builtin_amdgcn_permlane32_swap(t[0], t[2], false, false);
            const float s = row16_sum(x16_sum(UF(p[0]) + UF(p[1])));
            st64c(u32x2{FU(s), tag}, rsWS, (l & 31) ? 0x80000000u : (unsigned)(l >> 2), WS_YF + col8);
        }
        if ((idx >> 7) == mtB && a == 0) {
            u32x4 t;
            wait_until(__builtin_amdgcn_readfirstlane(tm0) + K::Y_AT + K::X0);
#pragma unroll 1
            for (int it = 0; it < POLL_LIMIT; ++it) {
                t = ld128c(rsWS, (unsigned)(l & 31) * 16u, WS_YF + (unsigned)hpB * 512u);
                if (__all(t[1] == tag && t[3] == tag)) break;
                __builtin_amdgcn_s_sleep(2 * K::RETRY_S);
                fence_c();
            }
            lds_wait(lds0 + C_XE, 1);
            const int rr = idx & 127;
            const unsigned xx = *(AS3 const unsigned*)(size_t)(lds0 + L_XE + rr * 128 + ((((l & 31) >> 2) ^ ((rr >> 1) & 7)) << 4) + (l & 3) * 4);
            const float rsel = *(AS3 const float*)(size_t)(lds0 + L_IDX + 4);
            *(AS3 f32x2*)(size_t)(lds0 + L_Y + 256 + (l & 31) * 8) = f32x2{rbf(lo16(xx) + rbf(rbf(UF(t[0])) * rsel)), rbf(hi16(xx) + rbf(rbf(UF(t[2])) * rsel))};
            post(lds0 + C_YRDY, 1);
        }
        }
        asm volatile(".Lff_end0:");
        return;
    }

    const int v = w & 1, hd = w >> 1;
    const rsrc_t rsB = mk_rsrc(bulk_base(ws), WB_BYTES);
    unsigned tag, sof, t3, rid;
    unsigned long long rm;
    u32x16 ad;
    u32x4 g4;
    {
        const unsigned pi = (unsigned)((slot * 2 + (xcd & 1)) * 4 + w);
        const char* pa = bulk_base(ws) + ((unsigned)(xcd >> 1) * (2u << 20) + WB_PRB + (pi >> 5) * 16384u + (pi & 31u) * 256u + v * 8192u);
        const unsigned po = 8192u + v * 128u;
        unsigned j0, j1;
        asm volatile("s_load_dword %0, %2, 64\n\ts_load_dword %1, %2, %3" : "={s80}"(j0), "={s81}"(j1) : "s"(pa), "s"(po + 64u) : "memory");
        LANE_VARS;
        ad[0] = lds0 + (unsigned)(64 * v + c) * 128u + (unsigned)((g ^ ((c >> 1) & 7)) << 4);
        ad[1] = lds0 + 16384u + (WSW ^ (unsigned)(hd << 6));
        ad[2] = lds0 + L_VF + (unsigned)(hd * 2048 + v * 8192 + l * 16);
        ad[3] = lds0 + L_PHI + hd * 16384 + (unsigned)(64 * v + c) * 128u + (unsigned)((((c & 3) ^ ((c >> 2) & 1)) << 5) + ((g ^ (c >> 2)) << 3));
        ad[4] = lds0 + L_PHI + hd * 16384 + v * 8192 + ((PSW + (unsigned)((i3 ^ g) << 3)) ^ (unsigned)(v << 6));
        ad[5] = lds0 + L_WP + hd * 2048 + WPO;
        ad[6] = (unsigned)(l >> 5) * 16384u + (unsigned)(l & 31) * 16u;
        ad[7] = c < 3 ? (unsigned)(((g & 1) * 2 + (g >> 1)) * 1024 + c * 16) : 0x80000000u;
        ad[8] = lds0 + C_P + w * 4;
        ad[9] = lds0 + L_SM;
        ad[10] = c;
#pragma unroll
        for (int i = 11; i < 16; ++i) ad[i] = 0u;
        asm volatile(PHASE_AB_ASM
                     : "+{v[240:255]}"(ad), "={v[232:235]}"(g4), "={v236}"(tag), [sof] "=&s"(sof), [t3] "=&s"(t3), [rid] "=&s"(rid), [rm] "=&s"(rm), "+{s80}"(j0), "+{s81}"(j1)
                     : [rsb] "s"(rsB), [rsw] "s"(rsWS), [xs0] "s"(v ? XS1[0] - 8192u : XS0[0]), [xs1] "s"(v ? XS1[1] - 8192u : XS0[1]), [kpx] "s"(w * 65536),
                       [kpa] "s"(mtA * 512), [kma] "s"(hd * 16384 + v * 4096 + mtA * 64), [sg] "s"(WS_G + (unsigned)(mtB * 128 + 32 * w) * 8u),
                       [ofc] "s"(WB_OF + (unsigned)(mtB * 4 + w) * 65536u), [ofp] "s"(WB_OF + (unsigned)((mtA * 4 + 2 * v) * 32 + ((2 * hpA + hd + 1) & 31)) * 2048u),
                       [ogp] "s"(WB_OG + (unsigned)((mtA * 4 + 2 * v) * 32 + 2 * hpA + hd) * 8u), [kac] "s"(L_KVF + hd * 4096), [zac] "s"(L_ZF + hd * 512),
                       [xec] "s"(L_XE + w * 4096), [own] "s"(mtB * 4 + w), [prb] "s"(pa), [pro] "s"(po)
                     : "memory", "vcc", "scc", PHASE_AB_CLOB);
    }
    {
        const rsrc_t rsOut = mk_rsrc(out, 2048u * 2048u);
        const unsigned sog = WB_OG + sof + (unsigned)(mtB * 4 + w) * 256u;
        unsigned grp = (t3 & 1u) * 2u, flip = 0;
#pragma unroll 1
        for (int att = 0; att < TRY_LIMIT; ++att) {
            unsigned cs, st;
            asm volatile(PHASE_C_ASM
                         : "={v230}"(cs), [st] "=&s"(st)
                         : "{v[240:255]}"(ad), "{v[232:235]}"(g4), "{v236}"(tag), [rsb] "s"(rsB), [rso] "s"(rsOut), [sog] "s"(sog), [grp] "s"(grp), [flip] "s"(flip),
                           [t3] "s"(t3), [rid] "s"(rid), [rm] "s"(rm), [sout] "s"((unsigned)(mtB * 128 + 32 * w) * 2048u + (unsigned)hpB * 128u)
                         : "memory", "vcc", "scc", PHASE_C_CLOB);
            if (K::FORCE_REDO && att < 2) st = 2 - att, cs += 1u;
            if (__builtin_expect(st == 0u, 1)) break;
            u32x2 og = ld64c(rsB, ad[4], sog);
            const unsigned miss = (unsigned)__builtin_amdgcn_ballot_w64(og[1] != tag);
            if (st == 1u && ((miss && !(miss & ~(0xffffu << (8u * grp)))) || K::FORCE_REDO)) {
                grp ^= 2u;
                flip ^= 65536u;
                lds_wait(lds0 + C_WO, 4);
                continue;
            }
            int it = 0;
#pragma unroll 1
            for (; it < POLL_LIMIT && !__all(og[1] == tag); ++it) {
                __builtin_amdgcn_s_sleep(K::RETRY_S);
                fence_c();
                og = ld64c(rsB, ad[4], sog);
            }
            if (it == POLL_LIMIT) break;
            if (st == 2u) {
                if (__all(wave_usum(cs - (ad[10] < 32u ? og[0] : 0u)) == 0u)) break;
            }
        }
    }
    asm volatile(".Lff_end2:\n.Lpad_x:\n\ts_cbranch_execz .Lpad_top");
}

extern "C" void kernel_launch(void* const* d_in, const int* in_sizes, int n_in, void* d_out, int out_size,
                              void* d_ws, size_t ws_size, hipStream_t stream) {
    const u16* x = (const u16*)d_in[0];
    const u16* wqkv = (const u16*)d_in[1];
    const u16* wphi = (const u16*)d_in[2];
    const u16* wo = (const u16*)d_in[3];
    const u16* wr = (const u16*)d_in[5];
    const u16* wg = (const u16*)d_in[6];
    const u16* wu = (const u16*)d_in[7];
    const u16* wd = (const u16*)d_in[8];
    const u16* gw = (const u16*)d_in[9];
    fused_fwd<<<256, 512, 0, stream>>>(x, wqkv, wphi, wo, wr, wg, wu, wd, gw, (char*)d_ws, (u16*)d_out);
}
```

```cpp
#include <hip/hip_runtime.h>
#include <stdint.h>
#include <type_traits>

typedef __attribute__((ext_vector_type(8))) short bf16x8;
typedef __attribute__((ext_vector_type(4))) short s16x4;
typedef __attribute__((ext_vector_type(4))) float f32x4;
typedef __attribute__((ext_vector_type(2))) float f32x2;
typedef __attribute__((ext_vector_type(32))) float f32x32;
typedef __attribute__((ext_vector_type(2))) unsigned u32x2;
typedef __attribute__((ext_vector_type(4))) unsigned u32x4;
typedef __attribute__((ext_vector_type(8))) unsigned u32x8;
typedef __attribute__((ext_vector_type(16))) unsigned u32x16;
typedef __attribute__((ext_vector_type(32))) unsigned u32x32;
typedef __attribute__((ext_vector_type(2))) __bf16 bf16x2_t;
typedef unsigned short u16;
typedef __amdgpu_buffer_rsrc_t rsrc_t;

#define DI __device__ __forceinline__
#define AS3 __attribute__((address_space(3)))
#define RLX_WG __ATOMIC_RELAXED, __HIP_MEMORY_SCOPE_WORKGROUP

constexpr int AUX_SC1 = 16;

DI float bf2f(u16 v) { return __uint_as_float(((unsigned)v) << 16); }
DI u16 f2bf(float f) { __bf16 b = (__bf16)f; return __builtin_bit_cast(u16, b); }
DI float rbf(float f) { return bf2f(f2bf(f)); }
DI unsigned pack2(float lo, float hi) {
    f32x2 v = {lo, hi};
    bf16x2_t b = __builtin_convertvector(v, bf16x2_t);
    return __builtin_bit_cast(unsigned, b);
}
DI float lo16(unsigned u) { return __uint_as_float(u << 16); }
DI float hi16(unsigned u) { return __uint_as_float(u & 0xffff0000u); }
DI float dot2(unsigned a, unsigned b, float c) {
    return __builtin_amdgcn_fdot2_f32_bf16(__builtin_bit_cast(bf16x2_t, a), __builtin_bit_cast(bf16x2_t, b), c, false);
}
DI s16x4 tr_read(unsigned a) { return __builtin_amdgcn_ds_read_tr16_b64_v4i16((AS3 s16x4*)(size_t)a); }
DI bf16x8 cat8(s16x4 a, s16x4 b) { return __builtin_shufflevector(a, b, 0, 1, 2, 3, 4, 5, 6, 7); }
DI bf16x8 lds_rd128(unsigned a) { return *(AS3 const bf16x8*)(size_t)a; }
DI void lds_wr128(unsigned a, bf16x8 v) { *(AS3 bf16x8*)(size_t)a = v; }
DI f32x4 mfma16(bf16x8 a, bf16x8 b, f32x4 c) { return __builtin_amdgcn_mfma_f32_16x16x32_bf16(a, b, c, 0, 0, 0); }
DI bf16x8 pack44(f32x4 a, f32x4 b) {
    u32x4 p = {pack2(a[0], a[1]), pack2(a[2], a[3]), pack2(b[0], b[1]), pack2(b[2], b[3])};
    return __builtin_bit_cast(bf16x8, p);
}
#define FU(x) __float_as_uint(x)
#define UF(x) __uint_as_float(x)
DI float x32_sum(float v) { auto r = __builtin_amdgcn_permlane32_swap(FU(v), FU(v), false, false); return UF(r[0]) + UF(r[1]); }
DI float x16_sum(float v) { auto r = __builtin_amdgcn_permlane16_swap(FU(v), FU(v), false, false); return UF(r[0]) + UF(r[1]); }
DI float xg_sum(float v) { return x16_sum(x32_sum(v)); }
template <int CTRL> DI float dpp_add(float v) { return v + UF(__builtin_amdgcn_update_dpp(0, (int)FU(v), CTRL, 0xf, 0xf, false)); }
DI float row16_sum(float v) { return dpp_add<0x121>(dpp_add<0x122>(dpp_add<0x124>(dpp_add<0x128>(v)))); }
template <int CTRL> DI unsigned dpp_umax(unsigned v) { return max(v, (unsigned)__builtin_amdgcn_update_dpp(0, (int)v, CTRL, 0xf, 0xf, false)); }
DI unsigned wave_umax(unsigned v) {
    auto r = __builtin_amdgcn_permlane32_swap(v, v, false, false);
    v = max(r[0], r[1]);
    auto q = __builtin_amdgcn_permlane16_swap(v, v, false, false);
    v = max(q[0], q[1]);
    return dpp_umax<0x121>(dpp_umax<0x122>(dpp_umax<0x124>(dpp_umax<0x128>(v))));
}
DI unsigned add4(u32x4 v, unsigned s) { return (v[0] + v[1] + v[2]) + (v[3] + s); }
template <int CTRL> DI unsigned dpp_uadd(unsigned v) { return v + (unsigned)__builtin_amdgcn_update_dpp(0, (int)v, CTRL, 0xf, 0xf, false); }
DI unsigned row16_usum(unsigned v) { return dpp_uadd<0x121>(dpp_uadd<0x122>(dpp_uadd<0x124>(dpp_uadd<0x128>(v)))); }
DI rsrc_t mk_rsrc(const void* p, unsigned bytes) { return __builtin_amdgcn_make_buffer_rsrc((void*)p, (short)0, (int)bytes, 0x00020000); }
DI u32x4 ld128c(rsrc_t rs, unsigned vo, unsigned so) { return __builtin_amdgcn_raw_buffer_load_b128(rs, vo, so, AUX_SC1); }
DI u32x2 ld64c(rsrc_t rs, unsigned vo, unsigned so) { return __builtin_amdgcn_raw_buffer_load_b64(rs, vo, so, AUX_SC1); }
DI unsigned ld32c(rsrc_t rs, unsigned vo, unsigned so) { return __builtin_amdgcn_raw_buffer_load_b32(rs, vo, so, AUX_SC1); }
DI void st128c(u32x4 v, rsrc_t rs, unsigned vo, unsigned so) {
    __builtin_amdgcn_raw_buffer_store_b128(v, rs, vo, so, AUX_SC1);
    asm volatile("s_nop 1" :: "v"(v) : "memory");
}
DI void st64c(u32x2 v, rsrc_t rs, unsigned vo, unsigned so) { __builtin_amdgcn_raw_buffer_store_b64(v, rs, vo, so, AUX_SC1); }
DI void st32c(unsigned v, rsrc_t rs, unsigned vo, unsigned so) { __builtin_amdgcn_raw_buffer_store_b32(v, rs, vo, so, AUX_SC1); }

DI void dma16(rsrc_t rs, unsigned voff, unsigned soff, unsigned lds_addr) {
    asm volatile("s_mov_b32 m0, %0\n\ts_nop 0\n\tbuffer_load_dwordx4 %1, %2, %3 offen lds"
                 :: "s"(lds_addr), "v"(voff), "s"(rs), "s"(soff) : "memory");
}
template <int PC> DI void dma4(rsrc_t rs, const unsigned* v, unsigned soff, unsigned lds_addr) {
#define D4(p) asm volatile("s_mov_b32 m0, %0\n\ts_nop 0\n\tbuffer_load_dwordx4 %1, %5, %6 offen lds\n\t" p "buffer_load_dwordx4 %2, %5, %6 offen offset:1024 lds\n\t" p \
                 "buffer_load_dwordx4 %3, %5, %6 offen offset:2048 lds\n\t" p "buffer_load_dwordx4 %4, %5, %6 offen offset:3072 lds" \
                 :: "s"(lds_addr), "v"(v[0]), "v"(v[1]), "v"(v[2]), "v"(v[3]), "s"(rs), "s"(soff), "i"(PC ? PC - 1 : 0) : "memory")
    if (PC) D4("s_nop %7\n\t"); else D4("");
}
template <int N> DI void wait_vm() { asm volatile("s_waitcnt vmcnt(%0)" :: "i"(N) : "memory"); }
DI void wait_lgkm0() { asm volatile("" ::: "memory"); __builtin_amdgcn_s_waitcnt(0xC07F); asm volatile("" ::: "memory"); }
DI void bar() { asm volatile("" ::: "memory"); __builtin_amdgcn_s_barrier(); asm volatile("" ::: "memory"); }
DI void fence_c() { asm volatile("" ::: "memory"); }
DI int fresh_lane() {
    int v;
    asm volatile("v_mbcnt_lo_u32_b32 %0, -1, 0\n\tv_mbcnt_hi_u32_b32 %0, -1, %0" : "=v"(v));
    return v;
}
#define LANE_VARS const int l = fresh_lane(), c = l & 15, g = l >> 4, i4 = (l & 15) >> 2, i3 = l & 3; (void)c; (void)g; (void)i4; (void)i3
#define PSW ((unsigned)(4 * g + i4) * 128u + (unsigned)((i4 ^ (g & 1)) << 5))
#define WPO ((unsigned)(4 * g + i4) * 64u + (unsigned)((g & 1) << 5) + (unsigned)(8 * i3))
#define WSW ((unsigned)(8 * g + i4) * 128u + (unsigned)((i4 ^ (g & 1)) << 5) + (unsigned)(8 * i3))

#define WO_VOFF ((unsigned)(l >> 3) * 2048u + (unsigned)(((((((l & 7) >> 1) ^ ((l >> 3) & 3) ^ (l >> 5)) << 1) | (l & 1))) << 4))

DI void lds_wait(unsigned addr, unsigned target) {
#pragma unroll 1
    for (int it = 0; it < (1 << 22); ++it) {
        unsigned v = __hip_atomic_load((AS3 unsigned*)(size_t)addr, RLX_WG);
        if (__builtin_amdgcn_readfirstlane(v) >= target) break;
        __builtin_amdgcn_s_sleep(1);
    }
    fence_c();
}
DI unsigned now32() { unsigned long long t; asm volatile("s_memrealtime %0\n\ts_waitcnt lgkmcnt(0)" : "=s"(t) :: "memory"); return (unsigned)t; }
DI void wait_until(unsigned t) {
#pragma unroll 1
    for (int it = 0; it < 256; ++it) {
        const int rem = (int)(t - now32());
        if (rem <= 4) break;
        if (rem > 12) __builtin_amdgcn_s_sleep(2);
    }
    fence_c();
}
DI unsigned wave_usum(unsigned m) {
    auto p = __builtin_amdgcn_permlane32_swap(m, m, false, false);
    m = p[0] + p[1];
    auto q = __builtin_amdgcn_permlane16_swap(m, m, false, false);
    return row16_usum(q[0] + q[1]);
}
DI void post(unsigned addr, unsigned v) {
    fence_c();
    *(AS3 volatile unsigned*)(size_t)addr = v;
    fence_c();
}
DI void wait4(unsigned addr, unsigned target) {
#pragma unroll 1
    for (int it = 0; it < (1 << 22); ++it) {
        const u32x4 v = *(AS3 volatile const u32x4*)(size_t)addr;
        if (__builtin_amdgcn_readfirstlane(min(min(v[0], v[1]), min(v[2], v[3]))) >= target) break;
        __builtin_amdgcn_s_sleep(1);
    }
    fence_c();
}
constexpr int POLL_LIMIT = 1 << 16, TRY_LIMIT = 1 << 12;

#define KNOB(n, v) static constexpr int n = v
#define KNOBS
struct Dflt {
    KNOB(KT0, 0x76534210); KNOB(KT1, 0x76453201); KNOB(KT2, 0x75643120); KNOB(KT3, 0x67542310);
    KNOB(BGT, 0x76227622); KNOB(BUT, 0x77327732); KNOB(BRT, 0x77777777); KNOB(BST, 0x33333333); KNOB(PBT, (int)0xaaaaaaaa);
    KNOB(SLG, 0); KNOB(SLS, 160); KNOB(X0, 130); KNOB(X1, 2); KNOB(X2, 2); KNOB(TOPV, 1);
    KNOB(PRED_K, 16); KNOB(PRED_C, 85);
    KNOB(D1, 40); KNOB(D2, 110); KNOB(D3, 132); KNOB(FG_AT, -100); KNOB(Y_AT, 250); KNOB(QA, 30); KNOB(QB, 40);
    KNOB(RETRY_S, 8); KNOB(PT_S, 12); KNOB(KF_S, 10); KNOB(WO_D, 24); KNOB(FD, 12);
    KNOB(PT_B, 10); KNOB(XE, 20);
    KNOB(RE1, 1); KNOB(L2_S, 12);
    KNOB(FORCE_REDO, 0);
    KNOB(ND, 6); KNOB(XAH, 0); KNOB(QG, 2); KNOB(HP, 2); KNOB(Q5E, 1); KNOB(PADS, 1); KNOB(WOSK, 0); KNOB(WOE, 2); KNOB(MAP8, 0); KNOB(DLATE, 1); KNOB(HUSH, 0); KNOB(TNT, 0); KNOB(TWB, 13); KNOB(TMW, 0); KNOB(DWP, 1); KNOB(WOG, 0); KNOB(PK13, 3); KNOB(DS1, 2); KNOB(DS2, 9); KNOB(DS3, 16); KNOB(PACE, 0);
};
struct K : Dflt { KNOBS };

constexpr unsigned WS_EPOCH = 0;
constexpr unsigned WS_RG = 4096;
constexpr unsigned WS_PT = 6144;
constexpr unsigned WS_G = 8192;
constexpr unsigned WS_YF = 24576;
constexpr unsigned WS_FG = 65536;
constexpr unsigned WS_Y = 1u << 19;
constexpr unsigned WS_BYTES = 1u << 20;
constexpr unsigned WB_KVP = 0;
constexpr unsigned WB_SM = 1u << 20;
constexpr unsigned WB_PRB = 3u << 19;
constexpr unsigned WB_OG = 7u << 18;
constexpr unsigned WB_OF = 8u << 20;
constexpr unsigned WB_BYTES = 13u << 20;
DI char* bulk_base(char* ws) { return (char*)(((size_t)ws + (3u << 20)) & ~(size_t)((2u << 20) - 1)); }

constexpr unsigned SLOT = 40960;
constexpr unsigned L_PHI = 98304;
constexpr unsigned L_VF = 131072;
constexpr unsigned L_MISC = 147456;
constexpr unsigned L_WP = L_MISC, L_KVF = L_MISC + 4096, L_SM = L_MISC + 12288;
constexpr unsigned C_ST = L_SM + 0  , C_R = L_SM + 8  , C_P = L_SM + 16  ;
constexpr unsigned C_WO = L_SM + 56, C_YRDY = L_SM + 68, C_IDX = L_SM + 76, C_TM = L_SM + 80, C_XE = L_SM + 84, C_Y1 = L_SM + 88;
constexpr unsigned P_E14 = 1, P_E15 = 2, P_PHA = 3, P_PHB = 4, P_KVD = 5, P_PUB = 6, P_Q1 = 7  , P_QRD = 11, P_O = 12;
constexpr unsigned C_Q = L_SM + 92, C_HOLD = L_SM + 96;
constexpr unsigned XS0[3] = {16384, 98304, L_VF}, XS1[3] = {57344, 122880, L_VF + 8192};
constexpr unsigned QX[3] = {40960, 81920, 0}, QW[6] = {L_KVF, 65536, 73728, L_PHI + 8192, L_PHI + 16384, L_PHI};
constexpr unsigned R_L1I = 1, R_L1 = 2, R_L2I = 3, R_KVF = 4;
constexpr unsigned L_XE = L_VF;
constexpr unsigned L_KEY = L_SM + 256, L_TAG = L_SM + 288, L_IDX = L_SM + 296, L_TM = L_SM + 304  , L_PLC = L_SM + 320  , L_PRB = L_SM + 336  , L_ZF = L_SM + 512, L_Y = L_SM + 1536;

constexpr unsigned long long L2E2 = 0x3fb8aa3b3fb8aa3bull;

#define PHASE_AB_ASM \
    "s_branch .Lpad_ab0\n\t" \
    ".Lab_s1:\n\t" \
    "s_mov_b32 s89, 0x400000\n\t" \
    ".Lab_s1a:\n\t" \
    "s_waitcnt lgkmcnt(0)\n\t" \
    "ds_read_b32 v237, v249\n\t" \
    "ds_read_b32 v238, v249 offset:4\n\t" \
    "s_waitcnt lgkmcnt(0)\n\t" \
    "v_min_u32 v237, v237, v238\n\t" \
    "s_nop 1\n\t" \
    "v_readfirstlane_b32 s85, v237\n\t" \
    "s_sub_u32 s89, s89, 1\n\t" \
    "s_cbranch_scc1 .Lab_s1b\n\t" \
    "s_cmp_lt_u32 s85, 1\n\t" \
    "s_cbranch_scc1 .Lab_s1a\n\t" \
    ".Lab_s1b:\n\t" \
    "s_nop 7\n\t" \
    "s_waitcnt lgkmcnt(0)\n\t" \
    "s_branch .Lab_s1r\n\t" \
    ".Lab_s2:\n\t" \
    "s_mov_b32 s89, 0x400000\n\t" \
    ".Lab_s2a:\n\t" \
    "s_waitcnt lgkmcnt(0)\n\t" \
    "ds_read_b32 v237, v249 offset:16\n\t" \
    "ds_read_b32 v238, v249 offset:20\n\t" \
    "ds_read_b32 v255, v249 offset:24\n\t" \
    "s_waitcnt lgkmcnt(0)\n\t" \
    "v_min3_u32 v237, v237, v238, v255\n\t" \
    "ds_read_b32 v238, v249 offset:28\n\t" \
    "s_waitcnt lgkmcnt(0)\n\t" \
    "v_min_u32 v237, v237, v238\n\t" \
    "s_nop 1\n\t" \
    "v_readfirstlane_b32 s85, v237\n\t" \
    "s_sub_u32 s89, s89, 1\n\t" \
    "s_cbranch_scc1 .Lab_s2b\n\t" \
    "s_cmp_lt_u32 s85, 2\n\t" \
    "s_cbranch_scc1 .Lab_s2a\n\t" \
    ".Lab_s2b:\n\t" \
    "s_nop 7\n\t" \
    "s_waitcnt lgkmcnt(0)\n\t" \
    "s_branch .Lab_s2r\n\t" \
    ".Lab_s4:\n\t" \
    "s_mov_b32 s89, 0x400000\n\t" \
    ".Lab_s4a:\n\t" \
    "s_waitcnt lgkmcnt(0)\n\t" \
    "ds_read_b32 v237, v250\n\t" \
    "s_waitcnt lgkmcnt(0)\n\t" \
    "s_nop 1\n\t" \
    "v_readfirstlane_b32 s85, v237\n\t" \
    "s_sub_u32 s89, s89, 1\n\t" \
    "s_cbranch_scc1 .Lab_s4b\n\t" \
    "s_cmp_lt_u32 s85, 4\n\t" \
    "s_cbranch_scc1 .Lab_s4a\n\t" \
    ".Lab_s4b:\n\t" \
    "s_nop 7\n\t" \
    "ds_read_b128 v[0:3], v252\n\t" \
    "ds_read_b128 v[4:7], v254\n\t" \
    "ds_read_b128 v[20:23], v254 offset:4096\n\t" \
    "ds_read_b128 v[12:15], v254 offset:1024\n\t" \
    "ds_read_b128 v[28:31], v254 offset:5120\n\t" \
    "s_waitcnt lgkmcnt(0)\n\t" \
    "s_branch .Lab_s4r\n\t" \
    ".Lab_s5:\n\t" \
    "s_mov_b32 s89, 0x400000\n\t" \
    ".Lab_s5a:\n\t" \
    "s_waitcnt lgkmcnt(0)\n\t" \
    "ds_read_b32 v237, v249 offset:92\n\t" \
    "s_waitcnt lgkmcnt(0)\n\t" \
    "s_nop 1\n\t" \
    "v_readfirstlane_b32 s85, v237\n\t" \
    "s_sub_u32 s89, s89, 1\n\t" \
    "s_cbranch_scc1 .Lab_s5b\n\t" \
    "s_cmp_lt_u32 s85, 1\n\t" \
    "s_cbranch_scc1 .Lab_s5a\n\t" \
    ".Lab_s5b:\n\t" \
    "s_nop 7\n\t" \
    "ds_read_b64_tr_b16 v[32:33], v253\n\t" \
    "ds_read_b64_tr_b16 v[34:35], v253 offset:512\n\t" \
    "ds_read_b64_tr_b16 v[36:37], v251\n\t" \
    "ds_read_b64_tr_b16 v[38:39], v251 offset:512\n\t" \
    "ds_read_b64_tr_b16 v[40:41], v253 offset:4096\n\t" \
    "ds_read_b64_tr_b16 v[42:43], v253 offset:4608\n\t" \
    "ds_read_b64_tr_b16 v[44:45], v251 offset:4096\n\t" \
    "ds_read_b64_tr_b16 v[46:47], v251 offset:4608\n\t" \
    "s_waitcnt lgkmcnt(0)\n\t" \
    "s_branch .Lab_s5r\n\t" \
    ".Lab_s7:\n\t" \
    "s_mov_b32 s89, 0x400000\n\t" \
    ".Lab_s7a:\n\t" \
    "s_sleep 1\n\t" \
    "ds_read_b32 v237, v249 offset:8\n\t" \
    "s_waitcnt lgkmcnt(0)\n\t" \
    "v_readfirstlane_b32 s85, v237\n\t" \
    "s_sub_u32 s89, s89, 1\n\t" \
    "s_cbranch_scc1 .Lab_s7r\n\t" \
    "s_cmp_lt_u32 s85, 3\n\t" \
    "s_cbranch_scc1 .Lab_s7a\n\t" \
    "s_sleep 5\n\t" \
    "s_branch .Lab_s7r\n\t" \
    ".Lab_sa:\n\t" \
    "ds_read_b32 v36, v249 offset:8\n\t" \
    "s_waitcnt lgkmcnt(0)\n\t" \
    "v_readfirstlane_b32 s80, v36\n\t" \
    "s_sub_u32 s89, s89, 1\n\t" \
    "s_cbranch_scc1 .Lab_sar\n\t" \
    "s_cmp_lt_u32 s80, s82\n\t" \
    "s_cbranch_scc1 .Lab_sa\n\t" \
    "s_cmp_eq_u32 s80, s82\n\t" \
    "s_cbranch_scc1 .Lab_sar\n\t" \
    "s_waitcnt vmcnt(0)\n\t" \
    "s_branch .Lab_k\n\t" \
    ".Lab_s8:\n\t" \
    "s_mov_b32 s89, 0x10000\n\t" \
    "v_lshlrev_b32 v35, 3, v34\n\t" \
    ".Lab_s8a:\n\t" \
    "s_sleep 8\n\t" \
    "buffer_load_dwordx2 v[232:233], v35, %[rsw], %[sg] offen sc1\n\t" \
    "buffer_load_dwordx2 v[234:235], v35, %[rsw], %[sg] offen offset:128 sc1\n\t" \
    "s_sub_u32 s89, s89, 1\n\t" \
    "s_waitcnt vmcnt(0)\n\t" \
    "s_cbranch_scc1 .Lab_s8r\n\t" \
    "v_cmp_ne_u32 vcc, v236, v233\n\t" \
    "v_cmp_ne_u32_e64 s[92:93], v236, v235\n\t" \
    "s_nop 0\n\t" \
    "s_or_b64 vcc, vcc, s[92:93]\n\t" \
    "s_cbranch_scc1 .Lab_s8a\n\t" \
    "s_branch .Lab_s8r\n\t" \
    ".Lab_s9:\n\t" \
    "s_mov_b32 s89, 0x400000\n\t" \
    ".Lab_s9a:\n\t" \
    "s_waitcnt lgkmcnt(0)\n\t" \
    "ds_read_b32 v237, v249 offset:56\n\t" \
    "s_waitcnt lgkmcnt(0)\n\t" \
    "s_nop 1\n\t" \
    "v_readfirstlane_b32 s85, v237\n\t" \
    "s_sub_u32 s89, s89, 1\n\t" \
    "s_cbranch_scc1 .Lab_s9b\n\t" \
    "s_cmp_lt_u32 s85, 2\n\t" \
    "s_cbranch_scc1 .Lab_s9a\n\t" \
    ".Lab_s9b:\n\t" \
    "s_nop 7\n\t" \
    "s_waitcnt lgkmcnt(0)\n\t" \
    "s_branch .Lab_s9r\n\t" \
    ".p2align 12\n\t" \
    ".Lpad_ab0:\n\t" \
    "s_cbranch_execz .Lpad_top\n\t" \
    "v_mov_b32 v32, 0\n\t" \
    "v_mov_b32 v33, 0\n\t" \
    "v_mov_b32 v34, 0\n\t" \
    "v_mov_b32 v35, 0\n\t" \
    "v_mov_b32 v36, 0\n\t" \
    "v_mov_b32 v37, 0\n\t" \
    "v_mov_b32 v38, 0\n\t" \
    "v_mov_b32 v39, 0\n\t" \
    "v_mov_b32 v40, 0\n\t" \
    "v_mov_b32 v41, 0\n\t" \
    "v_mov_b32 v42, 0\n\t" \
    "v_mov_b32 v43, 0\n\t" \
    "v_mov_b32 v44, 0\n\t" \
    "v_mov_b32 v45, 0\n\t" \
    "v_mov_b32 v46, 0\n\t" \
    "v_mov_b32 v47, 0\n\t" \
    "v_mov_b32 v48, 0\n\t" \
    "v_mov_b32 v49, 0\n\t" \
    "v_mov_b32 v50, 0\n\t" \
    "v_mov_b32 v51, 0\n\t" \
    "v_mov_b32 v52, 0\n\t" \
    "v_mov_b32 v53, 0\n\t" \
    "v_mov_b32 v54, 0\n\t" \
    "v_mov_b32 v55, 0\n\t" \
    "v_mov_b32 v56, 0\n\t" \
    "v_mov_b32 v57, 0\n\t" \
    "v_mov_b32 v58, 0\n\t" \
    "v_mov_b32 v59, 0\n\t" \
    "v_mov_b32 v60, 0\n\t" \
    "v_mov_b32 v61, 0\n\t" \
    "v_mov_b32 v62, 0\n\t" \
    "v_mov_b32 v63, 0\n\t" \
    "v_mov_b32 v80, 0\n\t" \
    "v_mov_b32 v81, 0\n\t" \
    "v_mov_b32 v82, 0\n\t" \
    "v_mov_b32 v83, 0\n\t" \
    "v_mov_b32 v84, 0\n\t" \
    "v_mov_b32 v85, 0\n\t" \
    "v_mov_b32 v86, 0\n\t" \
    "v_mov_b32 v87, 0\n\t" \
    "v_mov_b32 v88, 0\n\t" \
    "v_mov_b32 v89, 0\n\t" \
    "v_mov_b32 v90, 0\n\t" \
    "v_mov_b32 v91, 0\n\t" \
    "v_mov_b32 v92, 0\n\t" \
    "v_mov_b32 v93, 0\n\t" \
    "v_mov_b32 v94, 0\n\t" \
    "v_mov_b32 v95, 0\n\t" \
    "v_mov_b32 v96, 0\n\t" \
    "v_mov_b32 v97, 0\n\t" \
    "v_mov_b32 v98, 0\n\t" \
    "v_mov_b32 v99, 0\n\t" \
    "v_mov_b32 v100, 0\n\t" \
    "v_mov_b32 v101, 0\n\t" \
    "v_mov_b32 v102, 0\n\t" \
    "v_mov_b32 v103, 0\n\t" \
    "v_mov_b32 v104, 0\n\t" \
    "v_mov_b32 v105, 0\n\t" \
    "v_mov_b32 v106, 0\n\t" \
    "v_mov_b32 v107, 0\n\t" \
    "v_mov_b32 v108, 0\n\t" \
    "v_mov_b32 v109, 0\n\t" \
    "v_mov_b32 v110, 0\n\t" \
    "v_mov_b32 v111, 0\n\t" \
    "v_mov_b32 v192, 0\n\t" \
    "v_mov_b32 v193, 0\n\t" \
    "v_mov_b32 v194, 0\n\t" \
    "v_mov_b32 v195, 0\n\t" \
    "v_mov_b32 v196, 0\n\t" \
    "v_mov_b32 v197, 0\n\t" \
    "v_mov_b32 v198, 0\n\t" \
    "v_mov_b32 v199, 0\n\t" \
    "v_mov_b32 v200, 0\n\t" \
    "v_mov_b32 v201, 0\n\t" \
    "v_mov_b32 v202, 0\n\t" \
    "v_mov_b32 v203, 0\n\t" \
    "v_mov_b32 v204, 0\n\t" \
    "v_mov_b32 v205, 0\n\t" \
    "v_mov_b32 v206, 0\n\t" \
    "v_mov_b32 v207, 0\n\t" \
    "v_mov_b32 v208, 0\n\t" \
    "v_mov_b32 v209, 0\n\t" \
    "v_mov_b32 v210, 0\n\t" \
    "v_mov_b32 v211, 0\n\t" \
    "v_mov_b32 v212, 0\n\t" \
    "v_mov_b32 v213, 0\n\t" \
    "v_mov_b32 v214, 0\n\t" \
    "v_mov_b32 v215, 0\n\t" \
    "v_mov_b32 v216, 0\n\t" \
    "v_mov_b32 v217, 0\n\t" \
    "v_mov_b32 v218, 0\n\t" \
    "v_mov_b32 v219, 0\n\t" \
    "v_mov_b32 v220, 0\n\t" \
    "v_mov_b32 v221, 0\n\t" \
    "v_mov_b32 v222, 0\n\t" \
    "v_mov_b32 v223, 0\n\t" \
    "v_mov_b32 v232, 0x3f803f80\n\t" \
    "v_mov_b32 v233, 0x3f803f80\n\t" \
    "v_mov_b32 v234, 0x3f803f80\n\t" \
    "v_mov_b32 v235, 0x3f803f80\n\t" \
    "v_mov_b32 v251, v240\n\t" \
    "v_xor_b32 v252, 64, v240\n\t" \
    "v_mov_b32 v253, v241\n\t" \
    "v_xor_b32 v254, 32, v241\n\t" \
    "s_mov_b32 s90, 0x3fb8aa3b\n\t" \
    "s_mov_b32 s91, 0x3fb8aa3b\n\t" \
    "v_cmp_eq_u32_e64 s[92:93], 2, v250\n\t" \
    "v_cmp_gt_u32_e64 s[94:95], 2, v250\n\t" \
    "v_cmp_eq_u32 vcc, 0, v250\n\t" \
    "v_xor_b32 v250, 4, v248\n\t" \
    "s_mov_b32 s84, 0x140a0014\n\t" \
    "s_lshl_b32 s83, %[xs0], 4\n\t" \
    "s_lshl_b32 s85, %[xs1], 12\n\t" \
    "s_or_b32 s83, s83, s85\n\t" \
    "s_or_b32 s83, s83, 0x14\n\t" \
    "s_waitcnt lgkmcnt(0)\n\t" \
    "s_mov_b32 s80, 64\n\t" \
    "s_add_u32 s81, %[pro], 64\n\t" \
    ".Lab_p:\n\t" \
    "s_memtime s[86:87]\n\t" \
    "s_load_dword s85, %[prb], s80\n\t" \
    "s_waitcnt lgkmcnt(0)\n\t" \
    "s_memtime s[88:89]\n\t" \
    "s_load_dword s85, %[prb], s81\n\t" \
    "s_waitcnt lgkmcnt(0)\n\t" \
    "s_memtime s[96:97]\n\t" \
    "s_mov_b32 s81, %[pro]\n\t" \
    "s_sub_u32 s80, s80, 64\n\t" \
    "s_waitcnt lgkmcnt(0)\n\t" \
    "s_cbranch_scc0 .Lab_p\n\t" \
    "s_sub_u32 s86, s88, s86\n\t" \
    "s_sub_u32 s88, s96, s88\n\t" \
    "s_mov_b32 s80, 0\n\t" \
    "s_mov_b32 s81, 10\n\t" \
    "s_pack_ll_b32_b16 s86, s86, s88\n\t" \
    "v_mov_b32 v237, s86\n\t" \
    "ds_write_b32 v248, v237 offset:320\n\t" \
    "s_waitcnt lgkmcnt(0)\n\t" \
    "s_barrier\n\t" \
    "ds_read_b128 v[128:131], v251\n\t" \
    "ds_read_b128 v[132:135], v251 offset:2048\n\t" \
    "ds_read_b128 v[136:139], v251 offset:4096\n\t" \
    "ds_read_b128 v[140:143], v251 offset:6144\n\t" \
    "ds_read_b64_tr_b16 v[0:1], v253 offset:8192\n\t" \
    "ds_read_b64_tr_b16 v[2:3], v253 offset:8704\n\t" \
    "ds_read_b64_tr_b16 v[4:5], v254 offset:8192\n\t" \
    "ds_read_b64_tr_b16 v[6:7], v254 offset:8704\n\t" \
    "ds_read_b64_tr_b16 v[8:9], v253 offset:16384\n\t" \
    "ds_read_b64_tr_b16 v[10:11], v253 offset:16896\n\t" \
    "ds_read_b64_tr_b16 v[12:13], v254 offset:16384\n\t" \
    "ds_read_b64_tr_b16 v[14:15], v254 offset:16896\n\t" \
    "ds_read_b64_tr_b16 v[16:17], v253\n\t" \
    "ds_read_b64_tr_b16 v[18:19], v253 offset:512\n\t" \
    "ds_read_b64_tr_b16 v[20:21], v254\n\t" \
    "ds_read_b64_tr_b16 v[22:23], v254 offset:512\n\t" \
    ".Lab_l1:\n\t" \
    "s_waitcnt lgkmcnt(0)\n\t" \
    "v_mfma_f32_16x16x32_bf16 v[32:35], v[0:3], v[128:131], v[32:35]\n\t" \
    "ds_read_b128 v[144:147], v252\n\t" \
    "v_mfma_f32_16x16x32_bf16 v[40:43], v[0:3], v[132:135], v[40:43]\n\t" \
    "ds_read_b128 v[148:151], v252 offset:2048\n\t" \
    "v_mfma_f32_16x16x32_bf16 v[48:51], v[0:3], v[136:139], v[48:51]\n\t" \
    "ds_read_b128 v[152:155], v252 offset:4096\n\t" \
    "v_mfma_f32_16x16x32_bf16 v[56:59], v[0:3], v[140:143], v[56:59]\n\t" \
    "ds_read_b128 v[156:159], v252 offset:6144\n\t" \
    "v_mfma_f32_16x16x32_bf16 v[36:39], v[4:7], v[128:131], v[36:39]\n\t" \
    "ds_read_b64_tr_b16 v[112:113], v253 offset:12288\n\t" \
    "v_mfma_f32_16x16x32_bf16 v[44:47], v[4:7], v[132:135], v[44:47]\n\t" \
    "ds_read_b64_tr_b16 v[114:115], v253 offset:12800\n\t" \
    "v_mfma_f32_16x16x32_bf16 v[52:55], v[4:7], v[136:139], v[52:55]\n\t" \
    "ds_read_b64_tr_b16 v[116:117], v254 offset:12288\n\t" \
    "v_mfma_f32_16x16x32_bf16 v[60:63], v[4:7], v[140:143], v[60:63]\n\t" \
    "ds_read_b64_tr_b16 v[118:119], v254 offset:12800\n\t" \
    "v_mfma_f32_16x16x32_bf16 v[80:83], v[128:131], v[8:11], v[80:83]\n\t" \
    "ds_read_b64_tr_b16 v[120:121], v253 offset:20480\n\t" \
    "v_mfma_f32_16x16x32_bf16 v[88:91], v[132:135], v[8:11], v[88:91]\n\t" \
    "ds_read_b64_tr_b16 v[122:123], v253 offset:20992\n\t" \
    "v_mfma_f32_16x16x32_bf16 v[96:99], v[136:139], v[8:11], v[96:99]\n\t" \
    "ds_read_b64_tr_b16 v[124:125], v254 offset:20480\n\t" \
    "v_mfma_f32_16x16x32_bf16 v[104:107], v[140:143], v[8:11], v[104:107]\n\t" \
    "ds_read_b64_tr_b16 v[126:127], v254 offset:20992\n\t" \
    "v_mfma_f32_16x16x32_bf16 v[84:87], v[128:131], v[12:15], v[84:87]\n\t" \
    "ds_read_b64_tr_b16 v[24:25], v253 offset:4096\n\t" \
    "v_mfma_f32_16x16x32_bf16 v[92:95], v[132:135], v[12:15], v[92:95]\n\t" \
    "ds_read_b64_tr_b16 v[26:27], v253 offset:4608\n\t" \
    "v_mfma_f32_16x16x32_bf16 v[100:103], v[136:139], v[12:15], v[100:103]\n\t" \
    "ds_read_b64_tr_b16 v[28:29], v254 offset:4096\n\t" \
    "v_mfma_f32_16x16x32_bf16 v[108:111], v[140:143], v[12:15], v[108:111]\n\t" \
    "ds_read_b64_tr_b16 v[30:31], v254 offset:4608\n\t" \
    "v_mfma_f32_16x16x32_bf16 v[192:195], v[16:19], v[128:131], v[192:195]\n\t" \
    "s_add_u32 s85, s80, 40960\n\t" \
    "s_cmp_lg_u32 s80, 81920\n\t" \
    "v_mfma_f32_16x16x32_bf16 v[200:203], v[16:19], v[132:135], v[200:203]\n\t" \
    "s_cselect_b32 s80, s85, 0\n\t" \
    "v_mfma_f32_16x16x32_bf16 v[208:211], v[16:19], v[136:139], v[208:211]\n\t" \
    "v_add_u32 v251, s80, v240\n\t" \
    "v_mfma_f32_16x16x32_bf16 v[216:219], v[16:19], v[140:143], v[216:219]\n\t" \
    "v_add_u32 v253, s80, v241\n\t" \
    "v_mfma_f32_16x16x32_bf16 v[196:199], v[20:23], v[128:131], v[196:199]\n\t" \
    "v_xor_b32 v254, 32, v253\n\t" \
    "v_mfma_f32_16x16x32_bf16 v[204:207], v[20:23], v[132:135], v[204:207]\n\t" \
    "v_mfma_f32_16x16x32_bf16 v[212:215], v[20:23], v[136:139], v[212:215]\n\t" \
    "v_mfma_f32_16x16x32_bf16 v[220:223], v[20:23], v[140:143], v[220:223]\n\t" \
    "s_waitcnt lgkmcnt(0)\n\t" \
    "s_barrier\n\t" \
    "v_mfma_f32_16x16x32_bf16 v[32:35], v[112:115], v[144:147], v[32:35]\n\t" \
    "ds_read_b128 v[128:131], v251\n\t" \
    "v_mfma_f32_16x16x32_bf16 v[40:43], v[112:115], v[148:151], v[40:43]\n\t" \
    "ds_read_b128 v[132:135], v251 offset:2048\n\t" \
    "v_mfma_f32_16x16x32_bf16 v[48:51], v[112:115], v[152:155], v[48:51]\n\t" \
    "ds_read_b128 v[136:139], v251 offset:4096\n\t" \
    "v_mfma_f32_16x16x32_bf16 v[56:59], v[112:115], v[156:159], v[56:59]\n\t" \
    "ds_read_b128 v[140:143], v251 offset:6144\n\t" \
    "v_mfma_f32_16x16x32_bf16 v[36:39], v[116:119], v[144:147], v[36:39]\n\t" \
    "ds_read_b64_tr_b16 v[0:1], v253 offset:8192\n\t" \
    "v_mfma_f32_16x16x32_bf16 v[44:47], v[116:119], v[148:151], v[44:47]\n\t" \
    "ds_read_b64_tr_b16 v[2:3], v253 offset:8704\n\t" \
    "v_mfma_f32_16x16x32_bf16 v[52:55], v[116:119], v[152:155], v[52:55]\n\t" \
    "ds_read_b64_tr_b16 v[4:5], v254 offset:8192\n\t" \
    "v_mfma_f32_16x16x32_bf16 v[60:63], v[116:119], v[156:159], v[60:63]\n\t" \
    "ds_read_b64_tr_b16 v[6:7], v254 offset:8704\n\t" \
    "v_mfma_f32_16x16x32_bf16 v[80:83], v[144:147], v[120:123], v[80:83]\n\t" \
    "ds_read_b64_tr_b16 v[8:9], v253 offset:16384\n\t" \
    "v_mfma_f32_16x16x32_bf16 v[88:91], v[148:151], v[120:123], v[88:91]\n\t" \
    "ds_read_b64_tr_b16 v[10:11], v253 offset:16896\n\t" \
    "v_mfma_f32_16x16x32_bf16 v[96:99], v[152:155], v[120:123], v[96:99]\n\t" \
    "ds_read_b64_tr_b16 v[12:13], v254 offset:16384\n\t" \
    "v_mfma_f32_16x16x32_bf16 v[104:107], v[156:159], v[120:123], v[104:107]\n\t" \
    "ds_read_b64_tr_b16 v[14:15], v254 offset:16896\n\t" \
    "v_mfma_f32_16x16x32_bf16 v[84:87], v[144:147], v[124:127], v[84:87]\n\t" \
    "ds_read_b64_tr_b16 v[16:17], v253\n\t" \
    "v_mfma_f32_16x16x32_bf16 v[92:95], v[148:151], v[124:127], v[92:95]\n\t" \
    "ds_read_b64_tr_b16 v[18:19], v253 offset:512\n\t" \
    "v_mfma_f32_16x16x32_bf16 v[100:103], v[152:155], v[124:127], v[100:103]\n\t" \
    "ds_read_b64_tr_b16 v[20:21], v254\n\t" \
    "v_mfma_f32_16x16x32_bf16 v[108:111], v[156:159], v[124:127], v[108:111]\n\t" \
    "ds_read_b64_tr_b16 v[22:23], v254 offset:512\n\t" \
    "v_mfma_f32_16x16x32_bf16 v[192:195], v[24:27], v[144:147], v[192:195]\n\t" \
    "v_xor_b32 v252, 64, v251\n\t" \
    "v_mfma_f32_16x16x32_bf16 v[200:203], v[24:27], v[148:151], v[200:203]\n\t" \
    "v_mfma_f32_16x16x32_bf16 v[208:211], v[24:27], v[152:155], v[208:211]\n\t" \
    "s_sub_u32 s81, s81, 1\n\t" \
    "v_mfma_f32_16x16x32_bf16 v[216:219], v[24:27], v[156:159], v[216:219]\n\t" \
    "s_cmp_lg_u32 s81, 0\n\t" \
    "v_mfma_f32_16x16x32_bf16 v[196:199], v[28:31], v[144:147], v[196:199]\n\t" \
    "v_mfma_f32_16x16x32_bf16 v[204:207], v[28:31], v[148:151], v[204:207]\n\t" \
    "v_mfma_f32_16x16x32_bf16 v[212:215], v[28:31], v[152:155], v[212:215]\n\t" \
    "v_mfma_f32_16x16x32_bf16 v[220:223], v[28:31], v[156:159], v[220:223]\n\t" \
    "s_cbranch_scc1 .Lab_l1\n\t" \
    ".Lab_l2:\n\t" \
    "s_waitcnt lgkmcnt(0)\n\t" \
    "v_mfma_f32_16x16x32_bf16 v[32:35], v[0:3], v[128:131], v[32:35]\n\t" \
    "ds_read_b128 v[144:147], v252\n\t" \
    "ds_read_b64_tr_b16 v[112:113], v253 offset:12288\n\t" \
    "v_mfma_f32_16x16x32_bf16 v[40:43], v[0:3], v[132:135], v[40:43]\n\t" \
    "ds_read_b128 v[148:151], v252 offset:2048\n\t" \
    "ds_read_b64_tr_b16 v[114:115], v253 offset:12800\n\t" \
    "v_mfma_f32_16x16x32_bf16 v[48:51], v[0:3], v[136:139], v[48:51]\n\t" \
    "ds_read_b128 v[152:155], v252 offset:4096\n\t" \
    "s_and_b32 s80, s84, 255\n\t" \
    "v_mfma_f32_16x16x32_bf16 v[56:59], v[0:3], v[140:143], v[56:59]\n\t" \
    "ds_read_b128 v[156:159], v252 offset:6144\n\t" \
    "s_and_b32 s82, s83, 255\n\t" \
    "v_mfma_f32_16x16x32_bf16 v[36:39], v[4:7], v[128:131], v[36:39]\n\t" \
    "ds_read_b64_tr_b16 v[116:117], v254 offset:12288\n\t" \
    "s_lshl_b32 s80, s80, 12\n\t" \
    "v_mfma_f32_16x16x32_bf16 v[44:47], v[4:7], v[132:135], v[44:47]\n\t" \
    "ds_read_b64_tr_b16 v[118:119], v254 offset:12800\n\t" \
    "s_lshl_b32 s82, s82, 12\n\t" \
    "v_mfma_f32_16x16x32_bf16 v[52:55], v[4:7], v[136:139], v[52:55]\n\t" \
    "ds_read_b64_tr_b16 v[120:121], v253 offset:20480\n\t" \
    "v_mfma_f32_16x16x32_bf16 v[60:63], v[4:7], v[140:143], v[60:63]\n\t" \
    "ds_read_b64_tr_b16 v[122:123], v253 offset:20992\n\t" \
    "v_mfma_f32_16x16x32_bf16 v[80:83], v[128:131], v[8:11], v[80:83]\n\t" \
    "ds_read_b64_tr_b16 v[124:125], v254 offset:20480\n\t" \
    "v_mfma_f32_16x16x32_bf16 v[88:91], v[132:135], v[8:11], v[88:91]\n\t" \
    "ds_read_b64_tr_b16 v[126:127], v254 offset:20992\n\t" \
    "v_mfma_f32_16x16x32_bf16 v[96:99], v[136:139], v[8:11], v[96:99]\n\t" \
    "v_add_u32 v251, s82, v240\n\t" \
    "v_mfma_f32_16x16x32_bf16 v[104:107], v[140:143], v[8:11], v[104:107]\n\t" \
    "v_add_u32 v253, s80, v241\n\t" \
    "v_mfma_f32_16x16x32_bf16 v[84:87], v[128:131], v[12:15], v[84:87]\n\t" \
    "v_xor_b32 v254, 32, v253\n\t" \
    "v_mfma_f32_16x16x32_bf16 v[92:95], v[132:135], v[12:15], v[92:95]\n\t" \
    "v_mfma_f32_16x16x32_bf16 v[100:103], v[136:139], v[12:15], v[100:103]\n\t" \
    "v_mfma_f32_16x16x32_bf16 v[108:111], v[140:143], v[12:15], v[108:111]\n\t" \
    "s_waitcnt lgkmcnt(0)\n\t" \
    "s_barrier\n\t" \
    "v_mfma_f32_16x16x32_bf16 v[32:35], v[112:115], v[144:147], v[32:35]\n\t" \
    "ds_read_b128 v[128:131], v251\n\t" \
    "ds_read_b64_tr_b16 v[0:1], v253 offset:8192\n\t" \
    "v_mfma_f32_16x16x32_bf16 v[40:43], v[112:115], v[148:151], v[40:43]\n\t" \
    "ds_read_b128 v[132:135], v251 offset:2048\n\t" \
    "ds_read_b64_tr_b16 v[2:3], v253 offset:8704\n\t" \
    "v_mfma_f32_16x16x32_bf16 v[48:51], v[112:115], v[152:155], v[48:51]\n\t" \
    "ds_read_b128 v[136:139], v251 offset:4096\n\t" \
    "v_mfma_f32_16x16x32_bf16 v[56:59], v[112:115], v[156:159], v[56:59]\n\t" \
    "ds_read_b128 v[140:143], v251 offset:6144\n\t" \
    "v_mfma_f32_16x16x32_bf16 v[36:39], v[116:119], v[144:147], v[36:39]\n\t" \
    "ds_read_b64_tr_b16 v[4:5], v254 offset:8192\n\t" \
    "v_mfma_f32_16x16x32_bf16 v[44:47], v[116:119], v[148:151], v[44:47]\n\t" \
    "ds_read_b64_tr_b16 v[6:7], v254 offset:8704\n\t" \
    "v_mfma_f32_16x16x32_bf16 v[52:55], v[116:119], v[152:155], v[52:55]\n\t" \
    "ds_read_b64_tr_b16 v[8:9], v253 offset:16384\n\t" \
    "v_mfma_f32_16x16x32_bf16 v[60:63], v[116:119], v[156:159], v[60:63]\n\t" \
    "ds_read_b64_tr_b16 v[10:11], v253 offset:16896\n\t" \
    "v_mfma_f32_16x16x32_bf16 v[80:83], v[144:147], v[120:123], v[80:83]\n\t" \
    "ds_read_b64_tr_b16 v[12:13], v254 offset:16384\n\t" \
    "v_mfma_f32_16x16x32_bf16 v[88:91], v[148:151], v[120:123], v[88:91]\n\t" \
    "ds_read_b64_tr_b16 v[14:15], v254 offset:16896\n\t" \
    "v_mfma_f32_16x16x32_bf16 v[96:99], v[152:155], v[120:123], v[96:99]\n\t" \
    "v_xor_b32 v252, 64, v251\n\t" \
    "v_mfma_f32_16x16x32_bf16 v[104:107], v[156:159], v[120:123], v[104:107]\n\t" \
    "s_lshr_b32 s83, s83, 8\n\t" \
    "v_mfma_f32_16x16x32_bf16 v[84:87], v[144:147], v[124:127], v[84:87]\n\t" \
    "s_lshr_b32 s84, s84, 8\n\t" \
    "v_mfma_f32_16x16x32_bf16 v[92:95], v[148:151], v[124:127], v[92:95]\n\t" \
    "v_mfma_f32_16x16x32_bf16 v[100:103], v[152:155], v[124:127], v[100:103]\n\t" \
    "v_mfma_f32_16x16x32_bf16 v[108:111], v[156:159], v[124:127], v[108:111]\n\t" \
    "s_cbranch_scc1 .Lab_l2\n\t" \
    "s_waitcnt lgkmcnt(0)\n\t" \
    "v_mfma_f32_16x16x32_bf16 v[32:35], v[0:3], v[128:131], v[32:35]\n\t" \
    "ds_read_b128 v[144:147], v252\n\t" \
    "ds_read_b64_tr_b16 v[112:113], v253 offset:12288\n\t" \
    "v_mfma_f32_16x16x32_bf16 v[40:43], v[0:3], v[132:135], v[40:43]\n\t" \
    "ds_read_b128 v[148:151], v252 offset:2048\n\t" \
    "ds_read_b64_tr_b16 v[114:115], v253 offset:12800\n\t" \
    "v_mfma_f32_16x16x32_bf16 v[48:51], v[0:3], v[136:139], v[48:51]\n\t" \
    "ds_read_b128 v[152:155], v252 offset:4096\n\t" \
    "ds_read_b64_tr_b16 v[116:117], v254 offset:12288\n\t" \
    "v_mfma_f32_16x16x32_bf16 v[56:59], v[0:3], v[140:143], v[56:59]\n\t" \
    "ds_read_b128 v[156:159], v252 offset:6144\n\t" \
    "ds_read_b64_tr_b16 v[118:119], v254 offset:12800\n\t" \
    "v_mfma_f32_16x16x32_bf16 v[36:39], v[4:7], v[128:131], v[36:39]\n\t" \
    "ds_read_b64_tr_b16 v[120:121], v253 offset:20480\n\t" \
    "ds_read_b64_tr_b16 v[122:123], v253 offset:20992\n\t" \
    "v_mov_b32 v237, 1\n\t" \
    "v_mfma_f32_16x16x32_bf16 v[44:47], v[4:7], v[132:135], v[44:47]\n\t" \
    "ds_read_b64_tr_b16 v[124:125], v254 offset:20480\n\t" \
    "ds_read_b64_tr_b16 v[126:127], v254 offset:20992\n\t" \
    "ds_write_b32 v248, v237\n\t" \
    "v_mfma_f32_16x16x32_bf16 v[52:55], v[4:7], v[136:139], v[52:55]\n\t" \
    "ds_read_b64_tr_b16 v[224:225], v245\n\t" \
    "ds_read_b64_tr_b16 v[226:227], v245 offset:1024\n\t" \
    "v_mfma_f32_16x16x32_bf16 v[60:63], v[4:7], v[140:143], v[60:63]\n\t" \
    "v_xor_b32 v245, 32, v245\n\t" \
    "ds_read_b32 v236, v249 offset:288\n\t" \
    "v_mfma_f32_16x16x32_bf16 v[80:83], v[128:131], v[8:11], v[80:83]\n\t" \
    "ds_read_b64_tr_b16 v[228:229], v245\n\t" \
    "ds_read_b64_tr_b16 v[230:231], v245 offset:1024\n\t" \
    "v_mfma_f32_16x16x32_bf16 v[88:91], v[132:135], v[8:11], v[88:91]\n\t" \
    "ds_read_b128 v[68:71], v249 offset:320\n\t" \
    "ds_read_b64 v[64:65], v249 offset:0\n\t" \
    "v_mfma_f32_16x16x32_bf16 v[96:99], v[136:139], v[8:11], v[96:99]\n\t" \
    "v_add_u32 v251, 131072, v240\n\t" \
    "v_mfma_f32_16x16x32_bf16 v[104:107], v[140:143], v[8:11], v[104:107]\n\t" \
    "v_xor_b32 v254, 32, v241\n\t" \
    "v_mfma_f32_16x16x32_bf16 v[84:87], v[128:131], v[12:15], v[84:87]\n\t" \
    "v_mov_b32 v253, v241\n\t" \
    "v_mfma_f32_16x16x32_bf16 v[92:95], v[132:135], v[12:15], v[92:95]\n\t" \
    "v_xor_b32 v252, 64, v251\n\t" \
    "v_mfma_f32_16x16x32_bf16 v[100:103], v[136:139], v[12:15], v[100:103]\n\t" \
    "v_and_b32 v245, 0x3ff, v242\n\t" \
    "v_mfma_f32_16x16x32_bf16 v[108:111], v[140:143], v[12:15], v[108:111]\n\t" \
    "v_add_u32 v245, %[zx], v245\n\t" \
    "s_waitcnt lgkmcnt(0)\n\t" \
    "v_mfma_f32_16x16x32_bf16 v[32:35], v[112:115], v[144:147], v[32:35]\n\t" \
    "v_min_u32 v64, v64, v65\n\t" \
    "v_mfma_f32_16x16x32_bf16 v[40:43], v[112:115], v[148:151], v[40:43]\n\t" \
    "v_readfirstlane_b32 s85, v64\n\t" \
    "s_cmp_lt_u32 s85, 1\n\t" \
    "s_cbranch_scc1 .Lab_s1\n\t" \
    ".Lab_s1r:\n\t" \
    "v_mfma_f32_16x16x32_bf16 v[48:51], v[112:115], v[152:155], v[48:51]\n\t" \
    "ds_read_b128 v[160:163], v251\n\t" \
    "ds_read_b64_tr_b16 v[0:1], v253 offset:8192\n\t" \
    "v_mfma_f32_16x16x32_bf16 v[56:59], v[112:115], v[156:159], v[56:59]\n\t" \
    "ds_read_b128 v[164:167], v251 offset:2048\n\t" \
    "ds_read_b64_tr_b16 v[2:3], v253 offset:8704\n\t" \
    "v_mfma_f32_16x16x32_bf16 v[36:39], v[116:119], v[144:147], v[36:39]\n\t" \
    "ds_read_b128 v[168:171], v251 offset:4096\n\t" \
    "ds_read_b64_tr_b16 v[4:5], v254 offset:8192\n\t" \
    "v_mfma_f32_16x16x32_bf16 v[44:47], v[116:119], v[148:151], v[44:47]\n\t" \
    "ds_read_b128 v[172:175], v251 offset:6144\n\t" \
    "ds_read_b64_tr_b16 v[6:7], v254 offset:8704\n\t" \
    "v_mfma_f32_16x16x32_bf16 v[52:55], v[116:119], v[152:155], v[52:55]\n\t" \
    "ds_read_b64_tr_b16 v[8:9], v253 offset:16384\n\t" \
    "ds_read_b64_tr_b16 v[10:11], v253 offset:16896\n\t" \
    "v_mfma_f32_16x16x32_bf16 v[60:63], v[116:119], v[156:159], v[60:63]\n\t" \
    "ds_read_b64_tr_b16 v[12:13], v254 offset:16384\n\t" \
    "ds_read_b64_tr_b16 v[14:15], v254 offset:16896\n\t" \
    "v_mfma_f32_16x16x32_bf16 v[80:83], v[144:147], v[120:123], v[80:83]\n\t" \
    "v_readfirstlane_b32 s87, v68\n\t" \
    "v_mfma_f32_16x16x32_bf16 v[88:91], v[148:151], v[120:123], v[88:91]\n\t" \
    "v_readfirstlane_b32 s88, v69\n\t" \
    "v_mfma_f32_16x16x32_bf16 v[96:99], v[152:155], v[120:123], v[96:99]\n\t" \
    "v_readfirstlane_b32 %[sof], v70\n\t" \
    "v_mfma_f32_16x16x32_bf16 v[104:107], v[156:159], v[120:123], v[104:107]\n\t" \
    "s_xor_b32 s87, s87, %[kpx]\n\t" \
    "s_add_u32 s88, s88, %[kma]\n\t" \
    "v_mfma_f32_16x16x32_bf16 v[84:87], v[144:147], v[124:127], v[84:87]\n\t" \
    "s_add_u32 s87, s87, %[kpa]\n\t" \
    "v_mfma_f32_16x16x32_bf16 v[92:95], v[148:151], v[124:127], v[92:95]\n\t" \
    "s_xor_b32 s86, s87, 0x8000\n\t" \
    "v_mfma_f32_16x16x32_bf16 v[100:103], v[152:155], v[124:127], v[100:103]\n\t" \
    "v_mfma_f32_16x16x32_bf16 v[108:111], v[156:159], v[124:127], v[108:111]\n\t" \
    "s_waitcnt lgkmcnt(0)\n\t" \
    "v_mfma_f32_16x16x32_bf16 v[32:35], v[0:3], v[160:163], v[32:35]\n\t" \
    "ds_read_b128 v[176:179], v252\n\t" \
    "ds_read_b64_tr_b16 v[112:113], v253 offset:12288\n\t" \
    "v_mfma_f32_16x16x32_bf16 v[40:43], v[0:3], v[164:167], v[40:43]\n\t" \
    "ds_read_b128 v[180:183], v252 offset:2048\n\t" \
    "ds_read_b64_tr_b16 v[114:115], v253 offset:12800\n\t" \
    "v_mfma_f32_16x16x32_bf16 v[48:51], v[0:3], v[168:171], v[48:51]\n\t" \
    "ds_read_b128 v[184:187], v252 offset:4096\n\t" \
    "v_mov_b32 v237, 2\n\t" \
    "v_mfma_f32_16x16x32_bf16 v[56:59], v[0:3], v[172:175], v[56:59]\n\t" \
    "ds_read_b128 v[188:191], v252 offset:6144\n\t" \
    "v_mfma_f32_16x16x32_bf16 v[36:39], v[4:7], v[160:163], v[36:39]\n\t" \
    "ds_read_b64_tr_b16 v[116:117], v254 offset:12288\n\t" \
    "v_mfma_f32_16x16x32_bf16 v[44:47], v[4:7], v[164:167], v[44:47]\n\t" \
    "ds_read_b64_tr_b16 v[118:119], v254 offset:12800\n\t" \
    "v_mfma_f32_16x16x32_bf16 v[52:55], v[4:7], v[168:171], v[52:55]\n\t" \
    "ds_read_b64_tr_b16 v[120:121], v253 offset:20480\n\t" \
    "v_mfma_f32_16x16x32_bf16 v[60:63], v[4:7], v[172:175], v[60:63]\n\t" \
    "ds_read_b64_tr_b16 v[122:123], v253 offset:20992\n\t" \
    "v_mfma_f32_16x16x32_bf16 v[80:83], v[160:163], v[8:11], v[80:83]\n\t" \
    "ds_read_b64_tr_b16 v[124:125], v254 offset:20480\n\t" \
    "v_mfma_f32_16x16x32_bf16 v[88:91], v[164:167], v[8:11], v[88:91]\n\t" \
    "ds_read_b64_tr_b16 v[126:127], v254 offset:20992\n\t" \
    "v_mfma_f32_16x16x32_bf16 v[96:99], v[168:171], v[8:11], v[96:99]\n\t" \
    "ds_write_b32 v248, v237\n\t" \
    "v_mfma_f32_16x16x32_bf16 v[104:107], v[172:175], v[8:11], v[104:107]\n\t" \
    "v_xor_b32 v224, %[neg], v224\n\t" \
    "v_xor_b32 v225, %[neg], v225\n\t" \
    "v_mfma_f32_16x16x32_bf16 v[84:87], v[160:163], v[12:15], v[84:87]\n\t" \
    "v_xor_b32 v226, %[neg], v226\n\t" \
    "v_xor_b32 v227, %[neg], v227\n\t" \
    "v_mfma_f32_16x16x32_bf16 v[92:95], v[164:167], v[12:15], v[92:95]\n\t" \
    "v_xor_b32 v228, %[neg], v228\n\t" \
    "v_xor_b32 v229, %[neg], v229\n\t" \
    "v_mfma_f32_16x16x32_bf16 v[100:103], v[168:171], v[12:15], v[100:103]\n\t" \
    "v_xor_b32 v230, %[neg], v230\n\t" \
    "v_xor_b32 v231, %[neg], v231\n\t" \
    "v_mfma_f32_16x16x32_bf16 v[108:111], v[172:175], v[12:15], v[108:111]\n\t" \
    "s_setprio 3\n\t" \
    "s_waitcnt lgkmcnt(0)\n\t" \
    "v_mfma_f32_16x16x32_bf16 v[32:35], v[112:115], v[176:179], v[32:35]\n\t" \
    "v_mfma_f32_16x16x32_bf16 v[36:39], v[116:119], v[176:179], v[36:39]\n\t" \
    "v_mfma_f32_16x16x32_bf16 v[40:43], v[112:115], v[180:183], v[40:43]\n\t" \
    "v_mfma_f32_16x16x32_bf16 v[44:47], v[116:119], v[180:183], v[44:47]\n\t" \
    "ds_read_b128 v[28:31], v249 offset:16\n\t" \
    "v_mfma_f32_16x16x32_bf16 v[48:51], v[112:115], v[184:187], v[48:51]\n\t" \
    "v_mfma_f32_16x16x32_bf16 v[52:55], v[116:119], v[184:187], v[52:55]\n\t" \
    "v_mfma_f32_16x16x32_bf16 v[56:59], v[112:115], v[188:191], v[56:59]\n\t" \
    "v_mfma_f32_16x16x32_bf16 v[60:63], v[116:119], v[188:191], v[60:63]\n\t" \
    "v_cvt_pk_bf16_f32 v64, v32, v33\n\t" \
    "v_cvt_pk_bf16_f32 v65, v34, v35\n\t" \
    "v_mfma_f32_16x16x32_bf16 v[80:83], v[176:179], v[120:123], v[80:83]\n\t" \
    "v_cvt_pk_bf16_f32 v66, v36, v37\n\t" \
    "v_cvt_pk_bf16_f32 v67, v38, v39\n\t" \
    "v_mfma_f32_16x16x32_bf16 v[88:91], v[180:183], v[120:123], v[88:91]\n\t" \
    "v_cvt_pk_bf16_f32 v68, v40, v41\n\t" \
    "v_cvt_pk_bf16_f32 v69, v42, v43\n\t" \
    "v_mfma_f32_16x16x32_bf16 v[84:87], v[176:179], v[124:127], v[84:87]\n\t" \
    "v_cvt_pk_bf16_f32 v70, v44, v45\n\t" \
    "v_cvt_pk_bf16_f32 v71, v46, v47\n\t" \
    "v_mfma_f32_16x16x32_bf16 v[92:95], v[180:183], v[124:127], v[92:95]\n\t" \
    "v_cvt_pk_bf16_f32 v72, v48, v49\n\t" \
    "v_cvt_pk_bf16_f32 v73, v50, v51\n\t" \
    "v_mfma_f32_16x16x32_bf16 v[96:99], v[184:187], v[120:123], v[96:99]\n\t" \
    "v_cvt_pk_bf16_f32 v74, v52, v53\n\t" \
    "v_cvt_pk_bf16_f32 v75, v54, v55\n\t" \
    "v_mfma_f32_16x16x32_bf16 v[104:107], v[188:191], v[120:123], v[104:107]\n\t" \
    "v_cvt_pk_bf16_f32 v76, v56, v57\n\t" \
    "v_cvt_pk_bf16_f32 v77, v58, v59\n\t" \
    "v_mfma_f32_16x16x32_bf16 v[100:103], v[184:187], v[124:127], v[100:103]\n\t" \
    "v_cvt_pk_bf16_f32 v78, v60, v61\n\t" \
    "v_cvt_pk_bf16_f32 v79, v62, v63\n\t" \
    "v_mfma_f32_16x16x32_bf16 v[108:111], v[188:191], v[124:127], v[108:111]\n\t" \
    "s_waitcnt lgkmcnt(0)\n\t" \
    "v_min_u32 v238, v28, v29\n\t" \
    "v_min3_u32 v238, v238, v30, v31\n\t" \
    "v_mfma_f32_16x16x32_bf16 v[0:3], v[224:227], v[64:67], 0\n\t" \
    "v_cvt_pk_bf16_f32 v112, v80, v81\n\t" \
    "v_cvt_pk_bf16_f32 v113, v82, v83\n\t" \
    "v_mfma_f32_16x16x32_bf16 v[4:7], v[228:231], v[64:67], 0\n\t" \
    "v_cvt_pk_bf16_f32 v114, v88, v89\n\t" \
    "v_cvt_pk_bf16_f32 v115, v90, v91\n\t" \
    "v_mfma_f32_16x16x32_bf16 v[8:11], v[224:227], v[68:71], 0\n\t" \
    "v_cvt_pk_bf16_f32 v116, v84, v85\n\t" \
    "v_cvt_pk_bf16_f32 v117, v86, v87\n\t" \
    "v_mfma_f32_16x16x32_bf16 v[12:15], v[228:231], v[68:71], 0\n\t" \
    "v_cvt_pk_bf16_f32 v118, v92, v93\n\t" \
    "v_cvt_pk_bf16_f32 v119, v94, v95\n\t" \
    "v_mfma_f32_16x16x32_bf16 v[16:19], v[224:227], v[72:75], 0\n\t" \
    "v_cvt_pk_bf16_f32 v120, v96, v97\n\t" \
    "v_cvt_pk_bf16_f32 v121, v98, v99\n\t" \
    "v_mfma_f32_16x16x32_bf16 v[20:23], v[228:231], v[72:75], 0\n\t" \
    "v_cvt_pk_bf16_f32 v122, v104, v105\n\t" \
    "v_cvt_pk_bf16_f32 v123, v106, v107\n\t" \
    "v_mfma_f32_16x16x32_bf16 v[24:27], v[224:227], v[76:79], 0\n\t" \
    "v_cvt_pk_bf16_f32 v124, v100, v101\n\t" \
    "v_cvt_pk_bf16_f32 v125, v102, v103\n\t" \
    "v_mfma_f32_16x16x32_bf16 v[28:31], v[228:231], v[76:79], 0\n\t" \
    "v_cvt_pk_bf16_f32 v126, v108, v109\n\t" \
    "v_cvt_pk_bf16_f32 v127, v110, v111\n\t" \
    "v_readfirstlane_b32 s85, v238\n\t" \
    "s_cmp_lt_u32 s85, 2\n\t" \
    "s_cbranch_scc1 .Lab_s2\n\t" \
    ".Lab_s2r:\n\t" \
    "v_mov_b32 v104, 0\n\t" \
    "v_mov_b32 v105, 0\n\t" \
    "v_mov_b32 v106, v243\n\t" \
    "v_mov_b32 v107, v244\n\t" \
    "v_mov_b32 v108, 0\n\t" \
    "v_mov_b32 v109, 0\n\t" \
    "v_max3_f32 v64, v0, v1, v2\n\t" \
    "v_max3_f32 v66, -v0, -v1, -v2\n\t" \
    "v_max3_f32 v68, v8, v9, v10\n\t" \
    "v_max3_f32 v64, v64, v3, v4\n\t" \
    "v_max3_f32 v66, v66, -v3, -v4\n\t" \
    "v_max3_f32 v68, v68, v11, v12\n\t" \
    "v_max3_f32 v64, v64, v5, v6\n\t" \
    "v_max3_f32 v66, v66, -v5, -v6\n\t" \
    "v_max3_f32 v68, v68, v13, v14\n\t" \
    "v_max_f32_e64 v64, v64, v7\n\t" \
    "v_max_f32_e64 v66, v66, -v7\n\t" \
    "v_max_f32_e64 v68, v68, v15\n\t" \
    "v_max3_f32 v70, -v8, -v9, -v10\n\t" \
    "v_permlane32_swap_b32 v64, v66\n\t" \
    "v_max3_f32 v72, v16, v17, v18\n\t" \
    "v_max3_f32 v70, v70, -v11, -v12\n\t" \
    "v_max_f32 v64, v64, v66\n\t" \
    "v_max3_f32 v72, v72, v19, v20\n\t" \
    "v_max3_f32 v70, v70, -v13, -v14\n\t" \
    "v_max3_f32 v74, -v16, -v17, -v18\n\t" \
    "v_max3_f32 v72, v72, v21, v22\n\t" \
    "v_max_f32_e64 v70, v70, -v15\n\t" \
    "v_max3_f32 v74, v74, -v19, -v20\n\t" \
    "v_max_f32_e64 v72, v72, v23\n\t" \
    "v_permlane32_swap_b32 v68, v70\n\t" \
    "v_max3_f32 v74, v74, -v21, -v22\n\t" \
    "v_max3_f32 v76, v24, v25, v26\n\t" \
    "v_max_f32 v68, v68, v70\n\t" \
    "v_max_f32_e64 v74, v74, -v23\n\t" \
    "v_max3_f32 v76, v76, v27, v28\n\t" \
    "v_permlane16_swap_b32 v64, v68\n\t" \
    "v_permlane32_swap_b32 v72, v74\n\t" \
    "v_max3_f32 v76, v76, v29, v30\n\t" \
    "v_max_f32 v66, v64, v68\n\t" \
    "v_max_f32 v72, v72, v74\n\t" \
    "v_max_f32_e64 v76, v76, v31\n\t" \
    "v_mul_f32 v64, 0xbfb8aa3b, v66\n\t" \
    "v_mul_f32 v68, 0xbfb8aa3b, v66\n\t" \
    "v_max3_f32 v78, -v24, -v25, -v26\n\t" \
    "v_max3_f32 v78, v78, -v27, -v28\n\t" \
    "v_permlane16_swap_b32 v64, v68\n\t" \
    "v_mov_b32 v66, v64\n\t" \
    "v_max3_f32 v78, v78, -v29, -v30\n\t" \
    "v_mov_b32 v70, v68\n\t" \
    "v_permlane32_swap_b32 v64, v66\n\t" \
    "v_max_f32_e64 v78, v78, -v31\n\t" \
    "v_permlane32_swap_b32 v68, v70\n\t" \
    "v_pk_fma_f32 v[32:33], v[0:1], s[90:91], v[64:65] op_sel_hi:[1,1,0]\n\t" \
    "v_permlane32_swap_b32 v76, v78\n\t" \
    "v_pk_fma_f32 v[0:1], v[0:1], s[90:91], v[66:67] op_sel_hi:[1,1,0] neg_lo:[1,0,0] neg_hi:[1,0,0]\n\t" \
    "v_pk_fma_f32 v[34:35], v[2:3], s[90:91], v[64:65] op_sel_hi:[1,1,0]\n\t" \
    "v_max_f32 v76, v76, v78\n\t" \
    "v_pk_fma_f32 v[2:3], v[2:3], s[90:91], v[66:67] op_sel_hi:[1,1,0] neg_lo:[1,0,0] neg_hi:[1,0,0]\n\t" \
    "v_pk_fma_f32 v[36:37], v[4:5], s[90:91], v[64:65] op_sel_hi:[1,1,0]\n\t" \
    "v_permlane16_swap_b32 v72, v76\n\t" \
    "v_pk_fma_f32 v[4:5], v[4:5], s[90:91], v[66:67] op_sel_hi:[1,1,0] neg_lo:[1,0,0] neg_hi:[1,0,0]\n\t" \
    "v_pk_fma_f32 v[38:39], v[6:7], s[90:91], v[64:65] op_sel_hi:[1,1,0]\n\t" \
    "v_max_f32 v74, v72, v76\n\t" \
    "v_pk_fma_f32 v[6:7], v[6:7], s[90:91], v[66:67] op_sel_hi:[1,1,0] neg_lo:[1,0,0] neg_hi:[1,0,0]\n\t" \
    "v_exp_f32 v32, v32\n\t" \
    "v_mul_f32 v72, 0xbfb8aa3b, v74\n\t" \
    "v_mul_f32 v76, 0xbfb8aa3b, v74\n\t" \
    "v_exp_f32 v0, v0\n\t" \
    "v_exp_f32 v33, v33\n\t" \
    "v_permlane16_swap_b32 v72, v76\n\t" \
    "v_exp_f32 v1, v1\n\t" \
    "v_exp_f32 v34, v34\n\t" \
    "v_mov_b32 v74, v72\n\t" \
    "v_mov_b32 v78, v76\n\t" \
    "v_exp_f32 v2, v2\n\t" \
    "v_permlane32_swap_b32 v72, v74\n\t" \
    "v_permlane32_swap_b32 v76, v78\n\t" \
    "v_exp_f32 v35, v35\n\t" \
    "v_exp_f32 v3, v3\n\t" \
    "v_exp_f32 v36, v36\n\t" \
    "v_pk_add_f32 v[64:65], v[32:33], v[34:35]\n\t" \
    "v_pk_add_f32 v[66:67], v[0:1], v[2:3]\n\t" \
    "v_exp_f32 v4, v4\n\t" \
    "v_exp_f32 v37, v37\n\t" \
    "v_exp_f32 v5, v5\n\t" \
    "v_exp_f32 v38, v38\n\t" \
    "v_pk_add_f32 v[64:65], v[64:65], v[36:37]\n\t" \
    "v_pk_add_f32 v[66:67], v[66:67], v[4:5]\n\t" \
    "v_exp_f32 v6, v6\n\t" \
    "v_exp_f32 v39, v39\n\t" \
    "v_exp_f32 v7, v7\n\t" \
    "v_pk_fma_f32 v[40:41], v[8:9], s[90:91], v[68:69] op_sel_hi:[1,1,0]\n\t" \
    "v_pk_add_f32 v[64:65], v[64:65], v[38:39]\n\t" \
    "v_pk_add_f32 v[66:67], v[66:67], v[6:7]\n\t" \
    "v_pk_fma_f32 v[8:9], v[8:9], s[90:91], v[70:71] op_sel_hi:[1,1,0] neg_lo:[1,0,0] neg_hi:[1,0,0]\n\t" \
    "v_add_f32 v64, v64, v65\n\t" \
    "v_add_f32 v66, v66, v67\n\t" \
    "v_pk_fma_f32 v[42:43], v[10:11], s[90:91], v[68:69] op_sel_hi:[1,1,0]\n\t" \
    "v_pk_fma_f32 v[10:11], v[10:11], s[90:91], v[70:71] op_sel_hi:[1,1,0] neg_lo:[1,0,0] neg_hi:[1,0,0]\n\t" \
    "v_permlane32_swap_b32 v64, v66\n\t" \
    "v_pk_fma_f32 v[44:45], v[12:13], s[90:91], v[68:69] op_sel_hi:[1,1,0]\n\t" \
    "v_pk_fma_f32 v[12:13], v[12:13], s[90:91], v[70:71] op_sel_hi:[1,1,0] neg_lo:[1,0,0] neg_hi:[1,0,0]\n\t" \
    "v_add_f32 v64, v64, v66\n\t" \
    "v_pk_fma_f32 v[46:47], v[14:15], s[90:91], v[68:69] op_sel_hi:[1,1,0]\n\t" \
    "v_pk_fma_f32 v[14:15], v[14:15], s[90:91], v[70:71] op_sel_hi:[1,1,0] neg_lo:[1,0,0] neg_hi:[1,0,0]\n\t" \
    "v_exp_f32 v40, v40\n\t" \
    "v_exp_f32 v8, v8\n\t" \
    "v_exp_f32 v41, v41\n\t" \
    "v_exp_f32 v9, v9\n\t" \
    "v_exp_f32 v42, v42\n\t" \
    "v_exp_f32 v10, v10\n\t" \
    "v_exp_f32 v43, v43\n\t" \
    "v_exp_f32 v11, v11\n\t" \
    "v_exp_f32 v44, v44\n\t" \
    "v_pk_add_f32 v[68:69], v[40:41], v[42:43]\n\t" \
    "v_pk_add_f32 v[70:71], v[8:9], v[10:11]\n\t" \
    "v_exp_f32 v12, v12\n\t" \
    "v_exp_f32 v45, v45\n\t" \
    "v_exp_f32 v13, v13\n\t" \
    "v_exp_f32 v46, v46\n\t" \
    "v_pk_add_f32 v[68:69], v[68:69], v[44:45]\n\t" \
    "v_pk_add_f32 v[70:71], v[70:71], v[12:13]\n\t" \
    "v_exp_f32 v14, v14\n\t" \
    "v_exp_f32 v47, v47\n\t" \
    "v_exp_f32 v15, v15\n\t" \
    "v_pk_fma_f32 v[48:49], v[16:17], s[90:91], v[72:73] op_sel_hi:[1,1,0]\n\t" \
    "v_pk_add_f32 v[68:69], v[68:69], v[46:47]\n\t" \
    ".Lpad_ab1:\n\t" \
    "s_cbranch_execz .Lpad_ret\n\t" \
    "v_pk_add_f32 v[70:71], v[70:71], v[14:15]\n\t" \
    "v_pk_fma_f32 v[16:17], v[16:17], s[90:91], v[74:75] op_sel_hi:[1,1,0] neg_lo:[1,0,0] neg_hi:[1,0,0]\n\t" \
    "v_add_f32 v68, v68, v69\n\t" \
    "v_add_f32 v70, v70, v71\n\t" \
    "v_pk_fma_f32 v[50:51], v[18:19], s[90:91], v[72:73] op_sel_hi:[1,1,0]\n\t" \
    "v_pk_fma_f32 v[18:19], v[18:19], s[90:91], v[74:75] op_sel_hi:[1,1,0] neg_lo:[1,0,0] neg_hi:[1,0,0]\n\t" \
    "v_permlane32_swap_b32 v68, v70\n\t" \
    "v_pk_fma_f32 v[52:53], v[20:21], s[90:91], v[72:73] op_sel_hi:[1,1,0]\n\t" \
    "v_pk_fma_f32 v[20:21], v[20:21], s[90:91], v[74:75] op_sel_hi:[1,1,0] neg_lo:[1,0,0] neg_hi:[1,0,0]\n\t" \
    "v_add_f32 v68, v68, v70\n\t" \
    "v_pk_fma_f32 v[54:55], v[22:23], s[90:91], v[72:73] op_sel_hi:[1,1,0]\n\t" \
    "v_pk_fma_f32 v[22:23], v[22:23], s[90:91], v[74:75] op_sel_hi:[1,1,0] neg_lo:[1,0,0] neg_hi:[1,0,0]\n\t" \
    "v_permlane16_swap_b32 v64, v68\n\t" \
    "v_exp_f32 v48, v48\n\t" \
    "v_exp_f32 v16, v16\n\t" \
    "v_add_f32 v66, v64, v68\n\t" \
    "v_exp_f32 v49, v49\n\t" \
    "v_exp_f32 v17, v17\n\t" \
    "v_rcp_f32 v64, v66\n\t" \
    "v_exp_f32 v50, v50\n\t" \
    "v_exp_f32 v18, v18\n\t" \
    "v_mov_b32 v68, v64\n\t" \
    "v_exp_f32 v51, v51\n\t" \
    "v_exp_f32 v19, v19\n\t" \
    "v_permlane16_swap_b32 v64, v68\n\t" \
    "v_pk_add_f32 v[72:73], v[48:49], v[50:51]\n\t" \
    "v_pk_add_f32 v[74:75], v[16:17], v[18:19]\n\t" \
    "v_mov_b32 v66, v64\n\t" \
    "v_mov_b32 v70, v68\n\t" \
    "v_exp_f32 v52, v52\n\t" \
    "v_permlane32_swap_b32 v64, v66\n\t" \
    "v_permlane32_swap_b32 v68, v70\n\t" \
    "v_exp_f32 v20, v20\n\t" \
    "v_exp_f32 v53, v53\n\t" \
    "v_exp_f32 v21, v21\n\t" \
    "v_exp_f32 v54, v54\n\t" \
    "v_pk_add_f32 v[72:73], v[72:73], v[52:53]\n\t" \
    "v_pk_add_f32 v[74:75], v[74:75], v[20:21]\n\t" \
    "v_exp_f32 v22, v22\n\t" \
    "v_exp_f32 v55, v55\n\t" \
    "v_exp_f32 v23, v23\n\t" \
    "v_pk_fma_f32 v[56:57], v[24:25], s[90:91], v[76:77] op_sel_hi:[1,1,0]\n\t" \
    "v_pk_add_f32 v[72:73], v[72:73], v[54:55]\n\t" \
    "v_pk_add_f32 v[74:75], v[74:75], v[22:23]\n\t" \
    "v_pk_fma_f32 v[24:25], v[24:25], s[90:91], v[78:79] op_sel_hi:[1,1,0] neg_lo:[1,0,0] neg_hi:[1,0,0]\n\t" \
    "v_add_f32 v72, v72, v73\n\t" \
    "v_add_f32 v74, v74, v75\n\t" \
    "v_pk_fma_f32 v[58:59], v[26:27], s[90:91], v[76:77] op_sel_hi:[1,1,0]\n\t" \
    "v_pk_fma_f32 v[26:27], v[26:27], s[90:91], v[78:79] op_sel_hi:[1,1,0] neg_lo:[1,0,0] neg_hi:[1,0,0]\n\t" \
    "v_permlane32_swap_b32 v72, v74\n\t" \
    "v_pk_fma_f32 v[60:61], v[28:29], s[90:91], v[76:77] op_sel_hi:[1,1,0]\n\t" \
    "v_pk_fma_f32 v[28:29], v[28:29], s[90:91], v[78:79] op_sel_hi:[1,1,0] neg_lo:[1,0,0] neg_hi:[1,0,0]\n\t" \
    "v_add_f32 v72, v72, v74\n\t" \
    "v_pk_fma_f32 v[62:63], v[30:31], s[90:91], v[76:77] op_sel_hi:[1,1,0]\n\t" \
    "v_pk_fma_f32 v[30:31], v[30:31], s[90:91], v[78:79] op_sel_hi:[1,1,0] neg_lo:[1,0,0] neg_hi:[1,0,0]\n\t" \
    "v_exp_f32 v56, v56\n\t" \
    "v_exp_f32 v24, v24\n\t" \
    "v_exp_f32 v57, v57\n\t" \
    "v_exp_f32 v25, v25\n\t" \
    "v_exp_f32 v58, v58\n\t" \
    "v_exp_f32 v26, v26\n\t" \
    "v_exp_f32 v59, v59\n\t" \
    "v_exp_f32 v27, v27\n\t" \
    "v_exp_f32 v60, v60\n\t" \
    "v_pk_add_f32 v[76:77], v[56:57], v[58:59]\n\t" \
    "v_pk_add_f32 v[78:79], v[24:25], v[26:27]\n\t" \
    "v_exp_f32 v28, v28\n\t" \
    "v_exp_f32 v61, v61\n\t" \
    "v_exp_f32 v29, v29\n\t" \
    "v_exp_f32 v62, v62\n\t" \
    "v_pk_add_f32 v[76:77], v[76:77], v[60:61]\n\t" \
    "v_pk_add_f32 v[78:79], v[78:79], v[28:29]\n\t" \
    "v_exp_f32 v30, v30\n\t" \
    "v_exp_f32 v63, v63\n\t" \
    "v_exp_f32 v31, v31\n\t" \
    "v_pk_mul_f32 v[0:1], v[0:1], v[66:67] op_sel_hi:[1,0]\n\t" \
    "v_pk_add_f32 v[76:77], v[76:77], v[62:63]\n\t" \
    "v_pk_add_f32 v[78:79], v[78:79], v[30:31]\n\t" \
    "v_pk_mul_f32 v[32:33], v[32:33], v[64:65] op_sel_hi:[1,0]\n\t" \
    "v_add_f32 v76, v76, v77\n\t" \
    "v_add_f32 v78, v78, v79\n\t" \
    "v_pk_mul_f32 v[2:3], v[2:3], v[66:67] op_sel_hi:[1,0]\n\t" \
    "v_pk_mul_f32 v[34:35], v[34:35], v[64:65] op_sel_hi:[1,0]\n\t" \
    "v_permlane32_swap_b32 v76, v78\n\t" \
    "v_pk_mul_f32 v[4:5], v[4:5], v[66:67] op_sel_hi:[1,0]\n\t" \
    "v_pk_mul_f32 v[36:37], v[36:37], v[64:65] op_sel_hi:[1,0]\n\t" \
    "v_add_f32 v76, v76, v78\n\t" \
    "v_pk_mul_f32 v[6:7], v[6:7], v[66:67] op_sel_hi:[1,0]\n\t" \
    "v_pk_mul_f32 v[38:39], v[38:39], v[64:65] op_sel_hi:[1,0]\n\t" \
    "v_permlane16_swap_b32 v72, v76\n\t" \
    "v_cvt_pk_bf16_f32 v7, v6, v7\n\t" \
    "v_cvt_pk_bf16_f32 v6, v4, v5\n\t" \
    "v_add_f32 v74, v72, v76\n\t" \
    "v_cvt_pk_bf16_f32 v5, v2, v3\n\t" \
    "v_cvt_pk_bf16_f32 v4, v0, v1\n\t" \
    "v_rcp_f32 v72, v74\n\t" \
    "v_pk_mul_f32 v[8:9], v[8:9], v[70:71] op_sel_hi:[1,0]\n\t" \
    "v_pk_mul_f32 v[40:41], v[40:41], v[68:69] op_sel_hi:[1,0]\n\t" \
    "v_mov_b32 v76, v72\n\t" \
    "v_pk_mul_f32 v[10:11], v[10:11], v[70:71] op_sel_hi:[1,0]\n\t" \
    "v_pk_mul_f32 v[42:43], v[42:43], v[68:69] op_sel_hi:[1,0]\n\t" \
    "v_permlane16_swap_b32 v72, v76\n\t" \
    "v_pk_mul_f32 v[12:13], v[12:13], v[70:71] op_sel_hi:[1,0]\n\t" \
    "v_pk_mul_f32 v[44:45], v[44:45], v[68:69] op_sel_hi:[1,0]\n\t" \
    "v_mov_b32 v74, v72\n\t" \
    "v_mov_b32 v78, v76\n\t" \
    "v_pk_mul_f32 v[14:15], v[14:15], v[70:71] op_sel_hi:[1,0]\n\t" \
    "v_permlane32_swap_b32 v72, v74\n\t" \
    "v_permlane32_swap_b32 v76, v78\n\t" \
    "v_pk_mul_f32 v[46:47], v[46:47], v[68:69] op_sel_hi:[1,0]\n\t" \
    "v_cvt_pk_bf16_f32 v15, v14, v15\n\t" \
    "v_cvt_pk_bf16_f32 v14, v12, v13\n\t" \
    "v_cvt_pk_bf16_f32 v13, v10, v11\n\t" \
    "v_cvt_pk_bf16_f32 v12, v8, v9\n\t" \
    "v_pk_mul_f32 v[16:17], v[16:17], v[74:75] op_sel_hi:[1,0]\n\t" \
    "v_pk_mul_f32 v[48:49], v[48:49], v[72:73] op_sel_hi:[1,0]\n\t" \
    "v_pk_mul_f32 v[18:19], v[18:19], v[74:75] op_sel_hi:[1,0]\n\t" \
    "v_pk_mul_f32 v[50:51], v[50:51], v[72:73] op_sel_hi:[1,0]\n\t" \
    "v_pk_mul_f32 v[20:21], v[20:21], v[74:75] op_sel_hi:[1,0]\n\t" \
    "v_pk_mul_f32 v[52:53], v[52:53], v[72:73] op_sel_hi:[1,0]\n\t" \
    "v_pk_mul_f32 v[22:23], v[22:23], v[74:75] op_sel_hi:[1,0]\n\t" \
    "v_pk_mul_f32 v[54:55], v[54:55], v[72:73] op_sel_hi:[1,0]\n\t" \
    "v_pk_mul_f32 v[24:25], v[24:25], v[78:79] op_sel_hi:[1,0]\n\t" \
    "v_cvt_pk_bf16_f32 v23, v22, v23\n\t" \
    "v_cvt_pk_bf16_f32 v22, v20, v21\n\t" \
    "v_cvt_pk_bf16_f32 v21, v18, v19\n\t" \
    "v_cvt_pk_bf16_f32 v20, v16, v17\n\t" \
    "v_pk_mul_f32 v[56:57], v[56:57], v[76:77] op_sel_hi:[1,0]\n\t" \
    "v_pk_mul_f32 v[26:27], v[26:27], v[78:79] op_sel_hi:[1,0]\n\t" \
    "v_pk_mul_f32 v[58:59], v[58:59], v[76:77] op_sel_hi:[1,0]\n\t" \
    "v_pk_mul_f32 v[28:29], v[28:29], v[78:79] op_sel_hi:[1,0]\n\t" \
    "v_pk_mul_f32 v[60:61], v[60:61], v[76:77] op_sel_hi:[1,0]\n\t" \
    "v_pk_mul_f32 v[30:31], v[30:31], v[78:79] op_sel_hi:[1,0]\n\t" \
    "v_pk_mul_f32 v[62:63], v[62:63], v[76:77] op_sel_hi:[1,0]\n\t" \
    "v_cvt_pk_bf16_f32 v31, v30, v31\n\t" \
    "v_cvt_pk_bf16_f32 v30, v28, v29\n\t" \
    "v_cvt_pk_bf16_f32 v29, v26, v27\n\t" \
    "v_cvt_pk_bf16_f32 v28, v24, v25\n\t" \
    "v_mfma_f32_16x16x32_bf16 v[80:83], v[4:7], v[106:109], 0\n\t" \
    "v_cvt_pk_bf16_f32 v0, v32, v33\n\t" \
    "v_cvt_pk_bf16_f32 v1, v34, v35\n\t" \
    "v_mfma_f32_16x16x32_bf16 v[84:87], v[12:15], v[106:109], 0\n\t" \
    "v_cvt_pk_bf16_f32 v2, v36, v37\n\t" \
    "v_cvt_pk_bf16_f32 v3, v38, v39\n\t" \
    "v_mfma_f32_16x16x32_bf16 v[88:91], v[4:7], v[104:107], 0\n\t" \
    "v_cvt_pk_bf16_f32 v8, v40, v41\n\t" \
    "v_cvt_pk_bf16_f32 v9, v42, v43\n\t" \
    "v_mfma_f32_16x16x32_bf16 v[92:95], v[12:15], v[104:107], 0\n\t" \
    "v_cvt_pk_bf16_f32 v10, v44, v45\n\t" \
    "v_cvt_pk_bf16_f32 v11, v46, v47\n\t" \
    "v_mfma_f32_16x16x32_bf16 v[64:67], v[20:23], v[106:109], 0\n\t" \
    "v_cvt_pk_bf16_f32 v16, v48, v49\n\t" \
    "v_cvt_pk_bf16_f32 v17, v50, v51\n\t" \
    "v_mfma_f32_16x16x32_bf16 v[68:71], v[28:31], v[106:109], 0\n\t" \
    "v_cvt_pk_bf16_f32 v18, v52, v53\n\t" \
    "v_cvt_pk_bf16_f32 v19, v54, v55\n\t" \
    "v_mfma_f32_16x16x32_bf16 v[72:75], v[20:23], v[104:107], 0\n\t" \
    "v_cvt_pk_bf16_f32 v24, v56, v57\n\t" \
    "v_cvt_pk_bf16_f32 v25, v58, v59\n\t" \
    "v_mfma_f32_16x16x32_bf16 v[76:79], v[28:31], v[104:107], 0\n\t" \
    "v_cvt_pk_bf16_f32 v26, v60, v61\n\t" \
    "v_cvt_pk_bf16_f32 v27, v62, v63\n\t" \
    "v_mfma_f32_16x16x32_bf16 v[32:35], v[0:3], v[106:109], 0\n\t" \
    "v_cvt_pk_bf16_f32 v80, v80, v81\n\t" \
    "v_cvt_pk_bf16_f32 v81, v82, v83\n\t" \
    "v_mfma_f32_16x16x32_bf16 v[36:39], v[8:11], v[106:109], 0\n\t" \
    "v_cvt_pk_bf16_f32 v82, v84, v85\n\t" \
    "v_cvt_pk_bf16_f32 v83, v86, v87\n\t" \
    "v_mfma_f32_16x16x32_bf16 v[40:43], v[0:3], v[104:107], 0\n\t" \
    "v_cvt_pk_bf16_f32 v88, v88, v89\n\t" \
    "v_cvt_pk_bf16_f32 v89, v90, v91\n\t" \
    "v_mfma_f32_16x16x32_bf16 v[44:47], v[8:11], v[104:107], 0\n\t" \
    "v_cvt_pk_bf16_f32 v90, v92, v93\n\t" \
    "v_cvt_pk_bf16_f32 v91, v94, v95\n\t" \
    "v_mfma_f32_16x16x32_bf16 v[96:99], v[80:83], v[232:235], 0\n\t" \
    "v_cvt_pk_bf16_f32 v64, v64, v65\n\t" \
    "v_cvt_pk_bf16_f32 v65, v66, v67\n\t" \
    "v_mfma_f32_16x16x32_bf16 v[100:103], v[88:91], v[232:235], 0\n\t" \
    "v_cvt_pk_bf16_f32 v66, v68, v69\n\t" \
    "v_cvt_pk_bf16_f32 v67, v70, v71\n\t" \
    "v_mfma_f32_16x16x32_bf16 v[4:7], v[80:83], v[112:115], 0\n\t" \
    "v_cvt_pk_bf16_f32 v72, v72, v73\n\t" \
    "v_cvt_pk_bf16_f32 v73, v74, v75\n\t" \
    "v_mfma_f32_16x16x32_bf16 v[20:23], v[88:91], v[112:115], 0\n\t" \
    "v_cvt_pk_bf16_f32 v74, v76, v77\n\t" \
    "v_cvt_pk_bf16_f32 v75, v78, v79\n\t" \
    "v_mfma_f32_16x16x32_bf16 v[12:15], v[80:83], v[116:119], 0\n\t" \
    "v_add_u32 v253, 135168, v241\n\t" \
    "v_xor_b32 v254, 0x2000, v242\n\t" \
    "v_mfma_f32_16x16x32_bf16 v[28:31], v[88:91], v[116:119], 0\n\t" \
    "v_xor_b32 v251, 32, v253\n\t" \
    "v_xor_b32 v252, 0x400, v245\n\t" \
    "v_mfma_f32_16x16x32_bf16 v[96:99], v[64:67], v[232:235], v[96:99]\n\t" \
    "v_cvt_pk_bf16_f32 v32, v32, v33\n\t" \
    "v_cvt_pk_bf16_f32 v33, v34, v35\n\t" \
    "v_mfma_f32_16x16x32_bf16 v[100:103], v[72:75], v[232:235], v[100:103]\n\t" \
    "v_cvt_pk_bf16_f32 v34, v36, v37\n\t" \
    "v_cvt_pk_bf16_f32 v35, v38, v39\n\t" \
    "v_mfma_f32_16x16x32_bf16 v[4:7], v[64:67], v[120:123], v[4:7]\n\t" \
    "v_cvt_pk_bf16_f32 v40, v40, v41\n\t" \
    "v_cvt_pk_bf16_f32 v41, v42, v43\n\t" \
    "v_mfma_f32_16x16x32_bf16 v[20:23], v[72:75], v[120:123], v[20:23]\n\t" \
    "v_cvt_pk_bf16_f32 v42, v44, v45\n\t" \
    "v_cvt_pk_bf16_f32 v43, v46, v47\n\t" \
    "v_mfma_f32_16x16x32_bf16 v[12:15], v[64:67], v[124:127], v[12:15]\n\t" \
    "v_mov_b32 v237, 4\n\t" \
    "v_mfma_f32_16x16x32_bf16 v[28:31], v[72:75], v[124:127], v[28:31]\n\t" \
    "v_cndmask_b32 v96, v100, v96, vcc\n\t" \
    "v_cndmask_b32 v97, v101, v97, vcc\n\t" \
    "v_mfma_f32_16x16x32_bf16 v[48:51], v[16:19], v[106:109], 0\n\t" \
    "v_cndmask_b32 v98, v102, v98, vcc\n\t" \
    "v_cndmask_b32 v99, v103, v99, vcc\n\t" \
    "v_mfma_f32_16x16x32_bf16 v[52:55], v[24:27], v[106:109], 0\n\t" \
    "ds_write_b128 v245, v[96:99]\n\t" \
    "v_mfma_f32_16x16x32_bf16 v[56:59], v[16:19], v[104:107], 0\n\t" \
    "ds_write_b128 v242, v[4:7]\n\t" \
    "v_mfma_f32_16x16x32_bf16 v[60:63], v[24:27], v[104:107], 0\n\t" \
    "ds_write_b128 v242, v[20:23] offset:4096\n\t" \
    "v_mfma_f32_16x16x32_bf16 v[84:87], v[32:35], v[112:115], 0\n\t" \
    "ds_write_b128 v242, v[12:15] offset:1024\n\t" \
    "v_mfma_f32_16x16x32_bf16 v[68:71], v[40:43], v[112:115], 0\n\t" \
    "ds_write_b128 v242, v[28:31] offset:5120\n\t" \
    "ds_write_b32 v248, v237\n\t" \
    "v_mfma_f32_16x16x32_bf16 v[92:95], v[32:35], v[116:119], 0\n\t" \
    "v_cvt_pk_bf16_f32 v48, v48, v49\n\t" \
    "v_cvt_pk_bf16_f32 v49, v50, v51\n\t" \
    "v_mfma_f32_16x16x32_bf16 v[76:79], v[40:43], v[116:119], 0\n\t" \
    "v_cvt_pk_bf16_f32 v50, v52, v53\n\t" \
    "v_cvt_pk_bf16_f32 v51, v54, v55\n\t" \
    "v_mfma_f32_16x16x32_bf16 v[96:99], v[32:35], v[232:235], 0\n\t" \
    "v_cvt_pk_bf16_f32 v56, v56, v57\n\t" \
    "v_cvt_pk_bf16_f32 v57, v58, v59\n\t" \
    "v_mfma_f32_16x16x32_bf16 v[100:103], v[40:43], v[232:235], 0\n\t" \
    "v_cvt_pk_bf16_f32 v58, v60, v61\n\t" \
    "v_cvt_pk_bf16_f32 v59, v62, v63\n\t" \
    "v_mfma_f32_16x16x32_bf16 v[96:99], v[48:51], v[232:235], v[96:99]\n\t" \
    "ds_read_b32 v238, v250\n\t" \
    "ds_read_b128 v[0:3], v252\n\t" \
    "v_mfma_f32_16x16x32_bf16 v[100:103], v[56:59], v[232:235], v[100:103]\n\t" \
    "ds_read_b128 v[4:7], v254\n\t" \
    "ds_read_b128 v[20:23], v254 offset:4096\n\t" \
    "v_mfma_f32_16x16x32_bf16 v[84:87], v[48:51], v[120:123], v[84:87]\n\t" \
    "ds_read_b128 v[12:15], v254 offset:1024\n\t" \
    "ds_read_b128 v[28:31], v254 offset:5120\n\t" \
    "v_mfma_f32_16x16x32_bf16 v[68:71], v[56:59], v[120:123], v[68:71]\n\t" \
    "v_mfma_f32_16x16x32_bf16 v[92:95], v[48:51], v[124:127], v[92:95]\n\t" \
    "ds_read_b32 v255, v249 offset:92\n\t" \
    "v_mfma_f32_16x16x32_bf16 v[76:79], v[56:59], v[124:127], v[76:79]\n\t" \
    "ds_read_b64_tr_b16 v[32:33], v253\n\t" \
    "v_cndmask_b32 v96, v100, v96, vcc\n\t" \
    "v_cndmask_b32 v97, v101, v97, vcc\n\t" \
    "v_cndmask_b32 v98, v102, v98, vcc\n\t" \
    "v_cndmask_b32 v99, v103, v99, vcc\n\t" \
    "s_waitcnt lgkmcnt(7)\n\t" \
    "v_readfirstlane_b32 s85, v238\n\t" \
    "s_cmp_lt_u32 s85, 4\n\t" \
    "s_cbranch_scc1 .Lab_s4\n\t" \
    ".Lab_s4r:\n\t" \
    "s_waitcnt lgkmcnt(6)\n\t" \
    "v_pk_add_f32 v[0:1], v[0:1], v[96:97]\n\t" \
    "v_pk_add_f32 v[2:3], v[2:3], v[98:99]\n\t" \
    "s_waitcnt lgkmcnt(5)\n\t" \
    "v_pk_add_f32 v[84:85], v[84:85], v[4:5]\n\t" \
    "v_pk_add_f32 v[86:87], v[86:87], v[6:7]\n\t" \
    "s_waitcnt lgkmcnt(4)\n\t" \
    "v_pk_add_f32 v[68:69], v[68:69], v[20:21]\n\t" \
    "v_pk_add_f32 v[70:71], v[70:71], v[22:23]\n\t" \
    "ds_read_b64_tr_b16 v[34:35], v253 offset:512\n\t" \
    "ds_read_b64_tr_b16 v[36:37], v251\n\t" \
    "s_waitcnt lgkmcnt(5)\n\t" \
    "v_pk_add_f32 v[92:93], v[92:93], v[12:13]\n\t" \
    "v_pk_add_f32 v[94:95], v[94:95], v[14:15]\n\t" \
    "s_waitcnt lgkmcnt(4)\n\t" \
    "v_pk_add_f32 v[76:77], v[76:77], v[28:29]\n\t" \
    "v_pk_add_f32 v[78:79], v[78:79], v[30:31]\n\t" \
    "v_add3_u32 v26, v0, v1, v2\n\t" \
    "v_add_u32 v26, v26, v3\n\t" \
    "v_cvt_pk_bf16_f32 v8, v84, v85\n\t" \
    "v_cvt_pk_bf16_f32 v9, v86, v87\n\t" \
    "v_cvt_pk_bf16_f32 v10, v68, v69\n\t" \
    "v_cvt_pk_bf16_f32 v11, v70, v71\n\t" \
    "v_cvt_pk_bf16_f32 v16, v92, v93\n\t" \
    "v_cvt_pk_bf16_f32 v17, v94, v95\n\t" \
    "v_cvt_pk_bf16_f32 v18, v76, v77\n\t" \
    "v_cvt_pk_bf16_f32 v19, v78, v79\n\t" \
    "v_add3_u32 v24, v8, v9, v10\n\t" \
    "v_add3_u32 v25, v16, v17, v18\n\t" \
    "v_add_u32 v24, v24, v11\n\t" \
    "v_add_u32 v25, v25, v19\n\t" \
    "v_cndmask_b32_e64 v26, 0, v26, s[94:95]\n\t" \
    "buffer_store_dwordx4 v[8:11], v246, %[rsb], s87 offen sc1\n\t" \
    "v_permlane16_swap_b32 v24, v25\n\t" \
    "v_add3_u32 v24, v24, v25, v26\n\t" \
    "buffer_store_dwordx4 v[16:19], v246, %[rsb], s86 offen sc1\n\t" \
    "v_cndmask_b32_e64 v1, v1, v236, s[92:93]\n\t" \
    "v_add_u32_dpp v24, v24, v24 row_ror:8 row_mask:0xf bank_mask:0xf bound_ctrl:1\n\t" \
    "v_mov_b32 v237, 6\n\t" \
    "ds_read_b64_tr_b16 v[38:39], v251 offset:512\n\t" \
    "v_add_u32_dpp v24, v24, v24 row_ror:4 row_mask:0xf bank_mask:0xf bound_ctrl:1\n\t" \
    "ds_read_b64_tr_b16 v[40:41], v253 offset:4096\n\t" \
    "ds_read_b64_tr_b16 v[42:43], v253 offset:4608\n\t" \
    "v_add_u32_dpp v24, v24, v24 row_ror:2 row_mask:0xf bank_mask:0xf bound_ctrl:1\n\t" \
    "ds_read_b64_tr_b16 v[44:45], v251 offset:4096\n\t" \
    "ds_read_b64_tr_b16 v[46:47], v251 offset:4608\n\t" \
    "v_add_u32_dpp v24, v24, v24 row_ror:1 row_mask:0xf bank_mask:0xf bound_ctrl:1\n\t" \
    "v_cndmask_b32_e64 v0, v0, v24, s[92:93]\n\t" \
    "buffer_store_dwordx4 v[0:3], v247, %[rsb], s88 offen sc1\n\t" \
    "ds_write_b32 v248, v237\n\t" \
    "s_setprio 1\n\t" \
    "s_waitcnt lgkmcnt(9)\n\t" \
    "v_readfirstlane_b32 s85, v255\n\t" \
    "s_cmp_lt_u32 s85, 1\n\t" \
    "s_cbranch_scc1 .Lab_s5\n\t" \
    ".Lab_s5r:\n\t" \
    "s_waitcnt lgkmcnt(7)\n\t" \
    "v_mfma_f32_16x16x32_bf16 v[192:195], v[32:35], v[128:131], v[192:195]\n\t" \
    "v_add_u32 v244, 49152, v241\n\t" \
    "v_xor_b32 v245, 32, v244\n\t" \
    "v_mfma_f32_16x16x32_bf16 v[200:203], v[32:35], v[132:135], v[200:203]\n\t" \
    "ds_read_b64_tr_b16 v[48:49], v244\n\t" \
    "ds_read_b64_tr_b16 v[50:51], v244 offset:512\n\t" \
    "v_mfma_f32_16x16x32_bf16 v[208:211], v[32:35], v[136:139], v[208:211]\n\t" \
    "ds_read_b64_tr_b16 v[52:53], v245\n\t" \
    "ds_read_b64_tr_b16 v[54:55], v245 offset:512\n\t" \
    "v_mfma_f32_16x16x32_bf16 v[216:219], v[32:35], v[140:143], v[216:219]\n\t" \
    "ds_read_b64_tr_b16 v[56:57], v244 offset:4096\n\t" \
    "ds_read_b64_tr_b16 v[58:59], v244 offset:4608\n\t" \
    "s_waitcnt lgkmcnt(11)\n\t" \
    "v_mfma_f32_16x16x32_bf16 v[196:199], v[36:39], v[128:131], v[196:199]\n\t" \
    "ds_read_b64_tr_b16 v[60:61], v245 offset:4096\n\t" \
    "ds_read_b64_tr_b16 v[62:63], v245 offset:4608\n\t" \
    "v_mfma_f32_16x16x32_bf16 v[204:207], v[36:39], v[132:135], v[204:207]\n\t" \
    "v_xor_b32 v246, 64, v240\n\t" \
    "v_subrev_u32 v242, 16384, v241\n\t" \
    "v_mfma_f32_16x16x32_bf16 v[212:215], v[36:39], v[136:139], v[212:215]\n\t" \
    "v_xor_b32 v243, 32, v242\n\t" \
    "v_add_u32 v247, 65536, v240\n\t" \
    "v_mfma_f32_16x16x32_bf16 v[220:223], v[36:39], v[140:143], v[220:223]\n\t" \
    "v_mov_b32 v237, 7\n\t" \
    "ds_write_b32 v248, v237\n\t" \
    "s_waitcnt lgkmcnt(12)\n\t" \
    "v_mfma_f32_16x16x32_bf16 v[192:195], v[40:43], v[144:147], v[192:195]\n\t" \
    "ds_read_b64_tr_b16 v[32:33], v244 offset:8192\n\t" \
    "v_mfma_f32_16x16x32_bf16 v[200:203], v[40:43], v[148:151], v[200:203]\n\t" \
    "ds_read_b128 v[128:131], v240 offset:40960\n\t" \
    "v_mfma_f32_16x16x32_bf16 v[208:211], v[40:43], v[152:155], v[208:211]\n\t" \
    "ds_read_b64_tr_b16 v[34:35], v244 offset:8704\n\t" \
    "v_add_u32 v237, 1, v237\n\t" \
    "v_mfma_f32_16x16x32_bf16 v[216:219], v[40:43], v[156:159], v[216:219]\n\t" \
    "ds_read_b128 v[132:135], v240 offset:43008\n\t" \
    "s_waitcnt lgkmcnt(14)\n\t" \
    "v_mfma_f32_16x16x32_bf16 v[196:199], v[44:47], v[144:147], v[196:199]\n\t" \
    "ds_read_b64_tr_b16 v[36:37], v245 offset:8192\n\t" \
    "v_xor_b32 v255, 64, v247\n\t" \
    "v_mfma_f32_16x16x32_bf16 v[204:207], v[44:47], v[148:151], v[204:207]\n\t" \
    "ds_read_b128 v[136:139], v240 offset:45056\n\t" \
    "v_mfma_f32_16x16x32_bf16 v[212:215], v[44:47], v[152:155], v[212:215]\n\t" \
    "ds_read_b64_tr_b16 v[38:39], v245 offset:8704\n\t" \
    "v_xor_b32 v224, %[neg], v224\n\t" \
    "v_mfma_f32_16x16x32_bf16 v[220:223], v[44:47], v[156:159], v[220:223]\n\t" \
    "ds_read_b128 v[140:143], v240 offset:47104\n\t" \
    "s_waitcnt lgkmcnt(14)\n\t" \
    "v_mfma_f32_16x16x32_bf16 v[192:195], v[48:51], v[160:163], v[192:195]\n\t" \
    "ds_read_b64_tr_b16 v[40:41], v244 offset:12288\n\t" \
    "v_mfma_f32_16x16x32_bf16 v[200:203], v[48:51], v[164:167], v[200:203]\n\t" \
    "ds_read_b128 v[144:147], v246 offset:40960\n\t" \
    "v_mfma_f32_16x16x32_bf16 v[208:211], v[48:51], v[168:171], v[208:211]\n\t" \
    "ds_read_b64_tr_b16 v[42:43], v244 offset:12800\n\t" \
    "v_mfma_f32_16x16x32_bf16 v[216:219], v[48:51], v[172:175], v[216:219]\n\t" \
    "ds_read_b128 v[148:151], v246 offset:43008\n\t" \
    "s_waitcnt lgkmcnt(14)\n\t" \
    "v_mfma_f32_16x16x32_bf16 v[196:199], v[52:55], v[160:163], v[196:199]\n\t" \
    "ds_read_b64_tr_b16 v[44:45], v245 offset:12288\n\t" \
    "v_mfma_f32_16x16x32_bf16 v[204:207], v[52:55], v[164:167], v[204:207]\n\t" \
    "ds_read_b128 v[152:155], v246 offset:45056\n\t" \
    "v_mfma_f32_16x16x32_bf16 v[212:215], v[52:55], v[168:171], v[212:215]\n\t" \
    "ds_read_b64_tr_b16 v[46:47], v245 offset:12800\n\t" \
    "v_xor_b32 v225, %[neg], v225\n\t" \
    "v_mfma_f32_16x16x32_bf16 v[220:223], v[52:55], v[172:175], v[220:223]\n\t" \
    "ds_read_b128 v[156:159], v246 offset:47104\n\t" \
    "ds_write_b32 v248, v237\n\t" \
    "v_mfma_f32_16x16x32_bf16 v[192:195], v[56:59], v[176:179], v[192:195]\n\t" \
    "ds_read_b64_tr_b16 v[48:49], v244 offset:40960\n\t" \
    "v_mfma_f32_16x16x32_bf16 v[200:203], v[56:59], v[180:183], v[200:203]\n\t" \
    "ds_read_b128 v[160:163], v247 offset:16384\n\t" \
    "v_mfma_f32_16x16x32_bf16 v[208:211], v[56:59], v[184:187], v[208:211]\n\t" \
    "ds_read_b64_tr_b16 v[50:51], v244 offset:41472\n\t" \
    "v_add_u32 v237, 1, v237\n\t" \
    "v_mfma_f32_16x16x32_bf16 v[216:219], v[56:59], v[188:191], v[216:219]\n\t" \
    "ds_read_b128 v[164:167], v247 offset:18432\n\t" \
    "s_waitcnt lgkmcnt(14)\n\t" \
    "v_mfma_f32_16x16x32_bf16 v[196:199], v[60:63], v[176:179], v[196:199]\n\t" \
    "ds_read_b64_tr_b16 v[52:53], v245 offset:40960\n\t" \
    "v_mfma_f32_16x16x32_bf16 v[204:207], v[60:63], v[180:183], v[204:207]\n\t" \
    "ds_read_b128 v[168:171], v247 offset:20480\n\t" \
    "v_mfma_f32_16x16x32_bf16 v[212:215], v[60:63], v[184:187], v[212:215]\n\t" \
    "ds_read_b64_tr_b16 v[54:55], v245 offset:41472\n\t" \
    "v_xor_b32 v226, %[neg], v226\n\t" \
    "v_mfma_f32_16x16x32_bf16 v[220:223], v[60:63], v[188:191], v[220:223]\n\t" \
    "ds_read_b128 v[172:175], v247 offset:22528\n\t" \
    "v_mfma_f32_16x16x32_bf16 v[192:195], v[32:35], v[128:131], v[192:195]\n\t" \
    "ds_read_b64_tr_b16 v[56:57], v244 offset:45056\n\t" \
    "v_mfma_f32_16x16x32_bf16 v[200:203], v[32:35], v[132:135], v[200:203]\n\t" \
    "ds_read_b128 v[176:179], v255 offset:16384\n\t" \
    "v_mfma_f32_16x16x32_bf16 v[208:211], v[32:35], v[136:139], v[208:211]\n\t" \
    "ds_read_b64_tr_b16 v[58:59], v244 offset:45568\n\t" \
    "s_waitcnt lgkmcnt(14)\n\t" \
    "v_mfma_f32_16x16x32_bf16 v[216:219], v[32:35], v[140:143], v[216:219]\n\t" \
    "ds_read_b128 v[180:183], v255 offset:18432\n\t" \
    "v_mfma_f32_16x16x32_bf16 v[196:199], v[36:39], v[128:131], v[196:199]\n\t" \
    "ds_read_b64_tr_b16 v[60:61], v245 offset:45056\n\t" \
    "v_mfma_f32_16x16x32_bf16 v[204:207], v[36:39], v[132:135], v[204:207]\n\t" \
    "ds_read_b128 v[184:187], v255 offset:20480\n\t" \
    "v_mfma_f32_16x16x32_bf16 v[212:215], v[36:39], v[136:139], v[212:215]\n\t" \
    "ds_read_b64_tr_b16 v[62:63], v245 offset:45568\n\t" \
    "v_xor_b32 v227, %[neg], v227\n\t" \
    "v_mfma_f32_16x16x32_bf16 v[220:223], v[36:39], v[140:143], v[220:223]\n\t" \
    "ds_read_b128 v[188:191], v255 offset:22528\n\t" \
    "ds_write_b32 v248, v237\n\t" \
    "v_mfma_f32_16x16x32_bf16 v[192:195], v[40:43], v[144:147], v[192:195]\n\t" \
    "ds_read_b64_tr_b16 v[32:33], v244 offset:49152\n\t" \
    "v_mfma_f32_16x16x32_bf16 v[200:203], v[40:43], v[148:151], v[200:203]\n\t" \
    "ds_read_b128 v[128:131], v240\n\t" \
    "v_mfma_f32_16x16x32_bf16 v[208:211], v[40:43], v[152:155], v[208:211]\n\t" \
    "ds_read_b64_tr_b16 v[34:35], v244 offset:49664\n\t" \
    "v_add_u32 v237, 1, v237\n\t" \
    "s_waitcnt lgkmcnt(14)\n\t" \
    "v_mfma_f32_16x16x32_bf16 v[216:219], v[40:43], v[156:159], v[216:219]\n\t" \
    "ds_read_b128 v[132:135], v240 offset:2048\n\t" \
    "v_mfma_f32_16x16x32_bf16 v[196:199], v[44:47], v[144:147], v[196:199]\n\t" \
    "ds_read_b64_tr_b16 v[36:37], v245 offset:49152\n\t" \
    "v_mfma_f32_16x16x32_bf16 v[204:207], v[44:47], v[148:151], v[204:207]\n\t" \
    "ds_read_b128 v[136:139], v240 offset:4096\n\t" \
    "v_mfma_f32_16x16x32_bf16 v[212:215], v[44:47], v[152:155], v[212:215]\n\t" \
    "ds_read_b64_tr_b16 v[38:39], v245 offset:49664\n\t" \
    "v_xor_b32 v228, %[neg], v228\n\t" \
    "v_mfma_f32_16x16x32_bf16 v[220:223], v[44:47], v[156:159], v[220:223]\n\t" \
    "ds_read_b128 v[140:143], v240 offset:6144\n\t" \
    "v_mfma_f32_16x16x32_bf16 v[192:195], v[48:51], v[160:163], v[192:195]\n\t" \
    "ds_read_b64_tr_b16 v[40:41], v244 offset:53248\n\t" \
    "v_add_u32 v247, %[xs0], v240\n\t" \
    "v_mfma_f32_16x16x32_bf16 v[200:203], v[48:51], v[164:167], v[200:203]\n\t" \
    "ds_read_b128 v[144:147], v246\n\t" \
    "v_mfma_f32_16x16x32_bf16 v[208:211], v[48:51], v[168:171], v[208:211]\n\t" \
    "ds_read_b64_tr_b16 v[42:43], v244 offset:53760\n\t" \
    "v_xor_b32 v255, 64, v247\n\t" \
    "s_waitcnt lgkmcnt(14)\n\t" \
    "v_mfma_f32_16x16x32_bf16 v[216:219], v[48:51], v[172:175], v[216:219]\n\t" \
    "ds_read_b128 v[148:151], v246 offset:2048\n\t" \
    "v_mfma_f32_16x16x32_bf16 v[196:199], v[52:55], v[160:163], v[196:199]\n\t" \
    "ds_read_b64_tr_b16 v[44:45], v245 offset:53248\n\t" \
    "v_mfma_f32_16x16x32_bf16 v[204:207], v[52:55], v[164:167], v[204:207]\n\t" \
    "ds_read_b128 v[152:155], v246 offset:4096\n\t" \
    "v_mfma_f32_16x16x32_bf16 v[212:215], v[52:55], v[168:171], v[212:215]\n\t" \
    "ds_read_b64_tr_b16 v[46:47], v245 offset:53760\n\t" \
    "v_xor_b32 v229, %[neg], v229\n\t" \
    "v_mfma_f32_16x16x32_bf16 v[220:223], v[52:55], v[172:175], v[220:223]\n\t" \
    "ds_read_b128 v[156:159], v246 offset:6144\n\t" \
    "ds_write_b32 v248, v237\n\t" \
    "v_mfma_f32_16x16x32_bf16 v[192:195], v[56:59], v[176:179], v[192:195]\n\t" \
    "ds_read_b64_tr_b16 v[48:49], v244 offset:32768\n\t" \
    "v_mfma_f32_16x16x32_bf16 v[200:203], v[56:59], v[180:183], v[200:203]\n\t" \
    "ds_read_b128 v[160:163], v247\n\t" \
    "v_mfma_f32_16x16x32_bf16 v[208:211], v[56:59], v[184:187], v[208:211]\n\t" \
    "ds_read_b64_tr_b16 v[50:51], v244 offset:33280\n\t" \
    "v_add_u32 v237, 1, v237\n\t" \
    "s_waitcnt lgkmcnt(14)\n\t" \
    "v_mfma_f32_16x16x32_bf16 v[216:219], v[56:59], v[188:191], v[216:219]\n\t" \
    "ds_read_b128 v[164:167], v247 offset:2048\n\t" \
    "v_mfma_f32_16x16x32_bf16 v[196:199], v[60:63], v[176:179], v[196:199]\n\t" \
    "ds_read_b64_tr_b16 v[52:53], v245 offset:32768\n\t" \
    "v_mfma_f32_16x16x32_bf16 v[204:207], v[60:63], v[180:183], v[204:207]\n\t" \
    "ds_read_b128 v[168:171], v247 offset:4096\n\t" \
    "v_mfma_f32_16x16x32_bf16 v[212:215], v[60:63], v[184:187], v[212:215]\n\t" \
    "ds_read_b64_tr_b16 v[54:55], v245 offset:33280\n\t" \
    "v_xor_b32 v230, %[neg], v230\n\t" \
    "v_mfma_f32_16x16x32_bf16 v[220:223], v[60:63], v[188:191], v[220:223]\n\t" \
    "ds_read_b128 v[172:175], v247 offset:6144\n\t" \
    "v_mfma_f32_16x16x32_bf16 v[192:195], v[32:35], v[128:131], v[192:195]\n\t" \
    "ds_read_b64_tr_b16 v[56:57], v244 offset:36864\n\t" \
    "v_mfma_f32_16x16x32_bf16 v[200:203], v[32:35], v[132:135], v[200:203]\n\t" \
    "ds_read_b128 v[176:179], v255\n\t" \
    "v_mfma_f32_16x16x32_bf16 v[208:211], v[32:35], v[136:139], v[208:211]\n\t" \
    "ds_read_b64_tr_b16 v[58:59], v244 offset:37376\n\t" \
    "s_waitcnt lgkmcnt(14)\n\t" \
    "v_mfma_f32_16x16x32_bf16 v[216:219], v[32:35], v[140:143], v[216:219]\n\t" \
    "ds_read_b128 v[180:183], v255 offset:2048\n\t" \
    "v_mfma_f32_16x16x32_bf16 v[196:199], v[36:39], v[128:131], v[196:199]\n\t" \
    "ds_read_b64_tr_b16 v[60:61], v245 offset:36864\n\t" \
    "v_mfma_f32_16x16x32_bf16 v[204:207], v[36:39], v[132:135], v[204:207]\n\t" \
    "ds_read_b128 v[184:187], v255 offset:4096\n\t" \
    "v_mfma_f32_16x16x32_bf16 v[212:215], v[36:39], v[136:139], v[212:215]\n\t" \
    "ds_read_b64_tr_b16 v[62:63], v245 offset:37376\n\t" \
    "v_xor_b32 v231, %[neg], v231\n\t" \
    "v_mfma_f32_16x16x32_bf16 v[220:223], v[36:39], v[140:143], v[220:223]\n\t" \
    "ds_read_b128 v[188:191], v255 offset:6144\n\t" \
    "ds_write_b32 v248, v237\n\t" \
    "v_mfma_f32_16x16x32_bf16 v[192:195], v[40:43], v[144:147], v[192:195]\n\t" \
    "v_mbcnt_lo_u32_b32 v250, -1, 0\n\t" \
    "v_mbcnt_hi_u32_b32 v250, -1, v250\n\t" \
    "v_mfma_f32_16x16x32_bf16 v[200:203], v[40:43], v[148:151], v[200:203]\n\t" \
    "v_and_b32 v238, 15, v250\n\t" \
    "v_lshlrev_b32 v238, 3, v238\n\t" \
    "v_mfma_f32_16x16x32_bf16 v[208:211], v[40:43], v[152:155], v[208:211]\n\t" \
    "v_add_u32 v237, 1, v237\n\t" \
    "s_waitcnt lgkmcnt(14)\n\t" \
    "v_mfma_f32_16x16x32_bf16 v[216:219], v[40:43], v[156:159], v[216:219]\n\t" \
    "buffer_load_dwordx2 v[232:233], v238, %[rsw], %[sg] offen sc1\n\t" \
    "v_mfma_f32_16x16x32_bf16 v[196:199], v[44:47], v[144:147], v[196:199]\n\t" \
    "v_lshlrev_b32 v251, 4, v250\n\t" \
    "s_add_u32 s85, %[sof], %[ofc]\n\t" \
    "v_mfma_f32_16x16x32_bf16 v[204:207], v[44:47], v[148:151], v[204:207]\n\t" \
    "v_add_u32 v240, s85, v251\n\t" \
    "s_add_u32 s87, %[sof], %[ofp]\n\t" \
    "v_mfma_f32_16x16x32_bf16 v[212:215], v[44:47], v[152:155], v[212:215]\n\t" \
    "buffer_load_dwordx2 v[234:235], v238, %[rsw], %[sg] offen offset:128 sc1\n\t" \
    "v_mfma_f32_16x16x32_bf16 v[220:223], v[44:47], v[156:159], v[220:223]\n\t" \
    "v_add_u32 v241, 0x1000, v240\n\t" \
    "s_add_u32 s88, %[sof], %[ogp]\n\t" \
    "v_mfma_f32_16x16x32_bf16 v[192:195], v[48:51], v[160:163], v[192:195]\n\t" \
    "s_mov_b32 s86, 0x10000\n\t" \
    "v_add_u32 v242, 0x2000, v240\n\t" \
    "v_add_u32 v243, 0x3000, v240\n\t" \
    "s_waitcnt lgkmcnt(10)\n\t" \
    "v_mfma_f32_16x16x32_bf16 v[196:199], v[52:55], v[160:163], v[196:199]\n\t" \
    "v_bfe_u32 v244, v251, 1, 8\n\t" \
    "v_add_u32 v253, %[kac], v251\n\t" \
    "s_waitcnt lgkmcnt(6)\n\t" \
    "v_mfma_f32_16x16x32_bf16 v[192:195], v[56:59], v[176:179], v[192:195]\n\t" \
    "v_lshrrev_b32 v112, 2, v250\n\t" \
    "v_bfe_u32 v113, v250, 4, 1\n\t" \
    "s_waitcnt lgkmcnt(2)\n\t" \
    "v_mfma_f32_16x16x32_bf16 v[196:199], v[60:63], v[176:179], v[196:199]\n\t" \
    "v_lshlrev_b32 v245, 7, v112\n\t" \
    "v_bitop3_b32 v112, v112, v113, 3 bitop3:0x28\n\t" \
    "v_mfma_f32_16x16x32_bf16 v[200:203], v[48:51], v[164:167], v[200:203]\n\t" \
    "v_lshl_or_b32 v245, v112, 5, v245\n\t" \
    "v_and_b32 v112, 24, v238\n\t" \
    "v_mfma_f32_16x16x32_bf16 v[204:207], v[52:55], v[164:167], v[204:207]\n\t" \
    "v_or3_b32 v245, v245, v112, s86\n\t" \
    "v_lshlrev_b32 v247, 8, v238\n\t" \
    "v_mfma_f32_16x16x32_bf16 v[200:203], v[56:59], v[180:183], v[200:203]\n\t" \
    "v_lshl_or_b32 v247, v113, 5, v247\n\t" \
    "v_lshrrev_b32 v112, 5, v250\n\t" \
    "v_mfma_f32_16x16x32_bf16 v[204:207], v[60:63], v[180:183], v[204:207]\n\t" \
    "v_lshl_or_b32 v247, v112, 4, v247\n\t" \
    "v_and_b32 v252, 48, v247\n\t" \
    "v_mfma_f32_16x16x32_bf16 v[208:211], v[48:51], v[168:171], v[208:211]\n\t" \
    "v_lshl_add_u32 v252, v252, 2, %[zac]\n\t" \
    "v_lshlrev_b32 v246, 4, v238\n\t" \
    "v_mfma_f32_16x16x32_bf16 v[212:215], v[52:55], v[168:171], v[212:215]\n\t" \
    "v_cvt_pk_bf16_f32 v64, v192, v193\n\t" \
    "v_cvt_pk_bf16_f32 v65, v194, v195\n\t" \
    "v_mfma_f32_16x16x32_bf16 v[208:211], v[56:59], v[184:187], v[208:211]\n\t" \
    "v_cvt_pk_bf16_f32 v66, v196, v197\n\t" \
    "v_cvt_pk_bf16_f32 v67, v198, v199\n\t" \
    "v_mfma_f32_16x16x32_bf16 v[212:215], v[60:63], v[184:187], v[212:215]\n\t" \
    "v_lshl_or_b32 v246, v113, 3, v246\n\t" \
    "v_lshrrev_b32 v113, 4, v238\n\t" \
    "v_mfma_f32_16x16x32_bf16 v[216:219], v[48:51], v[172:175], v[216:219]\n\t" \
    "v_cvt_pk_bf16_f32 v68, v200, v201\n\t" \
    "v_cvt_pk_bf16_f32 v69, v202, v203\n\t" \
    "v_mfma_f32_16x16x32_bf16 v[220:223], v[52:55], v[172:175], v[220:223]\n\t" \
    "v_cvt_pk_bf16_f32 v70, v204, v205\n\t" \
    "v_cvt_pk_bf16_f32 v71, v206, v207\n\t" \
    "s_waitcnt lgkmcnt(1)\n\t" \
    "v_mfma_f32_16x16x32_bf16 v[216:219], v[56:59], v[188:191], v[216:219]\n\t" \
    "v_xor_b32 v112, v112, v113\n\t" \
    "v_lshl_or_b32 v246, v112, 4, v246\n\t" \
    "v_mfma_f32_16x16x32_bf16 v[220:223], v[60:63], v[188:191], v[220:223]\n\t" \
    "v_add_u32 v246, %[xec], v246\n\t" \
    "v_mfma_f32_16x16x32_bf16 v[0:3], v[224:227], v[64:67], 0\n\t" \
    "v_cvt_pk_bf16_f32 v72, v208, v209\n\t" \
    "v_cvt_pk_bf16_f32 v73, v210, v211\n\t" \
    "v_mfma_f32_16x16x32_bf16 v[4:7], v[228:231], v[64:67], 0\n\t" \
    "v_cvt_pk_bf16_f32 v74, v212, v213\n\t" \
    "v_cvt_pk_bf16_f32 v75, v214, v215\n\t" \
    "v_mfma_f32_16x16x32_bf16 v[8:11], v[224:227], v[68:71], 0\n\t" \
    "v_mov_b32 v120, 0x358637bd\n\t" \
    "v_mov_b32 v121, 0x358637bd\n\t" \
    "v_mfma_f32_16x16x32_bf16 v[12:15], v[228:231], v[68:71], 0\n\t" \
    "v_mov_b32 v122, 0x358637bd\n\t" \
    "v_mov_b32 v123, 0x358637bd\n\t" \
    "v_mfma_f32_16x16x32_bf16 v[16:19], v[224:227], v[72:75], 0\n\t" \
    "v_cvt_pk_bf16_f32 v76, v216, v217\n\t" \
    "v_cvt_pk_bf16_f32 v77, v218, v219\n\t" \
    "v_mfma_f32_16x16x32_bf16 v[20:23], v[228:231], v[72:75], 0\n\t" \
    ".Lpad_ab2:\n\t" \
    "s_cbranch_execz .Lpad_ret\n\t" \
    "v_cvt_pk_bf16_f32 v78, v220, v221\n\t" \
    "v_cvt_pk_bf16_f32 v79, v222, v223\n\t" \
    "s_nop 1\n\t" \
    "v_mfma_f32_16x16x32_bf16 v[24:27], v[224:227], v[76:79], 0\n\t" \
    "v_mfma_f32_16x16x32_bf16 v[28:31], v[228:231], v[76:79], 0\n\t" \
    "v_max3_f32 v64, v0, v1, v2\n\t" \
    "v_max3_f32 v66, -v0, -v1, -v2\n\t" \
    "v_max3_f32 v68, v8, v9, v10\n\t" \
    "v_max3_f32 v64, v64, v3, v4\n\t" \
    "v_max3_f32 v66, v66, -v3, -v4\n\t" \
    "v_max3_f32 v68, v68, v11, v12\n\t" \
    "v_max3_f32 v64, v64, v5, v6\n\t" \
    "v_max3_f32 v66, v66, -v5, -v6\n\t" \
    "v_max3_f32 v68, v68, v13, v14\n\t" \
    "v_max_f32_e64 v64, v64, v7\n\t" \
    "v_max_f32_e64 v66, v66, -v7\n\t" \
    "v_max_f32_e64 v68, v68, v15\n\t" \
    "v_max3_f32 v70, -v8, -v9, -v10\n\t" \
    "v_permlane32_swap_b32 v64, v66\n\t" \
    "v_max3_f32 v72, v16, v17, v18\n\t" \
    "v_max3_f32 v70, v70, -v11, -v12\n\t" \
    "v_max_f32 v64, v64, v66\n\t" \
    "v_max3_f32 v72, v72, v19, v20\n\t" \
    "v_max3_f32 v70, v70, -v13, -v14\n\t" \
    "v_max3_f32 v74, -v16, -v17, -v18\n\t" \
    "v_max3_f32 v72, v72, v21, v22\n\t" \
    "v_max_f32_e64 v70, v70, -v15\n\t" \
    "v_max3_f32 v74, v74, -v19, -v20\n\t" \
    "v_max_f32_e64 v72, v72, v23\n\t" \
    "v_permlane32_swap_b32 v68, v70\n\t" \
    "v_max3_f32 v74, v74, -v21, -v22\n\t" \
    "v_max3_f32 v76, v24, v25, v26\n\t" \
    "v_max_f32 v68, v68, v70\n\t" \
    "v_max_f32_e64 v74, v74, -v23\n\t" \
    "v_max3_f32 v76, v76, v27, v28\n\t" \
    "v_permlane16_swap_b32 v64, v68\n\t" \
    "v_permlane32_swap_b32 v72, v74\n\t" \
    "v_max3_f32 v76, v76, v29, v30\n\t" \
    "v_max_f32 v66, v64, v68\n\t" \
    "v_max_f32 v72, v72, v74\n\t" \
    "v_max_f32_e64 v76, v76, v31\n\t" \
    "v_mul_f32 v64, 0xbfb8aa3b, v66\n\t" \
    "v_mul_f32 v68, 0xbfb8aa3b, v66\n\t" \
    "v_max3_f32 v78, -v24, -v25, -v26\n\t" \
    "v_max3_f32 v78, v78, -v27, -v28\n\t" \
    "v_permlane16_swap_b32 v64, v68\n\t" \
    "v_mov_b32 v66, v64\n\t" \
    "v_max3_f32 v78, v78, -v29, -v30\n\t" \
    "v_mov_b32 v70, v68\n\t" \
    "v_permlane32_swap_b32 v64, v66\n\t" \
    "v_max_f32_e64 v78, v78, -v31\n\t" \
    "v_permlane32_swap_b32 v68, v70\n\t" \
    "v_pk_fma_f32 v[32:33], v[0:1], s[90:91], v[64:65] op_sel_hi:[1,1,0]\n\t" \
    "v_permlane32_swap_b32 v76, v78\n\t" \
    "v_pk_fma_f32 v[0:1], v[0:1], s[90:91], v[66:67] op_sel_hi:[1,1,0] neg_lo:[1,0,0] neg_hi:[1,0,0]\n\t" \
    "v_pk_fma_f32 v[34:35], v[2:3], s[90:91], v[64:65] op_sel_hi:[1,1,0]\n\t" \
    "v_max_f32 v76, v76, v78\n\t" \
    "v_pk_fma_f32 v[2:3], v[2:3], s[90:91], v[66:67] op_sel_hi:[1,1,0] neg_lo:[1,0,0] neg_hi:[1,0,0]\n\t" \
    "v_pk_fma_f32 v[36:37], v[4:5], s[90:91], v[64:65] op_sel_hi:[1,1,0]\n\t" \
    "v_permlane16_swap_b32 v72, v76\n\t" \
    "v_pk_fma_f32 v[4:5], v[4:5], s[90:91], v[66:67] op_sel_hi:[1,1,0] neg_lo:[1,0,0] neg_hi:[1,0,0]\n\t" \
    "v_pk_fma_f32 v[38:39], v[6:7], s[90:91], v[64:65] op_sel_hi:[1,1,0]\n\t" \
    "v_max_f32 v74, v72, v76\n\t" \
    "v_pk_fma_f32 v[6:7], v[6:7], s[90:91], v[66:67] op_sel_hi:[1,1,0] neg_lo:[1,0,0] neg_hi:[1,0,0]\n\t" \
    "v_exp_f32 v32, v32\n\t" \
    "v_mul_f32 v72, 0xbfb8aa3b, v74\n\t" \
    "v_mul_f32 v76, 0xbfb8aa3b, v74\n\t" \
    "v_exp_f32 v0, v0\n\t" \
    "v_exp_f32 v33, v33\n\t" \
    "v_permlane16_swap_b32 v72, v76\n\t" \
    "v_exp_f32 v1, v1\n\t" \
    "v_exp_f32 v34, v34\n\t" \
    "v_mov_b32 v74, v72\n\t" \
    "v_mov_b32 v78, v76\n\t" \
    "v_exp_f32 v2, v2\n\t" \
    "v_permlane32_swap_b32 v72, v74\n\t" \
    "v_permlane32_swap_b32 v76, v78\n\t" \
    "v_exp_f32 v35, v35\n\t" \
    "v_exp_f32 v3, v3\n\t" \
    "v_exp_f32 v36, v36\n\t" \
    "v_pk_add_f32 v[64:65], v[32:33], v[34:35]\n\t" \
    "v_pk_add_f32 v[66:67], v[0:1], v[2:3]\n\t" \
    "v_exp_f32 v4, v4\n\t" \
    "v_exp_f32 v37, v37\n\t" \
    "v_exp_f32 v5, v5\n\t" \
    "v_exp_f32 v38, v38\n\t" \
    "v_pk_add_f32 v[64:65], v[64:65], v[36:37]\n\t" \
    "v_pk_add_f32 v[66:67], v[66:67], v[4:5]\n\t" \
    "v_exp_f32 v6, v6\n\t" \
    "v_exp_f32 v39, v39\n\t" \
    "v_exp_f32 v7, v7\n\t" \
    "v_pk_fma_f32 v[40:41], v[8:9], s[90:91], v[68:69] op_sel_hi:[1,1,0]\n\t" \
    "v_pk_add_f32 v[64:65], v[64:65], v[38:39]\n\t" \
    "v_pk_add_f32 v[66:67], v[66:67], v[6:7]\n\t" \
    "v_pk_fma_f32 v[8:9], v[8:9], s[90:91], v[70:71] op_sel_hi:[1,1,0] neg_lo:[1,0,0] neg_hi:[1,0,0]\n\t" \
    "v_add_f32 v64, v64, v65\n\t" \
    "v_add_f32 v66, v66, v67\n\t" \
    "v_pk_fma_f32 v[42:43], v[10:11], s[90:91], v[68:69] op_sel_hi:[1,1,0]\n\t" \
    "v_pk_fma_f32 v[10:11], v[10:11], s[90:91], v[70:71] op_sel_hi:[1,1,0] neg_lo:[1,0,0] neg_hi:[1,0,0]\n\t" \
    "v_permlane32_swap_b32 v64, v66\n\t" \
    "v_pk_fma_f32 v[44:45], v[12:13], s[90:91], v[68:69] op_sel_hi:[1,1,0]\n\t" \
    "v_pk_fma_f32 v[12:13], v[12:13], s[90:91], v[70:71] op_sel_hi:[1,1,0] neg_lo:[1,0,0] neg_hi:[1,0,0]\n\t" \
    "v_add_f32 v64, v64, v66\n\t" \
    "v_pk_fma_f32 v[46:47], v[14:15], s[90:91], v[68:69] op_sel_hi:[1,1,0]\n\t" \
    "v_pk_fma_f32 v[14:15], v[14:15], s[90:91], v[70:71] op_sel_hi:[1,1,0] neg_lo:[1,0,0] neg_hi:[1,0,0]\n\t" \
    "v_exp_f32 v40, v40\n\t" \
    "v_exp_f32 v8, v8\n\t" \
    "v_exp_f32 v41, v41\n\t" \
    "v_exp_f32 v9, v9\n\t" \
    "v_exp_f32 v42, v42\n\t" \
    "v_exp_f32 v10, v10\n\t" \
    "v_exp_f32 v43, v43\n\t" \
    "v_exp_f32 v11, v11\n\t" \
    "v_exp_f32 v44, v44\n\t" \
    "v_pk_add_f32 v[68:69], v[40:41], v[42:43]\n\t" \
    "v_pk_add_f32 v[70:71], v[8:9], v[10:11]\n\t" \
    "v_exp_f32 v12, v12\n\t" \
    "v_exp_f32 v45, v45\n\t" \
    "v_exp_f32 v13, v13\n\t" \
    "v_exp_f32 v46, v46\n\t" \
    "v_pk_add_f32 v[68:69], v[68:69], v[44:45]\n\t" \
    "v_pk_add_f32 v[70:71], v[70:71], v[12:13]\n\t" \
    "v_exp_f32 v14, v14\n\t" \
    "v_exp_f32 v47, v47\n\t" \
    "v_exp_f32 v15, v15\n\t" \
    "v_pk_fma_f32 v[48:49], v[16:17], s[90:91], v[72:73] op_sel_hi:[1,1,0]\n\t" \
    "v_pk_add_f32 v[68:69], v[68:69], v[46:47]\n\t" \
    "v_pk_add_f32 v[70:71], v[70:71], v[14:15]\n\t" \
    "v_pk_fma_f32 v[16:17], v[16:17], s[90:91], v[74:75] op_sel_hi:[1,1,0] neg_lo:[1,0,0] neg_hi:[1,0,0]\n\t" \
    "v_add_f32 v68, v68, v69\n\t" \
    "v_add_f32 v70, v70, v71\n\t" \
    "v_pk_fma_f32 v[50:51], v[18:19], s[90:91], v[72:73] op_sel_hi:[1,1,0]\n\t" \
    "v_pk_fma_f32 v[18:19], v[18:19], s[90:91], v[74:75] op_sel_hi:[1,1,0] neg_lo:[1,0,0] neg_hi:[1,0,0]\n\t" \
    "v_permlane32_swap_b32 v68, v70\n\t" \
    "v_pk_fma_f32 v[52:53], v[20:21], s[90:91], v[72:73] op_sel_hi:[1,1,0]\n\t" \
    "v_pk_fma_f32 v[20:21], v[20:21], s[90:91], v[74:75] op_sel_hi:[1,1,0] neg_lo:[1,0,0] neg_hi:[1,0,0]\n\t" \
    "v_add_f32 v68, v68, v70\n\t" \
    "v_pk_fma_f32 v[54:55], v[22:23], s[90:91], v[72:73] op_sel_hi:[1,1,0]\n\t" \
    "v_pk_fma_f32 v[22:23], v[22:23], s[90:91], v[74:75] op_sel_hi:[1,1,0] neg_lo:[1,0,0] neg_hi:[1,0,0]\n\t" \
    "v_permlane16_swap_b32 v64, v68\n\t" \
    "v_exp_f32 v48, v48\n\t" \
    "v_exp_f32 v16, v16\n\t" \
    "v_add_f32 v66, v64, v68\n\t" \
    "v_exp_f32 v49, v49\n\t" \
    "v_exp_f32 v17, v17\n\t" \
    "v_rcp_f32 v64, v66\n\t" \
    "v_exp_f32 v50, v50\n\t" \
    "v_exp_f32 v18, v18\n\t" \
    "v_mov_b32 v68, v64\n\t" \
    "v_exp_f32 v51, v51\n\t" \
    "v_exp_f32 v19, v19\n\t" \
    "v_permlane16_swap_b32 v64, v68\n\t" \
    "v_pk_add_f32 v[72:73], v[48:49], v[50:51]\n\t" \
    "v_pk_add_f32 v[74:75], v[16:17], v[18:19]\n\t" \
    "v_mov_b32 v66, v64\n\t" \
    "v_mov_b32 v70, v68\n\t" \
    "v_exp_f32 v52, v52\n\t" \
    "v_permlane32_swap_b32 v64, v66\n\t" \
    "v_permlane32_swap_b32 v68, v70\n\t" \
    "v_exp_f32 v20, v20\n\t" \
    "v_exp_f32 v53, v53\n\t" \
    "v_exp_f32 v21, v21\n\t" \
    "v_exp_f32 v54, v54\n\t" \
    "v_pk_add_f32 v[72:73], v[72:73], v[52:53]\n\t" \
    "v_pk_add_f32 v[74:75], v[74:75], v[20:21]\n\t" \
    "v_exp_f32 v22, v22\n\t" \
    "v_exp_f32 v55, v55\n\t" \
    "v_exp_f32 v23, v23\n\t" \
    "v_pk_fma_f32 v[56:57], v[24:25], s[90:91], v[76:77] op_sel_hi:[1,1,0]\n\t" \
    "v_pk_add_f32 v[72:73], v[72:73], v[54:55]\n\t" \
    "v_pk_add_f32 v[74:75], v[74:75], v[22:23]\n\t" \
    "v_pk_fma_f32 v[24:25], v[24:25], s[90:91], v[78:79] op_sel_hi:[1,1,0] neg_lo:[1,0,0] neg_hi:[1,0,0]\n\t" \
    "v_add_f32 v72, v72, v73\n\t" \
    "v_add_f32 v74, v74, v75\n\t" \
    "v_pk_fma_f32 v[58:59], v[26:27], s[90:91], v[76:77] op_sel_hi:[1,1,0]\n\t" \
    "v_pk_fma_f32 v[26:27], v[26:27], s[90:91], v[78:79] op_sel_hi:[1,1,0] neg_lo:[1,0,0] neg_hi:[1,0,0]\n\t" \
    "v_permlane32_swap_b32 v72, v74\n\t" \
    "v_pk_fma_f32 v[60:61], v[28:29], s[90:91], v[76:77] op_sel_hi:[1,1,0]\n\t" \
    "v_pk_fma_f32 v[28:29], v[28:29], s[90:91], v[78:79] op_sel_hi:[1,1,0] neg_lo:[1,0,0] neg_hi:[1,0,0]\n\t" \
    "v_add_f32 v72, v72, v74\n\t" \
    "v_pk_fma_f32 v[62:63], v[30:31], s[90:91], v[76:77] op_sel_hi:[1,1,0]\n\t" \
    "v_pk_fma_f32 v[30:31], v[30:31], s[90:91], v[78:79] op_sel_hi:[1,1,0] neg_lo:[1,0,0] neg_hi:[1,0,0]\n\t" \
    "v_exp_f32 v56, v56\n\t" \
    "v_exp_f32 v24, v24\n\t" \
    "v_exp_f32 v57, v57\n\t" \
    "v_exp_f32 v25, v25\n\t" \
    "v_exp_f32 v58, v58\n\t" \
    "v_exp_f32 v26, v26\n\t" \
    "v_exp_f32 v59, v59\n\t" \
    "v_exp_f32 v27, v27\n\t" \
    "v_exp_f32 v60, v60\n\t" \
    "v_pk_add_f32 v[76:77], v[56:57], v[58:59]\n\t" \
    "v_pk_add_f32 v[78:79], v[24:25], v[26:27]\n\t" \
    "v_exp_f32 v28, v28\n\t" \
    "v_exp_f32 v61, v61\n\t" \
    "v_exp_f32 v29, v29\n\t" \
    "v_exp_f32 v62, v62\n\t" \
    "v_pk_add_f32 v[76:77], v[76:77], v[60:61]\n\t" \
    "v_pk_add_f32 v[78:79], v[78:79], v[28:29]\n\t" \
    "v_exp_f32 v30, v30\n\t" \
    "v_exp_f32 v63, v63\n\t" \
    "v_exp_f32 v31, v31\n\t" \
    "v_pk_mul_f32 v[0:1], v[0:1], v[66:67] op_sel_hi:[1,0]\n\t" \
    "v_pk_add_f32 v[76:77], v[76:77], v[62:63]\n\t" \
    "v_pk_add_f32 v[78:79], v[78:79], v[30:31]\n\t" \
    "v_pk_mul_f32 v[32:33], v[32:33], v[64:65] op_sel_hi:[1,0]\n\t" \
    "v_add_f32 v76, v76, v77\n\t" \
    "v_add_f32 v78, v78, v79\n\t" \
    "v_pk_mul_f32 v[2:3], v[2:3], v[66:67] op_sel_hi:[1,0]\n\t" \
    "v_pk_mul_f32 v[34:35], v[34:35], v[64:65] op_sel_hi:[1,0]\n\t" \
    "v_permlane32_swap_b32 v76, v78\n\t" \
    "v_pk_mul_f32 v[4:5], v[4:5], v[66:67] op_sel_hi:[1,0]\n\t" \
    "v_pk_mul_f32 v[36:37], v[36:37], v[64:65] op_sel_hi:[1,0]\n\t" \
    "v_add_f32 v76, v76, v78\n\t" \
    "v_pk_mul_f32 v[6:7], v[6:7], v[66:67] op_sel_hi:[1,0]\n\t" \
    "v_pk_mul_f32 v[38:39], v[38:39], v[64:65] op_sel_hi:[1,0]\n\t" \
    "v_permlane16_swap_b32 v72, v76\n\t" \
    "v_cvt_pk_bf16_f32 v7, v6, v7\n\t" \
    "v_cvt_pk_bf16_f32 v6, v4, v5\n\t" \
    "v_add_f32 v74, v72, v76\n\t" \
    "v_cvt_pk_bf16_f32 v5, v2, v3\n\t" \
    "v_cvt_pk_bf16_f32 v4, v0, v1\n\t" \
    "v_rcp_f32 v72, v74\n\t" \
    "v_cvt_pk_bf16_f32 v0, v32, v33\n\t" \
    "v_cvt_pk_bf16_f32 v1, v34, v35\n\t" \
    "v_mov_b32 v76, v72\n\t" \
    "v_cvt_pk_bf16_f32 v2, v36, v37\n\t" \
    "v_cvt_pk_bf16_f32 v3, v38, v39\n\t" \
    "v_permlane16_swap_b32 v72, v76\n\t" \
    "v_pk_mul_f32 v[8:9], v[8:9], v[70:71] op_sel_hi:[1,0]\n\t" \
    "v_pk_mul_f32 v[40:41], v[40:41], v[68:69] op_sel_hi:[1,0]\n\t" \
    "v_mov_b32 v74, v72\n\t" \
    "v_mov_b32 v78, v76\n\t" \
    "v_pk_mul_f32 v[10:11], v[10:11], v[70:71] op_sel_hi:[1,0]\n\t" \
    "v_permlane32_swap_b32 v72, v74\n\t" \
    "v_permlane32_swap_b32 v76, v78\n\t" \
    "v_pk_mul_f32 v[42:43], v[42:43], v[68:69] op_sel_hi:[1,0]\n\t" \
    "v_pk_mul_f32 v[12:13], v[12:13], v[70:71] op_sel_hi:[1,0]\n\t" \
    "v_pk_mul_f32 v[44:45], v[44:45], v[68:69] op_sel_hi:[1,0]\n\t" \
    "v_pk_mul_f32 v[14:15], v[14:15], v[70:71] op_sel_hi:[1,0]\n\t" \
    "v_pk_mul_f32 v[46:47], v[46:47], v[68:69] op_sel_hi:[1,0]\n\t" \
    "v_pk_mul_f32 v[16:17], v[16:17], v[74:75] op_sel_hi:[1,0]\n\t" \
    "v_cvt_pk_bf16_f32 v15, v14, v15\n\t" \
    "v_cvt_pk_bf16_f32 v14, v12, v13\n\t" \
    "v_cvt_pk_bf16_f32 v13, v10, v11\n\t" \
    "v_cvt_pk_bf16_f32 v12, v8, v9\n\t" \
    "v_cvt_pk_bf16_f32 v8, v40, v41\n\t" \
    "v_cvt_pk_bf16_f32 v9, v42, v43\n\t" \
    "v_cvt_pk_bf16_f32 v10, v44, v45\n\t" \
    "v_cvt_pk_bf16_f32 v11, v46, v47\n\t" \
    "v_pk_mul_f32 v[48:49], v[48:49], v[72:73] op_sel_hi:[1,0]\n\t" \
    "v_pk_mul_f32 v[18:19], v[18:19], v[74:75] op_sel_hi:[1,0]\n\t" \
    "v_pk_mul_f32 v[50:51], v[50:51], v[72:73] op_sel_hi:[1,0]\n\t" \
    "v_pk_mul_f32 v[20:21], v[20:21], v[74:75] op_sel_hi:[1,0]\n\t" \
    "v_pk_mul_f32 v[52:53], v[52:53], v[72:73] op_sel_hi:[1,0]\n\t" \
    "v_pk_mul_f32 v[22:23], v[22:23], v[74:75] op_sel_hi:[1,0]\n\t" \
    "v_pk_mul_f32 v[54:55], v[54:55], v[72:73] op_sel_hi:[1,0]\n\t" \
    "ds_read_b32 v237, v249 offset:8\n\t" \
    "v_pk_mul_f32 v[24:25], v[24:25], v[78:79] op_sel_hi:[1,0]\n\t" \
    "v_cvt_pk_bf16_f32 v23, v22, v23\n\t" \
    "v_cvt_pk_bf16_f32 v22, v20, v21\n\t" \
    "v_cvt_pk_bf16_f32 v21, v18, v19\n\t" \
    "v_cvt_pk_bf16_f32 v20, v16, v17\n\t" \
    "v_cvt_pk_bf16_f32 v16, v48, v49\n\t" \
    "v_cvt_pk_bf16_f32 v17, v50, v51\n\t" \
    "v_cvt_pk_bf16_f32 v18, v52, v53\n\t" \
    "v_cvt_pk_bf16_f32 v19, v54, v55\n\t" \
    "v_pk_mul_f32 v[56:57], v[56:57], v[76:77] op_sel_hi:[1,0]\n\t" \
    "v_pk_mul_f32 v[26:27], v[26:27], v[78:79] op_sel_hi:[1,0]\n\t" \
    "v_pk_mul_f32 v[58:59], v[58:59], v[76:77] op_sel_hi:[1,0]\n\t" \
    "v_pk_mul_f32 v[28:29], v[28:29], v[78:79] op_sel_hi:[1,0]\n\t" \
    "v_pk_mul_f32 v[60:61], v[60:61], v[76:77] op_sel_hi:[1,0]\n\t" \
    "v_pk_mul_f32 v[30:31], v[30:31], v[78:79] op_sel_hi:[1,0]\n\t" \
    "v_pk_mul_f32 v[62:63], v[62:63], v[76:77] op_sel_hi:[1,0]\n\t" \
    "v_cvt_pk_bf16_f32 v31, v30, v31\n\t" \
    "v_cvt_pk_bf16_f32 v30, v28, v29\n\t" \
    "v_cvt_pk_bf16_f32 v29, v26, v27\n\t" \
    "v_cvt_pk_bf16_f32 v28, v24, v25\n\t" \
    "v_cvt_pk_bf16_f32 v24, v56, v57\n\t" \
    "v_cvt_pk_bf16_f32 v25, v58, v59\n\t" \
    "v_cvt_pk_bf16_f32 v26, v60, v61\n\t" \
    "v_cvt_pk_bf16_f32 v27, v62, v63\n\t" \
    "s_setprio 2\n\t" \
    "s_waitcnt lgkmcnt(0)\n\t" \
    "v_readfirstlane_b32 s85, v237\n\t" \
    "s_add_u32 s86, s87, 0x10000\n\t" \
    "s_cmp_lt_u32 s85, 3\n\t" \
    "s_cbranch_scc1 .Lab_s7\n\t" \
    ".Lab_s7r:\n\t" \
    "s_mov_b32 s89, 0x400000\n\t" \
    "s_mov_b32 s82, 4\n\t" \
    ".Lab_k:\n\t" \
    "ds_read_b32 v237, v249 offset:8\n\t" \
    "ds_read_b128 v[32:35], v252\n\t" \
    "ds_read_b128 v[36:39], v252 offset:256\n\t" \
    "ds_read_b128 v[40:43], v253\n\t" \
    "ds_read_b128 v[44:47], v253 offset:1024\n\t" \
    "ds_read_b128 v[48:51], v253 offset:2048\n\t" \
    "ds_read_b128 v[52:55], v253 offset:3072\n\t" \
    "s_sub_u32 s89, s89, 1\n\t" \
    "s_cbranch_scc1 .Lab_kr\n\t" \
    "s_waitcnt lgkmcnt(6)\n\t" \
    "v_readfirstlane_b32 s85, v237\n\t" \
    "s_cmp_lt_u32 s85, s82\n\t" \
    "s_cbranch_scc1 .Lab_k\n\t" \
    ".Lab_kr:\n\t" \
    "s_waitcnt lgkmcnt(5)\n\t" \
    "v_mfma_f32_16x16x32_bf16 v[56:59], v[32:35], v[0:3], v[120:123]\n\t" \
    "ds_read_b32 v126, v249 offset:296\n\t" \
    "v_mfma_f32_16x16x32_bf16 v[60:63], v[32:35], v[8:11], v[120:123]\n\t" \
    "ds_read_b32 v127, v249 offset:308\n\t" \
    "v_mfma_f32_16x16x32_bf16 v[64:67], v[32:35], v[16:19], v[120:123]\n\t" \
    "v_cmp_eq_u32 vcc, 0, v244\n\t" \
    "v_mfma_f32_16x16x32_bf16 v[68:71], v[32:35], v[24:27], v[120:123]\n\t" \
    "v_lshrrev_b32 v237, 1, v251\n\t" \
    "v_bfrev_b32 v238, 1\n\t" \
    "s_waitcnt lgkmcnt(6)\n\t" \
    "v_mfma_f32_16x16x32_bf16 v[56:59], v[36:39], v[4:7], v[56:59]\n\t" \
    "v_mfma_f32_16x16x32_bf16 v[60:63], v[36:39], v[12:15], v[60:63]\n\t" \
    "v_cndmask_b32 v254, v238, v237, vcc\n\t" \
    "v_mfma_f32_16x16x32_bf16 v[64:67], v[36:39], v[20:23], v[64:67]\n\t" \
    "s_or_b32 s82, s85, 1\n\t" \
    "v_mfma_f32_16x16x32_bf16 v[68:71], v[36:39], v[28:31], v[68:71]\n\t" \
    "s_waitcnt lgkmcnt(5)\n\t" \
    "v_mfma_f32_16x16x32_bf16 v[72:75], v[40:43], v[0:3], 0\n\t" \
    "s_waitcnt lgkmcnt(4)\n\t" \
    "v_mfma_f32_16x16x32_bf16 v[76:79], v[44:47], v[0:3], 0\n\t" \
    "s_waitcnt lgkmcnt(3)\n\t" \
    "v_mfma_f32_16x16x32_bf16 v[72:75], v[48:51], v[4:7], v[72:75]\n\t" \
    "s_waitcnt lgkmcnt(2)\n\t" \
    "v_mfma_f32_16x16x32_bf16 v[76:79], v[52:55], v[4:7], v[76:79]\n\t" \
    "v_mfma_f32_16x16x32_bf16 v[80:83], v[40:43], v[8:11], 0\n\t" \
    "v_rcp_f32 v56, v56\n\t" \
    "v_mfma_f32_16x16x32_bf16 v[84:87], v[44:47], v[8:11], 0\n\t" \
    "v_rcp_f32 v60, v60\n\t" \
    "v_mfma_f32_16x16x32_bf16 v[80:83], v[48:51], v[12:15], v[80:83]\n\t" \
    "v_rcp_f32 v64, v64\n\t" \
    "v_mfma_f32_16x16x32_bf16 v[84:87], v[52:55], v[12:15], v[84:87]\n\t" \
    "v_rcp_f32 v68, v68\n\t" \
    "v_mfma_f32_16x16x32_bf16 v[88:91], v[40:43], v[16:19], 0\n\t" \
    "v_mfma_f32_16x16x32_bf16 v[92:95], v[44:47], v[16:19], 0\n\t" \
    "v_mul_f32 v72, v72, v56\n\t" \
    "v_mul_f32 v73, v73, v56\n\t" \
    "v_mfma_f32_16x16x32_bf16 v[88:91], v[48:51], v[20:23], v[88:91]\n\t" \
    "v_mul_f32 v74, v74, v56\n\t" \
    "v_mul_f32 v75, v75, v56\n\t" \
    "v_mfma_f32_16x16x32_bf16 v[92:95], v[52:55], v[20:23], v[92:95]\n\t" \
    "v_mul_f32 v76, v76, v56\n\t" \
    "v_mul_f32 v77, v77, v56\n\t" \
    "v_mfma_f32_16x16x32_bf16 v[96:99], v[40:43], v[24:27], 0\n\t" \
    "v_mul_f32 v78, v78, v56\n\t" \
    "v_mul_f32 v79, v79, v56\n\t" \
    "v_mfma_f32_16x16x32_bf16 v[100:103], v[44:47], v[24:27], 0\n\t" \
    "v_cvt_pk_bf16_f32 v104, v72, v73\n\t" \
    "v_cvt_pk_bf16_f32 v105, v74, v75\n\t" \
    "v_mfma_f32_16x16x32_bf16 v[96:99], v[48:51], v[28:31], v[96:99]\n\t" \
    "v_cvt_pk_bf16_f32 v106, v76, v77\n\t" \
    "v_cvt_pk_bf16_f32 v107, v78, v79\n\t" \
    "v_mfma_f32_16x16x32_bf16 v[100:103], v[52:55], v[28:31], v[100:103]\n\t" \
    "buffer_store_dwordx4 v[104:107], v251, %[rsb], s87 offen sc1\n\t" \
    "v_pk_mul_f32 v[80:81], v[80:81], v[60:61] op_sel_hi:[1,0]\n\t" \
    "v_pk_mul_f32 v[82:83], v[82:83], v[60:61] op_sel_hi:[1,0]\n\t" \
    "v_pk_mul_f32 v[84:85], v[84:85], v[60:61] op_sel_hi:[1,0]\n\t" \
    "v_pk_mul_f32 v[86:87], v[86:87], v[60:61] op_sel_hi:[1,0]\n\t" \
    "v_cvt_pk_bf16_f32 v108, v80, v81\n\t" \
    "v_cvt_pk_bf16_f32 v109, v82, v83\n\t" \
    "v_cvt_pk_bf16_f32 v110, v84, v85\n\t" \
    "v_cvt_pk_bf16_f32 v111, v86, v87\n\t" \
    "buffer_store_dwordx4 v[108:111], v251, %[rsb], s87 offen offset:1024 sc1\n\t" \
    "v_pk_mul_f32 v[88:89], v[88:89], v[64:65] op_sel_hi:[1,0]\n\t" \
    "v_pk_mul_f32 v[90:91], v[90:91], v[64:65] op_sel_hi:[1,0]\n\t" \
    "v_pk_mul_f32 v[92:93], v[92:93], v[64:65] op_sel_hi:[1,0]\n\t" \
    "v_pk_mul_f32 v[94:95], v[94:95], v[64:65] op_sel_hi:[1,0]\n\t" \
    "v_cvt_pk_bf16_f32 v112, v88, v89\n\t" \
    "v_cvt_pk_bf16_f32 v113, v90, v91\n\t" \
    "v_cvt_pk_bf16_f32 v114, v92, v93\n\t" \
    "v_cvt_pk_bf16_f32 v115, v94, v95\n\t" \
    "buffer_store_dwordx4 v[112:115], v251, %[rsb], s86 offen sc1\n\t" \
    "v_pk_mul_f32 v[96:97], v[96:97], v[68:69] op_sel_hi:[1,0]\n\t" \
    "v_pk_mul_f32 v[98:99], v[98:99], v[68:69] op_sel_hi:[1,0]\n\t" \
    "v_pk_mul_f32 v[100:101], v[100:101], v[68:69] op_sel_hi:[1,0]\n\t" \
    "v_pk_mul_f32 v[102:103], v[102:103], v[68:69] op_sel_hi:[1,0]\n\t" \
    "v_cvt_pk_bf16_f32 v116, v96, v97\n\t" \
    "v_cvt_pk_bf16_f32 v117, v98, v99\n\t" \
    "v_cvt_pk_bf16_f32 v118, v100, v101\n\t" \
    "v_cvt_pk_bf16_f32 v119, v102, v103\n\t" \
    "v_add3_u32 v32, v104, v105, 0\n\t" \
    "v_add3_u32 v32, v106, v107, v32\n\t" \
    "v_add3_u32 v33, v112, v113, 0\n\t" \
    "v_add3_u32 v33, v114, v115, v33\n\t" \
    "v_add3_u32 v32, v108, v109, v32\n\t" \
    "v_add3_u32 v32, v110, v111, v32\n\t" \
    "v_add3_u32 v33, v116, v117, v33\n\t" \
    "v_add3_u32 v33, v118, v119, v33\n\t" \
    "buffer_store_dwordx4 v[116:119], v251, %[rsb], s86 offen offset:1024 sc1\n\t" \
    "ds_read_b32 v36, v249 offset:8\n\t" \
    "v_permlane32_swap_b32 v32, v33\n\t" \
    "v_add_u32 v32, v32, v33\n\t" \
    "v_mov_b32 v33, v32\n\t" \
    "s_waitcnt lgkmcnt(2)\n\t" \
    "v_readfirstlane_b32 %[rid], v126\n\t" \
    "s_waitcnt lgkmcnt(1)\n\t" \
    "v_readfirstlane_b32 %[t3], v127\n\t" \
    "v_permlane16_swap_b32 v32, v33\n\t" \
    "v_add_u32 v32, v32, v33\n\t" \
    "v_mov_b32 v33, v236\n\t" \
    "ds_read_b32 v238, v249 offset:56\n\t" \
    "v_add_u32_dpp v32, v32, v32 row_ror:8 row_mask:0xf bank_mask:0xf bound_ctrl:1\n\t" \
    "v_mov_b32 v237, 12\n\t" \
    "v_and_b32 v34, 15, v250\n\t" \
    "v_add_u32_dpp v32, v32, v32 row_ror:4 row_mask:0xf bank_mask:0xf bound_ctrl:1\n\t" \
    "s_waitcnt lgkmcnt(1)\n\t" \
    "v_readfirstlane_b32 s80, v36\n\t" \
    "s_and_b32 s85, %[rid], 15\n\t" \
    "v_add_u32_dpp v32, v32, v32 row_ror:2 row_mask:0xf bank_mask:0xf bound_ctrl:1\n\t" \
    "v_cmp_eq_u32_e64 %[rm], s85, v34\n\t" \
    "s_cmp_lg_u32 s80, s82\n\t" \
    "v_add_u32_dpp v32, v32, v32 row_ror:1 row_mask:0xf bank_mask:0xf bound_ctrl:1\n\t" \
    "s_cbranch_scc1 .Lab_sa\n\t" \
    ".Lab_sar:\n\t" \
    "buffer_store_dwordx2 v[32:33], v254, %[rsb], s88 offen sc1\n\t" \
    "ds_write_b32 v248, v237\n\t" \
    "v_and_b32 v252, 48, v250\n\t" \
    "s_lshr_b32 s85, %[rid], 5\n\t" \
    "v_add_u32 v252, 161536, v252\n\t" \
    "s_cmp_eq_u32 s85, %[own]\n\t" \
    "s_cselect_b64 %[rm], %[rm], 0\n\t" \
    "s_waitcnt vmcnt(5)\n\t" \
    "v_cmp_ne_u32 vcc, v236, v233\n\t" \
    "v_cmp_ne_u32_e64 s[92:93], v236, v235\n\t" \
    "s_nop 0\n\t" \
    "s_or_b64 vcc, vcc, s[92:93]\n\t" \
    "s_cbranch_scc1 .Lab_s8\n\t" \
    ".Lab_s8r:\n\t" \
    "v_sub_f32 v254, 1.0, v232\n\t" \
    "v_sub_f32 v255, 1.0, v234\n\t" \
    "v_cvt_pk_bf16_f32 v255, v254, v255\n\t" \
    "v_lshlrev_b32 v254, 16, v255\n\t" \
    "v_and_b32 v255, 0xffff0000, v255\n\t" \
    "s_waitcnt lgkmcnt(1)\n\t" \
    "v_readfirstlane_b32 s85, v238\n\t" \
    "s_cmp_lt_u32 s85, 2\n\t" \
    "s_cbranch_scc1 .Lab_s9\n\t" \
    ".Lab_s9r:\n\t" \
    "s_waitcnt lgkmcnt(0)\n\t"
#define PHASE_C_ASM \
    "s_branch .Lpad_c0\n\t" \
    ".Lc_x:\n\t" \
    "s_mov_b32 s86, 2\n\t" \
    ".Lc_xa:\n\t" \
    "s_lshl_b32 s80, %[grp], 14\n\t" \
    "s_sleep 2\n\t" \
    "buffer_load_dwordx2 v[160:161], v231, %[rsb], s85 offen sc1\n\t" \
    "buffer_load_dwordx4 v[32:35], v240, %[rsb], s80 offen offset:2048 sc1\n\t" \
    "buffer_load_dwordx4 v[36:39], v240, %[rsb], s80 offen offset:3072 sc1\n\t" \
    "buffer_load_dwordx4 v[40:43], v241, %[rsb], s80 offen sc1\n\t" \
    "buffer_load_dwordx4 v[44:47], v241, %[rsb], s80 offen offset:1024 sc1\n\t" \
    "buffer_load_dwordx4 v[48:51], v241, %[rsb], s80 offen offset:2048 sc1\n\t" \
    "buffer_load_dwordx4 v[52:55], v241, %[rsb], s80 offen offset:3072 sc1\n\t" \
    "buffer_load_dwordx4 v[56:59], v242, %[rsb], s80 offen sc1\n\t" \
    "buffer_load_dwordx4 v[60:63], v242, %[rsb], s80 offen offset:1024 sc1\n\t" \
    "buffer_load_dwordx4 v[64:67], v242, %[rsb], s80 offen offset:2048 sc1\n\t" \
    "buffer_load_dwordx4 v[68:71], v242, %[rsb], s80 offen offset:3072 sc1\n\t" \
    "buffer_load_dwordx4 v[72:75], v243, %[rsb], s80 offen sc1\n\t" \
    "buffer_load_dwordx4 v[76:79], v243, %[rsb], s80 offen offset:1024 sc1\n\t" \
    "buffer_load_dwordx4 v[80:83], v243, %[rsb], s80 offen offset:2048 sc1\n\t" \
    "buffer_load_dwordx4 v[84:87], v243, %[rsb], s80 offen offset:3072 sc1\n\t" \
    "s_add_u32 s80, s80, 0x4000\n\t" \
    "s_and_b32 s80, s80, 0xc000\n\t" \
    "s_waitcnt vmcnt(14)\n\t" \
    "v_cmp_ne_u32 vcc, v236, v161\n\t" \
    "s_nop 1\n\t" \
    "s_and_b32 vcc_lo, vcc_lo, s84\n\t" \
    "s_cbranch_scc0 .Lc_l\n\t" \
    "s_sub_u32 s86, s86, 1\n\t" \
    "s_cmp_lg_u32 s86, 0\n\t" \
    "s_cbranch_scc1 .Lc_xa\n\t" \
    "s_waitcnt vmcnt(0) lgkmcnt(0)\n\t" \
    "s_branch .Lc_end\n\t" \
    ".Lc_w0:\n\t" \
    "s_mov_b32 s86, 0x400000\n\t" \
    ".Lc_a0:\n\t" \
    "ds_read_b32 v160, v249 offset:56\n\t" \
    "s_waitcnt lgkmcnt(0)\n\t" \
    "v_readfirstlane_b32 s83, v160\n\t" \
    "s_sub_u32 s86, s86, 1\n\t" \
    "s_cbranch_scc1 .Lc_r0\n\t" \
    "s_cmp_lt_u32 s83, s82\n\t" \
    "s_cbranch_scc1 .Lc_a0\n\t" \
    "s_branch .Lc_r0\n\t" \
    ".Lc_w1:\n\t" \
    "s_mov_b32 s86, 0x400000\n\t" \
    ".Lc_a1:\n\t" \
    "ds_read_b32 v160, v249 offset:56\n\t" \
    "s_waitcnt lgkmcnt(0)\n\t" \
    "v_readfirstlane_b32 s83, v160\n\t" \
    "s_sub_u32 s86, s86, 1\n\t" \
    "s_cbranch_scc1 .Lc_r1\n\t" \
    "s_cmp_lt_u32 s83, s82\n\t" \
    "s_cbranch_scc1 .Lc_a1\n\t" \
    "s_branch .Lc_r1\n\t" \
    ".Lc_y:\n\t" \
    "s_mov_b32 s86, 0x400000\n\t" \
    ".Lc_ya:\n\t" \
    "s_waitcnt lgkmcnt(0)\n\t" \
    "v_readfirstlane_b32 s83, v231\n\t" \
    "s_sub_u32 s86, s86, 1\n\t" \
    "s_cbranch_scc1 .Lc_yb\n\t" \
    "s_cmp_lg_u32 s83, 0\n\t" \
    "s_cbranch_scc1 .Lc_yb\n\t" \
    "ds_read_b32 v231, v249 offset:68\n\t" \
    "ds_read_b128 v[96:99], v252\n\t" \
    "ds_read_b128 v[100:103], v252 offset:64\n\t" \
    "ds_read_b128 v[104:107], v252 offset:128\n\t" \
    "ds_read_b128 v[108:111], v252 offset:192\n\t" \
    "s_branch .Lc_ya\n\t" \
    ".Lc_yb:\n\t" \
    "s_mov_b64 exec, %[rm]\n\t" \
    "s_bitcmp1_b32 %[rid], 4\n\t" \
    "s_cbranch_scc1 .Lc_yc\n\t" \
    "v_pk_mul_f32 v[194:195], v[96:97], v[254:255] op_sel_hi:[1,0]\n\t" \
    "v_pk_mul_f32 v[196:197], v[98:99], v[254:255] op_sel_hi:[1,0]\n\t" \
    "v_pk_mul_f32 v[198:199], v[100:101], v[254:255] op_sel_hi:[1,0]\n\t" \
    "v_pk_mul_f32 v[200:201], v[102:103], v[254:255] op_sel_hi:[1,0]\n\t" \
    "v_pk_mul_f32 v[202:203], v[104:105], v[254:255] op_sel_hi:[1,0]\n\t" \
    "v_pk_mul_f32 v[204:205], v[106:107], v[254:255] op_sel_hi:[1,0]\n\t" \
    "v_pk_mul_f32 v[206:207], v[108:109], v[254:255] op_sel_hi:[1,0]\n\t" \
    "v_pk_mul_f32 v[208:209], v[110:111], v[254:255] op_sel_hi:[1,0]\n\t" \
    "s_mov_b64 exec, -1\n\t" \
    "s_branch .Lc_yr\n\t" \
    ".Lc_yc:\n\t" \
    "v_pk_mul_f32 v[210:211], v[96:97], v[254:255] op_sel:[0,1] op_sel_hi:[1,1]\n\t" \
    "v_pk_mul_f32 v[212:213], v[98:99], v[254:255] op_sel:[0,1] op_sel_hi:[1,1]\n\t" \
    "v_pk_mul_f32 v[214:215], v[100:101], v[254:255] op_sel:[0,1] op_sel_hi:[1,1]\n\t" \
    "v_pk_mul_f32 v[216:217], v[102:103], v[254:255] op_sel:[0,1] op_sel_hi:[1,1]\n\t" \
    "v_pk_mul_f32 v[218:219], v[104:105], v[254:255] op_sel:[0,1] op_sel_hi:[1,1]\n\t" \
    "v_pk_mul_f32 v[220:221], v[106:107], v[254:255] op_sel:[0,1] op_sel_hi:[1,1]\n\t" \
    "v_pk_mul_f32 v[222:223], v[108:109], v[254:255] op_sel:[0,1] op_sel_hi:[1,1]\n\t" \
    "v_pk_mul_f32 v[224:225], v[110:111], v[254:255] op_sel:[0,1] op_sel_hi:[1,1]\n\t" \
    "s_mov_b64 exec, -1\n\t" \
    "s_branch .Lc_yr\n\t" \
    ".p2align 12\n\t" \
    ".Lpad_c0:\n\t" \
    "s_cbranch_execz .Lpad_ret\n\t" \
    "s_setprio 2\n\t" \
    "v_xor_b32 v226, %[flip], v245\n\t" \
    "v_xor_b32 v227, 32, v226\n\t" \
    "v_xor_b32 v228, 64, v226\n\t" \
    "v_xor_b32 v229, 96, v226\n\t" \
    "v_and_b32 v231, 0x7f, v244\n\t" \
    "s_lshl_b32 s80, %[grp], 14\n\t" \
    "s_lshl_b32 s83, %[grp], 3\n\t" \
    "s_lshl_b32 s84, 0xffff, s83\n\t" \
    "s_lshl_b32 s85, %[grp], 6\n\t" \
    "s_add_u32 s85, s85, %[sog]\n\t" \
    "s_mov_b32 s81, 256\n\t" \
    "v_xor_b32 v1, 32, v246\n\t" \
    "v_xor_b32 v2, 64, v246\n\t" \
    "v_xor_b32 v3, 96, v246\n\t" \
    ".Lc_t:\n\t" \
    "s_memrealtime s[86:87]\n\t" \
    "s_sub_u32 s81, s81, 1\n\t" \
    "s_cbranch_scc1 .Lc_go\n\t" \
    "s_waitcnt lgkmcnt(0)\n\t" \
    "s_sub_u32 s86, %[t3], s86\n\t" \
    "s_cmp_lt_i32 s86, 5\n\t" \
    "s_cbranch_scc1 .Lc_go\n\t" \
    "s_cmp_lt_i32 s86, 13\n\t" \
    "s_cbranch_scc1 .Lc_t\n\t" \
    "s_sleep 2\n\t" \
    "s_branch .Lc_t\n\t" \
    ".Lc_go:\n\t" \
    "buffer_load_dwordx2 v[160:161], v231, %[rsb], s85 offen sc1\n\t" \
    "buffer_load_dwordx4 v[32:35], v240, %[rsb], s80 offen offset:2048 sc1\n\t" \
    "buffer_load_dwordx4 v[36:39], v240, %[rsb], s80 offen offset:3072 sc1\n\t" \
    "buffer_load_dwordx4 v[40:43], v241, %[rsb], s80 offen sc1\n\t" \
    "buffer_load_dwordx4 v[44:47], v241, %[rsb], s80 offen offset:1024 sc1\n\t" \
    "buffer_load_dwordx4 v[48:51], v241, %[rsb], s80 offen offset:2048 sc1\n\t" \
    "buffer_load_dwordx4 v[52:55], v241, %[rsb], s80 offen offset:3072 sc1\n\t" \
    "buffer_load_dwordx4 v[56:59], v242, %[rsb], s80 offen sc1\n\t" \
    "buffer_load_dwordx4 v[60:63], v242, %[rsb], s80 offen offset:1024 sc1\n\t" \
    "buffer_load_dwordx4 v[64:67], v242, %[rsb], s80 offen offset:2048 sc1\n\t" \
    "buffer_load_dwordx4 v[68:71], v242, %[rsb], s80 offen offset:3072 sc1\n\t" \
    "buffer_load_dwordx4 v[72:75], v243, %[rsb], s80 offen sc1\n\t" \
    "buffer_load_dwordx4 v[76:79], v243, %[rsb], s80 offen offset:1024 sc1\n\t" \
    "buffer_load_dwordx4 v[80:83], v243, %[rsb], s80 offen offset:2048 sc1\n\t" \
    "buffer_load_dwordx4 v[84:87], v243, %[rsb], s80 offen offset:3072 sc1\n\t" \
    "s_add_u32 s80, s80, 0x4000\n\t" \
    "s_and_b32 s80, s80, 0xc000\n\t" \
    "ds_read_b64 v[196:197], v246\n\t" \
    "ds_read_b64 v[200:201], v1\n\t" \
    "ds_read_b64 v[204:205], v2\n\t" \
    "ds_read_b64 v[208:209], v3\n\t" \
    "ds_read_b64 v[212:213], v246 offset:2048\n\t" \
    "ds_read_b64 v[216:217], v1 offset:2048\n\t" \
    "ds_read_b64 v[220:221], v2 offset:2048\n\t" \
    "ds_read_b64 v[224:225], v3 offset:2048\n\t" \
    "ds_read_b64_tr_b16 v[96:97], v226\n\t" \
    "ds_read_b64_tr_b16 v[98:99], v226 offset:2048\n\t" \
    "ds_read_b64_tr_b16 v[100:101], v227\n\t" \
    "ds_read_b64_tr_b16 v[102:103], v227 offset:2048\n\t" \
    "ds_read_b64_tr_b16 v[104:105], v228\n\t" \
    "ds_read_b64_tr_b16 v[106:107], v228 offset:2048\n\t" \
    "ds_read_b64_tr_b16 v[108:109], v229\n\t" \
    "ds_read_b64_tr_b16 v[110:111], v229 offset:2048\n\t" \
    "ds_read_b64_tr_b16 v[112:113], v226 offset:4096\n\t" \
    "ds_read_b64_tr_b16 v[114:115], v226 offset:6144\n\t" \
    "ds_read_b64_tr_b16 v[116:117], v227 offset:4096\n\t" \
    "ds_read_b64_tr_b16 v[118:119], v227 offset:6144\n\t" \
    "ds_read_b64_tr_b16 v[120:121], v228 offset:4096\n\t" \
    "ds_read_b64_tr_b16 v[122:123], v228 offset:6144\n\t" \
    "ds_read_b64_tr_b16 v[124:125], v229 offset:4096\n\t" \
    "ds_read_b64_tr_b16 v[126:127], v229 offset:6144\n\t" \
    "v_mov_b32 v0, 0\n\t" \
    "v_mov_b32 v1, 0\n\t" \
    "v_mov_b32 v2, 0\n\t" \
    "v_mov_b32 v3, 0\n\t" \
    "v_mov_b32 v4, 0\n\t" \
    "v_mov_b32 v5, 0\n\t" \
    "v_mov_b32 v6, 0\n\t" \
    "v_mov_b32 v7, 0\n\t" \
    "v_mov_b32 v8, 0\n\t" \
    "v_mov_b32 v9, 0\n\t" \
    "v_mov_b32 v10, 0\n\t" \
    "v_mov_b32 v11, 0\n\t" \
    "v_mov_b32 v12, 0\n\t" \
    "v_mov_b32 v13, 0\n\t" \
    "v_mov_b32 v14, 0\n\t" \
    "v_mov_b32 v15, 0\n\t" \
    "v_mov_b32 v16, 0\n\t" \
    "v_mov_b32 v17, 0\n\t" \
    "v_mov_b32 v18, 0\n\t" \
    "v_mov_b32 v19, 0\n\t" \
    "v_mov_b32 v20, 0\n\t" \
    "v_mov_b32 v21, 0\n\t" \
    "v_mov_b32 v22, 0\n\t" \
    "v_mov_b32 v23, 0\n\t" \
    "v_mov_b32 v24, 0\n\t" \
    "v_mov_b32 v25, 0\n\t" \
    "v_mov_b32 v26, 0\n\t" \
    "v_mov_b32 v27, 0\n\t" \
    "v_mov_b32 v28, 0\n\t" \
    "v_mov_b32 v29, 0\n\t" \
    "v_mov_b32 v30, 0\n\t" \
    "v_mov_b32 v31, 0\n\t" \
    "s_waitcnt lgkmcnt(0)\n\t" \
    "v_lshlrev_b32 v194, 16, v196\n\t" \
    "v_and_b32 v195, 0xffff0000, v196\n\t" \
    "v_lshlrev_b32 v196, 16, v197\n\t" \
    "v_and_b32 v197, 0xffff0000, v197\n\t" \
    "v_lshlrev_b32 v198, 16, v200\n\t" \
    "v_and_b32 v199, 0xffff0000, v200\n\t" \
    "v_lshlrev_b32 v200, 16, v201\n\t" \
    "v_and_b32 v201, 0xffff0000, v201\n\t" \
    "v_lshlrev_b32 v202, 16, v204\n\t" \
    "v_and_b32 v203, 0xffff0000, v204\n\t" \
    "v_lshlrev_b32 v204, 16, v205\n\t" \
    "v_and_b32 v205, 0xffff0000, v205\n\t" \
    "v_lshlrev_b32 v206, 16, v208\n\t" \
    "v_and_b32 v207, 0xffff0000, v208\n\t" \
    "v_lshlrev_b32 v208, 16, v209\n\t" \
    "v_and_b32 v209, 0xffff0000, v209\n\t" \
    "v_lshlrev_b32 v210, 16, v212\n\t" \
    "v_and_b32 v211, 0xffff0000, v212\n\t" \
    "v_lshlrev_b32 v212, 16, v213\n\t" \
    "v_and_b32 v213, 0xffff0000, v213\n\t" \
    "v_lshlrev_b32 v214, 16, v216\n\t" \
    "v_and_b32 v215, 0xffff0000, v216\n\t" \
    "v_lshlrev_b32 v216, 16, v217\n\t" \
    "v_and_b32 v217, 0xffff0000, v217\n\t" \
    "v_lshlrev_b32 v218, 16, v220\n\t" \
    "v_and_b32 v219, 0xffff0000, v220\n\t" \
    "v_lshlrev_b32 v220, 16, v221\n\t" \
    "v_and_b32 v221, 0xffff0000, v221\n\t" \
    "v_lshlrev_b32 v222, 16, v224\n\t" \
    "v_and_b32 v223, 0xffff0000, v224\n\t" \
    "v_lshlrev_b32 v224, 16, v225\n\t" \
    "v_and_b32 v225, 0xffff0000, v225\n\t" \
    "v_pk_mul_f32 v[194:195], v[194:195], v[254:255] op_sel_hi:[1,0]\n\t" \
    "v_pk_mul_f32 v[196:197], v[196:197], v[254:255] op_sel_hi:[1,0]\n\t" \
    "v_pk_mul_f32 v[198:199], v[198:199], v[254:255] op_sel_hi:[1,0]\n\t" \
    "v_pk_mul_f32 v[200:201], v[200:201], v[254:255] op_sel_hi:[1,0]\n\t" \
    "v_pk_mul_f32 v[202:203], v[202:203], v[254:255] op_sel_hi:[1,0]\n\t" \
    "v_pk_mul_f32 v[204:205], v[204:205], v[254:255] op_sel_hi:[1,0]\n\t" \
    "v_pk_mul_f32 v[206:207], v[206:207], v[254:255] op_sel_hi:[1,0]\n\t" \
    "v_pk_mul_f32 v[208:209], v[208:209], v[254:255] op_sel_hi:[1,0]\n\t" \
    "v_pk_mul_f32 v[210:211], v[210:211], v[254:255] op_sel:[0,1] op_sel_hi:[1,1]\n\t" \
    "v_pk_mul_f32 v[212:213], v[212:213], v[254:255] op_sel:[0,1] op_sel_hi:[1,1]\n\t" \
    "v_pk_mul_f32 v[214:215], v[214:215], v[254:255] op_sel:[0,1] op_sel_hi:[1,1]\n\t" \
    "v_pk_mul_f32 v[216:217], v[216:217], v[254:255] op_sel:[0,1] op_sel_hi:[1,1]\n\t" \
    "v_pk_mul_f32 v[218:219], v[218:219], v[254:255] op_sel:[0,1] op_sel_hi:[1,1]\n\t" \
    "v_pk_mul_f32 v[220:221], v[220:221], v[254:255] op_sel:[0,1] op_sel_hi:[1,1]\n\t" \
    "v_pk_mul_f32 v[222:223], v[222:223], v[254:255] op_sel:[0,1] op_sel_hi:[1,1]\n\t" \
    "v_pk_mul_f32 v[224:225], v[224:225], v[254:255] op_sel:[0,1] op_sel_hi:[1,1]\n\t" \
    "v_mov_b32 v230, 0\n\t" \
    "s_mov_b32 s81, 2\n\t" \
    "s_mov_b32 s82, 1\n\t" \
    "s_mov_b32 %[st], 1\n\t" \
    "s_waitcnt vmcnt(14)\n\t" \
    "v_cmp_ne_u32 vcc, v236, v161\n\t" \
    "s_nop 1\n\t" \
    "s_and_b32 vcc_lo, vcc_lo, s84\n\t" \
    "s_cbranch_scc1 .Lc_x\n\t" \
    ".Lc_l:\n\t" \
    "s_waitcnt vmcnt(12) lgkmcnt(8)\n\t" \
    "v_mfma_f32_16x16x32_bf16 v[0:3], v[96:99], v[32:35], v[0:3]\n\t" \
    "ds_read_b64_tr_b16 v[128:129], v226 offset:8192\n\t" \
    "buffer_load_dwordx4 v[88:91], v240, %[rsb], s80 offen sc1\n\t" \
    "v_mfma_f32_16x16x32_bf16 v[16:19], v[96:99], v[36:39], v[16:19]\n\t" \
    "ds_read_b64_tr_b16 v[130:131], v226 offset:10240\n\t" \
    "v_add3_u32 v230, v32, v33, v230\n\t" \
    "v_mfma_f32_16x16x32_bf16 v[4:7], v[100:103], v[32:35], v[4:7]\n\t" \
    "ds_read_b64_tr_b16 v[132:133], v227 offset:8192\n\t" \
    "buffer_load_dwordx4 v[92:95], v240, %[rsb], s80 offen offset:1024 sc1\n\t" \
    "v_mfma_f32_16x16x32_bf16 v[20:23], v[100:103], v[36:39], v[20:23]\n\t" \
    "ds_read_b64_tr_b16 v[134:135], v227 offset:10240\n\t" \
    "v_add3_u32 v230, v34, v35, v230\n\t" \
    "v_mfma_f32_16x16x32_bf16 v[8:11], v[104:107], v[32:35], v[8:11]\n\t" \
    "ds_read_b64_tr_b16 v[136:137], v228 offset:8192\n\t" \
    "v_add3_u32 v230, v36, v37, v230\n\t" \
    "v_mfma_f32_16x16x32_bf16 v[24:27], v[104:107], v[36:39], v[24:27]\n\t" \
    "ds_read_b64_tr_b16 v[138:139], v228 offset:10240\n\t" \
    "v_add3_u32 v230, v38, v39, v230\n\t" \
    "v_mfma_f32_16x16x32_bf16 v[12:15], v[108:111], v[32:35], v[12:15]\n\t" \
    "ds_read_b64_tr_b16 v[140:141], v229 offset:8192\n\t" \
    "v_mfma_f32_16x16x32_bf16 v[28:31], v[108:111], v[36:39], v[28:31]\n\t" \
    "ds_read_b64_tr_b16 v[142:143], v229 offset:10240\n\t" \
    "s_waitcnt vmcnt(12) lgkmcnt(8)\n\t" \
    "v_mfma_f32_16x16x32_bf16 v[0:3], v[112:115], v[40:43], v[0:3]\n\t" \
    "ds_read_b64_tr_b16 v[144:145], v226 offset:12288\n\t" \
    "buffer_load_dwordx4 v[32:35], v240, %[rsb], s80 offen offset:2048 sc1\n\t" \
    "v_mfma_f32_16x16x32_bf16 v[16:19], v[112:115], v[44:47], v[16:19]\n\t" \
    "ds_read_b64_tr_b16 v[146:147], v226 offset:14336\n\t" \
    "v_add3_u32 v230, v40, v41, v230\n\t" \
    "v_mfma_f32_16x16x32_bf16 v[4:7], v[116:119], v[40:43], v[4:7]\n\t" \
    "ds_read_b64_tr_b16 v[148:149], v227 offset:12288\n\t" \
    "buffer_load_dwordx4 v[36:39], v240, %[rsb], s80 offen offset:3072 sc1\n\t" \
    "v_mfma_f32_16x16x32_bf16 v[20:23], v[116:119], v[44:47], v[20:23]\n\t" \
    "ds_read_b64_tr_b16 v[150:151], v227 offset:14336\n\t" \
    "v_add3_u32 v230, v42, v43, v230\n\t" \
    "v_mfma_f32_16x16x32_bf16 v[8:11], v[120:123], v[40:43], v[8:11]\n\t" \
    "ds_read_b64_tr_b16 v[152:153], v228 offset:12288\n\t" \
    "v_add3_u32 v230, v44, v45, v230\n\t" \
    "v_mfma_f32_16x16x32_bf16 v[24:27], v[120:123], v[44:47], v[24:27]\n\t" \
    "ds_read_b64_tr_b16 v[154:155], v228 offset:14336\n\t" \
    "v_add3_u32 v230, v46, v47, v230\n\t" \
    "v_mfma_f32_16x16x32_bf16 v[12:15], v[124:127], v[40:43], v[12:15]\n\t" \
    "ds_read_b64_tr_b16 v[156:157], v229 offset:12288\n\t" \
    "v_mfma_f32_16x16x32_bf16 v[28:31], v[124:127], v[44:47], v[28:31]\n\t" \
    "ds_read_b64_tr_b16 v[158:159], v229 offset:14336\n\t" \
    "s_waitcnt vmcnt(12) lgkmcnt(8)\n\t" \
    "v_mfma_f32_16x16x32_bf16 v[0:3], v[128:131], v[48:51], v[0:3]\n\t" \
    "ds_read_b64_tr_b16 v[96:97], v226 offset:16384\n\t" \
    "buffer_load_dwordx4 v[40:43], v241, %[rsb], s80 offen sc1\n\t" \
    "v_mfma_f32_16x16x32_bf16 v[16:19], v[128:131], v[52:55], v[16:19]\n\t" \
    "ds_read_b64_tr_b16 v[98:99], v226 offset:18432\n\t" \
    "v_add3_u32 v230, v48, v49, v230\n\t" \
    "v_mfma_f32_16x16x32_bf16 v[4:7], v[132:135], v[48:51], v[4:7]\n\t" \
    "ds_read_b64_tr_b16 v[100:101], v227 offset:16384\n\t" \
    "buffer_load_dwordx4 v[44:47], v241, %[rsb], s80 offen offset:1024 sc1\n\t" \
    "v_mfma_f32_16x16x32_bf16 v[20:23], v[132:135], v[52:55], v[20:23]\n\t" \
    "ds_read_b64_tr_b16 v[102:103], v227 offset:18432\n\t" \
    "v_add3_u32 v230, v50, v51, v230\n\t" \
    "v_mfma_f32_16x16x32_bf16 v[8:11], v[136:139], v[48:51], v[8:11]\n\t" \
    "ds_read_b64_tr_b16 v[104:105], v228 offset:16384\n\t" \
    "v_add3_u32 v230, v52, v53, v230\n\t" \
    "v_mfma_f32_16x16x32_bf16 v[24:27], v[136:139], v[52:55], v[24:27]\n\t" \
    "ds_read_b64_tr_b16 v[106:107], v228 offset:18432\n\t" \
    "v_add3_u32 v230, v54, v55, v230\n\t" \
    "v_mfma_f32_16x16x32_bf16 v[12:15], v[140:143], v[48:51], v[12:15]\n\t" \
    "ds_read_b64_tr_b16 v[108:109], v229 offset:16384\n\t" \
    "v_mfma_f32_16x16x32_bf16 v[28:31], v[140:143], v[52:55], v[28:31]\n\t" \
    "ds_read_b64_tr_b16 v[110:111], v229 offset:18432\n\t" \
    "s_waitcnt vmcnt(12) lgkmcnt(8)\n\t" \
    "v_mfma_f32_16x16x32_bf16 v[0:3], v[144:147], v[56:59], v[0:3]\n\t" \
    "ds_read_b64_tr_b16 v[112:113], v226 offset:20480\n\t" \
    "buffer_load_dwordx4 v[48:51], v241, %[rsb], s80 offen offset:2048 sc1\n\t" \
    "v_mfma_f32_16x16x32_bf16 v[16:19], v[144:147], v[60:63], v[16:19]\n\t" \
    "ds_read_b64_tr_b16 v[114:115], v226 offset:22528\n\t" \
    "v_add3_u32 v230, v56, v57, v230\n\t" \
    "v_mfma_f32_16x16x32_bf16 v[4:7], v[148:151], v[56:59], v[4:7]\n\t" \
    "ds_read_b64_tr_b16 v[116:117], v227 offset:20480\n\t" \
    "buffer_load_dwordx4 v[52:55], v241, %[rsb], s80 offen offset:3072 sc1\n\t" \
    "v_mfma_f32_16x16x32_bf16 v[20:23], v[148:151], v[60:63], v[20:23]\n\t" \
    "ds_read_b64_tr_b16 v[118:119], v227 offset:22528\n\t" \
    "v_add3_u32 v230, v58, v59, v230\n\t" \
    "v_mfma_f32_16x16x32_bf16 v[8:11], v[152:155], v[56:59], v[8:11]\n\t" \
    "ds_read_b64_tr_b16 v[120:121], v228 offset:20480\n\t" \
    "v_add3_u32 v230, v60, v61, v230\n\t" \
    "v_mfma_f32_16x16x32_bf16 v[24:27], v[152:155], v[60:63], v[24:27]\n\t" \
    "ds_read_b64_tr_b16 v[122:123], v228 offset:22528\n\t" \
    "v_add3_u32 v230, v62, v63, v230\n\t" \
    "v_mfma_f32_16x16x32_bf16 v[12:15], v[156:159], v[56:59], v[12:15]\n\t" \
    "ds_read_b64_tr_b16 v[124:125], v229 offset:20480\n\t" \
    "v_mfma_f32_16x16x32_bf16 v[28:31], v[156:159], v[60:63], v[28:31]\n\t" \
    "ds_read_b64_tr_b16 v[126:127], v229 offset:22528\n\t" \
    "s_waitcnt vmcnt(12) lgkmcnt(8)\n\t" \
    "ds_read_b32 v160, v249 offset:56\n\t" \
    "v_mfma_f32_16x16x32_bf16 v[0:3], v[96:99], v[64:67], v[0:3]\n\t" \
    "ds_read_b64_tr_b16 v[128:129], v226 offset:24576\n\t" \
    "buffer_load_dwordx4 v[56:59], v242, %[rsb], s80 offen sc1\n\t" \
    "v_mfma_f32_16x16x32_bf16 v[16:19], v[96:99], v[68:71], v[16:19]\n\t" \
    "ds_read_b64_tr_b16 v[130:131], v226 offset:26624\n\t" \
    "v_add3_u32 v230, v64, v65, v230\n\t" \
    "v_mfma_f32_16x16x32_bf16 v[4:7], v[100:103], v[64:67], v[4:7]\n\t" \
    "ds_read_b64_tr_b16 v[132:133], v227 offset:24576\n\t" \
    "buffer_load_dwordx4 v[60:63], v242, %[rsb], s80 offen offset:1024 sc1\n\t" \
    "v_mfma_f32_16x16x32_bf16 v[20:23], v[100:103], v[68:71], v[20:23]\n\t" \
    "ds_read_b64_tr_b16 v[134:135], v227 offset:26624\n\t" \
    "v_add3_u32 v230, v66, v67, v230\n\t" \
    "v_mfma_f32_16x16x32_bf16 v[8:11], v[104:107], v[64:67], v[8:11]\n\t" \
    "ds_read_b64_tr_b16 v[136:137], v228 offset:24576\n\t" \
    "v_add3_u32 v230, v68, v69, v230\n\t" \
    "v_mfma_f32_16x16x32_bf16 v[24:27], v[104:107], v[68:71], v[24:27]\n\t" \
    "ds_read_b64_tr_b16 v[138:139], v228 offset:26624\n\t" \
    "v_add3_u32 v230, v70, v71, v230\n\t" \
    "v_mfma_f32_16x16x32_bf16 v[12:15], v[108:111], v[64:67], v[12:15]\n\t" \
    "ds_read_b64_tr_b16 v[140:141], v229 offset:24576\n\t" \
    "v_mfma_f32_16x16x32_bf16 v[28:31], v[108:111], v[68:71], v[28:31]\n\t" \
    "ds_read_b64_tr_b16 v[142:143], v229 offset:26624\n\t" \
    "s_waitcnt vmcnt(12) lgkmcnt(8)\n\t" \
    "v_mfma_f32_16x16x32_bf16 v[0:3], v[112:115], v[72:75], v[0:3]\n\t" \
    "ds_read_b64_tr_b16 v[144:145], v226 offset:28672\n\t" \
    "buffer_load_dwordx4 v[64:67], v242, %[rsb], s80 offen offset:2048 sc1\n\t" \
    "v_mfma_f32_16x16x32_bf16 v[16:19], v[112:115], v[76:79], v[16:19]\n\t" \
    "ds_read_b64_tr_b16 v[146:147], v226 offset:30720\n\t" \
    "v_add3_u32 v230, v72, v73, v230\n\t" \
    "v_mfma_f32_16x16x32_bf16 v[4:7], v[116:119], v[72:75], v[4:7]\n\t" \
    "ds_read_b64_tr_b16 v[148:149], v227 offset:28672\n\t" \
    "buffer_load_dwordx4 v[68:71], v242, %[rsb], s80 offen offset:3072 sc1\n\t" \
    "v_add_u32 v226, 0x8000, v226\n\t" \
    "v_and_b32 v226, 0x1ffff, v226\n\t" \
    "v_mfma_f32_16x16x32_bf16 v[20:23], v[116:119], v[76:79], v[20:23]\n\t" \
    "ds_read_b64_tr_b16 v[150:151], v227 offset:30720\n\t" \
    "v_add3_u32 v230, v74, v75, v230\n\t" \
    "v_mfma_f32_16x16x32_bf16 v[8:11], v[120:123], v[72:75], v[8:11]\n\t" \
    "ds_read_b64_tr_b16 v[152:153], v228 offset:28672\n\t" \
    "v_add3_u32 v230, v76, v77, v230\n\t" \
    "v_add_u32 v227, 0x8000, v227\n\t" \
    "v_and_b32 v227, 0x1ffff, v227\n\t" \
    "v_mfma_f32_16x16x32_bf16 v[24:27], v[120:123], v[76:79], v[24:27]\n\t" \
    "ds_read_b64_tr_b16 v[154:155], v228 offset:30720\n\t" \
    "v_add3_u32 v230, v78, v79, v230\n\t" \
    "v_mfma_f32_16x16x32_bf16 v[12:15], v[124:127], v[72:75], v[12:15]\n\t" \
    "v_readfirstlane_b32 s83, v160\n\t" \
    "s_add_u32 s82, s82, 1\n\t" \
    "ds_read_b64_tr_b16 v[156:157], v229 offset:28672\n\t" \
    "v_add_u32 v228, 0x8000, v228\n\t" \
    "v_and_b32 v228, 0x1ffff, v228\n\t" \
    "v_mfma_f32_16x16x32_bf16 v[28:31], v[124:127], v[76:79], v[28:31]\n\t" \
    "s_cmp_lt_u32 s83, s82\n\t" \
    "s_cbranch_scc1 .Lc_w0\n\t" \
    ".Lc_r0:\n\t" \
    "ds_read_b64_tr_b16 v[158:159], v229 offset:30720\n\t" \
    "v_add_u32 v229, 0x8000, v229\n\t" \
    "v_and_b32 v229, 0x1ffff, v229\n\t" \
    "s_waitcnt vmcnt(12) lgkmcnt(8)\n\t" \
    "v_mfma_f32_16x16x32_bf16 v[0:3], v[128:131], v[80:83], v[0:3]\n\t" \
    "ds_read_b64_tr_b16 v[96:97], v226\n\t" \
    "buffer_load_dwordx4 v[72:75], v243, %[rsb], s80 offen sc1\n\t" \
    "v_mfma_f32_16x16x32_bf16 v[16:19], v[128:131], v[84:87], v[16:19]\n\t" \
    "ds_read_b64_tr_b16 v[98:99], v226 offset:2048\n\t" \
    "v_add3_u32 v230, v80, v81, v230\n\t" \
    "v_mfma_f32_16x16x32_bf16 v[4:7], v[132:135], v[80:83], v[4:7]\n\t" \
    "ds_read_b64_tr_b16 v[100:101], v227\n\t" \
    "buffer_load_dwordx4 v[76:79], v243, %[rsb], s80 offen offset:1024 sc1\n\t" \
    "v_mfma_f32_16x16x32_bf16 v[20:23], v[132:135], v[84:87], v[20:23]\n\t" \
    "ds_read_b64_tr_b16 v[102:103], v227 offset:2048\n\t" \
    "v_add3_u32 v230, v82, v83, v230\n\t" \
    "v_mfma_f32_16x16x32_bf16 v[8:11], v[136:139], v[80:83], v[8:11]\n\t" \
    "ds_read_b64_tr_b16 v[104:105], v228\n\t" \
    "v_add3_u32 v230, v84, v85, v230\n\t" \
    "v_mfma_f32_16x16x32_bf16 v[24:27], v[136:139], v[84:87], v[24:27]\n\t" \
    "ds_read_b64_tr_b16 v[106:107], v228 offset:2048\n\t" \
    "v_add3_u32 v230, v86, v87, v230\n\t" \
    "v_mfma_f32_16x16x32_bf16 v[12:15], v[140:143], v[80:83], v[12:15]\n\t" \
    "ds_read_b64_tr_b16 v[108:109], v229\n\t" \
    "v_mfma_f32_16x16x32_bf16 v[28:31], v[140:143], v[84:87], v[28:31]\n\t" \
    "ds_read_b64_tr_b16 v[110:111], v229 offset:2048\n\t" \
    "s_waitcnt vmcnt(12) lgkmcnt(8)\n\t" \
    "v_mfma_f32_16x16x32_bf16 v[0:3], v[144:147], v[88:91], v[0:3]\n\t" \
    "ds_read_b64_tr_b16 v[112:113], v226 offset:4096\n\t" \
    "buffer_load_dwordx4 v[80:83], v243, %[rsb], s80 offen offset:2048 sc1\n\t" \
    "v_mfma_f32_16x16x32_bf16 v[16:19], v[144:147], v[92:95], v[16:19]\n\t" \
    "ds_read_b64_tr_b16 v[114:115], v226 offset:6144\n\t" \
    "v_add3_u32 v230, v88, v89, v230\n\t" \
    "v_mfma_f32_16x16x32_bf16 v[4:7], v[148:151], v[88:91], v[4:7]\n\t" \
    "ds_read_b64_tr_b16 v[116:117], v227 offset:4096\n\t" \
    "buffer_load_dwordx4 v[84:87], v243, %[rsb], s80 offen offset:3072 sc1\n\t" \
    "v_mfma_f32_16x16x32_bf16 v[20:23], v[148:151], v[92:95], v[20:23]\n\t" \
    "ds_read_b64_tr_b16 v[118:119], v227 offset:6144\n\t" \
    "v_add3_u32 v230, v90, v91, v230\n\t" \
    "v_mfma_f32_16x16x32_bf16 v[8:11], v[152:155], v[88:91], v[8:11]\n\t" \
    "ds_read_b64_tr_b16 v[120:121], v228 offset:4096\n\t" \
    "v_add3_u32 v230, v92, v93, v230\n\t" \
    "v_mfma_f32_16x16x32_bf16 v[24:27], v[152:155], v[92:95], v[24:27]\n\t" \
    "ds_read_b64_tr_b16 v[122:123], v228 offset:6144\n\t" \
    "v_add3_u32 v230, v94, v95, v230\n\t" \
    "v_mfma_f32_16x16x32_bf16 v[12:15], v[156:159], v[88:91], v[12:15]\n\t" \
    "ds_read_b64_tr_b16 v[124:125], v229 offset:4096\n\t" \
    "s_add_u32 s80, s80, 0x4000\n\t" \
    "s_and_b32 s80, s80, 0xc000\n\t" \
    "v_mfma_f32_16x16x32_bf16 v[28:31], v[156:159], v[92:95], v[28:31]\n\t" \
    "ds_read_b64_tr_b16 v[126:127], v229 offset:6144\n\t" \
    "s_sub_u32 s81, s81, 1\n\t" \
    "s_cmp_lg_u32 s81, 0\n\t" \
    "s_cbranch_scc1 .Lc_l\n\t" \
    "s_waitcnt vmcnt(12) lgkmcnt(8)\n\t" \
    "v_mfma_f32_16x16x32_bf16 v[162:165], v[96:99], v[32:35], 0\n\t" \
    "ds_read_b64_tr_b16 v[128:129], v226 offset:8192\n\t" \
    "buffer_load_dwordx4 v[88:91], v240, %[rsb], s80 offen sc1\n\t" \
    "v_mfma_f32_16x16x32_bf16 v[178:181], v[96:99], v[36:39], 0\n\t" \
    "ds_read_b64_tr_b16 v[130:131], v226 offset:10240\n\t" \
    "v_add3_u32 v230, v32, v33, v230\n\t" \
    "v_mfma_f32_16x16x32_bf16 v[166:169], v[100:103], v[32:35], 0\n\t" \
    "ds_read_b64_tr_b16 v[132:133], v227 offset:8192\n\t" \
    "buffer_load_dwordx4 v[92:95], v240, %[rsb], s80 offen offset:1024 sc1\n\t" \
    "v_mfma_f32_16x16x32_bf16 v[182:185], v[100:103], v[36:39], 0\n\t" \
    "ds_read_b64_tr_b16 v[134:135], v227 offset:10240\n\t" \
    "v_add3_u32 v230, v34, v35, v230\n\t" \
    "v_mfma_f32_16x16x32_bf16 v[170:173], v[104:107], v[32:35], 0\n\t" \
    "ds_read_b64_tr_b16 v[136:137], v228 offset:8192\n\t" \
    "v_add3_u32 v230, v36, v37, v230\n\t" \
    "v_mfma_f32_16x16x32_bf16 v[186:189], v[104:107], v[36:39], 0\n\t" \
    "ds_read_b64_tr_b16 v[138:139], v228 offset:10240\n\t" \
    "v_add3_u32 v230, v38, v39, v230\n\t" \
    "v_mfma_f32_16x16x32_bf16 v[174:177], v[108:111], v[32:35], 0\n\t" \
    "ds_read_b64_tr_b16 v[140:141], v229 offset:8192\n\t" \
    "v_mfma_f32_16x16x32_bf16 v[190:193], v[108:111], v[36:39], 0\n\t" \
    "ds_read_b64_tr_b16 v[142:143], v229 offset:10240\n\t" \
    "s_waitcnt vmcnt(12) lgkmcnt(8)\n\t" \
    "v_mfma_f32_16x16x32_bf16 v[162:165], v[112:115], v[40:43], v[162:165]\n\t" \
    "ds_read_b64_tr_b16 v[144:145], v226 offset:12288\n\t" \
    "buffer_load_dwordx4 v[32:35], v240, %[rsb], s80 offen offset:2048 sc1\n\t" \
    "v_mfma_f32_16x16x32_bf16 v[178:181], v[112:115], v[44:47], v[178:181]\n\t" \
    "ds_read_b64_tr_b16 v[146:147], v226 offset:14336\n\t" \
    "v_add3_u32 v230, v40, v41, v230\n\t" \
    "v_mfma_f32_16x16x32_bf16 v[166:169], v[116:119], v[40:43], v[166:169]\n\t" \
    "ds_read_b64_tr_b16 v[148:149], v227 offset:12288\n\t" \
    "buffer_load_dwordx4 v[36:39], v240, %[rsb], s80 offen offset:3072 sc1\n\t" \
    "v_mfma_f32_16x16x32_bf16 v[182:185], v[116:119], v[44:47], v[182:185]\n\t" \
    "ds_read_b64_tr_b16 v[150:151], v227 offset:14336\n\t" \
    "v_add3_u32 v230, v42, v43, v230\n\t" \
    "v_mfma_f32_16x16x32_bf16 v[170:173], v[120:123], v[40:43], v[170:173]\n\t" \
    "ds_read_b64_tr_b16 v[152:153], v228 offset:12288\n\t" \
    "v_add3_u32 v230, v44, v45, v230\n\t" \
    "v_mfma_f32_16x16x32_bf16 v[186:189], v[120:123], v[44:47], v[186:189]\n\t" \
    "ds_read_b64_tr_b16 v[154:155], v228 offset:14336\n\t" \
    "v_add3_u32 v230, v46, v47, v230\n\t" \
    "v_mfma_f32_16x16x32_bf16 v[174:177], v[124:127], v[40:43], v[174:177]\n\t" \
    "ds_read_b64_tr_b16 v[156:157], v229 offset:12288\n\t" \
    "v_mfma_f32_16x16x32_bf16 v[190:193], v[124:127], v[44:47], v[190:193]\n\t" \
    "ds_read_b64_tr_b16 v[158:159], v229 offset:14336\n\t" \
    "s_waitcnt vmcnt(12) lgkmcnt(8)\n\t" \
    "v_mfma_f32_16x16x32_bf16 v[162:165], v[128:131], v[48:51], v[162:165]\n\t" \
    "ds_read_b64_tr_b16 v[96:97], v226 offset:16384\n\t" \
    "buffer_load_dwordx4 v[40:43], v241, %[rsb], s80 offen sc1\n\t" \
    "v_mfma_f32_16x16x32_bf16 v[178:181], v[128:131], v[52:55], v[178:181]\n\t" \
    "ds_read_b64_tr_b16 v[98:99], v226 offset:18432\n\t" \
    "v_add3_u32 v230, v48, v49, v230\n\t" \
    "v_mfma_f32_16x16x32_bf16 v[166:169], v[132:135], v[48:51], v[166:169]\n\t" \
    "ds_read_b64_tr_b16 v[100:101], v227 offset:16384\n\t" \
    "buffer_load_dwordx4 v[44:47], v241, %[rsb], s80 offen offset:1024 sc1\n\t" \
    "v_mfma_f32_16x16x32_bf16 v[182:185], v[132:135], v[52:55], v[182:185]\n\t" \
    "ds_read_b64_tr_b16 v[102:103], v227 offset:18432\n\t" \
    "v_add3_u32 v230, v50, v51, v230\n\t" \
    "v_mfma_f32_16x16x32_bf16 v[170:173], v[136:139], v[48:51], v[170:173]\n\t" \
    "ds_read_b64_tr_b16 v[104:105], v228 offset:16384\n\t" \
    "v_add3_u32 v230, v52, v53, v230\n\t" \
    "v_mfma_f32_16x16x32_bf16 v[186:189], v[136:139], v[52:55], v[186:189]\n\t" \
    "ds_read_b64_tr_b16 v[106:107], v228 offset:18432\n\t" \
    "v_add3_u32 v230, v54, v55, v230\n\t" \
    "v_mfma_f32_16x16x32_bf16 v[174:177], v[140:143], v[48:51], v[174:177]\n\t" \
    "ds_read_b64_tr_b16 v[108:109], v229 offset:16384\n\t" \
    "v_mfma_f32_16x16x32_bf16 v[190:193], v[140:143], v[52:55], v[190:193]\n\t" \
    "ds_read_b64_tr_b16 v[110:111], v229 offset:18432\n\t" \
    "s_waitcnt vmcnt(12) lgkmcnt(8)\n\t" \
    "v_mfma_f32_16x16x32_bf16 v[162:165], v[144:147], v[56:59], v[162:165]\n\t" \
    "ds_read_b64_tr_b16 v[112:113], v226 offset:20480\n\t" \
    "buffer_load_dwordx4 v[48:51], v241, %[rsb], s80 offen offset:2048 sc1\n\t" \
    "v_mfma_f32_16x16x32_bf16 v[178:181], v[144:147], v[60:63], v[178:181]\n\t" \
    "ds_read_b64_tr_b16 v[114:115], v226 offset:22528\n\t" \
    "v_add3_u32 v230, v56, v57, v230\n\t" \
    "v_mfma_f32_16x16x32_bf16 v[166:169], v[148:151], v[56:59], v[166:169]\n\t" \
    "ds_read_b64_tr_b16 v[116:117], v227 offset:20480\n\t" \
    "buffer_load_dwordx4 v[52:55], v241, %[rsb], s80 offen offset:3072 sc1\n\t" \
    "v_mfma_f32_16x16x32_bf16 v[182:185], v[148:151], v[60:63], v[182:185]\n\t" \
    "ds_read_b64_tr_b16 v[118:119], v227 offset:22528\n\t" \
    "v_add3_u32 v230, v58, v59, v230\n\t" \
    "v_mfma_f32_16x16x32_bf16 v[170:173], v[152:155], v[56:59], v[170:173]\n\t" \
    "ds_read_b64_tr_b16 v[120:121], v228 offset:20480\n\t" \
    "v_add3_u32 v230, v60, v61, v230\n\t" \
    "v_mfma_f32_16x16x32_bf16 v[186:189], v[152:155], v[60:63], v[186:189]\n\t" \
    "ds_read_b64_tr_b16 v[122:123], v228 offset:22528\n\t" \
    "v_add3_u32 v230, v62, v63, v230\n\t" \
    "v_mfma_f32_16x16x32_bf16 v[174:177], v[156:159], v[56:59], v[174:177]\n\t" \
    "ds_read_b64_tr_b16 v[124:125], v229 offset:20480\n\t" \
    "v_mfma_f32_16x16x32_bf16 v[190:193], v[156:159], v[60:63], v[190:193]\n\t" \
    "ds_read_b64_tr_b16 v[126:127], v229 offset:22528\n\t" \
    "s_waitcnt vmcnt(12) lgkmcnt(8)\n\t" \
    "ds_read_b32 v160, v249 offset:56\n\t" \
    "v_mfma_f32_16x16x32_bf16 v[162:165], v[96:99], v[64:67], v[162:165]\n\t" \
    "ds_read_b64_tr_b16 v[128:129], v226 offset:24576\n\t" \
    "buffer_load_dwordx4 v[56:59], v242, %[rsb], s80 offen sc1\n\t" \
    "v_mfma_f32_16x16x32_bf16 v[178:181], v[96:99], v[68:71], v[178:181]\n\t" \
    "ds_read_b64_tr_b16 v[130:131], v226 offset:26624\n\t" \
    "v_add3_u32 v230, v64, v65, v230\n\t" \
    "v_mfma_f32_16x16x32_bf16 v[166:169], v[100:103], v[64:67], v[166:169]\n\t" \
    "ds_read_b64_tr_b16 v[132:133], v227 offset:24576\n\t" \
    "buffer_load_dwordx4 v[60:63], v242, %[rsb], s80 offen offset:1024 sc1\n\t" \
    "v_mfma_f32_16x16x32_bf16 v[182:185], v[100:103], v[68:71], v[182:185]\n\t" \
    "ds_read_b64_tr_b16 v[134:135], v227 offset:26624\n\t" \
    "v_add3_u32 v230, v66, v67, v230\n\t" \
    "v_mfma_f32_16x16x32_bf16 v[170:173], v[104:107], v[64:67], v[170:173]\n\t" \
    "ds_read_b64_tr_b16 v[136:137], v228 offset:24576\n\t" \
    "v_add3_u32 v230, v68, v69, v230\n\t" \
    "v_mfma_f32_16x16x32_bf16 v[186:189], v[104:107], v[68:71], v[186:189]\n\t" \
    "ds_read_b64_tr_b16 v[138:139], v228 offset:26624\n\t" \
    "v_add3_u32 v230, v70, v71, v230\n\t" \
    "v_mfma_f32_16x16x32_bf16 v[174:177], v[108:111], v[64:67], v[174:177]\n\t" \
    "ds_read_b64_tr_b16 v[140:141], v229 offset:24576\n\t" \
    "v_mfma_f32_16x16x32_bf16 v[190:193], v[108:111], v[68:71], v[190:193]\n\t" \
    "ds_read_b64_tr_b16 v[142:143], v229 offset:26624\n\t" \
    "s_waitcnt vmcnt(12) lgkmcnt(8)\n\t" \
    "v_mfma_f32_16x16x32_bf16 v[162:165], v[112:115], v[72:75], v[162:165]\n\t" \
    "ds_read_b64_tr_b16 v[144:145], v226 offset:28672\n\t" \
    "buffer_load_dwordx4 v[64:67], v242, %[rsb], s80 offen offset:2048 sc1\n\t" \
    "v_mfma_f32_16x16x32_bf16 v[178:181], v[112:115], v[76:79], v[178:181]\n\t" \
    "ds_read_b64_tr_b16 v[146:147], v226 offset:30720\n\t" \
    "v_add3_u32 v230, v72, v73, v230\n\t" \
    "v_mfma_f32_16x16x32_bf16 v[166:169], v[116:119], v[72:75], v[166:169]\n\t" \
    "ds_read_b64_tr_b16 v[148:149], v227 offset:28672\n\t" \
    "buffer_load_dwordx4 v[68:71], v242, %[rsb], s80 offen offset:3072 sc1\n\t" \
    "v_add_u32 v226, 0x8000, v226\n\t" \
    "v_and_b32 v226, 0x1ffff, v226\n\t" \
    "v_mfma_f32_16x16x32_bf16 v[182:185], v[116:119], v[76:79], v[182:185]\n\t" \
    "ds_read_b64_tr_b16 v[150:151], v227 offset:30720\n\t" \
    "v_add3_u32 v230, v74, v75, v230\n\t" \
    "v_mfma_f32_16x16x32_bf16 v[170:173], v[120:123], v[72:75], v[170:173]\n\t" \
    "ds_read_b64_tr_b16 v[152:153], v228 offset:28672\n\t" \
    "v_add3_u32 v230, v76, v77, v230\n\t" \
    "v_add_u32 v227, 0x8000, v227\n\t" \
    "v_and_b32 v227, 0x1ffff, v227\n\t" \
    "v_mfma_f32_16x16x32_bf16 v[186:189], v[120:123], v[76:79], v[186:189]\n\t" \
    "ds_read_b64_tr_b16 v[154:155], v228 offset:30720\n\t" \
    "v_add3_u32 v230, v78, v79, v230\n\t" \
    "v_mfma_f32_16x16x32_bf16 v[174:177], v[124:127], v[72:75], v[174:177]\n\t" \
    "v_readfirstlane_b32 s83, v160\n\t" \
    "s_add_u32 s82, s82, 1\n\t" \
    "ds_read_b64_tr_b16 v[156:157], v229 offset:28672\n\t" \
    "v_add_u32 v228, 0x8000, v228\n\t" \
    "v_and_b32 v228, 0x1ffff, v228\n\t" \
    "v_mfma_f32_16x16x32_bf16 v[190:193], v[124:127], v[76:79], v[190:193]\n\t" \
    "s_cmp_lt_u32 s83, s82\n\t" \
    "s_cbranch_scc1 .Lc_w1\n\t" \
    ".Lc_r1:\n\t" \
    "ds_read_b64_tr_b16 v[158:159], v229 offset:30720\n\t" \
    "v_add_u32 v229, 0x8000, v229\n\t" \
    "v_and_b32 v229, 0x1ffff, v229\n\t" \
    "s_waitcnt vmcnt(12) lgkmcnt(8)\n\t" \
    "v_mfma_f32_16x16x32_bf16 v[162:165], v[128:131], v[80:83], v[162:165]\n\t" \
    "ds_read_b64_tr_b16 v[96:97], v226\n\t" \
    "buffer_load_dwordx4 v[72:75], v243, %[rsb], s80 offen sc1\n\t" \
    "v_mfma_f32_16x16x32_bf16 v[178:181], v[128:131], v[84:87], v[178:181]\n\t" \
    "ds_read_b64_tr_b16 v[98:99], v226 offset:2048\n\t" \
    "v_add3_u32 v230, v80, v81, v230\n\t" \
    "v_mfma_f32_16x16x32_bf16 v[166:169], v[132:135], v[80:83], v[166:169]\n\t" \
    "ds_read_b64_tr_b16 v[100:101], v227\n\t" \
    "buffer_load_dwordx4 v[76:79], v243, %[rsb], s80 offen offset:1024 sc1\n\t" \
    "v_mfma_f32_16x16x32_bf16 v[182:185], v[132:135], v[84:87], v[182:185]\n\t" \
    "ds_read_b64_tr_b16 v[102:103], v227 offset:2048\n\t" \
    "v_add3_u32 v230, v82, v83, v230\n\t" \
    "v_mfma_f32_16x16x32_bf16 v[170:173], v[136:139], v[80:83], v[170:173]\n\t" \
    "ds_read_b64_tr_b16 v[104:105], v228\n\t" \
    "v_add3_u32 v230, v84, v85, v230\n\t" \
    "v_mfma_f32_16x16x32_bf16 v[186:189], v[136:139], v[84:87], v[186:189]\n\t" \
    "ds_read_b64_tr_b16 v[106:107], v228 offset:2048\n\t" \
    "v_add3_u32 v230, v86, v87, v230\n\t" \
    "v_mfma_f32_16x16x32_bf16 v[174:177], v[140:143], v[80:83], v[174:177]\n\t" \
    "ds_read_b64_tr_b16 v[108:109], v229\n\t" \
    "v_mfma_f32_16x16x32_bf16 v[190:193], v[140:143], v[84:87], v[190:193]\n\t" \
    "ds_read_b64_tr_b16 v[110:111], v229 offset:2048\n\t" \
    "s_waitcnt vmcnt(12) lgkmcnt(8)\n\t" \
    "v_mfma_f32_16x16x32_bf16 v[162:165], v[144:147], v[88:91], v[162:165]\n\t" \
    "ds_read_b64_tr_b16 v[112:113], v226 offset:4096\n\t" \
    "buffer_load_dwordx4 v[80:83], v243, %[rsb], s80 offen offset:2048 sc1\n\t" \
    "v_mfma_f32_16x16x32_bf16 v[178:181], v[144:147], v[92:95], v[178:181]\n\t" \
    "ds_read_b64_tr_b16 v[114:115], v226 offset:6144\n\t" \
    "v_add3_u32 v230, v88, v89, v230\n\t" \
    "v_mfma_f32_16x16x32_bf16 v[166:169], v[148:151], v[88:91], v[166:169]\n\t" \
    "ds_read_b64_tr_b16 v[116:117], v227 offset:4096\n\t" \
    "buffer_load_dwordx4 v[84:87], v243, %[rsb], s80 offen offset:3072 sc1\n\t" \
    "v_mfma_f32_16x16x32_bf16 v[182:185], v[148:151], v[92:95], v[182:185]\n\t" \
    "ds_read_b64_tr_b16 v[118:119], v227 offset:6144\n\t" \
    "v_add3_u32 v230, v90, v91, v230\n\t" \
    "v_mfma_f32_16x16x32_bf16 v[170:173], v[152:155], v[88:91], v[170:173]\n\t" \
    "ds_read_b64_tr_b16 v[120:121], v228 offset:4096\n\t" \
    "v_add3_u32 v230, v92, v93, v230\n\t" \
    "v_mfma_f32_16x16x32_bf16 v[186:189], v[152:155], v[92:95], v[186:189]\n\t" \
    "ds_read_b64_tr_b16 v[122:123], v228 offset:6144\n\t" \
    "v_add3_u32 v230, v94, v95, v230\n\t" \
    "v_mfma_f32_16x16x32_bf16 v[174:177], v[156:159], v[88:91], v[174:177]\n\t" \
    "ds_read_b64_tr_b16 v[124:125], v229 offset:4096\n\t" \
    "s_add_u32 s80, s80, 0x4000\n\t" \
    "s_and_b32 s80, s80, 0xc000\n\t" \
    "v_mfma_f32_16x16x32_bf16 v[190:193], v[156:159], v[92:95], v[190:193]\n\t" \
    "ds_read_b64_tr_b16 v[126:127], v229 offset:6144\n\t" \
    "s_waitcnt vmcnt(12) lgkmcnt(8)\n\t" \
    "v_mfma_f32_16x16x32_bf16 v[162:165], v[96:99], v[32:35], v[162:165]\n\t" \
    "ds_read_b64_tr_b16 v[128:129], v226 offset:8192\n\t" \
    "buffer_load_dwordx4 v[88:91], v240, %[rsb], s80 offen sc1\n\t" \
    "v_mfma_f32_16x16x32_bf16 v[178:181], v[96:99], v[36:39], v[178:181]\n\t" \
    "ds_read_b64_tr_b16 v[130:131], v226 offset:10240\n\t" \
    "v_add3_u32 v230, v32, v33, v230\n\t" \
    ".Lpad_c1:\n\t" \
    "s_cbranch_execz .Lpad_ret\n\t" \
    "v_mfma_f32_16x16x32_bf16 v[166:169], v[100:103], v[32:35], v[166:169]\n\t" \
    "ds_read_b64_tr_b16 v[132:133], v227 offset:8192\n\t" \
    "buffer_load_dwordx4 v[92:95], v240, %[rsb], s80 offen offset:1024 sc1\n\t" \
    "v_mfma_f32_16x16x32_bf16 v[182:185], v[100:103], v[36:39], v[182:185]\n\t" \
    "ds_read_b64_tr_b16 v[134:135], v227 offset:10240\n\t" \
    "v_add3_u32 v230, v34, v35, v230\n\t" \
    "v_mfma_f32_16x16x32_bf16 v[170:173], v[104:107], v[32:35], v[170:173]\n\t" \
    "ds_read_b64_tr_b16 v[136:137], v228 offset:8192\n\t" \
    "v_add3_u32 v230, v36, v37, v230\n\t" \
    "v_mfma_f32_16x16x32_bf16 v[186:189], v[104:107], v[36:39], v[186:189]\n\t" \
    "ds_read_b64_tr_b16 v[138:139], v228 offset:10240\n\t" \
    "v_add3_u32 v230, v38, v39, v230\n\t" \
    "v_mfma_f32_16x16x32_bf16 v[174:177], v[108:111], v[32:35], v[174:177]\n\t" \
    "ds_read_b64_tr_b16 v[140:141], v229 offset:8192\n\t" \
    "buffer_load_dwordx2 v[160:161], v244, %[rsb], %[sog] offen sc1\n\t" \
    "v_mfma_f32_16x16x32_bf16 v[190:193], v[108:111], v[36:39], v[190:193]\n\t" \
    "ds_read_b64_tr_b16 v[142:143], v229 offset:10240\n\t" \
    "s_waitcnt vmcnt(13) lgkmcnt(8)\n\t" \
    "v_mfma_f32_16x16x32_bf16 v[162:165], v[112:115], v[40:43], v[162:165]\n\t" \
    "ds_read_b64_tr_b16 v[144:145], v226 offset:12288\n\t" \
    "v_mfma_f32_16x16x32_bf16 v[178:181], v[112:115], v[44:47], v[178:181]\n\t" \
    "ds_read_b64_tr_b16 v[146:147], v226 offset:14336\n\t" \
    "v_add3_u32 v230, v40, v41, v230\n\t" \
    "v_mfma_f32_16x16x32_bf16 v[166:169], v[116:119], v[40:43], v[166:169]\n\t" \
    "ds_read_b64_tr_b16 v[148:149], v227 offset:12288\n\t" \
    "v_mfma_f32_16x16x32_bf16 v[182:185], v[116:119], v[44:47], v[182:185]\n\t" \
    "ds_read_b64_tr_b16 v[150:151], v227 offset:14336\n\t" \
    "v_add3_u32 v230, v42, v43, v230\n\t" \
    "v_mfma_f32_16x16x32_bf16 v[170:173], v[120:123], v[40:43], v[170:173]\n\t" \
    "ds_read_b64_tr_b16 v[152:153], v228 offset:12288\n\t" \
    "v_add3_u32 v230, v44, v45, v230\n\t" \
    "v_mfma_f32_16x16x32_bf16 v[186:189], v[120:123], v[44:47], v[186:189]\n\t" \
    "ds_read_b64_tr_b16 v[154:155], v228 offset:14336\n\t" \
    "v_add3_u32 v230, v46, v47, v230\n\t" \
    "v_mfma_f32_16x16x32_bf16 v[174:177], v[124:127], v[40:43], v[174:177]\n\t" \
    "ds_read_b64_tr_b16 v[156:157], v229 offset:12288\n\t" \
    "v_mfma_f32_16x16x32_bf16 v[190:193], v[124:127], v[44:47], v[190:193]\n\t" \
    "ds_read_b64_tr_b16 v[158:159], v229 offset:14336\n\t" \
    "s_waitcnt vmcnt(11) lgkmcnt(8)\n\t" \
    "v_mfma_f32_16x16x32_bf16 v[162:165], v[128:131], v[48:51], v[162:165]\n\t" \
    "ds_read_b64_tr_b16 v[96:97], v226 offset:16384\n\t" \
    "v_mfma_f32_16x16x32_bf16 v[178:181], v[128:131], v[52:55], v[178:181]\n\t" \
    "ds_read_b64_tr_b16 v[98:99], v226 offset:18432\n\t" \
    "v_add3_u32 v230, v48, v49, v230\n\t" \
    "v_mfma_f32_16x16x32_bf16 v[166:169], v[132:135], v[48:51], v[166:169]\n\t" \
    "ds_read_b64_tr_b16 v[100:101], v227 offset:16384\n\t" \
    "v_mfma_f32_16x16x32_bf16 v[182:185], v[132:135], v[52:55], v[182:185]\n\t" \
    "ds_read_b64_tr_b16 v[102:103], v227 offset:18432\n\t" \
    "v_add3_u32 v230, v50, v51, v230\n\t" \
    "v_mfma_f32_16x16x32_bf16 v[170:173], v[136:139], v[48:51], v[170:173]\n\t" \
    "ds_read_b64_tr_b16 v[104:105], v228 offset:16384\n\t" \
    "v_add3_u32 v230, v52, v53, v230\n\t" \
    "v_mfma_f32_16x16x32_bf16 v[186:189], v[136:139], v[52:55], v[186:189]\n\t" \
    "ds_read_b64_tr_b16 v[106:107], v228 offset:18432\n\t" \
    "v_add3_u32 v230, v54, v55, v230\n\t" \
    "v_mfma_f32_16x16x32_bf16 v[174:177], v[140:143], v[48:51], v[174:177]\n\t" \
    "ds_read_b64_tr_b16 v[108:109], v229 offset:16384\n\t" \
    "v_mfma_f32_16x16x32_bf16 v[190:193], v[140:143], v[52:55], v[190:193]\n\t" \
    "ds_read_b64_tr_b16 v[110:111], v229 offset:18432\n\t" \
    "s_waitcnt vmcnt(9) lgkmcnt(8)\n\t" \
    "v_mfma_f32_16x16x32_bf16 v[162:165], v[144:147], v[56:59], v[162:165]\n\t" \
    "ds_read_b64_tr_b16 v[112:113], v226 offset:20480\n\t" \
    "v_mfma_f32_16x16x32_bf16 v[178:181], v[144:147], v[60:63], v[178:181]\n\t" \
    "ds_read_b64_tr_b16 v[114:115], v226 offset:22528\n\t" \
    "v_add3_u32 v230, v56, v57, v230\n\t" \
    "v_mfma_f32_16x16x32_bf16 v[166:169], v[148:151], v[56:59], v[166:169]\n\t" \
    "ds_read_b64_tr_b16 v[116:117], v227 offset:20480\n\t" \
    "v_mfma_f32_16x16x32_bf16 v[182:185], v[148:151], v[60:63], v[182:185]\n\t" \
    "ds_read_b64_tr_b16 v[118:119], v227 offset:22528\n\t" \
    "v_add3_u32 v230, v58, v59, v230\n\t" \
    "v_mfma_f32_16x16x32_bf16 v[170:173], v[152:155], v[56:59], v[170:173]\n\t" \
    "ds_read_b64_tr_b16 v[120:121], v228 offset:20480\n\t" \
    "v_add3_u32 v230, v60, v61, v230\n\t" \
    "v_mfma_f32_16x16x32_bf16 v[186:189], v[152:155], v[60:63], v[186:189]\n\t" \
    "ds_read_b64_tr_b16 v[122:123], v228 offset:22528\n\t" \
    "v_add3_u32 v230, v62, v63, v230\n\t" \
    "v_mfma_f32_16x16x32_bf16 v[174:177], v[156:159], v[56:59], v[174:177]\n\t" \
    "ds_read_b64_tr_b16 v[124:125], v229 offset:20480\n\t" \
    "v_mfma_f32_16x16x32_bf16 v[190:193], v[156:159], v[60:63], v[190:193]\n\t" \
    "ds_read_b64_tr_b16 v[126:127], v229 offset:22528\n\t" \
    "s_waitcnt vmcnt(7) lgkmcnt(8)\n\t" \
    "v_mfma_f32_16x16x32_bf16 v[162:165], v[96:99], v[64:67], v[162:165]\n\t" \
    "ds_read_b64_tr_b16 v[128:129], v226 offset:24576\n\t" \
    "v_mfma_f32_16x16x32_bf16 v[178:181], v[96:99], v[68:71], v[178:181]\n\t" \
    "ds_read_b64_tr_b16 v[130:131], v226 offset:26624\n\t" \
    "v_add3_u32 v230, v64, v65, v230\n\t" \
    "v_mfma_f32_16x16x32_bf16 v[166:169], v[100:103], v[64:67], v[166:169]\n\t" \
    "ds_read_b64_tr_b16 v[132:133], v227 offset:24576\n\t" \
    "v_mfma_f32_16x16x32_bf16 v[182:185], v[100:103], v[68:71], v[182:185]\n\t" \
    "ds_read_b64_tr_b16 v[134:135], v227 offset:26624\n\t" \
    "v_add3_u32 v230, v66, v67, v230\n\t" \
    "v_mfma_f32_16x16x32_bf16 v[170:173], v[104:107], v[64:67], v[170:173]\n\t" \
    "ds_read_b64_tr_b16 v[136:137], v228 offset:24576\n\t" \
    "v_add3_u32 v230, v68, v69, v230\n\t" \
    "v_mfma_f32_16x16x32_bf16 v[186:189], v[104:107], v[68:71], v[186:189]\n\t" \
    "ds_read_b64_tr_b16 v[138:139], v228 offset:26624\n\t" \
    "v_add3_u32 v230, v70, v71, v230\n\t" \
    "v_mfma_f32_16x16x32_bf16 v[174:177], v[108:111], v[64:67], v[174:177]\n\t" \
    "ds_read_b64_tr_b16 v[140:141], v229 offset:24576\n\t" \
    "v_mfma_f32_16x16x32_bf16 v[190:193], v[108:111], v[68:71], v[190:193]\n\t" \
    "ds_read_b64_tr_b16 v[142:143], v229 offset:26624\n\t" \
    "s_waitcnt vmcnt(5) lgkmcnt(8)\n\t" \
    "v_mfma_f32_16x16x32_bf16 v[162:165], v[112:115], v[72:75], v[162:165]\n\t" \
    "ds_read_b64_tr_b16 v[144:145], v226 offset:28672\n\t" \
    "v_mfma_f32_16x16x32_bf16 v[178:181], v[112:115], v[76:79], v[178:181]\n\t" \
    "ds_read_b64_tr_b16 v[146:147], v226 offset:30720\n\t" \
    "v_add3_u32 v230, v72, v73, v230\n\t" \
    "v_mfma_f32_16x16x32_bf16 v[166:169], v[116:119], v[72:75], v[166:169]\n\t" \
    "ds_read_b64_tr_b16 v[148:149], v227 offset:28672\n\t" \
    "v_mfma_f32_16x16x32_bf16 v[182:185], v[116:119], v[76:79], v[182:185]\n\t" \
    "ds_read_b64_tr_b16 v[150:151], v227 offset:30720\n\t" \
    "v_add3_u32 v230, v74, v75, v230\n\t" \
    "v_mfma_f32_16x16x32_bf16 v[170:173], v[120:123], v[72:75], v[170:173]\n\t" \
    "ds_read_b64_tr_b16 v[152:153], v228 offset:28672\n\t" \
    "v_add3_u32 v230, v76, v77, v230\n\t" \
    "v_mfma_f32_16x16x32_bf16 v[186:189], v[120:123], v[76:79], v[186:189]\n\t" \
    "ds_read_b64_tr_b16 v[154:155], v228 offset:30720\n\t" \
    "v_add3_u32 v230, v78, v79, v230\n\t" \
    "v_mfma_f32_16x16x32_bf16 v[174:177], v[124:127], v[72:75], v[174:177]\n\t" \
    "ds_read_b64_tr_b16 v[156:157], v229 offset:28672\n\t" \
    "v_mfma_f32_16x16x32_bf16 v[190:193], v[124:127], v[76:79], v[190:193]\n\t" \
    "ds_read_b64_tr_b16 v[158:159], v229 offset:30720\n\t" \
    "s_waitcnt vmcnt(3) lgkmcnt(8)\n\t" \
    "v_mfma_f32_16x16x32_bf16 v[162:165], v[128:131], v[80:83], v[162:165]\n\t" \
    "ds_read_b32 v231, v249 offset:68\n\t" \
    "v_mfma_f32_16x16x32_bf16 v[178:181], v[128:131], v[84:87], v[178:181]\n\t" \
    "v_add3_u32 v230, v80, v81, v230\n\t" \
    "v_mfma_f32_16x16x32_bf16 v[166:169], v[132:135], v[80:83], v[166:169]\n\t" \
    "ds_read_b128 v[96:99], v252\n\t" \
    "v_mfma_f32_16x16x32_bf16 v[182:185], v[132:135], v[84:87], v[182:185]\n\t" \
    "ds_read_b128 v[100:103], v252 offset:64\n\t" \
    "v_add3_u32 v230, v82, v83, v230\n\t" \
    "v_mfma_f32_16x16x32_bf16 v[170:173], v[136:139], v[80:83], v[170:173]\n\t" \
    "ds_read_b128 v[104:107], v252 offset:128\n\t" \
    "v_add3_u32 v230, v84, v85, v230\n\t" \
    "v_mfma_f32_16x16x32_bf16 v[186:189], v[136:139], v[84:87], v[186:189]\n\t" \
    "ds_read_b128 v[108:111], v252 offset:192\n\t" \
    "v_add3_u32 v230, v86, v87, v230\n\t" \
    "v_mfma_f32_16x16x32_bf16 v[174:177], v[140:143], v[80:83], v[174:177]\n\t" \
    "v_mfma_f32_16x16x32_bf16 v[190:193], v[140:143], v[84:87], v[190:193]\n\t" \
    "s_waitcnt vmcnt(1) lgkmcnt(5)\n\t" \
    "v_mfma_f32_16x16x32_bf16 v[162:165], v[144:147], v[88:91], v[162:165]\n\t" \
    "v_mfma_f32_16x16x32_bf16 v[166:169], v[148:151], v[88:91], v[166:169]\n\t" \
    "v_add3_u32 v230, v88, v89, v230\n\t" \
    "v_mfma_f32_16x16x32_bf16 v[170:173], v[152:155], v[88:91], v[170:173]\n\t" \
    "v_mfma_f32_16x16x32_bf16 v[174:177], v[156:159], v[88:91], v[174:177]\n\t" \
    "v_add3_u32 v230, v90, v91, v230\n\t" \
    "v_mfma_f32_16x16x32_bf16 v[178:181], v[144:147], v[92:95], v[178:181]\n\t" \
    "v_add3_u32 v230, v92, v93, v230\n\t" \
    "v_mfma_f32_16x16x32_bf16 v[182:185], v[148:151], v[92:95], v[182:185]\n\t" \
    "v_add3_u32 v230, v94, v95, v230\n\t" \
    "v_mfma_f32_16x16x32_bf16 v[186:189], v[152:155], v[92:95], v[186:189]\n\t" \
    "s_cmp_lg_u64 %[rm], 0\n\t" \
    "v_mfma_f32_16x16x32_bf16 v[190:193], v[156:159], v[92:95], v[190:193]\n\t" \
    "s_cbranch_scc1 .Lc_y\n\t" \
    ".Lc_yr:\n\t" \
    "s_add_u32 s85, %[sout], 0x8000\n\t" \
    "v_pk_add_f32 v[0:1], v[0:1], v[162:163]\n\t" \
    "v_pk_add_f32 v[2:3], v[2:3], v[164:165]\n\t" \
    "v_pk_add_f32 v[4:5], v[4:5], v[166:167]\n\t" \
    "v_pk_add_f32 v[6:7], v[6:7], v[168:169]\n\t" \
    "v_pk_fma_f32 v[0:1], v[232:233], v[0:1], v[194:195] op_sel_hi:[0,1,1]\n\t" \
    "v_pk_fma_f32 v[2:3], v[232:233], v[2:3], v[196:197] op_sel_hi:[0,1,1]\n\t" \
    "v_pk_fma_f32 v[4:5], v[232:233], v[4:5], v[198:199] op_sel_hi:[0,1,1]\n\t" \
    "v_pk_fma_f32 v[6:7], v[232:233], v[6:7], v[200:201] op_sel_hi:[0,1,1]\n\t" \
    "v_cvt_pk_bf16_f32 v32, v0, v1\n\t" \
    "v_cvt_pk_bf16_f32 v34, v4, v5\n\t" \
    "v_cvt_pk_bf16_f32 v33, v2, v3\n\t" \
    "v_cvt_pk_bf16_f32 v35, v6, v7\n\t" \
    "s_nop 0\n\t" \
    "v_permlane16_swap_b32 v32, v34\n\t" \
    "v_permlane16_swap_b32 v33, v35\n\t" \
    "buffer_store_dwordx4 v[32:35], v247, %[rso], %[sout] offen sc1\n\t" \
    "v_pk_add_f32 v[8:9], v[8:9], v[170:171]\n\t" \
    "v_pk_add_f32 v[10:11], v[10:11], v[172:173]\n\t" \
    "v_pk_add_f32 v[12:13], v[12:13], v[174:175]\n\t" \
    "v_pk_add_f32 v[14:15], v[14:15], v[176:177]\n\t" \
    "v_pk_fma_f32 v[8:9], v[232:233], v[8:9], v[202:203] op_sel_hi:[0,1,1]\n\t" \
    "v_pk_fma_f32 v[10:11], v[232:233], v[10:11], v[204:205] op_sel_hi:[0,1,1]\n\t" \
    "v_pk_fma_f32 v[12:13], v[232:233], v[12:13], v[206:207] op_sel_hi:[0,1,1]\n\t" \
    "v_pk_fma_f32 v[14:15], v[232:233], v[14:15], v[208:209] op_sel_hi:[0,1,1]\n\t" \
    "v_cvt_pk_bf16_f32 v36, v8, v9\n\t" \
    "v_cvt_pk_bf16_f32 v38, v12, v13\n\t" \
    "v_cvt_pk_bf16_f32 v37, v10, v11\n\t" \
    "v_cvt_pk_bf16_f32 v39, v14, v15\n\t" \
    "v_cmp_gt_u32 vcc, 32, v250\n\t" \
    "v_permlane16_swap_b32 v36, v38\n\t" \
    "v_permlane16_swap_b32 v37, v39\n\t" \
    "buffer_store_dwordx4 v[36:39], v247, %[rso], %[sout] offen offset:64 sc1\n\t" \
    "v_pk_add_f32 v[16:17], v[16:17], v[178:179]\n\t" \
    "v_pk_add_f32 v[18:19], v[18:19], v[180:181]\n\t" \
    "v_pk_add_f32 v[20:21], v[20:21], v[182:183]\n\t" \
    "v_pk_add_f32 v[22:23], v[22:23], v[184:185]\n\t" \
    "v_pk_fma_f32 v[16:17], v[234:235], v[16:17], v[210:211] op_sel_hi:[0,1,1]\n\t" \
    "v_pk_fma_f32 v[18:19], v[234:235], v[18:19], v[212:213] op_sel_hi:[0,1,1]\n\t" \
    "v_pk_fma_f32 v[20:21], v[234:235], v[20:21], v[214:215] op_sel_hi:[0,1,1]\n\t" \
    "v_pk_fma_f32 v[22:23], v[234:235], v[22:23], v[216:217] op_sel_hi:[0,1,1]\n\t" \
    "v_cvt_pk_bf16_f32 v40, v16, v17\n\t" \
    "v_cvt_pk_bf16_f32 v42, v20, v21\n\t" \
    "v_cvt_pk_bf16_f32 v41, v18, v19\n\t" \
    "v_cvt_pk_bf16_f32 v43, v22, v23\n\t" \
    "s_waitcnt vmcnt(2)\n\t" \
    "v_permlane16_swap_b32 v40, v42\n\t" \
    "v_permlane16_swap_b32 v41, v43\n\t" \
    "buffer_store_dwordx4 v[40:43], v247, %[rso], s85 offen sc1\n\t" \
    "v_pk_add_f32 v[24:25], v[24:25], v[186:187]\n\t" \
    "v_pk_add_f32 v[26:27], v[26:27], v[188:189]\n\t" \
    "v_pk_add_f32 v[28:29], v[28:29], v[190:191]\n\t" \
    "v_pk_add_f32 v[30:31], v[30:31], v[192:193]\n\t" \
    "v_pk_fma_f32 v[24:25], v[234:235], v[24:25], v[218:219] op_sel_hi:[0,1,1]\n\t" \
    "v_pk_fma_f32 v[26:27], v[234:235], v[26:27], v[220:221] op_sel_hi:[0,1,1]\n\t" \
    "v_pk_fma_f32 v[28:29], v[234:235], v[28:29], v[222:223] op_sel_hi:[0,1,1]\n\t" \
    "v_pk_fma_f32 v[30:31], v[234:235], v[30:31], v[224:225] op_sel_hi:[0,1,1]\n\t" \
    "v_cvt_pk_bf16_f32 v44, v24, v25\n\t" \
    "v_cvt_pk_bf16_f32 v46, v28, v29\n\t" \
    "v_cvt_pk_bf16_f32 v45, v26, v27\n\t" \
    "v_cvt_pk_bf16_f32 v47, v30, v31\n\t" \
    "s_waitcnt lgkmcnt(4)\n\t" \
    "v_cndmask_b32 v231, 0, v160, vcc\n\t" \
    "v_permlane16_swap_b32 v44, v46\n\t" \
    "v_permlane16_swap_b32 v45, v47\n\t" \
    "buffer_store_dwordx4 v[44:47], v247, %[rso], s85 offen offset:64 sc1\n\t" \
    "v_sub_u32 v231, v230, v231\n\t" \
    "v_cmp_ne_u32 vcc, v236, v161\n\t" \
    "v_mov_b32 v0, v231\n\t" \
    "s_nop 1\n\t" \
    "v_permlane32_swap_b32 v231, v0\n\t" \
    "v_add_u32 v231, v231, v0\n\t" \
    "v_mov_b32 v0, v231\n\t" \
    "s_nop 1\n\t" \
    "v_permlane16_swap_b32 v231, v0\n\t" \
    "v_add_u32 v231, v231, v0\n\t" \
    "s_nop 1\n\t" \
    "v_add_u32_dpp v231, v231, v231 row_ror:8 row_mask:0xf bank_mask:0xf bound_ctrl:1\n\t" \
    "s_nop 1\n\t" \
    "v_add_u32_dpp v231, v231, v231 row_ror:4 row_mask:0xf bank_mask:0xf bound_ctrl:1\n\t" \
    "s_nop 1\n\t" \
    "v_add_u32_dpp v231, v231, v231 row_ror:2 row_mask:0xf bank_mask:0xf bound_ctrl:1\n\t" \
    "s_nop 1\n\t" \
    "v_add_u32_dpp v231, v231, v231 row_ror:1 row_mask:0xf bank_mask:0xf bound_ctrl:1\n\t" \
    "s_nop 0\n\t" \
    "v_readfirstlane_b32 s83, v231\n\t" \
    "s_or_b32 s83, s83, vcc_lo\n\t" \
    "s_cselect_b32 %[st], 2, 0\n\t" \
    "s_waitcnt lgkmcnt(0)\n\t" \
    ".Lc_end:\n\t"
#define RED1_ASM \
    "s_mov_b32 s80, 256\n\t" \
    "s_add_u32 s81, %[s0], 0x1000\n\t" \
    "v_mov_b32 v41, 1\n\t" \
    ".Lr_t:\n\t" \
    "s_memrealtime s[82:83]\n\t" \
    "s_sub_u32 s80, s80, 1\n\t" \
    "s_cbranch_scc1 .Lr_go\n\t" \
    "s_waitcnt lgkmcnt(0)\n\t" \
    "s_sub_u32 s82, %[t1], s82\n\t" \
    "s_cmp_lt_i32 s82, 5\n\t" \
    "s_cbranch_scc1 .Lr_go\n\t" \
    "s_cmp_lt_i32 s82, 13\n\t" \
    "s_cbranch_scc1 .Lr_t\n\t" \
    "s_sleep 2\n\t" \
    "s_branch .Lr_t\n\t" \
    ".Lr_go:\n\t" \
    "buffer_load_dwordx4 v[0:3], %[v16], %[rsb], %[sm] offen sc1\n\t" \
    "buffer_load_dwordx4 v[4:7], %[v16], %[rsb], %[s0] offen sc1\n\t" \
    "buffer_load_dwordx4 v[8:11], %[v16], %[rsb], %[s0] offen offset:1024 sc1\n\t" \
    "buffer_load_dwordx4 v[12:15], %[v16], %[rsb], %[s0] offen offset:2048 sc1\n\t" \
    "buffer_load_dwordx4 v[16:19], %[v16], %[rsb], %[s0] offen offset:3072 sc1\n\t" \
    "buffer_load_dwordx4 v[20:23], %[v16], %[rsb], s81 offen sc1\n\t" \
    "buffer_load_dwordx4 v[24:27], %[v16], %[rsb], s81 offen offset:1024 sc1\n\t" \
    "buffer_load_dwordx4 v[28:31], %[v16], %[rsb], s81 offen offset:2048 sc1\n\t" \
    "buffer_load_dwordx4 v[32:35], %[v16], %[rsb], s81 offen offset:3072 sc1\n\t" \
    "ds_write_b32 %[ra], v41\n\t" \
    "s_waitcnt vmcnt(8)\n\t" \
    "v_add3_u32 v40, v0, v1, v2\n\t" \
    "v_xor_b32 v41, %[tag], v1\n\t" \
    "v_and_b32 v42, %[mk2], v0\n\t" \
    "v_add_u32 v40, v40, v3\n\t" \
    "v_add_f32_dpp v0, v0, v0 row_ror:8 row_mask:0xf bank_mask:0xf bound_ctrl:1\n\t" \
    "v_add_f32_dpp v1, v1, v1 row_ror:8 row_mask:0xf bank_mask:0xf bound_ctrl:1\n\t" \
    "v_add_f32_dpp v2, v2, v2 row_ror:8 row_mask:0xf bank_mask:0xf bound_ctrl:1\n\t" \
    "v_add_f32_dpp v3, v3, v3 row_ror:8 row_mask:0xf bank_mask:0xf bound_ctrl:1\n\t" \
    "v_and_b32 v40, %[mk01], v40\n\t" \
    "v_sub_u32 v40, v40, v42\n\t" \
    "s_waitcnt vmcnt(7)\n\t" \
    "v_mfma_f32_16x16x32_bf16 v[36:39], %[sel], v[4:7], 0\n\t" \
    "v_add3_u32 v40, v4, v5, v40\n\t" \
    "v_add3_u32 v40, v6, v7, v40\n\t" \
    "v_add_f32_dpp v0, v0, v0 row_ror:4 row_mask:0xf bank_mask:0xf bound_ctrl:1\n\t" \
    "v_add_f32_dpp v1, v1, v1 row_ror:4 row_mask:0xf bank_mask:0xf bound_ctrl:1\n\t" \
    "s_waitcnt vmcnt(6)\n\t" \
    "v_mfma_f32_16x16x32_bf16 v[36:39], %[sel], v[8:11], v[36:39]\n\t" \
    "v_add3_u32 v40, v8, v9, v40\n\t" \
    "v_add3_u32 v40, v10, v11, v40\n\t" \
    "v_add_f32_dpp v2, v2, v2 row_ror:4 row_mask:0xf bank_mask:0xf bound_ctrl:1\n\t" \
    "v_add_f32_dpp v3, v3, v3 row_ror:4 row_mask:0xf bank_mask:0xf bound_ctrl:1\n\t" \
    "s_waitcnt vmcnt(5)\n\t" \
    "v_mfma_f32_16x16x32_bf16 v[36:39], %[sel], v[12:15], v[36:39]\n\t" \
    "v_add3_u32 v40, v12, v13, v40\n\t" \
    "v_add3_u32 v40, v14, v15, v40\n\t" \
    "v_and_b32 v41, %[mk2], v41\n\t" \
    "v_cmp_ne_u32 vcc, 0, v41\n\t" \
    "s_waitcnt vmcnt(4)\n\t" \
    "v_mfma_f32_16x16x32_bf16 v[36:39], %[sel], v[16:19], v[36:39]\n\t" \
    "v_add3_u32 v40, v16, v17, v40\n\t" \
    "v_add3_u32 v40, v18, v19, v40\n\t" \
    "v_permlane32_swap_b32 v0, v2\n\t" \
    "v_permlane32_swap_b32 v1, v3\n\t" \
    "s_waitcnt vmcnt(3)\n\t" \
    "v_mfma_f32_16x16x32_bf16 v[36:39], %[sel], v[20:23], v[36:39]\n\t" \
    "v_add3_u32 v40, v20, v21, v40\n\t" \
    "v_add3_u32 v40, v22, v23, v40\n\t" \
    "v_add_f32 v0, v0, v2\n\t" \
    "v_add_f32 v1, v1, v3\n\t" \
    "s_waitcnt vmcnt(2)\n\t" \
    "v_mfma_f32_16x16x32_bf16 v[36:39], %[sel], v[24:27], v[36:39]\n\t" \
    "v_add3_u32 v40, v24, v25, v40\n\t" \
    "v_add3_u32 v40, v26, v27, v40\n\t" \
    "v_mov_b32 v2, v0\n\t" \
    "v_mov_b32 v3, v1\n\t" \
    "s_waitcnt vmcnt(1)\n\t" \
    "v_mfma_f32_16x16x32_bf16 v[36:39], %[sel], v[28:31], v[36:39]\n\t" \
    "v_add3_u32 v40, v28, v29, v40\n\t" \
    "v_add3_u32 v40, v30, v31, v40\n\t" \
    "s_or_b32 s80, vcc_lo, vcc_hi\n\t" \
    "s_waitcnt vmcnt(0)\n\t" \
    "v_mfma_f32_16x16x32_bf16 v[36:39], %[sel], v[32:35], v[36:39]\n\t" \
    "v_add3_u32 v40, v32, v33, v40\n\t" \
    "v_add3_u32 v40, v34, v35, v40\n\t" \
    "v_permlane16_swap_b32 v0, v2\n\t" \
    "v_permlane16_swap_b32 v1, v3\n\t" \
    "v_add_f32 v0, v0, v2\n\t" \
    "v_add_f32 v1, v1, v3\n\t" \
    "v_cvt_pk_bf16_f32 v44, v0, v1\n\t" \
    "v_mov_b32 v45, v44\n\t" \
    "v_cvt_pk_bf16_f32 v48, v36, v37\n\t" \
    "v_cvt_pk_bf16_f32 v49, v38, v39\n\t" \
    "v_and_b32 v46, %[mkl], v44\n\t" \
    "v_add3_u32 v46, v46, v48, v49\n\t" \
    "v_permlane32_swap_b32 v44, v45\n\t" \
    "buffer_store_dwordx2 v[48:49], %[vkf], %[rsb], %[sf] offen sc1\n\t" \
    "v_permlane32_swap_b32 v40, v46\n\t" \
    "v_add_u32 v40, v40, v46\n\t" \
    "v_mov_b32 v46, v40\n\t" \
    "v_cndmask_b32_e64 v45, v45, %[tag], %[hi]\n\t" \
    "s_nop 0\n\t" \
    "v_permlane16_swap_b32 v40, v46\n\t" \
    "v_add_u32 v40, v40, v46\n\t" \
    "s_nop 1\n\t" \
    "v_add_u32_dpp v40, v40, v40 row_ror:8 row_mask:0xf bank_mask:0xf bound_ctrl:1\n\t" \
    "s_nop 1\n\t" \
    "v_add_u32_dpp v40, v40, v40 row_ror:4 row_mask:0xf bank_mask:0xf bound_ctrl:1\n\t" \
    "s_nop 1\n\t" \
    "v_add_u32_dpp v40, v40, v40 row_ror:2 row_mask:0xf bank_mask:0xf bound_ctrl:1\n\t" \
    "s_nop 1\n\t" \
    "v_add_u32_dpp v40, v40, v40 row_ror:1 row_mask:0xf bank_mask:0xf bound_ctrl:1\n\t" \
    "s_nop 0\n\t" \
    "v_readfirstlane_b32 s81, v40\n\t" \
    "v_cndmask_b32_e64 v44, v44, v40, %[hi]\n\t" \
    "s_or_b32 %[st], s80, s81\n\t" \
    "s_cselect_b32 s82, 0x70000000, %[sg]\n\t" \
    "buffer_store_dwordx2 v[44:45], %[vfm], %[rsb], s82 offen sc1\n\t"
#define RED2_ASM \
    "s_mov_b32 s80, 256\n\t" \
    "s_add_u32 s81, %[s0], 0x1000\n\t" \
    "s_add_u32 s84, %[s0], 0x4000\n\t" \
    "s_mov_b32 m0, 160256\n\t" \
    ".Lr_u:\n\t" \
    "s_memrealtime s[82:83]\n\t" \
    "s_sub_u32 s80, s80, 1\n\t" \
    "s_cbranch_scc1 .Lr_g2\n\t" \
    "s_waitcnt lgkmcnt(0)\n\t" \
    "s_sub_u32 s82, %[t2], s82\n\t" \
    "s_cmp_lt_i32 s82, 5\n\t" \
    "s_cbranch_scc1 .Lr_g2\n\t" \
    "s_cmp_lt_i32 s82, 13\n\t" \
    "s_cbranch_scc1 .Lr_u\n\t" \
    "s_sleep 2\n\t" \
    "s_branch .Lr_u\n\t" \
    ".Lr_g2:\n\t" \
    "buffer_load_dwordx4 %[v16], %[rsb], s84 offen sc1 lds\n\t" \
    "s_mov_b32 m0, 151552\n\t" \
    "s_nop 0\n\t" \
    "buffer_load_dwordx4 %[v16], %[rsb], %[s0] offen sc1 lds\n\t" \
    "buffer_load_dwordx4 %[v16], %[rsb], %[s0] offen offset:1024 sc1 lds\n\t" \
    "buffer_load_dwordx4 %[v16], %[rsb], %[s0] offen offset:2048 sc1 lds\n\t" \
    "buffer_load_dwordx4 %[v16], %[rsb], %[s0] offen offset:3072 sc1 lds\n\t" \
    "s_mov_b32 m0, 155648\n\t" \
    "s_nop 0\n\t" \
    "buffer_load_dwordx4 %[v16], %[rsb], s81 offen sc1 lds\n\t" \
    "buffer_load_dwordx4 %[v16], %[rsb], s81 offen offset:1024 sc1 lds\n\t" \
    "buffer_load_dwordx4 %[v16], %[rsb], s81 offen offset:2048 sc1 lds\n\t" \
    "buffer_load_dwordx4 %[v16], %[rsb], s81 offen offset:3072 sc1 lds\n\t" \
    "ds_write_b32 %[ra], %[pv]\n\t" \
    "v_or_b32 %[pv], 1, %[pv]\n\t" \
    "v_add_u32 %[pv], 1, %[pv]\n\t" \
    "s_waitcnt vmcnt(0)\n\t" \
    "ds_write_b32 %[ra], %[pv]\n\t" \
    "ds_read_b128 v[0:3], %[kwa] offset:8704\n\t" \
    "ds_read_b128 v[4:7], %[kwa]\n\t" \
    "ds_read_b128 v[8:11], %[kwa] offset:1024\n\t" \
    "ds_read_b128 v[12:15], %[kwa] offset:2048\n\t" \
    "ds_read_b128 v[16:19], %[kwa] offset:3072\n\t" \
    "ds_read_b128 v[20:23], %[kwa] offset:4096\n\t" \
    "ds_read_b128 v[24:27], %[kwa] offset:5120\n\t" \
    "ds_read_b128 v[28:31], %[kwa] offset:6144\n\t" \
    "ds_read_b128 v[32:35], %[kwa] offset:7168\n\t" \
    "s_waitcnt lgkmcnt(8)\n\t" \
    "v_add3_u32 v40, v0, v1, v2\n\t" \
    "v_xor_b32 v41, %[tag], v1\n\t" \
    "v_and_b32 v42, %[mk1], v0\n\t" \
    "v_add_u32 v40, v40, v3\n\t" \
    "v_and_b32 v41, %[mk1], v41\n\t" \
    "v_and_b32 v40, %[mk0], v40\n\t" \
    "v_cmp_ne_u32 vcc, 0, v41\n\t" \
    "v_sub_u32 v40, v40, v42\n\t" \
    "v_mov_b32 v42, %[frc]\n\t" \
    "s_waitcnt lgkmcnt(4)\n\t" \
    "v_add3_u32 v40, v4, v5, v40\n\t" \
    "v_add3_u32 v42, v6, v7, v42\n\t" \
    "v_add3_u32 v40, v8, v9, v40\n\t" \
    "v_add3_u32 v42, v10, v11, v42\n\t" \
    "v_add3_u32 v40, v12, v13, v40\n\t" \
    "v_add3_u32 v42, v14, v15, v42\n\t" \
    "v_add3_u32 v40, v16, v17, v40\n\t" \
    "v_add3_u32 v42, v18, v19, v42\n\t" \
    "s_waitcnt lgkmcnt(0)\n\t" \
    "v_add3_u32 v40, v20, v21, v40\n\t" \
    "v_add3_u32 v42, v22, v23, v42\n\t" \
    "v_add3_u32 v40, v24, v25, v40\n\t" \
    "v_add3_u32 v42, v26, v27, v42\n\t" \
    "v_add3_u32 v40, v28, v29, v40\n\t" \
    "v_add3_u32 v42, v30, v31, v42\n\t" \
    "v_add3_u32 v40, v32, v33, v40\n\t" \
    "v_add3_u32 v42, v34, v35, v42\n\t" \
    "s_or_b32 s80, vcc_lo, vcc_hi\n\t" \
    "s_nop 0\n\t" \
    "v_permlane32_swap_b32 v40, v42\n\t" \
    "v_add_u32 v40, v40, v42\n\t" \
    "s_nop 1\n\t" \
    "v_add_u32_dpp v40, v40, v40 row_ror:8 row_mask:0xf bank_mask:0xf bound_ctrl:1\n\t" \
    "s_nop 1\n\t" \
    "v_add_u32_dpp v40, v40, v40 row_ror:4 row_mask:0xf bank_mask:0xf bound_ctrl:1\n\t" \
    "s_nop 1\n\t" \
    "v_add_u32_dpp v40, v40, v40 row_ror:2 row_mask:0xf bank_mask:0xf bound_ctrl:1\n\t" \
    "s_nop 1\n\t" \
    "v_add_u32_dpp v40, v40, v40 row_ror:1 row_mask:0xf bank_mask:0xf bound_ctrl:1\n\t" \
    "s_nop 0\n\t" \
    "v_readlane_b32 s81, v40, 0\n\t" \
    "v_readlane_b32 s82, v40, 16\n\t" \
    "v_readlane_b32 s83, v40, 32\n\t" \
    "v_readlane_b32 s84, v40, 48\n\t" \
    "s_add_u32 s81, s81, s82\n\t" \
    "s_add_u32 s83, s83, s84\n\t" \
    "s_add_u32 s81, s81, s83\n\t" \
    "s_or_b32 %[st], s80, s81\n\t" \
    "s_cselect_b32 s81, 0, 1\n\t" \
    "v_add_u32 v41, s81, %[pv]\n\t" \
    "ds_write_b32 %[ra], v41\n\t"
#define PHASE_AB_CLOB "v0", "v1", "v2", "v3", "v4", "v5", "v6", "v7", "v8", "v9", "v10", "v11", "v12", "v13", "v14", "v15", "v16", "v17", "v18", "v19", "v20", "v21", "v22", "v23", "v24", "v25", "v26", "v27", "v28", "v29", "v30", "v31", "v32", "v33", "v34", "v35", "v36", "v37", "v38", "v39", "v40", "v41", "v42", "v43", "v44", "v45", "v46", "v47", "v48", "v49", "v50", "v51", "v52", "v53", "v54", "v55", "v56", "v57", "v58", "v59", "v60", "v61", "v62", "v63", "v64", "v65", "v66", "v67", "v68", "v69", "v70", "v71", "v72", "v73", "v74", "v75", "v76", "v77", "v78", "v79", "v80", "v81", "v82", "v83", "v84", "v85", "v86", "v87", "v88", "v89", "v90", "v91", "v92", "v93", "v94", "v95", "v96", "v97", "v98", "v99", "v100", "v101", "v102", "v103", "v104", "v105", "v106", "v107", "v108", "v109", "v110", "v111", "v112", "v113", "v114", "v115", "v116", "v117", "v118", "v119", "v120", "v121", "v122", "v123", "v124", "v125", "v126", "v127", "v128", "v129", "v130", "v131", "v132", "v133", "v134", "v135", "v136", "v137", "v138", "v139", "v140", "v141", "v142", "v143", "v144", "v145", "v146", "v147", "v148", "v149", "v150", "v151", "v152", "v153", "v154", "v155", "v156", "v157", "v158", "v159", "v160", "v161", "v162", "v163", "v164", "v165", "v166", "v167", "v168", "v169", "v170", "v171", "v172", "v173", "v174", "v175", "v176", "v177", "v178", "v179", "v180", "v181", "v182", "v183", "v184", "v185", "v186", "v187", "v188", "v189", "v190", "v191", "v192", "v193", "v194", "v195", "v196", "v197", "v198", "v199", "v200", "v201", "v202", "v203", "v204", "v205", "v206", "v207", "v208", "v209", "v210", "v211", "v212", "v213", "v214", "v215", "v216", "v217", "v218", "v219", "v220", "v221", "v222", "v223", "v224", "v225", "v226", "v227", "v228", "v229", "v230", "v231", "v237", "v238", "s82", "s83", "s84", "s85", "s86", "s87", "s88", "s89", "s90", "s91", "s92", "s93", "s94", "s95", "s96", "s97"
#define PHASE_C_CLOB "v0", "v1", "v2", "v3", "v4", "v5", "v6", "v7", "v8", "v9", "v10", "v11", "v12", "v13", "v14", "v15", "v16", "v17", "v18", "v19", "v20", "v21", "v22", "v23", "v24", "v25", "v26", "v27", "v28", "v29", "v30", "v31", "v32", "v33", "v34", "v35", "v36", "v37", "v38", "v39", "v40", "v41", "v42", "v43", "v44", "v45", "v46", "v47", "v48", "v49", "v50", "v51", "v52", "v53", "v54", "v55", "v56", "v57", "v58", "v59", "v60", "v61", "v62", "v63", "v64", "v65", "v66", "v67", "v68", "v69", "v70", "v71", "v72", "v73", "v74", "v75", "v76", "v77", "v78", "v79", "v80", "v81", "v82", "v83", "v84", "v85", "v86", "v87", "v88", "v89", "v90", "v91", "v92", "v93", "v94", "v95", "v96", "v97", "v98", "v99", "v100", "v101", "v102", "v103", "v104", "v105", "v106", "v107", "v108", "v109", "v110", "v111", "v112", "v113", "v114", "v115", "v116", "v117", "v118", "v119", "v120", "v121", "v122", "v123", "v124", "v125", "v126", "v127", "v128", "v129", "v130", "v131", "v132", "v133", "v134", "v135", "v136", "v137", "v138", "v139", "v140", "v141", "v142", "v143", "v144", "v145", "v146", "v147", "v148", "v149", "v150", "v151", "v152", "v153", "v154", "v155", "v156", "v157", "v158", "v159", "v160", "v161", "v162", "v163", "v164", "v165", "v166", "v167", "v168", "v169", "v170", "v171", "v172", "v173", "v174", "v175", "v176", "v177", "v178", "v179", "v180", "v181", "v182", "v183", "v184", "v185", "v186", "v187", "v188", "v189", "v190", "v191", "v192", "v193", "v194", "v195", "v196", "v197", "v198", "v199", "v200", "v201", "v202", "v203", "v204", "v205", "v206", "v207", "v208", "v209", "v210", "v211", "v212", "v213", "v214", "v215", "v216", "v217", "v218", "v219", "v220", "v221", "v222", "v223", "v224", "v225", "v226", "v227", "v228", "v229", "v231", "s80", "s81", "s82", "s83", "s84", "s85", "s86", "s87"
#define RED1_CLOB "v0", "v1", "v2", "v3", "v4", "v5", "v6", "v7", "v8", "v9", "v10", "v11", "v12", "v13", "v14", "v15", "v16", "v17", "v18", "v19", "v20", "v21", "v22", "v23", "v24", "v25", "v26", "v27", "v28", "v29", "v30", "v31", "v32", "v33", "v34", "v35", "v36", "v37", "v38", "v39", "v40", "v41", "v42", "v43", "v44", "v45", "v46", "v47", "v48", "v49", "s80", "s81", "s82", "s83", "s84"

#define KNAME "fused_fwd"
#define CODE_FETCH(dst, kb) do { unsigned long long pc_; unsigned len_, off_; \
    asm volatile("s_getpc_b64 %0\n1:\n\ts_mov_b32 %1, 1b-" KNAME "\n\ts_mov_b32 %2, .Lff_end0-" KNAME "\n\ts_max_u32 %2, %2, .Lff_end1-" KNAME \
                 "\n\ts_max_u32 %2, %2, .Lff_end2-" KNAME : "=s"(pc_), "=s"(off_), "=s"(len_) :: "scc"); \
    const rsrc_t rsC_ = mk_rsrc((const void*)(pc_ - off_), len_); \
    asm volatile("s_nop 4\n\tbuffer_load_dwordx4 %0, %1, %2, %3 offen" : "=v"(dst) : "v"(fresh_lane() * 16), "s"(rsC_), "s"((unsigned)(kb) * 1024u) : "memory"); } while (0)

extern "C" __global__ __launch_bounds__(512, 2) void fused_fwd(const u16* __restrict__ x, const u16* __restrict__ wqkv,
                                                    const u16* __restrict__ wphi, const u16* __restrict__ wo,
                                                    const u16* __restrict__ wr, const u16* __restrict__ wgp,
                                                    const u16* __restrict__ wup, const u16* __restrict__ wdp,
                                                    const u16* __restrict__ gw, char* ws, u16* out) {
    __shared__ __attribute__((aligned(16))) char smem[163840];
    const int w = __builtin_amdgcn_readfirstlane(threadIdx.x >> 6);
    if (K::TOPV) {
        asm volatile("s_mov_b64 exec, 0\n\ts_cmp_eq_u32 %0, 6\n\ts_cbranch_scc1 .Lpad_x\n\t"
                     "s_cmp_eq_u32 %0, 0\n\ts_cbranch_scc1 .Lpad_d\n\ts_cmp_eq_u32 %0, 1\n\ts_cbranch_scc1 .Lpad_ab0\n"
                     ".Lpad_top:\n\ts_mov_b64 exec, -1" :: "s"(w) : "scc", "memory");
    } else {
        asm volatile(".Lpad_top:");
    }
    asm volatile("" :: "s"(x), "s"(wqkv));
    const int bid = blockIdx.x, xcd = bid & 7, slot = bid >> 3;
    const int mtA = (xcd & 1) * 8 + (slot & 7), hpA = (xcd >> 1) * 4 + (slot >> 3);
    const int mtB = K::MAP8 ? mtA : (xcd & 1) * 8 + ((xcd >> 1) & 1) * 4 + (slot & 3), hpB = K::MAP8 ? hpA : (xcd >> 2) * 8 + (slot >> 2);
    const int rotA = 8 * (xcd & 1);
    const bool has_ffn = slot < 24 || (xcd & 2);
    const int fb = slot < 24 ? bid : 192 + (slot - 24) * 4 + (xcd >> 2) * 2 + (xcd & 1);
    const unsigned lds0 = (unsigned)(size_t)(AS3 char*)smem;
    const rsrc_t rsWS = mk_rsrc(ws, WS_BYTES);
    const rsrc_t rsX = mk_rsrc(x, 2048u * 2048u);

    auto touch3 = [&](unsigned& dst, int half, bool on = true) {
        const rsrc_t rsT = mk_rsrc(wqkv, (on && slot < 24 && !(xcd & 2)) ? 1024u * 6144u : 0u);
        const unsigned so = (unsigned)(((slot & 7) + 8 * half + rotA) & 15) * (64u * 6144u) + (unsigned)(slot >> 3) * 2048u + (unsigned)((xcd >> 1) * 4 + 3) * 128u;
        asm volatile("s_nop 4\n\tbuffer_load_dword %0, %1, %2, %3 offen" : "=v"(dst) : "v"(fresh_lane() * 6144), "s"(rsT), "s"(so) : "memory");
    };
    unsigned gate_t = 0;
    int gate_s = 0;
    auto hush = [&]() {
        if (gate_s == 0) {
            gate_t = __builtin_amdgcn_readfirstlane(*(AS3 volatile const unsigned*)(size_t)(lds0 + L_TM + 8)) - K::QA;
            gate_s = 1;
        }
        if (gate_s == 3 || (int)(now32() - gate_t) < 0) return;
        if (gate_s == 1) {
            wait4(lds0 + C_P, P_PUB);
            lds_wait(lds0 + C_TM, 1);
            gate_t = __builtin_amdgcn_readfirstlane(*(AS3 volatile const unsigned*)(size_t)(lds0 + L_TM)) - K::QB;
            gate_s = 2;
            if ((int)(now32() - gate_t) < 0) return;
        }
        lds_wait(lds0 + C_R, R_L1I);
        gate_s = 3;
    };

    unsigned ep, pf0;
    u32x4 xv[4][2], rv[2], gv[2], pf;
    auto ldi = [&]() {
            const int l = fresh_lane(), a = w & 1, row0 = mtB * 128 + hpB * 8 + 4 * a;
            const rsrc_t rsR = mk_rsrc(wr, 2048u), rsGW = mk_rsrc(gw, 2048u);
            const unsigned v16 = (unsigned)l * 16u;
            asm volatile("s_nop 4\n\tbuffer_load_dword %0, off, %1, 0 sc1" : "=v"(ep) : "s"(rsWS) : "memory");
#pragma unroll
            for (int r = 0; r < 4; ++r) {
                asm volatile("buffer_load_dwordx4 %0, %1, %2, %3 offen" : "=v"(xv[r][0]) : "v"(v16), "s"(rsX), "s"((unsigned)(row0 + r) * 2048u) : "memory");
                asm volatile("buffer_load_dwordx4 %0, %1, %2, %3 offen offset:1024" : "=v"(xv[r][1]) : "v"(v16), "s"(rsX), "s"((unsigned)(row0 + r) * 2048u) : "memory");
            }
            asm volatile("buffer_load_dwordx4 %0, %1, %2, 0 offen" : "=v"(rv[0]) : "v"(v16), "s"(rsR) : "memory");
            asm volatile("buffer_load_dwordx4 %0, %1, %2, 0 offen offset:1024" : "=v"(rv[1]) : "v"(v16), "s"(rsR) : "memory");
            asm volatile("buffer_load_dwordx4 %0, %1, %2, 0 offen" : "=v"(gv[0]) : "v"(v16), "s"(rsGW) : "memory");
            asm volatile("buffer_load_dwordx4 %0, %1, %2, 0 offen offset:1024" : "=v"(gv[1]) : "v"(v16), "s"(rsGW) : "memory");
            {
                const rsrc_t rsP = mk_rsrc(wphi, 32u * 2048u);
                const unsigned vp = (unsigned)(l ^ (((l >> 4) & 1) << 1)) * 16u;
#pragma unroll
                for (int i = 0; i < 2; ++i) dma16(rsP, vp, (unsigned)hpA * 4096u + a * 2048u + i * 1024u, lds0 + L_WP + a * 2048 + i * 1024);
            }
            CODE_FETCH(pf, slot * 2 + a);
            touch3(pf0, 0, a == 0);
    };
    if (__builtin_expect((w >> 1) == 2, 1)) {
        const int d = w - 4;
        LANE_VARS;
        const int srow = l >> 3, pos = l & 7;
        const rsrc_t rsW = mk_rsrc(wqkv, 1024u * 6144u);
        const unsigned vx0 = srow * 2048 + ((pos ^ ((srow >> 1) & 7)) << 4);
        const unsigned vw0 = srow * 6144 + (((((pos >> 1) ^ (srow & 3)) << 1) | (pos & 1)) << 4);
        unsigned vxs[4], vws[4];
#pragma unroll
        for (int i = 0; i < 4; ++i) {
            vxs[i] = (vx0 ^ ((i & 1) * 64u)) + i * (16384u - 1024u);
            vws[i] = (vw0 ^ ((i & 1) * 32u)) + i * (8u * 6144u - 1024u);
        }
        const unsigned sx0 = (unsigned)(mtA * 128) * 2048u, sw0 = (unsigned)hpA * 128u;
        const int lq = ((slot >> 3) - slot) & 3;
        const unsigned ktab = lq == 0 ? K::KT0 : lq == 1 ? K::KT1 : lq == 2 ? K::KT2 : K::KT3;
        auto chunk = [&](int kt) { return (((ktab >> (4 * (kt >> 1))) & 7u) * 2u + (unsigned)((kt & 1) + rotA)) & 15u; };
        auto dx = [&](unsigned kk, int i, unsigned la) { dma4<K::PACE>(rsX, vxs, sx0 + kk * 128u + i * 65536u, lds0 + la + i * 4096); };
        auto dw = [&](unsigned kk, int p, int i, unsigned la) { dma4<K::PACE>(rsW, vws, sw0 + p * 2048u + (kk * 64u + i * 32u) * 6144u, lds0 + la + i * 4096); };
        asm volatile("s_nop 4" ::: "memory");
        unsigned lb = 0;
#pragma unroll 1
        for (int kt = 0; kt < 13; ++kt) {
            if (kt >= 2) {
                if (kt <= 10) wait_vm<20>(); else wait_vm<16>();
                bar();
            }
            const unsigned kk = chunk(kt);
            dx(kk, 2 * d, lb);
            dw(kk, 1 + d, 0, lb + 24576 + d * 8192);
            dx(kk, 2 * d + 1, lb);
            dw(kk, 1 + d, 1, lb + 24576 + d * 8192);
            if (kt < 10) dw(kk, 0, d, lb + 16384);
            lb = lb == 2 * SLOT ? 0u : lb + SLOT;
        }
        auto dxs = [&](int i) {
            const unsigned kk = chunk(13 + i), lx = d ? XS1[i] - 8192u : XS0[i];
            dx(kk, 2 * d, lx);
            dx(kk, 2 * d + 1, lx);
        };
        auto dws = [&](int i) {
            const unsigned kk = chunk(13 + i), lw = ((1 + i) % 3) * SLOT + 24576 + d * 8192;
            dw(kk, 1 + d, 0, lw);
            dw(kk, 1 + d, 1, lw);
        };
        auto dq = [&](int i) {
            const unsigned kk = chunk(i < 2 ? 14 + i : 8 + i);
            dw(kk, 0, 0, QW[i]);
            dw(kk, 0, 1, QW[i]);
        };
        if (K::XAH) dxs(0);
        wait_vm<K::XAH ? 24 : 16>();
        bar();
        dws(0);
        dxs(K::XAH ? 1 : 0);
        wait_vm<K::XAH ? 24 : 16>();
        bar();
        dws(1);
        dxs(K::XAH ? 2 : 1);
        wait_vm<K::XAH ? 24 : 16>();
        bar();
        dws(2);
        if (!K::XAH) dxs(2);
        if (d == 0) {
            dq(0);
            wait_vm<24>();
        } else {
            wait_vm<16>();
        }
        bar();
        if (d == 0) wait_vm<8>(); else wait_vm<0>();
        post(lds0 + C_ST + d * 4, 1);
        __builtin_amdgcn_s_setprio(3);
        if (d == 0) {
            const rsrc_t rsO = mk_rsrc(wo, 1024u * 2048u);
            unsigned vos[4];
#pragma unroll
            for (int i = 0; i < 4; ++i) vos[i] = WO_VOFF + i * (16384u - 1024u);
            unsigned sb[2] = {0u, 0u};
            auto wo_run = [&](int b0, int b1) {
#pragma unroll
                for (int b = b0; b < b1; ++b) {
                    unsigned so;
                    asm volatile("s_mov_b32 m0, %7\n\ts_add_u32 %0, %6, %8\n\tbuffer_load_dwordx4 %1, %5, %0 offen lds\n\tbuffer_load_dwordx4 %2, %5, %0 offen offset:1024 lds\n\t"
                                 "buffer_load_dwordx4 %3, %5, %0 offen offset:2048 lds\n\tbuffer_load_dwordx4 %4, %5, %0 offen offset:3072 lds\n\ts_waitcnt vmcnt(%9)"
                                 : "=&s"(so) : "v"(vos[0]), "v"(vos[1]), "v"(vos[2]), "v"(vos[3]), "s"(rsO), "s"(sb[b >> 4]), "i"(b * 4096), "i"((b & 15) * 65536), "i"(K::WO_D)
                                 : "memory", "scc");
                }
            };
            dq(1);
            dq(2);
            wait4(lds0 + C_P, P_E14);
            dq(3);
            dq(4);
            dq(5);
            post(lds0 + C_HOLD, 1);
            wait_vm<0>();
            post(lds0 + C_Q, 1);
            lds_wait(lds0 + C_TM, 1);
            sb[1] = (__builtin_amdgcn_readfirstlane(*(AS3 volatile const unsigned*)(size_t)(lds0 + L_TM + 4)) & 1u) * (16u * 65536u) + (unsigned)hpB * 128u;
            sb[0] = sb[1] ^ (16u * 65536u);
            asm volatile("s_nop 4" : "+s"(sb[0]), "+s"(sb[1]));
            wait4(lds0 + C_P, K::WOE ? P_E15 : P_PUB);
            wo_run(30, 32);
            wo_run(6, 10);
            wait4(lds0 + C_P, P_PUB);
            {
                const unsigned s0 = (unsigned)(mtB * 128) * 2048u + (unsigned)hpB * 128u;
#pragma unroll
                for (int i = 0; i < 4; ++i) dma4<0>(rsX, vxs, s0 + i * 65536u, lds0 + L_XE + i * 4096);
            }
            wait4(lds0 + C_P, P_Q1 + 2);
            wo_run(16, 24);
            post(lds0 + C_XE, 1);
            wait4(lds0 + C_P, P_QRD);
            wo_run(24, 30);
            wo_run(0, 6);
            post(lds0 + C_WO, 2);
            wo_run(10, 16);
            post(lds0 + C_WO, 3);
            wait_vm<0>();
            post(lds0 + C_WO, 4);
        } else {
            const unsigned v16 = (unsigned)l * 16u;
            const unsigned tag = *(AS3 const unsigned*)(size_t)(lds0 + L_TAG);
            const rsrc_t rsB = mk_rsrc(bulk_base(ws), WB_BYTES);
            lds_wait(lds0 + C_IDX, 1);
            const u32x2 pl = *(AS3 const u32x2*)(size_t)(lds0 + L_PLC);
            const unsigned okp = __builtin_amdgcn_readfirstlane(pl[0]), osm = __builtin_amdgcn_readfirstlane(pl[1]);
            unsigned t1 = 0;
            const unsigned base = now32() - 4096u;
            u32x4 p0, p1;
            CODE_FETCH(p0, slot * 2);
            CODE_FETCH(p1, slot * 2 + 1);
            const int ch = l & 3;
            unsigned mk01 = ch < 2 ? ~0u : 0u, mk0 = ch == 0 ? ~0u : 0u, mk1 = ch == 1 ? ~0u : 0u, mk2 = ch == 2 ? ~0u : 0u, mkl = (l & 31) < 2 ? ~0u : 0u;
            unsigned vkf = (unsigned)((g >> 1) * 16 + c) * 16u + (unsigned)(g & 1) * 8u, vfm = l < 2 ? (unsigned)l * 8u : l == 32 ? 16u : 0x80000000u;
            unsigned kwa = lds0 + L_KVF + l * 16, ra = lds0 + C_R;
            asm volatile("" : "+v"(mk01), "+v"(mk0), "+v"(mk1), "+v"(mk2), "+v"(mkl), "+v"(vkf), "+v"(vfm), "+v"(kwa), "+v"(ra));
            bf16x8 sel;
#pragma unroll
            for (int j = 0; j < 8; ++j) sel[j] = ((c & 7) == j && (c >> 3) == (g & 1)) ? (short)0x3f80 : (short)0;
            asm volatile("" : "+v"(sel));
            {
                unsigned ta, tb;
                auto latest = [&](unsigned off, bool& all) {
                    const u32x4 pa = ld128c(rsWS, v16, off), pb = ld128c(rsWS, v16 + 1024u, off);
                    const unsigned ma = max(pa[1] == tag ? min(pa[0] - base, 8192u) : 0u, pa[3] == tag ? min(pa[2] - base, 8192u) : 0u);
                    const unsigned mb = max(pb[1] == tag ? min(pb[0] - base, 8192u) : 0u, pb[3] == tag ? min(pb[2] - base, 8192u) : 0u);
                    all = __all(pa[1] == tag && pa[3] == tag && pb[1] == tag && pb[3] == tag);
                    ta = __builtin_amdgcn_readfirstlane(wave_umax(ma));
                    tb = __builtin_amdgcn_readfirstlane(wave_umax(mb));
                    const unsigned av = __builtin_amdgcn_readfirstlane(wave_usum(min(pa[0] - base, 8192u) + min(pa[2] - base, 8192u) + min(pb[0] - base, 8192u) + min(pb[2] - base, 8192u))) >> 8;
                    return base + max(max(ta, tb), all ? av + K::SLS : 0u);
                };
                bool all;
                wait4(lds0 + C_P, has_ffn ? K::TMW : P_PUB);
#pragma unroll 1
                for (int it = 0; it < 4; ++it) {
                    t1 = latest(WS_PT, all) + K::D1;
                    if (all) break;
                    wait4(lds0 + C_P, P_PUB);
                    __builtin_amdgcn_s_sleep(K::PT_S);
                }
                asm volatile("s_waitcnt vmcnt(0)" : "+v"(p0), "+v"(p1) :: "memory");
                post(lds0 + L_TM, t1);
                const unsigned t3 = t1 + (K::D2 + K::D3);
                post(lds0 + L_TM + 4, (t3 & ~1u) | (tb < ta ? 1u : 0u));
                post(lds0 + C_TM, 1);
                asm volatile("s_branch .Lpad_r\n\t.p2align 12\n.Lpad_r:\n\ts_cbranch_execz .Lpad_ret" ::: "memory");
                wait4(lds0 + C_P, P_PUB);
            }
            bool late = false;
#pragma unroll 1
            for (int it = 0; it < TRY_LIMIT; ++it) {
                unsigned bad;
                asm volatile(RED1_ASM : [st] "=&s"(bad)
                             : [rsb] "s"(rsB), [t1] "s"(t1), [s0] "s"(okp ^ ((unsigned)mtA * 16384u)), [sm] "s"(osm + (unsigned)((mtA >> 3) * 16384 + (mtA & 7) * 1024)),
                               [sf] "s"(osm + 32768u + (unsigned)mtA * 512u), [sg] "s"(osm + 49152u + (unsigned)mtA * 64u), [hi] "s"(0xffffffff00000000ull),
                               [v16] "v"(v16), [ra] "v"(ra), [tag] "v"(tag), [mk01] "v"(mk01), [mk2] "v"(mk2), [mkl] "v"(mkl), [sel] "v"(sel), [vkf] "v"(vkf), [vfm] "v"(vfm)
                             : "memory", "vcc", "scc", RED1_CLOB);
                if (__builtin_expect(!bad, 1)) break;
                late = true;
                if (it < K::RE1) continue;
                int i2 = 0;
#pragma unroll 1
                for (; i2 < POLL_LIMIT; ++i2) {
                    __builtin_amdgcn_s_sleep(K::RETRY_S);
                    fence_c();
                    if (__all(ld32c(rsB, (unsigned)c * 64u + 36u, osm + (unsigned)((mtA >> 3) * 16384 + (mtA & 7) * 1024)) == tag)) break;
                }
                if (i2 == POLL_LIMIT) break;
            }
            post(lds0 + C_R, R_L1);
            wait4(lds0 + C_P, P_Q1);
            if (late) __builtin_amdgcn_s_sleep(K::L2_S);
            unsigned pv = R_L2I;
#pragma unroll 1
            for (int it = 0; it < TRY_LIMIT; ++it) {
                unsigned bad;
                asm volatile(RED2_ASM : [st] "=&s"(bad), [pv] "+v"(pv)
                             : [rsb] "s"(rsB), [t2] "s"(t1 + K::D2), [s0] "s"(osm + 32768u), [frc] "s"(K::FORCE_REDO && it == 0 ? 1 : 0),
                               [v16] "v"(v16), [ra] "v"(ra), [kwa] "v"(kwa), [tag] "v"(tag), [mk0] "v"(mk0), [mk1] "v"(mk1)
                             : "memory", "vcc", "scc", RED1_CLOB);
                if (__builtin_expect(!bad, 1)) break;
                if (it < K::RE1) continue;
                int i2 = 0;
#pragma unroll 1
                for (; i2 < POLL_LIMIT; ++i2) {
                    __builtin_amdgcn_s_sleep(K::RETRY_S);
                    fence_c();
                    if (__all(ld32c(rsB, (unsigned)c * 64u + 20u, osm + 49152u) == tag)) break;
                }
                if (i2 == POLL_LIMIT) break;
            }
            post(ra, pv + 1u);
            asm volatile(".Lff_end1:");
        }
        return;
    }

    asm volatile(".Lpad_d:\n\ts_cbranch_execz .Lpad_top" ::: "memory");
    __builtin_amdgcn_sched_barrier(0);
    if (w >= 6) {
        const int a = w - 6;
        const int cb = fb >> 2, kq = fb & 3;
        u32x4 wg_[16], wu_[16], wd_[16];
        unsigned tag;
        int idx;
        u32x4 xs[2];
        {
        LANE_VARS;
        const int srow = l >> 3, pos = l & 7;
        const unsigned nb = has_ffn ? 1024u * 7168u : 0u;
        const rsrc_t rsG = mk_rsrc(wgp, nb), rsU = mk_rsrc(wup, nb), rsD = mk_rsrc(wdp, nb);
        const unsigned vf = (unsigned)(16 * srow) * 7168u + (unsigned)pos * 16u;
        const int k8 = fb & 7, cg = (fb >> 4) * 4 + ((fb >> 3) & 1) * 2 + a, cu = cg ^ 2;
        const unsigned sg = (unsigned)(k8 * 128) * 7168u + (unsigned)cg * 128u, su = sg ^ 256u;
        auto ldg = [&]() {
#pragma unroll
            for (int i = 0; i < 16; ++i) {
                asm volatile("buffer_load_dwordx4 %0, %1, %2, %3 offen nt" : "=v"(wg_[i]) : "v"(vf), "s"(rsG), "s"(sg + i * 7168u) : "memory");
            }
        };
        auto ldu = [&]() {
#pragma unroll
            for (int i = 0; i < 16; ++i) {
                asm volatile("buffer_load_dwordx4 %0, %1, %2, %3 offen nt" : "=v"(wu_[i]) : "v"(vf), "s"(rsU), "s"(su + i * 7168u) : "memory");
            }
        };
        auto ldd = [&](int i0, int i1) {
            const unsigned vd = (unsigned)g * (16u * 2048u) + (unsigned)c * 16u;
            const unsigned sd = (unsigned)(cb * 64) * 2048u + (unsigned)(kq * 512 + a * 256);
#pragma unroll
            for (int i = i0; i < i1; ++i) {
                if ((i & 3) == 0 && K::HUSH) { wait_vm<K::FD>(); hush(); }
                asm volatile("buffer_load_dwordx4 %0, %1, %2, %3 offen nt" : "=v"(wd_[i]) : "v"(vd), "s"(rsD), "s"(sd + i * 2048u) : "memory");
            }
        };
        const int row0 = mtB * 128 + hpB * 8 + 4 * a;
        if (a == 0) {
            *(AS3 unsigned*)(size_t)(lds0 + L_SM + l * 4) = 0u;
            wait_lgkm0();
        }
        int b = 0;
        unsigned tl = 0, pmin = 255;
        auto tick = [&]() {
            bar();
            if (a == 1) {
                const unsigned t = now32();
                if (b > 2 && b <= 16 - K::ND) pmin = min(pmin, t - tl);
                tl = t;
            }
            ++b;
        };
        auto upto = [&](int e) {
#pragma unroll 1
            while (b <= e) tick();
        };
        auto pads = [&]() {
#pragma unroll 1
            for (int i = a; i < 6; i += 2) {
                unsigned long long ex;
                asm volatile("s_mov_b64 %0, exec\n\ts_mov_b64 exec, 0\n\t"
                             "s_cmp_eq_u32 %1, 0\n\ts_cbranch_scc1 .Lpad_ab1\n\ts_cmp_eq_u32 %1, 1\n\ts_cbranch_scc1 .Lpad_ab2\n\t"
                             "s_cmp_eq_u32 %1, 2\n\ts_cbranch_scc1 .Lpad_c0\n\ts_cmp_eq_u32 %1, 3\n\ts_cbranch_scc1 .Lpad_c1\n\t"
                             "s_cmp_eq_u32 %1, 4\n\ts_cbranch_scc1 .Lpad_r\n"
                             ".Lpad_ret:\n\ts_mov_b64 exec, %0" : "=&s"(ex) : "s"(i) : "scc", "memory");
                if (i < 4 && b <= 9) tick();
            }
        };
        auto nap = [&](int n) {
#pragma unroll 1
            for (int i = 0; i < n; ++i) __builtin_amdgcn_s_sleep(1);
        };
        const int lq4 = ((((slot >> 3) - slot) & 3) + (slot & 4)) * 4;
        const int bg = ((unsigned)K::BGT >> lq4) & 15, bu = ((unsigned)K::BUT >> lq4) & 15;
        unsigned pf3 = 0;
        ldi();
        u32x2 gr;
        unsigned vgr;
        auto router = [&](auto n_) {
            tick();
            asm volatile("s_waitcnt vmcnt(%15)"
                         : "+v"(ep), "+v"(xv[0][0]), "+v"(xv[0][1]), "+v"(xv[1][0]), "+v"(xv[1][1]), "+v"(xv[2][0]), "+v"(xv[2][1]), "+v"(xv[3][0]),
                           "+v"(xv[3][1]), "+v"(rv[0]), "+v"(rv[1]), "+v"(gv[0]), "+v"(gv[1]), "+v"(pf), "+v"(pf0)
                         : "i"(decltype(n_)::value) : "memory");
            tag = __builtin_amdgcn_readfirstlane(ep) * 2654435761u + 0x9e3779b9u;
            float ra[4], ga[4];
#pragma unroll
            for (int r = 0; r < 4; ++r) {
                float racc = 0.f, gacc = 0.f;
#pragma unroll
                for (int i = 0; i < 2; ++i)
#pragma unroll
                    for (int j = 0; j < 4; ++j) {
                        racc = dot2(xv[r][i][j], rv[i][j], racc);
                        gacc = dot2(xv[r][i][j], gv[i][j], gacc);
                    }
                ra[r] = racc;
                ga[r] = gacc;
                asm volatile("" : "+v"(ra[r]), "+v"(ga[r]));
            }
            tick();
            float r2[2], g2[2];
#pragma unroll
            for (int i = 0; i < 2; ++i) {
                auto p = __builtin_amdgcn_permlane32_swap(FU(ra[2 * i]), FU(ra[2 * i + 1]), false, false);
                r2[i] = UF(p[0]) + UF(p[1]);
                auto q = __builtin_amdgcn_permlane32_swap(FU(ga[2 * i]), FU(ga[2 * i + 1]), false, false);
                g2[i] = UF(q[0]) + UF(q[1]);
            }
            auto p = __builtin_amdgcn_permlane16_swap(FU(r2[0]), FU(r2[1]), false, false);
            auto q = __builtin_amdgcn_permlane16_swap(FU(g2[0]), FU(g2[1]), false, false);
            float rs = row16_sum(UF(p[0]) + UF(p[1])), gs = row16_sum(UF(q[0]) + UF(q[1]));
            asm volatile("" : "+v"(rs), "+v"(gs));
            const int row = row0 + (g >> 1) + 2 * (g & 1);
            const unsigned rbits = f2bf(rs);
            const unsigned okey = (rbits & 0x8000u) ? (~rbits & 0xffffu) : (rbits | 0x8000u);
            if (c == 0) *(AS3 unsigned*)(size_t)(lds0 + L_KEY + (4 * a + g) * 4) = (okey << 16) | (unsigned)(0xffff - row);
            if (l == 0 && a == 0) *(AS3 unsigned*)(size_t)(lds0 + L_TAG) = tag;
            gr = u32x2{FU(rbf(1.f / (1.f + __expf(-rbf(gs))))), tag};
            vgr = c == 0 ? WS_G + (unsigned)row * 8u : 0x80000000u;
            asm volatile("" : "+v"(gr), "+v"(vgr));
            wait_lgkm0();
            tick();
            if (a == 0) {
                unsigned k8 = *(AS3 const unsigned*)(size_t)(lds0 + L_KEY + (l & 7) * 4);
                k8 = max(k8, (unsigned)__shfl_xor((int)k8, 1));
                k8 = max(k8, (unsigned)__shfl_xor((int)k8, 2));
                k8 = max(k8, (unsigned)__shfl_xor((int)k8, 4));
                const u32x4 pv = *(AS3 const u32x4*)(size_t)(lds0 + L_PRB);
                const unsigned lo = min(min(pv[0] & 0xffffu, pv[2] & 0xffffu), min(pv[1] >> 16, pv[3] >> 16));
                const unsigned up = min(min(pv[0] >> 16, pv[2] >> 16), min(pv[1] & 0xffffu, pv[3] & 0xffffu));
                st64c(u32x2{k8 ^ (up < lo ? 0x2000u : 0u), tag}, rsWS, l ? 0x80000000u : WS_RG + (unsigned)bid * 8u, 0);
            }
        };
        u32x4 ga, gb;
        const unsigned vr = WS_RG + (unsigned)l * 16u;
        auto ldrg = [&]() {
            asm volatile("buffer_load_dwordx4 %0, %1, %2, 0 offen sc1" : "=v"(ga) : "v"(vr), "s"(rsWS) : "memory");
            asm volatile("buffer_load_dwordx4 %0, %1, %2, 0 offen offset:1024 sc1" : "=v"(gb) : "v"(vr), "s"(rsWS) : "memory");
        };
        router(std::integral_constant<int, 0>{});
        __builtin_amdgcn_s_setprio(2);
        upto(bg);
        nap(K::SLG);
        if (a == 0) {
            touch3(pf3, 1);
        } else {
            const rsrc_t rsT = mk_rsrc(wqkv, 1024u * 6144u);
            asm volatile("s_nop 4\n\tbuffer_load_dword %0, %1, %2, %3 offen" : "=v"(pf3) : "v"((unsigned)(l & 31) * 6144u), "s"(rsT),
                         "s"((unsigned)(((12 + rotA) & 15) * 64 + (slot & 7) * 32) * 6144u + (unsigned)hpA * 128u) : "memory");
        }
        ldg();
        upto(bu);
        if (bu != bg) nap(K::SLG);
        ldu();
        upto(((unsigned)K::BRT >> lq4) & 15);
        ldrg();
        pads();
        upto(9);
        asm volatile("s_waitcnt vmcnt(0)"
                     : "+v"(wg_[0]), "+v"(wg_[1]), "+v"(wg_[2]), "+v"(wg_[3]), "+v"(wg_[4]), "+v"(wg_[5]), "+v"(wg_[6]), "+v"(wg_[7]),
                       "+v"(wg_[8]), "+v"(wg_[9]), "+v"(wg_[10]), "+v"(wg_[11]), "+v"(wg_[12]), "+v"(wg_[13]), "+v"(wg_[14]), "+v"(wg_[15]),
                       "+v"(ga), "+v"(gb), "+v"(pf3)
                     :: "memory");
        asm volatile(""
                     : "+v"(wu_[0]), "+v"(wu_[1]), "+v"(wu_[2]), "+v"(wu_[3]), "+v"(wu_[4]), "+v"(wu_[5]), "+v"(wu_[6]), "+v"(wu_[7]),
                       "+v"(wu_[8]), "+v"(wu_[9]), "+v"(wu_[10]), "+v"(wu_[11]), "+v"(wu_[12]), "+v"(wu_[13]), "+v"(wu_[14]), "+v"(wu_[15])
                     :: "memory");
#pragma unroll 1
        for (int it = 0; it < POLL_LIMIT; ++it) {
            if (__all((ga[1] == tag) && (ga[3] == tag) && (gb[1] == tag) && (gb[3] == tag))) break;
            __builtin_amdgcn_s_sleep(4 * K::RETRY_S);
            fence_c();
            ga = ld128c(rsWS, vr, 0);
            gb = ld128c(rsWS, vr + 1024u, 0);
        }
        const unsigned key = __builtin_amdgcn_readfirstlane(wave_umax(max(max(ga[0] | 0x3800u, ga[2] | 0x3800u), max(gb[0] | 0x3800u, gb[2] | 0x3800u))));
        idx = (0xffff - (int)(key & 0xffffu)) & 2047;
        unsigned tw[2];
        auto twf = [&]() {
            const rsrc_t rsO = mk_rsrc(wo, 1024u * 2048u);
            const unsigned vt = (unsigned)srow * 2048u + (unsigned)pos * 128u, st = (unsigned)(slot * 32 + a * 16) * 2048u + (unsigned)(xcd >> 2) * 1024u;
            nap(K::X1);
            if (K::MAP8) {
                asm volatile("s_nop 4\n\tbuffer_load_dword %0, %1, %2, %3 offen" : "=v"(tw[0]) : "v"((unsigned)(l >> 2) * 2048u + (unsigned)(l & 3) * 128u), "s"(rsO),
                             "s"((unsigned)(slot * 32 + a * 16) * 2048u + (unsigned)(xcd >> 1) * 512u) : "memory");
                tw[1] = 0;
            } else {
                asm volatile("s_nop 4\n\tbuffer_load_dword %0, %1, %2, %3 offen" : "=v"(tw[0]) : "v"(vt), "s"(rsO), "s"(st) : "memory");
                asm volatile("buffer_load_dword %0, %1, %2, %3 offen" : "=v"(tw[1]) : "v"(vt), "s"(rsO), "s"(st + 16384u) : "memory");
            }
        };
        tick();
        if (K::TWB == 10) twf();
        {
            const unsigned vs = (unsigned)srow * 32u, ss = (unsigned)idx * 2048u + (unsigned)(k8 * 256);
            asm volatile("s_nop 4\n\tbuffer_load_dwordx4 %0, %1, %2, %3 offen" : "=v"(xs[0]) : "v"(vs), "s"(rsX), "s"(ss) : "memory");
            asm volatile("buffer_load_dwordx4 %0, %1, %2, %3 offen offset:16" : "=v"(xs[1]) : "v"(vs), "s"(rsX), "s"(ss) : "memory");
        }
        if (a == 1) {
            const u32x2 pt = {tl + (pmin * (K::PRED_K + 12) >> 2) + K::PRED_C + K::XE, tag};
            st64c(pt, rsWS, l ? 0x80000000u : WS_PT + (unsigned)(hpA * 16 + mtA) * 8u, 0);
        } else {
            const unsigned kb = key >> 16;
            const unsigned rb_ = (kb & 0x8000u) ? (kb & 0x7fffu) : (~kb & 0xffffu);
            const float rsel = rbf(1.f / (1.f + __expf(-UF(rb_ << 16))));
            const u32x2 iv = {(unsigned)idx, FU(rsel)};
            *(AS3 u32x2*)(size_t)(lds0 + L_IDX) = iv;
            const unsigned nv = (~ga[0] & 0x3800u) + (~ga[2] & 0x3800u) + (~gb[0] & 0x3800u) + (~gb[2] & 0x3800u);
            const unsigned votes = wave_usum((l & 3) == (xcd >> 1) ? nv >> 11 : 0u);
            const unsigned o0 = (unsigned)(xcd >> 1) * (2u << 20) + (K::X2 < 2 ? K::X2 * 8192u : votes > 128u ? 8192u : 0u);
            const u32x4 pl = {o0 + WB_KVP + (((unsigned)(hpA & 3) * 8u) ^ (tag & 31u)) * 32768u, o0 + WB_SM + (((unsigned)hpA & 3u) ^ ((tag >> 5) & 7u)) * 65536u,
                              ((tag >> 8) & 63u) * 2048u, 0u};
            *(AS3 u32x4*)(size_t)(lds0 + L_PLC) = pl;
            post(lds0 + C_IDX, 1);
            wait_lgkm0();
        }
        unsigned mk[4];
#pragma unroll
        for (int k = 0; k < 4; ++k) mk[k] = (pos >> 1) == k ? ((pos & 1) ? 0xffff0000u : 0x0000ffffu) : 0u;
        f32x4 sg4 = {0.f, 0.f, 0.f, 0.f}, su4 = sg4;
        auto dot = [&](int i0, int i1) {
#pragma unroll
            for (int i = i0; i < i1; ++i) {
                const unsigned xw = xs[i >> 3][(i >> 1) & 3];
                const unsigned xd = __builtin_amdgcn_perm(xw, xw, (i & 1) ? 0x03020302u : 0x01000100u);
                const u32x4 am = {xd & mk[0], xd & mk[1], xd & mk[2], xd & mk[3]};
                sg4 = mfma16(__builtin_bit_cast(bf16x8, am), __builtin_bit_cast(bf16x8, wg_[i]), sg4);
                su4 = mfma16(__builtin_bit_cast(bf16x8, am), __builtin_bit_cast(bf16x8, wu_[i]), su4);
            }
            asm volatile("" : "+v"(sg4), "+v"(su4));
        };
        tick();
        asm volatile("s_waitcnt vmcnt(0)" : "+v"(xs[0]), "+v"(xs[1]) :: "memory");
        if (K::TWB == 11) twf();
        dot(0, K::DS1);
        tick();
        dot(K::DS1, K::DS2);
        if (K::TWB == 12) twf();
        tick();
        if (a == 1) {
            post(lds0 + L_TM + 8, tl + (pmin * (K::PRED_K + K::PK13) >> 2) + K::PRED_C);
            wait_lgkm0();
        }
        dot(K::DS2, K::DS3);
        if (K::TWB == 13) twf();
        __builtin_amdgcn_s_setprio(3);
        tick();
        if (!K::DLATE) ldd(0, 8);
        dot(K::DS3, 16);
        {
            unsigned fo[4];
#pragma unroll
            for (int r = 0; r < 4; ++r) {
                auto p = __builtin_amdgcn_permlane32_swap(FU(sg4[r]), FU(su4[r]), false, false);
                fo[r] = FU(UF(p[0]) + UF(__builtin_amdgcn_update_dpp(0, (int)p[1], 0x128, 0xf, 0xf, false)));
            }
            const unsigned vf0 = ((l & 8) || !has_ffn) ? 0x80000000u : WS_FG + (unsigned)(((l >> 5) ? cu : cg) * 8 + k8) * 1024u + (unsigned)((l >> 5) * 512 + (pos * 8 + (g & 1) * 4) * 8);
            const u32x4 s0 = {fo[0], tag, fo[1], tag}, s1 = {fo[2], tag, fo[3], tag};
            st128c(s0, rsWS, vf0, 0);
            st128c(s1, rsWS, vf0 + 16u, 0);
            st64c(gr, rsWS, vgr, 0);
        }
        if (bid == 0 && a == 0 && l == 0) st32c(tag, rsWS, WS_EPOCH, 0);
        if (K::DLATE) {
            lds_wait(lds0 + C_HOLD, 1);
            ldd(0, 8);
        }
        ldd(8, 16);
        lds_wait(lds0 + C_TM, 1);
        const unsigned tm0 = *(AS3 const unsigned*)(size_t)(lds0 + L_TM);
        const unsigned vq = (unsigned)((l & 8) * 64 + (g * 8 + (l & 7)) * 16);
        u32x4 fq[8];
        unsigned ds[4];
#pragma unroll
        for (int k = 0; k < 4; ++k) ds[k] = (((l & 8) ? 0x03020302u : 0x01000100u) & mk[k]) | (0x0c0c0c0cu & ~mk[k]);
        asm volatile("" : "+v"(ds[0]), "+v"(ds[1]), "+v"(ds[2]), "+v"(ds[3]));
        if (has_ffn) {
            wait_until(__builtin_amdgcn_readfirstlane(tm0) + K::FG_AT);
#pragma unroll 1
            for (int it = 0; it < POLL_LIMIT; ++it) {
                unsigned bad = 0;
#pragma unroll
                for (int i = 0; i < 8; ++i) fq[i] = ld128c(rsWS, vq + (i & 3) * 1024u, WS_FG + (unsigned)cb * 8192u + (i >> 2) * 4096u);
#pragma unroll
                for (int i = 0; i < 8; ++i) bad |= (fq[i][1] ^ tag) | (fq[i][3] ^ tag);
                if (__all(bad == 0u)) break;
                __builtin_amdgcn_s_sleep(2 * K::RETRY_S);
                fence_c();
            }
        }
        asm volatile("s_waitcnt vmcnt(0)"
                     : "+v"(wd_[0]), "+v"(wd_[1]), "+v"(wd_[2]), "+v"(wd_[3]), "+v"(wd_[4]), "+v"(wd_[5]), "+v"(wd_[6]), "+v"(wd_[7]),
                       "+v"(wd_[8]), "+v"(wd_[9]), "+v"(wd_[10]), "+v"(wd_[11]), "+v"(wd_[12]), "+v"(wd_[13]), "+v"(wd_[14]), "+v"(wd_[15]),
                       "+v"(tw[0]), "+v"(tw[1])
                     :: "memory");
        if (has_ffn) {
            if (K::DWP) lds_wait(lds0 + C_P + (2 + a) * 4, P_PUB);
            __builtin_amdgcn_s_setprio(K::HP);
            float hv[2];
#pragma unroll
            for (int j = 0; j < 2; ++j) {
                const float gs = rbf(((UF(fq[0][2 * j]) + UF(fq[1][2 * j])) + (UF(fq[2][2 * j]) + UF(fq[3][2 * j]))) +
                                     ((UF(fq[4][2 * j]) + UF(fq[5][2 * j])) + (UF(fq[6][2 * j]) + UF(fq[7][2 * j]))));
                const float us = UF(__builtin_amdgcn_update_dpp(0, (int)FU(gs), 0x128, 0xf, 0xf, false));
                hv[j] = rbf(gs / (1.f + __expf(-gs))) * us;
            }
            const unsigned hp = pack2(hv[0], hv[1]);
            f32x4 ye = {0.f, 0.f, 0.f, 0.f}, yo = ye;
            auto dn = [&](auto mc) {
                constexpr int m = decltype(mc)::value;
                const unsigned hb = (unsigned)__builtin_amdgcn_update_dpp(0, (int)hp, 0x150 + m, 0xf, 0xf, false);
                const u32x4 am = {__builtin_amdgcn_perm(hb, hb, ds[0]), __builtin_amdgcn_perm(hb, hb, ds[1]), __builtin_amdgcn_perm(hb, hb, ds[2]), __builtin_amdgcn_perm(hb, hb, ds[3])};
                ye = mfma16(__builtin_bit_cast(bf16x8, am), __builtin_bit_cast(bf16x8, wd_[2 * m]), ye);
                yo = mfma16(__builtin_bit_cast(bf16x8, am), __builtin_bit_cast(bf16x8, wd_[2 * m + 1]), yo);
            };
            dn(std::integral_constant<int, 0>{}); dn(std::integral_constant<int, 1>{}); dn(std::integral_constant<int, 2>{}); dn(std::integral_constant<int, 3>{});
            dn(std::integral_constant<int, 4>{}); dn(std::integral_constant<int, 5>{}); dn(std::integral_constant<int, 6>{}); dn(std::integral_constant<int, 7>{});
            float y2[2];
#pragma unroll
            for (int j = 0; j < 2; ++j) {
                auto p = __builtin_amdgcn_permlane32_swap(FU(ye[j]), FU(ye[2 + j]), false, false);
                auto q = __builtin_amdgcn_permlane32_swap(FU(yo[j]), FU(yo[2 + j]), false, false);
                y2[j] = UF(p[0]) + UF(q[1]);
            }
            const u32x4 st = {FU(y2[0]), tag, FU(y2[1]), tag};
            st128c(st, rsWS, (unsigned)(c * 64 + (g & 1) * 32 + (g >> 1) * 16), WS_Y + (unsigned)cb * 8192u + (unsigned)(kq * 2048 + a * 1024));
        }
        {
            const unsigned col8 = (unsigned)(xcd * 128 + slot * 4 + a * 2) * 8u;
            const unsigned vy = l < 56 ? (unsigned)l * 8192u : 0x80000000u;
            u32x4 t;
            __builtin_amdgcn_s_setprio(3);
            wait_until(__builtin_amdgcn_readfirstlane(tm0) + K::Y_AT);
#pragma unroll 1
            for (int it = 0; it < POLL_LIMIT; ++it) {
                t = ld128c(rsWS, vy, WS_Y + col8);
                if (__all(l >= 56 || (t[1] == tag && t[3] == tag))) break;
                __builtin_amdgcn_s_sleep(4 * K::RETRY_S);
                fence_c();
            }
            auto p = __builtin_amdgcn_permlane32_swap(t[0], t[2], false, false);
            const float s = row16_sum(x16_sum(UF(p[0]) + UF(p[1])));
            st64c(u32x2{FU(s), tag}, rsWS, (l & 31) ? 0x80000000u : (unsigned)(l >> 2), WS_YF + col8);
        }
        if ((idx >> 7) == mtB && a == 0) {
            u32x4 t;
            wait_until(__builtin_amdgcn_readfirstlane(tm0) + K::Y_AT + K::X0);
#pragma unroll 1
            for (int it = 0; it < POLL_LIMIT; ++it) {
                t = ld128c(rsWS, (unsigned)(l & 31) * 16u, WS_YF + (unsigned)hpB * 512u);
                if (__all(t[1] == tag && t[3] == tag)) break;
                __builtin_amdgcn_s_sleep(2 * K::RETRY_S);
                fence_c();
            }
            lds_wait(lds0 + C_XE, 1);
            const int rr = idx & 127;
            const unsigned xx = *(AS3 const unsigned*)(size_t)(lds0 + L_XE + rr * 128 + ((((l & 31) >> 2) ^ ((rr >> 1) & 7)) << 4) + (l & 3) * 4);
            const float rsel = *(AS3 const float*)(size_t)(lds0 + L_IDX + 4);
            *(AS3 f32x2*)(size_t)(lds0 + L_Y + 256 + (l & 31) * 8) = f32x2{rbf(lo16(xx) + rbf(rbf(UF(t[0])) * rsel)), rbf(hi16(xx) + rbf(rbf(UF(t[2])) * rsel))};
            post(lds0 + C_YRDY, 1);
        }
        }
        asm volatile(".Lff_end0:");
        return;
    }

    const int v = w & 1, hd = w >> 1;
    const rsrc_t rsB = mk_rsrc(bulk_base(ws), WB_BYTES);
    unsigned tag, sof, t3, rid;
    unsigned long long rm;
    u32x16 ad;
    u32x4 g4;
    {
        const unsigned pi = (unsigned)((slot * 2 + (xcd & 1)) * 4 + w);
        const char* pa = bulk_base(ws) + ((unsigned)(xcd >> 1) * (2u << 20) + WB_PRB + (pi >> 5) * 16384u + (pi & 31u) * 256u + v * 8192u);
        const unsigned po = 8192u + v * 128u;
        unsigned j0, j1;
        asm volatile("s_load_dword %0, %2, 64\n\ts_load_dword %1, %2, %3" : "={s80}"(j0), "={s81}"(j1) : "s"(pa), "s"(po + 64u) : "memory");
        LANE_VARS;
        ad[0] = lds0 + (unsigned)(64 * v + c) * 128u + (unsigned)((g ^ ((c >> 1) & 7)) << 4);
        ad[1] = lds0 + 16384u + (WSW ^ (unsigned)(hd << 6));
        ad[2] = lds0 + L_VF + (unsigned)(hd * 2048 + v * 8192 + l * 16);
        ad[3] = g == i4 && i3 < 2 ? 0x3f80u << (16 * i3) : 0u;
        ad[4] = g == i4 && i3 >= 2 ? 0x3f80u << (16 * (i3 - 2)) : 0u;
        ad[5] = lds0 + L_WP + hd * 2048 + WPO;
        ad[6] = (unsigned)(l >> 5) * 16384u + (unsigned)(l & 31) * 16u;
        ad[7] = c < 3 ? (unsigned)(((g & 1) * 2 + (g >> 1)) * 1024 + c * 16) : 0x80000000u;
        ad[8] = lds0 + C_P + w * 4;
        ad[9] = lds0 + L_SM;
        ad[10] = c;
#pragma unroll
        for (int i = 11; i < 16; ++i) ad[i] = 0u;
        asm volatile(PHASE_AB_ASM
                     : "+{v[240:255]}"(ad), "={v[232:235]}"(g4), "={v236}"(tag), [sof] "=&s"(sof), [t3] "=&s"(t3), [rid] "=&s"(rid), [rm] "=&s"(rm), "+{s80}"(j0), "+{s81}"(j1)
                     : [rsb] "s"(rsB), [rsw] "s"(rsWS), [xs0] "s"(v ? XS1[0] - 8192u : XS0[0]), [xs1] "s"(v ? XS1[1] - 8192u : XS0[1]), [kpx] "s"(w * 65536),
                       [kpa] "s"(mtA * 512), [kma] "s"(hd * 16384 + v * 4096 + mtA * 64), [sg] "s"(WS_G + (unsigned)(mtB * 128 + 32 * w) * 8u),
                       [ofc] "s"(WB_OF + (unsigned)(mtB * 4 + w) * 65536u), [ofp] "s"(WB_OF + (unsigned)((mtA * 4 + 2 * v) * 32 + ((2 * hpA + hd + 1) & 31)) * 2048u),
                       [ogp] "s"(WB_OG + (unsigned)((mtA * 4 + 2 * v) * 32 + 2 * hpA + hd) * 8u), [kac] "s"(L_KVF + hd * 4096), [zac] "s"(L_ZF + hd * 512),
                       [xec] "s"(L_XE + w * 4096), [own] "s"(mtB * 4 + w), [prb] "s"(pa), [pro] "s"(po), [neg] "s"(v * 0x80008000u), [zx] "s"(L_WP + w * 1024)
                     : "memory", "vcc", "scc", PHASE_AB_CLOB);
    }
    {
        const rsrc_t rsOut = mk_rsrc(out, 2048u * 2048u);
        const unsigned sog = WB_OG + sof + (unsigned)(mtB * 4 + w) * 256u;
        unsigned grp = (t3 & 1u) * 2u, flip = 0;
#pragma unroll 1
        for (int att = 0; att < TRY_LIMIT; ++att) {
            unsigned cs, st;
            asm volatile(PHASE_C_ASM
                         : "={v230}"(cs), [st] "=&s"(st)
                         : "{v[240:255]}"(ad), "{v[232:235]}"(g4), "{v236}"(tag), [rsb] "s"(rsB), [rso] "s"(rsOut), [sog] "s"(sog), [grp] "s"(grp), [flip] "s"(flip),
                           [t3] "s"(t3), [rid] "s"(rid), [rm] "s"(rm), [sout] "s"((unsigned)(mtB * 128 + 32 * w) * 2048u + (unsigned)hpB * 128u)
                         : "memory", "vcc", "scc", PHASE_C_CLOB);
            if (K::FORCE_REDO && att < 2) st = 2 - att, cs += 1u;
            if (__builtin_expect(st == 0u, 1)) break;
            u32x2 og = ld64c(rsB, ad[4], sog);
            const unsigned miss = (unsigned)__builtin_amdgcn_ballot_w64(og[1] != tag);
            if (st == 1u && ((miss && !(miss & ~(0xffffu << (8u * grp)))) || K::FORCE_REDO)) {
                grp ^= 2u;
                flip ^= 65536u;
                lds_wait(lds0 + C_WO, 4);
                continue;
            }
            int it = 0;
#pragma unroll 1
            for (; it < POLL_LIMIT && !__all(og[1] == tag); ++it) {
                __builtin_amdgcn_s_sleep(K::RETRY_S);
                fence_c();
                og = ld64c(rsB, ad[4], sog);
            }
            if (it == POLL_LIMIT) break;
            if (st == 2u) {
                if (__all(wave_usum(cs - (ad[10] < 32u ? og[0] : 0u)) == 0u)) break;
            }
        }
    }
    asm volatile(".Lff_end2:\n.Lpad_x:\n\ts_cbranch_execz .Lpad_top");
}

extern "C" void kernel_launch(void* const* d_in, const int* in_sizes, int n_in, void* d_out, int out_size,
                              void* d_ws, size_t ws_size, hipStream_t stream) {
    const u16* x = (const u16*)d_in[0];
    const u16* wqkv = (const u16*)d_in[1];
    const u16* wphi = (const u16*)d_in[2];
    const u16* wo = (const u16*)d_in[3];
    const u16* wr = (const u16*)d_in[5];
    const u16* wg = (const u16*)d_in[6];
    const u16* wu = (const u16*)d_in[7];
    const u16* wd = (const u16*)d_in[8];
    const u16* gw = (const u16*)d_in[9];
    fused_fwd<<<256, 512, 0, stream>>>(x, wqkv, wphi, wo, wr, wg, wu, wd, gw, (char*)d_ws, (u16*)d_out);
}
```
